```python
import torch

def _softmax(x, axis=-1):
    return torch.softmax(x, dim=axis)

def _gelu(x):
    return torch.nn.functional.gelu(x, approximate='tanh')

def _silu(x):
    return torch.nn.functional.silu(x)

def _sigmoid(x):
    return torch.sigmoid(x)

def _layernorm(x, axis=-1, eps=1e-05):
    m = x.mean(dim=axis, keepdim=True)
    v = x.var(dim=axis, keepdim=True, unbiased=False)
    return (x - m) / torch.sqrt(v + eps)

def _scatter_add(tgt, idx, src, axis=0):
    return tgt.clone().index_add_(axis, idx, src)

def _segment_sum(src, seg, n):
    out = torch.zeros((n,) + src.shape[1:], dtype=src.dtype)
    return out.index_add_(0, seg, src)

def _pad(x, pw):
    flat = tuple((v for pair in reversed(pw) for v in pair))
    return torch.nn.functional.pad(x, flat)
kernel_name = 'adaLN_cond__ssm_conv_s4d__moe_arch_mod__prompt'

def setup_inputs():

    _w = lambda a: (lambda t: t.to(torch.bfloat16) if t.is_floating_point() else t)(torch.as_tensor(a))

    def _A_setup():
        Bsz, L, D, N = (1, 2048, 576, 128)
        return {'u': _w((lambda *s: torch.randn(*s, dtype=torch.bfloat16) * 0.1)(Bsz, L, D)), 'log_A_real': _w((lambda *s: torch.randn(*s, dtype=torch.bfloat16) * 3.2)(D, N) * 0.5), 'A_imag': _w((lambda *s: torch.randn(*s, dtype=torch.bfloat16) * 0.4)(D, N) * 3.14), 'C_real': _w((lambda *s: torch.randn(*s, dtype=torch.bfloat16) * 0.8)(D, N) / N ** 0.5), 'C_imag': _w((lambda *s: torch.randn(*s, dtype=torch.bfloat16) * 0.8)(D, N) / N ** 0.5), 'log_dt': _w((lambda *s: torch.randn(*s, dtype=torch.bfloat16) * 0.4)(D) * 0.5 - 4.0), 'D_skip': _w((lambda *s: torch.randn(*s, dtype=torch.bfloat16) * 0.4)(D))}
    _A_in = _A_setup()

    def _B_setup():
        B, S, D, F, C = (1, 2048, 576, 4608, 8)
        return {'x': _w((lambda *s: torch.randn(*s, dtype=torch.bfloat16) * 0.1)(B, S, D)), 'w_r': _w((lambda *s: torch.randn(*s, dtype=torch.bfloat16) * 0.4)(D)), 'w_gate': _w((lambda *s: torch.randn(*s, dtype=torch.bfloat16) * 0.4)(D, F)), 'w_up': _w((lambda *s: torch.randn(*s, dtype=torch.bfloat16) * 0.4)(D, F)), 'w_down': _w((lambda *s: torch.randn(*s, dtype=torch.bfloat16) * 0.4)(F, D))}
    _B_in = _B_setup()
    return {'A_u': _A_in['u'], 'A_log_A_real': _A_in['log_A_real'], 'A_A_imag': _A_in['A_imag'], 'A_C_real': _A_in['C_real'], 'A_C_imag': _A_in['C_imag'], 'A_log_dt': _A_in['log_dt'], 'A_D_skip': _A_in['D_skip'], 'B_x': _B_in['x'], 'B_w_r': _B_in['w_r'], 'B_w_gate': _B_in['w_gate'], 'B_w_up': _B_in['w_up'], 'B_w_down': _B_in['w_down'], 'c': _w((lambda *s: torch.randn(*s, dtype=torch.bfloat16) * 0.2)(1, 576) * 8.0), 'w_mod': _w((lambda *s: torch.randn(*s, dtype=torch.bfloat16) * 0.2)(576, 6 * 576) * 576 ** (-0.5) * 8.0)}

def reference(A_u, A_log_A_real, A_A_imag, A_C_real, A_C_imag, A_log_dt, A_D_skip, B_x, B_w_r, B_w_gate, B_w_up, B_w_down, c, w_mod):

    def _A_ref(u, log_A_real, A_imag, C_real, C_imag, log_dt, D_skip):
        L = u.shape[1]
        dt = torch.exp(log_dt)
        A = -torch.exp(log_A_real) + 1j * A_imag
        C = (C_real + 1j * C_imag) * dt[:, None]
        pos = (lambda x, dt: x.to(dt))(torch.arange(L), log_dt.dtype)
        P = torch.exp((A * dt[:, None])[:, :, None] * pos[None, None, :])
        K = 2.0 * torch.einsum('dn,dnl->dl', C, P).real
        L2 = 2 * L
        u_t = (lambda x, *d: x.permute(*d))(u, 0, 2, 1)
        Kf = (lambda x, *a, **k: torch.fft.rfft(x.float(), *a, **k))(K, L2)
        Uf = (lambda x, *a, **k: torch.fft.rfft(x.float(), *a, **k))(u_t, L2)
        y = (lambda x, *a, **k: torch.fft.irfft(x, *a, **k).to(torch.bfloat16))(Uf * Kf[None, :, :], L2)[:, :, :L]
        y = (lambda x, *d: x.permute(*d))(y, 0, 2, 1)
        return y + u * D_skip[None, None, :]

    def _B_ref(x, w_r, w_gate, w_up, w_down):
        B, S, D = x.shape
        C = 8
        r = x @ w_r
        idx = torch.argsort(-r, axis=-1)[:, :C]
        idx_e = (lambda x, n, axis: torch.repeat_interleave(x, n, dim=axis))(idx.reshape(B, C, 1), D, axis=2)
        x_sel = (lambda x, i, axis: torch.take_along_dim(x, i, dim=axis))(x, idx_e, axis=1)
        g = _silu(x_sel @ w_gate)
        u = x_sel @ w_up
        y = g * u @ w_down
        r_sel = _sigmoid((lambda x, i, axis: torch.take_along_dim(x, i, dim=axis))(r, idx, axis=1))
        y = y * r_sel.reshape(B, C, 1)
        b_off = (torch.arange(B) * S).reshape(B, 1)
        flat_idx = (idx + b_off).reshape(B * C)
        zero = (x * 0.0).reshape(B * S, D)
        upd = _scatter_add(zero, flat_idx, y.reshape(B * C, D), axis=0)
        return x + upd.reshape(B, S, D)
    x = A_u
    mod = (_silu(c) @ w_mod).reshape(c.shape[0], 6, 1, -1)
    sa, ca, ga, sb, cb, gb = (mod[:, 0].clone(), mod[:, 1].clone(), mod[:, 2].clone(), mod[:, 3].clone(), mod[:, 4].clone(), mod[:, 5].clone())
    h = _layernorm(x, axis=-1) * (1 + ca) + sa
    x = x + ga * _A_ref(h, A_log_A_real, A_A_imag, A_C_real, A_C_imag, A_log_dt, A_D_skip)
    h = _layernorm(x, axis=-1) * (1 + cb) + sb
    x = x + gb * _B_ref(h, B_w_r, B_w_gate, B_w_up, B_w_down)
    return x
```

```cpp
#include <hip/hip_runtime.h>
#include <stdint.h>

struct Args {
  const void *x, *lar, *aim, *ldt, *cvec, *wmod;
  void* ws; uint32_t ws_bytes, ws_flags;
  const void *cre, *cim, *dsk, *wr, *wg, *wu, *wd;
  void* out;
};
extern "C" __device__ __attribute__((used, aligned(128))) const uint32_t mega_tab[3840] = {
  0x0, 0x0, 0x0, 0x0, 0x0, 0x0, 0x0, 0x0, 0x0, 0x0, 0x0, 0x0, 0x0, 0x0, 0x0, 0x0,
  0x0, 0x0, 0x0, 0x0, 0x0, 0x0, 0x0, 0x0, 0x0, 0x0, 0x0, 0x0, 0x0, 0x0, 0x0, 0x0,
  0x0, 0x0, 0x0, 0x0, 0x0, 0x0, 0x0, 0x0, 0x0, 0x0, 0x0, 0x0, 0x0, 0x0, 0x0, 0x3c000000,
  0x0, 0x0, 0x0, 0x3c003c00, 0x0, 0x0, 0x3c000000, 0x3c003c00, 0x0, 0x0, 0x3c003c00, 0x3c00, 0x0, 0x3c000000, 0x3c003c00, 0x0,
  0x0, 0x0, 0x0, 0x0, 0x0, 0x0, 0x0, 0x0, 0x0, 0x0, 0x0, 0x0, 0x0, 0x0, 0x0, 0x3c000000,
  0x0, 0x0, 0x0, 0x3c003c00, 0x0, 0x0, 0x3c000000, 0x3c003c00, 0x0, 0x0, 0x3c003c00, 0x3c00, 0x0, 0x3c000000, 0x3c003c00, 0x0,
  0x0, 0x3c003c00, 0x3c00, 0x0, 0x3c000000, 0x3c003c00, 0x0, 0x0, 0x3c003c00, 0x3c00, 0x0, 0x0, 0x3c003c00, 0x0, 0x0, 0x0,
  0x3c00, 0x0, 0x0, 0x0, 0x0, 0x0, 0x0, 0x0, 0x0, 0x0, 0x0, 0x0, 0x0, 0x0, 0x0, 0x0,
  0x0, 0x3c003c00, 0x3c00, 0x0, 0x3c000000, 0x3c003c00, 0x0, 0x0, 0x3c003c00, 0x3c00, 0x0, 0x0, 0x3c003c00, 0x0, 0x0, 0x0,
  0x3c00, 0x0, 0x0, 0x0, 0x0, 0x0, 0x0, 0x0, 0x0, 0x0, 0x0, 0x0, 0x0, 0x0, 0x0, 0x0,
  0x0, 0x0, 0x0, 0x0, 0x0, 0x0, 0x0, 0x0, 0x0, 0x0, 0x0, 0x0, 0x0, 0x0, 0x0, 0x0,
  0x0, 0x0, 0x0, 0x0, 0x0, 0x0, 0x0, 0x0, 0x0, 0x0, 0x0, 0x0, 0x0, 0x0, 0x0, 0x0,
  0x0, 0x0, 0x0, 0x0, 0x0, 0x0, 0x0, 0x0, 0x0, 0x0, 0x0, 0x0, 0x0, 0x0, 0x0, 0x0,
  0x0, 0x0, 0x0, 0x0, 0x0, 0x0, 0x0, 0x0, 0x0, 0x0, 0x0, 0x0, 0x0, 0x0, 0x0, 0x0,
  0x0, 0x0, 0x0, 0x0, 0x0, 0x0, 0x0, 0x0, 0x0, 0x0, 0x0, 0x0, 0x0, 0x0, 0x0, 0x0,
  0x0, 0x0, 0x0, 0x0, 0x0, 0x0, 0x0, 0x0, 0x0, 0x0, 0x0, 0x0, 0x0, 0x0, 0x0, 0x0,
  0x0, 0x0, 0x0, 0x0, 0x0, 0x0, 0x0, 0x0, 0x0, 0x0, 0x0, 0x0, 0x0, 0x0, 0x0, 0x0,
  0x0, 0x0, 0x0, 0x0, 0x0, 0x0, 0x0, 0x0, 0x0, 0x0, 0x0, 0x0, 0x0, 0x0, 0x0, 0x0,
  0x0, 0x0, 0x0, 0x0, 0x0, 0x0, 0x0, 0x0, 0x0, 0x0, 0x0, 0x0, 0x0, 0x0, 0x0, 0x0,
  0x0, 0x0, 0x0, 0x0, 0x0, 0x0, 0x0, 0x0, 0x0, 0x0, 0x0, 0x0, 0x0, 0x0, 0x0, 0x0,
  0x0, 0x0, 0x0, 0x0, 0x0, 0x0, 0x0, 0x0, 0x0, 0x0, 0x0, 0x0, 0x0, 0x0, 0x0, 0x0,
  0x0, 0x0, 0x0, 0x0, 0x0, 0x0, 0x0, 0x0, 0x0, 0x0, 0x0, 0x0, 0x0, 0x0, 0x0, 0x0,
  0x0, 0x0, 0x0, 0x0, 0x0, 0x0, 0x0, 0x0, 0x0, 0x0, 0x0, 0x0, 0x0, 0x0, 0x0, 0x3c000000,
  0x0, 0x0, 0x0, 0x3c003c00, 0x0, 0x0, 0x3c000000, 0x3c003c00, 0x0, 0x0, 0x3c003c00, 0x3c00, 0x0, 0x3c000000, 0x3c003c00, 0x0,
  0x0, 0x0, 0x0, 0x0, 0x0, 0x0, 0x0, 0x0, 0x0, 0x0, 0x0, 0x0, 0x0, 0x0, 0x0, 0x3c000000,
  0x0, 0x0, 0x0, 0x3c003c00, 0x0, 0x0, 0x3c000000, 0x3c003c00, 0x0, 0x0, 0x3c003c00, 0x3c00, 0x0, 0x3c000000, 0x3c003c00, 0x0,
  0x0, 0x3c003c00, 0x3c00, 0x0, 0x3c000000, 0x3c003c00, 0x0, 0x0, 0x3c003c00, 0x3c00, 0x0, 0x0, 0x3c003c00, 0x0, 0x0, 0x0,
  0x3c00, 0x0, 0x0, 0x0, 0x0, 0x0, 0x0, 0x0, 0x0, 0x0, 0x0, 0x0, 0x0, 0x0, 0x0, 0x0,
  0x0, 0x3c003c00, 0x3c00, 0x0, 0x3c000000, 0x3c003c00, 0x0, 0x0, 0x3c003c00, 0x3c00, 0x0, 0x0, 0x3c003c00, 0x0, 0x0, 0x0,
  0x3c00, 0x0, 0x0, 0x0, 0x0, 0x0, 0x0, 0x0, 0x0, 0x0, 0x0, 0x0, 0x0, 0x0, 0x0, 0x0,
  0x0, 0x0, 0x0, 0x0, 0x0, 0x0, 0x0, 0x0, 0x0, 0x0, 0x0, 0x0, 0x0, 0x0, 0x0, 0x0,
  0x0, 0x0, 0x0, 0x0, 0x0, 0x0, 0x0, 0x0, 0x0, 0x0, 0x0, 0x0, 0x0, 0x0, 0x0, 0x0,
  0x0, 0x0, 0x0, 0x0, 0x0, 0x0, 0x0, 0x0, 0x0, 0x0, 0x0, 0x0, 0x0, 0x0, 0x0, 0x0,
  0x0, 0x0, 0x0, 0x0, 0x0, 0x0, 0x0, 0x0, 0x0, 0x0, 0x0, 0x0, 0x0, 0x0, 0x0, 0x0,
  0x0, 0x0, 0x0, 0x0, 0x0, 0x0, 0x0, 0x0, 0x0, 0x0, 0x0, 0x0, 0x0, 0x0, 0x0, 0x0,
  0x0, 0x0, 0x0, 0x0, 0x0, 0x0, 0x0, 0x0, 0x0, 0x0, 0x0, 0x3c000000, 0x0, 0x0, 0x0, 0x3c003c00,
  0x0, 0x0, 0x0, 0x0, 0x0, 0x0, 0x0, 0x0, 0x0, 0x0, 0x0, 0x0, 0x0, 0x0, 0x0, 0x0,
  0x0, 0x0, 0x0, 0x0, 0x0, 0x0, 0x0, 0x0, 0x0, 0x0, 0x0, 0x3c000000, 0x0, 0x0, 0x0, 0x3c003c00,
  0x0, 0x0, 0x3c000000, 0x3c003c00, 0x0, 0x0, 0x3c003c00, 0x3c003c00, 0x0, 0x3c000000, 0x3c003c00, 0x3c003c00, 0x0, 0x3c003c00, 0x3c003c00, 0x3c00,
  0x3c000000, 0x3c003c00, 0x3c003c00, 0x0, 0x3c003c00, 0x3c003c00, 0x3c00, 0x0, 0x3c003c00, 0x3c003c00, 0x0, 0x0, 0x3c003c00, 0x3c00, 0x0, 0x0,
  0x0, 0x0, 0x3c000000, 0x3c003c00, 0x0, 0x0, 0x3c003c00, 0x3c003c00, 0x0, 0x3c000000, 0x3c003c00, 0x3c003c00, 0x0, 0x3c003c00, 0x3c003c00, 0x3c00,
  0x3c000000, 0x3c003c00, 0x3c003c00, 0x0, 0x3c003c00, 0x3c003c00, 0x3c00, 0x0, 0x3c003c00, 0x3c003c00, 0x0, 0x0, 0x3c003c00, 0x3c00, 0x0, 0x0,
  0x3c003c00, 0x0, 0x0, 0x0, 0x3c00, 0x0, 0x0, 0x0, 0x0, 0x0, 0x0, 0x0, 0x0, 0x0, 0x0, 0x0,
  0x0, 0x0, 0x0, 0x0, 0x0, 0x0, 0x0, 0x0, 0x0, 0x0, 0x0, 0x0, 0x0, 0x0, 0x0, 0x0,
  0x3c003c00, 0x0, 0x0, 0x0, 0x3c00, 0x0, 0x0, 0x0, 0x0, 0x0, 0x0, 0x0, 0x0, 0x0, 0x0, 0x0,
  0x0, 0x0, 0x0, 0x0, 0x0, 0x0, 0x0, 0x0, 0x0, 0x0, 0x0, 0x0, 0x0, 0x0, 0x0, 0x0,
  0x0, 0x0, 0x0, 0x0, 0x0, 0x0, 0x0, 0x0, 0x0, 0x0, 0x0, 0x0, 0x0, 0x0, 0x0, 0x0,
  0x0, 0x0, 0x0, 0x0, 0x0, 0x0, 0x0, 0x0, 0x0, 0x0, 0x0, 0x0, 0x0, 0x0, 0x0, 0x0,
  0x0, 0x0, 0x0, 0x0, 0x0, 0x0, 0x0, 0x0, 0x0, 0x0, 0x0, 0x0, 0x0, 0x0, 0x0, 0x0,
  0x0, 0x0, 0x0, 0x0, 0x0, 0x0, 0x0, 0x0, 0x0, 0x0, 0x0, 0x0, 0x0, 0x0, 0x0, 0x0,
  0x0, 0x0, 0x0, 0x0, 0x0, 0x0, 0x0, 0x0, 0x0, 0x0, 0x0, 0x0, 0x0, 0x0, 0x0, 0x0,
  0x0, 0x0, 0x0, 0x0, 0x0, 0x0, 0x0, 0x3c000000, 0x0, 0x0, 0x0, 0x3c003c00, 0x0, 0x0, 0x3c000000, 0x3c003c00,
  0x0, 0x0, 0x0, 0x0, 0x0, 0x0, 0x0, 0x0, 0x0, 0x0, 0x0, 0x0, 0x0, 0x0, 0x0, 0x0,
  0x0, 0x0, 0x0, 0x0, 0x0, 0x0, 0x0, 0x3c000000, 0x0, 0x0, 0x0, 0x3c003c00, 0x0, 0x0, 0x3c000000, 0x3c003c00,
  0x0, 0x0, 0x3c003c00, 0x3c003c00, 0x0, 0x3c000000, 0x3c003c00, 0x3c003c00, 0x0, 0x3c003c00, 0x3c003c00, 0x3c003c00, 0x3c000000, 0x3c003c00, 0x3c003c00, 0x3c003c00,
  0x3c003c00, 0x3c003c00, 0x3c003c00, 0x3c00, 0x3c003c00, 0x3c003c00, 0x3c003c00, 0x0, 0x3c003c00, 0x3c003c00, 0x3c00, 0x0, 0x3c003c00, 0x3c003c00, 0x0, 0x0,
  0x0, 0x0, 0x3c003c00, 0x3c003c00, 0x0, 0x3c000000, 0x3c003c00, 0x3c003c00, 0x0, 0x3c003c00, 0x3c003c00, 0x3c003c00, 0x3c000000, 0x3c003c00, 0x3c003c00, 0x3c003c00,
  0x3c003c00, 0x3c003c00, 0x3c003c00, 0x3c00, 0x3c003c00, 0x3c003c00, 0x3c003c00, 0x0, 0x3c003c00, 0x3c003c00, 0x3c00, 0x0, 0x3c003c00, 0x3c003c00, 0x0, 0x0,
  0x3c003c00, 0x3c00, 0x0, 0x0, 0x3c003c00, 0x0, 0x0, 0x0, 0x3c00, 0x0, 0x0, 0x0, 0x0, 0x0, 0x0, 0x0,
  0x0, 0x0, 0x0, 0x0, 0x0, 0x0, 0x0, 0x0, 0x0, 0x0, 0x0, 0x0, 0x0, 0x0, 0x0, 0x0,
  0x3c003c00, 0x3c00, 0x0, 0x0, 0x3c003c00, 0x0, 0x0, 0x0, 0x3c00, 0x0, 0x0, 0x0, 0x0, 0x0, 0x0, 0x0,
  0x0, 0x0, 0x0, 0x0, 0x0, 0x0, 0x0, 0x0, 0x0, 0x0, 0x0, 0x0, 0x0, 0x0, 0x0, 0x0,
  0x0, 0x0, 0x0, 0x0, 0x0, 0x0, 0x0, 0x0, 0x0, 0x0, 0x0, 0x0, 0x0, 0x0, 0x0, 0x0,
  0x0, 0x0, 0x0, 0x0, 0x0, 0x0, 0x0, 0x0, 0x0, 0x0, 0x0, 0x0, 0x0, 0x0, 0x0, 0x0,
  0x0, 0x0, 0x0, 0x0, 0x0, 0x0, 0x0, 0x0, 0x0, 0x0, 0x0, 0x0, 0x0, 0x0, 0x0, 0x0,
  0x0, 0x0, 0x0, 0x0, 0x0, 0x0, 0x0, 0x0, 0x0, 0x0, 0x0, 0x0, 0x0, 0x0, 0x0, 0x0,
  0x0, 0x0, 0x0, 0x0, 0x0, 0x0, 0x0, 0x0, 0x0, 0x0, 0x0, 0x0, 0x0, 0x0, 0x0, 0x0,
  0x0, 0x0, 0x0, 0x3c000000, 0x0, 0x0, 0x0, 0x3c003c00, 0x0, 0x0, 0x3c000000, 0x3c003c00, 0x0, 0x0, 0x3c003c00, 0x3c003c00,
  0x0, 0x0, 0x0, 0x0, 0x0, 0x0, 0x0, 0x0, 0x0, 0x0, 0x0, 0x0, 0x0, 0x0, 0x0, 0x0,
  0x0, 0x0, 0x0, 0x3c000000, 0x0, 0x0, 0x0, 0x3c003c00, 0x0, 0x0, 0x3c000000, 0x3c003c00, 0x0, 0x0, 0x3c003c00, 0x3c003c00,
  0x0, 0x3c000000, 0x3c003c00, 0x3c003c00, 0x0, 0x3c003c00, 0x3c003c00, 0x3c003c00, 0x3c000000, 0x3c003c00, 0x3c003c00, 0x3c003c00, 0x3c003c00, 0x3c003c00, 0x3c003c00, 0x3c003c00,
  0x3c003c00, 0x3c003c00, 0x3c003c00, 0x3c003c00, 0x3c003c00, 0x3c003c00, 0x3c003c00, 0x3c00, 0x3c003c00, 0x3c003c00, 0x3c003c00, 0x0, 0x3c003c00, 0x3c003c00, 0x3c00, 0x0,
  0x0, 0x3c000000, 0x3c003c00, 0x3c003c00, 0x0, 0x3c003c00, 0x3c003c00, 0x3c003c00, 0x3c000000, 0x3c003c00, 0x3c003c00, 0x3c003c00, 0x3c003c00, 0x3c003c00, 0x3c003c00, 0x3c003c00,
  0x3c003c00, 0x3c003c00, 0x3c003c00, 0x3c003c00, 0x3c003c00, 0x3c003c00, 0x3c003c00, 0x3c00, 0x3c003c00, 0x3c003c00, 0x3c003c00, 0x0, 0x3c003c00, 0x3c003c00, 0x3c00, 0x0,
  0x3c003c00, 0x3c003c00, 0x0, 0x0, 0x3c003c00, 0x3c00, 0x0, 0x0, 0x3c003c00, 0x0, 0x0, 0x0, 0x3c00, 0x0, 0x0, 0x0,
  0x0, 0x0, 0x0, 0x0, 0x0, 0x0, 0x0, 0x0, 0x0, 0x0, 0x0, 0x0, 0x0, 0x0, 0x0, 0x0,
  0x3c003c00, 0x3c003c00, 0x0, 0x0, 0x3c003c00, 0x3c00, 0x0, 0x0, 0x3c003c00, 0x0, 0x0, 0x0, 0x3c00, 0x0, 0x0, 0x0,
  0x0, 0x0, 0x0, 0x0, 0x0, 0x0, 0x0, 0x0, 0x0, 0x0, 0x0, 0x0, 0x0, 0x0, 0x0, 0x0,
  0x0, 0x0, 0x0, 0x0, 0x0, 0x0, 0x0, 0x0, 0x0, 0x0, 0x0, 0x0, 0x0, 0x0, 0x0, 0x0,
  0x0, 0x0, 0x0, 0x0, 0x0, 0x0, 0x0, 0x0, 0x0, 0x0, 0x0, 0x0, 0x0, 0x0, 0x0, 0x0,
  0x0, 0x0, 0x0, 0x0, 0x0, 0x0, 0x0, 0x0, 0x0, 0x0, 0x0, 0x0, 0x0, 0x0, 0x0, 0x0,
  0x0, 0x0, 0x0, 0x0, 0x0, 0x0, 0x0, 0x0, 0x0, 0x0, 0x0, 0x0, 0x0, 0x0, 0x0, 0x0,
  0x0, 0x0, 0x0, 0x0, 0x0, 0x0, 0x0, 0x0, 0x0, 0x0, 0x0, 0x0, 0x0, 0x0, 0x0, 0xffff0000,
  0x0, 0x0, 0x0, 0xffffffff, 0x0, 0x0, 0xffff0000, 0xffff, 0x0, 0x0, 0xffffffff, 0x0, 0x0, 0xffff0000, 0xffff, 0x0,
  0x0, 0x0, 0x0, 0x0, 0x0, 0x0, 0x0, 0x0, 0x0, 0x0, 0x0, 0x0, 0x0, 0x0, 0x0, 0xffff0000,
  0x0, 0x0, 0x0, 0xffffffff, 0x0, 0x0, 0xffff0000, 0xffff, 0x0, 0x0, 0xffffffff, 0x0, 0x0, 0xffff0000, 0xffff, 0x0,
  0x0, 0xffffffff, 0x0, 0x0, 0xffff0000, 0xffff, 0x0, 0x0, 0xffffffff, 0x0, 0x0, 0x0, 0xffff, 0x0, 0x0, 0x0,
  0x0, 0x0, 0x0, 0x0, 0x0, 0x0, 0x0, 0x0, 0x0, 0x0, 0x0, 0x0, 0x0, 0x0, 0x0, 0x0,
  0x0, 0xffffffff, 0x0, 0x0, 0xffff0000, 0xffff, 0x0, 0x0, 0xffffffff, 0x0, 0x0, 0x0, 0xffff, 0x0, 0x0, 0x0,
  0x0, 0x0, 0x0, 0x0, 0x0, 0x0, 0x0, 0x0, 0x0, 0x0, 0x0, 0x0, 0x0, 0x0, 0x0, 0x0,
  0x0, 0x0, 0x0, 0x0, 0x0, 0x0, 0x0, 0x0, 0x0, 0x0, 0x0, 0x0, 0x0, 0x0, 0x0, 0x0,
  0x0, 0x0, 0x0, 0x0, 0x0, 0x0, 0x0, 0x0, 0x0, 0x0, 0x0, 0x0, 0x0, 0x0, 0x0, 0x0,
  0x0, 0x0, 0x0, 0x0, 0x0, 0x0, 0x0, 0x0, 0x0, 0x0, 0x0, 0x0, 0x0, 0x0, 0x0, 0x0,
  0x0, 0x0, 0x0, 0x0, 0x0, 0x0, 0x0, 0x0, 0x0, 0x0, 0x0, 0x0, 0x0, 0x0, 0x0, 0x0,
  0x0, 0x0, 0x0, 0x0, 0x0, 0x0, 0x0, 0x0, 0x0, 0x0, 0x0, 0x0, 0x0, 0x0, 0x0, 0x0,
  0x0, 0x0, 0x0, 0x0, 0x0, 0x0, 0x0, 0x0, 0x0, 0x0, 0x0, 0x0, 0x0, 0x0, 0x0, 0x0,
  0x16520, 0x3020100, 0x7060504, 0x3020100, 0x16620, 0x3020100, 0x7060504, 0x3020100, 0x16530, 0x3020100, 0x7060504, 0x3020100, 0x16630, 0x3020100, 0x7060504, 0x3020100,
  0x16540, 0x3020100, 0x7060504, 0x3020100, 0x16640, 0x3020100, 0x7060504, 0x3020100, 0x16550, 0x3020100, 0x7060504, 0x3020100, 0x16650, 0x3020100, 0x7060504, 0x3020100,
  0x16560, 0x3020100, 0x7060504, 0x3020100, 0x16660, 0x3020100, 0x7060504, 0x3020100, 0x16570, 0x3020100, 0x7060504, 0x3020100, 0x16670, 0x3020100, 0x7060504, 0x3020100,
  0x16600, 0x3020100, 0x7060504, 0x3020100, 0x16700, 0x3020100, 0x7060504, 0x3020100, 0x16610, 0x3020100, 0x7060504, 0x3020100, 0x16710, 0x3020100, 0x7060504, 0x3020100,
  0x16528, 0x3020100, 0x7060504, 0x3020100, 0x16628, 0x3020100, 0x7060504, 0x3020100, 0x16538, 0x3020100, 0x7060504, 0x3020100, 0x16638, 0x3020100, 0x7060504, 0x3020100,
  0x16548, 0x3020100, 0x7060504, 0x3020100, 0x16648, 0x3020100, 0x7060504, 0x3020100, 0x16558, 0x3020100, 0x7060504, 0x3020100, 0x16658, 0x3020100, 0x7060504, 0x3020100,
  0x16568, 0x3020100, 0x7060504, 0x3020100, 0x16668, 0x3020100, 0x7060504, 0x3020100, 0x16578, 0x3020100, 0x7060504, 0x3020100, 0x16678, 0x3020100, 0x7060504, 0x3020100,
  0x16608, 0x3020100, 0x7060504, 0x3020100, 0x16708, 0x3020100, 0x7060504, 0x3020100, 0x16618, 0x3020100, 0x7060504, 0x3020100, 0x16718, 0x3020100, 0x7060504, 0x3020100,
  0x16720, 0x1000100, 0x5040302, 0x5040100, 0x16820, 0x1000100, 0x5040302, 0x5040100, 0x16730, 0x1000100, 0x5040302, 0x5040100, 0x16830, 0x1000100, 0x5040302, 0x5040100,
  0x16740, 0x1000100, 0x5040302, 0x5040100, 0x16840, 0x1000100, 0x5040302, 0x5040100, 0x16750, 0x1000100, 0x5040302, 0x5040100, 0x16850, 0x1000100, 0x5040302, 0x5040100,
  0x16760, 0x1000100, 0x5040302, 0x5040100, 0x16860, 0x1000100, 0x5040302, 0x5040100, 0x16770, 0x1000100, 0x5040302, 0x5040100, 0x16870, 0x1000100, 0x5040302, 0x5040100,
  0x16800, 0x1000100, 0x5040302, 0x5040100, 0x16900, 0x1000100, 0x5040302, 0x5040100, 0x16810, 0x1000100, 0x5040302, 0x5040100, 0x16910, 0x1000100, 0x5040302, 0x5040100,
  0x16920, 0x5040504, 0xc0c0504, 0x3020100, 0x16cc8, 0x3020100, 0x7060504, 0x3020100, 0x16cd0, 0x3020100, 0x7060504, 0x3020100, 0x16cd8, 0x3020100, 0x7060504, 0x3020100,
  0x16ce0, 0x3020100, 0x7060504, 0x3020100, 0x16ce8, 0x3020100, 0x7060504, 0x3020100, 0x16cf0, 0x3020100, 0x7060504, 0x3020100, 0x16cf8, 0x3020100, 0x7060504, 0x3020100,
  0x16d00, 0x3020100, 0x7060504, 0x3020100, 0x16d08, 0x3020100, 0x7060504, 0x3020100, 0x16d10, 0x3020100, 0x7060504, 0x3020100, 0x16d18, 0x3020100, 0x7060504, 0x3020100,
  0x16d20, 0x3020100, 0x7060504, 0x3020100, 0x16d28, 0x3020100, 0x7060504, 0x3020100, 0x16d30, 0x3020100, 0x7060504, 0x3020100, 0x16d38, 0x3020100, 0x7060504, 0x3020100,
  0x16d5e, 0x16d60, 0x16d62, 0x16d64, 0x16e5e, 0x16e60, 0x16e62, 0x16e64, 0x16d6e, 0x16d70, 0x16d72, 0x16d74, 0x16e6e, 0x16e70, 0x16e72, 0x16e74,
  0x16d7e, 0x16d80, 0x16d82, 0x16d84, 0x16e7e, 0x16e80, 0x16e82, 0x16e84, 0x16d8e, 0x16d90, 0x16d92, 0x16d94, 0x16e8e, 0x16e90, 0x16e92, 0x16e94,
  0x16d9e, 0x16da0, 0x16da2, 0x16da4, 0x16e9e, 0x16ea0, 0x16ea2, 0x16ea4, 0x16dae, 0x16db0, 0x16db2, 0x16db4, 0x16eae, 0x16eb0, 0x16eb2, 0x16eb4,
  0x16e3e, 0x16e40, 0x16e42, 0x16e44, 0x16f3e, 0x16f40, 0x16f42, 0x16f44, 0x16e4e, 0x16e50, 0x16e52, 0x16e54, 0x16f4e, 0x16f50, 0x16f52, 0x16f54,
  0x16d66, 0x16d68, 0x16d6a, 0x16d6c, 0x16e66, 0x16e68, 0x16e6a, 0x16e6c, 0x16d76, 0x16d78, 0x16d7a, 0x16d7c, 0x16e76, 0x16e78, 0x16e7a, 0x16e7c,
  0x16d86, 0x16d88, 0x16d8a, 0x16d8c, 0x16e86, 0x16e88, 0x16e8a, 0x16e8c, 0x16d96, 0x16d98, 0x16d9a, 0x16d9c, 0x16e96, 0x16e98, 0x16e9a, 0x16e9c,
  0x16da6, 0x16da8, 0x16daa, 0x16dac, 0x16ea6, 0x16ea8, 0x16eaa, 0x16eac, 0x16db6, 0x16db8, 0x16dba, 0x16dbc, 0x16eb6, 0x16eb8, 0x16eba, 0x16ebc,
  0x16e46, 0x16e48, 0x16e4a, 0x16e4c, 0x16f46, 0x16f48, 0x16f4a, 0x16f4c, 0x16e56, 0x16e58, 0x16e5a, 0x16e5c, 0x16f56, 0x16f58, 0x16f5a, 0x16f5c,
  0x16cc0, 0x16728, 0x1672a, 0x1672c, 0x1681e, 0x16828, 0x1682a, 0x1682c, 0x1672e, 0x16738, 0x1673a, 0x1673c, 0x1682e, 0x16838, 0x1683a, 0x1683c,
  0x1673e, 0x16748, 0x1674a, 0x1674c, 0x1683e, 0x16848, 0x1684a, 0x1684c, 0x1674e, 0x16758, 0x1675a, 0x1675c, 0x1684e, 0x16858, 0x1685a, 0x1685c,
  0x1675e, 0x16768, 0x1676a, 0x1676c, 0x1685e, 0x16868, 0x1686a, 0x1686c, 0x1676e, 0x16778, 0x1677a, 0x1677c, 0x1686e, 0x16878, 0x1687a, 0x1687c,
  0x167fe, 0x16808, 0x1680a, 0x1680c, 0x168fe, 0x16908, 0x1690a, 0x1690c, 0x1680e, 0x16818, 0x1681a, 0x1681c, 0x1690e, 0x16918, 0x1691a, 0x1691c,
  0x169c0, 0x16a00, 0x1691e, 0x16afe, 0x169e0, 0x16a20, 0x16ade, 0x16b1e, 0x169c2, 0x16a02, 0x16ac0, 0x16b00, 0x169e2, 0x16a22, 0x16ae0, 0x16b20,
  0x169c4, 0x16a04, 0x16ac2, 0x16b02, 0x169e4, 0x16a24, 0x16ae2, 0x16b22, 0x169c6, 0x16a06, 0x16ac4, 0x16b04, 0x169e6, 0x16a26, 0x16ae4, 0x16b24,
  0x169c8, 0x16a08, 0x16ac6, 0x16b06, 0x169e8, 0x16a28, 0x16ae6, 0x16b26, 0x169ca, 0x16a0a, 0x16ac8, 0x16b08, 0x169ea, 0x16a2a, 0x16ae8, 0x16b28,
  0x169dc, 0x16a1c, 0x16ada, 0x16b1a, 0x169fc, 0x16a3c, 0x16afa, 0x16b3a, 0x169de, 0x16a1e, 0x16adc, 0x16b1c, 0x169fe, 0x16a3e, 0x16afc, 0x16b3c,
  0x16b3e, 0x16bfe, 0x16c3e, 0x16c7e, 0x16bde, 0x16c1e, 0x16c5e, 0x16c9e, 0x16bc0, 0x16c00, 0x16c40, 0x16c80, 0x16be0, 0x16c20, 0x16c60, 0x16ca0,
  0x16bc2, 0x16c02, 0x16c42, 0x16c82, 0x16be2, 0x16c22, 0x16c62, 0x16ca2, 0x16bc4, 0x16c04, 0x16c44, 0x16c84, 0x16be4, 0x16c24, 0x16c64, 0x16ca4,
  0x16bc6, 0x16c06, 0x16c46, 0x16c86, 0x16be6, 0x16c26, 0x16c66, 0x16ca6, 0x16bc8, 0x16c08, 0x16c48, 0x16c88, 0x16be8, 0x16c28, 0x16c68, 0x16ca8,
  0x16bda, 0x16c1a, 0x16c5a, 0x16c9a, 0x16bfa, 0x16c3a, 0x16c7a, 0x16cba, 0x16bdc, 0x16c1c, 0x16c5c, 0x16c9c, 0x16bfc, 0x16c3c, 0x16c7c, 0x16cbc,
  0x16cbe, 0x16cc0, 0x16cc0, 0x16cc0, 0x16cc8, 0x16cc8, 0x16cc8, 0x16cc8, 0x16cd0, 0x16cd0, 0x16cd0, 0x16cd0, 0x16cd8, 0x16cd8, 0x16cd8, 0x16cd8,
  0x16ce0, 0x16ce0, 0x16ce0, 0x16ce0, 0x16ce8, 0x16ce8, 0x16ce8, 0x16ce8, 0x16cf0, 0x16cf0, 0x16cf0, 0x16cf0, 0x16cf8, 0x16cf8, 0x16cf8, 0x16cf8,
  0x16d00, 0x16d00, 0x16d00, 0x16d00, 0x16d08, 0x16d08, 0x16d08, 0x16d08, 0x16d10, 0x16d10, 0x16d10, 0x16d10, 0x16d18, 0x16d18, 0x16d18, 0x16d18,
  0x16d20, 0x16d20, 0x16d20, 0x16d20, 0x16d28, 0x16d28, 0x16d28, 0x16d28, 0x16d30, 0x16d30, 0x16d30, 0x16d30, 0x16d38, 0x16d38, 0x16d38, 0x16d38,
  0x16cc0, 0x16cc0, 0x16cc0, 0x16cc0, 0x16cc8, 0x16cc8, 0x16cc8, 0x16cc8, 0x16cd0, 0x16cd0, 0x16cd0, 0x16cd0, 0x16cd8, 0x16cd8, 0x16cd8, 0x16cd8,
  0x16ce0, 0x16ce0, 0x16ce0, 0x16ce0, 0x16ce8, 0x16ce8, 0x16ce8, 0x16ce8, 0x16cf0, 0x16cf0, 0x16cf0, 0x16cf0, 0x16cf8, 0x16cf8, 0x16cf8, 0x16cf8,
  0x16d00, 0x16d00, 0x16d00, 0x16d00, 0x16d08, 0x16d08, 0x16d08, 0x16d08, 0x16d10, 0x16d10, 0x16d10, 0x16d10, 0x16d18, 0x16d18, 0x16d18, 0x16d18,
  0x16d20, 0x16d20, 0x16d20, 0x16d20, 0x16d28, 0x16d28, 0x16d28, 0x16d28, 0x16d30, 0x16d30, 0x16d30, 0x16d30, 0x16d38, 0x16d38, 0x16d38, 0x16d38,
  0x16cc0, 0x16cc0, 0x16cc0, 0x16cc0, 0x16cc8, 0x16cc8, 0x16cc8, 0x16cc8, 0x16cd0, 0x16cd0, 0x16cd0, 0x16cd0, 0x16cd8, 0x16cd8, 0x16cd8, 0x16cd8,
  0x16ce0, 0x16ce0, 0x16ce0, 0x16ce0, 0x16ce8, 0x16ce8, 0x16ce8, 0x16ce8, 0x16cf0, 0x16cf0, 0x16cf0, 0x16cf0, 0x16cf8, 0x16cf8, 0x16cf8, 0x16cf8,
  0x16d00, 0x16d00, 0x16d00, 0x16d00, 0x16d08, 0x16d08, 0x16d08, 0x16d08, 0x16d10, 0x16d10, 0x16d10, 0x16d10, 0x16d18, 0x16d18, 0x16d18, 0x16d18,
  0x16d20, 0x16d20, 0x16d20, 0x16d20, 0x16d28, 0x16d28, 0x16d28, 0x16d28, 0x16d30, 0x16d30, 0x16d30, 0x16d30, 0x16d38, 0x16d38, 0x16d38, 0x16d38,
  0x16cc0, 0x16cc0, 0x3020100, 0x16500, 0x16cc8, 0x16cc8, 0x3020100, 0x16500, 0x16cd0, 0x16cd0, 0x3020100, 0x16500, 0x16cd8, 0x16cd8, 0x3020100, 0x16500,
  0x16ce0, 0x16ce0, 0x3020100, 0x16500, 0x16ce8, 0x16ce8, 0x3020100, 0x16500, 0x16cf0, 0x16cf0, 0x3020100, 0x16500, 0x16cf8, 0x16cf8, 0x3020100, 0x16500,
  0x16d00, 0x16d00, 0x3020100, 0x16500, 0x16d08, 0x16d08, 0x3020100, 0x16500, 0x16d10, 0x16d10, 0x3020100, 0x16500, 0x16d18, 0x16d18, 0x3020100, 0x16500,
  0x16d20, 0x16d20, 0x3020100, 0x16500, 0x16d28, 0x16d28, 0x3020100, 0x16500, 0x16d30, 0x16d30, 0x3020100, 0x16500, 0x16d38, 0x16d38, 0x3020100, 0x16500,
  0x16cc0, 0x16cc0, 0x3020100, 0x16500, 0x16cc8, 0x16cc8, 0x3020100, 0x16500, 0x16cd0, 0x16cd0, 0x3020100, 0x16500, 0x16cd8, 0x16cd8, 0x3020100, 0x16500,
  0x16ce0, 0x16ce0, 0x3020100, 0x16500, 0x16ce8, 0x16ce8, 0x3020100, 0x16500, 0x16cf0, 0x16cf0, 0x3020100, 0x16500, 0x16cf8, 0x16cf8, 0x3020100, 0x16500,
  0x16d00, 0x16d00, 0x3020100, 0x16500, 0x16d08, 0x16d08, 0x3020100, 0x16500, 0x16d10, 0x16d10, 0x3020100, 0x16500, 0x16d18, 0x16d18, 0x3020100, 0x16500,
  0x16d20, 0x16d20, 0x3020100, 0x16500, 0x16d28, 0x16d28, 0x3020100, 0x16500, 0x16d30, 0x16d30, 0x3020100, 0x16500, 0x16d38, 0x16d38, 0x3020100, 0x16500,
  0x16f5c, 0x16f64, 0x3020100, 0x16500, 0x1705c, 0x17064, 0x3020100, 0x16500, 0x16f6c, 0x16f74, 0x3020100, 0x16500, 0x1706c, 0x17074, 0x3020100, 0x16500,
  0x16f7c, 0x16f84, 0x3020100, 0x16500, 0x1707c, 0x17084, 0x3020100, 0x16500, 0x16f8c, 0x16f94, 0x3020100, 0x16500, 0x1708c, 0x17094, 0x3020100, 0x16500,
  0x16f9c, 0x16fa4, 0x3020100, 0x16500, 0x1709c, 0x170a4, 0x3020100, 0x16500, 0x16fac, 0x16fb4, 0x3020100, 0x16500, 0x170ac, 0x170b4, 0x3020100, 0x16500,
  0x1703c, 0x17044, 0x3020100, 0x16500, 0x1713c, 0x17144, 0x3020100, 0x16500, 0x1704c, 0x17054, 0x3020100, 0x16500, 0x1714c, 0x17154, 0x3020100, 0x16500,
  0x1715c, 0x16cc0, 0x5040504, 0x16500, 0x16cc8, 0x16cc8, 0x3020100, 0x16500, 0x16cd0, 0x16cd0, 0x3020100, 0x16500, 0x16cd8, 0x16cd8, 0x3020100, 0x16500,
  0x16ce0, 0x16ce0, 0x3020100, 0x16500, 0x16ce8, 0x16ce8, 0x3020100, 0x16500, 0x16cf0, 0x16cf0, 0x3020100, 0x16500, 0x16cf8, 0x16cf8, 0x3020100, 0x16500,
  0x16d00, 0x16d00, 0x3020100, 0x16500, 0x16d08, 0x16d08, 0x3020100, 0x16500, 0x16d10, 0x16d10, 0x3020100, 0x16500, 0x16d18, 0x16d18, 0x3020100, 0x16500,
  0x16d20, 0x16d20, 0x3020100, 0x16500, 0x16d28, 0x16d28, 0x3020100, 0x16500, 0x16d30, 0x16d30, 0x3020100, 0x16500, 0x16d38, 0x16d38, 0x3020100, 0x16500,
  0x16580, 0x3020100, 0x7060504, 0x3020100, 0x16680, 0x3020100, 0x7060504, 0x3020100, 0x16590, 0x3020100, 0x7060504, 0x3020100, 0x16690, 0x3020100, 0x7060504, 0x3020100,
  0x165a0, 0x3020100, 0x7060504, 0x3020100, 0x166a0, 0x3020100, 0x7060504, 0x3020100, 0x165b0, 0x3020100, 0x7060504, 0x3020100, 0x166b0, 0x3020100, 0x7060504, 0x3020100,
  0x165c0, 0x3020100, 0x7060504, 0x3020100, 0x166c0, 0x3020100, 0x7060504, 0x3020100, 0x165d0, 0x3020100, 0x7060504, 0x3020100, 0x166d0, 0x3020100, 0x7060504, 0x3020100,
  0x165e0, 0x3020100, 0x7060504, 0x3020100, 0x166e0, 0x3020100, 0x7060504, 0x3020100, 0x165f0, 0x3020100, 0x7060504, 0x3020100, 0x166f0, 0x3020100, 0x7060504, 0x3020100,
  0x16588, 0x3020100, 0x7060504, 0x3020100, 0x16688, 0x3020100, 0x7060504, 0x3020100, 0x16598, 0x3020100, 0x7060504, 0x3020100, 0x16698, 0x3020100, 0x7060504, 0x3020100,
  0x165a8, 0x3020100, 0x7060504, 0x3020100, 0x166a8, 0x3020100, 0x7060504, 0x3020100, 0x165b8, 0x3020100, 0x7060504, 0x3020100, 0x166b8, 0x3020100, 0x7060504, 0x3020100,
  0x165c8, 0x3020100, 0x7060504, 0x3020100, 0x166c8, 0x3020100, 0x7060504, 0x3020100, 0x165d8, 0x3020100, 0x7060504, 0x3020100, 0x166d8, 0x3020100, 0x7060504, 0x3020100,
  0x165e8, 0x3020100, 0x7060504, 0x3020100, 0x166e8, 0x3020100, 0x7060504, 0x3020100, 0x165f8, 0x3020100, 0x7060504, 0x3020100, 0x166f8, 0x3020100, 0x7060504, 0x3020100,
  0x16780, 0x1000100, 0x5040302, 0x5040100, 0x16880, 0x1000100, 0x5040302, 0x5040100, 0x16790, 0x1000100, 0x5040302, 0x5040100, 0x16890, 0x1000100, 0x5040302, 0x5040100,
  0x167a0, 0x1000100, 0x5040302, 0x5040100, 0x168a0, 0x1000100, 0x5040302, 0x5040100, 0x167b0, 0x1000100, 0x5040302, 0x5040100, 0x168b0, 0x1000100, 0x5040302, 0x5040100,
  0x167c0, 0x1000100, 0x5040302, 0x5040100, 0x168c0, 0x1000100, 0x5040302, 0x5040100, 0x167d0, 0x1000100, 0x5040302, 0x5040100, 0x168d0, 0x1000100, 0x5040302, 0x5040100,
  0x167e0, 0x1000100, 0x5040302, 0x5040100, 0x168e0, 0x1000100, 0x5040302, 0x5040100, 0x167f0, 0x1000100, 0x5040302, 0x5040100, 0x168f0, 0x1000100, 0x5040302, 0x5040100,
  0x16cc0, 0x3020100, 0x7060504, 0x3020100, 0x16cc8, 0x3020100, 0x7060504, 0x3020100, 0x16cd0, 0x3020100, 0x7060504, 0x3020100, 0x16cd8, 0x3020100, 0x7060504, 0x3020100,
  0x16ce0, 0x3020100, 0x7060504, 0x3020100, 0x16ce8, 0x3020100, 0x7060504, 0x3020100, 0x16cf0, 0x3020100, 0x7060504, 0x3020100, 0x16cf8, 0x3020100, 0x7060504, 0x3020100,
  0x16d00, 0x3020100, 0x7060504, 0x3020100, 0x16d08, 0x3020100, 0x7060504, 0x3020100, 0x16d10, 0x3020100, 0x7060504, 0x3020100, 0x16d18, 0x3020100, 0x7060504, 0x3020100,
  0x16d20, 0x3020100, 0x7060504, 0x3020100, 0x16d28, 0x3020100, 0x7060504, 0x3020100, 0x16d30, 0x3020100, 0x7060504, 0x3020100, 0x16d38, 0x3020100, 0x7060504, 0x3020100,
  0x16dbe, 0x16dc0, 0x16dc2, 0x16dc4, 0x16ebe, 0x16ec0, 0x16ec2, 0x16ec4, 0x16dce, 0x16dd0, 0x16dd2, 0x16dd4, 0x16ece, 0x16ed0, 0x16ed2, 0x16ed4,
  0x16dde, 0x16de0, 0x16de2, 0x16de4, 0x16ede, 0x16ee0, 0x16ee2, 0x16ee4, 0x16dee, 0x16df0, 0x16df2, 0x16df4, 0x16eee, 0x16ef0, 0x16ef2, 0x16ef4,
  0x16dfe, 0x16e00, 0x16e02, 0x16e04, 0x16efe, 0x16f00, 0x16f02, 0x16f04, 0x16e0e, 0x16e10, 0x16e12, 0x16e14, 0x16f0e, 0x16f10, 0x16f12, 0x16f14,
  0x16e1e, 0x16e20, 0x16e22, 0x16e24, 0x16f1e, 0x16f20, 0x16f22, 0x16f24, 0x16e2e, 0x16e30, 0x16e32, 0x16e34, 0x16f2e, 0x16f30, 0x16f32, 0x16f34,
  0x16dc6, 0x16dc8, 0x16dca, 0x16dcc, 0x16ec6, 0x16ec8, 0x16eca, 0x16ecc, 0x16dd6, 0x16dd8, 0x16dda, 0x16ddc, 0x16ed6, 0x16ed8, 0x16eda, 0x16edc,
  0x16de6, 0x16de8, 0x16dea, 0x16dec, 0x16ee6, 0x16ee8, 0x16eea, 0x16eec, 0x16df6, 0x16df8, 0x16dfa, 0x16dfc, 0x16ef6, 0x16ef8, 0x16efa, 0x16efc,
  0x16e06, 0x16e08, 0x16e0a, 0x16e0c, 0x16f06, 0x16f08, 0x16f0a, 0x16f0c, 0x16e16, 0x16e18, 0x16e1a, 0x16e1c, 0x16f16, 0x16f18, 0x16f1a, 0x16f1c,
  0x16e26, 0x16e28, 0x16e2a, 0x16e2c, 0x16f26, 0x16f28, 0x16f2a, 0x16f2c, 0x16e36, 0x16e38, 0x16e3a, 0x16e3c, 0x16f36, 0x16f38, 0x16f3a, 0x16f3c,
  0x1677e, 0x16788, 0x1678a, 0x1678c, 0x1687e, 0x16888, 0x1688a, 0x1688c, 0x1678e, 0x16798, 0x1679a, 0x1679c, 0x1688e, 0x16898, 0x1689a, 0x1689c,
  0x1679e, 0x167a8, 0x167aa, 0x167ac, 0x1689e, 0x168a8, 0x168aa, 0x168ac, 0x167ae, 0x167b8, 0x167ba, 0x167bc, 0x168ae, 0x168b8, 0x168ba, 0x168bc,
  0x167be, 0x167c8, 0x167ca, 0x167cc, 0x168be, 0x168c8, 0x168ca, 0x168cc, 0x167ce, 0x167d8, 0x167da, 0x167dc, 0x168ce, 0x168d8, 0x168da, 0x168dc,
  0x167de, 0x167e8, 0x167ea, 0x167ec, 0x168de, 0x168e8, 0x168ea, 0x168ec, 0x167ee, 0x167f8, 0x167fa, 0x167fc, 0x168ee, 0x168f8, 0x168fa, 0x168fc,
  0x169cc, 0x16a0c, 0x16aca, 0x16b0a, 0x169ec, 0x16a2c, 0x16aea, 0x16b2a, 0x169ce, 0x16a0e, 0x16acc, 0x16b0c, 0x169ee, 0x16a2e, 0x16aec, 0x16b2c,
  0x169d0, 0x16a10, 0x16ace, 0x16b0e, 0x169f0, 0x16a30, 0x16aee, 0x16b2e, 0x169d2, 0x16a12, 0x16ad0, 0x16b10, 0x169f2, 0x16a32, 0x16af0, 0x16b30,
  0x169d4, 0x16a14, 0x16ad2, 0x16b12, 0x169f4, 0x16a34, 0x16af2, 0x16b32, 0x169d6, 0x16a16, 0x16ad4, 0x16b14, 0x169f6, 0x16a36, 0x16af4, 0x16b34,
  0x169d8, 0x16a18, 0x16ad6, 0x16b16, 0x169f8, 0x16a38, 0x16af6, 0x16b36, 0x169da, 0x16a1a, 0x16ad8, 0x16b18, 0x169fa, 0x16a3a, 0x16af8, 0x16b38,
  0x16bca, 0x16c0a, 0x16c4a, 0x16c8a, 0x16bea, 0x16c2a, 0x16c6a, 0x16caa, 0x16bcc, 0x16c0c, 0x16c4c, 0x16c8c, 0x16bec, 0x16c2c, 0x16c6c, 0x16cac,
  0x16bce, 0x16c0e, 0x16c4e, 0x16c8e, 0x16bee, 0x16c2e, 0x16c6e, 0x16cae, 0x16bd0, 0x16c10, 0x16c50, 0x16c90, 0x16bf0, 0x16c30, 0x16c70, 0x16cb0,
  0x16bd2, 0x16c12, 0x16c52, 0x16c92, 0x16bf2, 0x16c32, 0x16c72, 0x16cb2, 0x16bd4, 0x16c14, 0x16c54, 0x16c94, 0x16bf4, 0x16c34, 0x16c74, 0x16cb4,
  0x16bd6, 0x16c16, 0x16c56, 0x16c96, 0x16bf6, 0x16c36, 0x16c76, 0x16cb6, 0x16bd8, 0x16c18, 0x16c58, 0x16c98, 0x16bf8, 0x16c38, 0x16c78, 0x16cb8,
  0x16cc0, 0x16cc0, 0x16cc0, 0x16cc0, 0x16cc8, 0x16cc8, 0x16cc8, 0x16cc8, 0x16cd0, 0x16cd0, 0x16cd0, 0x16cd0, 0x16cd8, 0x16cd8, 0x16cd8, 0x16cd8,
  0x16ce0, 0x16ce0, 0x16ce0, 0x16ce0, 0x16ce8, 0x16ce8, 0x16ce8, 0x16ce8, 0x16cf0, 0x16cf0, 0x16cf0, 0x16cf0, 0x16cf8, 0x16cf8, 0x16cf8, 0x16cf8,
  0x16d00, 0x16d00, 0x16d00, 0x16d00, 0x16d08, 0x16d08, 0x16d08, 0x16d08, 0x16d10, 0x16d10, 0x16d10, 0x16d10, 0x16d18, 0x16d18, 0x16d18, 0x16d18,
  0x16d20, 0x16d20, 0x16d20, 0x16d20, 0x16d28, 0x16d28, 0x16d28, 0x16d28, 0x16d30, 0x16d30, 0x16d30, 0x16d30, 0x16d38, 0x16d38, 0x16d38, 0x16d38,
  0x16cc0, 0x16cc0, 0x16cc0, 0x16cc0, 0x16cc8, 0x16cc8, 0x16cc8, 0x16cc8, 0x16cd0, 0x16cd0, 0x16cd0, 0x16cd0, 0x16cd8, 0x16cd8, 0x16cd8, 0x16cd8,
  0x16ce0, 0x16ce0, 0x16ce0, 0x16ce0, 0x16ce8, 0x16ce8, 0x16ce8, 0x16ce8, 0x16cf0, 0x16cf0, 0x16cf0, 0x16cf0, 0x16cf8, 0x16cf8, 0x16cf8, 0x16cf8,
  0x16d00, 0x16d00, 0x16d00, 0x16d00, 0x16d08, 0x16d08, 0x16d08, 0x16d08, 0x16d10, 0x16d10, 0x16d10, 0x16d10, 0x16d18, 0x16d18, 0x16d18, 0x16d18,
  0x16d20, 0x16d20, 0x16d20, 0x16d20, 0x16d28, 0x16d28, 0x16d28, 0x16d28, 0x16d30, 0x16d30, 0x16d30, 0x16d30, 0x16d38, 0x16d38, 0x16d38, 0x16d38,
  0x16cc0, 0x16cc0, 0x16cc0, 0x16cc0, 0x16cc8, 0x16cc8, 0x16cc8, 0x16cc8, 0x16cd0, 0x16cd0, 0x16cd0, 0x16cd0, 0x16cd8, 0x16cd8, 0x16cd8, 0x16cd8,
  0x16ce0, 0x16ce0, 0x16ce0, 0x16ce0, 0x16ce8, 0x16ce8, 0x16ce8, 0x16ce8, 0x16cf0, 0x16cf0, 0x16cf0, 0x16cf0, 0x16cf8, 0x16cf8, 0x16cf8, 0x16cf8,
  0x16d00, 0x16d00, 0x16d00, 0x16d00, 0x16d08, 0x16d08, 0x16d08, 0x16d08, 0x16d10, 0x16d10, 0x16d10, 0x16d10, 0x16d18, 0x16d18, 0x16d18, 0x16d18,
  0x16d20, 0x16d20, 0x16d20, 0x16d20, 0x16d28, 0x16d28, 0x16d28, 0x16d28, 0x16d30, 0x16d30, 0x16d30, 0x16d30, 0x16d38, 0x16d38, 0x16d38, 0x16d38,
  0x16cc0, 0x16cc0, 0x3020100, 0x16500, 0x16cc8, 0x16cc8, 0x3020100, 0x16500, 0x16cd0, 0x16cd0, 0x3020100, 0x16500, 0x16cd8, 0x16cd8, 0x3020100, 0x16500,
  0x16ce0, 0x16ce0, 0x3020100, 0x16500, 0x16ce8, 0x16ce8, 0x3020100, 0x16500, 0x16cf0, 0x16cf0, 0x3020100, 0x16500, 0x16cf8, 0x16cf8, 0x3020100, 0x16500,
  0x16d00, 0x16d00, 0x3020100, 0x16500, 0x16d08, 0x16d08, 0x3020100, 0x16500, 0x16d10, 0x16d10, 0x3020100, 0x16500, 0x16d18, 0x16d18, 0x3020100, 0x16500,
  0x16d20, 0x16d20, 0x3020100, 0x16500, 0x16d28, 0x16d28, 0x3020100, 0x16500, 0x16d30, 0x16d30, 0x3020100, 0x16500, 0x16d38, 0x16d38, 0x3020100, 0x16500,
  0x16cc0, 0x16cc0, 0x3020100, 0x16500, 0x16cc8, 0x16cc8, 0x3020100, 0x16500, 0x16cd0, 0x16cd0, 0x3020100, 0x16500, 0x16cd8, 0x16cd8, 0x3020100, 0x16500,
  0x16ce0, 0x16ce0, 0x3020100, 0x16500, 0x16ce8, 0x16ce8, 0x3020100, 0x16500, 0x16cf0, 0x16cf0, 0x3020100, 0x16500, 0x16cf8, 0x16cf8, 0x3020100, 0x16500,
  0x16d00, 0x16d00, 0x3020100, 0x16500, 0x16d08, 0x16d08, 0x3020100, 0x16500, 0x16d10, 0x16d10, 0x3020100, 0x16500, 0x16d18, 0x16d18, 0x3020100, 0x16500,
  0x16d20, 0x16d20, 0x3020100, 0x16500, 0x16d28, 0x16d28, 0x3020100, 0x16500, 0x16d30, 0x16d30, 0x3020100, 0x16500, 0x16d38, 0x16d38, 0x3020100, 0x16500,
  0x16fbc, 0x16fc4, 0x3020100, 0x16500, 0x170bc, 0x170c4, 0x3020100, 0x16500, 0x16fcc, 0x16fd4, 0x3020100, 0x16500, 0x170cc, 0x170d4, 0x3020100, 0x16500,
  0x16fdc, 0x16fe4, 0x3020100, 0x16500, 0x170dc, 0x170e4, 0x3020100, 0x16500, 0x16fec, 0x16ff4, 0x3020100, 0x16500, 0x170ec, 0x170f4, 0x3020100, 0x16500,
  0x16ffc, 0x17004, 0x3020100, 0x16500, 0x170fc, 0x17104, 0x3020100, 0x16500, 0x1700c, 0x17014, 0x3020100, 0x16500, 0x1710c, 0x17114, 0x3020100, 0x16500,
  0x1701c, 0x17024, 0x3020100, 0x16500, 0x1711c, 0x17124, 0x3020100, 0x16500, 0x1702c, 0x17034, 0x3020100, 0x16500, 0x1712c, 0x17134, 0x3020100, 0x16500,
  0x16cc0, 0x16cc0, 0x3020100, 0x16500, 0x16cc8, 0x16cc8, 0x3020100, 0x16500, 0x16cd0, 0x16cd0, 0x3020100, 0x16500, 0x16cd8, 0x16cd8, 0x3020100, 0x16500,
  0x16ce0, 0x16ce0, 0x3020100, 0x16500, 0x16ce8, 0x16ce8, 0x3020100, 0x16500, 0x16cf0, 0x16cf0, 0x3020100, 0x16500, 0x16cf8, 0x16cf8, 0x3020100, 0x16500,
  0x16d00, 0x16d00, 0x3020100, 0x16500, 0x16d08, 0x16d08, 0x3020100, 0x16500, 0x16d10, 0x16d10, 0x3020100, 0x16500, 0x16d18, 0x16d18, 0x3020100, 0x16500,
  0x16d20, 0x16d20, 0x3020100, 0x16500, 0x16d28, 0x16d28, 0x3020100, 0x16500, 0x16d30, 0x16d30, 0x3020100, 0x16500, 0x16d38, 0x16d38, 0x3020100, 0x16500,
  0x0, 0x0, 0x0, 0x0, 0x0, 0x0, 0x0, 0x0, 0x0, 0x0, 0x0, 0x0, 0x0, 0x0, 0x0, 0x0,
  0x0, 0x0, 0x0, 0x0, 0x0, 0x0, 0x0, 0x0, 0x0, 0x0, 0x0, 0x0, 0x0, 0x0, 0x0, 0x0,
  0x0, 0x0, 0x0, 0x0, 0x0, 0x0, 0x0, 0x0, 0x0, 0x0, 0x0, 0x0, 0x0, 0x0, 0x0, 0x0,
  0x0, 0x0, 0x0, 0x0, 0x0, 0x0, 0x0, 0x0, 0x0, 0x0, 0x0, 0x0, 0x0, 0x0, 0x0, 0x0,
  0x0, 0x0, 0x0, 0x0, 0x0, 0x0, 0x0, 0x0, 0x0, 0x0, 0x0, 0x0, 0x0, 0x0, 0x0, 0x0,
  0x0, 0x0, 0x0, 0x0, 0x0, 0x0, 0x0, 0x0, 0x0, 0x0, 0x0, 0x0, 0x0, 0x0, 0x0, 0x0,
  0x0, 0x0, 0x0, 0x0, 0x0, 0x0, 0x0, 0x0, 0x0, 0x0, 0x0, 0x0, 0x0, 0x0, 0x0, 0x0,
  0x0, 0x0, 0x0, 0x0, 0x0, 0x0, 0x0, 0x0, 0x0, 0x0, 0x0, 0x0, 0x0, 0x0, 0x0, 0x0,
  0x0, 0x0, 0x0, 0x0, 0x0, 0x0, 0x0, 0x0, 0x0, 0x0, 0x0, 0x0, 0x0, 0x0, 0x0, 0x0,
  0x0, 0x0, 0x0, 0x0, 0x0, 0x0, 0x0, 0x0, 0x0, 0x0, 0x0, 0x0, 0x0, 0x0, 0x0, 0x0,
  0x0, 0x0, 0x0, 0x0, 0x0, 0x0, 0x0, 0x0, 0x0, 0x0, 0x0, 0x0, 0x0, 0x0, 0x0, 0x0,
  0x0, 0x0, 0x0, 0x0, 0x0, 0x0, 0x0, 0x0, 0x0, 0x0, 0x0, 0x0, 0x0, 0x0, 0x0, 0x0,
  0x0, 0x0, 0x0, 0x0, 0x0, 0x0, 0x0, 0x0, 0x0, 0x0, 0x0, 0x0, 0x0, 0x0, 0x0, 0x0,
  0x0, 0x0, 0x0, 0x0, 0x0, 0x0, 0x0, 0x0, 0x0, 0x0, 0x0, 0x0, 0x0, 0x0, 0x0, 0x0,
  0x0, 0x0, 0x0, 0x0, 0x0, 0x0, 0x0, 0x0, 0x0, 0x0, 0x0, 0x0, 0x0, 0x0, 0x0, 0x0,
  0x0, 0x0, 0x0, 0x0, 0x0, 0x0, 0x0, 0x0, 0x0, 0x0, 0x0, 0x0, 0x0, 0x0, 0x0, 0x0
};

__global__ void __launch_bounds__(1024) mega(Args) {
  asm volatile(
    "s_memrealtime s[96:97]\n"
    "s_load_dwordx16 s[4:19], s[0:1], 0\n"
    "s_load_dwordx16 s[20:35], s[0:1], 64\n"
    "v_readfirstlane_b32 s37, v0\n"
    "s_getpc_b64 s[40:41]\n"
    "K0:\n"
    "s_lshr_b32 s37, s37, 6\n"
    "s_cmpk_ge_u32 s2, 192\n"
    "s_cselect_b32 s42, 16, 0\n"
    "s_add_u32 s42, s42, s37\n"
    "s_lshl2_add_u32 s40, s42, s40\n"
    "s_add_u32 s40, s40, TBL1-K0\n"
    "s_mov_b32 s39, 0x100000\n"
    "s_setpc_b64 s[40:41]\n"
    "TBL1:\n"
    "s_branch GA0\n"
    "s_branch GA0\n"
    "s_branch GA0\n"
    "s_branch GA0\n"
    "s_branch GA0\n"
    "s_branch GA0\n"
    "s_branch S0\n"
    "s_branch S0\n"
    "s_branch S0\n"
    "s_branch S0\n"
    "s_branch S0\n"
    "s_branch S0\n"
    "s_branch MD0\n"
    "s_branch AUX0\n"
    "s_branch AUX0\n"
    "s_branch AUX0\n"
    "s_branch AUX0\n"
    "s_branch AUX0\n"
    "s_branch AUX0\n"
    "s_branch AUX0\n"
    "s_branch SIL0\n"
    "s_branch SIL0\n"
    "s_branch S0\n"
    "s_branch S0\n"
    "s_branch S0\n"
    "s_branch S0\n"
    "s_branch S0\n"
    "s_branch S0\n"
    "s_branch SIL0\n"
    "s_branch SIL0\n"
    "s_branch SIL0\n"
    "s_branch MD0\n"
    "TBL2:\n"
    "s_branch PAD0\n"
    "s_branch PAD1\n"
    "s_branch PAD2\n"
    "s_branch PAD3\n"
    "s_branch PAD4\n"
    "s_branch PAD5\n"
    "s_branch PAD6\n"
    "s_branch PAD7\n"
    "s_branch PAD8\n"
    "s_branch PAD9\n"
    "s_branch PAD10\n"
    "s_branch PAD11\n"
    "s_branch PAD12\n"
    "s_branch PAD13\n"
    "s_branch PAD14\n"
    "s_branch PAD15\n"
    "s_branch PAD16\n"
    "s_branch PAD17\n"
    "s_branch PAD18\n"
    "s_branch PAD19\n"
    "s_branch PAD20\n"
    "s_branch PAD21\n"
    "s_branch S1\n"
    "s_branch S1\n"
    "s_branch S1\n"
    "s_branch S1\n"
    "s_branch S1\n"
    "s_branch S1\n"
    "s_branch PAD28\n"
    "s_branch PAD29\n"
    "s_branch PAD30\n"
    "s_branch MD1\n"
    "AUX0:\n"
    "v_and_b32 v1, 31, v0\n"
    "v_lshlrev_b32 v1, 4, v1\n"
    "v_mov_b32 v4, 0\n"
    "v_mov_b32 v5, 0\n"
    "v_mov_b32 v6, 0\n"
    "v_mov_b32 v7, 0\n"
    "v_add_u32 v1, 147840, v1\n"
    "ds_write_b128 v1, v[4:7]\n"
    "s_cmpk_ge_u32 s2, 16\n"
    "s_cbranch_scc1 AUX0e\n"
    "s_and_b32 s42, s2, 8\n"
    "s_mul_i32 s42, s42, 24\n"
    "s_addk_i32 s42, 7-832\n"
    "v_add_u32 v1, s42, v0\n"
    "v_min_u32 v1, (END-K0)/128, v1\n"
    "v_lshlrev_b32 v1, 7, v1\n"
    "s_mov_b32 m0, 148480\n"
    "s_nop 0\n"
    "global_load_lds_dword v1, s[40:41]\n"
    "AUX0e:\n"
    "s_add_u32 s40, s40, 128\n"
    "s_mov_b64 exec, 0\n"
    "s_setpc_b64 s[40:41]\n"
    "S0:\n"
    "s_setprio 3\n"
    "v_and_b32 v2, 3, v0\n"
    "v_lshrrev_b32 v3, 2, v0\n"
    "v_mul_u32_u24 v2, 0x900, v2\n"
    "s_mul_i32 s42, s2, 9216\n"
    "v_mad_u32_u24 v1, v3, 12, v2\n"
    "v_add_u32 v1, s42, v1\n"
    "s_waitcnt lgkmcnt(0)\n"
    "global_load_dwordx3 v[4:6], v1, s[4:5] offset:-1152 nt\n"
    "global_load_dwordx3 v[8:10], v1, s[4:5] nt\n"
    "s_load_dword s36, s[16:17], 0\n"
    "s_add_u32 s40, s40, 128\n"
    "s_mov_b64 exec, 0\n"
    "s_setpc_b64 s[40:41]\n"
    "GA0:\n"
    "s_and_b32 s44, s2, 7\n"
    "s_lshr_b32 s45, s2, 3\n"
    "s_mul_i32 s44, s44, 24\n"
    "s_add_u32 s38, s44, s45\n"
    "s_lshr_b32 s42, s37, 1\n"
    "s_mul_i32 s43, s38, 3\n"
    "s_add_u32 s43, s43, s42\n"
    "s_lshl_b32 s44, s43, 8\n"
    "s_lshl_b32 s45, s43, 1\n"
    "s_and_b32 s45, s45, -4\n"
    "v_and_b32 v1, 63, v0\n"
    "v_lshl_add_u32 v2, v1, 2, s44\n"
    "s_waitcnt lgkmcnt(0)\n"
    "global_load_dword v4, v2, s[6:7] nt\n"
    "global_load_dword v5, v2, s[8:9] nt\n"
    "s_load_dword s46, s[10:11], s45\n"
    "global_load_dword v6, v2, s[20:21] nt\n"
    "global_load_dword v7, v2, s[22:23] nt\n"
    "s_load_dword s36, s[16:17], 0\n"
    "s_add_u32 s40, s40, 128\n"
    "s_mov_b64 exec, 0\n"
    "s_setpc_b64 s[40:41]\n"
    "SIL0:\n"
    "s_cmpk_ge_u32 s37, 12\n"
    "s_cselect_b32 s42, 384, 0\n"
    "s_addk_i32 s42, 256\n"
    "v_subrev_u32 v1, s42, v0\n"
    "v_min_u32 v1, 287, v1\n"
    "v_lshlrev_b32 v1, 2, v1\n"
    "s_waitcnt lgkmcnt(0)\n"
    "global_load_dword v4, v1, s[12:13]\n"
    "s_load_dword s36, s[16:17], 0\n"
    "s_add_u32 s40, s40, 128\n"
    "s_mov_b64 exec, 0\n"
    "s_setpc_b64 s[40:41]\n"
    "MD0:\n"
    "s_setprio 3\n"
    "s_mul_i32 s43, s2, 19\n"
    "s_lshr_b32 s43, s43, 9\n"
    "s_mul_i32 s44, s43, 27\n"
    "s_sub_u32 s44, s2, s44\n"
    "s_mul_i32 s45, s43, 442368\n"
    "s_lshl_b32 s46, s44, 7\n"
    "s_add_u32 s45, s45, s46\n"
    "v_bfe_u32 v1, v0, 3, 3\n"
    "v_and_b32 v2, 2, v1\n"
    "v_mul_u32_u24 v1, 0x1b00, v1\n"
    "v_xor_b32 v2, v2, v0\n"
    "v_and_b32 v2, 7, v2\n"
    "v_lshl_add_u32 v1, v2, 4, v1\n"
    "v_add_u32 v1, s45, v1\n"
    "v_xor_b32 v2, 64, v1\n"
    "v_add_u32 v2, 55296, v2\n"
    "s_lshl_b32 s46, s43, 7\n"
    "v_and_b32 v3, 31, v0\n"
    "v_lshl_add_u32 v5, v3, 2, s46\n"
    "s_mov_b32 m0, 138240\n"
    "s_cmpk_ge_u32 s2, 243\n"
    "s_waitcnt lgkmcnt(0)\n"
    "s_cbranch_scc1 MD0e\n"
    "global_load_dword v4, v5, s[12:13]\n"
    "global_load_lds_dwordx4 v1, s[14:15] nt\n"
    "s_add_u32 m0, m0, 1024\n"
    "v_add_u32 v1, 110592, v1\n"
    "global_load_lds_dwordx4 v2, s[14:15] nt\n"
    "s_add_u32 m0, m0, 1024\n"
    "v_add_u32 v2, 110592, v2\n"
    "global_load_lds_dwordx4 v1, s[14:15] nt\n"
    "s_add_u32 m0, m0, 1024\n"
    "v_add_u32 v1, 110592, v1\n"
    "global_load_lds_dwordx4 v2, s[14:15] nt\n"
    "s_add_u32 m0, m0, 1024\n"
    "v_add_u32 v2, 110592, v2\n"
    "global_load_lds_dwordx4 v1, s[14:15] nt\n"
    "s_add_u32 m0, m0, 1024\n"
    "v_add_u32 v1, 110592, v1\n"
    "global_load_lds_dwordx4 v2, s[14:15] nt\n"
    "s_add_u32 m0, m0, 1024\n"
    "v_add_u32 v2, 110592, v2\n"
    "global_load_lds_dwordx4 v1, s[14:15] nt\n"
    "s_add_u32 m0, m0, 1024\n"
    "s_nop 0\n"
    "global_load_lds_dwordx4 v2, s[14:15] nt\n"
    "MD0e:\n"
    "s_load_dword s36, s[16:17], 0\n"
    "s_add_u32 s40, s40, 128\n"
    "s_mov_b64 exec, 0\n"
    "s_setpc_b64 s[40:41]\n"
    "ABORT:\n"
    "s_endpgm\n"
    "PAD13:\n"
    "s_cbranch_execz AUX1\n"
    "PAD16:\n"
    "s_cbranch_execz M1\n"
    "S1:\n"
    "s_mov_b64 exec, -1\n"
    "s_barrier\n"
    "s_mul_i32 s42, s2, 12416\n"
    "s_add_u32 s42, s42, 6548480\n"
    "v_lshl_add_u32 v11, v0, 4, s42\n"
    "v_bfe_u32 v2, v0, 4, 2\n"
    "v_and_b32 v3, 0x3c3, v0\n"
    "v_and_b32 v12, 3, v0\n"
    "v_mad_u32_u24 v3, v12, 15, v3\n"
    "v_lshl_add_u32 v12, v2, 2, v3\n"
    "v_add_u32 v12, 147072, v12\n"
    "v_and_b32 v13, 31, v0\n"
    "v_lshlrev_b32 v13, 4, v13\n"
    "v_add_u32 v13, 147456, v13\n"
    "v_mov_b32 v14, 147968\n"
    "v_mov_b32 v15, 1\n"
    "s_mov_b32 s43, 0x3f803f80\n"
    "s_movk_i32 s44, 6144\n"
    "s_sub_u32 s47, s37, 8\n"
    "s_cmp_ge_u32 s47, 3\n"
    "s_cselect_b32 s48, 1, 0\n"
    "s_add_u32 s45, s47, 3\n"
    "s_cmp_lt_i32 s47, 0\n"
    "s_cselect_b32 s47, s45, s47\n"
    "s_cmp_eq_u32 s47, 3\n"
    "s_cselect_b32 s47, 0, s47\n"
    "s_mul_i32 s45, s47, 30464\n"
    "v_and_b32 v72, 3, v0\n"
    "v_mul_u32_u24 v72, 1088, v72\n"
    "v_bfe_u32 v73, v0, 2, 4\n"
    "v_lshlrev_b32 v73, 3, v73\n"
    "v_add3_u32 v72, v72, v73, s45\n"
    "s_mul_i32 s45, s48, 4352\n"
    "v_add_u32 v73, s45, v72\n"
    "s_lshl_b32 s45, s48, 12\n"
    "s_add_u32 s45, s45, 107520\n"
    "v_and_b32 v74, 63, v0\n"
    "v_lshl_add_u32 v74, v74, 4, s45\n"
    "s_lshl_b32 s45, s47, 2\n"
    "v_add_u32 v75, s45, v14\n"
    "s_lshl_b32 s45, s45, 1\n"
    "s_add_u32 s45, s45, 148416\n"
    "v_mov_b32 v76, s45\n"
    "s_mul_i32 s49, s47, 3328\n"
    "s_waitcnt vmcnt(1)\n"
    "v_dot2_f32_bf16 v16, v4, s43, 0\n"
    "v_dot2_f32_bf16 v17, v4, v4, 0\n"
    "v_dot2_f32_bf16 v16, v5, s43, v16\n"
    "v_dot2_f32_bf16 v17, v5, v5, v17\n"
    "v_dot2_f32_bf16 v16, v6, s43, v16\n"
    "v_dot2_f32_bf16 v17, v6, v6, v17\n"
    "s_waitcnt vmcnt(0)\n"
    "v_dot2_f32_bf16 v18, v8, s43, 0\n"
    "v_dot2_f32_bf16 v19, v8, v8, 0\n"
    "v_dot2_f32_bf16 v18, v9, s43, v18\n"
    "v_dot2_f32_bf16 v19, v9, v9, v19\n"
    "v_dot2_f32_bf16 v18, v10, s43, v18\n"
    "v_dot2_f32_bf16 v19, v10, v10, v19\n"
    "v_lshlrev_b32 v24, 16, v4\n"
    "v_lshlrev_b32 v25, 16, v8\n"
    "v_and_b32 v26, 0xffff0000, v4\n"
    "v_permlane32_swap_b32 v16, v18\n"
    "v_permlane32_swap_b32 v17, v19\n"
    "v_add_f32 v16, v16, v18\n"
    "v_add_f32 v17, v17, v19\n"
    "v_and_b32 v27, 0xffff0000, v8\n"
    "v_lshlrev_b32 v28, 16, v5\n"
    "v_permlane16_swap_b32 v16, v17\n"
    "v_add_f32 v16, v16, v17\n"
    "v_lshlrev_b32 v29, 16, v9\n"
    "v_and_b32 v30, 0xffff0000, v5\n"
    "v_add_f32_dpp v16, v16, v16 row_ror:8 row_mask:0xf bank_mask:0xf\n"
    "v_and_b32 v31, 0xffff0000, v9\n"
    "v_lshlrev_b32 v32, 16, v6\n"
    "v_add_f32_dpp v16, v16, v16 row_ror:4 row_mask:0xf bank_mask:0xf\n"
    "ds_write_b32 v12, v16\n"
    "s_mov_b64 exec, 1\n"
    "ds_add_u32 v14, v15 offset:0\n"
    "s_mov_b64 exec, -1\n"
    "v_lshlrev_b32 v33, 16, v10\n"
    "v_and_b32 v34, 0xffff0000, v6\n"
    "v_and_b32 v35, 0xffff0000, v10\n"
    "s_branch E_2\n"
    "R_1:\n"
    "s_sub_u32 s39, s39, 1\n"
    "s_cbranch_scc1 ABORT\n"
    "E_2:\n"
    "ds_read_b32 v20, v14 offset:0\n"
    "ds_read_b128 v[16:19], v13\n"
    "s_waitcnt lgkmcnt(0)\n"
    "v_cmp_ne_u32 vcc, 6, v20\n"
    "s_cbranch_vccnz R_1\n"
    "s_add_u32 s36, s36, 1\n"
    "v_permlane16_swap_b32 v16, v18\n"
    "v_permlane16_swap_b32 v17, v19\n"
    "v_add_f32 v16, v16, v18\n"
    "v_add_f32 v17, v17, v19\n"
    "v_mov_b32 v39, s36\n"
    "v_add_f32_dpp v16, v16, v16 row_ror:8 row_mask:0xf bank_mask:0xf\n"
    "v_add_f32_dpp v17, v17, v17 row_ror:8 row_mask:0xf bank_mask:0xf\n"
    "v_mov_b32 v43, s36\n"
    "v_add_f32_dpp v16, v16, v16 row_ror:4 row_mask:0xf bank_mask:0xf\n"
    "v_add_f32_dpp v17, v17, v17 row_ror:4 row_mask:0xf bank_mask:0xf\n"
    "v_mul_f32 v16, 0x3ae38e39, v16\n"
    "v_mul_f32 v17, 0x3ae38e39, v17\n"
    "v_fma_f32 v17, -v16, v16, v17\n"
    "v_max_f32 v17, 0, v17\n"
    "v_cvt_pk_bf16_f32 v17, 0, v17\n"
    "v_add_f32 v17, 0x3727c5ac, v17\n"
    "v_cvt_pk_bf16_f32 v17, 0, v17\n"
    "v_sqrt_f32 v17, v17\n"
    "s_nop 0\n"
    "v_cvt_pk_bf16_f32 v16, v16, v17\n"
    "v_mov_b32 v17, v16\n"
    "s_nop 1\n"
    "v_permlane16_swap_b32 v16, v17\n"
    "v_and_b32 v20, 0xffff0000, v16\n"
    "v_and_b32 v21, 0xffff0000, v17\n"
    "v_lshlrev_b32 v18, 16, v16\n"
    "v_lshlrev_b32 v19, 16, v17\n"
    "v_rcp_f32 v20, v20\n"
    "v_rcp_f32 v21, v21\n"
    "v_pk_add_f32 v[24:25], v[24:25], v[18:19] neg_lo:[0,1] neg_hi:[0,1]\n"
    "v_pk_add_f32 v[26:27], v[26:27], v[18:19] neg_lo:[0,1] neg_hi:[0,1]\n"
    "v_pk_add_f32 v[28:29], v[28:29], v[18:19] neg_lo:[0,1] neg_hi:[0,1]\n"
    "v_cvt_pk_bf16_f32 v24, 0, v24\n"
    "v_cvt_pk_bf16_f32 v25, 0, v25\n"
    "v_cvt_pk_bf16_f32 v26, 0, v26\n"
    "v_cvt_pk_bf16_f32 v27, 0, v27\n"
    "v_cvt_pk_bf16_f32 v28, 0, v28\n"
    "v_cvt_pk_bf16_f32 v29, 0, v29\n"
    "v_pk_mul_f32 v[24:25], v[24:25], v[20:21]\n"
    "v_pk_mul_f32 v[26:27], v[26:27], v[20:21]\n"
    "v_pk_mul_f32 v[28:29], v[28:29], v[20:21]\n"
    "v_cvt_pk_bf16_f32 v36, v24, v25\n"
    "v_cvt_pk_bf16_f32 v37, v26, v27\n"
    "v_cvt_pk_bf16_f32 v38, v28, v29\n"
    "buffer_store_dwordx4 v[36:39], v11, s[16:19], 0 offen sc1\n"
    "v_pk_add_f32 v[30:31], v[30:31], v[18:19] neg_lo:[0,1] neg_hi:[0,1]\n"
    "v_pk_add_f32 v[32:33], v[32:33], v[18:19] neg_lo:[0,1] neg_hi:[0,1]\n"
    "v_pk_add_f32 v[34:35], v[34:35], v[18:19] neg_lo:[0,1] neg_hi:[0,1]\n"
    "v_cvt_pk_bf16_f32 v30, 0, v30\n"
    "v_cvt_pk_bf16_f32 v31, 0, v31\n"
    "v_cvt_pk_bf16_f32 v32, 0, v32\n"
    "v_cvt_pk_bf16_f32 v33, 0, v33\n"
    "v_cvt_pk_bf16_f32 v34, 0, v34\n"
    "v_cvt_pk_bf16_f32 v35, 0, v35\n"
    "v_pk_mul_f32 v[30:31], v[30:31], v[20:21]\n"
    "v_pk_mul_f32 v[32:33], v[32:33], v[20:21]\n"
    "v_pk_mul_f32 v[34:35], v[34:35], v[20:21]\n"
    "v_cvt_pk_bf16_f32 v40, v30, v31\n"
    "v_cvt_pk_bf16_f32 v41, v32, v33\n"
    "v_cvt_pk_bf16_f32 v42, v34, v35\n"
    "buffer_store_dwordx4 v[40:43], v11, s[16:19], s44 offen sc1\n"
    "s_setprio 0\n"
    "s_cmpk_ge_u32 s2, 192\n"
    "s_cbranch_scc1 ES1\n"
    "s_branch KST\n"
    "PAD12:\n"
    "s_cbranch_execz MD1\n"
    "PAD17:\n"
    "s_cbranch_execz M1\n"
    "MD1:\n"
    "s_mov_b64 exec, -1\n"
    "s_barrier\n"
    "s_cmpk_ge_u32 s2, 243\n"
    "s_cbranch_scc1 M9\n"
    "v_bfe_u32 v5, v0, 4, 2\n"
    "v_lshlrev_b32 v1, 2, v3\n"
    "v_add_u32 v1, 149888, v1\n"
    "v_lshlrev_b32 v2, 4, v5\n"
    "v_add_u32 v2, 149888, v2\n"
    "v_bfe_u32 v6, v0, 2, 2\n"
    "v_bfe_u32 v7, v0, 1, 1\n"
    "v_and_b32 v8, 2, v6\n"
    "v_or_b32 v7, v7, v8\n"
    "v_and_b32 v8, 1, v5\n"
    "v_lshl_add_u32 v7, v8, 2, v7\n"
    "v_lshlrev_b32 v8, 10, v5\n"
    "v_lshl_add_u32 v8, v6, 7, v8\n"
    "v_lshl_add_u32 v8, v7, 4, v8\n"
    "v_and_b32 v7, 1, v0\n"
    "v_lshl_add_u32 v8, v7, 3, v8\n"
    "v_xor_b32 v9, 32, v8\n"
    "v_xor_b32 v10, 64, v8\n"
    "v_xor_b32 v11, 0x60, v8\n"
    "v_add_u32 v8, 138240, v8\n"
    "v_add_u32 v9, 138240, v9\n"
    "v_add_u32 v10, 138240, v10\n"
    "v_add_u32 v11, 138240, v11\n"
    "v_and_b32 v3, 63, v0\n"
    "v_lshlrev_b32 v3, 3, v3\n"
    "s_mul_i32 s43, s43, 13824\n"
    "s_lshl_b32 s44, s44, 9\n"
    "s_add_u32 s43, s43, s44\n"
    "s_add_u32 s43, s43, 9733120\n"
    "s_waitcnt vmcnt(8)\n"
    "v_lshlrev_b32 v5, 16, v4\n"
    "v_and_b32 v4, 0xffff0000, v4\n"
    "v_mul_f32 v6, 0xbfb8aa3b, v5\n"
    "v_mul_f32 v7, 0xbfb8aa3b, v4\n"
    "v_exp_f32 v6, v6\n"
    "v_exp_f32 v7, v7\n"
    "v_add_f32 v6, 1.0, v6\n"
    "v_add_f32 v7, 1.0, v7\n"
    "v_rcp_f32 v6, v6\n"
    "v_rcp_f32 v7, v7\n"
    "v_mul_f32 v5, v5, v6\n"
    "v_mul_f32 v4, v4, v7\n"
    "v_cvt_pk_bf16_f32 v4, v5, v4\n"
    "ds_write_b32 v1, v4\n"
    "ds_read_b128 v[12:15], v2\n"
    "ds_read_b128 v[16:19], v2 offset:64\n"
    "s_waitcnt lgkmcnt(0)\n"
    "s_add_u32 s36, s36, 1\n"
    "s_waitcnt vmcnt(4)\n"
    "ds_read_b64_tr_b16 v[40:41], v8 offset:0\n"
    "ds_read_b64_tr_b16 v[42:43], v8 offset:512\n"
    "ds_read_b64_tr_b16 v[44:45], v9 offset:0\n"
    "ds_read_b64_tr_b16 v[46:47], v9 offset:512\n"
    "ds_read_b64_tr_b16 v[48:49], v10 offset:0\n"
    "ds_read_b64_tr_b16 v[50:51], v10 offset:512\n"
    "ds_read_b64_tr_b16 v[52:53], v11 offset:0\n"
    "ds_read_b64_tr_b16 v[54:55], v11 offset:512\n"
    "s_waitcnt lgkmcnt(6)\n"
    "v_mfma_f32_16x16x32_bf16 v[20:23], v[12:15], v[40:43], 0\n"
    "s_waitcnt lgkmcnt(4)\n"
    "v_mfma_f32_16x16x32_bf16 v[24:27], v[12:15], v[44:47], 0\n"
    "s_waitcnt lgkmcnt(2)\n"
    "v_mfma_f32_16x16x32_bf16 v[28:31], v[12:15], v[48:51], 0\n"
    "s_waitcnt lgkmcnt(0)\n"
    "v_mfma_f32_16x16x32_bf16 v[32:35], v[12:15], v[52:55], 0\n"
    "s_waitcnt vmcnt(0)\n"
    "ds_read_b64_tr_b16 v[56:57], v8 offset:4096\n"
    "ds_read_b64_tr_b16 v[58:59], v8 offset:4608\n"
    "ds_read_b64_tr_b16 v[60:61], v9 offset:4096\n"
    "ds_read_b64_tr_b16 v[62:63], v9 offset:4608\n"
    "ds_read_b64_tr_b16 v[64:65], v10 offset:4096\n"
    "ds_read_b64_tr_b16 v[66:67], v10 offset:4608\n"
    "ds_read_b64_tr_b16 v[68:69], v11 offset:4096\n"
    "ds_read_b64_tr_b16 v[70:71], v11 offset:4608\n"
    "s_waitcnt lgkmcnt(6)\n"
    "v_mfma_f32_16x16x32_bf16 v[20:23], v[16:19], v[56:59], v[20:23]\n"
    "s_waitcnt lgkmcnt(4)\n"
    "v_mfma_f32_16x16x32_bf16 v[24:27], v[16:19], v[60:63], v[24:27]\n"
    "s_waitcnt lgkmcnt(2)\n"
    "v_mfma_f32_16x16x32_bf16 v[28:31], v[16:19], v[64:67], v[28:31]\n"
    "s_waitcnt lgkmcnt(0)\n"
    "v_mfma_f32_16x16x32_bf16 v[32:35], v[16:19], v[68:71], v[32:35]\n"
    "s_nop 1\n"
    "v_mov_b32 v21, s36\n"
    "s_nop 0\n"
    "v_mov_b32_dpp v20, v24 quad_perm:[0,1,2,3] row_mask:0x2 bank_mask:0xf\n"
    "s_nop 1\n"
    "v_mov_b32_dpp v20, v28 quad_perm:[0,1,2,3] row_mask:0x4 bank_mask:0xf\n"
    "s_nop 1\n"
    "v_mov_b32_dpp v20, v32 quad_perm:[0,1,2,3] row_mask:0x8 bank_mask:0xf\n"
    "buffer_store_dwordx2 v[20:21], v3, s[16:19], s43 offen sc1\n"
    "s_setprio 0\n"
    "s_cmpk_ge_u32 s2, 192\n"
    "s_cbranch_scc1 ES1\n"
    "s_branch AUX3\n"
    "PAD14:\n"
    "s_cbranch_execz AUX1\n"
    "GA1:\n"
    "s_mov_b64 exec, 1\n"
    "s_cmpk_lg_u32 s37, 0\n"
    "s_cbranch_scc1 GA2\n"
    "v_mov_b32 v8, 148448\n"
    "v_mov_b32 v9, s96\n"
    "ds_write_b32 v8, v9\n"
    "GA2:\n"
    "s_mov_b64 exec, -1\n"
    "s_barrier\n"
    "s_setprio 1\n"
    "s_mov_b32 s48, 0x01000c0c\n"
    "s_bitcmp1_b32 s37, 0\n"
    "s_cselect_b32 s47, 0x03020c0c, s48\n"
    "s_and_b32 s43, s43, 1\n"
    "s_lshl_b32 s43, s43, 4\n"
    "s_mul_i32 s44, s42, 30464\n"
    "s_and_b32 s45, s37, 1\n"
    "s_lshl_b32 s45, s45, 9\n"
    "s_add_u32 s44, s44, s45\n"
    "v_lshl_add_u32 v2, v1, 3, s44\n"
    "v_lshlrev_b32 v3, 4, v0\n"
    "v_add_u32 v3, 126368, v3\n"
    "s_lshl_b32 s45, s37, 2\n"
    "s_add_u32 s45, s45, 147952\n"
    "v_mov_b32 v12, s45\n"
    "s_lshl_b32 s45, s42, 2\n"
    "s_add_u32 s44, s45, 147968\n"
    "v_mov_b32 v13, s44\n"
    "s_lshl_b32 s45, s45, 1\n"
    "s_add_u32 s45, s45, 148416\n"
    "v_mov_b32 v14, s45\n"
    "v_mov_b32 v15, 1\n"
    "v_mov_b32 v8, 0x3fb8aa3b\n"
    "s_mov_b32 s48, 0x3e22f983\n"
    "s_mov_b32 s49, 0x31dc9c88\n"
    "s_mov_b32 s50, 0x41000000\n"
    "s_mov_b32 s51, 0x43000000\n"
    "s_waitcnt lgkmcnt(0)\n"
    "s_lshr_b32 s46, s46, s43\n"
    "s_lshl_b32 s46, s46, 16\n"
    "s_add_u32 s36, s36, 1\n"
    "v_mul_f32 v9, s46, v8\n"
    "v_exp_f32 v9, v9\n"
    "s_waitcnt vmcnt(3)\n"
    "v_perm_b32 v4, v4, v4, s47\n"
    "v_mul_f32 v4, v4, v8\n"
    "v_cvt_pk_bf16_f32 v9, 0, v9\n"
    "v_exp_f32 v4, v4\n"
    "s_waitcnt vmcnt(2)\n"
    "v_perm_b32 v5, v5, v5, s47\n"
    "v_mul_f32 v5, v5, v9\n"
    "v_cvt_pk_bf16_f32 v4, 0, v4\n"
    "v_mul_f32 v10, s48, v5\n"
    "v_mul_f32 v4, v4, v9\n"
    "v_fma_f32 v11, v5, s48, -v10\n"
    "v_mul_f32_e64 v4, v4, -v8\n"
    "v_fma_f32 v11, v5, s49, v11\n"
    "v_mul_f32 v20, s50, v10\n"
    "v_mul_f32 v21, s50, v4\n"
    "v_fract_f32 v20, v20\n"
    "v_exp_f32 v21, v21\n"
    "v_fma_f32 v20, v11, s50, v20\n"
    "v_cos_f32 v16, v20\n"
    "v_sin_f32 v17, v20\n"
    "v_mul_f32 v22, s51, v10\n"
    "v_mul_f32 v23, s51, v4\n"
    "v_fract_f32 v22, v22\n"
    "v_exp_f32 v23, v23\n"
    "v_fma_f32 v22, v11, s51, v22\n"
    "v_cos_f32 v18, v22\n"
    "v_sin_f32 v19, v22\n"
    "v_pk_mul_f32 v[16:17], v[20:21], v[16:17] op_sel:[1,0] op_sel_hi:[1,1]\n"
    "v_pk_mul_f32 v[18:19], v[22:23], v[18:19] op_sel:[1,0] op_sel_hi:[1,1]\n"
    "s_cmpk_lt_u32 s37, 4\n"
    "s_cbranch_scc1 BSIDE0\n"
    "ds_write_b128 v3, v[16:19]\n"
    "s_mov_b64 exec, 1\n"
    "ds_write_b32 v12, v15 offset:4\n"
    "s_mov_b64 exec, -1\n"
    "s_setprio 0\n"
    "s_branch ASIDE\n"
    "BSIDE0:\n"
    "s_setprio 0\n"
    "BSIDE:\n"
    "v_mov_b32 v36, 0x46800000\n"
    "v_mov_b32 v37, 0\n"
    "v_pk_mul_f32 v[38:39], v[18:19], v[36:37] op_sel_hi:[1,0]\n"
    "v_cvt_pk_f16_f32 v44, v36, v38\n"
    "v_cvt_pk_f16_f32 v46, v37, v39\n"
    "v_pk_mul_f32 v[36:37], v[16:17], v[36:37] op_sel_hi:[1,0]\n"
    "v_pk_mul_f32 v[42:43], v[38:39], v[16:17] op_sel:[1,1] op_sel_hi:[1,0] neg_lo:[0,1]\n"
    "v_pk_fma_f32 v[38:39], v[38:39], v[16:17], v[42:43] op_sel_hi:[0,1,1]\n"
    "v_cvt_pk_f16_f32 v45, v36, v38\n"
    "v_cvt_pk_f16_f32 v47, v37, v39\n"
    "v_pk_mul_f32 v[40:41], v[36:37], v[16:17] op_sel:[1,1] op_sel_hi:[1,0] neg_lo:[0,1]\n"
    "v_pk_fma_f32 v[36:37], v[36:37], v[16:17], v[40:41] op_sel_hi:[0,1,1]\n"
    "ds_write_b64 v2, v[44:45] offset:13056\n"
    "v_pk_mul_f32 v[42:43], v[38:39], v[16:17] op_sel:[1,1] op_sel_hi:[1,0] neg_lo:[0,1]\n"
    "v_pk_fma_f32 v[38:39], v[38:39], v[16:17], v[42:43] op_sel_hi:[0,1,1]\n"
    "ds_write_b64 v2, v[46:47] offset:21760\n"
    "v_cvt_pk_f16_f32 v48, v36, v38\n"
    "v_cvt_pk_f16_f32 v50, v37, v39\n"
    "v_pk_mul_f32 v[40:41], v[36:37], v[16:17] op_sel:[1,1] op_sel_hi:[1,0] neg_lo:[0,1]\n"
    "v_pk_fma_f32 v[36:37], v[36:37], v[16:17], v[40:41] op_sel_hi:[0,1,1]\n"
    "v_pk_mul_f32 v[42:43], v[38:39], v[16:17] op_sel:[1,1] op_sel_hi:[1,0] neg_lo:[0,1]\n"
    "v_pk_fma_f32 v[38:39], v[38:39], v[16:17], v[42:43] op_sel_hi:[0,1,1]\n"
    "v_cvt_pk_f16_f32 v49, v36, v38\n"
    "v_cvt_pk_f16_f32 v51, v37, v39\n"
    "v_pk_mul_f32 v[40:41], v[36:37], v[16:17] op_sel:[1,1] op_sel_hi:[1,0] neg_lo:[0,1]\n"
    "v_pk_fma_f32 v[36:37], v[36:37], v[16:17], v[40:41] op_sel_hi:[0,1,1]\n"
    "ds_write_b64 v2, v[48:49] offset:14144\n"
    "v_pk_mul_f32 v[42:43], v[38:39], v[16:17] op_sel:[1,1] op_sel_hi:[1,0] neg_lo:[0,1]\n"
    "v_pk_fma_f32 v[38:39], v[38:39], v[16:17], v[42:43] op_sel_hi:[0,1,1]\n"
    "ds_write_b64 v2, v[50:51] offset:22848\n"
    "v_cvt_pk_f16_f32 v44, v36, v38\n"
    "v_cvt_pk_f16_f32 v46, v37, v39\n"
    "v_pk_mul_f32 v[40:41], v[36:37], v[16:17] op_sel:[1,1] op_sel_hi:[1,0] neg_lo:[0,1]\n"
    "v_pk_fma_f32 v[36:37], v[36:37], v[16:17], v[40:41] op_sel_hi:[0,1,1]\n"
    "v_pk_mul_f32 v[42:43], v[38:39], v[16:17] op_sel:[1,1] op_sel_hi:[1,0] neg_lo:[0,1]\n"
    "v_pk_fma_f32 v[38:39], v[38:39], v[16:17], v[42:43] op_sel_hi:[0,1,1]\n"
    "v_cvt_pk_f16_f32 v45, v36, v38\n"
    "v_cvt_pk_f16_f32 v47, v37, v39\n"
    "v_pk_mul_f32 v[40:41], v[36:37], v[16:17] op_sel:[1,1] op_sel_hi:[1,0] neg_lo:[0,1]\n"
    "v_pk_fma_f32 v[36:37], v[36:37], v[16:17], v[40:41] op_sel_hi:[0,1,1]\n"
    "ds_write_b64 v2, v[44:45] offset:15232\n"
    "v_pk_mul_f32 v[42:43], v[38:39], v[16:17] op_sel:[1,1] op_sel_hi:[1,0] neg_lo:[0,1]\n"
    "v_pk_fma_f32 v[38:39], v[38:39], v[16:17], v[42:43] op_sel_hi:[0,1,1]\n"
    "ds_write_b64 v2, v[46:47] offset:23936\n"
    "v_cvt_pk_f16_f32 v48, v36, v38\n"
    "v_cvt_pk_f16_f32 v50, v37, v39\n"
    "v_pk_mul_f32 v[40:41], v[36:37], v[16:17] op_sel:[1,1] op_sel_hi:[1,0] neg_lo:[0,1]\n"
    "v_pk_fma_f32 v[36:37], v[36:37], v[16:17], v[40:41] op_sel_hi:[0,1,1]\n"
    "v_pk_mul_f32 v[42:43], v[38:39], v[16:17] op_sel:[1,1] op_sel_hi:[1,0] neg_lo:[0,1]\n"
    "v_pk_fma_f32 v[38:39], v[38:39], v[16:17], v[42:43] op_sel_hi:[0,1,1]\n"
    "v_cvt_pk_f16_f32 v49, v36, v38\n"
    "v_cvt_pk_f16_f32 v51, v37, v39\n"
    "v_pk_mul_f32 v[40:41], v[36:37], v[16:17] op_sel:[1,1] op_sel_hi:[1,0] neg_lo:[0,1]\n"
    "v_pk_fma_f32 v[36:37], v[36:37], v[16:17], v[40:41] op_sel_hi:[0,1,1]\n"
    "ds_write_b64 v2, v[48:49] offset:17408\n"
    "v_pk_mul_f32 v[42:43], v[38:39], v[16:17] op_sel:[1,1] op_sel_hi:[1,0] neg_lo:[0,1]\n"
    "v_pk_fma_f32 v[38:39], v[38:39], v[16:17], v[42:43] op_sel_hi:[0,1,1]\n"
    "ds_write_b64 v2, v[50:51] offset:26112\n"
    "v_cvt_pk_f16_f32 v44, v36, v38\n"
    "v_cvt_pk_f16_f32 v46, v37, v39\n"
    "v_pk_mul_f32 v[40:41], v[36:37], v[16:17] op_sel:[1,1] op_sel_hi:[1,0] neg_lo:[0,1]\n"
    "v_pk_fma_f32 v[36:37], v[36:37], v[16:17], v[40:41] op_sel_hi:[0,1,1]\n"
    "v_pk_mul_f32 v[42:43], v[38:39], v[16:17] op_sel:[1,1] op_sel_hi:[1,0] neg_lo:[0,1]\n"
    "v_pk_fma_f32 v[38:39], v[38:39], v[16:17], v[42:43] op_sel_hi:[0,1,1]\n"
    "v_cvt_pk_f16_f32 v45, v36, v38\n"
    "v_cvt_pk_f16_f32 v47, v37, v39\n"
    "v_pk_mul_f32 v[40:41], v[36:37], v[16:17] op_sel:[1,1] op_sel_hi:[1,0] neg_lo:[0,1]\n"
    "v_pk_fma_f32 v[36:37], v[36:37], v[16:17], v[40:41] op_sel_hi:[0,1,1]\n"
    "ds_write_b64 v2, v[44:45] offset:18496\n"
    "PAD15:\n"
    "s_cbranch_execz AUX1\n"
    "v_pk_mul_f32 v[42:43], v[38:39], v[16:17] op_sel:[1,1] op_sel_hi:[1,0] neg_lo:[0,1]\n"
    "v_pk_fma_f32 v[38:39], v[38:39], v[16:17], v[42:43] op_sel_hi:[0,1,1]\n"
    "ds_write_b64 v2, v[46:47] offset:27200\n"
    "v_cvt_pk_f16_f32 v48, v36, v38\n"
    "v_cvt_pk_f16_f32 v50, v37, v39\n"
    "v_pk_mul_f32 v[40:41], v[36:37], v[16:17] op_sel:[1,1] op_sel_hi:[1,0] neg_lo:[0,1]\n"
    "v_pk_fma_f32 v[36:37], v[36:37], v[16:17], v[40:41] op_sel_hi:[0,1,1]\n"
    "v_pk_mul_f32 v[42:43], v[38:39], v[16:17] op_sel:[1,1] op_sel_hi:[1,0] neg_lo:[0,1]\n"
    "v_pk_fma_f32 v[38:39], v[38:39], v[16:17], v[42:43] op_sel_hi:[0,1,1]\n"
    "v_cvt_pk_f16_f32 v49, v36, v38\n"
    "v_cvt_pk_f16_f32 v51, v37, v39\n"
    "v_pk_mul_f32 v[40:41], v[36:37], v[16:17] op_sel:[1,1] op_sel_hi:[1,0] neg_lo:[0,1]\n"
    "v_pk_fma_f32 v[36:37], v[36:37], v[16:17], v[40:41] op_sel_hi:[0,1,1]\n"
    "ds_write_b64 v2, v[48:49] offset:19584\n"
    "v_pk_mul_f32 v[42:43], v[38:39], v[16:17] op_sel:[1,1] op_sel_hi:[1,0] neg_lo:[0,1]\n"
    "v_pk_fma_f32 v[38:39], v[38:39], v[16:17], v[42:43] op_sel_hi:[0,1,1]\n"
    "ds_write_b64 v2, v[50:51] offset:28288\n"
    "v_cvt_pk_f16_f32 v44, v36, v38\n"
    "v_cvt_pk_f16_f32 v46, v37, v39\n"
    "v_pk_mul_f32 v[40:41], v[36:37], v[16:17] op_sel:[1,1] op_sel_hi:[1,0] neg_lo:[0,1]\n"
    "v_pk_fma_f32 v[36:37], v[36:37], v[16:17], v[40:41] op_sel_hi:[0,1,1]\n"
    "v_pk_mul_f32 v[42:43], v[38:39], v[16:17] op_sel:[1,1] op_sel_hi:[1,0] neg_lo:[0,1]\n"
    "v_pk_fma_f32 v[38:39], v[38:39], v[16:17], v[42:43] op_sel_hi:[0,1,1]\n"
    "v_cvt_pk_f16_f32 v45, v36, v38\n"
    "v_cvt_pk_f16_f32 v47, v37, v39\n"
    "v_pk_mul_f32 v[40:41], v[36:37], v[16:17] op_sel:[1,1] op_sel_hi:[1,0] neg_lo:[0,1]\n"
    "v_pk_fma_f32 v[36:37], v[36:37], v[16:17], v[40:41] op_sel_hi:[0,1,1]\n"
    "ds_write_b64 v2, v[44:45] offset:20672\n"
    "v_pk_mul_f32 v[42:43], v[38:39], v[16:17] op_sel:[1,1] op_sel_hi:[1,0] neg_lo:[0,1]\n"
    "v_pk_fma_f32 v[38:39], v[38:39], v[16:17], v[42:43] op_sel_hi:[0,1,1]\n"
    "ds_write_b64 v2, v[46:47] offset:29376\n"
    "v_cvt_pk_f16_f32 v48, v36, v38\n"
    "v_cvt_pk_f16_f32 v50, v37, v39\n"
    "v_pk_mul_f32 v[40:41], v[36:37], v[16:17] op_sel:[1,1] op_sel_hi:[1,0] neg_lo:[0,1]\n"
    "v_pk_fma_f32 v[36:37], v[36:37], v[16:17], v[40:41] op_sel_hi:[0,1,1]\n"
    "v_pk_mul_f32 v[42:43], v[38:39], v[16:17] op_sel:[1,1] op_sel_hi:[1,0] neg_lo:[0,1]\n"
    "v_pk_fma_f32 v[38:39], v[38:39], v[16:17], v[42:43] op_sel_hi:[0,1,1]\n"
    "v_cvt_pk_f16_f32 v49, v36, v38\n"
    "v_cvt_pk_f16_f32 v51, v37, v39\n"
    "ds_write_b64 v2, v[48:49] offset:16320\n"
    "ds_write_b64 v2, v[50:51] offset:25024\n"
    "s_mov_b64 exec, 1\n"
    "ds_add_u32 v13, v15 offset:28\n"
    "s_mov_b64 exec, -1\n"
    "s_cmpk_ge_u32 s37, 14\n"
    "s_cbranch_scc1 AUX3\n"
    "ASIDE:\n"
    "v_fract_f32 v22, v10\n"
    "v_exp_f32 v23, v4\n"
    "v_add_f32 v22, v11, v22\n"
    "v_cos_f32 v24, v22\n"
    "v_sin_f32 v25, v22\n"
    "s_waitcnt vmcnt(0)\n"
    "v_and_b32 v32, 0x7fff7fff, v6\n"
    "v_and_b32 v33, 0x7fff7fff, v7\n"
    "v_pk_max_u16 v32, v32, v33\n"
    "v_pk_mul_f32 v[24:25], v[22:23], v[24:25] op_sel:[1,0] op_sel_hi:[1,1]\n"
    "v_lshlrev_b32 v33, 16, v32\n"
    "v_max_u32 v32, v32, v33\n"
    "v_perm_b32 v6, v6, v6, s47\n"
    "v_perm_b32 v7, v7, v7, s47\n"
    "v_max_u32_dpp v32, v32, v32 row_ror:8 row_mask:0xf bank_mask:0xf\n"
    "v_pk_mul_f32 v[20:21], v[18:19], v[18:19] op_sel:[1,1] op_sel_hi:[1,0] neg_lo:[0,1]\n"
    "v_pk_fma_f32 v[30:31], v[18:19], v[18:19], v[20:21] op_sel_hi:[0,1,1]\n"
    "v_max_u32_dpp v32, v32, v32 row_ror:4 row_mask:0xf bank_mask:0xf\n"
    "v_pk_mul_f32 v[20:21], v[24:25], v[24:25] op_sel:[1,1] op_sel_hi:[1,0] neg_lo:[0,1]\n"
    "v_pk_fma_f32 v[26:27], v[24:25], v[24:25], v[20:21] op_sel_hi:[0,1,1]\n"
    "v_max_u32_dpp v32, v32, v32 row_ror:2 row_mask:0xf bank_mask:0xf\n"
    "v_pk_mul_f32 v[20:21], v[26:27], v[26:27] op_sel:[1,1] op_sel_hi:[1,0] neg_lo:[0,1]\n"
    "v_pk_fma_f32 v[28:29], v[26:27], v[26:27], v[20:21] op_sel_hi:[0,1,1]\n"
    "v_max_u32_dpp v32, v32, v32 row_ror:1 row_mask:0xf bank_mask:0xf\n"
    "v_and_b32 v34, 0x7fffff, v9\n"
    "v_or_b32 v34, 0x35800000, v34\n"
    "v_max_u32_dpp v32, v32, v32 row_bcast:15 row_mask:0xa bank_mask:0xf\n"
    "v_and_b32 v35, 0x7f800000, v9\n"
    "s_nop 0\n"
    "v_max_u32_dpp v32, v32, v32 row_bcast:31 row_mask:0xc bank_mask:0xf\n"
    "s_nop 0\n"
    "v_readlane_b32 s44, v32, 63\n"
    "s_lshr_b32 s44, s44, 23\n"
    "s_max_u32 s44, s44, 26\n"
    "s_min_u32 s44, s44, 226\n"
    "s_sub_u32 s46, 266, s44\n"
    "s_lshl_b32 s46, s46, 23\n"
    "s_sub_u32 s44, s44, 132\n"
    "v_pk_mul_f32 v[36:37], v[6:7], s[46:47] op_sel_hi:[1,0]\n"
    "v_ldexp_f32 v35, v35, s44\n"
    "ds_write_b64 v14, v[34:35]\n"
    "v_pk_mul_f32 v[20:21], v[36:37], v[24:25] op_sel:[1,1] op_sel_hi:[1,0] neg_lo:[0,1]\n"
    "v_pk_fma_f32 v[38:39], v[36:37], v[24:25], v[20:21] op_sel_hi:[0,1,1]\n"
    "v_pk_mul_f32 v[20:21], v[38:39], v[24:25] op_sel:[1,1] op_sel_hi:[1,0] neg_lo:[0,1]\n"
    "v_pk_fma_f32 v[40:41], v[38:39], v[24:25], v[20:21] op_sel_hi:[0,1,1]\n"
    "v_pk_mul_f32 v[20:21], v[40:41], v[24:25] op_sel:[1,1] op_sel_hi:[1,0] neg_lo:[0,1]\n"
    "v_pk_fma_f32 v[42:43], v[40:41], v[24:25], v[20:21] op_sel_hi:[0,1,1]\n"
    "v_pk_mul_f32 v[20:21], v[36:37], v[30:31] op_sel:[1,1] op_sel_hi:[1,0] neg_lo:[0,1]\n"
    "v_pk_fma_f32 v[44:45], v[36:37], v[30:31], v[20:21] op_sel_hi:[0,1,1]\n"
    "v_cvt_pk_f16_f32 v60, v36, v38\n"
    "v_cvt_pk_f16_f32 v61, v40, v42\n"
    "v_cvt_pk_f16_f32 v62, -v37, -v39\n"
    "v_cvt_pk_f16_f32 v63, -v41, -v43\n"
    "ds_write_b64 v2, v[60:61] offset:0\n"
    "ds_write_b64 v2, v[62:63] offset:6528\n"
    "v_pk_mul_f32 v[20:21], v[42:43], v[24:25] op_sel:[1,1] op_sel_hi:[1,0] neg_lo:[0,1]\n"
    "v_pk_fma_f32 v[36:37], v[42:43], v[24:25], v[20:21] op_sel_hi:[0,1,1]\n"
    "v_pk_mul_f32 v[20:21], v[36:37], v[24:25] op_sel:[1,1] op_sel_hi:[1,0] neg_lo:[0,1]\n"
    "v_pk_fma_f32 v[38:39], v[36:37], v[24:25], v[20:21] op_sel_hi:[0,1,1]\n"
    "v_pk_mul_f32 v[20:21], v[38:39], v[24:25] op_sel:[1,1] op_sel_hi:[1,0] neg_lo:[0,1]\n"
    "v_pk_fma_f32 v[40:41], v[38:39], v[24:25], v[20:21] op_sel_hi:[0,1,1]\n"
    "v_pk_mul_f32 v[20:21], v[40:41], v[24:25] op_sel:[1,1] op_sel_hi:[1,0] neg_lo:[0,1]\n"
    "v_pk_fma_f32 v[42:43], v[40:41], v[24:25], v[20:21] op_sel_hi:[0,1,1]\n"
    "v_cvt_pk_f16_f32 v64, v36, v38\n"
    "v_cvt_pk_f16_f32 v65, v40, v42\n"
    "v_cvt_pk_f16_f32 v66, -v37, -v39\n"
    "v_cvt_pk_f16_f32 v67, -v41, -v43\n"
    "ds_write_b64 v2, v[64:65] offset:1088\n"
    "ds_write_b64 v2, v[66:67] offset:7616\n"
    "v_pk_mul_f32 v[20:21], v[44:45], v[26:27] op_sel:[1,1] op_sel_hi:[1,0] neg_lo:[0,1]\n"
    "v_pk_fma_f32 v[46:47], v[44:45], v[26:27], v[20:21] op_sel_hi:[0,1,1]\n"
    "v_pk_mul_f32 v[20:21], v[44:45], v[28:29] op_sel:[1,1] op_sel_hi:[1,0] neg_lo:[0,1]\n"
    "v_pk_fma_f32 v[48:49], v[44:45], v[28:29], v[20:21] op_sel_hi:[0,1,1]\n"
    "v_pk_mul_f32 v[20:21], v[48:49], v[26:27] op_sel:[1,1] op_sel_hi:[1,0] neg_lo:[0,1]\n"
    "v_pk_fma_f32 v[50:51], v[48:49], v[26:27], v[20:21] op_sel_hi:[0,1,1]\n"
    "v_pk_mul_f32 v[20:21], v[44:45], v[30:31] op_sel:[1,1] op_sel_hi:[1,0] neg_lo:[0,1]\n"
    "v_pk_fma_f32 v[36:37], v[44:45], v[30:31], v[20:21] op_sel_hi:[0,1,1]\n"
    "v_cvt_pk_f16_f32 v60, v44, v46\n"
    "v_cvt_pk_f16_f32 v61, v48, v50\n"
    "v_cvt_pk_f16_f32 v62, -v45, -v47\n"
    "v_cvt_pk_f16_f32 v63, -v49, -v51\n"
    "ds_write_b64 v2, v[60:61] offset:2176\n"
    "ds_write_b64 v2, v[62:63] offset:8704\n"
    "v_pk_mul_f32 v[20:21], v[36:37], v[30:31] op_sel:[1,1] op_sel_hi:[1,0] neg_lo:[0,1]\n"
    "v_pk_fma_f32 v[38:39], v[36:37], v[30:31], v[20:21] op_sel_hi:[0,1,1]\n"
    "PAD0:\n"
    "s_cbranch_execz GA1\n"
    "v_pk_mul_f32 v[20:21], v[36:37], v[28:29] op_sel:[1,1] op_sel_hi:[1,0] neg_lo:[0,1]\n"
    "v_pk_fma_f32 v[40:41], v[36:37], v[28:29], v[20:21] op_sel_hi:[0,1,1]\n"
    "v_pk_mul_f32 v[20:21], v[38:39], v[28:29] op_sel:[1,1] op_sel_hi:[1,0] neg_lo:[0,1]\n"
    "v_pk_fma_f32 v[42:43], v[38:39], v[28:29], v[20:21] op_sel_hi:[0,1,1]\n"
    "v_pk_mul_f32 v[20:21], v[38:39], v[30:31] op_sel:[1,1] op_sel_hi:[1,0] neg_lo:[0,1]\n"
    "v_pk_fma_f32 v[44:45], v[38:39], v[30:31], v[20:21] op_sel_hi:[0,1,1]\n"
    "v_cvt_pk_f16_f32 v64, v40, v42\n"
    "v_cvt_pk_f16_f32 v65, v36, v38\n"
    "v_cvt_pk_f16_f32 v66, -v41, -v43\n"
    "v_cvt_pk_f16_f32 v67, -v37, -v39\n"
    "ds_write_b64 v2, v[64:65] offset:3264\n"
    "ds_write_b64 v2, v[66:67] offset:9792\n"
    "v_pk_mul_f32 v[20:21], v[44:45], v[30:31] op_sel:[1,1] op_sel_hi:[1,0] neg_lo:[0,1]\n"
    "v_pk_fma_f32 v[46:47], v[44:45], v[30:31], v[20:21] op_sel_hi:[0,1,1]\n"
    "v_pk_mul_f32 v[20:21], v[46:47], v[30:31] op_sel:[1,1] op_sel_hi:[1,0] neg_lo:[0,1]\n"
    "v_pk_fma_f32 v[48:49], v[46:47], v[30:31], v[20:21] op_sel_hi:[0,1,1]\n"
    "v_pk_mul_f32 v[20:21], v[48:49], v[30:31] op_sel:[1,1] op_sel_hi:[1,0] neg_lo:[0,1]\n"
    "v_pk_fma_f32 v[50:51], v[48:49], v[30:31], v[20:21] op_sel_hi:[0,1,1]\n"
    "v_pk_mul_f32 v[20:21], v[50:51], v[30:31] op_sel:[1,1] op_sel_hi:[1,0] neg_lo:[0,1]\n"
    "v_pk_fma_f32 v[36:37], v[50:51], v[30:31], v[20:21] op_sel_hi:[0,1,1]\n"
    "v_cvt_pk_f16_f32 v60, v44, v46\n"
    "v_cvt_pk_f16_f32 v61, v48, v50\n"
    "v_cvt_pk_f16_f32 v62, -v45, -v47\n"
    "v_cvt_pk_f16_f32 v63, -v49, -v51\n"
    "ds_write_b64 v2, v[60:61] offset:4352\n"
    "ds_write_b64 v2, v[62:63] offset:10880\n"
    "v_cvt_pk_f16_f32 v38, v36, -v37\n"
    "ds_write_b16 v2, v38 offset:5440\n"
    "ds_write_b16_d16_hi v2, v38 offset:11968\n"
    "s_mov_b64 exec, 1\n"
    "ds_add_u32 v13, v15 offset:28\n"
    "s_mov_b64 exec, -1\n"
    "v_mov_b32 v14, 147968\n"
    "s_cmpk_lt_u32 s37, 4\n"
    "s_cbranch_scc1 STAGE\n"
    "s_branch HELP\n"
    "KST:\n"
    "s_branch E_4\n"
    "R_3:\n"
    "s_sleep 1\n"
    "s_sub_u32 s39, s39, 1\n"
    "s_cbranch_scc1 ABORT\n"
    "E_4:\n"
    "ds_read_b32 v4, v14 offset:20\n"
    "ds_read_b128 v[16:19], v74 offset:0\n"
    "ds_read_b128 v[20:23], v74 offset:1024\n"
    "ds_read_b128 v[24:27], v74 offset:2048\n"
    "ds_read_b128 v[28:31], v74 offset:3072\n"
    "s_waitcnt lgkmcnt(0)\n"
    "v_cmp_ne_u32 vcc, 1, v4\n"
    "s_cbranch_vccnz R_3\n"
    "v_add_u32 v28, s49, v28\n"
    "v_add_u32 v29, s49, v29\n"
    "s_mov_b32 s46, 0x01000100\n"
    "s_mov_b32 s42, s47\n"
    "s_branch E_6\n"
    "R_5:\n"
    "s_sleep 1\n"
    "s_sub_u32 s39, s39, 1\n"
    "s_cbranch_scc1 ABORT\n"
    "E_6:\n"
    "ds_read_b32 v4, v75 offset:28\n"
    "s_waitcnt lgkmcnt(0)\n"
    "v_cmp_ne_u32 vcc, 4, v4\n"
    "s_cbranch_vccnz R_5\n"
    "s_setprio 1\n"
    "ds_read_b64_tr_b16 v[32:33], v72 offset:0\n"
    "ds_read_b64_tr_b16 v[34:35], v72 offset:6528\n"
    "ds_read_b64_tr_b16 v[36:37], v73 offset:13056\n"
    "ds_read_b64_tr_b16 v[38:39], v73 offset:21760\n"
    "ds_read_b64_tr_b16 v[40:41], v72 offset:4352\n"
    "ds_read_b64_tr_b16 v[42:43], v72 offset:10880\n"
    "ds_read_b64_tr_b16 v[44:45], v72 offset:128\n"
    "ds_read_b64_tr_b16 v[46:47], v72 offset:6656\n"
    "ds_read_b64_tr_b16 v[48:49], v73 offset:13184\n"
    "ds_read_b64_tr_b16 v[50:51], v73 offset:21888\n"
    "ds_read_b64_tr_b16 v[52:53], v72 offset:4480\n"
    "ds_read_b64_tr_b16 v[54:55], v72 offset:11008\n"
    "ds_read_b64_tr_b16 v[56:57], v72 offset:256\n"
    "ds_read_b64_tr_b16 v[58:59], v72 offset:6784\n"
    "ds_read_b64_tr_b16 v[60:61], v73 offset:13312\n"
    "ds_read_b64_tr_b16 v[62:63], v73 offset:22016\n"
    "ds_read_b64_tr_b16 v[64:65], v72 offset:4608\n"
    "ds_read_b64_tr_b16 v[66:67], v72 offset:11136\n"
    "s_waitcnt lgkmcnt(14)\n"
    "v_mfma_f32_16x16x32_f16 v[4:7], v[32:35], v[36:39], 0\n"
    "s_waitcnt lgkmcnt(12)\n"
    "v_mfma_f32_16x16x32_f16 v[8:11], v[40:43], v[36:39], 0\n"
    "ds_read_b64_tr_b16 v[32:33], v72 offset:384\n"
    "ds_read_b64_tr_b16 v[34:35], v72 offset:6912\n"
    "ds_read_b64_tr_b16 v[36:37], v73 offset:13440\n"
    "ds_read_b64_tr_b16 v[38:39], v73 offset:22144\n"
    "ds_read_b64_tr_b16 v[40:41], v72 offset:4736\n"
    "ds_read_b64_tr_b16 v[42:43], v72 offset:11264\n"
    "s_waitcnt lgkmcnt(14)\n"
    "v_mfma_f32_16x16x32_f16 v[4:7], v[44:47], v[48:51], v[4:7]\n"
    "s_waitcnt lgkmcnt(12)\n"
    "v_mfma_f32_16x16x32_f16 v[8:11], v[52:55], v[48:51], v[8:11]\n"
    "ds_read_b64_tr_b16 v[44:45], v72 offset:512\n"
    "ds_read_b64_tr_b16 v[46:47], v72 offset:7040\n"
    "ds_read_b64_tr_b16 v[48:49], v73 offset:13568\n"
    "ds_read_b64_tr_b16 v[50:51], v73 offset:22272\n"
    "ds_read_b64_tr_b16 v[52:53], v72 offset:4864\n"
    "ds_read_b64_tr_b16 v[54:55], v72 offset:11392\n"
    "s_waitcnt lgkmcnt(14)\n"
    "v_mfma_f32_16x16x32_f16 v[4:7], v[56:59], v[60:63], v[4:7]\n"
    "s_waitcnt lgkmcnt(12)\n"
    "v_mfma_f32_16x16x32_f16 v[8:11], v[64:67], v[60:63], v[8:11]\n"
    "ds_read_b64_tr_b16 v[56:57], v72 offset:640\n"
    "ds_read_b64_tr_b16 v[58:59], v72 offset:7168\n"
    "ds_read_b64_tr_b16 v[60:61], v73 offset:13696\n"
    "ds_read_b64_tr_b16 v[62:63], v73 offset:22400\n"
    "ds_read_b64_tr_b16 v[64:65], v72 offset:4992\n"
    "ds_read_b64_tr_b16 v[66:67], v72 offset:11520\n"
    "s_waitcnt lgkmcnt(14)\n"
    "v_mfma_f32_16x16x32_f16 v[4:7], v[32:35], v[36:39], v[4:7]\n"
    "s_waitcnt lgkmcnt(12)\n"
    "v_mfma_f32_16x16x32_f16 v[8:11], v[40:43], v[36:39], v[8:11]\n"
    "ds_read_b64_tr_b16 v[32:33], v72 offset:768\n"
    "ds_read_b64_tr_b16 v[34:35], v72 offset:7296\n"
    "ds_read_b64_tr_b16 v[36:37], v73 offset:13824\n"
    "ds_read_b64_tr_b16 v[38:39], v73 offset:22528\n"
    "ds_read_b64_tr_b16 v[40:41], v72 offset:5120\n"
    "ds_read_b64_tr_b16 v[42:43], v72 offset:11648\n"
    "s_waitcnt lgkmcnt(14)\n"
    "v_mfma_f32_16x16x32_f16 v[4:7], v[44:47], v[48:51], v[4:7]\n"
    "s_waitcnt lgkmcnt(12)\n"
    "v_mfma_f32_16x16x32_f16 v[8:11], v[52:55], v[48:51], v[8:11]\n"
    "ds_read_b32 v68, v76\n"
    "ds_read_b64_tr_b16 v[44:45], v72 offset:896\n"
    "ds_read_b64_tr_b16 v[46:47], v72 offset:7424\n"
    "ds_read_b64_tr_b16 v[48:49], v73 offset:13952\n"
    "ds_read_b64_tr_b16 v[50:51], v73 offset:22656\n"
    "ds_read_b64_tr_b16 v[52:53], v72 offset:5248\n"
    "ds_read_b64_tr_b16 v[54:55], v72 offset:11776\n"
    "s_waitcnt lgkmcnt(15)\n"
    "v_mfma_f32_16x16x32_f16 v[4:7], v[56:59], v[60:63], v[4:7]\n"
    "s_waitcnt lgkmcnt(13)\n"
    "v_mfma_f32_16x16x32_f16 v[8:11], v[64:67], v[60:63], v[8:11]\n"
    "s_waitcnt lgkmcnt(9)\n"
    "v_mfma_f32_16x16x32_f16 v[4:7], v[32:35], v[36:39], v[4:7]\n"
    "s_waitcnt lgkmcnt(7)\n"
    "v_mfma_f32_16x16x32_f16 v[8:11], v[40:43], v[36:39], v[8:11]\n"
    "s_waitcnt lgkmcnt(2)\n"
    "v_mfma_f32_16x16x32_f16 v[4:7], v[44:47], v[48:51], v[4:7]\n"
    "s_waitcnt lgkmcnt(0)\n"
    "v_mfma_f32_16x16x32_f16 v[8:11], v[52:55], v[48:51], v[8:11]\n"
    "PAD1:\n"
    "s_cbranch_execz GA1\n"
    "s_cmp_eq_u32 s47, 1\n"
    "s_cbranch_scc1 KT1\n"
    "s_cmp_eq_u32 s47, 2\n"
    "s_cbranch_scc1 KT2\n"
    "KT0:\n"
    "s_nop 1\n"
    "v_pk_mul_f32 v[4:5], v[4:5], v[68:69] op_sel_hi:[1,0]\n"
    "v_pk_mul_f32 v[6:7], v[6:7], v[68:69] op_sel_hi:[1,0]\n"
    "v_cvt_pk_f16_f32 v4, v4, v5\n"
    "v_cvt_pk_f16_f32 v5, v6, v7\n"
    "v_pk_mul_f32 v[8:9], v[8:9], v[68:69] op_sel_hi:[1,0]\n"
    "v_perm_b32 v6, v5, v4, v17\n"
    "v_perm_b32 v7, v5, v4, v18\n"
    "v_perm_b32 v32, v5, v4, v30\n"
    "v_perm_b32 v33, v5, v5, s46\n"
    "ds_write_b64 v16, v[6:7] offset:0\n"
    "ds_write2_b32 v28, v6, v32 offset1:1\n"
    "v_perm_b32 v4, v5, v4, v19\n"
    "ds_write2_b32 v29, v33, v5 offset1:1\n"
    "v_pk_mul_f32 v[10:11], v[10:11], v[68:69] op_sel_hi:[1,0]\n"
    "ds_write_b16 v22, v5 offset:0\n"
    "ds_write_b16_d16_hi v23, v5 offset:0\n"
    "v_cvt_pk_f16_f32 v8, v8, v9\n"
    "ds_write_b16 v20, v4 offset:0\n"
    "ds_write_b16_d16_hi v21, v4 offset:0\n"
    "v_cvt_pk_f16_f32 v9, v10, v11\n"
    "ds_write_b16 v24, v8 offset:0\n"
    "ds_write_b16_d16_hi v25, v8 offset:0\n"
    "ds_write_b16 v26, v9 offset:0\n"
    "ds_write_b16_d16_hi v27, v9 offset:0\n"
    "s_mov_b64 exec, 1\n"
    "ds_add_u32 v14, v15 offset:224\n"
    "s_mov_b64 exec, -1\n"
    "s_branch KT3\n"
    "KT1:\n"
    "s_nop 3\n"
    "v_pk_mul_f32 v[4:5], v[4:5], v[68:69] op_sel_hi:[1,0]\n"
    "v_pk_mul_f32 v[6:7], v[6:7], v[68:69] op_sel_hi:[1,0]\n"
    "v_cvt_pk_f16_f32 v4, v4, v5\n"
    "v_cvt_pk_f16_f32 v5, v6, v7\n"
    "v_pk_mul_f32 v[8:9], v[8:9], v[68:69] op_sel_hi:[1,0]\n"
    "v_perm_b32 v6, v5, v4, v17\n"
    "v_perm_b32 v7, v5, v4, v18\n"
    "v_perm_b32 v32, v5, v4, v30\n"
    "v_perm_b32 v33, v5, v5, s46\n"
    "ds_write_b64 v16, v[6:7] offset:3328\n"
    "ds_write2_b32 v28, v6, v32 offset1:1\n"
    "v_perm_b32 v4, v5, v4, v19\n"
    "ds_write2_b32 v29, v33, v5 offset1:1\n"
    "v_pk_mul_f32 v[10:11], v[10:11], v[68:69] op_sel_hi:[1,0]\n"
    "ds_write_b16 v22, v5 offset:3328\n"
    "ds_write_b16_d16_hi v23, v5 offset:3328\n"
    "v_cvt_pk_f16_f32 v8, v8, v9\n"
    "ds_write_b16 v20, v4 offset:3328\n"
    "ds_write_b16_d16_hi v21, v4 offset:3328\n"
    "v_cvt_pk_f16_f32 v9, v10, v11\n"
    "ds_write_b16 v24, v8 offset:3328\n"
    "ds_write_b16_d16_hi v25, v8 offset:3328\n"
    "ds_write_b16 v26, v9 offset:3328\n"
    "ds_write_b16_d16_hi v27, v9 offset:3328\n"
    "s_mov_b64 exec, 1\n"
    "ds_add_u32 v14, v15 offset:224\n"
    "s_mov_b64 exec, -1\n"
    "s_branch KT3\n"
    "KT2:\n"
    "s_nop 1\n"
    "v_pk_mul_f32 v[4:5], v[4:5], v[68:69] op_sel_hi:[1,0]\n"
    "v_pk_mul_f32 v[6:7], v[6:7], v[68:69] op_sel_hi:[1,0]\n"
    "v_cvt_pk_f16_f32 v4, v4, v5\n"
    "v_cvt_pk_f16_f32 v5, v6, v7\n"
    "v_pk_mul_f32 v[8:9], v[8:9], v[68:69] op_sel_hi:[1,0]\n"
    "v_perm_b32 v6, v5, v4, v17\n"
    "v_perm_b32 v7, v5, v4, v18\n"
    "v_perm_b32 v32, v5, v4, v30\n"
    "v_perm_b32 v33, v5, v5, s46\n"
    "ds_write_b64 v16, v[6:7] offset:6656\n"
    "ds_write2_b32 v28, v6, v32 offset1:1\n"
    "v_perm_b32 v4, v5, v4, v19\n"
    "ds_write2_b32 v29, v33, v5 offset1:1\n"
    "v_pk_mul_f32 v[10:11], v[10:11], v[68:69] op_sel_hi:[1,0]\n"
    "ds_write_b16 v22, v5 offset:6656\n"
    "ds_write_b16_d16_hi v23, v5 offset:6656\n"
    "v_cvt_pk_f16_f32 v8, v8, v9\n"
    "ds_write_b16 v20, v4 offset:6656\n"
    "ds_write_b16_d16_hi v21, v4 offset:6656\n"
    "v_cvt_pk_f16_f32 v9, v10, v11\n"
    "ds_write_b16 v24, v8 offset:6656\n"
    "ds_write_b16_d16_hi v25, v8 offset:6656\n"
    "ds_write_b16 v26, v9 offset:6656\n"
    "ds_write_b16_d16_hi v27, v9 offset:6656\n"
    "s_mov_b64 exec, 1\n"
    "ds_add_u32 v14, v15 offset:224\n"
    "s_mov_b64 exec, -1\n"
    "KT3:\n"
    "s_cmp_eq_u32 s48, 0\n"
    "s_cbranch_scc1 NEAR\n"
    "s_cmpk_eq_u32 s37, 6\n"
    "s_cbranch_scc1 HELP\n"
    "s_branch FAR\n"
    "PAD2:\n"
    "s_cbranch_execz GA1\n"
    "AUX1:\n"
    "s_mov_b64 exec, -1\n"
    "v_lshlrev_b32 v1, 4, v0\n"
    "v_add_u32 v2, 78080, v1\n"
    "ds_write_b128 v2, v[4:7] offset:0\n"
    "ds_write_b128 v2, v[4:7] offset:3072\n"
    "ds_write_b128 v2, v[4:7] offset:6144\n"
    "v_min_u32 v2, 92144, v2\n"
    "ds_write_b128 v2, v[4:7] offset:9216\n"
    "s_mul_i32 s42, s37, 4576\n"
    "s_add_u32 s42, s42, 61344\n"
    "v_and_b32 v8, 63, v0\n"
    "v_min_u32 v2, 29, v8\n"
    "v_lshl_add_u32 v2, v2, 4, s42\n"
    "ds_write_b128 v2, v[4:7]\n"
    "v_mov_b32 v15, 1\n"
    "v_mov_b32 v14, 147968\n"
    "s_waitcnt lgkmcnt(0)\n"
    "s_load_dword s36, s[16:17], 0\n"
    "s_barrier\n"
    "s_cmpk_gt_u32 s37, 13\n"
    "s_cbranch_scc1 AUXB\n"
    "s_getpc_b64 s[44:45]\n"
    "s_add_u32 s44, s44, mega_tab@rel32@lo+4\n"
    "s_addc_u32 s45, s45, mega_tab@rel32@hi+12\n"
    "s_mov_b32 m0, 107520\n"
    "v_lshlrev_b32 v2, 4, v8\n"
    "v_add_u32 v3, 6144, v2\n"
    "global_load_lds_dwordx4 v3, s[44:45] offset:0\n"
    "global_load_lds_dwordx4 v3, s[44:45] offset:1024\n"
    "global_load_lds_dwordx4 v3, s[44:45] offset:2048\n"
    "global_load_lds_dwordx4 v3, s[44:45] offset:3072\n"
    "s_mov_b32 m0, 111616\n"
    "v_add_u32 v3, 4096, v3\n"
    "global_load_lds_dwordx4 v3, s[44:45] offset:0\n"
    "global_load_lds_dwordx4 v3, s[44:45] offset:1024\n"
    "global_load_lds_dwordx4 v3, s[44:45] offset:2048\n"
    "global_load_lds_dwordx4 v3, s[44:45] offset:3072\n"
    "s_mov_b32 m0, 101376\n"
    "s_nop 0\n"
    "global_load_lds_dwordx4 v2, s[44:45] offset:0\n"
    "global_load_lds_dwordx4 v2, s[44:45] offset:1024\n"
    "global_load_lds_dwordx4 v2, s[44:45] offset:2048\n"
    "global_load_lds_dwordx4 v2, s[44:45] offset:3072\n"
    "s_mov_b32 m0, 105472\n"
    "v_add_u32 v2, 4096, v2\n"
    "global_load_lds_dwordx4 v2, s[44:45] offset:0\n"
    "global_load_lds_dwordx4 v2, s[44:45] offset:1024\n"
    "s_mov_b64 exec, 0\n"
    "CH13:\n"
    "s_branch CH13_0\n"
    "AUXB:\n"
    "s_lshl_b32 s42, s37, 9\n"
    "s_add_u32 s42, s42, 53760\n"
    "v_lshl_add_u32 v2, v8, 3, s42\n"
    "v_add_u32 v3, 116128, v1\n"
    "s_lshl_b32 s42, s37, 2\n"
    "s_add_u32 s42, s42, 147912\n"
    "v_mov_b32 v12, s42\n"
    "v_mov_b32 v13, 147976\n"
    "s_branch E_8\n"
    "R_7:\n"
    "s_sleep 1\n"
    "s_sub_u32 s39, s39, 1\n"
    "s_cbranch_scc1 ABORT\n"
    "E_8:\n"
    "ds_read_b32 v20, v12 offset:4\n"
    "ds_read_b128 v[16:19], v3\n"
    "s_waitcnt lgkmcnt(0)\n"
    "v_cmp_ne_u32 vcc, 1, v20\n"
    "s_cbranch_vccnz R_7\n"
    "s_add_u32 s36, s36, 1\n"
    "s_setprio 1\n"
    "s_branch BSIDE\n"
    "AUX2:\n"
    "s_mov_b64 exec, -1\n"
    "s_waitcnt vmcnt(0) lgkmcnt(0)\n"
    "s_add_u32 s36, s36, 1\n"
    "s_mov_b64 exec, 1\n"
    "ds_add_u32 v14, v15 offset:20\n"
    "s_mov_b64 exec, -1\n"
    "AUX3:\n"
    "s_setprio 0\n"
    "s_and_b32 s44, s2, 7\n"
    "s_lshr_b32 s45, s2, 3\n"
    "s_mul_i32 s44, s44, 24\n"
    "s_add_u32 s38, s44, s45\n"
    "s_mul_i32 s42, s38, 6\n"
    "s_and_b32 s42, s42, -4\n"
    "s_load_dwordx2 s[52:53], s[24:25], s42\n"
    "v_mov_b32 v15, 1\n"
    "v_mov_b32 v14, 147968\n"
    "ds_read_b32 v2, v14 offset:480\n"
    "s_waitcnt lgkmcnt(0)\n"
    "v_readfirstlane_b32 s96, v2\n"
    "s_cmpk_lg_u32 s37, 13\n"
    "s_cbranch_scc1 STAGE\n"
    "s_setprio 3\n"
    "v_and_b32 v2, 15, v0\n"
    "v_bfe_u32 v3, v0, 4, 2\n"
    "v_mul_u32_u24 v3, 4608, v3\n"
    "s_movk_i32 s43, 13824\n"
    "v_mad_u32_u24 v3, v2, s43, v3\n"
    "s_mul_i32 s42, s38, 24\n"
    "s_add_u32 s42, s42, 9733120\n"
    "v_lshlrev_b32 v2, 1, v2\n"
    "v_add_u32 v2, 148352, v2\n"
    "s_mov_b32 s43, 0x42000000\n"
    "s_mov_b32 s46, 0x01ff01ff\n"
    "s_movk_i32 s47, 0x01ff\n"
    "v_mov_b32 v8, 0\n"
    "s_mov_b64 exec, s[46:47]\n"
    "U_9:\n"
    "s_memrealtime s[98:99]\n"
    "s_waitcnt lgkmcnt(0)\n"
    "s_sub_u32 s98, s98, s96\n"
    "s_cmpk_lt_u32 s98, 240\n"
    "s_cbranch_scc1 U_9\n"
    "s_branch E_11\n"
    "R_10:\n"
    "s_sleep 2\n"
    "s_sub_u32 s39, s39, 1\n"
    "s_cbranch_scc1 ABORT\n"
    "E_11:\n"
    "buffer_load_dwordx4 v[4:7], v3, s[16:19], s42 offen sc1\n"
    "buffer_load_dwordx2 v[8:9], v3, s[16:19], s42 offen offset:16 sc1\n"
    "s_waitcnt vmcnt(0)\n"
    "v_bitop3_b32 v10, v5, v7, s36 bitop3:0x7e\n"
    "v_bitop3_b32 v10, v10, v9, s36 bitop3:0xf6\n"
    "v_cmp_ne_u32 vcc, 0, v10\n"
    "s_cbranch_vccnz R_10\n"
    "s_mov_b64 exec, -1\n"
    "v_add_f32_dpp v4, v4, v4 row_ror:8 row_mask:0xf bank_mask:0xf\n"
    "v_add_f32_dpp v6, v6, v6 row_ror:8 row_mask:0xf bank_mask:0xf\n"
    "v_add_f32_dpp v8, v8, v8 row_ror:8 row_mask:0xf bank_mask:0xf\n"
    "v_add_f32_dpp v4, v4, v4 row_ror:4 row_mask:0xf bank_mask:0xf\n"
    "v_add_f32_dpp v6, v6, v6 row_ror:4 row_mask:0xf bank_mask:0xf\n"
    "v_add_f32_dpp v8, v8, v8 row_ror:4 row_mask:0xf bank_mask:0xf\n"
    "s_nop 0\n"
    "v_mov_b32_dpp v4, v6 quad_perm:[0,1,2,3] row_mask:0xf bank_mask:0x2\n"
    "s_nop 1\n"
    "v_mov_b32_dpp v4, v8 quad_perm:[0,1,2,3] row_mask:0xf bank_mask:0x4\n"
    "s_nop 1\n"
    "v_add_f32_dpp v4, v4, v4 quad_perm:[1,0,3,2] row_mask:0xf bank_mask:0xf\n"
    "s_nop 1\n"
    "v_add_f32_dpp v4, v4, v4 quad_perm:[2,3,0,1] row_mask:0xf bank_mask:0xf\n"
    "v_cvt_pk_bf16_f32 v5, 0, v4\n"
    "v_add_f32 v4, 1.0, v5\n"
    "v_cvt_pk_bf16_f32 v4, 0, v4\n"
    "v_mov_b32 v9, v4\n"
    "s_nop 1\n"
    "v_permlane16_swap_b32 v9, v4\n"
    "v_fma_f32 v6, abs(v4), s43, abs(v5)\n"
    "v_frexp_exp_i32_f32 v6, v6\n"
    "v_sub_u32 v7, 11, v6\n"
    "v_ldexp_f32 v10, v4, v7\n"
    "v_ldexp_f32 v11, v5, v7\n"
    "s_mov_b64 exec, 0x0111\n"
    "ds_write_b64 v2, v[10:11]\n"
    "ds_write_b32 v14, v15 offset:40\n"
    "v_subrev_u32 v8, 11, v6\n"
    "v_ldexp_f32 v8, 1.0, v8\n"
    "ds_write_b32 v2, v8 offset:32\n"
    "s_mov_b32 exec_lo, 0\n"
    "s_mov_b32 exec_hi, 0x0111\n"
    "ds_write_b32 v2, v5 offset:36\n"
    "s_mov_b64 exec, -1\n"
    "s_setprio 0\n"
    "STAGE:\n"
    "s_and_b32 s42, s38, 1\n"
    "s_mul_i32 s42, s42, 6144\n"
    "s_lshr_b32 s43, s38, 1\n"
    "s_lshl_b32 s43, s43, 6\n"
    "s_add_u32 s42, s42, s43\n"
    "PAD3:\n"
    "s_cbranch_execz GA1\n"
    "s_add_u32 s42, s42, 6554624\n"
    "s_add_u32 s43, s42, 1589248\n"
    "v_bfe_u32 v2, v0, 2, 7\n"
    "v_mul_u32_u24 v2, 12416, v2\n"
    "v_and_b32 v3, 3, v0\n"
    "v_lshl_add_u32 v2, v3, 4, v2\n"
    "U_12:\n"
    "s_memrealtime s[98:99]\n"
    "s_waitcnt lgkmcnt(0)\n"
    "s_sub_u32 s98, s98, s96\n"
    "s_cmpk_lt_u32 s98, 255\n"
    "s_cbranch_scc1 U_12\n"
    "buffer_load_dwordx4 v[4:7], v2, s[16:19], s42 offen sc1\n"
    "buffer_load_dwordx4 v[8:11], v2, s[16:19], s43 offen sc1\n"
    "v_and_b32 v3, 511, v0\n"
    "v_lshlrev_b32 v3, 2, v3\n"
    "v_sub_u32 v3, 118780, v3\n"
    "v_mov_b32 v12, 148352\n"
    "s_branch E_14\n"
    "R_13:\n"
    "s_sleep 1\n"
    "s_sub_u32 s39, s39, 1\n"
    "s_cbranch_scc1 ABORT\n"
    "E_14:\n"
    "ds_read_b32 v13, v14 offset:40\n"
    "ds_read_b128 v[16:19], v12\n"
    "ds_read_b64 v[20:21], v12 offset:16\n"
    "s_waitcnt lgkmcnt(0)\n"
    "v_cmp_ne_u32 vcc, 1, v13\n"
    "s_cbranch_vccnz R_13\n"
    "s_branch E_16\n"
    "R_15:\n"
    "s_sub_u32 s39, s39, 1\n"
    "s_cbranch_scc1 ABORT\n"
    "s_sleep 2\n"
    "s_wqm_b64 exec, vcc\n"
    "buffer_load_dwordx4 v[4:7], v2, s[16:19], s42 offen sc1\n"
    "buffer_load_dwordx4 v[8:11], v2, s[16:19], s43 offen sc1\n"
    "s_mov_b64 exec, -1\n"
    "E_16:\n"
    "s_waitcnt vmcnt(0)\n"
    "v_bitop3_b32 v13, v7, v11, s36 bitop3:0x7e\n"
    "v_cmp_ne_u32 vcc, 0, v13\n"
    "s_cbranch_vccnz R_15\n"
    "v_lshlrev_b32 v24, 16, v4\n"
    "v_and_b32 v25, 0xffff0000, v4\n"
    "v_lshlrev_b32 v26, 16, v5\n"
    "v_and_b32 v27, 0xffff0000, v5\n"
    "v_lshlrev_b32 v28, 16, v6\n"
    "v_and_b32 v29, 0xffff0000, v6\n"
    "v_pk_mul_f32 v[24:25], v[24:25], v[16:17] op_sel_hi:[1,0]\n"
    "v_pk_mul_f32 v[26:27], v[26:27], v[18:19] op_sel_hi:[1,0]\n"
    "v_pk_mul_f32 v[28:29], v[28:29], v[20:21] op_sel_hi:[1,0]\n"
    "v_cvt_pk_bf16_f32 v24, 0, v24\n"
    "v_cvt_pk_bf16_f32 v25, 0, v25\n"
    "v_cvt_pk_bf16_f32 v26, 0, v26\n"
    "v_cvt_pk_bf16_f32 v27, 0, v27\n"
    "v_cvt_pk_bf16_f32 v28, 0, v28\n"
    "v_cvt_pk_bf16_f32 v29, 0, v29\n"
    "v_pk_add_f32 v[24:25], v[16:17], v[24:25] op_sel:[1,0] op_sel_hi:[1,1]\n"
    "v_pk_add_f32 v[26:27], v[18:19], v[26:27] op_sel:[1,0] op_sel_hi:[1,1]\n"
    "v_pk_add_f32 v[28:29], v[20:21], v[28:29] op_sel:[1,0] op_sel_hi:[1,1]\n"
    "v_cvt_pk_bf16_f32 v24, 0, v24\n"
    "v_cvt_pk_bf16_f32 v25, 0, v25\n"
    "v_cvt_pk_bf16_f32 v26, 0, v26\n"
    "v_cvt_pk_bf16_f32 v27, 0, v27\n"
    "v_cvt_pk_bf16_f32 v28, 0, v28\n"
    "v_cvt_pk_bf16_f32 v29, 0, v29\n"
    "v_cvt_pkrtz_f16_f32 v30, v25, v24\n"
    "ds_write_b32 v3, v30 offset:2048\n"
    "v_cvt_pkrtz_f16_f32 v31, v27, v26\n"
    "ds_write_b32 v3, v31 offset:6624\n"
    "v_cvt_pkrtz_f16_f32 v32, v29, v28\n"
    "ds_write_b32 v3, v32 offset:11200\n"
    "v_lshlrev_b32 v24, 16, v8\n"
    "v_and_b32 v25, 0xffff0000, v8\n"
    "v_lshlrev_b32 v26, 16, v9\n"
    "v_and_b32 v27, 0xffff0000, v9\n"
    "v_lshlrev_b32 v28, 16, v10\n"
    "v_and_b32 v29, 0xffff0000, v10\n"
    "v_pk_mul_f32 v[24:25], v[24:25], v[16:17] op_sel_hi:[1,0]\n"
    "v_pk_mul_f32 v[26:27], v[26:27], v[18:19] op_sel_hi:[1,0]\n"
    "v_pk_mul_f32 v[28:29], v[28:29], v[20:21] op_sel_hi:[1,0]\n"
    "v_cvt_pk_bf16_f32 v24, 0, v24\n"
    "v_cvt_pk_bf16_f32 v25, 0, v25\n"
    "v_cvt_pk_bf16_f32 v26, 0, v26\n"
    "v_cvt_pk_bf16_f32 v27, 0, v27\n"
    "v_cvt_pk_bf16_f32 v28, 0, v28\n"
    "v_cvt_pk_bf16_f32 v29, 0, v29\n"
    "v_pk_add_f32 v[24:25], v[16:17], v[24:25] op_sel:[1,0] op_sel_hi:[1,1]\n"
    "v_pk_add_f32 v[26:27], v[18:19], v[26:27] op_sel:[1,0] op_sel_hi:[1,1]\n"
    "v_pk_add_f32 v[28:29], v[20:21], v[28:29] op_sel:[1,0] op_sel_hi:[1,1]\n"
    "v_cvt_pk_bf16_f32 v24, 0, v24\n"
    "v_cvt_pk_bf16_f32 v25, 0, v25\n"
    "v_cvt_pk_bf16_f32 v26, 0, v26\n"
    "v_cvt_pk_bf16_f32 v27, 0, v27\n"
    "v_cvt_pk_bf16_f32 v28, 0, v28\n"
    "v_cvt_pk_bf16_f32 v29, 0, v29\n"
    "v_cvt_pkrtz_f16_f32 v33, v25, v24\n"
    "ds_write_b32 v3, v33 offset:0\n"
    "v_cvt_pkrtz_f16_f32 v34, v27, v26\n"
    "ds_write_b32 v3, v34 offset:4576\n"
    "v_cvt_pkrtz_f16_f32 v35, v29, v28\n"
    "ds_write_b32 v3, v35 offset:9152\n"
    "s_cmpk_ge_u32 s37, 12\n"
    "s_cbranch_scc1 EPI\n"
    "s_cmpk_eq_u32 s37, 3\n"
    "s_cbranch_scc0 MISC\n"
    "FAR:\n"
    "s_lshr_b32 s42, s37, 2\n"
    "v_and_b32 v6, 15, v0\n"
    "v_bfe_u32 v7, v0, 4, 2\n"
    "v_and_b32 v1, 63, v0\n"
    "v_lshlrev_b32 v1, 4, v1\n"
    "v_add_u32 v1, 101376, v1\n"
    "s_mul_i32 s43, s42, 4576\n"
    "s_add_u32 s43, s43, 117216\n"
    "v_lshlrev_b32 v2, 1, v6\n"
    "v_sub_u32 v2, v7, v2\n"
    "v_lshl_add_u32 v2, v2, 4, s43\n"
    "s_mul_i32 s44, s42, 3328\n"
    "s_add_u32 s43, s44, 92596\n"
    "v_lshlrev_b32 v3, 2, v7\n"
    "v_sub_u32 v3, v6, v3\n"
    "v_lshl_add_u32 v3, v3, 2, s43\n"
    "s_mul_i32 s43, s42, 7680\n"
    "s_add_u32 s43, s43, 28160\n"
    "v_mul_u32_u24 v4, 320, v7\n"
    "v_lshl_add_u32 v4, v6, 4, v4\n"
    "v_add_u32 v4, s43, v4\n"
    "s_add_u32 s43, s42, 147968\n"
    "v_mov_b32 v5, s43\n"
    "v_mov_b32 v8, 1\n"
    "v_mov_b32 v11, 147968\n"
    "s_branch E_18\n"
    "PAD4:\n"
    "s_cbranch_execz GA1\n"
    "R_17:\n"
    "s_sleep 1\n"
    "s_sub_u32 s39, s39, 1\n"
    "s_cbranch_scc1 ABORT\n"
    "E_18:\n"
    "ds_read_b32 v12, v11 offset:224\n"
    "s_waitcnt lgkmcnt(0)\n"
    "v_cmp_ne_u32 vcc, 6, v12\n"
    "s_cbranch_vccnz R_17\n"
    "ds_read_b128 v[12:15], v1 offset:0\n"
    "ds_read_b128 v[16:19], v1 offset:1024\n"
    "ds_read_b128 v[20:23], v1 offset:2048\n"
    "ds_read2_b32 v[24:25], v3 offset0:0 offset1:1\n"
    "ds_read2_b32 v[26:27], v3 offset0:2 offset1:3\n"
    "ds_read2_b32 v[36:37], v3 offset0:64 offset1:65\n"
    "ds_read2_b32 v[38:39], v3 offset0:66 offset1:67\n"
    "ds_read2_b32 v[28:29], v3 offset0:16 offset1:17\n"
    "ds_read2_b32 v[30:31], v3 offset0:18 offset1:19\n"
    "ds_read2_b32 v[40:41], v3 offset0:80 offset1:81\n"
    "ds_read2_b32 v[42:43], v3 offset0:82 offset1:83\n"
    "ds_read2_b32 v[32:33], v3 offset0:32 offset1:33\n"
    "ds_read2_b32 v[34:35], v3 offset0:34 offset1:35\n"
    "ds_read2_b32 v[44:45], v3 offset0:96 offset1:97\n"
    "ds_read2_b32 v[46:47], v3 offset0:98 offset1:99\n"
    "s_setprio 1\n"
    "s_waitcnt lgkmcnt(0)\n"
    "s_barrier\n"
    "ds_read_b128 v[48:51], v2 offset:3584\n"
    "ds_read_b128 v[52:55], v2 offset:3600\n"
    "ds_read_b128 v[56:59], v2 offset:3072\n"
    "ds_read_b128 v[60:63], v2 offset:3088\n"
    "ds_read_b128 v[64:67], v2 offset:2560\n"
    "ds_read_b128 v[68:71], v2 offset:2576\n"
    "s_waitcnt lgkmcnt(4)\n"
    "v_mfma_f32_16x16x32_f16 v[72:75], v[48:51], v[12:15], 0\n"
    "v_mfma_f32_16x16x32_f16 v[76:79], v[48:51], v[16:19], 0\n"
    "v_mfma_f32_16x16x32_f16 v[80:83], v[48:51], v[20:23], 0\n"
    "v_mfma_f32_16x16x32_f16 v[84:87], v[52:55], v[20:23], 0\n"
    "s_nop 5\n"
    "v_cvt_pk_f16_f32 v88, v75, v79\n"
    "v_cvt_pk_f16_f32 v89, v74, v78\n"
    "v_cvt_pk_f16_f32 v90, v73, v77\n"
    "v_cvt_pk_f16_f32 v91, v72, v76\n"
    "v_cvt_pk_f16_f32 v92, v83, v87\n"
    "v_cvt_pk_f16_f32 v93, v82, v86\n"
    "v_cvt_pk_f16_f32 v94, v81, v85\n"
    "v_cvt_pk_f16_f32 v95, v80, v84\n"
    "s_waitcnt lgkmcnt(2)\n"
    "v_mfma_f32_16x16x32_f16 v[72:75], v[56:59], v[12:15], 0\n"
    "v_mfma_f32_16x16x32_f16 v[76:79], v[56:59], v[16:19], 0\n"
    "v_mfma_f32_16x16x32_f16 v[80:83], v[56:59], v[20:23], 0\n"
    "v_mfma_f32_16x16x32_f16 v[84:87], v[60:63], v[20:23], 0\n"
    "ds_read_b128 v[48:51], v2 offset:2048\n"
    "ds_read_b128 v[52:55], v2 offset:2064\n"
    "v_mfma_f32_16x16x32_f16 v[96:99], v[88:91], v[24:27], 0\n"
    "v_mfma_f32_16x16x32_f16 v[96:99], v[92:95], v[36:39], v[96:99]\n"
    "v_mfma_f32_16x16x32_f16 v[100:103], v[88:91], v[28:31], 0\n"
    "v_mfma_f32_16x16x32_f16 v[100:103], v[92:95], v[40:43], v[100:103]\n"
    "v_mfma_f32_16x16x32_f16 v[104:107], v[88:91], v[32:35], 0\n"
    "v_mfma_f32_16x16x32_f16 v[104:107], v[92:95], v[44:47], v[104:107]\n"
    "v_cvt_pk_f16_f32 v88, v75, v79\n"
    "v_cvt_pk_f16_f32 v89, v74, v78\n"
    "v_cvt_pk_f16_f32 v90, v73, v77\n"
    "v_cvt_pk_f16_f32 v91, v72, v76\n"
    "v_cvt_pk_f16_f32 v92, v83, v87\n"
    "v_cvt_pk_f16_f32 v93, v82, v86\n"
    "v_cvt_pk_f16_f32 v94, v81, v85\n"
    "v_cvt_pk_f16_f32 v95, v80, v84\n"
    "ds_write_b128 v4, v[96:99] offset:2560\n"
    "s_mov_b64 exec, 1\n"
    "ds_write_b8 v5, v8 offset:83\n"
    "s_mov_b64 exec, -1\n"
    "s_waitcnt lgkmcnt(4)\n"
    "v_mfma_f32_16x16x32_f16 v[72:75], v[64:67], v[12:15], 0\n"
    "v_mfma_f32_16x16x32_f16 v[76:79], v[64:67], v[16:19], 0\n"
    "v_mfma_f32_16x16x32_f16 v[80:83], v[64:67], v[20:23], 0\n"
    "v_mfma_f32_16x16x32_f16 v[84:87], v[68:71], v[20:23], 0\n"
    "ds_read_b128 v[56:59], v2 offset:1536\n"
    "ds_read_b128 v[60:63], v2 offset:1552\n"
    "v_mfma_f32_16x16x32_f16 v[100:103], v[88:91], v[24:27], v[100:103]\n"
    "v_mfma_f32_16x16x32_f16 v[100:103], v[92:95], v[36:39], v[100:103]\n"
    "v_mfma_f32_16x16x32_f16 v[104:107], v[88:91], v[28:31], v[104:107]\n"
    "v_mfma_f32_16x16x32_f16 v[104:107], v[92:95], v[40:43], v[104:107]\n"
    "v_mfma_f32_16x16x32_f16 v[108:111], v[88:91], v[32:35], 0\n"
    "v_mfma_f32_16x16x32_f16 v[108:111], v[92:95], v[44:47], v[108:111]\n"
    "v_cvt_pk_f16_f32 v88, v75, v79\n"
    "v_cvt_pk_f16_f32 v89, v74, v78\n"
    "v_cvt_pk_f16_f32 v90, v73, v77\n"
    "v_cvt_pk_f16_f32 v91, v72, v76\n"
    "v_cvt_pk_f16_f32 v92, v83, v87\n"
    "v_cvt_pk_f16_f32 v93, v82, v86\n"
    "v_cvt_pk_f16_f32 v94, v81, v85\n"
    "v_cvt_pk_f16_f32 v95, v80, v84\n"
    "ds_write_b128 v4, v[100:103] offset:3840\n"
    "s_mov_b64 exec, 1\n"
    "ds_write_b8 v5, v8 offset:91\n"
    "s_mov_b64 exec, -1\n"
    "s_waitcnt lgkmcnt(6)\n"
    "v_mfma_f32_16x16x32_f16 v[72:75], v[48:51], v[12:15], 0\n"
    "v_mfma_f32_16x16x32_f16 v[76:79], v[48:51], v[16:19], 0\n"
    "v_mfma_f32_16x16x32_f16 v[80:83], v[48:51], v[20:23], 0\n"
    "v_mfma_f32_16x16x32_f16 v[84:87], v[52:55], v[20:23], 0\n"
    "v_mfma_f32_16x16x32_f16 v[104:107], v[88:91], v[24:27], v[104:107]\n"
    "v_mfma_f32_16x16x32_f16 v[104:107], v[92:95], v[36:39], v[104:107]\n"
    "v_mfma_f32_16x16x32_f16 v[108:111], v[88:91], v[28:31], v[108:111]\n"
    "v_mfma_f32_16x16x32_f16 v[108:111], v[92:95], v[40:43], v[108:111]\n"
    "v_mfma_f32_16x16x32_f16 v[112:115], v[88:91], v[32:35], 0\n"
    "v_mfma_f32_16x16x32_f16 v[112:115], v[92:95], v[44:47], v[112:115]\n"
    "v_cvt_pk_f16_f32 v88, v75, v79\n"
    "v_cvt_pk_f16_f32 v89, v74, v78\n"
    "v_cvt_pk_f16_f32 v90, v73, v77\n"
    "v_cvt_pk_f16_f32 v91, v72, v76\n"
    "v_cvt_pk_f16_f32 v92, v83, v87\n"
    "v_cvt_pk_f16_f32 v93, v82, v86\n"
    "v_cvt_pk_f16_f32 v94, v81, v85\n"
    "v_cvt_pk_f16_f32 v95, v80, v84\n"
    "ds_write_b128 v4, v[104:107] offset:5120\n"
    "s_mov_b64 exec, 1\n"
    "ds_write_b8 v5, v8 offset:99\n"
    "s_mov_b64 exec, -1\n"
    "s_waitcnt lgkmcnt(4)\n"
    "v_mfma_f32_16x16x32_f16 v[72:75], v[56:59], v[12:15], 0\n"
    "v_mfma_f32_16x16x32_f16 v[76:79], v[56:59], v[16:19], 0\n"
    "v_mfma_f32_16x16x32_f16 v[80:83], v[56:59], v[20:23], 0\n"
    "v_mfma_f32_16x16x32_f16 v[84:87], v[60:63], v[20:23], 0\n"
    "v_mfma_f32_16x16x32_f16 v[108:111], v[88:91], v[24:27], v[108:111]\n"
    "v_mfma_f32_16x16x32_f16 v[108:111], v[92:95], v[36:39], v[108:111]\n"
    "v_mfma_f32_16x16x32_f16 v[112:115], v[88:91], v[28:31], v[112:115]\n"
    "v_mfma_f32_16x16x32_f16 v[112:115], v[92:95], v[40:43], v[112:115]\n"
    "v_mfma_f32_16x16x32_f16 v[116:119], v[88:91], v[32:35], 0\n"
    "v_mfma_f32_16x16x32_f16 v[116:119], v[92:95], v[44:47], v[116:119]\n"
    "v_cvt_pk_f16_f32 v88, v75, v79\n"
    "v_cvt_pk_f16_f32 v89, v74, v78\n"
    "v_cvt_pk_f16_f32 v90, v73, v77\n"
    "v_cvt_pk_f16_f32 v91, v72, v76\n"
    "PAD5:\n"
    "s_cbranch_execz GA1\n"
    "v_cvt_pk_f16_f32 v92, v83, v87\n"
    "v_cvt_pk_f16_f32 v93, v82, v86\n"
    "v_cvt_pk_f16_f32 v94, v81, v85\n"
    "v_cvt_pk_f16_f32 v95, v80, v84\n"
    "ds_write_b128 v4, v[108:111] offset:6400\n"
    "s_mov_b64 exec, 1\n"
    "ds_write_b8 v5, v8 offset:107\n"
    "s_mov_b64 exec, -1\n"
    "v_mfma_f32_16x16x32_f16 v[112:115], v[88:91], v[24:27], v[112:115]\n"
    "v_mfma_f32_16x16x32_f16 v[112:115], v[92:95], v[36:39], v[112:115]\n"
    "v_mfma_f32_16x16x32_f16 v[116:119], v[88:91], v[28:31], v[116:119]\n"
    "v_mfma_f32_16x16x32_f16 v[116:119], v[92:95], v[40:43], v[116:119]\n"
    "s_nop 5\n"
    "ds_write_b128 v4, v[112:115] offset:7680\n"
    "s_mov_b64 exec, 1\n"
    "ds_write_b8 v5, v8 offset:115\n"
    "s_mov_b64 exec, -1\n"
    "ds_write_b128 v4, v[116:119] offset:8960\n"
    "s_mov_b64 exec, 1\n"
    "ds_write_b8 v5, v8 offset:123\n"
    "s_mov_b64 exec, -1\n"
    "s_setprio 0\n"
    "s_branch LN2\n"
    "HELP:\n"
    "s_sub_u32 s42, s37, 4\n"
    "v_and_b32 v6, 15, v0\n"
    "v_bfe_u32 v7, v0, 4, 2\n"
    "v_and_b32 v1, 63, v0\n"
    "v_lshlrev_b32 v1, 4, v1\n"
    "v_add_u32 v1, 101376, v1\n"
    "s_mul_i32 s43, s42, 4576\n"
    "s_add_u32 s43, s43, 117216\n"
    "v_lshlrev_b32 v2, 1, v6\n"
    "v_sub_u32 v2, v7, v2\n"
    "v_lshl_add_u32 v2, v2, 4, s43\n"
    "s_mul_i32 s44, s42, 3328\n"
    "s_add_u32 s43, s44, 92596\n"
    "v_lshlrev_b32 v3, 2, v7\n"
    "v_sub_u32 v3, v6, v3\n"
    "v_lshl_add_u32 v3, v3, 2, s43\n"
    "s_mul_i32 s43, s42, 10240\n"
    "s_add_u32 s43, s43, 0\n"
    "v_mul_u32_u24 v4, 320, v7\n"
    "v_lshl_add_u32 v4, v6, 4, v4\n"
    "v_add_u32 v4, s43, v4\n"
    "s_add_u32 s43, s42, 147968\n"
    "v_mov_b32 v5, s43\n"
    "v_mov_b32 v8, 1\n"
    "s_add_u32 s44, s44, 92990\n"
    "v_mov_b32 v9, s44\n"
    "s_mul_i32 s43, s42, 3\n"
    "v_add_u32 v5, s43, v5\n"
    "s_mov_b32 s43, 0x01000100\n"
    "v_mov_b32 v11, 147968\n"
    "s_branch E_20\n"
    "R_19:\n"
    "s_sleep 1\n"
    "s_sub_u32 s39, s39, 1\n"
    "s_cbranch_scc1 ABORT\n"
    "E_20:\n"
    "ds_read_b32 v12, v11 offset:224\n"
    "s_waitcnt lgkmcnt(0)\n"
    "v_cmp_ne_u32 vcc, 6, v12\n"
    "s_cbranch_vccnz R_19\n"
    "ds_read_b128 v[12:15], v1 offset:3072\n"
    "ds_read_b128 v[16:19], v1 offset:4096\n"
    "ds_read_b128 v[20:23], v1 offset:5120\n"
    "ds_read_u16 v10, v9\n"
    "ds_read2_b32 v[24:25], v3 offset0:128 offset1:129\n"
    "ds_read2_b32 v[26:27], v3 offset0:130 offset1:131\n"
    "ds_read2_b32 v[28:29], v3 offset0:144 offset1:145\n"
    "ds_read2_b32 v[30:31], v3 offset0:146 offset1:147\n"
    "ds_read2_b32 v[32:33], v3 offset0:160 offset1:161\n"
    "ds_read2_b32 v[34:35], v3 offset0:162 offset1:163\n"
    "ds_read2_b32 v[36:37], v3 offset0:176 offset1:177\n"
    "ds_read2_b32 v[38:39], v3 offset0:178 offset1:179\n"
    "s_waitcnt lgkmcnt(8)\n"
    "v_perm_b32 v10, v10, v10, s43\n"
    "v_and_b32 v20, v20, v10\n"
    "v_and_b32 v21, v21, v10\n"
    "v_and_b32 v22, v22, v10\n"
    "v_and_b32 v23, v23, v10\n"
    "s_setprio 3\n"
    "s_waitcnt lgkmcnt(0)\n"
    "s_barrier\n"
    "ds_read_b128 v[40:43], v2 offset:3584\n"
    "ds_read_b128 v[44:47], v2 offset:3600\n"
    "ds_read_b128 v[48:51], v2 offset:3072\n"
    "ds_read_b128 v[52:55], v2 offset:3088\n"
    "ds_read_b128 v[56:59], v2 offset:2560\n"
    "ds_read_b128 v[60:63], v2 offset:2576\n"
    "ds_read_b128 v[64:67], v2 offset:2048\n"
    "ds_read_b128 v[68:71], v2 offset:2064\n"
    "s_waitcnt lgkmcnt(6)\n"
    "v_mfma_f32_16x16x32_f16 v[72:75], v[40:43], v[12:15], 0\n"
    "v_mfma_f32_16x16x32_f16 v[76:79], v[44:47], v[16:19], 0\n"
    "s_waitcnt lgkmcnt(4)\n"
    "v_mfma_f32_16x16x32_f16 v[80:83], v[48:51], v[12:15], 0\n"
    "v_mfma_f32_16x16x32_f16 v[84:87], v[52:55], v[16:19], 0\n"
    "s_waitcnt lgkmcnt(2)\n"
    "v_mfma_f32_16x16x32_f16 v[88:91], v[56:59], v[12:15], 0\n"
    "v_mfma_f32_16x16x32_f16 v[92:95], v[60:63], v[16:19], 0\n"
    "s_waitcnt lgkmcnt(0)\n"
    "v_mfma_f32_16x16x32_f16 v[96:99], v[64:67], v[12:15], 0\n"
    "v_mfma_f32_16x16x32_f16 v[100:103], v[68:71], v[16:19], 0\n"
    "v_mfma_f32_16x16x32_f16 v[108:111], v[20:23], v[40:43], 0\n"
    "v_mfma_f32_16x16x32_f16 v[112:115], v[20:23], v[48:51], 0\n"
    "v_mfma_f32_16x16x32_f16 v[116:119], v[20:23], v[56:59], 0\n"
    "v_mfma_f32_16x16x32_f16 v[120:123], v[20:23], v[64:67], 0\n"
    "ds_read_b128 v[40:43], v2 offset:1024\n"
    "ds_read_b128 v[44:47], v2 offset:1040\n"
    "ds_read_b128 v[48:51], v1 offset:0\n"
    "ds_read_b128 v[52:55], v1 offset:1024\n"
    "ds_read_b128 v[56:59], v1 offset:2048\n"
    "ds_read2_b32 v[60:61], v3 offset0:0 offset1:1\n"
    "ds_read2_b32 v[62:63], v3 offset0:2 offset1:3\n"
    "ds_read2_b32 v[64:65], v3 offset0:64 offset1:65\n"
    "ds_read2_b32 v[66:67], v3 offset0:66 offset1:67\n"
    "v_cvt_pk_f16_f32 v12, v75, v79\n"
    "v_cvt_pk_f16_f32 v13, v74, v78\n"
    "v_cvt_pk_f16_f32 v14, v73, v77\n"
    "v_cvt_pk_f16_f32 v15, v72, v76\n"
    "s_nop 1\n"
    "v_mfma_f32_16x16x32_f16 v[108:111], v[12:15], v[24:27], v[108:111]\n"
    "v_mfma_f32_16x16x32_f16 v[112:115], v[12:15], v[28:31], v[112:115]\n"
    "v_mfma_f32_16x16x32_f16 v[116:119], v[12:15], v[32:35], v[116:119]\n"
    "v_mfma_f32_16x16x32_f16 v[120:123], v[12:15], v[36:39], v[120:123]\n"
    "v_cvt_pk_f16_f32 v104, v83, v87\n"
    "v_cvt_pk_f16_f32 v105, v82, v86\n"
    "v_cvt_pk_f16_f32 v106, v81, v85\n"
    "v_cvt_pk_f16_f32 v107, v80, v84\n"
    "s_nop 1\n"
    "v_mfma_f32_16x16x32_f16 v[112:115], v[104:107], v[24:27], v[112:115]\n"
    "v_mfma_f32_16x16x32_f16 v[116:119], v[104:107], v[28:31], v[116:119]\n"
    "v_mfma_f32_16x16x32_f16 v[120:123], v[104:107], v[32:35], v[120:123]\n"
    "v_cvt_pk_f16_f32 v12, v91, v95\n"
    "v_cvt_pk_f16_f32 v13, v90, v94\n"
    "v_cvt_pk_f16_f32 v14, v89, v93\n"
    "v_cvt_pk_f16_f32 v15, v88, v92\n"
    "s_waitcnt lgkmcnt(4)\n"
    "v_mfma_f32_16x16x32_f16 v[72:75], v[40:43], v[48:51], 0\n"
    "v_mfma_f32_16x16x32_f16 v[76:79], v[40:43], v[52:55], 0\n"
    "v_mfma_f32_16x16x32_f16 v[80:83], v[40:43], v[56:59], 0\n"
    "v_mfma_f32_16x16x32_f16 v[84:87], v[44:47], v[56:59], 0\n"
    "v_mfma_f32_16x16x32_f16 v[116:119], v[12:15], v[24:27], v[116:119]\n"
    "v_mfma_f32_16x16x32_f16 v[120:123], v[12:15], v[28:31], v[120:123]\n"
    "v_cvt_pk_f16_f32 v104, v99, v103\n"
    "v_cvt_pk_f16_f32 v105, v98, v102\n"
    "PAD6:\n"
    "s_cbranch_execz S1\n"
    "v_cvt_pk_f16_f32 v106, v97, v101\n"
    "v_cvt_pk_f16_f32 v107, v96, v100\n"
    "s_nop 1\n"
    "v_mfma_f32_16x16x32_f16 v[120:123], v[104:107], v[24:27], v[120:123]\n"
    "v_cvt_pk_f16_f32 v12, v75, v79\n"
    "v_cvt_pk_f16_f32 v13, v74, v78\n"
    "v_cvt_pk_f16_f32 v14, v73, v77\n"
    "v_cvt_pk_f16_f32 v15, v72, v76\n"
    "v_cvt_pk_f16_f32 v16, v83, v87\n"
    "v_cvt_pk_f16_f32 v17, v82, v86\n"
    "v_cvt_pk_f16_f32 v18, v81, v85\n"
    "v_cvt_pk_f16_f32 v19, v80, v84\n"
    "s_waitcnt lgkmcnt(0)\n"
    "v_mfma_f32_16x16x32_f16 v[120:123], v[12:15], v[60:63], v[120:123]\n"
    "v_mfma_f32_16x16x32_f16 v[120:123], v[16:19], v[64:67], v[120:123]\n"
    "ds_write_b128 v4, v[108:111] offset:5120\n"
    "ds_write_b128 v4, v[112:115] offset:6400\n"
    "ds_write_b128 v4, v[116:119] offset:7680\n"
    "s_nop 4\n"
    "ds_write_b128 v4, v[120:123] offset:8960\n"
    "s_mov_b64 exec, 1\n"
    "ds_write_b8 v5, v8 offset:44\n"
    "s_mov_b64 exec, -1\n"
    "s_setprio 0\n"
    "s_branch MISC2\n"
    "NEAR:\n"
    "v_and_b32 v6, 15, v0\n"
    "v_bfe_u32 v7, v0, 4, 2\n"
    "v_and_b32 v1, 63, v0\n"
    "v_lshlrev_b32 v1, 4, v1\n"
    "v_add_u32 v1, 101376, v1\n"
    "s_mul_i32 s43, s42, 4576\n"
    "s_add_u32 s43, s43, 117216\n"
    "v_lshlrev_b32 v2, 1, v6\n"
    "v_sub_u32 v2, v7, v2\n"
    "v_lshl_add_u32 v2, v2, 4, s43\n"
    "s_mul_i32 s44, s42, 3328\n"
    "s_add_u32 s43, s44, 92596\n"
    "v_lshlrev_b32 v3, 2, v7\n"
    "v_sub_u32 v3, v6, v3\n"
    "v_lshl_add_u32 v3, v3, 2, s43\n"
    "s_mul_i32 s43, s42, 10240\n"
    "s_add_u32 s43, s43, 0\n"
    "v_mul_u32_u24 v4, 320, v7\n"
    "v_lshl_add_u32 v4, v6, 4, v4\n"
    "v_add_u32 v4, s43, v4\n"
    "s_add_u32 s43, s42, 147968\n"
    "v_mov_b32 v5, s43\n"
    "v_mov_b32 v8, 1\n"
    "v_lshl_add_u32 v3, v7, 3, v6\n"
    "v_add_u32 v3, 1, v3\n"
    "v_and_b32 v9, 1, v3\n"
    "v_mul_u32_u24 v9, 2110, v9\n"
    "s_add_u32 s44, s44, 91392\n"
    "v_lshl_add_u32 v3, v3, 1, s44\n"
    "v_add_u32 v3, v3, v9\n"
    "v_add_u32 v1, 512, v3\n"
    "s_mul_i32 s43, s42, 3\n"
    "v_add_u32 v10, s43, v5\n"
    "s_setprio 2\n"
    "v_mov_b32 v11, 147968\n"
    "s_branch E_22\n"
    "R_21:\n"
    "s_sub_u32 s39, s39, 1\n"
    "s_cbranch_scc1 ABORT\n"
    "E_22:\n"
    "ds_read_b32 v12, v11 offset:224\n"
    "s_waitcnt lgkmcnt(0)\n"
    "v_cmp_ne_u32 vcc, 6, v12\n"
    "s_cbranch_vccnz R_21\n"
    "ds_read2_b32 v[88:89], v3 offset0:112 offset1:113\n"
    "ds_read2_b32 v[90:91], v3 offset0:114 offset1:115\n"
    "ds_read2_b32 v[120:121], v1 offset0:112 offset1:113\n"
    "ds_read2_b32 v[122:123], v1 offset0:114 offset1:115\n"
    "ds_read2_b32 v[84:85], v3 offset0:96 offset1:97\n"
    "ds_read2_b32 v[86:87], v3 offset0:98 offset1:99\n"
    "ds_read2_b32 v[116:117], v1 offset0:96 offset1:97\n"
    "ds_read2_b32 v[118:119], v1 offset0:98 offset1:99\n"
    "s_barrier\n"
    "ds_read_b128 v[24:27], v2 offset:4032\n"
    "ds_read_b128 v[28:31], v2 offset:3968\n"
    "ds_read_b128 v[32:35], v2 offset:3904\n"
    "s_waitcnt lgkmcnt(2)\n"
    "v_mfma_f32_16x16x32_f16 v[48:51], v[88:91], v[24:27], 0\n"
    "ds_read2_b32 v[80:81], v3 offset0:80 offset1:81\n"
    "ds_read2_b32 v[82:83], v3 offset0:82 offset1:83\n"
    "ds_read2_b32 v[112:113], v1 offset0:80 offset1:81\n"
    "ds_read2_b32 v[114:115], v1 offset0:82 offset1:83\n"
    "ds_read_b128 v[36:39], v2 offset:3840\n"
    "v_mfma_f32_16x16x32_f16 v[52:55], v[120:123], v[24:27], 0\n"
    "s_waitcnt lgkmcnt(6)\n"
    "v_mfma_f32_16x16x32_f16 v[48:51], v[84:87], v[28:31], v[48:51]\n"
    "ds_read2_b32 v[76:77], v3 offset0:64 offset1:65\n"
    "ds_read2_b32 v[78:79], v3 offset0:66 offset1:67\n"
    "ds_read2_b32 v[108:109], v1 offset0:64 offset1:65\n"
    "ds_read2_b32 v[110:111], v1 offset0:66 offset1:67\n"
    "ds_read_b128 v[40:43], v2 offset:3776\n"
    "v_mfma_f32_16x16x32_f16 v[52:55], v[116:119], v[28:31], v[52:55]\n"
    "s_waitcnt lgkmcnt(8)\n"
    "v_mfma_f32_16x16x32_f16 v[48:51], v[80:83], v[32:35], v[48:51]\n"
    "ds_read2_b32 v[72:73], v3 offset0:48 offset1:49\n"
    "ds_read2_b32 v[74:75], v3 offset0:50 offset1:51\n"
    "ds_read2_b32 v[104:105], v1 offset0:48 offset1:49\n"
    "ds_read2_b32 v[106:107], v1 offset0:50 offset1:51\n"
    "ds_read_b128 v[44:47], v2 offset:3712\n"
    "s_waitcnt lgkmcnt(11)\n"
    "v_mfma_f32_16x16x32_f16 v[52:55], v[112:115], v[32:35], v[52:55]\n"
    "s_waitcnt lgkmcnt(8)\n"
    "v_mfma_f32_16x16x32_f16 v[48:51], v[76:79], v[36:39], v[48:51]\n"
    "ds_read2_b32 v[68:69], v3 offset0:32 offset1:33\n"
    "ds_read2_b32 v[70:71], v3 offset0:34 offset1:35\n"
    "ds_read2_b32 v[100:101], v1 offset0:32 offset1:33\n"
    "ds_read2_b32 v[102:103], v1 offset0:34 offset1:35\n"
    "ds_read_b128 v[24:27], v2 offset:3648\n"
    "s_waitcnt lgkmcnt(11)\n"
    "v_mfma_f32_16x16x32_f16 v[52:55], v[108:111], v[36:39], v[52:55]\n"
    "s_waitcnt lgkmcnt(8)\n"
    "v_mfma_f32_16x16x32_f16 v[48:51], v[72:75], v[40:43], v[48:51]\n"
    "ds_read2_b32 v[64:65], v3 offset0:16 offset1:17\n"
    "ds_read2_b32 v[66:67], v3 offset0:18 offset1:19\n"
    "ds_read2_b32 v[96:97], v1 offset0:16 offset1:17\n"
    "ds_read2_b32 v[98:99], v1 offset0:18 offset1:19\n"
    "ds_read_b128 v[28:31], v2 offset:3584\n"
    "s_waitcnt lgkmcnt(11)\n"
    "v_mfma_f32_16x16x32_f16 v[52:55], v[104:107], v[40:43], v[52:55]\n"
    "s_waitcnt lgkmcnt(8)\n"
    "v_mfma_f32_16x16x32_f16 v[48:51], v[68:71], v[44:47], v[48:51]\n"
    "ds_read2_b32 v[60:61], v3 offset0:0 offset1:1\n"
    "ds_read2_b32 v[62:63], v3 offset0:2 offset1:3\n"
    "ds_read2_b32 v[92:93], v1 offset0:0 offset1:1\n"
    "ds_read2_b32 v[94:95], v1 offset0:2 offset1:3\n"
    "ds_read2_b32 v[124:125], v1 offset0:128 offset1:129\n"
    "ds_read2_b32 v[126:127], v1 offset0:130 offset1:131\n"
    "ds_read_b128 v[32:35], v2 offset:3520\n"
    "s_waitcnt lgkmcnt(13)\n"
    "v_mfma_f32_16x16x32_f16 v[52:55], v[100:103], v[44:47], v[52:55]\n"
    "s_waitcnt lgkmcnt(10)\n"
    "v_mfma_f32_16x16x32_f16 v[48:51], v[64:67], v[24:27], v[48:51]\n"
    "ds_read_b128 v[36:39], v2 offset:3456\n"
    "ds_read_b128 v[40:43], v2 offset:3392\n"
    "s_waitcnt lgkmcnt(10)\n"
    "v_mfma_f32_16x16x32_f16 v[52:55], v[96:99], v[24:27], v[52:55]\n"
    "s_waitcnt lgkmcnt(7)\n"
    "v_mfma_f32_16x16x32_f16 v[48:51], v[60:63], v[28:31], v[48:51]\n"
    "ds_read_b128 v[44:47], v2 offset:3328\n"
    "ds_read_b128 v[24:27], v2 offset:3264\n"
    "s_waitcnt lgkmcnt(7)\n"
    "v_mfma_f32_16x16x32_f16 v[52:55], v[92:95], v[28:31], v[52:55]\n"
    "PAD7:\n"
    "s_cbranch_execz S1\n"
    "s_waitcnt lgkmcnt(5)\n"
    "v_mfma_f32_16x16x32_f16 v[56:59], v[124:127], v[28:31], 0\n"
    "s_waitcnt lgkmcnt(4)\n"
    "v_mfma_f32_16x16x32_f16 v[52:55], v[88:91], v[32:35], v[52:55]\n"
    "ds_read_b128 v[28:31], v2 offset:3200\n"
    "v_mfma_f32_16x16x32_f16 v[56:59], v[120:123], v[32:35], v[56:59]\n"
    "s_waitcnt lgkmcnt(4)\n"
    "v_mfma_f32_16x16x32_f16 v[52:55], v[84:87], v[36:39], v[52:55]\n"
    "ds_read_b128 v[32:35], v2 offset:3136\n"
    "v_mfma_f32_16x16x32_f16 v[56:59], v[116:119], v[36:39], v[56:59]\n"
    "ds_write_b128 v4, v[48:51] offset:0\n"
    "s_mov_b64 exec, 1\n"
    "ds_write_b8 v5, v8 offset:64\n"
    "s_mov_b64 exec, -1\n"
    "s_waitcnt lgkmcnt(6)\n"
    "v_mfma_f32_16x16x32_f16 v[52:55], v[80:83], v[40:43], v[52:55]\n"
    "ds_read_b128 v[36:39], v2 offset:3072\n"
    "v_mfma_f32_16x16x32_f16 v[56:59], v[112:115], v[40:43], v[56:59]\n"
    "s_waitcnt lgkmcnt(6)\n"
    "v_mfma_f32_16x16x32_f16 v[52:55], v[76:79], v[44:47], v[52:55]\n"
    "ds_read_b128 v[40:43], v2 offset:3008\n"
    "v_mfma_f32_16x16x32_f16 v[56:59], v[108:111], v[44:47], v[56:59]\n"
    "s_waitcnt lgkmcnt(6)\n"
    "v_mfma_f32_16x16x32_f16 v[52:55], v[72:75], v[24:27], v[52:55]\n"
    "ds_read_b128 v[44:47], v2 offset:2944\n"
    "v_mfma_f32_16x16x32_f16 v[56:59], v[104:107], v[24:27], v[56:59]\n"
    "s_waitcnt lgkmcnt(6)\n"
    "v_mfma_f32_16x16x32_f16 v[52:55], v[68:71], v[28:31], v[52:55]\n"
    "ds_read_b128 v[24:27], v2 offset:2880\n"
    "v_mfma_f32_16x16x32_f16 v[56:59], v[100:103], v[28:31], v[56:59]\n"
    "s_waitcnt lgkmcnt(6)\n"
    "v_mfma_f32_16x16x32_f16 v[52:55], v[64:67], v[32:35], v[52:55]\n"
    "ds_read_b128 v[28:31], v2 offset:2816\n"
    "v_mfma_f32_16x16x32_f16 v[56:59], v[96:99], v[32:35], v[56:59]\n"
    "s_waitcnt lgkmcnt(4)\n"
    "v_mfma_f32_16x16x32_f16 v[52:55], v[60:63], v[36:39], v[52:55]\n"
    "ds_read_b128 v[32:35], v2 offset:2752\n"
    "v_mfma_f32_16x16x32_f16 v[56:59], v[92:95], v[36:39], v[56:59]\n"
    "v_mfma_f32_16x16x32_f16 v[48:51], v[124:127], v[36:39], 0\n"
    "s_waitcnt lgkmcnt(4)\n"
    "v_mfma_f32_16x16x32_f16 v[56:59], v[88:91], v[40:43], v[56:59]\n"
    "ds_read_b128 v[36:39], v2 offset:2688\n"
    "v_mfma_f32_16x16x32_f16 v[48:51], v[120:123], v[40:43], v[48:51]\n"
    "s_waitcnt lgkmcnt(4)\n"
    "v_mfma_f32_16x16x32_f16 v[56:59], v[84:87], v[44:47], v[56:59]\n"
    "ds_read_b128 v[40:43], v2 offset:2624\n"
    "v_mfma_f32_16x16x32_f16 v[48:51], v[116:119], v[44:47], v[48:51]\n"
    "ds_write_b128 v4, v[52:55] offset:1280\n"
    "s_mov_b64 exec, 1\n"
    "ds_write_b8 v5, v8 offset:72\n"
    "s_mov_b64 exec, -1\n"
    "ds_read_b32 v11, v10 offset:44\n"
    "s_waitcnt lgkmcnt(7)\n"
    "v_mfma_f32_16x16x32_f16 v[56:59], v[80:83], v[24:27], v[56:59]\n"
    "ds_read_b128 v[44:47], v2 offset:2560\n"
    "v_mfma_f32_16x16x32_f16 v[48:51], v[112:115], v[24:27], v[48:51]\n"
    "s_waitcnt lgkmcnt(7)\n"
    "v_mfma_f32_16x16x32_f16 v[56:59], v[76:79], v[28:31], v[56:59]\n"
    "ds_read_b128 v[24:27], v2 offset:2496\n"
    "v_mfma_f32_16x16x32_f16 v[48:51], v[108:111], v[28:31], v[48:51]\n"
    "s_waitcnt lgkmcnt(7)\n"
    "v_mfma_f32_16x16x32_f16 v[56:59], v[72:75], v[32:35], v[56:59]\n"
    "ds_read_b128 v[28:31], v2 offset:2432\n"
    "v_mfma_f32_16x16x32_f16 v[48:51], v[104:107], v[32:35], v[48:51]\n"
    "s_waitcnt lgkmcnt(3)\n"
    "v_cmp_ne_u32 vcc, 1, v11\n"
    "s_cbranch_vccnz NEARW\n"
    "NEARC:\n"
    "ds_read_b128 v[52:55], v4 offset:5120\n"
    "v_mfma_f32_16x16x32_f16 v[56:59], v[68:71], v[36:39], v[56:59]\n"
    "ds_read_b128 v[32:35], v2 offset:2368\n"
    "v_mfma_f32_16x16x32_f16 v[48:51], v[100:103], v[36:39], v[48:51]\n"
    "v_mfma_f32_16x16x32_f16 v[56:59], v[64:67], v[40:43], v[56:59]\n"
    "ds_read_b128 v[36:39], v2 offset:2304\n"
    "v_mfma_f32_16x16x32_f16 v[48:51], v[96:99], v[40:43], v[48:51]\n"
    "s_waitcnt lgkmcnt(5)\n"
    "v_mfma_f32_16x16x32_f16 v[56:59], v[60:63], v[44:47], v[56:59]\n"
    "ds_read_b128 v[40:43], v2 offset:2240\n"
    "v_mfma_f32_16x16x32_f16 v[48:51], v[92:95], v[44:47], v[48:51]\n"
    "s_waitcnt lgkmcnt(3)\n"
    "v_mfma_f32_16x16x32_f16 v[52:55], v[124:127], v[44:47], v[52:55]\n"
    "v_mfma_f32_16x16x32_f16 v[48:51], v[88:91], v[24:27], v[48:51]\n"
    "ds_read_b128 v[44:47], v2 offset:2176\n"
    "v_mfma_f32_16x16x32_f16 v[52:55], v[120:123], v[24:27], v[52:55]\n"
    "v_mfma_f32_16x16x32_f16 v[48:51], v[84:87], v[28:31], v[48:51]\n"
    "ds_read_b128 v[24:27], v2 offset:2112\n"
    "v_mfma_f32_16x16x32_f16 v[52:55], v[116:119], v[28:31], v[52:55]\n"
    "ds_write_b128 v4, v[56:59] offset:2560\n"
    "s_mov_b64 exec, 1\n"
    "ds_write_b8 v5, v8 offset:80\n"
    "s_mov_b64 exec, -1\n"
    "ds_read_b128 v[56:59], v4 offset:6400\n"
    "s_waitcnt lgkmcnt(7)\n"
    "v_mfma_f32_16x16x32_f16 v[48:51], v[80:83], v[32:35], v[48:51]\n"
    "ds_read_b128 v[28:31], v2 offset:2048\n"
    "v_mfma_f32_16x16x32_f16 v[52:55], v[112:115], v[32:35], v[52:55]\n"
    "s_waitcnt lgkmcnt(7)\n"
    "v_mfma_f32_16x16x32_f16 v[48:51], v[76:79], v[36:39], v[48:51]\n"
    "ds_read_b128 v[32:35], v2 offset:1984\n"
    "v_mfma_f32_16x16x32_f16 v[52:55], v[108:111], v[36:39], v[52:55]\n"
    "s_waitcnt lgkmcnt(7)\n"
    "v_mfma_f32_16x16x32_f16 v[48:51], v[72:75], v[40:43], v[48:51]\n"
    "ds_read_b128 v[36:39], v2 offset:1920\n"
    "v_mfma_f32_16x16x32_f16 v[52:55], v[104:107], v[40:43], v[52:55]\n"
    "s_waitcnt lgkmcnt(7)\n"
    "v_mfma_f32_16x16x32_f16 v[48:51], v[68:71], v[44:47], v[48:51]\n"
    "ds_read_b128 v[40:43], v2 offset:1856\n"
    "v_mfma_f32_16x16x32_f16 v[52:55], v[100:103], v[44:47], v[52:55]\n"
    "s_waitcnt lgkmcnt(7)\n"
    "v_mfma_f32_16x16x32_f16 v[48:51], v[64:67], v[24:27], v[48:51]\n"
    "ds_read_b128 v[44:47], v2 offset:1792\n"
    "v_mfma_f32_16x16x32_f16 v[52:55], v[96:99], v[24:27], v[52:55]\n"
    "s_waitcnt lgkmcnt(4)\n"
    "v_mfma_f32_16x16x32_f16 v[48:51], v[60:63], v[28:31], v[48:51]\n"
    "ds_read_b128 v[24:27], v2 offset:1728\n"
    "v_mfma_f32_16x16x32_f16 v[52:55], v[92:95], v[28:31], v[52:55]\n"
    "v_mfma_f32_16x16x32_f16 v[56:59], v[124:127], v[28:31], v[56:59]\n"
    "s_waitcnt lgkmcnt(4)\n"
    "v_mfma_f32_16x16x32_f16 v[52:55], v[88:91], v[32:35], v[52:55]\n"
    "ds_read_b128 v[28:31], v2 offset:1664\n"
    "v_mfma_f32_16x16x32_f16 v[56:59], v[120:123], v[32:35], v[56:59]\n"
    "s_waitcnt lgkmcnt(4)\n"
    "v_mfma_f32_16x16x32_f16 v[52:55], v[84:87], v[36:39], v[52:55]\n"
    "ds_read_b128 v[32:35], v2 offset:1600\n"
    "v_mfma_f32_16x16x32_f16 v[56:59], v[116:119], v[36:39], v[56:59]\n"
    "ds_write_b128 v4, v[48:51] offset:3840\n"
    "s_mov_b64 exec, 1\n"
    "ds_write_b8 v5, v8 offset:88\n"
    "s_mov_b64 exec, -1\n"
    "ds_read_b128 v[48:51], v4 offset:7680\n"
    "s_waitcnt lgkmcnt(7)\n"
    "v_mfma_f32_16x16x32_f16 v[52:55], v[80:83], v[40:43], v[52:55]\n"
    "ds_read_b128 v[36:39], v2 offset:1536\n"
    "v_mfma_f32_16x16x32_f16 v[56:59], v[112:115], v[40:43], v[56:59]\n"
    "s_waitcnt lgkmcnt(7)\n"
    "v_mfma_f32_16x16x32_f16 v[52:55], v[76:79], v[44:47], v[52:55]\n"
    "ds_read_b128 v[40:43], v2 offset:1472\n"
    "v_mfma_f32_16x16x32_f16 v[56:59], v[108:111], v[44:47], v[56:59]\n"
    "s_waitcnt lgkmcnt(7)\n"
    "v_mfma_f32_16x16x32_f16 v[52:55], v[72:75], v[24:27], v[52:55]\n"
    "ds_read_b128 v[44:47], v2 offset:1408\n"
    "v_mfma_f32_16x16x32_f16 v[56:59], v[104:107], v[24:27], v[56:59]\n"
    "s_waitcnt lgkmcnt(7)\n"
    "PAD8:\n"
    "s_cbranch_execz S1\n"
    "v_mfma_f32_16x16x32_f16 v[52:55], v[68:71], v[28:31], v[52:55]\n"
    "ds_read_b128 v[24:27], v2 offset:1344\n"
    "v_mfma_f32_16x16x32_f16 v[56:59], v[100:103], v[28:31], v[56:59]\n"
    "s_waitcnt lgkmcnt(7)\n"
    "v_mfma_f32_16x16x32_f16 v[52:55], v[64:67], v[32:35], v[52:55]\n"
    "ds_read_b128 v[28:31], v2 offset:1280\n"
    "v_mfma_f32_16x16x32_f16 v[56:59], v[96:99], v[32:35], v[56:59]\n"
    "s_waitcnt lgkmcnt(4)\n"
    "v_mfma_f32_16x16x32_f16 v[52:55], v[60:63], v[36:39], v[52:55]\n"
    "ds_read_b128 v[32:35], v2 offset:1216\n"
    "v_mfma_f32_16x16x32_f16 v[56:59], v[92:95], v[36:39], v[56:59]\n"
    "v_mfma_f32_16x16x32_f16 v[48:51], v[124:127], v[36:39], v[48:51]\n"
    "s_waitcnt lgkmcnt(4)\n"
    "v_mfma_f32_16x16x32_f16 v[56:59], v[88:91], v[40:43], v[56:59]\n"
    "ds_read_b128 v[36:39], v2 offset:1152\n"
    "v_mfma_f32_16x16x32_f16 v[48:51], v[120:123], v[40:43], v[48:51]\n"
    "s_waitcnt lgkmcnt(4)\n"
    "v_mfma_f32_16x16x32_f16 v[56:59], v[84:87], v[44:47], v[56:59]\n"
    "ds_read_b128 v[40:43], v2 offset:1088\n"
    "v_mfma_f32_16x16x32_f16 v[48:51], v[116:119], v[44:47], v[48:51]\n"
    "ds_write_b128 v4, v[52:55] offset:5120\n"
    "s_mov_b64 exec, 1\n"
    "ds_write_b8 v5, v8 offset:96\n"
    "s_mov_b64 exec, -1\n"
    "ds_read_b128 v[52:55], v4 offset:8960\n"
    "s_waitcnt lgkmcnt(7)\n"
    "v_mfma_f32_16x16x32_f16 v[56:59], v[80:83], v[24:27], v[56:59]\n"
    "ds_read_b128 v[44:47], v2 offset:1024\n"
    "v_mfma_f32_16x16x32_f16 v[48:51], v[112:115], v[24:27], v[48:51]\n"
    "s_waitcnt lgkmcnt(7)\n"
    "v_mfma_f32_16x16x32_f16 v[56:59], v[76:79], v[28:31], v[56:59]\n"
    "ds_read_b128 v[24:27], v2 offset:960\n"
    "v_mfma_f32_16x16x32_f16 v[48:51], v[108:111], v[28:31], v[48:51]\n"
    "s_waitcnt lgkmcnt(7)\n"
    "v_mfma_f32_16x16x32_f16 v[56:59], v[72:75], v[32:35], v[56:59]\n"
    "ds_read_b128 v[28:31], v2 offset:896\n"
    "v_mfma_f32_16x16x32_f16 v[48:51], v[104:107], v[32:35], v[48:51]\n"
    "s_waitcnt lgkmcnt(7)\n"
    "v_mfma_f32_16x16x32_f16 v[56:59], v[68:71], v[36:39], v[56:59]\n"
    "ds_read_b128 v[32:35], v2 offset:832\n"
    "v_mfma_f32_16x16x32_f16 v[48:51], v[100:103], v[36:39], v[48:51]\n"
    "s_waitcnt lgkmcnt(7)\n"
    "v_mfma_f32_16x16x32_f16 v[56:59], v[64:67], v[40:43], v[56:59]\n"
    "ds_read_b128 v[36:39], v2 offset:768\n"
    "v_mfma_f32_16x16x32_f16 v[48:51], v[96:99], v[40:43], v[48:51]\n"
    "s_waitcnt lgkmcnt(4)\n"
    "v_mfma_f32_16x16x32_f16 v[56:59], v[60:63], v[44:47], v[56:59]\n"
    "ds_read_b128 v[40:43], v2 offset:704\n"
    "v_mfma_f32_16x16x32_f16 v[48:51], v[92:95], v[44:47], v[48:51]\n"
    "v_mfma_f32_16x16x32_f16 v[52:55], v[124:127], v[44:47], v[52:55]\n"
    "s_waitcnt lgkmcnt(4)\n"
    "v_mfma_f32_16x16x32_f16 v[48:51], v[88:91], v[24:27], v[48:51]\n"
    "ds_read_b128 v[44:47], v2 offset:640\n"
    "v_mfma_f32_16x16x32_f16 v[52:55], v[120:123], v[24:27], v[52:55]\n"
    "s_waitcnt lgkmcnt(4)\n"
    "v_mfma_f32_16x16x32_f16 v[48:51], v[84:87], v[28:31], v[48:51]\n"
    "ds_read_b128 v[24:27], v2 offset:576\n"
    "v_mfma_f32_16x16x32_f16 v[52:55], v[116:119], v[28:31], v[52:55]\n"
    "ds_write_b128 v4, v[56:59] offset:6400\n"
    "s_mov_b64 exec, 1\n"
    "ds_write_b8 v5, v8 offset:104\n"
    "s_mov_b64 exec, -1\n"
    "s_waitcnt lgkmcnt(6)\n"
    "v_mfma_f32_16x16x32_f16 v[48:51], v[80:83], v[32:35], v[48:51]\n"
    "ds_read_b128 v[28:31], v2 offset:512\n"
    "v_mfma_f32_16x16x32_f16 v[52:55], v[112:115], v[32:35], v[52:55]\n"
    "s_waitcnt lgkmcnt(6)\n"
    "v_mfma_f32_16x16x32_f16 v[48:51], v[76:79], v[36:39], v[48:51]\n"
    "ds_read_b128 v[32:35], v2 offset:448\n"
    "v_mfma_f32_16x16x32_f16 v[52:55], v[108:111], v[36:39], v[52:55]\n"
    "s_waitcnt lgkmcnt(6)\n"
    "v_mfma_f32_16x16x32_f16 v[48:51], v[72:75], v[40:43], v[48:51]\n"
    "ds_read_b128 v[36:39], v2 offset:384\n"
    "v_mfma_f32_16x16x32_f16 v[52:55], v[104:107], v[40:43], v[52:55]\n"
    "s_waitcnt lgkmcnt(6)\n"
    "v_mfma_f32_16x16x32_f16 v[48:51], v[68:71], v[44:47], v[48:51]\n"
    "ds_read_b128 v[40:43], v2 offset:320\n"
    "v_mfma_f32_16x16x32_f16 v[52:55], v[100:103], v[44:47], v[52:55]\n"
    "s_waitcnt lgkmcnt(6)\n"
    "v_mfma_f32_16x16x32_f16 v[48:51], v[64:67], v[24:27], v[48:51]\n"
    "ds_read_b128 v[44:47], v2 offset:256\n"
    "v_mfma_f32_16x16x32_f16 v[52:55], v[96:99], v[24:27], v[52:55]\n"
    "s_waitcnt lgkmcnt(4)\n"
    "v_mfma_f32_16x16x32_f16 v[48:51], v[60:63], v[28:31], v[48:51]\n"
    "ds_read_b128 v[24:27], v2 offset:192\n"
    "v_mfma_f32_16x16x32_f16 v[52:55], v[92:95], v[28:31], v[52:55]\n"
    "s_waitcnt lgkmcnt(4)\n"
    "v_mfma_f32_16x16x32_f16 v[52:55], v[88:91], v[32:35], v[52:55]\n"
    "ds_read_b128 v[28:31], v2 offset:128\n"
    "s_waitcnt lgkmcnt(4)\n"
    "v_mfma_f32_16x16x32_f16 v[52:55], v[84:87], v[36:39], v[52:55]\n"
    "ds_read_b128 v[32:35], v2 offset:64\n"
    "ds_write_b128 v4, v[48:51] offset:7680\n"
    "s_mov_b64 exec, 1\n"
    "ds_write_b8 v5, v8 offset:112\n"
    "s_mov_b64 exec, -1\n"
    "s_waitcnt lgkmcnt(6)\n"
    "v_mfma_f32_16x16x32_f16 v[52:55], v[80:83], v[40:43], v[52:55]\n"
    "ds_read_b128 v[36:39], v2 offset:0\n"
    "s_waitcnt lgkmcnt(6)\n"
    "v_mfma_f32_16x16x32_f16 v[52:55], v[76:79], v[44:47], v[52:55]\n"
    "s_waitcnt lgkmcnt(5)\n"
    "v_mfma_f32_16x16x32_f16 v[52:55], v[72:75], v[24:27], v[52:55]\n"
    "s_waitcnt lgkmcnt(4)\n"
    "v_mfma_f32_16x16x32_f16 v[52:55], v[68:71], v[28:31], v[52:55]\n"
    "s_waitcnt lgkmcnt(3)\n"
    "v_mfma_f32_16x16x32_f16 v[52:55], v[64:67], v[32:35], v[52:55]\n"
    "s_waitcnt lgkmcnt(0)\n"
    "v_mfma_f32_16x16x32_f16 v[52:55], v[60:63], v[36:39], v[52:55]\n"
    "s_nop 7\n"
    "ds_write_b128 v4, v[52:55] offset:8960\n"
    "s_mov_b64 exec, 1\n"
    "ds_write_b8 v5, v8 offset:120\n"
    "s_mov_b64 exec, -1\n"
    "s_setprio 0\n"
    "s_branch LN2\n"
    "NEARW:\n"
    "s_branch E_24\n"
    "R_23:\n"
    "s_sleep 1\n"
    "s_sub_u32 s39, s39, 1\n"
    "s_cbranch_scc1 ABORT\n"
    "E_24:\n"
    "ds_read_b32 v11, v10 offset:44\n"
    "s_waitcnt lgkmcnt(0)\n"
    "v_cmp_ne_u32 vcc, 1, v11\n"
    "s_cbranch_vccnz R_23\n"
    "s_branch NEARC\n"
    "PAD9:\n"
    "s_cbranch_execz S1\n"
    "EPI:\n"
    "v_bfe_u32 v2, v0, 3, 4\n"
    "v_bfe_u32 v4, v0, 1, 2\n"
    "v_mul_u32_u24 v4, 320, v4\n"
    "v_lshl_add_u32 v2, v2, 4, v4\n"
    "v_and_b32 v4, 1, v0\n"
    "v_lshl_add_u32 v2, v4, 3, v2\n"
    "s_lshr_b32 s42, s37, 1\n"
    "s_and_b32 s42, s42, 1\n"
    "s_mul_i32 s43, s42, 1280\n"
    "v_add_u32 v2, s43, v2\n"
    "s_lshl_b32 s43, s42, 3\n"
    "v_add_u32 v13, s43, v14\n"
    "s_mul_i32 s43, s38, 16512\n"
    "s_add_u32 s43, s43, 118784\n"
    "v_lshl_add_u32 v1, v0, 4, s43\n"
    "s_and_b32 s42, s38, 1\n"
    "s_lshl_b32 s42, s42, 4\n"
    "s_lshr_b64 s[52:53], s[52:53], s42\n"
    "s_lshl_b32 s54, s52, 16\n"
    "s_and_b32 s55, s52, 0xffff0000\n"
    "s_lshl_b32 s56, s53, 16\n"
    "v_mov_b32 v43, s36\n"
    "v_mov_b32 v47, s36\n"
    "s_mov_b32 s46, 0x01010101\n"
    "s_movk_i32 s47, 0x0101\n"
    "s_movk_i32 s48, 4096\n"
    "s_movk_i32 s49, 8192\n"
    "s_movk_i32 s50, 12288\n"
    "s_setprio 2\n"
    "s_waitcnt lgkmcnt(0)\n"
    "s_barrier\n"
    "ds_read_b128 v[16:19], v12 offset:32\n"
    "ds_read_b64 v[20:21], v12 offset:48\n"
    "ds_read_b128 v[4:7], v12 offset:64\n"
    "ds_read_b64 v[8:9], v12 offset:80\n"
    "ds_read_b32 v24, v3 offset:3072\n"
    "ds_read_b32 v25, v3 offset:7648\n"
    "ds_read_b32 v26, v3 offset:12224\n"
    "ds_read_b32 v27, v3 offset:2048\n"
    "ds_read_b32 v28, v3 offset:6624\n"
    "ds_read_b32 v29, v3 offset:11200\n"
    "ds_read_b32 v30, v3 offset:1024\n"
    "ds_read_b32 v31, v3 offset:5600\n"
    "ds_read_b32 v32, v3 offset:10176\n"
    "ds_read_b32 v33, v3 offset:0\n"
    "ds_read_b32 v34, v3 offset:4576\n"
    "ds_read_b32 v35, v3 offset:9152\n"
    "s_waitcnt lgkmcnt(0)\n"
    "v_mul_f32 v5, v5, v16\n"
    "v_mul_f32 v16, s54, v16\n"
    "v_mul_f32 v7, v7, v18\n"
    "v_mul_f32 v18, s55, v18\n"
    "v_mul_f32 v9, v9, v20\n"
    "v_mul_f32 v20, s56, v20\n"
    "v_fma_mix_f32 v64, v24, v16, 0 op_sel:[1,0,0] op_sel_hi:[1,0,0]\n"
    "v_fma_mix_f32 v65, v24, v16, 0 op_sel_hi:[1,0,0]\n"
    "v_fma_mix_f32 v66, v25, v18, 0 op_sel:[1,0,0] op_sel_hi:[1,0,0]\n"
    "v_fma_mix_f32 v67, v25, v18, 0 op_sel_hi:[1,0,0]\n"
    "v_fma_mix_f32 v68, v26, v20, 0 op_sel:[1,0,0] op_sel_hi:[1,0,0]\n"
    "v_fma_mix_f32 v69, v26, v20, 0 op_sel_hi:[1,0,0]\n"
    "v_cvt_pk_bf16_f32 v64, 0, v64\n"
    "v_cvt_pk_bf16_f32 v65, 0, v65\n"
    "v_cvt_pk_bf16_f32 v66, 0, v66\n"
    "v_cvt_pk_bf16_f32 v67, 0, v67\n"
    "v_cvt_pk_bf16_f32 v68, 0, v68\n"
    "v_cvt_pk_bf16_f32 v69, 0, v69\n"
    "v_fma_mix_f32 v70, v27, v16, 0 op_sel:[1,0,0] op_sel_hi:[1,0,0]\n"
    "v_fma_mix_f32 v71, v27, v16, 0 op_sel_hi:[1,0,0]\n"
    "v_fma_mix_f32 v72, v28, v18, 0 op_sel:[1,0,0] op_sel_hi:[1,0,0]\n"
    "v_fma_mix_f32 v73, v28, v18, 0 op_sel_hi:[1,0,0]\n"
    "v_fma_mix_f32 v74, v29, v20, 0 op_sel:[1,0,0] op_sel_hi:[1,0,0]\n"
    "v_fma_mix_f32 v75, v29, v20, 0 op_sel_hi:[1,0,0]\n"
    "v_cvt_pk_bf16_f32 v70, 0, v70\n"
    "v_cvt_pk_bf16_f32 v71, 0, v71\n"
    "v_cvt_pk_bf16_f32 v72, 0, v72\n"
    "v_cvt_pk_bf16_f32 v73, 0, v73\n"
    "v_cvt_pk_bf16_f32 v74, 0, v74\n"
    "v_cvt_pk_bf16_f32 v75, 0, v75\n"
    "v_fma_mix_f32 v76, v30, v16, 0 op_sel:[1,0,0] op_sel_hi:[1,0,0]\n"
    "v_fma_mix_f32 v77, v30, v16, 0 op_sel_hi:[1,0,0]\n"
    "v_fma_mix_f32 v78, v31, v18, 0 op_sel:[1,0,0] op_sel_hi:[1,0,0]\n"
    "v_fma_mix_f32 v79, v31, v18, 0 op_sel_hi:[1,0,0]\n"
    "v_fma_mix_f32 v80, v32, v20, 0 op_sel:[1,0,0] op_sel_hi:[1,0,0]\n"
    "v_fma_mix_f32 v81, v32, v20, 0 op_sel_hi:[1,0,0]\n"
    "v_cvt_pk_bf16_f32 v76, 0, v76\n"
    "v_cvt_pk_bf16_f32 v77, 0, v77\n"
    "v_cvt_pk_bf16_f32 v78, 0, v78\n"
    "v_cvt_pk_bf16_f32 v79, 0, v79\n"
    "v_cvt_pk_bf16_f32 v80, 0, v80\n"
    "v_cvt_pk_bf16_f32 v81, 0, v81\n"
    "v_fma_mix_f32 v82, v33, v16, 0 op_sel:[1,0,0] op_sel_hi:[1,0,0]\n"
    "v_fma_mix_f32 v83, v33, v16, 0 op_sel_hi:[1,0,0]\n"
    "v_fma_mix_f32 v84, v34, v18, 0 op_sel:[1,0,0] op_sel_hi:[1,0,0]\n"
    "v_fma_mix_f32 v85, v34, v18, 0 op_sel_hi:[1,0,0]\n"
    "v_fma_mix_f32 v86, v35, v20, 0 op_sel:[1,0,0] op_sel_hi:[1,0,0]\n"
    "v_fma_mix_f32 v87, v35, v20, 0 op_sel_hi:[1,0,0]\n"
    "v_cvt_pk_bf16_f32 v82, 0, v82\n"
    "v_cvt_pk_bf16_f32 v83, 0, v83\n"
    "v_cvt_pk_bf16_f32 v84, 0, v84\n"
    "v_cvt_pk_bf16_f32 v85, 0, v85\n"
    "v_cvt_pk_bf16_f32 v86, 0, v86\n"
    "v_cvt_pk_bf16_f32 v87, 0, v87\n"
    "s_branch E_26\n"
    "PAD10:\n"
    "s_cbranch_execz S1\n"
    "R_25:\n"
    "s_sleep 1\n"
    "s_sub_u32 s39, s39, 1\n"
    "s_cbranch_scc1 ABORT\n"
    "E_26:\n"
    "ds_read_b64 v[22:23], v13 offset:64\n"
    "s_waitcnt lgkmcnt(0)\n"
    "v_cmp_ne_u32 vcc, 0x010101, v22\n"
    "s_cbranch_vccnz R_25\n"
    "ds_read_b64 v[48:49], v2 offset:0\n"
    "ds_read_b64 v[50:51], v2 offset:10240\n"
    "ds_read_b64 v[52:53], v2 offset:20480\n"
    "s_waitcnt lgkmcnt(0)\n"
    "v_cvt_pk_bf16_f32 v48, 0, v48\n"
    "v_cvt_pk_bf16_f32 v49, 0, v49\n"
    "v_cvt_pk_bf16_f32 v50, 0, v50\n"
    "v_cvt_pk_bf16_f32 v51, 0, v51\n"
    "v_cvt_pk_bf16_f32 v52, 0, v52\n"
    "v_cvt_pk_bf16_f32 v53, 0, v53\n"
    "v_fma_f32 v48, v48, v5, v64\n"
    "v_fma_f32 v49, v49, v5, v65\n"
    "v_fma_f32 v50, v50, v7, v66\n"
    "v_fma_f32 v51, v51, v7, v67\n"
    "v_fma_f32 v52, v52, v9, v68\n"
    "v_fma_f32 v53, v53, v9, v69\n"
    "v_cvt_pk_bf16_f32 v48, 0, v48\n"
    "v_cvt_pk_bf16_f32 v49, 0, v49\n"
    "v_cvt_pk_bf16_f32 v50, 0, v50\n"
    "v_cvt_pk_bf16_f32 v51, 0, v51\n"
    "v_cvt_pk_bf16_f32 v52, 0, v52\n"
    "v_cvt_pk_bf16_f32 v53, 0, v53\n"
    "v_mul_f32 v48, v48, v17\n"
    "v_mul_f32 v49, v49, v17\n"
    "v_mul_f32 v50, v50, v19\n"
    "v_mul_f32 v51, v51, v19\n"
    "v_mul_f32 v52, v52, v21\n"
    "v_mul_f32 v53, v53, v21\n"
    "v_cvt_pk_bf16_f32 v40, v48, v49\n"
    "v_cvt_pk_bf16_f32 v41, v50, v51\n"
    "v_cvt_pk_bf16_f32 v42, v52, v53\n"
    "buffer_store_dwordx4 v[40:43], v1, s[16:19], 0 offen sc1\n"
    "s_mov_b64 exec, 1\n"
    "ds_add_u32 v14, v15 offset:60\n"
    "s_mov_b64 exec, -1\n"
    "s_branch E_28\n"
    "R_27:\n"
    "s_sleep 1\n"
    "s_sub_u32 s39, s39, 1\n"
    "s_cbranch_scc1 ABORT\n"
    "E_28:\n"
    "ds_read_b64 v[22:23], v13 offset:80\n"
    "s_waitcnt lgkmcnt(0)\n"
    "v_cmp_ne_u64 vcc, s[46:47], v[22:23]\n"
    "s_cbranch_vccnz R_27\n"
    "ds_read_b64 v[48:49], v2 offset:2560\n"
    "ds_read_b64 v[54:55], v2 offset:30720\n"
    "ds_read_b64 v[50:51], v2 offset:12800\n"
    "ds_read_b64 v[56:57], v2 offset:38400\n"
    "ds_read_b64 v[52:53], v2 offset:23040\n"
    "ds_read_b64 v[58:59], v2 offset:46080\n"
    "s_waitcnt lgkmcnt(0)\n"
    "v_add_f32 v48, v48, v54\n"
    "v_add_f32 v49, v49, v55\n"
    "v_add_f32 v50, v50, v56\n"
    "v_add_f32 v51, v51, v57\n"
    "v_add_f32 v52, v52, v58\n"
    "v_add_f32 v53, v53, v59\n"
    "v_cvt_pk_bf16_f32 v48, 0, v48\n"
    "v_cvt_pk_bf16_f32 v49, 0, v49\n"
    "v_cvt_pk_bf16_f32 v50, 0, v50\n"
    "v_cvt_pk_bf16_f32 v51, 0, v51\n"
    "v_cvt_pk_bf16_f32 v52, 0, v52\n"
    "v_cvt_pk_bf16_f32 v53, 0, v53\n"
    "v_fma_f32 v48, v48, v5, v70\n"
    "v_fma_f32 v49, v49, v5, v71\n"
    "v_fma_f32 v50, v50, v7, v72\n"
    "v_fma_f32 v51, v51, v7, v73\n"
    "v_fma_f32 v52, v52, v9, v74\n"
    "v_fma_f32 v53, v53, v9, v75\n"
    "v_cvt_pk_bf16_f32 v48, 0, v48\n"
    "v_cvt_pk_bf16_f32 v49, 0, v49\n"
    "v_cvt_pk_bf16_f32 v50, 0, v50\n"
    "v_cvt_pk_bf16_f32 v51, 0, v51\n"
    "v_cvt_pk_bf16_f32 v52, 0, v52\n"
    "v_cvt_pk_bf16_f32 v53, 0, v53\n"
    "v_mul_f32 v48, v48, v17\n"
    "v_mul_f32 v49, v49, v17\n"
    "v_mul_f32 v50, v50, v19\n"
    "v_mul_f32 v51, v51, v19\n"
    "v_mul_f32 v52, v52, v21\n"
    "v_mul_f32 v53, v53, v21\n"
    "v_cvt_pk_bf16_f32 v44, v48, v49\n"
    "v_cvt_pk_bf16_f32 v45, v50, v51\n"
    "v_cvt_pk_bf16_f32 v46, v52, v53\n"
    "buffer_store_dwordx4 v[44:47], v1, s[16:19], s48 offen sc1\n"
    "s_mov_b64 exec, 1\n"
    "ds_add_u32 v14, v15 offset:60\n"
    "s_mov_b64 exec, -1\n"
    "s_branch E_30\n"
    "R_29:\n"
    "s_sleep 1\n"
    "s_sub_u32 s39, s39, 1\n"
    "s_cbranch_scc1 ABORT\n"
    "E_30:\n"
    "ds_read_b64 v[22:23], v13 offset:96\n"
    "s_waitcnt lgkmcnt(0)\n"
    "v_cmp_ne_u64 vcc, s[46:47], v[22:23]\n"
    "s_cbranch_vccnz R_29\n"
    "ds_read_b64 v[48:49], v2 offset:5120\n"
    "ds_read_b64 v[54:55], v2 offset:33280\n"
    "ds_read_b64 v[50:51], v2 offset:15360\n"
    "ds_read_b64 v[56:57], v2 offset:40960\n"
    "ds_read_b64 v[52:53], v2 offset:25600\n"
    "ds_read_b64 v[58:59], v2 offset:48640\n"
    "s_waitcnt lgkmcnt(0)\n"
    "v_add_f32 v48, v48, v54\n"
    "v_add_f32 v49, v49, v55\n"
    "v_add_f32 v50, v50, v56\n"
    "v_add_f32 v51, v51, v57\n"
    "v_add_f32 v52, v52, v58\n"
    "v_add_f32 v53, v53, v59\n"
    "v_cvt_pk_bf16_f32 v48, 0, v48\n"
    "v_cvt_pk_bf16_f32 v49, 0, v49\n"
    "v_cvt_pk_bf16_f32 v50, 0, v50\n"
    "v_cvt_pk_bf16_f32 v51, 0, v51\n"
    "v_cvt_pk_bf16_f32 v52, 0, v52\n"
    "v_cvt_pk_bf16_f32 v53, 0, v53\n"
    "v_fma_f32 v48, v48, v5, v76\n"
    "v_fma_f32 v49, v49, v5, v77\n"
    "v_fma_f32 v50, v50, v7, v78\n"
    "v_fma_f32 v51, v51, v7, v79\n"
    "v_fma_f32 v52, v52, v9, v80\n"
    "v_fma_f32 v53, v53, v9, v81\n"
    "v_cvt_pk_bf16_f32 v48, 0, v48\n"
    "v_cvt_pk_bf16_f32 v49, 0, v49\n"
    "v_cvt_pk_bf16_f32 v50, 0, v50\n"
    "v_cvt_pk_bf16_f32 v51, 0, v51\n"
    "v_cvt_pk_bf16_f32 v52, 0, v52\n"
    "v_cvt_pk_bf16_f32 v53, 0, v53\n"
    "v_mul_f32 v48, v48, v17\n"
    "v_mul_f32 v49, v49, v17\n"
    "v_mul_f32 v50, v50, v19\n"
    "v_mul_f32 v51, v51, v19\n"
    "v_mul_f32 v52, v52, v21\n"
    "v_mul_f32 v53, v53, v21\n"
    "v_cvt_pk_bf16_f32 v40, v48, v49\n"
    "v_cvt_pk_bf16_f32 v41, v50, v51\n"
    "v_cvt_pk_bf16_f32 v42, v52, v53\n"
    "buffer_store_dwordx4 v[40:43], v1, s[16:19], s49 offen sc1\n"
    "s_mov_b64 exec, 1\n"
    "ds_add_u32 v14, v15 offset:60\n"
    "s_mov_b64 exec, -1\n"
    "s_branch E_32\n"
    "PAD11:\n"
    "s_cbranch_execz S1\n"
    "R_31:\n"
    "s_sub_u32 s39, s39, 1\n"
    "s_cbranch_scc1 ABORT\n"
    "E_32:\n"
    "ds_read_b64 v[22:23], v13 offset:112\n"
    "ds_read_b64 v[48:49], v2 offset:7680\n"
    "ds_read_b64 v[54:55], v2 offset:35840\n"
    "ds_read_b64 v[50:51], v2 offset:17920\n"
    "ds_read_b64 v[56:57], v2 offset:43520\n"
    "ds_read_b64 v[52:53], v2 offset:28160\n"
    "ds_read_b64 v[58:59], v2 offset:51200\n"
    "s_waitcnt lgkmcnt(0)\n"
    "v_cmp_ne_u64 vcc, s[46:47], v[22:23]\n"
    "s_cbranch_vccnz R_31\n"
    "v_add_f32 v48, v48, v54\n"
    "v_add_f32 v49, v49, v55\n"
    "v_add_f32 v50, v50, v56\n"
    "v_add_f32 v51, v51, v57\n"
    "v_add_f32 v52, v52, v58\n"
    "v_add_f32 v53, v53, v59\n"
    "v_cvt_pk_bf16_f32 v48, 0, v48\n"
    "v_cvt_pk_bf16_f32 v49, 0, v49\n"
    "v_cvt_pk_bf16_f32 v50, 0, v50\n"
    "v_cvt_pk_bf16_f32 v51, 0, v51\n"
    "v_cvt_pk_bf16_f32 v52, 0, v52\n"
    "v_cvt_pk_bf16_f32 v53, 0, v53\n"
    "v_fma_f32 v48, v48, v5, v82\n"
    "v_fma_f32 v49, v49, v5, v83\n"
    "v_fma_f32 v50, v50, v7, v84\n"
    "v_fma_f32 v51, v51, v7, v85\n"
    "v_fma_f32 v52, v52, v9, v86\n"
    "v_fma_f32 v53, v53, v9, v87\n"
    "v_cvt_pk_bf16_f32 v48, 0, v48\n"
    "v_cvt_pk_bf16_f32 v49, 0, v49\n"
    "v_cvt_pk_bf16_f32 v50, 0, v50\n"
    "v_cvt_pk_bf16_f32 v51, 0, v51\n"
    "v_cvt_pk_bf16_f32 v52, 0, v52\n"
    "v_cvt_pk_bf16_f32 v53, 0, v53\n"
    "v_mul_f32 v48, v48, v17\n"
    "v_mul_f32 v49, v49, v17\n"
    "v_mul_f32 v50, v50, v19\n"
    "v_mul_f32 v51, v51, v19\n"
    "v_mul_f32 v52, v52, v21\n"
    "v_mul_f32 v53, v53, v21\n"
    "v_cvt_pk_bf16_f32 v44, v48, v49\n"
    "v_cvt_pk_bf16_f32 v45, v50, v51\n"
    "v_cvt_pk_bf16_f32 v46, v52, v53\n"
    "buffer_store_dwordx4 v[44:47], v1, s[16:19], s50 offen sc1\n"
    "s_mov_b64 exec, 1\n"
    "ds_add_u32 v14, v15 offset:60\n"
    "s_mov_b64 exec, -1\n"
    "s_branch LN2K\n"
    "MISC:\n"
    "s_waitcnt lgkmcnt(0)\n"
    "s_barrier\n"
    "s_mov_b32 m0, 148480\n"
    "s_cmpk_eq_u32 s37, 2\n"
    "s_cbranch_scc1 MISC1\n"
    "s_lshr_b32 s42, s2, 3\n"
    "s_mul_i32 s43, s42, 128\n"
    "s_mul_i32 s44, s42, 392\n"
    "s_add_u32 s44, s44, 0xffffef80\n"
    "s_cmpk_ge_u32 s2, 128\n"
    "s_cselect_b32 s43, s44, s43\n"
    "s_and_b32 s44, s2, 7\n"
    "s_mul_i32 s44, s44, 663552\n"
    "v_add_u32 v1, s43, v0\n"
    "v_min_u32 v2, 5183, v1\n"
    "v_lshl_add_u32 v2, v2, 7, s44\n"
    "global_load_lds_dword v2, s[32:33]\n"
    "v_add_u32 v1, 128, v1\n"
    "s_cmpk_lt_u32 s2, 128\n"
    "s_cbranch_scc1 MISC2\n"
    "v_min_u32 v2, 5183, v1\n"
    "v_lshl_add_u32 v2, v2, 7, s44\n"
    "global_load_lds_dword v2, s[32:33]\n"
    "v_add_u32 v1, 128, v1\n"
    "v_min_u32 v2, 5183, v1\n"
    "v_lshl_add_u32 v2, v2, 7, s44\n"
    "global_load_lds_dword v2, s[32:33]\n"
    "v_add_u32 v1, 128, v1\n"
    "v_min_u32 v2, 5183, v1\n"
    "v_lshl_add_u32 v2, v2, 7, s44\n"
    "global_load_lds_dword v2, s[32:33]\n"
    "v_add_u32 v1, 128, v1\n"
    "s_branch MISC2\n"
    "MISC1:\n"
    "s_cmpk_ge_u32 s2, 128\n"
    "s_cbranch_scc1 PREF\n"
    "s_and_b32 s57, s2, 7\n"
    "s_lshl_b32 s57, s57, 4\n"
    "s_lshr_b32 s43, s2, 3\n"
    "s_add_u32 s57, s57, s43\n"
    "s_mul_i32 s42, s57, 18432\n"
    "v_and_b32 v1, 63, v0\n"
    "v_lshlrev_b32 v1, 4, v1\n"
    "v_add_u32 v2, s42, v1\n"
    "s_mov_b32 m0, 53760\n"
    "s_mov_b32 exec_hi, 0xffff\n"
    "global_load_lds_dwordx4 v2, s[4:5] offset:0\n"
    "global_load_lds_dwordx4 v2, s[4:5] offset:768\n"
    "global_load_lds_dwordx4 v2, s[4:5] offset:1536\n"
    "s_add_u32 m0, m0, 2320\n"
    "v_add_u32 v2, 2304, v2\n"
    "global_load_lds_dwordx4 v2, s[4:5] offset:0\n"
    "global_load_lds_dwordx4 v2, s[4:5] offset:768\n"
    "global_load_lds_dwordx4 v2, s[4:5] offset:1536\n"
    "s_add_u32 m0, m0, 2320\n"
    "v_add_u32 v2, 2304, v2\n"
    "global_load_lds_dwordx4 v2, s[4:5] offset:0\n"
    "global_load_lds_dwordx4 v2, s[4:5] offset:768\n"
    "global_load_lds_dwordx4 v2, s[4:5] offset:1536\n"
    "s_add_u32 m0, m0, 2320\n"
    "v_add_u32 v2, 2304, v2\n"
    "global_load_lds_dwordx4 v2, s[4:5] offset:0\n"
    "global_load_lds_dwordx4 v2, s[4:5] offset:768\n"
    "global_load_lds_dwordx4 v2, s[4:5] offset:1536\n"
    "s_add_u32 m0, m0, 2320\n"
    "v_add_u32 v2, 2304, v2\n"
    "global_load_lds_dwordx4 v2, s[4:5] offset:0\n"
    "global_load_lds_dwordx4 v2, s[4:5] offset:768\n"
    "global_load_lds_dwordx4 v2, s[4:5] offset:1536\n"
    "s_add_u32 m0, m0, 2320\n"
    "v_add_u32 v2, 2304, v2\n"
    "global_load_lds_dwordx4 v2, s[4:5] offset:0\n"
    "global_load_lds_dwordx4 v2, s[4:5] offset:768\n"
    "global_load_lds_dwordx4 v2, s[4:5] offset:1536\n"
    "s_add_u32 m0, m0, 2320\n"
    "v_add_u32 v2, 2304, v2\n"
    "global_load_lds_dwordx4 v2, s[4:5] offset:0\n"
    "global_load_lds_dwordx4 v2, s[4:5] offset:768\n"
    "global_load_lds_dwordx4 v2, s[4:5] offset:1536\n"
    "s_add_u32 m0, m0, 2320\n"
    "v_add_u32 v2, 2304, v2\n"
    "global_load_lds_dwordx4 v2, s[4:5] offset:0\n"
    "global_load_lds_dwordx4 v2, s[4:5] offset:768\n"
    "global_load_lds_dwordx4 v2, s[4:5] offset:1536\n"
    "s_add_u32 m0, m0, 2320\n"
    "v_add_u32 v2, 2304, v2\n"
    "s_mov_b64 exec, -1\n"
    "s_add_u32 s42, s42, 3301376\n"
    "v_mov_b32 v4, -1\n"
    "v_mov_b32 v5, -1\n"
    "v_mov_b32 v6, -1\n"
    "v_mov_b32 v7, -1\n"
    "v_mov_b32 v8, 0\n"
    "v_mov_b32 v9, 0\n"
    "v_mov_b32 v10, 0\n"
    "v_mov_b32 v11, 0\n"
    "buffer_store_dwordx4 v[4:7], v1, s[16:19], s42 offen offset:0 sc1\n"
    "buffer_store_dwordx4 v[4:7], v1, s[16:19], s42 offen offset:1024 sc1\n"
    "CH13_0:\n"
    "s_cbranch_execz CH13_1\n"
    "buffer_store_dwordx4 v[4:7], v1, s[16:19], s42 offen offset:2048 sc1\n"
    "buffer_store_dwordx4 v[4:7], v1, s[16:19], s42 offen offset:3072 sc1\n"
    "s_add_u32 s42, s42, 4096\n"
    "buffer_store_dwordx4 v[4:7], v1, s[16:19], s42 offen offset:0 sc1\n"
    "buffer_store_dwordx4 v[4:7], v1, s[16:19], s42 offen offset:1024 sc1\n"
    "buffer_store_dwordx4 v[4:7], v1, s[16:19], s42 offen offset:2048 sc1\n"
    "buffer_store_dwordx4 v[4:7], v1, s[16:19], s42 offen offset:3072 sc1\n"
    "s_add_u32 s42, s42, 4096\n"
    "buffer_store_dwordx4 v[4:7], v1, s[16:19], s42 offen offset:0 sc1\n"
    "buffer_store_dwordx4 v[4:7], v1, s[16:19], s42 offen offset:1024 sc1\n"
    "buffer_store_dwordx4 v[4:7], v1, s[16:19], s42 offen offset:2048 sc1\n"
    "buffer_store_dwordx4 v[4:7], v1, s[16:19], s42 offen offset:3072 sc1\n"
    "s_add_u32 s42, s42, 4096\n"
    "buffer_store_dwordx4 v[4:7], v1, s[16:19], s42 offen offset:0 sc1\n"
    "buffer_store_dwordx4 v[4:7], v1, s[16:19], s42 offen offset:1024 sc1\n"
    "buffer_store_dwordx4 v[4:7], v1, s[16:19], s42 offen offset:2048 sc1\n"
    "buffer_store_dwordx4 v[4:7], v1, s[16:19], s42 offen offset:3072 sc1\n"
    "s_add_u32 s42, s42, 4096\n"
    "buffer_store_dwordx4 v[4:7], v1, s[16:19], s42 offen offset:0 sc1\n"
    "buffer_store_dwordx4 v[4:7], v1, s[16:19], s42 offen offset:1024 sc1\n"
    "v_lshrrev_b32 v2, 9, v1\n"
    "v_mul_u32_u24 v2, 1536, v2\n"
    "v_add_u32 v2, v2, v1\n"
    "v_add_u32 v2, 134048, v2\n"
    "ds_write_b128 v2, v[8:11]\n"
    "v_mov_b32 v15, 1\n"
    "s_waitcnt vmcnt(18)\n"
    "s_mov_b64 exec, 1\n"
    "ds_add_u32 v14, v15 offset:56\n"
    "s_mov_b64 exec, -1\n"
    "s_branch LN2\n"
    "MISC2:\n"
    "s_cmpk_ge_u32 s2, 128\n"
    "s_cbranch_scc1 CDONE\n"
    "s_cmpk_ge_u32 s37, 4\n"
    "s_cselect_b32 s42, 128, 0\n"
    "v_subrev_u32 v3, s42, v0\n"
    "v_min_u32 v1, 287, v3\n"
    "v_lshlrev_b32 v29, 2, v1\n"
    "global_load_dword v28, v29, s[26:27]\n"
    "v_add_u32 v29, 72320, v29\n"
    "v_lshlrev_b32 v2, 4, v1\n"
    "v_lshlrev_b32 v1, 5, v1\n"
    "v_add_u32 v1, 138240, v1\n"
    "v_mov_b32 v14, 147968\n"
    "v_mov_b32 v15, 1\n"
    "s_movk_i32 s43, 288\n"
    "v_cmp_gt_u32 s[50:51], s43, v3\n"
    "v_lshrrev_b32 v3, 4, v3\n"
    "v_lshlrev_b32 v3, 3, v3\n"
    "v_add_u32 v3, 148736, v3\n"
    "s_mov_b32 s44, 34304\n"
    "s_mov_b32 s45, 61952\n"
    "s_mov_b32 s46, 38912\n"
    "s_mov_b32 s47, 66560\n"
    "s_mov_b32 s48, 43520\n"
    "s_mov_b32 s49, 71168\n"
    "ds_read_b32 v4, v14 offset:480\n"
    "s_waitcnt lgkmcnt(0)\n"
    "v_readfirstlane_b32 s96, v4\n"
    "U_33:\n"
    "s_memrealtime s[98:99]\n"
    "s_waitcnt lgkmcnt(0)\n"
    "s_sub_u32 s98, s98, s96\n"
    "s_cmpk_lt_u32 s98, 450\n"
    "s_cbranch_scc1 U_33\n"
    "s_branch E_35\n"
    "R_34:\n"
    "s_sleep 4\n"
    "s_sub_u32 s39, s39, 1\n"
    "s_cbranch_scc1 ABORT\n"
    "E_35:\n"
    "buffer_load_dwordx4 v[32:35], v2, s[16:19], s44 offen sc1\n"
    "buffer_load_dwordx4 v[36:39], v2, s[16:19], s45 offen sc1\n"
    "buffer_load_dwordx4 v[40:43], v2, s[16:19], s46 offen sc1\n"
    "buffer_load_dwordx4 v[44:47], v2, s[16:19], s47 offen sc1\n"
    "buffer_load_dwordx4 v[48:51], v2, s[16:19], s48 offen sc1\n"
    "buffer_load_dwordx4 v[52:55], v2, s[16:19], s49 offen sc1\n"
    "s_waitcnt vmcnt(0)\n"
    "v_bitop3_b32 v4, v33, v35, s36 bitop3:0x7e\n"
    "v_bitop3_b32 v5, v37, v39, s36 bitop3:0x7e\n"
    "v_bitop3_b32 v6, v41, v43, s36 bitop3:0x7e\n"
    "v_bitop3_b32 v7, v45, v47, s36 bitop3:0x7e\n"
    "v_bitop3_b32 v8, v49, v51, s36 bitop3:0x7e\n"
    "v_bitop3_b32 v9, v53, v55, s36 bitop3:0x7e\n"
    "v_or3_b32 v4, v4, v5, v6\n"
    "v_or3_b32 v7, v7, v8, v9\n"
    "v_or_b32 v4, v4, v7\n"
    "v_cmp_ne_u32 vcc, 0, v4\n"
    "s_cbranch_vccnz R_34\n"
    "s_setprio 1\n"
    "ds_write_b32 v29, v28\n"
    "v_add_f32 v4, v40, v44\n"
    "v_add_f32 v5, v32, v36\n"
    "v_add_f32 v7, v48, v52\n"
    "v_cvt_pk_bf16_f32 v4, 0, v4\n"
    "v_cvt_pk_bf16_f32 v5, 0, v5\n"
    "v_cvt_pk_bf16_f32 v7, 0, v7\n"
    "v_add_f32 v4, 1.0, v4\n"
    "v_lshlrev_b32 v12, 16, v28\n"
    "v_cvt_pk_bf16_f32 v4, 0, v4\n"
    "v_mul_f32 v6, v4, v12\n"
    "ds_write_b128 v1, v[4:7] offset:0\n"
    "v_add_f32 v8, v42, v46\n"
    "v_add_f32 v9, v34, v38\n"
    "v_add_f32 v11, v50, v54\n"
    "v_cvt_pk_bf16_f32 v8, 0, v8\n"
    "v_cvt_pk_bf16_f32 v9, 0, v9\n"
    "v_cvt_pk_bf16_f32 v11, 0, v11\n"
    "v_add_f32 v8, 1.0, v8\n"
    "v_and_b32 v13, 0xffff0000, v28\n"
    "v_cvt_pk_bf16_f32 v8, 0, v8\n"
    "v_mul_f32 v10, v8, v13\n"
    "ds_write_b128 v1, v[8:11] offset:16\n"
    "s_mov_b64 exec, 1\n"
    "ds_add_u32 v14, v15 offset:56\n"
    "s_mov_b64 exec, -1\n"
    "s_setprio 0\n"
    "v_mul_f32 v12, v12, v5\n"
    "v_mul_f32 v13, v13, v9\n"
    "v_add_f32 v16, v6, v10\n"
    "v_mul_f32 v17, v6, v6\n"
    "v_fmac_f32 v17, v10, v10\n"
    "v_mul_f32 v17, 4.0, v17\n"
    "v_fmac_f32 v17, v12, v12\n"
    "v_fmac_f32 v17, v13, v13\n"
    "v_cndmask_b32 v16, 0, v16, s[50:51]\n"
    "v_cndmask_b32 v17, 0, v17, s[50:51]\n"
    "s_nop 0\n"
    "v_add_f32_dpp v16, v16, v16 row_ror:8 row_mask:0xf bank_mask:0xf\n"
    "v_add_f32_dpp v17, v17, v17 row_ror:8 row_mask:0xf bank_mask:0xf\n"
    "s_nop 0\n"
    "v_add_f32_dpp v16, v16, v16 row_ror:4 row_mask:0xf bank_mask:0xf\n"
    "v_add_f32_dpp v17, v17, v17 row_ror:4 row_mask:0xf bank_mask:0xf\n"
    "s_nop 0\n"
    "v_add_f32_dpp v16, v16, v16 row_ror:2 row_mask:0xf bank_mask:0xf\n"
    "v_add_f32_dpp v17, v17, v17 row_ror:2 row_mask:0xf bank_mask:0xf\n"
    "s_nop 0\n"
    "v_add_f32_dpp v16, v16, v16 row_ror:1 row_mask:0xf bank_mask:0xf\n"
    "v_add_f32_dpp v17, v17, v17 row_ror:1 row_mask:0xf bank_mask:0xf\n"
    "ds_write_b64 v3, v[16:17]\n"
    "LN2:\n"
    "s_cmpk_ge_u32 s2, 128\n"
    "s_cbranch_scc1 CDONE\n"
    "s_and_b32 s57, s2, 7\n"
    "s_lshl_b32 s57, s57, 4\n"
    "s_lshr_b32 s43, s2, 3\n"
    "s_add_u32 s57, s57, s43\n"
    "v_and_b32 v2, 7, v0\n"
    "v_lshrrev_b32 v3, 3, v0\n"
    "v_mul_u32_u24 v6, 12, v3\n"
    "s_movk_i32 s44, 0x900\n"
    "v_mad_u32_u24 v4, v2, s44, v6\n"
    "v_lshlrev_b32 v2, 4, v2\n"
    "v_add_u32 v4, v2, v4\n"
    "v_add_u32 v5, 72320, v6\n"
    "s_mov_b32 s44, 33024\n"
    "v_mad_u32_u24 v1, v3, s44, v2\n"
    "s_lshl_b32 s42, s57, 7\n"
    "s_add_u32 s42, s42, 131072\n"
    "s_add_u32 s43, s42, 16512\n"
    "s_mov_b32 s44, 138240\n"
    "v_lshl_add_u32 v6, v6, 3, s44\n"
    "CH13_1:\n"
    "s_cbranch_execz CH13_2\n"
    "v_mov_b32 v7, 147968\n"
    "v_and_b32 v16, 8, v0\n"
    "v_lshl_add_u32 v16, v16, 8, v2\n"
    "v_bfe_u32 v17, v0, 4, 2\n"
    "v_lshlrev_b32 v18, 2, v17\n"
    "s_lshl_b32 s44, s37, 7\n"
    "s_add_u32 s44, s44, 132512\n"
    "v_add3_u32 v16, v16, v18, s44\n"
    "v_and_b32 v18, 2, v17\n"
    "v_lshl_add_u32 v18, v18, 2, v2\n"
    "s_lshl_b32 s44, s37, 2\n"
    "s_add_u32 s44, s44, 136608\n"
    "v_mad_u32_u24 v18, v18, 6, s44\n"
    "v_and_b32 v17, 63, v0\n"
    "s_mov_b32 s44, 132512\n"
    "v_lshl_add_u32 v17, v17, 4, s44\n"
    "s_lshr_b32 s44, s57, 4\n"
    "s_lshl_b32 s44, s44, 1\n"
    "s_add_u32 s44, s44, 2\n"
    "s_branch E_37\n"
    "R_36:\n"
    "s_sleep 2\n"
    "s_sub_u32 s39, s39, 1\n"
    "s_cbranch_scc1 ABORT\n"
    "E_37:\n"
    "ds_read_b32 v19, v7 offset:56\n"
    "s_waitcnt lgkmcnt(0)\n"
    "v_cmp_ne_u32 vcc, 6, v19\n"
    "s_cbranch_vccnz R_36\n"
    "ds_read_b32 v8, v4 offset:53760\n"
    "ds_read_b32 v9, v4 offset:53764\n"
    "ds_read_b32 v10, v4 offset:53768\n"
    "ds_read_b32 v12, v4 offset:54912\n"
    "ds_read_b32 v13, v4 offset:54916\n"
    "ds_read_b32 v14, v4 offset:54920\n"
    "ds_read_b128 v[40:43], v6 offset:0\n"
    "ds_read_b128 v[44:47], v6 offset:16\n"
    "ds_read_b128 v[48:51], v6 offset:32\n"
    "ds_read_b128 v[52:55], v6 offset:48\n"
    "ds_read_b128 v[56:59], v6 offset:64\n"
    "ds_read_b128 v[60:63], v6 offset:80\n"
    "ds_read2_b32 v[36:37], v5 offset1:1\n"
    "ds_read_b32 v38, v5 offset:8\n"
    "s_waitcnt lgkmcnt(8)\n"
    "v_lshlrev_b32 v24, 16, v8\n"
    "v_lshlrev_b32 v25, 16, v12\n"
    "v_and_b32 v26, 0xffff0000, v8\n"
    "v_and_b32 v27, 0xffff0000, v12\n"
    "v_lshlrev_b32 v28, 16, v9\n"
    "v_lshlrev_b32 v29, 16, v13\n"
    "v_and_b32 v30, 0xffff0000, v9\n"
    "v_and_b32 v31, 0xffff0000, v13\n"
    "v_lshlrev_b32 v32, 16, v10\n"
    "v_lshlrev_b32 v33, 16, v14\n"
    "v_and_b32 v34, 0xffff0000, v10\n"
    "v_and_b32 v35, 0xffff0000, v14\n"
    "s_branch E_39\n"
    "R_38:\n"
    "s_sleep 1\n"
    "s_sub_u32 s39, s39, 1\n"
    "s_cbranch_scc1 ABORT\n"
    "E_39:\n"
    "ds_read_b32 v19, v7 offset:60\n"
    "s_waitcnt lgkmcnt(0)\n"
    "v_cmp_gt_u32 vcc, s44, v19\n"
    "s_cbranch_vccnz R_38\n"
    "s_cmpk_lt_u32 s57, 96\n"
    "s_cbranch_scc1 LN2g\n"
    "s_sleep 8\n"
    "LN2g:\n"
    "buffer_load_dwordx4 v[72:75], v1, s[16:19], s42 offen sc1\n"
    "buffer_load_dwordx4 v[76:79], v1, s[16:19], s43 offen sc1\n"
    "s_branch E_41\n"
    "R_40:\n"
    "s_sub_u32 s39, s39, 1\n"
    "s_cbranch_scc1 ABORT\n"
    "s_wqm_b64 exec, vcc\n"
    "buffer_load_dwordx4 v[72:75], v1, s[16:19], s42 offen sc1\n"
    "buffer_load_dwordx4 v[76:79], v1, s[16:19], s43 offen sc1\n"
    "s_mov_b64 exec, -1\n"
    "E_41:\n"
    "s_waitcnt vmcnt(0)\n"
    "v_bitop3_b32 v19, v75, v79, s36 bitop3:0x7e\n"
    "v_cmp_ne_u32 vcc, 0, v19\n"
    "s_cbranch_vccnz R_40\n"
    "v_lshlrev_b32 v20, 16, v72\n"
    "v_and_b32 v21, 0xffff0000, v72\n"
    "v_pk_add_f32 v[24:25], v[24:25], v[20:21]\n"
    "v_cvt_pk_bf16_f32 v24, 0, v24\n"
    "v_cvt_pk_bf16_f32 v25, 0, v25\n"
    "v_pk_mul_f32 v[10:11], v[24:25], v[24:25]\n"
    "v_pk_mul_f32 v[12:13], v[24:25], v[42:43] op_sel_hi:[1,0]\n"
    "v_lshlrev_b32 v20, 16, v73\n"
    "v_and_b32 v21, 0xffff0000, v73\n"
    "v_pk_add_f32 v[26:27], v[26:27], v[20:21]\n"
    "v_cvt_pk_bf16_f32 v26, 0, v26\n"
    "v_cvt_pk_bf16_f32 v27, 0, v27\n"
    "v_pk_add_f32 v[8:9], v[26:27], v[24:25]\n"
    "v_pk_fma_f32 v[10:11], v[26:27], v[26:27], v[10:11]\n"
    "v_pk_fma_f32 v[12:13], v[26:27], v[46:47], v[12:13] op_sel:[0,0,0] op_sel_hi:[1,0,1]\n"
    "v_lshlrev_b32 v20, 16, v74\n"
    "v_and_b32 v21, 0xffff0000, v74\n"
    "v_pk_add_f32 v[28:29], v[28:29], v[20:21]\n"
    "v_cvt_pk_bf16_f32 v28, 0, v28\n"
    "v_cvt_pk_bf16_f32 v29, 0, v29\n"
    "v_pk_add_f32 v[8:9], v[28:29], v[8:9]\n"
    "v_pk_fma_f32 v[10:11], v[28:29], v[28:29], v[10:11]\n"
    "v_pk_fma_f32 v[12:13], v[28:29], v[50:51], v[12:13] op_sel:[0,0,0] op_sel_hi:[1,0,1]\n"
    "v_lshlrev_b32 v20, 16, v76\n"
    "v_and_b32 v21, 0xffff0000, v76\n"
    "v_pk_add_f32 v[30:31], v[30:31], v[20:21]\n"
    "v_cvt_pk_bf16_f32 v30, 0, v30\n"
    "v_cvt_pk_bf16_f32 v31, 0, v31\n"
    "v_pk_add_f32 v[8:9], v[30:31], v[8:9]\n"
    "v_pk_fma_f32 v[10:11], v[30:31], v[30:31], v[10:11]\n"
    "v_pk_fma_f32 v[12:13], v[30:31], v[54:55], v[12:13] op_sel:[0,0,0] op_sel_hi:[1,0,1]\n"
    "v_lshlrev_b32 v20, 16, v77\n"
    "v_and_b32 v21, 0xffff0000, v77\n"
    "v_pk_add_f32 v[32:33], v[32:33], v[20:21]\n"
    "v_cvt_pk_bf16_f32 v32, 0, v32\n"
    "v_cvt_pk_bf16_f32 v33, 0, v33\n"
    "v_pk_add_f32 v[8:9], v[32:33], v[8:9]\n"
    "v_pk_fma_f32 v[10:11], v[32:33], v[32:33], v[10:11]\n"
    "v_pk_fma_f32 v[12:13], v[32:33], v[58:59], v[12:13] op_sel:[0,0,0] op_sel_hi:[1,0,1]\n"
    "v_lshlrev_b32 v20, 16, v78\n"
    "v_and_b32 v21, 0xffff0000, v78\n"
    "v_pk_add_f32 v[34:35], v[34:35], v[20:21]\n"
    "v_cvt_pk_bf16_f32 v34, 0, v34\n"
    "v_cvt_pk_bf16_f32 v35, 0, v35\n"
    "v_pk_add_f32 v[8:9], v[34:35], v[8:9]\n"
    "v_pk_fma_f32 v[10:11], v[34:35], v[34:35], v[10:11]\n"
    "v_pk_fma_f32 v[12:13], v[34:35], v[62:63], v[12:13] op_sel:[0,0,0] op_sel_hi:[1,0,1]\n"
    "v_permlane32_swap_b32 v8, v9\n"
    "v_permlane32_swap_b32 v10, v11\n"
    "v_permlane32_swap_b32 v12, v13\n"
    "v_add_f32 v8, v8, v9\n"
    "v_add_f32 v10, v10, v11\n"
    "v_add_f32 v12, v12, v13\n"
    "s_nop 0\n"
    "v_permlane16_swap_b32 v8, v10\n"
    "v_add_f32 v8, v8, v10\n"
    "s_nop 1\n"
    "v_add_f32_dpp v9, v8, v8 row_ror:8 row_mask:0xf bank_mask:0x3\n"
    "s_nop 1\n"
    "v_add_f32_dpp v9, v12, v12 row_ror:8 row_mask:0xf bank_mask:0xc\n"
    "ds_write_b32 v16, v9\n"
    "s_waitcnt lgkmcnt(0)\n"
    "s_barrier\n"
    "ds_read_b128 v[8:11], v17\n"
    "ds_read_b128 v[12:15], v17 offset:1024\n"
    "s_waitcnt lgkmcnt(0)\n"
    "v_pk_add_f32 v[8:9], v[8:9], v[12:13]\n"
    "v_pk_add_f32 v[10:11], v[10:11], v[14:15]\n"
    "s_nop 1\n"
    "v_permlane32_swap_b32 v8, v10\n"
    "v_permlane32_swap_b32 v9, v11\n"
    "v_pk_add_f32 v[8:9], v[8:9], v[10:11]\n"
    "s_nop 1\n"
    "v_add_f32_dpp v8, v8, v8 row_ror:8 row_mask:0xf bank_mask:0xf\n"
    "v_add_f32_dpp v9, v9, v9 row_ror:8 row_mask:0xf bank_mask:0xf\n"
    "v_mov_b32 v10, v8\n"
    "v_mov_b32 v11, v9\n"
    "s_nop 0\n"
    "v_permlane16_swap_b32 v8, v10\n"
    "CH13_2:\n"
    "s_cbranch_execz CH13_3\n"
    "v_permlane16_swap_b32 v9, v11\n"
    "v_pk_add_f32 v[8:9], v[8:9], v[10:11]\n"
    "v_mul_f32 v8, 0x3ae38e39, v8\n"
    "v_mul_f32 v9, 0x3ae38e39, v9\n"
    "v_fma_f32 v9, -v8, v8, v9\n"
    "v_max_f32 v9, 0, v9\n"
    "v_cvt_pk_bf16_f32 v9, 0, v9\n"
    "v_add_f32 v9, 0x3727c5ac, v9\n"
    "v_cvt_pk_bf16_f32 v9, 0, v9\n"
    "v_sqrt_f32 v9, v9\n"
    "s_nop 0\n"
    "v_cvt_pk_bf16_f32 v8, v8, v9\n"
    "v_mov_b32 v9, v8\n"
    "s_nop 1\n"
    "v_permlane32_swap_b32 v8, v9\n"
    "v_and_b32 v12, 0xffff0000, v8\n"
    "v_and_b32 v13, 0xffff0000, v9\n"
    "v_lshlrev_b32 v10, 16, v8\n"
    "v_lshlrev_b32 v11, 16, v9\n"
    "v_rcp_f32 v12, v12\n"
    "v_rcp_f32 v13, v13\n"
    "v_pk_add_f32 v[80:81], v[24:25], v[10:11] neg_lo:[0,1] neg_hi:[0,1]\n"
    "v_pk_add_f32 v[82:83], v[26:27], v[10:11] neg_lo:[0,1] neg_hi:[0,1]\n"
    "v_cvt_pk_bf16_f32 v80, 0, v80\n"
    "v_cvt_pk_bf16_f32 v81, 0, v81\n"
    "v_cvt_pk_bf16_f32 v82, 0, v82\n"
    "v_cvt_pk_bf16_f32 v83, 0, v83\n"
    "v_pk_mul_f32 v[80:81], v[80:81], v[12:13]\n"
    "v_pk_mul_f32 v[82:83], v[82:83], v[12:13]\n"
    "v_cvt_pk_bf16_f32 v80, 0, v80\n"
    "v_cvt_pk_bf16_f32 v81, 0, v81\n"
    "v_cvt_pk_bf16_f32 v82, 0, v82\n"
    "v_cvt_pk_bf16_f32 v83, 0, v83\n"
    "v_pk_mul_f32 v[80:81], v[80:81], v[40:41] op_sel_hi:[1,0]\n"
    "v_pk_mul_f32 v[82:83], v[82:83], v[44:45] op_sel_hi:[1,0]\n"
    "v_cvt_pk_bf16_f32 v80, 0, v80\n"
    "v_cvt_pk_bf16_f32 v81, 0, v81\n"
    "v_cvt_pk_bf16_f32 v82, 0, v82\n"
    "v_cvt_pk_bf16_f32 v83, 0, v83\n"
    "v_pk_add_f32 v[80:81], v[40:41], v[80:81] op_sel:[1,0] op_sel_hi:[1,1]\n"
    "v_pk_add_f32 v[82:83], v[44:45], v[82:83] op_sel:[1,0] op_sel_hi:[1,1]\n"
    "v_cvt_pk_bf16_f32 v72, v80, v82\n"
    "v_cvt_pk_bf16_f32 v76, v81, v83\n"
    "ds_write_b32 v4, v72 offset:0\n"
    "ds_write_b32 v4, v76 offset:1152\n"
    "v_dot2_f32_bf16 v14, v72, v36, 0\n"
    "v_dot2_f32_bf16 v15, v76, v36, 0\n"
    "v_pk_add_f32 v[80:81], v[28:29], v[10:11] neg_lo:[0,1] neg_hi:[0,1]\n"
    "v_pk_add_f32 v[82:83], v[30:31], v[10:11] neg_lo:[0,1] neg_hi:[0,1]\n"
    "v_cvt_pk_bf16_f32 v80, 0, v80\n"
    "v_cvt_pk_bf16_f32 v81, 0, v81\n"
    "v_cvt_pk_bf16_f32 v82, 0, v82\n"
    "v_cvt_pk_bf16_f32 v83, 0, v83\n"
    "v_pk_mul_f32 v[80:81], v[80:81], v[12:13]\n"
    "v_pk_mul_f32 v[82:83], v[82:83], v[12:13]\n"
    "v_cvt_pk_bf16_f32 v80, 0, v80\n"
    "v_cvt_pk_bf16_f32 v81, 0, v81\n"
    "v_cvt_pk_bf16_f32 v82, 0, v82\n"
    "v_cvt_pk_bf16_f32 v83, 0, v83\n"
    "v_pk_mul_f32 v[80:81], v[80:81], v[48:49] op_sel_hi:[1,0]\n"
    "v_pk_mul_f32 v[82:83], v[82:83], v[52:53] op_sel_hi:[1,0]\n"
    "v_cvt_pk_bf16_f32 v80, 0, v80\n"
    "v_cvt_pk_bf16_f32 v81, 0, v81\n"
    "v_cvt_pk_bf16_f32 v82, 0, v82\n"
    "v_cvt_pk_bf16_f32 v83, 0, v83\n"
    "v_pk_add_f32 v[80:81], v[48:49], v[80:81] op_sel:[1,0] op_sel_hi:[1,1]\n"
    "v_pk_add_f32 v[82:83], v[52:53], v[82:83] op_sel:[1,0] op_sel_hi:[1,1]\n"
    "v_cvt_pk_bf16_f32 v73, v80, v82\n"
    "v_cvt_pk_bf16_f32 v77, v81, v83\n"
    "ds_write_b32 v4, v73 offset:4\n"
    "ds_write_b32 v4, v77 offset:1156\n"
    "v_dot2_f32_bf16 v14, v73, v37, v14\n"
    "v_dot2_f32_bf16 v15, v77, v37, v15\n"
    "v_pk_add_f32 v[80:81], v[32:33], v[10:11] neg_lo:[0,1] neg_hi:[0,1]\n"
    "v_pk_add_f32 v[82:83], v[34:35], v[10:11] neg_lo:[0,1] neg_hi:[0,1]\n"
    "v_cvt_pk_bf16_f32 v80, 0, v80\n"
    "v_cvt_pk_bf16_f32 v81, 0, v81\n"
    "v_cvt_pk_bf16_f32 v82, 0, v82\n"
    "v_cvt_pk_bf16_f32 v83, 0, v83\n"
    "v_pk_mul_f32 v[80:81], v[80:81], v[12:13]\n"
    "v_pk_mul_f32 v[82:83], v[82:83], v[12:13]\n"
    "v_cvt_pk_bf16_f32 v80, 0, v80\n"
    "v_cvt_pk_bf16_f32 v81, 0, v81\n"
    "v_cvt_pk_bf16_f32 v82, 0, v82\n"
    "v_cvt_pk_bf16_f32 v83, 0, v83\n"
    "v_pk_mul_f32 v[80:81], v[80:81], v[56:57] op_sel_hi:[1,0]\n"
    "v_pk_mul_f32 v[82:83], v[82:83], v[60:61] op_sel_hi:[1,0]\n"
    "v_cvt_pk_bf16_f32 v80, 0, v80\n"
    "v_cvt_pk_bf16_f32 v81, 0, v81\n"
    "v_cvt_pk_bf16_f32 v82, 0, v82\n"
    "v_cvt_pk_bf16_f32 v83, 0, v83\n"
    "v_pk_add_f32 v[80:81], v[56:57], v[80:81] op_sel:[1,0] op_sel_hi:[1,1]\n"
    "v_pk_add_f32 v[82:83], v[60:61], v[82:83] op_sel:[1,0] op_sel_hi:[1,1]\n"
    "v_cvt_pk_bf16_f32 v74, v80, v82\n"
    "v_cvt_pk_bf16_f32 v78, v81, v83\n"
    "ds_write_b32 v4, v74 offset:8\n"
    "ds_write_b32 v4, v78 offset:1160\n"
    "v_dot2_f32_bf16 v14, v74, v38, v14\n"
    "v_dot2_f32_bf16 v15, v78, v38, v15\n"
    "s_nop 2\n"
    "v_permlane32_swap_b32 v14, v15\n"
    "v_add_f32 v14, v14, v15\n"
    "s_nop 1\n"
    "v_add_f32_dpp v14, v14, v14 row_ror:8 row_mask:0xf bank_mask:0xf\n"
    "v_mov_b32 v15, v14\n"
    "s_nop 1\n"
    "v_permlane16_swap_b32 v14, v15\n"
    "v_add_f32 v14, v14, v15\n"
    "ds_write_b32 v18, v14\n"
    "s_waitcnt lgkmcnt(0)\n"
    "s_barrier\n"
    "v_lshlrev_b32 v80, 16, v72\n"
    "v_lshlrev_b32 v81, 16, v76\n"
    "v_and_b32 v82, 0xffff0000, v72\n"
    "v_and_b32 v83, 0xffff0000, v76\n"
    "v_pk_mul_f32 v[80:81], v[42:43], v[80:81] op_sel:[1,0] op_sel_hi:[1,1]\n"
    "v_pk_mul_f32 v[82:83], v[46:47], v[82:83] op_sel:[1,0] op_sel_hi:[1,1]\n"
    "v_cvt_pk_bf16_f32 v80, 0, v80\n"
    "v_cvt_pk_bf16_f32 v81, 0, v81\n"
    "v_cvt_pk_bf16_f32 v82, 0, v82\n"
    "v_cvt_pk_bf16_f32 v83, 0, v83\n"
    "v_pk_add_f32 v[80:81], v[80:81], v[24:25]\n"
    "v_pk_add_f32 v[82:83], v[82:83], v[26:27]\n"
    "v_cvt_pk_bf16_f32 v8, v80, v82\n"
    "v_cvt_pk_bf16_f32 v12, v81, v83\n"
    "ds_write_b32 v4, v8 offset:18560\n"
    "ds_write_b32 v4, v12 offset:19712\n"
    "v_lshlrev_b32 v80, 16, v73\n"
    "v_lshlrev_b32 v81, 16, v77\n"
    "v_and_b32 v82, 0xffff0000, v73\n"
    "v_and_b32 v83, 0xffff0000, v77\n"
    "v_pk_mul_f32 v[80:81], v[50:51], v[80:81] op_sel:[1,0] op_sel_hi:[1,1]\n"
    "v_pk_mul_f32 v[82:83], v[54:55], v[82:83] op_sel:[1,0] op_sel_hi:[1,1]\n"
    "v_cvt_pk_bf16_f32 v80, 0, v80\n"
    "v_cvt_pk_bf16_f32 v81, 0, v81\n"
    "CH13_3:\n"
    "s_cbranch_execz CH13_4\n"
    "v_cvt_pk_bf16_f32 v82, 0, v82\n"
    "v_cvt_pk_bf16_f32 v83, 0, v83\n"
    "v_pk_add_f32 v[80:81], v[80:81], v[28:29]\n"
    "v_pk_add_f32 v[82:83], v[82:83], v[30:31]\n"
    "v_cvt_pk_bf16_f32 v9, v80, v82\n"
    "v_cvt_pk_bf16_f32 v13, v81, v83\n"
    "ds_write_b32 v4, v9 offset:18564\n"
    "ds_write_b32 v4, v13 offset:19716\n"
    "v_lshlrev_b32 v80, 16, v74\n"
    "v_lshlrev_b32 v81, 16, v78\n"
    "v_and_b32 v82, 0xffff0000, v74\n"
    "v_and_b32 v83, 0xffff0000, v78\n"
    "v_pk_mul_f32 v[80:81], v[58:59], v[80:81] op_sel:[1,0] op_sel_hi:[1,1]\n"
    "v_pk_mul_f32 v[82:83], v[62:63], v[82:83] op_sel:[1,0] op_sel_hi:[1,1]\n"
    "v_cvt_pk_bf16_f32 v80, 0, v80\n"
    "v_cvt_pk_bf16_f32 v81, 0, v81\n"
    "v_cvt_pk_bf16_f32 v82, 0, v82\n"
    "v_cvt_pk_bf16_f32 v83, 0, v83\n"
    "v_pk_add_f32 v[80:81], v[80:81], v[32:33]\n"
    "v_pk_add_f32 v[82:83], v[82:83], v[34:35]\n"
    "v_cvt_pk_bf16_f32 v10, v80, v82\n"
    "v_cvt_pk_bf16_f32 v14, v81, v83\n"
    "ds_write_b32 v4, v10 offset:18568\n"
    "ds_write_b32 v4, v14 offset:19720\n"
    "s_waitcnt lgkmcnt(0)\n"
    "s_barrier\n"
    "s_branch END\n"
    "LN2K:\n"
    "s_setprio 0\n"
    "s_cmpk_ge_u32 s2, 128\n"
    "s_cbranch_scc1 CDONE\n"
    "s_and_b32 s57, s2, 7\n"
    "s_lshl_b32 s57, s57, 4\n"
    "s_lshr_b32 s43, s2, 3\n"
    "s_add_u32 s57, s57, s43\n"
    "v_and_b32 v1, 63, v0\n"
    "s_cmpk_lg_u32 s37, 12\n"
    "s_cbranch_scc1 LN2C\n"
    "s_setprio 3\n"
    "v_lshlrev_b32 v17, 4, v1\n"
    "v_add_u32 v17, 132512, v17\n"
    "v_and_b32 v2, 7, v0\n"
    "v_lshrrev_b32 v3, 5, v1\n"
    "v_lshl_add_u32 v2, v2, 1, v3\n"
    "v_lshlrev_b32 v3, 3, v2\n"
    "s_lshl_b32 s42, s57, 4\n"
    "s_sub_u32 s42, 2047, s42\n"
    "v_sub_u32 v2, s42, v2\n"
    "v_and_b32 v4, 15, v0\n"
    "v_sub_u32 v5, s42, v4\n"
    "v_lshlrev_b32 v6, 3, v4\n"
    "v_mul_u32_u24 v4, 48, v4\n"
    "v_add_u32 v4, 136608, v4\n"
    "s_lshl_b32 s42, s57, 7\n"
    "s_add_u32 s43, s42, 4096\n"
    "s_add_u32 s42, s42, 77824\n"
    "s_brev_b32 s46, 1\n"
    "s_movk_i32 s47, 0x7ff\n"
    "v_mov_b32 v21, s36\n"
    "s_mov_b32 s48, 0xff\n"
    "s_mov_b32 s49, 0xff\n"
    "v_min_u32 v7, 20, v1\n"
    "v_lshlrev_b32 v7, 3, v7\n"
    "v_add_u32 v7, 148736, v7\n"
    "v_mov_b32 v32, 0\n"
    "v_mov_b32 v33, 0\n"
    "s_mov_b64 exec, 0x100000\n"
    "ds_write_b64 v7, v[32:33]\n"
    "s_mov_b64 exec, -1\n"
    "s_waitcnt lgkmcnt(0)\n"
    "s_barrier\n"
    "ds_read_b128 v[8:11], v17\n"
    "ds_read_b128 v[12:15], v17 offset:1024\n"
    "ds_read_b128 v[24:27], v17 offset:2048\n"
    "ds_read_b128 v[28:31], v17 offset:3072\n"
    "ds_read_b64 v[32:33], v7\n"
    "s_waitcnt lgkmcnt(3)\n"
    "v_pk_add_f32 v[8:9], v[8:9], v[12:13]\n"
    "v_pk_add_f32 v[10:11], v[10:11], v[14:15]\n"
    "s_nop 1\n"
    "v_permlane32_swap_b32 v8, v10\n"
    "v_permlane32_swap_b32 v9, v11\n"
    "v_pk_add_f32 v[8:9], v[8:9], v[10:11]\n"
    "s_nop 1\n"
    "v_add_f32_dpp v8, v8, v8 row_ror:8 row_mask:0xf bank_mask:0xf\n"
    "v_add_f32_dpp v9, v9, v9 row_ror:8 row_mask:0xf bank_mask:0xf\n"
    "v_mov_b32 v10, v8\n"
    "v_mov_b32 v11, v9\n"
    "s_nop 0\n"
    "v_permlane16_swap_b32 v8, v10\n"
    "v_permlane16_swap_b32 v9, v11\n"
    "v_pk_add_f32 v[8:9], v[8:9], v[10:11]\n"
    "s_waitcnt lgkmcnt(0)\n"
    "v_pk_add_f32 v[24:25], v[24:25], v[28:29]\n"
    "v_pk_add_f32 v[26:27], v[26:27], v[30:31]\n"
    "v_add_f32_dpp v32, v32, v32 row_ror:8 row_mask:0xf bank_mask:0xf\n"
    "v_add_f32 v24, v24, v25\n"
    "v_add_f32 v26, v26, v27\n"
    "v_add_f32_dpp v32, v32, v32 row_ror:4 row_mask:0xf bank_mask:0xf\n"
    "v_mul_f32 v8, 0x3ae38e39, v8\n"
    "v_permlane32_swap_b32 v24, v26\n"
    "v_add_f32_dpp v32, v32, v32 row_ror:2 row_mask:0xf bank_mask:0xf\n"
    "v_add_f32 v24, v24, v26\n"
    "v_mul_f32 v9, 0x3ae38e39, v9\n"
    "v_add_f32_dpp v32, v32, v32 row_ror:1 row_mask:0xf bank_mask:0xf\n"
    "v_add_f32_dpp v24, v24, v24 row_ror:8 row_mask:0xf bank_mask:0xf\n"
    "v_fma_f32 v9, -v8, v8, v9\n"
    "v_add_f32_dpp v32, v32, v32 row_bcast:15 row_mask:0x2 bank_mask:0xf\n"
    "v_mov_b32 v25, v24\n"
    "v_max_f32 v9, 0, v9\n"
    "v_cvt_pk_bf16_f32 v9, 0, v9\n"
    "v_permlane16_swap_b32 v24, v25\n"
    "v_add_f32 v9, 0x3727c5ac, v9\n"
    "v_readlane_b32 s44, v32, 16\n"
    "v_cvt_pk_bf16_f32 v9, 0, v9\n"
    "v_sqrt_f32 v9, v9\n"
    "v_add_f32 v24, v24, v25\n"
    "v_cvt_pk_bf16_f32 v8, v8, v9\n"
    "v_and_b32 v9, 0xffff0000, v8\n"
    "v_lshlrev_b32 v8, 16, v8\n"
    "v_rcp_f32 v9, v9\n"
    "v_fma_f32 v24, -v8, s44, v24\n"
    "v_mul_f32 v24, v24, v9\n"
    "v_and_b32 v9, 0x7ff, v24\n"
    "v_add_u32 v9, 0x7ff, v9\n"
    "v_or_b32 v24, v24, v9\n"
    "v_ashrrev_i32 v9, 31, v24\n"
    "v_bitop3_b32 v24, v9, v24, s46 bitop3:0x36\n"
    "v_bfi_b32 v20, s47, v2, v24\n"
    "s_mov_b64 exec, s[48:49]\n"
    "buffer_store_dwordx2 v[20:21], v3, s[16:19], s42 offen sc1\n"
    "s_cmpk_lg_u32 s57, 0\n"
    "s_cbranch_scc1 LN2K2\n"
    "s_mov_b64 exec, -1\n"
    "v_add_f32_dpp v33, v33, v33 row_ror:8 row_mask:0xf bank_mask:0xf\n"
    "s_nop 1\n"
    "v_add_f32_dpp v33, v33, v33 row_ror:4 row_mask:0xf bank_mask:0xf\n"
    "s_nop 1\n"
    "v_add_f32_dpp v33, v33, v33 row_ror:2 row_mask:0xf bank_mask:0xf\n"
    "s_nop 1\n"
    "v_add_f32_dpp v33, v33, v33 row_ror:1 row_mask:0xf bank_mask:0xf\n"
    "s_nop 1\n"
    "v_add_f32_dpp v33, v33, v33 row_bcast:15 row_mask:0x2 bank_mask:0xf\n"
    "CH13_4:\n"
    "s_cbranch_execz AUX2\n"
    "s_mov_b64 exec, 0x10000\n"
    "v_sqrt_f32 v22, v33\n"
    "v_mov_b32 v23, s36\n"
    "v_mul_f32 v22, 0x3c343958, v22\n"
    "buffer_store_dwordx2 v[22:23], off, s[16:19], 0 offset:2048 sc1\n"
    "LN2K2:\n"
    "s_mov_b64 exec, 0xffff\n"
    "s_waitcnt lgkmcnt(0)\n"
    "s_barrier\n"
    "ds_read_b128 v[8:11], v4\n"
    "ds_read_b128 v[12:15], v4 offset:16\n"
    "ds_read_b128 v[24:27], v4 offset:32\n"
    "s_waitcnt lgkmcnt(1)\n"
    "v_pk_add_f32 v[8:9], v[8:9], v[12:13]\n"
    "v_pk_add_f32 v[10:11], v[10:11], v[14:15]\n"
    "s_waitcnt lgkmcnt(0)\n"
    "v_pk_add_f32 v[8:9], v[8:9], v[24:25]\n"
    "v_pk_add_f32 v[10:11], v[10:11], v[26:27]\n"
    "v_pk_add_f32 v[8:9], v[8:9], v[10:11]\n"
    "v_add_f32 v8, v8, v9\n"
    "v_and_b32 v9, 0x7ff, v8\n"
    "v_add_u32 v9, 0x7ff, v9\n"
    "v_or_b32 v8, v8, v9\n"
    "v_ashrrev_i32 v9, 31, v8\n"
    "v_bitop3_b32 v8, v9, v8, s46 bitop3:0x36\n"
    "v_bfi_b32 v20, s47, v5, v8\n"
    "buffer_store_dwordx2 v[20:21], v6, s[16:19], s43 offen sc1\n"
    "s_mov_b64 exec, -1\n"
    "s_branch END\n"
    "LN2C:\n"
    "s_setprio 2\n"
    "v_subrev_u32 v1, 832, v0\n"
    "v_add_u32 v2, 0, v1\n"
    "v_mul_u32_u24 v3, 7282, v2\n"
    "v_lshrrev_b32 v3, 20, v3\n"
    "v_add_lshl_u32 v8, v2, v3, 4\n"
    "v_add_u32 v2, 192, v1\n"
    "v_mul_u32_u24 v3, 7282, v2\n"
    "v_lshrrev_b32 v3, 20, v3\n"
    "v_add_lshl_u32 v9, v2, v3, 4\n"
    "v_add_u32 v2, 384, v1\n"
    "v_mul_u32_u24 v3, 7282, v2\n"
    "v_lshrrev_b32 v3, 20, v3\n"
    "v_add_lshl_u32 v10, v2, v3, 4\n"
    "v_add_u32 v2, 576, v1\n"
    "v_mul_u32_u24 v3, 7282, v2\n"
    "v_lshrrev_b32 v3, 20, v3\n"
    "v_add_lshl_u32 v11, v2, v3, 4\n"
    "v_add_u32 v2, 768, v1\n"
    "v_mul_u32_u24 v3, 7282, v2\n"
    "v_lshrrev_b32 v3, 20, v3\n"
    "v_add_lshl_u32 v12, v2, v3, 4\n"
    "v_add_u32 v2, 960, v1\n"
    "v_mul_u32_u24 v3, 7282, v2\n"
    "v_lshrrev_b32 v3, 20, v3\n"
    "v_add_lshl_u32 v13, v2, v3, 4\n"
    "s_mul_i32 s42, s57, 18432\n"
    "v_lshl_add_u32 v1, v1, 4, s42\n"
    "v_add_u32 v2, 6144, v1\n"
    "v_add_u32 v3, 12288, v1\n"
    "s_mov_b32 s42, 3301376\n"
    "s_lshl_b32 s43, s57, 2\n"
    "v_mov_b32 v4, s36\n"
    "s_barrier\n"
    "s_barrier\n"
    "ds_read_b128 v[16:19], v8 offset:0\n"
    "ds_read_b128 v[20:23], v9 offset:0\n"
    "ds_read_b128 v[24:27], v10 offset:0\n"
    "ds_read_b128 v[28:31], v11 offset:0\n"
    "ds_read_b128 v[32:35], v12 offset:0\n"
    "ds_read_b128 v[36:39], v13 offset:0\n"
    "s_waitcnt lgkmcnt(5)\n"
    "buffer_store_dwordx4 v[16:19], v1, s[16:19], s42 offen offset:0 sc1\n"
    "s_waitcnt lgkmcnt(4)\n"
    "buffer_store_dwordx4 v[20:23], v1, s[16:19], s42 offen offset:3072 sc1\n"
    "s_waitcnt lgkmcnt(3)\n"
    "buffer_store_dwordx4 v[24:27], v2, s[16:19], s42 offen offset:0 sc1\n"
    "s_waitcnt lgkmcnt(2)\n"
    "buffer_store_dwordx4 v[28:31], v2, s[16:19], s42 offen offset:3072 sc1\n"
    "s_waitcnt lgkmcnt(1)\n"
    "buffer_store_dwordx4 v[32:35], v3, s[16:19], s42 offen offset:0 sc1\n"
    "s_waitcnt lgkmcnt(0)\n"
    "buffer_store_dwordx4 v[36:39], v3, s[16:19], s42 offen offset:3072 sc1\n"
    "s_barrier\n"
    "ds_read_b128 v[40:43], v8 offset:18560\n"
    "ds_read_b128 v[44:47], v9 offset:18560\n"
    "ds_read_b128 v[48:51], v10 offset:18560\n"
    "ds_read_b128 v[52:55], v11 offset:18560\n"
    "ds_read_b128 v[56:59], v12 offset:18560\n"
    "ds_read_b128 v[60:63], v13 offset:18560\n"
    "s_waitcnt lgkmcnt(5)\n"
    "global_store_dwordx4 v1, v[40:43], s[34:35] offset:0 sc1\n"
    "s_waitcnt lgkmcnt(4)\n"
    "global_store_dwordx4 v1, v[44:47], s[34:35] offset:3072 sc1\n"
    "s_waitcnt lgkmcnt(3)\n"
    "global_store_dwordx4 v2, v[48:51], s[34:35] offset:0 sc1\n"
    "s_waitcnt lgkmcnt(2)\n"
    "global_store_dwordx4 v2, v[52:55], s[34:35] offset:3072 sc1\n"
    "s_waitcnt lgkmcnt(1)\n"
    "global_store_dwordx4 v3, v[56:59], s[34:35] offset:0 sc1\n"
    "s_waitcnt lgkmcnt(0)\n"
    "global_store_dwordx4 v3, v[60:63], s[34:35] offset:3072 sc1\n"
    "s_waitcnt vmcnt(0)\n"
    "s_barrier\n"
    "s_cmpk_lg_u32 s37, 15\n"
    "s_cbranch_scc1 END\n"
    "s_mov_b64 exec, 1\n"
    "buffer_store_dword v4, off, s[16:19], s43 offset:1024 sc1\n"
    "s_mov_b64 exec, -1\n"
    "s_branch END\n"
    "PREF:\n"
    "s_sub_u32 s42, s2, 128\n"
    "s_lshr_b32 s42, s42, 3\n"
    "v_and_b32 v1, 63, v0\n"
    "v_mul_u32_u24 v1, 9216, v1\n"
    "s_lshr_b32 s44, s28, 7\n"
    "s_add_u32 s44, s44, s2\n"
    "s_sub_u32 s44, 3, s44\n"
    "s_and_b32 s44, s44, 7\n"
    "s_add_u32 s44, s44, 16\n"
    "s_sub_u32 s44, s44, 8\n"
    "s_mul_i32 s45, s44, 57\n"
    "s_lshr_b32 s45, s45, 9\n"
    "s_mul_i32 s45, s45, 9\n"
    "s_sub_u32 s44, s44, s45\n"
    "s_and_b32 s43, s2, 7\n"
    "s_mul_i32 s43, s43, 9\n"
    "s_add_u32 s43, s43, s44\n"
    "s_mul_i32 s44, s42, 663552\n"
    "s_lshl_b32 s43, s43, 7\n"
    "s_add_u32 s44, s44, s43\n"
    "v_add_u32 v2, s44, v1\n"
    "global_load_lds_dword v2, s[28:29]\n"
    "global_load_lds_dword v2, s[30:31]\n"
    "v_add_u32 v2, 589824, v2\n"
    "s_mov_b64 exec, 0xff\n"
    "global_load_lds_dword v2, s[28:29]\n"
    "global_load_lds_dword v2, s[30:31]\n"
    "s_mov_b64 exec, -1\n"
    "s_lshr_b32 s44, s28, 7\n"
    "s_add_u32 s44, s44, s2\n"
    "s_sub_u32 s44, 3, s44\n"
    "s_and_b32 s44, s44, 7\n"
    "s_add_u32 s44, s44, 16\n"
    "s_sub_u32 s44, s44, 7\n"
    "s_mul_i32 s45, s44, 57\n"
    "s_lshr_b32 s45, s45, 9\n"
    "s_mul_i32 s45, s45, 9\n"
    "s_sub_u32 s44, s44, s45\n"
    "s_and_b32 s43, s2, 7\n"
    "s_mul_i32 s43, s43, 9\n"
    "s_add_u32 s43, s43, s44\n"
    "s_mul_i32 s44, s42, 663552\n"
    "s_lshl_b32 s43, s43, 7\n"
    "s_add_u32 s44, s44, s43\n"
    "v_add_u32 v2, s44, v1\n"
    "global_load_lds_dword v2, s[28:29]\n"
    "global_load_lds_dword v2, s[30:31]\n"
    "v_add_u32 v2, 589824, v2\n"
    "s_mov_b64 exec, 0xff\n"
    "global_load_lds_dword v2, s[28:29]\n"
    "global_load_lds_dword v2, s[30:31]\n"
    "s_mov_b64 exec, -1\n"
    "CDONE:\n"
    "s_cmpk_lt_u32 s2, 184\n"
    "s_cbranch_scc1 END\n"
    "s_waitcnt vmcnt(0) lgkmcnt(0)\n"
    "s_barrier\n"
    "s_branch ES1b\n"
    "PAD18:\n"
    "s_cbranch_execz M1\n"
    "SIL1:\n"
    "s_mov_b64 exec, -1\n"
    "s_barrier\n"
    "v_add_u32 v1, 148736, v1\n"
    "v_mov_b32 v14, 147968\n"
    "v_mov_b32 v15, 1\n"
    "s_waitcnt vmcnt(0)\n"
    "v_lshlrev_b32 v5, 16, v4\n"
    "v_and_b32 v4, 0xffff0000, v4\n"
    "v_mul_f32 v6, 0xbfb8aa3b, v5\n"
    "v_mul_f32 v7, 0xbfb8aa3b, v4\n"
    "v_exp_f32 v6, v6\n"
    "v_exp_f32 v7, v7\n"
    "v_add_f32 v6, 1.0, v6\n"
    "v_add_f32 v7, 1.0, v7\n"
    "v_rcp_f32 v6, v6\n"
    "v_rcp_f32 v7, v7\n"
    "v_mul_f32 v5, v5, v6\n"
    "v_mul_f32 v4, v4, v7\n"
    "v_cvt_pk_bf16_f32 v4, v5, v4\n"
    "ds_write_b32 v1, v4\n"
    "s_mov_b64 exec, 1\n"
    "ds_add_u32 v14, v15 offset:4\n"
    "s_mov_b64 exec, -1\n"
    "s_branch M9\n"
    "M1:\n"
    "s_mov_b64 exec, -1\n"
    "s_waitcnt lgkmcnt(0)\n"
    "s_load_dword s36, s[16:17], 0\n"
    "s_barrier\n"
    "s_cmpk_ge_u32 s2, 246\n"
    "s_cbranch_scc1 M9\n"
    "s_sub_u32 s42, s2, 192\n"
    "s_lshr_b32 s42, s42, 3\n"
    "s_min_u32 s42, s42, 8\n"
    "s_lshr_b32 s44, s28, 7\n"
    "s_add_u32 s44, s44, s2\n"
    "s_sub_u32 s44, 3, s44\n"
    "s_and_b32 s44, s44, 7\n"
    "s_add_u32 s44, s44, 16\n"
    "s_sub_u32 s44, s44, s42\n"
    "s_mul_i32 s45, s44, 57\n"
    "s_lshr_b32 s45, s45, 9\n"
    "s_mul_i32 s45, s45, 9\n"
    "s_sub_u32 s44, s44, s45\n"
    "s_and_b32 s58, s2, 7\n"
    "s_mul_i32 s58, s58, 9\n"
    "s_add_u32 s58, s58, s44\n"
    "v_lshrrev_b32 v12, 3, v0\n"
    "v_mul_u32_u24 v12, 9216, v12\n"
    "v_lshrrev_b32 v13, 4, v0\n"
    "v_and_b32 v13, 5, v13\n"
    "v_xor_b32 v13, v13, v0\n"
    "v_and_b32 v13, 7, v13\n"
    "v_lshl_add_u32 v12, v13, 4, v12\n"
    "s_lshl_b32 s42, s58, 7\n"
    "v_add_u32 v12, s42, v12\n"
    "v_xor_b32 v13, 32, v12\n"
    "s_sub_u32 s42, s2, 192\n"
    "s_lshr_b32 s43, s42, 1\n"
    "s_add_u32 s43, s43, 27\n"
    "s_and_b32 s44, s2, 1\n"
    "s_mul_i32 s45, s44, 1990656\n"
    "s_lshl_b32 s46, s43, 7\n"
    "s_add_u32 s45, s45, s46\n"
    "v_lshrrev_b32 v1, 3, v0\n"
    "v_mul_u32_u24 v1, 0x1b00, v1\n"
    "v_lshrrev_b32 v2, 3, v0\n"
    "v_lshrrev_b32 v3, 4, v0\n"
    "v_and_b32 v2, 2, v2\n"
    "v_and_b32 v3, 4, v3\n"
    "v_xor_b32 v2, v2, v0\n"
    "v_xor_b32 v2, v2, v3\n"
    "v_and_b32 v2, 7, v2\n"
    "v_lshl_add_u32 v1, v2, 4, v1\n"
    "v_add_u32 v1, s45, v1\n"
    "s_lshl_b32 s45, s37, 10\n"
    "s_add_u32 m0, s45, 98304\n"
    "s_setprio 3\n"
    "s_barrier\n"
    "global_load_lds_dwordx4 v1, s[14:15] nt\n"
    "s_add_u32 m0, m0, 4096\n"
    "v_add_u32 v1, 221184, v1\n"
    "global_load_lds_dwordx4 v1, s[14:15] nt\n"
    "s_add_u32 m0, m0, 4096\n"
    "v_add_u32 v1, 221184, v1\n"
    "global_load_lds_dwordx4 v1, s[14:15] nt\n"
    "s_add_u32 m0, m0, 4096\n"
    "v_add_u32 v1, 221184, v1\n"
    "global_load_lds_dwordx4 v1, s[14:15] nt\n"
    "s_add_u32 m0, m0, 4096\n"
    "v_add_u32 v1, 221184, v1\n"
    "global_load_lds_dwordx4 v1, s[14:15] nt\n"
    "s_add_u32 m0, m0, 4096\n"
    "v_add_u32 v1, 221184, v1\n"
    "global_load_lds_dwordx4 v1, s[14:15] nt\n"
    "s_add_u32 m0, m0, 4096\n"
    "v_add_u32 v1, 221184, v1\n"
    "global_load_lds_dwordx4 v1, s[14:15] nt\n"
    "s_add_u32 m0, m0, 4096\n"
    "v_add_u32 v1, 221184, v1\n"
    "global_load_lds_dwordx4 v1, s[14:15] nt\n"
    "s_add_u32 m0, m0, 4096\n"
    "v_add_u32 v1, 221184, v1\n"
    "global_load_lds_dwordx4 v1, s[14:15] nt\n"
    "s_add_u32 m0, m0, 4096\n"
    "v_add_u32 v1, 221184, v1\n"
    "v_bfe_u32 v5, v0, 4, 2\n"
    "v_bfe_u32 v6, v0, 2, 2\n"
    "v_bfe_u32 v7, v0, 1, 1\n"
    "s_lshl_b32 s45, s37, 1\n"
    "v_or_b32 v7, s45, v7\n"
    "v_and_b32 v8, 2, v6\n"
    "v_xor_b32 v7, v7, v8\n"
    "v_lshlrev_b32 v8, 2, v5\n"
    "v_and_b32 v8, 4, v8\n"
    "v_xor_b32 v7, v7, v8\n"
    "v_lshlrev_b32 v2, 10, v5\n"
    "v_lshl_add_u32 v2, v6, 7, v2\n"
    "v_lshl_add_u32 v2, v7, 4, v2\n"
    "v_and_b32 v8, 1, v0\n"
    "v_lshl_add_u32 v2, v8, 3, v2\n"
    "v_add_u32 v2, 98304, v2\n"
    "s_mul_i32 s45, s44, 576\n"
    "s_add_u32 s45, s45, 148736\n"
    "v_lshl_add_u32 v3, v5, 4, s45\n"
    "s_mul_i32 s45, s44, 27648\n"
    "s_lshl_b32 s46, s43, 9\n"
    "s_add_u32 s45, s45, s46\n"
    "s_lshl_b32 s46, s37, 7\n"
    "s_add_u32 s45, s45, s46\n"
    "s_add_u32 s45, s45, 20480\n"
    "v_and_b32 v4, 15, v0\n"
    "v_lshlrev_b32 v4, 3, v4\n"
    "v_mov_b32 v14, 147968\n"
    "v_mov_b32 v15, 1\n"
    "s_waitcnt lgkmcnt(0)\n"
    "s_add_u32 s36, s36, 1\n"
    "s_waitcnt vmcnt(0)\n"
    "s_mov_b64 exec, 1\n"
    "ds_add_u32 v14, v15 offset:4\n"
    "s_mov_b64 exec, -1\n"
    "s_branch E_43\n"
    "PAD19:\n"
    "s_cbranch_execz M1\n"
    "R_42:\n"
    "s_sub_u32 s39, s39, 1\n"
    "s_cbranch_scc1 ABORT\n"
    "E_43:\n"
    "ds_read_b32 v5, v14 offset:4\n"
    "s_waitcnt lgkmcnt(0)\n"
    "v_cmp_ne_u32 vcc, 9, v5\n"
    "s_cbranch_vccnz R_42\n"
    "ds_read_b128 v[16:19], v3 offset:0\n"
    "ds_read_b64_tr_b16 v[52:53], v2 offset:0\n"
    "ds_read_b64_tr_b16 v[54:55], v2 offset:512\n"
    "ds_read_b128 v[20:23], v3 offset:64\n"
    "ds_read_b64_tr_b16 v[56:57], v2 offset:4096\n"
    "ds_read_b64_tr_b16 v[58:59], v2 offset:4608\n"
    "ds_read_b128 v[24:27], v3 offset:128\n"
    "ds_read_b64_tr_b16 v[60:61], v2 offset:8192\n"
    "ds_read_b64_tr_b16 v[62:63], v2 offset:8704\n"
    "ds_read_b128 v[28:31], v3 offset:192\n"
    "ds_read_b64_tr_b16 v[64:65], v2 offset:12288\n"
    "ds_read_b64_tr_b16 v[66:67], v2 offset:12800\n"
    "ds_read_b128 v[32:35], v3 offset:256\n"
    "ds_read_b64_tr_b16 v[68:69], v2 offset:16384\n"
    "ds_read_b64_tr_b16 v[70:71], v2 offset:16896\n"
    "s_waitcnt lgkmcnt(12)\n"
    "v_mfma_f32_16x16x32_bf16 v[8:11], v[16:19], v[52:55], 0\n"
    "ds_read_b128 v[36:39], v3 offset:320\n"
    "ds_read_b64_tr_b16 v[72:73], v2 offset:20480\n"
    "ds_read_b64_tr_b16 v[74:75], v2 offset:20992\n"
    "s_waitcnt lgkmcnt(12)\n"
    "v_mfma_f32_16x16x32_bf16 v[8:11], v[20:23], v[56:59], v[8:11]\n"
    "ds_read_b128 v[40:43], v3 offset:384\n"
    "ds_read_b64_tr_b16 v[76:77], v2 offset:24576\n"
    "ds_read_b64_tr_b16 v[78:79], v2 offset:25088\n"
    "s_waitcnt lgkmcnt(12)\n"
    "v_mfma_f32_16x16x32_bf16 v[8:11], v[24:27], v[60:63], v[8:11]\n"
    "ds_read_b128 v[44:47], v3 offset:448\n"
    "ds_read_b64_tr_b16 v[80:81], v2 offset:28672\n"
    "ds_read_b64_tr_b16 v[82:83], v2 offset:29184\n"
    "s_waitcnt lgkmcnt(12)\n"
    "v_mfma_f32_16x16x32_bf16 v[8:11], v[28:31], v[64:67], v[8:11]\n"
    "ds_read_b128 v[48:51], v3 offset:512\n"
    "ds_read_b64_tr_b16 v[84:85], v2 offset:32768\n"
    "ds_read_b64_tr_b16 v[86:87], v2 offset:33280\n"
    "s_waitcnt lgkmcnt(12)\n"
    "v_mfma_f32_16x16x32_bf16 v[8:11], v[32:35], v[68:71], v[8:11]\n"
    "s_waitcnt lgkmcnt(9)\n"
    "v_mfma_f32_16x16x32_bf16 v[8:11], v[36:39], v[72:75], v[8:11]\n"
    "s_waitcnt lgkmcnt(6)\n"
    "v_mfma_f32_16x16x32_bf16 v[8:11], v[40:43], v[76:79], v[8:11]\n"
    "s_waitcnt lgkmcnt(3)\n"
    "v_mfma_f32_16x16x32_bf16 v[8:11], v[44:47], v[80:83], v[8:11]\n"
    "s_waitcnt lgkmcnt(0)\n"
    "v_mfma_f32_16x16x32_bf16 v[8:11], v[48:51], v[84:87], v[8:11]\n"
    "s_mov_b64 exec, 0xffff\n"
    "s_nop 6\n"
    "v_mov_b32 v9, s36\n"
    "buffer_store_dwordx2 v[8:9], v4, s[16:19], s45 offen sc1\n"
    "s_mov_b64 exec, -1\n"
    "s_setprio 0\n"
    "s_branch ES1c\n"
    "M9:\n"
    "s_waitcnt lgkmcnt(0)\n"
    "s_add_u32 s36, s36, 1\n"
    "ES1:\n"
    "s_barrier\n"
    "ES1b:\n"
    "s_sub_u32 s42, s2, 192\n"
    "s_lshr_b32 s42, s42, 3\n"
    "s_min_u32 s42, s42, 8\n"
    "s_lshr_b32 s44, s28, 7\n"
    "s_add_u32 s44, s44, s2\n"
    "s_sub_u32 s44, 3, s44\n"
    "s_and_b32 s44, s44, 7\n"
    "s_add_u32 s44, s44, 16\n"
    "s_sub_u32 s44, s44, s42\n"
    "s_mul_i32 s45, s44, 57\n"
    "s_lshr_b32 s45, s45, 9\n"
    "s_mul_i32 s45, s45, 9\n"
    "s_sub_u32 s44, s44, s45\n"
    "s_and_b32 s58, s2, 7\n"
    "s_mul_i32 s58, s58, 9\n"
    "s_add_u32 s58, s58, s44\n"
    "v_lshrrev_b32 v12, 3, v0\n"
    "v_mul_u32_u24 v12, 9216, v12\n"
    "v_lshrrev_b32 v13, 4, v0\n"
    "v_and_b32 v13, 5, v13\n"
    "v_xor_b32 v13, v13, v0\n"
    "v_and_b32 v13, 7, v13\n"
    "v_lshl_add_u32 v12, v13, 4, v12\n"
    "s_lshl_b32 s42, s58, 7\n"
    "v_add_u32 v12, s42, v12\n"
    "v_xor_b32 v13, 32, v12\n"
    "s_cmpk_ge_u32 s37, 8\n"
    "s_cbranch_scc1 ES2\n"
    "s_sleep 3\n"
    "ES1c:\n"
    "s_lshl_b32 m0, s37, 11\n"
    "s_nop 0\n"
    "global_load_lds_dwordx4 v12, s[28:29] nt\n"
    "s_add_u32 m0, m0, 1024\n"
    "v_add_u32 v12, 589824, v12\n"
    "global_load_lds_dwordx4 v13, s[30:31] nt\n"
    "s_add_u32 m0, m0, 15360\n"
    "v_add_u32 v13, 589824, v13\n"
    "global_load_lds_dwordx4 v12, s[28:29] nt\n"
    "s_add_u32 m0, m0, 1024\n"
    "v_add_u32 v12, 589824, v12\n"
    "global_load_lds_dwordx4 v13, s[30:31] nt\n"
    "s_add_u32 m0, m0, 15360\n"
    "v_add_u32 v13, 589824, v13\n"
    "global_load_lds_dwordx4 v12, s[28:29] nt\n"
    "s_add_u32 m0, m0, 1024\n"
    "v_add_u32 v12, 589824, v12\n"
    "global_load_lds_dwordx4 v13, s[30:31] nt\n"
    "s_add_u32 m0, m0, 15360\n"
    "v_add_u32 v13, 589824, v13\n"
    "global_load_lds_dwordx4 v12, s[28:29] nt\n"
    "s_add_u32 m0, m0, 1024\n"
    "v_add_u32 v12, 589824, v12\n"
    "global_load_lds_dwordx4 v13, s[30:31] nt\n"
    "s_add_u32 m0, m0, 15360\n"
    "v_add_u32 v13, 589824, v13\n"
    "global_load_lds_dwordx4 v12, s[28:29] nt\n"
    "s_add_u32 m0, m0, 1024\n"
    "v_add_u32 v12, 589824, v12\n"
    "global_load_lds_dwordx4 v13, s[30:31] nt\n"
    "s_add_u32 m0, m0, 15360\n"
    "v_add_u32 v13, 589824, v13\n"
    "global_load_lds_dwordx4 v12, s[28:29] nt\n"
    "s_add_u32 m0, m0, 1024\n"
    "v_add_u32 v12, 589824, v12\n"
    "global_load_lds_dwordx4 v13, s[30:31] nt\n"
    "s_add_u32 m0, m0, 15360\n"
    "v_add_u32 v13, 589824, v13\n"
    "ES2:\n"
    "s_barrier\n"
    "v_mov_b32 v14, 147968\n"
    "v_mov_b32 v15, 1\n"
    "v_and_b32 v1, 63, v0\n"
    "v_and_b32 v2, 15, v0\n"
    "v_bfe_u32 v3, v0, 4, 2\n"
    "s_cmpk_ge_u32 s37, 8\n"
    "s_cbranch_scc1 MOED\n"
    "global_load_lds_dwordx4 v12, s[28:29] nt\n"
    "s_add_u32 m0, m0, 1024\n"
    "v_add_u32 v12, 589824, v12\n"
    "global_load_lds_dwordx4 v13, s[30:31] nt\n"
    "s_add_u32 m0, m0, 15360\n"
    "v_add_u32 v13, 589824, v13\n"
    "global_load_lds_dwordx4 v12, s[28:29] nt\n"
    "s_add_u32 m0, m0, 1024\n"
    "v_add_u32 v12, 589824, v12\n"
    "global_load_lds_dwordx4 v13, s[30:31] nt\n"
    "s_add_u32 m0, m0, 15360\n"
    "v_add_u32 v13, 589824, v13\n"
    "global_load_lds_dwordx4 v12, s[28:29] nt\n"
    "s_add_u32 m0, m0, 1024\n"
    "PAD20:\n"
    "s_cbranch_execz SIL1\n"
    "v_add_u32 v12, 589824, v12\n"
    "global_load_lds_dwordx4 v13, s[30:31] nt\n"
    "s_add_u32 m0, m0, 15360\n"
    "v_add_u32 v13, 589824, v13\n"
    "v_bfe_u32 v6, v0, 1, 1\n"
    "v_bfe_u32 v7, v0, 2, 2\n"
    "v_lshrrev_b32 v8, 1, v7\n"
    "v_lshl_add_u32 v8, v6, 1, v8\n"
    "v_and_b32 v9, 1, v3\n"
    "v_lshl_add_u32 v8, v9, 2, v8\n"
    "v_xor_b32 v8, s37, v8\n"
    "v_lshlrev_b32 v4, 11, v3\n"
    "v_lshl_add_u32 v4, v6, 10, v4\n"
    "v_lshl_add_u32 v4, v7, 7, v4\n"
    "v_lshl_add_u32 v4, v8, 4, v4\n"
    "v_and_b32 v9, 1, v0\n"
    "v_lshl_add_u32 v4, v9, 3, v4\n"
    "v_add_u32 v5, 0x10000, v4\n"
    "v_add_u32 v6, 0x20000, v4\n"
    "s_waitcnt vmcnt(4)\n"
    "s_mov_b64 exec, 1\n"
    "ds_add_u32 v14, v15 offset:12\n"
    "s_mov_b64 exec, -1\n"
    "s_waitcnt vmcnt(0)\n"
    "s_barrier\n"
    "ds_read_b64_tr_b16 v[56:57], v4 offset:0\n"
    "ds_read_b64_tr_b16 v[58:59], v4 offset:512\n"
    "ds_read_b64_tr_b16 v[60:61], v4 offset:8192\n"
    "ds_read_b64_tr_b16 v[62:63], v4 offset:8704\n"
    "ds_read_b64_tr_b16 v[64:65], v4 offset:16384\n"
    "ds_read_b64_tr_b16 v[66:67], v4 offset:16896\n"
    "ds_read_b64_tr_b16 v[68:69], v4 offset:24576\n"
    "ds_read_b64_tr_b16 v[70:71], v4 offset:25088\n"
    "ds_read_b64_tr_b16 v[72:73], v4 offset:32768\n"
    "ds_read_b64_tr_b16 v[74:75], v4 offset:33280\n"
    "ds_read_b64_tr_b16 v[76:77], v4 offset:40960\n"
    "ds_read_b64_tr_b16 v[78:79], v4 offset:41472\n"
    "ds_read_b64_tr_b16 v[80:81], v4 offset:49152\n"
    "ds_read_b64_tr_b16 v[82:83], v4 offset:49664\n"
    "ds_read_b64_tr_b16 v[84:85], v4 offset:57344\n"
    "ds_read_b64_tr_b16 v[86:87], v4 offset:57856\n"
    "ds_read_b64_tr_b16 v[88:89], v5 offset:0\n"
    "ds_read_b64_tr_b16 v[90:91], v5 offset:512\n"
    "ds_read_b64_tr_b16 v[92:93], v5 offset:8192\n"
    "ds_read_b64_tr_b16 v[94:95], v5 offset:8704\n"
    "ds_read_b64_tr_b16 v[96:97], v5 offset:16384\n"
    "ds_read_b64_tr_b16 v[98:99], v5 offset:16896\n"
    "ds_read_b64_tr_b16 v[100:101], v5 offset:24576\n"
    "ds_read_b64_tr_b16 v[102:103], v5 offset:25088\n"
    "ds_read_b64_tr_b16 v[104:105], v5 offset:32768\n"
    "ds_read_b64_tr_b16 v[106:107], v5 offset:33280\n"
    "ds_read_b64_tr_b16 v[108:109], v5 offset:40960\n"
    "ds_read_b64_tr_b16 v[110:111], v5 offset:41472\n"
    "ds_read_b64_tr_b16 v[112:113], v5 offset:49152\n"
    "ds_read_b64_tr_b16 v[114:115], v5 offset:49664\n"
    "ds_read_b64_tr_b16 v[116:117], v5 offset:57344\n"
    "ds_read_b64_tr_b16 v[118:119], v5 offset:57856\n"
    "ds_read_b64_tr_b16 v[120:121], v6 offset:0\n"
    "ds_read_b64_tr_b16 v[122:123], v6 offset:512\n"
    "ds_read_b64_tr_b16 v[124:125], v6 offset:8192\n"
    "ds_read_b64_tr_b16 v[126:127], v6 offset:8704\n"
    "v_and_b32 v7, 7, v0\n"
    "v_lshlrev_b32 v8, 3, v7\n"
    "v_and_b32 v9, 8, v0\n"
    "s_waitcnt lgkmcnt(0)\n"
    "v_add_u32 v4, 151168, v8\n"
    "v_lshlrev_b32 v5, 3, v9\n"
    "v_lshl_add_u32 v5, v3, 4, v5\n"
    "v_and_b32 v6, 1, v3\n"
    "v_lshlrev_b32 v6, 2, v6\n"
    "v_and_b32 v8, 2, v3\n"
    "v_lshrrev_b32 v9, 3, v9\n"
    "v_add3_u32 v6, v6, v8, v9\n"
    "v_mul_u32_u24 v6, 144, v6\n"
    "v_lshl_add_u32 v6, v7, 1, v6\n"
    "s_lshl_b32 s42, s37, 4\n"
    "s_add_u32 s42, s42, 150016\n"
    "v_add_u32 v6, s42, v6\n"
    "s_branch MOE2\n"
    "MOED:\n"
    "s_cmpk_ge_u32 s37, 14\n"
    "s_cbranch_scc1 MOEX\n"
    "s_mul_i32 s42, s58, 73728\n"
    "s_mul_i32 s43, s37, 192\n"
    "s_add_u32 s42, s42, s43\n"
    "s_add_u32 s42, s42, 1920\n"
    "v_mul_u32_u24 v4, 18432, v3\n"
    "v_mad_u32_u24 v4, v2, 12, v4\n"
    "v_add_u32 v4, s42, v4\n"
    "v_add_u32 v5, 8064, v4\n"
    "v_add_u32 v6, 16128, v4\n"
    "s_cmpk_lt_u32 s2, 192\n"
    "s_cbranch_scc1 MOED2\n"
    "s_branch E_45\n"
    "R_44:\n"
    "s_sleep 4\n"
    "s_sub_u32 s39, s39, 1\n"
    "s_cbranch_scc1 ABORT\n"
    "E_45:\n"
    "ds_read_b32 v7, v14 offset:12\n"
    "s_waitcnt lgkmcnt(0)\n"
    "v_cmp_ne_u32 vcc, 8, v7\n"
    "s_cbranch_vccnz R_44\n"
    "MOED2:\n"
    "global_load_dwordx3 v[16:18], v4, s[32:33] offset:-3456\n"
    "global_load_dwordx3 v[20:22], v4, s[32:33] offset:-2304\n"
    "global_load_dwordx3 v[24:26], v4, s[32:33] offset:-1152\n"
    "global_load_dwordx3 v[28:30], v4, s[32:33] offset:0\n"
    "global_load_dwordx3 v[32:34], v4, s[32:33] offset:1152\n"
    "global_load_dwordx3 v[36:38], v4, s[32:33] offset:2304\n"
    "global_load_dwordx3 v[40:42], v4, s[32:33] offset:3456\n"
    "global_load_dwordx3 v[44:46], v5, s[32:33] offset:-3456\n"
    "global_load_dwordx3 v[48:50], v5, s[32:33] offset:-2304\n"
    "global_load_dwordx3 v[52:54], v5, s[32:33] offset:-1152\n"
    "global_load_dwordx3 v[104:106], v5, s[32:33] offset:0\n"
    "global_load_dwordx3 v[108:110], v5, s[32:33] offset:1152\n"
    "global_load_dwordx3 v[112:114], v5, s[32:33] offset:2304\n"
    "global_load_dwordx3 v[116:118], v5, s[32:33] offset:3456\n"
    "global_load_dwordx3 v[120:122], v6, s[32:33] offset:-3456\n"
    "global_load_dwordx3 v[124:126], v6, s[32:33] offset:-2304\n"
    "s_barrier\n"
    "s_mov_b32 s42, 0x05040100\n"
    "s_mov_b32 s43, 0x07060302\n"
    "s_waitcnt vmcnt(14)\n"
    "v_perm_b32 v56, v20, v16, s42\n"
    "v_perm_b32 v60, v20, v16, s43\n"
    "v_perm_b32 v64, v21, v17, s42\n"
    "v_perm_b32 v68, v21, v17, s43\n"
    "v_perm_b32 v72, v22, v18, s42\n"
    "v_perm_b32 v76, v22, v18, s43\n"
    "s_waitcnt vmcnt(12)\n"
    "v_perm_b32 v57, v28, v24, s42\n"
    "v_perm_b32 v61, v28, v24, s43\n"
    "v_perm_b32 v65, v29, v25, s42\n"
    "v_perm_b32 v69, v29, v25, s43\n"
    "v_perm_b32 v73, v30, v26, s42\n"
    "v_perm_b32 v77, v30, v26, s43\n"
    "s_waitcnt vmcnt(10)\n"
    "v_perm_b32 v58, v36, v32, s42\n"
    "v_perm_b32 v62, v36, v32, s43\n"
    "v_perm_b32 v66, v37, v33, s42\n"
    "v_perm_b32 v70, v37, v33, s43\n"
    "v_perm_b32 v74, v38, v34, s42\n"
    "v_perm_b32 v78, v38, v34, s43\n"
    "s_waitcnt vmcnt(8)\n"
    "v_perm_b32 v59, v44, v40, s42\n"
    "PAD21:\n"
    "s_cbranch_execz SIL1\n"
    "v_perm_b32 v63, v44, v40, s43\n"
    "v_perm_b32 v67, v45, v41, s42\n"
    "v_perm_b32 v71, v45, v41, s43\n"
    "v_perm_b32 v75, v46, v42, s42\n"
    "v_perm_b32 v79, v46, v42, s43\n"
    "s_waitcnt vmcnt(6)\n"
    "v_perm_b32 v80, v52, v48, s42\n"
    "v_perm_b32 v84, v52, v48, s43\n"
    "v_perm_b32 v88, v53, v49, s42\n"
    "v_perm_b32 v92, v53, v49, s43\n"
    "v_perm_b32 v96, v54, v50, s42\n"
    "v_perm_b32 v100, v54, v50, s43\n"
    "s_waitcnt vmcnt(4)\n"
    "v_perm_b32 v81, v108, v104, s42\n"
    "v_perm_b32 v85, v108, v104, s43\n"
    "v_perm_b32 v89, v109, v105, s42\n"
    "v_perm_b32 v93, v109, v105, s43\n"
    "v_perm_b32 v97, v110, v106, s42\n"
    "v_perm_b32 v101, v110, v106, s43\n"
    "s_waitcnt vmcnt(2)\n"
    "v_perm_b32 v82, v116, v112, s42\n"
    "v_perm_b32 v86, v116, v112, s43\n"
    "v_perm_b32 v90, v117, v113, s42\n"
    "v_perm_b32 v94, v117, v113, s43\n"
    "v_perm_b32 v98, v118, v114, s42\n"
    "v_perm_b32 v102, v118, v114, s43\n"
    "s_waitcnt vmcnt(0)\n"
    "v_perm_b32 v83, v124, v120, s42\n"
    "v_perm_b32 v87, v124, v120, s43\n"
    "v_perm_b32 v91, v125, v121, s42\n"
    "v_perm_b32 v95, v125, v121, s43\n"
    "v_perm_b32 v99, v126, v122, s42\n"
    "v_perm_b32 v103, v126, v122, s43\n"
    "v_and_b32 v7, 7, v0\n"
    "v_mul_u32_u24 v7, 144, v7\n"
    "v_lshl_add_u32 v7, v3, 5, v7\n"
    "v_add_u32 v7, 150016, v7\n"
    "v_mov_b32 v8, 148224\n"
    "v_cmp_gt_u32 vcc, 8, v2\n"
    "s_nop 1\n"
    "v_cndmask_b32 v4, v8, v7, vcc\n"
    "v_cndmask_b32 v5, v7, v8, vcc\n"
    "v_and_b32 v6, 1, v3\n"
    "v_mul_u32_u24 v6, 6144, v6\n"
    "v_lshl_add_u32 v6, v2, 5, v6\n"
    "v_lshrrev_b32 v7, 1, v3\n"
    "v_lshl_add_u32 v6, v7, 4, v6\n"
    "s_mul_i32 s42, s58, 12416\n"
    "s_lshl_b32 s43, s37, 9\n"
    "s_add_u32 s42, s42, s43\n"
    "s_add_u32 s42, s42, 5656576\n"
    "v_add_u32 v6, s42, v6\n"
    "s_branch MOE2\n"
    "MOEX:\n"
    "s_barrier\n"
    "MOE2:\n"
    "s_lshl_b32 s42, s37, 2\n"
    "s_add_u32 s42, s42, 18688\n"
    "v_mov_b32 v7, s42\n"
    "v_lshlrev_b32 v8, 4, v1\n"
    "v_min_u32 v9, 7, v1\n"
    "v_lshlrev_b32 v9, 4, v9\n"
    "v_add_u32 v9, 1024, v9\n"
    "s_mul_i32 s42, s37, 1168\n"
    "v_add_u32 v10, s42, v8\n"
    "v_add_u32 v11, s42, v9\n"
    "v_mov_b32 v12, 148096\n"
    "s_mov_b32 s59, 3301376\n"
    "s_movk_i32 s60, 1152\n"
    "s_movk_i32 s61, 0x7ff\n"
    "s_lshl_b32 s62, s37, 2\n"
    "s_cmpk_ge_u32 s37, 12\n"
    "s_cbranch_scc1 TOPK\n"
    "s_barrier\n"
    "s_barrier\n"
    "s_branch MOE3\n"
    "TOPK:\n"
    "s_setprio 3\n"
    "s_mul_i32 s42, s37, 9\n"
    "s_add_u32 s42, s42, s58\n"
    "s_mul_i32 s43, s42, 1821\n"
    "s_lshr_b32 s43, s43, 16\n"
    "s_mul_i32 s43, s43, 36\n"
    "s_sub_u32 s42, s42, s43\n"
    "s_lshl_b32 s42, s42, 7\n"
    "s_sub_u32 s42, 94084, s42\n"
    "v_lshlrev_b32 v13, 4, v0\n"
    "s_mov_b32 s44, 65536\n"
    "s_mov_b32 s45, 69632\n"
    "s_mov_b32 s46, 73728\n"
    "s_mov_b32 s47, 77824\n"
    "v_lshl_add_u32 v16, v2, 2, v12\n"
    "v_lshl_add_u32 v17, v3, 4, v12\n"
    "s_brev_b32 s48, 1\n"
    "v_mov_b32 v18, 4\n"
    "v_lshlrev_b32 v19, 2, v2\n"
    "v_mul_u32_u24 v52, 1168, v2\n"
    "s_branch E_47\n"
    "R_46:\n"
    "s_sleep 4\n"
    "s_sub_u32 s39, s39, 1\n"
    "s_cbranch_scc1 ABORT\n"
    "E_47:\n"
    "buffer_load_dword v20, off, s[16:19], s42 sc1\n"
    "s_waitcnt vmcnt(0)\n"
    "v_cmp_ne_u32 vcc, s36, v20\n"
    "s_cbranch_vccnz R_46\n"
    "buffer_load_dwordx4 v[24:27], v13, s[16:19], s44 offen sc1\n"
    "buffer_load_dwordx4 v[28:31], v13, s[16:19], s45 offen sc1\n"
    "buffer_load_dwordx4 v[32:35], v13, s[16:19], s46 offen sc1\n"
    "buffer_load_dwordx4 v[36:39], v13, s[16:19], s47 offen sc1\n"
    "buffer_load_dwordx2 v[40:41], off, s[16:19], 0 offset:2048 sc1\n"
    "s_branch E_49\n"
    "PAD28:\n"
    "s_cbranch_execz SIL1\n"
    "R_48:\n"
    "s_sub_u32 s39, s39, 1\n"
    "s_cbranch_scc1 ABORT\n"
    "s_wqm_b64 exec, vcc\n"
    "buffer_load_dwordx4 v[24:27], v13, s[16:19], s44 offen sc1\n"
    "buffer_load_dwordx4 v[28:31], v13, s[16:19], s45 offen sc1\n"
    "buffer_load_dwordx4 v[32:35], v13, s[16:19], s46 offen sc1\n"
    "buffer_load_dwordx4 v[36:39], v13, s[16:19], s47 offen sc1\n"
    "buffer_load_dwordx2 v[40:41], off, s[16:19], 0 offset:2048 sc1\n"
    "s_mov_b64 exec, -1\n"
    "E_49:\n"
    "s_waitcnt vmcnt(3)\n"
    "v_bitop3_b32 v20, v25, v27, s36 bitop3:0x7e\n"
    "v_bitop3_b32 v21, v29, v31, s36 bitop3:0x7e\n"
    "v_max3_u32 v42, v24, v26, v28\n"
    "s_waitcnt vmcnt(2)\n"
    "v_bitop3_b32 v22, v33, v35, s36 bitop3:0x7e\n"
    "v_max3_u32 v42, v42, v30, v32\n"
    "v_or3_b32 v20, v20, v21, v22\n"
    "s_waitcnt vmcnt(0)\n"
    "v_bitop3_b32 v21, v37, v39, s36 bitop3:0x7e\n"
    "v_xor_b32 v22, s36, v41\n"
    "v_max3_u32 v42, v42, v34, v36\n"
    "v_or3_b32 v20, v20, v21, v22\n"
    "v_max_u32 v42, v42, v38\n"
    "v_cmp_ne_u32 vcc, 0, v20\n"
    "s_cbranch_vccnz R_48\n"
    "ds_max_u32 v16, v42\n"
    "v_max3_u32 v45, v24, v26, v28\n"
    "v_med3_u32 v43, v24, v26, v28\n"
    "v_min3_u32 v44, v24, v26, v28\n"
    "v_med3_u32 v44, v43, v44, v30\n"
    "v_med3_u32 v43, v45, v43, v30\n"
    "v_max_u32 v45, v45, v30\n"
    "v_med3_u32 v44, v43, v44, v32\n"
    "v_med3_u32 v43, v45, v43, v32\n"
    "s_waitcnt lgkmcnt(0)\n"
    "s_barrier\n"
    "ds_read_b32 v20, v16\n"
    "ds_read_b128 v[46:49], v17\n"
    "v_max_u32 v45, v45, v32\n"
    "v_med3_u32 v44, v43, v44, v34\n"
    "v_med3_u32 v43, v45, v43, v34\n"
    "v_max_u32 v45, v45, v34\n"
    "v_med3_u32 v44, v43, v44, v36\n"
    "v_med3_u32 v43, v45, v43, v36\n"
    "v_max_u32 v45, v45, v36\n"
    "v_med3_u32 v44, v43, v44, v38\n"
    "v_med3_u32 v43, v45, v43, v38\n"
    "s_waitcnt lgkmcnt(0)\n"
    "v_cmp_gt_u32 s[50:51], v46, v20\n"
    "v_cmp_gt_u32 s[52:53], v47, v20\n"
    "v_cmp_gt_u32 s[54:55], v48, v20\n"
    "v_cmp_gt_u32 vcc, v49, v20\n"
    "v_cndmask_b32 v21, 0, 1, s[50:51]\n"
    "v_addc_co_u32 v21, s[50:51], 0, v21, s[52:53]\n"
    "v_addc_co_u32 v21, s[50:51], 0, v21, s[54:55]\n"
    "v_addc_co_u32 v21, vcc, 0, v21, vcc\n"
    "v_mov_b32 v22, v21\n"
    "v_ashrrev_i32 v23, 31, v20\n"
    "v_bitop3_b32 v23, v23, v20, s48 bitop3:0x63\n"
    "v_permlane16_swap_b32 v21, v22\n"
    "v_add_u32 v21, v21, v22\n"
    "v_mov_b32 v22, v21\n"
    "v_sub_f32 v23, v23, v40\n"
    "v_ashrrev_i32 v46, 31, v23\n"
    "v_permlane32_swap_b32 v21, v22\n"
    "v_bitop3_b32 v23, v46, v23, s48 bitop3:0x36\n"
    "v_add_u32 v21, v21, v22\n"
    "v_min_u32 v23, v23, v20\n"
    "v_cmp_eq_u32 vcc, 7, v21\n"
    "s_ff1_i32_b64 s49, vcc\n"
    "v_readlane_b32 s49, v23, s49\n"
    "s_andn2_b32 s49, s49, s61\n"
    "s_nop 0\n"
    "v_cmp_le_u32 s[50:51], s49, v43\n"
    "v_cmp_le_u32 s[52:53], s49, v44\n"
    "v_cmpx_le_u32 vcc, s49, v42\n"
    "v_cndmask_b32 v21, 4, 8, s[50:51]\n"
    "s_cmp_lg_u64 s[52:53], 0\n"
    "s_cbranch_scc1 TOPS\n"
    "ds_add_rtn_u32 v21, v12, v21 offset:64\n"
    "s_waitcnt lgkmcnt(0)\n"
    "ds_write_b32 v21, v42 offset:18688\n"
    "s_mov_b64 exec, s[50:51]\n"
    "ds_write_b32 v21, v43 offset:18692\n"
    "TOPC:\n"
    "s_mov_b64 exec, -1\n"
    "s_waitcnt lgkmcnt(0)\n"
    "s_barrier\n"
    "MOE3:\n"
    "ds_read_b32 v20, v12 offset:64\n"
    "ds_read_b32 v21, v7\n"
    "s_cmpk_eq_u32 s37, 15\n"
    "s_cbranch_scc1 MOE15\n"
    "s_waitcnt lgkmcnt(0)\n"
    "v_readfirstlane_b32 s42, v20\n"
    "v_bitop3_b32 v21, v21, s61, v21 bitop3:0x0c\n"
    "s_cmpk_gt_u32 s42, 64\n"
    "s_cbranch_scc1 MANY\n"
    "MOE4:\n"
    "v_mad_u32_u24 v22, v21, s60, v8\n"
    "v_mad_u32_u24 v23, v21, s60, v9\n"
    "s_cmp_le_u32 s42, s62\n"
    "s_cbranch_scc1 MOE5\n"
    "R_50:\n"
    "buffer_load_dwordx4 v[24:27], v22, s[16:19], s59 offen sc1\n"
    "buffer_load_dwordx4 v[28:31], v23, s[16:19], s59 offen sc1\n"
    "s_waitcnt vmcnt(1)\n"
    "v_max3_u32 v20, v24, v25, v26\n"
    "ds_write_b128 v10, v[24:27]\n"
    "s_waitcnt vmcnt(0)\n"
    "v_max3_u32 v21, v28, v29, v30\n"
    "ds_write_b128 v11, v[28:31]\n"
    "v_max3_u32 v20, v20, v27, v31\n"
    "v_max_u32 v20, v20, v21\n"
    "v_cmp_eq_u32 vcc, -1, v20\n"
    "s_cbranch_vccnz ROWS\n"
    "MOE5:\n"
    "s_waitcnt lgkmcnt(0)\n"
    "s_barrier\n"
    "s_cmpk_ge_u32 s37, 8\n"
    "s_cbranch_scc1 MOE6\n"
    "ds_read_b32 v7, v4\n"
    "s_waitcnt lgkmcnt(0)\n"
    "v_add_u32 v7, v7, v5\n"
    "ds_read_b128 v[20:23], v7 offset:0\n"
    "ds_read_b128 v[24:27], v7 offset:128\n"
    "ds_read_b128 v[28:31], v7 offset:256\n"
    "ds_read_b128 v[32:35], v7 offset:384\n"
    "ds_read_b128 v[36:39], v7 offset:512\n"
    "ds_read_b128 v[40:43], v7 offset:640\n"
    "ds_read_b128 v[44:47], v7 offset:768\n"
    "ds_read_b128 v[48:51], v7 offset:896\n"
    "ds_read_b128 v[52:55], v7 offset:1024\n"
    "s_waitcnt lgkmcnt(8)\n"
    "v_mfma_f32_16x16x32_bf16 v[12:15], v[20:23], v[56:59], 0\n"
    "v_mfma_f32_16x16x32_bf16 v[16:19], v[20:23], v[60:63], 0\n"
    "s_waitcnt lgkmcnt(7)\n"
    "v_mfma_f32_16x16x32_bf16 v[12:15], v[24:27], v[64:67], v[12:15]\n"
    "v_mfma_f32_16x16x32_bf16 v[16:19], v[24:27], v[68:71], v[16:19]\n"
    "s_waitcnt lgkmcnt(6)\n"
    "v_mfma_f32_16x16x32_bf16 v[12:15], v[28:31], v[72:75], v[12:15]\n"
    "v_mfma_f32_16x16x32_bf16 v[16:19], v[28:31], v[76:79], v[16:19]\n"
    "s_waitcnt lgkmcnt(5)\n"
    "v_mfma_f32_16x16x32_bf16 v[12:15], v[32:35], v[80:83], v[12:15]\n"
    "v_mfma_f32_16x16x32_bf16 v[16:19], v[32:35], v[84:87], v[16:19]\n"
    "s_waitcnt lgkmcnt(4)\n"
    "v_mfma_f32_16x16x32_bf16 v[12:15], v[36:39], v[88:91], v[12:15]\n"
    "v_mfma_f32_16x16x32_bf16 v[16:19], v[36:39], v[92:95], v[16:19]\n"
    "s_waitcnt lgkmcnt(3)\n"
    "v_mfma_f32_16x16x32_bf16 v[12:15], v[40:43], v[96:99], v[12:15]\n"
    "v_mfma_f32_16x16x32_bf16 v[16:19], v[40:43], v[100:103], v[16:19]\n"
    "s_waitcnt lgkmcnt(2)\n"
    "v_mfma_f32_16x16x32_bf16 v[12:15], v[44:47], v[104:107], v[12:15]\n"
    "v_mfma_f32_16x16x32_bf16 v[16:19], v[44:47], v[108:111], v[16:19]\n"
    "s_waitcnt lgkmcnt(1)\n"
    "v_mfma_f32_16x16x32_bf16 v[12:15], v[48:51], v[112:115], v[12:15]\n"
    "v_mfma_f32_16x16x32_bf16 v[16:19], v[48:51], v[116:119], v[16:19]\n"
    "s_waitcnt lgkmcnt(0)\n"
    "v_mfma_f32_16x16x32_bf16 v[12:15], v[52:55], v[120:123], v[12:15]\n"
    "v_mfma_f32_16x16x32_bf16 v[16:19], v[52:55], v[124:127], v[16:19]\n"
    "s_nop 6\n"
    "v_permlane32_swap_b32 v12, v14\n"
    "v_permlane32_swap_b32 v13, v15\n"
    "v_permlane32_swap_b32 v16, v18\n"
    "v_permlane32_swap_b32 v17, v19\n"
    "PAD29:\n"
    "s_cbranch_execz SIL1\n"
    "v_add_f32 v12, v12, v18\n"
    "v_add_f32 v13, v13, v19\n"
    "v_mov_b32 v14, v12\n"
    "s_nop 0\n"
    "v_mov_b32_dpp v12, v13 row_ror:8 row_mask:0xf bank_mask:0xc\n"
    "v_mov_b32_dpp v13, v14 row_ror:8 row_mask:0xf bank_mask:0x3\n"
    "v_cvt_pk_bf16_f32 v12, 0, v12\n"
    "v_mul_f32 v14, 0xbfb8aa3b, v12\n"
    "v_exp_f32 v14, v14\n"
    "v_cvt_pk_bf16_f32 v13, 0, v13\n"
    "v_add_f32 v14, 1.0, v14\n"
    "v_rcp_f32 v14, v14\n"
    "s_nop 0\n"
    "v_mul_f32 v12, v12, v14\n"
    "v_cvt_pk_bf16_f32 v12, 0, v12\n"
    "v_mul_f32 v12, v12, v13\n"
    "v_cvt_pk_bf16_f32 v12, v12, v12\n"
    "ds_write_b16 v6, v12\n"
    "s_waitcnt lgkmcnt(0)\n"
    "s_barrier\n"
    "s_cmpk_lg_u32 s2, 192\n"
    "s_cbranch_scc1 END\n"
    "s_cmpk_lg_u32 s37, 0\n"
    "s_cbranch_scc1 END\n"
    "v_mov_b32 v1, s36\n"
    "s_mov_b64 exec, 1\n"
    "buffer_store_dword v1, off, s[16:19], 0 sc1\n"
    "s_mov_b64 exec, -1\n"
    "s_branch END\n"
    "MOE6:\n"
    "s_cmpk_lt_u32 s37, 13\n"
    "s_cbranch_scc1 DOWN\n"
    "s_cmpk_lt_u32 s2, 192\n"
    "s_cbranch_scc1 DOWN\n"
    "s_and_b32 s42, s2, 7\n"
    "s_lshl_b32 s42, s42, 3\n"
    "s_sub_u32 s43, s2, 192\n"
    "s_lshr_b32 s43, s43, 3\n"
    "s_add_u32 s42, s42, s43\n"
    "s_mul_i32 s42, s42, 3\n"
    "s_add_u32 s42, s42, s37\n"
    "s_sub_u32 s42, s42, 13\n"
    "s_mul_i32 s43, s42, 1366\n"
    "s_lshr_b32 s43, s43, 16\n"
    "s_mul_i32 s44, s43, 48\n"
    "s_sub_u32 s42, s42, s44\n"
    "s_lshl_b32 s42, s42, 2\n"
    "s_lshl_b32 s44, s43, 4\n"
    "s_add_u32 s44, s44, 151168\n"
    "v_and_b32 v20, 1, v0\n"
    "v_lshl_add_u32 v21, v20, 3, s44\n"
    "ds_read_b64 v[22:23], v21\n"
    "v_add_u32 v24, s42, v3\n"
    "v_lshrrev_b32 v25, 1, v2\n"
    "v_min_u32 v26, 2, v25\n"
    "v_mad_u32_u24 v27, v24, 3, v26\n"
    "v_lshlrev_b32 v28, 3, v27\n"
    "s_mov_b32 s45, 43520\n"
    "s_mov_b32 s46, 71168\n"
    "v_mul_u32_u24 v24, 16512, v24\n"
    "s_waitcnt lgkmcnt(0)\n"
    "v_bitop3_b32 v29, v23, s61, v23 bitop3:0x0c\n"
    "v_lshl_add_u32 v22, v27, 1, v22\n"
    "v_mov_b32 v40, 0\n"
    "v_mov_b32 v41, 0\n"
    "ds_read_u16_d16_hi v40, v22\n"
    "v_mul_u32_u24 v30, 1152, v29\n"
    "v_lshl_add_u32 v30, v27, 1, v30\n"
    "global_load_short_d16_hi v41, v30, s[4:5]\n"
    "v_lshrrev_b32 v31, 1, v29\n"
    "v_lshl_add_u32 v24, v31, 4, v24\n"
    "s_mov_b32 s47, 131072\n"
    "v_lshrrev_b32 v31, 4, v29\n"
    "v_lshlrev_b32 v31, 2, v31\n"
    "s_setprio 0\n"
    "s_cmpk_eq_u32 s37, 13\n"
    "s_cbranch_scc1 DOWN\n"
    "s_waitcnt lgkmcnt(0)\n"
    "s_barrier\n"
    "s_branch FIN\n"
    "DOWN:\n"
    "s_cmpk_ge_u32 s37, 14\n"
    "s_cbranch_scc1 END\n"
    "s_setprio 3\n"
    "v_mov_b32 v47, s36\n"
    "v_mov_b32 v51, s36\n"
    "s_waitcnt lgkmcnt(0)\n"
    "s_barrier\n"
    "ds_read_b128 v[104:107], v4\n"
    "ds_read_b128 v[108:111], v4 offset:16\n"
    "ds_read_b128 v[112:115], v5\n"
    "ds_read_b128 v[116:119], v5 offset:16\n"
    "s_waitcnt lgkmcnt(3)\n"
    "v_mfma_f32_16x16x32_bf16 v[8:11], v[104:107], v[56:59], 0\n"
    "v_mfma_f32_16x16x32_bf16 v[12:15], v[104:107], v[60:63], 0\n"
    "v_mfma_f32_16x16x32_bf16 v[16:19], v[104:107], v[64:67], 0\n"
    "s_waitcnt lgkmcnt(2)\n"
    "v_mfma_f32_16x16x32_bf16 v[8:11], v[108:111], v[80:83], v[8:11]\n"
    "v_mfma_f32_16x16x32_bf16 v[12:15], v[108:111], v[84:87], v[12:15]\n"
    "v_mfma_f32_16x16x32_bf16 v[16:19], v[108:111], v[88:91], v[16:19]\n"
    "s_waitcnt lgkmcnt(1)\n"
    "v_mfma_f32_16x16x32_bf16 v[8:11], v[112:115], v[68:71], v[8:11]\n"
    "v_mfma_f32_16x16x32_bf16 v[12:15], v[112:115], v[72:75], v[12:15]\n"
    "v_mfma_f32_16x16x32_bf16 v[16:19], v[112:115], v[76:79], v[16:19]\n"
    "s_waitcnt lgkmcnt(0)\n"
    "v_mfma_f32_16x16x32_bf16 v[8:11], v[116:119], v[92:95], v[8:11]\n"
    "v_mfma_f32_16x16x32_bf16 v[12:15], v[116:119], v[96:99], v[12:15]\n"
    "v_mfma_f32_16x16x32_bf16 v[16:19], v[116:119], v[100:103], v[16:19]\n"
    "s_nop 5\n"
    "v_cvt_pk_bf16_f32 v44, v8, v9\n"
    "v_cvt_pk_bf16_f32 v45, v12, v13\n"
    "v_cvt_pk_bf16_f32 v46, v16, v17\n"
    "buffer_store_dwordx4 v[44:47], v6, s[16:19], 0 offen offset:0 sc1\n"
    "v_cvt_pk_bf16_f32 v48, v10, v11\n"
    "v_cvt_pk_bf16_f32 v49, v14, v15\n"
    "v_cvt_pk_bf16_f32 v50, v18, v19\n"
    "buffer_store_dwordx4 v[48:51], v6, s[16:19], 0 offen offset:3072 sc1\n"
    "s_cmpk_lg_u32 s37, 13\n"
    "s_cbranch_scc1 END\n"
    "s_cmpk_lt_u32 s2, 192\n"
    "s_cbranch_scc1 END\n"
    "FIN:\n"
    "s_branch E_52\n"
    "PAD30:\n"
    "s_cbranch_execz SIL1\n"
    "R_51:\n"
    "s_sub_u32 s39, s39, 1\n"
    "s_cbranch_scc1 ABORT\n"
    "E_52:\n"
    "buffer_load_dwordx4 v[32:35], v24, s[16:19], s47 offen sc1\n"
    "buffer_load_dwordx2 v[36:37], v28, s[16:19], s45 offen sc1\n"
    "buffer_load_dwordx2 v[38:39], v28, s[16:19], s46 offen sc1\n"
    "s_waitcnt vmcnt(0)\n"
    "v_bitop3_b32 v42, v37, v39, s36 bitop3:0x7e\n"
    "v_xor_b32 v43, s36, v35\n"
    "v_or_b32 v42, v42, v43\n"
    "v_cmp_ne_u32 vcc, 0, v42\n"
    "s_cbranch_vccnz R_51\n"
    "v_cmp_eq_u32 vcc, 1, v26\n"
    "v_cmp_eq_u32 s[50:51], 2, v26\n"
    "v_and_b32 v42, 1, v29\n"
    "v_cndmask_b32 v32, v32, v33, vcc\n"
    "v_cndmask_b32 v32, v32, v34, s[50:51]\n"
    "v_lshlrev_b32 v42, 4, v42\n"
    "v_lshrrev_b32 v32, v42, v32\n"
    "v_lshlrev_b32 v32, 16, v32\n"
    "v_add_f32 v41, v41, v32\n"
    "v_add_f32 v36, v36, v38\n"
    "v_cvt_pk_bf16_f32 v41, 0, v41\n"
    "v_cvt_pk_bf16_f32 v36, 0, v36\n"
    "s_brev_b32 s48, 1\n"
    "v_ashrrev_i32 v42, 31, v23\n"
    "v_bitop3_b32 v23, v42, v23, s48 bitop3:0x63\n"
    "v_and_b32 v23, 0xfffff800, v23\n"
    "v_cvt_pk_bf16_f32 v23, 0, v23\n"
    "v_mul_f32 v23, 0xbfb8aa3b, v23\n"
    "v_exp_f32 v23, v23\n"
    "v_lshlrev_b32 v20, 4, v20\n"
    "v_add_f32 v23, 1.0, v23\n"
    "v_rcp_f32 v23, v23\n"
    "s_movk_i32 s44, 0x3f80\n"
    "v_lshlrev_b32_e64 v20, v20, s44\n"
    "v_cvt_pk_bf16_f32 v23, 0, v23\n"
    "v_cmp_eq_u32 vcc, 0, v25\n"
    "s_nop 1\n"
    "v_cndmask_b32 v32, 0, v20, vcc\n"
    "v_cmp_eq_u32 vcc, 1, v25\n"
    "s_nop 1\n"
    "v_cndmask_b32 v33, 0, v20, vcc\n"
    "v_cmp_eq_u32 vcc, 2, v25\n"
    "s_nop 1\n"
    "v_cndmask_b32 v34, 0, v20, vcc\n"
    "v_mov_b32 v35, 0\n"
    "v_lshrrev_b32 v42, 2, v1\n"
    "v_mul_u32_u24 v42, 12416, v42\n"
    "v_and_b32 v43, 3, v0\n"
    "v_lshl_add_u32 v42, v43, 4, v42\n"
    "s_mul_i32 s43, s43, 3072\n"
    "s_lshl_b32 s42, s42, 4\n"
    "s_add_u32 s42, s42, s43\n"
    "s_add_u32 s50, s42, 5660672\n"
    "s_add_u32 s51, s42, 5859328\n"
    "s_add_u32 s52, s42, 6057984\n"
    "s_add_u32 s53, s42, 6256640\n"
    "s_add_u32 s54, s42, 6455296\n"
    "v_cmp_gt_u32 vcc, 32, v1\n"
    "v_mov_b32 v43, 0x7ffffff0\n"
    "v_mov_b32 v21, s36\n"
    "v_cndmask_b32 v43, v43, v42, vcc\n"
    "v_cndmask_b32 v21, 0, v21, vcc\n"
    "s_mov_b32 s46, 0x003f003f\n"
    "s_mov_b32 s47, 0x003f003f\n"
    "s_sleep 4\n"
    "buffer_load_dwordx4 v[104:107], v42, s[16:19], s50 offen sc1\n"
    "buffer_load_dwordx4 v[108:111], v42, s[16:19], s51 offen sc1\n"
    "buffer_load_dwordx4 v[112:115], v42, s[16:19], s52 offen sc1\n"
    "buffer_load_dwordx4 v[116:119], v42, s[16:19], s53 offen sc1\n"
    "buffer_load_dwordx4 v[120:123], v43, s[16:19], s54 offen sc1\n"
    "buffer_load_dword v22, v31, s[16:19], 0 offen offset:1024 sc1\n"
    "s_branch E_54\n"
    "R_53:\n"
    "s_sub_u32 s39, s39, 1\n"
    "s_cbranch_scc1 ABORT\n"
    "s_wqm_b64 exec, vcc\n"
    "buffer_load_dwordx4 v[104:107], v42, s[16:19], s50 offen sc1\n"
    "buffer_load_dwordx4 v[108:111], v42, s[16:19], s51 offen sc1\n"
    "buffer_load_dwordx4 v[112:115], v42, s[16:19], s52 offen sc1\n"
    "buffer_load_dwordx4 v[116:119], v42, s[16:19], s53 offen sc1\n"
    "buffer_load_dwordx4 v[120:123], v43, s[16:19], s54 offen sc1\n"
    "buffer_load_dword v22, v31, s[16:19], 0 offen offset:1024 sc1\n"
    "s_mov_b64 exec, -1\n"
    "E_54:\n"
    "s_waitcnt vmcnt(2)\n"
    "v_bitop3_b32 v24, v107, v111, s36 bitop3:0x7e\n"
    "v_bitop3_b32 v25, v115, v119, s36 bitop3:0x7e\n"
    "s_waitcnt vmcnt(0)\n"
    "v_xor_b32 v26, v21, v123\n"
    "v_bitop3_b32 v24, v24, v22, s36 bitop3:0xf6\n"
    "v_or3_b32 v24, v24, v25, v26\n"
    "v_cmp_ne_u32 vcc, 0, v24\n"
    "s_cbranch_vccnz R_53\n"
    "v_mov_b32 v107, 0\n"
    "v_mov_b32 v111, 0\n"
    "v_mov_b32 v115, 0\n"
    "v_mov_b32 v119, 0\n"
    "v_mov_b32 v123, 0\n"
    "v_mfma_f32_16x16x32_bf16 v[8:11], v[104:107], v[32:35], 0\n"
    "v_mfma_f32_16x16x32_bf16 v[8:11], v[108:111], v[32:35], v[8:11]\n"
    "v_mfma_f32_16x16x32_bf16 v[8:11], v[112:115], v[32:35], v[8:11]\n"
    "v_mfma_f32_16x16x32_bf16 v[8:11], v[116:119], v[32:35], v[8:11]\n"
    "v_mfma_f32_16x16x32_bf16 v[8:11], v[120:123], v[32:35], v[8:11]\n"
    "s_nop 7\n"
    "v_permlane32_swap_b32 v8, v10\n"
    "v_permlane32_swap_b32 v9, v11\n"
    "v_add_f32 v8, v8, v10\n"
    "v_add_f32 v9, v9, v11\n"
    "s_nop 1\n"
    "v_permlane16_swap_b32 v8, v9\n"
    "v_add_f32 v8, v8, v9\n"
    "v_cvt_pk_bf16_f32 v8, 0, v8\n"
    "v_mul_f32 v8, v8, v23\n"
    "v_cvt_pk_bf16_f32 v8, 0, v8\n"
    "v_add_f32 v8, v40, v8\n"
    "v_cvt_pk_bf16_f32 v8, 0, v8\n"
    "v_mul_f32 v8, v36, v8\n"
    "v_cvt_pk_bf16_f32 v8, 0, v8\n"
    "v_add_f32 v8, v41, v8\n"
    "v_cvt_pk_bf16_f32 v8, v8, v8\n"
    "s_mov_b64 exec, s[46:47]\n"
    "global_store_short v30, v8, s[34:35]\n"
    "s_mov_b64 exec, -1\n"
    "s_branch END\n"
    "MOE15:\n"
    "ds_read_b32 v32, v19 offset:18688\n"
    "s_waitcnt lgkmcnt(0)\n"
    "v_readfirstlane_b32 s42, v20\n"
    "s_cmpk_gt_u32 s42, 64\n"
    "s_cbranch_scc1 MANY\n"
    "MOE16:\n"
    "v_bitop3_b32 v32, v32, s61, v32 bitop3:0x0c\n"
    "v_lshlrev_b32 v32, 3, v32\n"
    "s_movk_i32 s43, 4096\n"
    "v_cmp_gt_u32 s[50:51], s42, v19\n"
    "v_bitop3_b32 v21, v21, s61, v21 bitop3:0x0c\n"
    "R_55:\n"
    "buffer_load_dwordx2 v[34:35], v32, s[16:19], s43 offen sc1\n"
    "s_waitcnt vmcnt(0)\n"
    "v_cmp_ne_u32 vcc, s36, v35\n"
    "v_cndmask_b32 v53, 0, v34, s[50:51]\n"
    "s_and_b64 vcc, vcc, s[50:51]\n"
    "s_cbranch_vccnz XKS\n"
    "v_mov_b32 v34, v53\n"
    "s_nop 1\n"
    "v_mov_b32_dpp v34, v53 row_ror:4 row_mask:0x2 bank_mask:0xf\n"
    "s_nop 1\n"
    "v_mov_b32_dpp v34, v53 row_ror:8 row_mask:0x4 bank_mask:0xf\n"
    "s_nop 1\n"
    "v_mov_b32_dpp v34, v53 row_ror:12 row_mask:0x8 bank_mask:0xf\n"
    "v_cmp_gt_u32 s[52:53], v34, v53\n"
    "s_nop 0\n"
    "v_mov_b32_dpp v35, v34 row_ror:1 row_mask:0xf bank_mask:0xf\n"
    "v_mov_b32_dpp v36, v34 row_ror:2 row_mask:0xf bank_mask:0xf\n"
    "v_mov_b32_dpp v37, v34 row_ror:3 row_mask:0xf bank_mask:0xf\n"
    "v_cmp_gt_u32 s[54:55], v35, v53\n"
    "v_cmp_gt_u32 s[50:51], v36, v53\n"
    "v_cmp_gt_u32 vcc, v37, v53\n"
    "v_cndmask_b32 v34, 0, 1, s[52:53]\n"
    "v_addc_co_u32 v34, s[52:53], 0, v34, s[54:55]\n"
    "v_addc_co_u32 v34, s[52:53], 0, v34, s[50:51]\n"
    "v_addc_co_u32 v34, vcc, 0, v34, vcc\n"
    "v_mov_b32 v35, v34\n"
    "s_nop 1\n"
    "v_permlane16_swap_b32 v34, v35\n"
    "v_add_u32 v34, v34, v35\n"
    "v_mov_b32 v35, v34\n"
    "s_nop 1\n"
    "v_permlane32_swap_b32 v34, v35\n"
    "v_add_u32 v34, v34, v35\n"
    "v_min_u32 v34, 8, v34\n"
    "v_lshl_add_u32 v34, v34, 3, v12\n"
    "ds_write_b64 v34, v[52:53] offset:3072\n"
    "s_branch MOE4\n"
    "XKS:\n"
    "s_sub_u32 s39, s39, 1\n"
    "s_cbranch_scc1 ABORT\n"
    "s_branch R_55\n"
    "ROWS:\n"
    "s_sub_u32 s39, s39, 16\n"
    "s_cbranch_scc0 R_50\n"
    "s_mov_b32 s39, 0x10000\n"
    "s_branch MOE5\n"
    "TOPS:\n"
    "s_mov_b64 exec, -1\n"
    "v_cmpx_le_u32 vcc, s49, v24\n"
    "ds_add_rtn_u32 v21, v12, v18 offset:64\n"
    "s_waitcnt lgkmcnt(0)\n"
    "ds_write_b32 v21, v24 offset:18688\n"
    "s_mov_b64 exec, -1\n"
    "v_cmpx_le_u32 vcc, s49, v26\n"
    "ds_add_rtn_u32 v21, v12, v18 offset:64\n"
    "s_waitcnt lgkmcnt(0)\n"
    "ds_write_b32 v21, v26 offset:18688\n"
    "s_mov_b64 exec, -1\n"
    "v_cmpx_le_u32 vcc, s49, v28\n"
    "ds_add_rtn_u32 v21, v12, v18 offset:64\n"
    "s_waitcnt lgkmcnt(0)\n"
    "ds_write_b32 v21, v28 offset:18688\n"
    "s_mov_b64 exec, -1\n"
    "v_cmpx_le_u32 vcc, s49, v30\n"
    "ds_add_rtn_u32 v21, v12, v18 offset:64\n"
    "s_waitcnt lgkmcnt(0)\n"
    "ds_write_b32 v21, v30 offset:18688\n"
    "s_mov_b64 exec, -1\n"
    "v_cmpx_le_u32 vcc, s49, v32\n"
    "ds_add_rtn_u32 v21, v12, v18 offset:64\n"
    "s_waitcnt lgkmcnt(0)\n"
    "ds_write_b32 v21, v32 offset:18688\n"
    "s_mov_b64 exec, -1\n"
    "v_cmpx_le_u32 vcc, s49, v34\n"
    "ds_add_rtn_u32 v21, v12, v18 offset:64\n"
    "s_waitcnt lgkmcnt(0)\n"
    "ds_write_b32 v21, v34 offset:18688\n"
    "s_mov_b64 exec, -1\n"
    "v_cmpx_le_u32 vcc, s49, v36\n"
    "ds_add_rtn_u32 v21, v12, v18 offset:64\n"
    "s_waitcnt lgkmcnt(0)\n"
    "ds_write_b32 v21, v36 offset:18688\n"
    "s_mov_b64 exec, -1\n"
    "v_cmpx_le_u32 vcc, s49, v38\n"
    "ds_add_rtn_u32 v21, v12, v18 offset:64\n"
    "s_waitcnt lgkmcnt(0)\n"
    "ds_write_b32 v21, v38 offset:18688\n"
    "s_branch TOPC\n"
    "MANY:\n"
    "v_lshlrev_b32 v22, 2, v0\n"
    "MANY1:\n"
    "v_cmpx_gt_u32 vcc, s42, v22\n"
    "s_cbranch_execz MANY3\n"
    "ds_read_b32 v23, v22 offset:18688\n"
    "v_mov_b32 v24, 0\n"
    "s_mov_b32 s43, 0\n"
    "MANY2:\n"
    "v_mov_b32 v25, s43\n"
    "ds_read_b32 v25, v25 offset:18688\n"
    "s_add_u32 s43, s43, 4\n"
    "s_waitcnt lgkmcnt(0)\n"
    "v_cmp_gt_u32 vcc, v25, v23\n"
    "s_nop 1\n"
    "v_addc_co_u32 v24, vcc, 0, v24, vcc\n"
    "s_cmp_lt_u32 s43, s42\n"
    "s_cbranch_scc1 MANY2\n"
    "v_min_u32 v24, 16, v24\n"
    "v_lshl_add_u32 v24, v24, 2, v12\n"
    "ds_write_b32 v24, v23 offset:3152\n"
    "v_add_u32 v22, 4096, v22\n"
    "s_branch MANY1\n"
    "MANY3:\n"
    "s_mov_b64 exec, -1\n"
    "v_add_u32 v22, s62, v12\n"
    "v_lshl_add_u32 v23, v2, 2, v12\n"
    "s_waitcnt lgkmcnt(0)\n"
    "s_barrier\n"
    "ds_read_b32 v21, v22 offset:3152\n"
    "ds_read_b32 v32, v23 offset:3152\n"
    "s_movk_i32 s42, 64\n"
    "s_waitcnt lgkmcnt(0)\n"
    "s_cmpk_eq_u32 s37, 15\n"
    "s_cbranch_scc1 MOE16\n"
    "v_bitop3_b32 v21, v21, s61, v21 bitop3:0x0c\n"
    "s_branch MOE4\n"
    "END:\n"
    "s_endpgm\n"

    :: "{s[0:1]}"(__builtin_amdgcn_kernarg_segment_ptr()), "{s2}"(blockIdx.x), "{v0}"(threadIdx.x) : "v127", "s99", "vcc", "memory");
}

extern "C" void kernel_launch(void* const* d_in, const int* in_sizes, int n_in, void* d_out, int out_size, void* d_ws, size_t ws_size, hipStream_t stream) {
  (void)hipFuncSetAttribute((const void*)mega, hipFuncAttributeMaxDynamicSharedMemorySize, 151328);
  Args p;
  p.x = d_in[0]; p.lar = d_in[1]; p.aim = d_in[2]; p.ldt = d_in[5]; p.cvec = d_in[12]; p.wmod = d_in[13];
  p.ws = d_ws; p.ws_bytes = 9857536; p.ws_flags = 0x00020000u;
  p.cre = d_in[3]; p.cim = d_in[4]; p.dsk = d_in[6]; p.wr = d_in[8]; p.wg = d_in[9]; p.wu = d_in[10]; p.wd = d_in[11];
  p.out = d_out;
  mega<<<256, 1024, 151328, stream>>>(p);
}
```

```cpp
#include <hip/hip_runtime.h>
#include <stdint.h>

struct Args {
  const void *x, *lar, *aim, *ldt, *cvec, *wmod;
  void* ws; uint32_t ws_bytes, ws_flags;
  const void *cre, *cim, *dsk, *wr, *wg, *wu, *wd;
  void* out;
};
extern "C" __device__ __attribute__((used, aligned(128))) const uint32_t mega_tab[3840] = {
  0x0, 0x0, 0x0, 0x0, 0x0, 0x0, 0x0, 0x0, 0x0, 0x0, 0x0, 0x0, 0x0, 0x0, 0x0, 0x0,
  0x0, 0x0, 0x0, 0x0, 0x0, 0x0, 0x0, 0x0, 0x0, 0x0, 0x0, 0x0, 0x0, 0x0, 0x0, 0x0,
  0x0, 0x0, 0x0, 0x0, 0x0, 0x0, 0x0, 0x0, 0x0, 0x0, 0x0, 0x0, 0x0, 0x0, 0x0, 0x3c000000,
  0x0, 0x0, 0x0, 0x3c003c00, 0x0, 0x0, 0x3c000000, 0x3c003c00, 0x0, 0x0, 0x3c003c00, 0x3c00, 0x0, 0x3c000000, 0x3c003c00, 0x0,
  0x0, 0x0, 0x0, 0x0, 0x0, 0x0, 0x0, 0x0, 0x0, 0x0, 0x0, 0x0, 0x0, 0x0, 0x0, 0x3c000000,
  0x0, 0x0, 0x0, 0x3c003c00, 0x0, 0x0, 0x3c000000, 0x3c003c00, 0x0, 0x0, 0x3c003c00, 0x3c00, 0x0, 0x3c000000, 0x3c003c00, 0x0,
  0x0, 0x3c003c00, 0x3c00, 0x0, 0x3c000000, 0x3c003c00, 0x0, 0x0, 0x3c003c00, 0x3c00, 0x0, 0x0, 0x3c003c00, 0x0, 0x0, 0x0,
  0x3c00, 0x0, 0x0, 0x0, 0x0, 0x0, 0x0, 0x0, 0x0, 0x0, 0x0, 0x0, 0x0, 0x0, 0x0, 0x0,
  0x0, 0x3c003c00, 0x3c00, 0x0, 0x3c000000, 0x3c003c00, 0x0, 0x0, 0x3c003c00, 0x3c00, 0x0, 0x0, 0x3c003c00, 0x0, 0x0, 0x0,
  0x3c00, 0x0, 0x0, 0x0, 0x0, 0x0, 0x0, 0x0, 0x0, 0x0, 0x0, 0x0, 0x0, 0x0, 0x0, 0x0,
  0x0, 0x0, 0x0, 0x0, 0x0, 0x0, 0x0, 0x0, 0x0, 0x0, 0x0, 0x0, 0x0, 0x0, 0x0, 0x0,
  0x0, 0x0, 0x0, 0x0, 0x0, 0x0, 0x0, 0x0, 0x0, 0x0, 0x0, 0x0, 0x0, 0x0, 0x0, 0x0,
  0x0, 0x0, 0x0, 0x0, 0x0, 0x0, 0x0, 0x0, 0x0, 0x0, 0x0, 0x0, 0x0, 0x0, 0x0, 0x0,
  0x0, 0x0, 0x0, 0x0, 0x0, 0x0, 0x0, 0x0, 0x0, 0x0, 0x0, 0x0, 0x0, 0x0, 0x0, 0x0,
  0x0, 0x0, 0x0, 0x0, 0x0, 0x0, 0x0, 0x0, 0x0, 0x0, 0x0, 0x0, 0x0, 0x0, 0x0, 0x0,
  0x0, 0x0, 0x0, 0x0, 0x0, 0x0, 0x0, 0x0, 0x0, 0x0, 0x0, 0x0, 0x0, 0x0, 0x0, 0x0,
  0x0, 0x0, 0x0, 0x0, 0x0, 0x0, 0x0, 0x0, 0x0, 0x0, 0x0, 0x0, 0x0, 0x0, 0x0, 0x0,
  0x0, 0x0, 0x0, 0x0, 0x0, 0x0, 0x0, 0x0, 0x0, 0x0, 0x0, 0x0, 0x0, 0x0, 0x0, 0x0,
  0x0, 0x0, 0x0, 0x0, 0x0, 0x0, 0x0, 0x0, 0x0, 0x0, 0x0, 0x0, 0x0, 0x0, 0x0, 0x0,
  0x0, 0x0, 0x0, 0x0, 0x0, 0x0, 0x0, 0x0, 0x0, 0x0, 0x0, 0x0, 0x0, 0x0, 0x0, 0x0,
  0x0, 0x0, 0x0, 0x0, 0x0, 0x0, 0x0, 0x0, 0x0, 0x0, 0x0, 0x0, 0x0, 0x0, 0x0, 0x0,
  0x0, 0x0, 0x0, 0x0, 0x0, 0x0, 0x0, 0x0, 0x0, 0x0, 0x0, 0x0, 0x0, 0x0, 0x0, 0x0,
  0x0, 0x0, 0x0, 0x0, 0x0, 0x0, 0x0, 0x0, 0x0, 0x0, 0x0, 0x0, 0x0, 0x0, 0x0, 0x3c000000,
  0x0, 0x0, 0x0, 0x3c003c00, 0x0, 0x0, 0x3c000000, 0x3c003c00, 0x0, 0x0, 0x3c003c00, 0x3c00, 0x0, 0x3c000000, 0x3c003c00, 0x0,
  0x0, 0x0, 0x0, 0x0, 0x0, 0x0, 0x0, 0x0, 0x0, 0x0, 0x0, 0x0, 0x0, 0x0, 0x0, 0x3c000000,
  0x0, 0x0, 0x0, 0x3c003c00, 0x0, 0x0, 0x3c000000, 0x3c003c00, 0x0, 0x0, 0x3c003c00, 0x3c00, 0x0, 0x3c000000, 0x3c003c00, 0x0,
  0x0, 0x3c003c00, 0x3c00, 0x0, 0x3c000000, 0x3c003c00, 0x0, 0x0, 0x3c003c00, 0x3c00, 0x0, 0x0, 0x3c003c00, 0x0, 0x0, 0x0,
  0x3c00, 0x0, 0x0, 0x0, 0x0, 0x0, 0x0, 0x0, 0x0, 0x0, 0x0, 0x0, 0x0, 0x0, 0x0, 0x0,
  0x0, 0x3c003c00, 0x3c00, 0x0, 0x3c000000, 0x3c003c00, 0x0, 0x0, 0x3c003c00, 0x3c00, 0x0, 0x0, 0x3c003c00, 0x0, 0x0, 0x0,
  0x3c00, 0x0, 0x0, 0x0, 0x0, 0x0, 0x0, 0x0, 0x0, 0x0, 0x0, 0x0, 0x0, 0x0, 0x0, 0x0,
  0x0, 0x0, 0x0, 0x0, 0x0, 0x0, 0x0, 0x0, 0x0, 0x0, 0x0, 0x0, 0x0, 0x0, 0x0, 0x0,
  0x0, 0x0, 0x0, 0x0, 0x0, 0x0, 0x0, 0x0, 0x0, 0x0, 0x0, 0x0, 0x0, 0x0, 0x0, 0x0,
  0x0, 0x0, 0x0, 0x0, 0x0, 0x0, 0x0, 0x0, 0x0, 0x0, 0x0, 0x0, 0x0, 0x0, 0x0, 0x0,
  0x0, 0x0, 0x0, 0x0, 0x0, 0x0, 0x0, 0x0, 0x0, 0x0, 0x0, 0x0, 0x0, 0x0, 0x0, 0x0,
  0x0, 0x0, 0x0, 0x0, 0x0, 0x0, 0x0, 0x0, 0x0, 0x0, 0x0, 0x0, 0x0, 0x0, 0x0, 0x0,
  0x0, 0x0, 0x0, 0x0, 0x0, 0x0, 0x0, 0x0, 0x0, 0x0, 0x0, 0x3c000000, 0x0, 0x0, 0x0, 0x3c003c00,
  0x0, 0x0, 0x0, 0x0, 0x0, 0x0, 0x0, 0x0, 0x0, 0x0, 0x0, 0x0, 0x0, 0x0, 0x0, 0x0,
  0x0, 0x0, 0x0, 0x0, 0x0, 0x0, 0x0, 0x0, 0x0, 0x0, 0x0, 0x3c000000, 0x0, 0x0, 0x0, 0x3c003c00,
  0x0, 0x0, 0x3c000000, 0x3c003c00, 0x0, 0x0, 0x3c003c00, 0x3c003c00, 0x0, 0x3c000000, 0x3c003c00, 0x3c003c00, 0x0, 0x3c003c00, 0x3c003c00, 0x3c00,
  0x3c000000, 0x3c003c00, 0x3c003c00, 0x0, 0x3c003c00, 0x3c003c00, 0x3c00, 0x0, 0x3c003c00, 0x3c003c00, 0x0, 0x0, 0x3c003c00, 0x3c00, 0x0, 0x0,
  0x0, 0x0, 0x3c000000, 0x3c003c00, 0x0, 0x0, 0x3c003c00, 0x3c003c00, 0x0, 0x3c000000, 0x3c003c00, 0x3c003c00, 0x0, 0x3c003c00, 0x3c003c00, 0x3c00,
  0x3c000000, 0x3c003c00, 0x3c003c00, 0x0, 0x3c003c00, 0x3c003c00, 0x3c00, 0x0, 0x3c003c00, 0x3c003c00, 0x0, 0x0, 0x3c003c00, 0x3c00, 0x0, 0x0,
  0x3c003c00, 0x0, 0x0, 0x0, 0x3c00, 0x0, 0x0, 0x0, 0x0, 0x0, 0x0, 0x0, 0x0, 0x0, 0x0, 0x0,
  0x0, 0x0, 0x0, 0x0, 0x0, 0x0, 0x0, 0x0, 0x0, 0x0, 0x0, 0x0, 0x0, 0x0, 0x0, 0x0,
  0x3c003c00, 0x0, 0x0, 0x0, 0x3c00, 0x0, 0x0, 0x0, 0x0, 0x0, 0x0, 0x0, 0x0, 0x0, 0x0, 0x0,
  0x0, 0x0, 0x0, 0x0, 0x0, 0x0, 0x0, 0x0, 0x0, 0x0, 0x0, 0x0, 0x0, 0x0, 0x0, 0x0,
  0x0, 0x0, 0x0, 0x0, 0x0, 0x0, 0x0, 0x0, 0x0, 0x0, 0x0, 0x0, 0x0, 0x0, 0x0, 0x0,
  0x0, 0x0, 0x0, 0x0, 0x0, 0x0, 0x0, 0x0, 0x0, 0x0, 0x0, 0x0, 0x0, 0x0, 0x0, 0x0,
  0x0, 0x0, 0x0, 0x0, 0x0, 0x0, 0x0, 0x0, 0x0, 0x0, 0x0, 0x0, 0x0, 0x0, 0x0, 0x0,
  0x0, 0x0, 0x0, 0x0, 0x0, 0x0, 0x0, 0x0, 0x0, 0x0, 0x0, 0x0, 0x0, 0x0, 0x0, 0x0,
  0x0, 0x0, 0x0, 0x0, 0x0, 0x0, 0x0, 0x0, 0x0, 0x0, 0x0, 0x0, 0x0, 0x0, 0x0, 0x0,
  0x0, 0x0, 0x0, 0x0, 0x0, 0x0, 0x0, 0x3c000000, 0x0, 0x0, 0x0, 0x3c003c00, 0x0, 0x0, 0x3c000000, 0x3c003c00,
  0x0, 0x0, 0x0, 0x0, 0x0, 0x0, 0x0, 0x0, 0x0, 0x0, 0x0, 0x0, 0x0, 0x0, 0x0, 0x0,
  0x0, 0x0, 0x0, 0x0, 0x0, 0x0, 0x0, 0x3c000000, 0x0, 0x0, 0x0, 0x3c003c00, 0x0, 0x0, 0x3c000000, 0x3c003c00,
  0x0, 0x0, 0x3c003c00, 0x3c003c00, 0x0, 0x3c000000, 0x3c003c00, 0x3c003c00, 0x0, 0x3c003c00, 0x3c003c00, 0x3c003c00, 0x3c000000, 0x3c003c00, 0x3c003c00, 0x3c003c00,
  0x3c003c00, 0x3c003c00, 0x3c003c00, 0x3c00, 0x3c003c00, 0x3c003c00, 0x3c003c00, 0x0, 0x3c003c00, 0x3c003c00, 0x3c00, 0x0, 0x3c003c00, 0x3c003c00, 0x0, 0x0,
  0x0, 0x0, 0x3c003c00, 0x3c003c00, 0x0, 0x3c000000, 0x3c003c00, 0x3c003c00, 0x0, 0x3c003c00, 0x3c003c00, 0x3c003c00, 0x3c000000, 0x3c003c00, 0x3c003c00, 0x3c003c00,
  0x3c003c00, 0x3c003c00, 0x3c003c00, 0x3c00, 0x3c003c00, 0x3c003c00, 0x3c003c00, 0x0, 0x3c003c00, 0x3c003c00, 0x3c00, 0x0, 0x3c003c00, 0x3c003c00, 0x0, 0x0,
  0x3c003c00, 0x3c00, 0x0, 0x0, 0x3c003c00, 0x0, 0x0, 0x0, 0x3c00, 0x0, 0x0, 0x0, 0x0, 0x0, 0x0, 0x0,
  0x0, 0x0, 0x0, 0x0, 0x0, 0x0, 0x0, 0x0, 0x0, 0x0, 0x0, 0x0, 0x0, 0x0, 0x0, 0x0,
  0x3c003c00, 0x3c00, 0x0, 0x0, 0x3c003c00, 0x0, 0x0, 0x0, 0x3c00, 0x0, 0x0, 0x0, 0x0, 0x0, 0x0, 0x0,
  0x0, 0x0, 0x0, 0x0, 0x0, 0x0, 0x0, 0x0, 0x0, 0x0, 0x0, 0x0, 0x0, 0x0, 0x0, 0x0,
  0x0, 0x0, 0x0, 0x0, 0x0, 0x0, 0x0, 0x0, 0x0, 0x0, 0x0, 0x0, 0x0, 0x0, 0x0, 0x0,
  0x0, 0x0, 0x0, 0x0, 0x0, 0x0, 0x0, 0x0, 0x0, 0x0, 0x0, 0x0, 0x0, 0x0, 0x0, 0x0,
  0x0, 0x0, 0x0, 0x0, 0x0, 0x0, 0x0, 0x0, 0x0, 0x0, 0x0, 0x0, 0x0, 0x0, 0x0, 0x0,
  0x0, 0x0, 0x0, 0x0, 0x0, 0x0, 0x0, 0x0, 0x0, 0x0, 0x0, 0x0, 0x0, 0x0, 0x0, 0x0,
  0x0, 0x0, 0x0, 0x0, 0x0, 0x0, 0x0, 0x0, 0x0, 0x0, 0x0, 0x0, 0x0, 0x0, 0x0, 0x0,
  0x0, 0x0, 0x0, 0x3c000000, 0x0, 0x0, 0x0, 0x3c003c00, 0x0, 0x0, 0x3c000000, 0x3c003c00, 0x0, 0x0, 0x3c003c00, 0x3c003c00,
  0x0, 0x0, 0x0, 0x0, 0x0, 0x0, 0x0, 0x0, 0x0, 0x0, 0x0, 0x0, 0x0, 0x0, 0x0, 0x0,
  0x0, 0x0, 0x0, 0x3c000000, 0x0, 0x0, 0x0, 0x3c003c00, 0x0, 0x0, 0x3c000000, 0x3c003c00, 0x0, 0x0, 0x3c003c00, 0x3c003c00,
  0x0, 0x3c000000, 0x3c003c00, 0x3c003c00, 0x0, 0x3c003c00, 0x3c003c00, 0x3c003c00, 0x3c000000, 0x3c003c00, 0x3c003c00, 0x3c003c00, 0x3c003c00, 0x3c003c00, 0x3c003c00, 0x3c003c00,
  0x3c003c00, 0x3c003c00, 0x3c003c00, 0x3c003c00, 0x3c003c00, 0x3c003c00, 0x3c003c00, 0x3c00, 0x3c003c00, 0x3c003c00, 0x3c003c00, 0x0, 0x3c003c00, 0x3c003c00, 0x3c00, 0x0,
  0x0, 0x3c000000, 0x3c003c00, 0x3c003c00, 0x0, 0x3c003c00, 0x3c003c00, 0x3c003c00, 0x3c000000, 0x3c003c00, 0x3c003c00, 0x3c003c00, 0x3c003c00, 0x3c003c00, 0x3c003c00, 0x3c003c00,
  0x3c003c00, 0x3c003c00, 0x3c003c00, 0x3c003c00, 0x3c003c00, 0x3c003c00, 0x3c003c00, 0x3c00, 0x3c003c00, 0x3c003c00, 0x3c003c00, 0x0, 0x3c003c00, 0x3c003c00, 0x3c00, 0x0,
  0x3c003c00, 0x3c003c00, 0x0, 0x0, 0x3c003c00, 0x3c00, 0x0, 0x0, 0x3c003c00, 0x0, 0x0, 0x0, 0x3c00, 0x0, 0x0, 0x0,
  0x0, 0x0, 0x0, 0x0, 0x0, 0x0, 0x0, 0x0, 0x0, 0x0, 0x0, 0x0, 0x0, 0x0, 0x0, 0x0,
  0x3c003c00, 0x3c003c00, 0x0, 0x0, 0x3c003c00, 0x3c00, 0x0, 0x0, 0x3c003c00, 0x0, 0x0, 0x0, 0x3c00, 0x0, 0x0, 0x0,
  0x0, 0x0, 0x0, 0x0, 0x0, 0x0, 0x0, 0x0, 0x0, 0x0, 0x0, 0x0, 0x0, 0x0, 0x0, 0x0,
  0x0, 0x0, 0x0, 0x0, 0x0, 0x0, 0x0, 0x0, 0x0, 0x0, 0x0, 0x0, 0x0, 0x0, 0x0, 0x0,
  0x0, 0x0, 0x0, 0x0, 0x0, 0x0, 0x0, 0x0, 0x0, 0x0, 0x0, 0x0, 0x0, 0x0, 0x0, 0x0,
  0x0, 0x0, 0x0, 0x0, 0x0, 0x0, 0x0, 0x0, 0x0, 0x0, 0x0, 0x0, 0x0, 0x0, 0x0, 0x0,
  0x0, 0x0, 0x0, 0x0, 0x0, 0x0, 0x0, 0x0, 0x0, 0x0, 0x0, 0x0, 0x0, 0x0, 0x0, 0x0,
  0x0, 0x0, 0x0, 0x0, 0x0, 0x0, 0x0, 0x0, 0x0, 0x0, 0x0, 0x0, 0x0, 0x0, 0x0, 0xffff0000,
  0x0, 0x0, 0x0, 0xffffffff, 0x0, 0x0, 0xffff0000, 0xffff, 0x0, 0x0, 0xffffffff, 0x0, 0x0, 0xffff0000, 0xffff, 0x0,
  0x0, 0x0, 0x0, 0x0, 0x0, 0x0, 0x0, 0x0, 0x0, 0x0, 0x0, 0x0, 0x0, 0x0, 0x0, 0xffff0000,
  0x0, 0x0, 0x0, 0xffffffff, 0x0, 0x0, 0xffff0000, 0xffff, 0x0, 0x0, 0xffffffff, 0x0, 0x0, 0xffff0000, 0xffff, 0x0,
  0x0, 0xffffffff, 0x0, 0x0, 0xffff0000, 0xffff, 0x0, 0x0, 0xffffffff, 0x0, 0x0, 0x0, 0xffff, 0x0, 0x0, 0x0,
  0x0, 0x0, 0x0, 0x0, 0x0, 0x0, 0x0, 0x0, 0x0, 0x0, 0x0, 0x0, 0x0, 0x0, 0x0, 0x0,
  0x0, 0xffffffff, 0x0, 0x0, 0xffff0000, 0xffff, 0x0, 0x0, 0xffffffff, 0x0, 0x0, 0x0, 0xffff, 0x0, 0x0, 0x0,
  0x0, 0x0, 0x0, 0x0, 0x0, 0x0, 0x0, 0x0, 0x0, 0x0, 0x0, 0x0, 0x0, 0x0, 0x0, 0x0,
  0x0, 0x0, 0x0, 0x0, 0x0, 0x0, 0x0, 0x0, 0x0, 0x0, 0x0, 0x0, 0x0, 0x0, 0x0, 0x0,
  0x0, 0x0, 0x0, 0x0, 0x0, 0x0, 0x0, 0x0, 0x0, 0x0, 0x0, 0x0, 0x0, 0x0, 0x0, 0x0,
  0x0, 0x0, 0x0, 0x0, 0x0, 0x0, 0x0, 0x0, 0x0, 0x0, 0x0, 0x0, 0x0, 0x0, 0x0, 0x0,
  0x0, 0x0, 0x0, 0x0, 0x0, 0x0, 0x0, 0x0, 0x0, 0x0, 0x0, 0x0, 0x0, 0x0, 0x0, 0x0,
  0x0, 0x0, 0x0, 0x0, 0x0, 0x0, 0x0, 0x0, 0x0, 0x0, 0x0, 0x0, 0x0, 0x0, 0x0, 0x0,
  0x0, 0x0, 0x0, 0x0, 0x0, 0x0, 0x0, 0x0, 0x0, 0x0, 0x0, 0x0, 0x0, 0x0, 0x0, 0x0,
  0x16520, 0x3020100, 0x7060504, 0x3020100, 0x16620, 0x3020100, 0x7060504, 0x3020100, 0x16530, 0x3020100, 0x7060504, 0x3020100, 0x16630, 0x3020100, 0x7060504, 0x3020100,
  0x16540, 0x3020100, 0x7060504, 0x3020100, 0x16640, 0x3020100, 0x7060504, 0x3020100, 0x16550, 0x3020100, 0x7060504, 0x3020100, 0x16650, 0x3020100, 0x7060504, 0x3020100,
  0x16560, 0x3020100, 0x7060504, 0x3020100, 0x16660, 0x3020100, 0x7060504, 0x3020100, 0x16570, 0x3020100, 0x7060504, 0x3020100, 0x16670, 0x3020100, 0x7060504, 0x3020100,
  0x16600, 0x3020100, 0x7060504, 0x3020100, 0x16700, 0x3020100, 0x7060504, 0x3020100, 0x16610, 0x3020100, 0x7060504, 0x3020100, 0x16710, 0x3020100, 0x7060504, 0x3020100,
  0x16528, 0x3020100, 0x7060504, 0x3020100, 0x16628, 0x3020100, 0x7060504, 0x3020100, 0x16538, 0x3020100, 0x7060504, 0x3020100, 0x16638, 0x3020100, 0x7060504, 0x3020100,
  0x16548, 0x3020100, 0x7060504, 0x3020100, 0x16648, 0x3020100, 0x7060504, 0x3020100, 0x16558, 0x3020100, 0x7060504, 0x3020100, 0x16658, 0x3020100, 0x7060504, 0x3020100,
  0x16568, 0x3020100, 0x7060504, 0x3020100, 0x16668, 0x3020100, 0x7060504, 0x3020100, 0x16578, 0x3020100, 0x7060504, 0x3020100, 0x16678, 0x3020100, 0x7060504, 0x3020100,
  0x16608, 0x3020100, 0x7060504, 0x3020100, 0x16708, 0x3020100, 0x7060504, 0x3020100, 0x16618, 0x3020100, 0x7060504, 0x3020100, 0x16718, 0x3020100, 0x7060504, 0x3020100,
  0x16720, 0x1000100, 0x5040302, 0x5040100, 0x16820, 0x1000100, 0x5040302, 0x5040100, 0x16730, 0x1000100, 0x5040302, 0x5040100, 0x16830, 0x1000100, 0x5040302, 0x5040100,
  0x16740, 0x1000100, 0x5040302, 0x5040100, 0x16840, 0x1000100, 0x5040302, 0x5040100, 0x16750, 0x1000100, 0x5040302, 0x5040100, 0x16850, 0x1000100, 0x5040302, 0x5040100,
  0x16760, 0x1000100, 0x5040302, 0x5040100, 0x16860, 0x1000100, 0x5040302, 0x5040100, 0x16770, 0x1000100, 0x5040302, 0x5040100, 0x16870, 0x1000100, 0x5040302, 0x5040100,
  0x16800, 0x1000100, 0x5040302, 0x5040100, 0x16900, 0x1000100, 0x5040302, 0x5040100, 0x16810, 0x1000100, 0x5040302, 0x5040100, 0x16910, 0x1000100, 0x5040302, 0x5040100,
  0x16920, 0x5040504, 0xc0c0504, 0x3020100, 0x16cc8, 0x3020100, 0x7060504, 0x3020100, 0x16cd0, 0x3020100, 0x7060504, 0x3020100, 0x16cd8, 0x3020100, 0x7060504, 0x3020100,
  0x16ce0, 0x3020100, 0x7060504, 0x3020100, 0x16ce8, 0x3020100, 0x7060504, 0x3020100, 0x16cf0, 0x3020100, 0x7060504, 0x3020100, 0x16cf8, 0x3020100, 0x7060504, 0x3020100,
  0x16d00, 0x3020100, 0x7060504, 0x3020100, 0x16d08, 0x3020100, 0x7060504, 0x3020100, 0x16d10, 0x3020100, 0x7060504, 0x3020100, 0x16d18, 0x3020100, 0x7060504, 0x3020100,
  0x16d20, 0x3020100, 0x7060504, 0x3020100, 0x16d28, 0x3020100, 0x7060504, 0x3020100, 0x16d30, 0x3020100, 0x7060504, 0x3020100, 0x16d38, 0x3020100, 0x7060504, 0x3020100,
  0x16d5e, 0x16d60, 0x16d62, 0x16d64, 0x16e5e, 0x16e60, 0x16e62, 0x16e64, 0x16d6e, 0x16d70, 0x16d72, 0x16d74, 0x16e6e, 0x16e70, 0x16e72, 0x16e74,
  0x16d7e, 0x16d80, 0x16d82, 0x16d84, 0x16e7e, 0x16e80, 0x16e82, 0x16e84, 0x16d8e, 0x16d90, 0x16d92, 0x16d94, 0x16e8e, 0x16e90, 0x16e92, 0x16e94,
  0x16d9e, 0x16da0, 0x16da2, 0x16da4, 0x16e9e, 0x16ea0, 0x16ea2, 0x16ea4, 0x16dae, 0x16db0, 0x16db2, 0x16db4, 0x16eae, 0x16eb0, 0x16eb2, 0x16eb4,
  0x16e3e, 0x16e40, 0x16e42, 0x16e44, 0x16f3e, 0x16f40, 0x16f42, 0x16f44, 0x16e4e, 0x16e50, 0x16e52, 0x16e54, 0x16f4e, 0x16f50, 0x16f52, 0x16f54,
  0x16d66, 0x16d68, 0x16d6a, 0x16d6c, 0x16e66, 0x16e68, 0x16e6a, 0x16e6c, 0x16d76, 0x16d78, 0x16d7a, 0x16d7c, 0x16e76, 0x16e78, 0x16e7a, 0x16e7c,
  0x16d86, 0x16d88, 0x16d8a, 0x16d8c, 0x16e86, 0x16e88, 0x16e8a, 0x16e8c, 0x16d96, 0x16d98, 0x16d9a, 0x16d9c, 0x16e96, 0x16e98, 0x16e9a, 0x16e9c,
  0x16da6, 0x16da8, 0x16daa, 0x16dac, 0x16ea6, 0x16ea8, 0x16eaa, 0x16eac, 0x16db6, 0x16db8, 0x16dba, 0x16dbc, 0x16eb6, 0x16eb8, 0x16eba, 0x16ebc,
  0x16e46, 0x16e48, 0x16e4a, 0x16e4c, 0x16f46, 0x16f48, 0x16f4a, 0x16f4c, 0x16e56, 0x16e58, 0x16e5a, 0x16e5c, 0x16f56, 0x16f58, 0x16f5a, 0x16f5c,
  0x16cc0, 0x16728, 0x1672a, 0x1672c, 0x1681e, 0x16828, 0x1682a, 0x1682c, 0x1672e, 0x16738, 0x1673a, 0x1673c, 0x1682e, 0x16838, 0x1683a, 0x1683c,
  0x1673e, 0x16748, 0x1674a, 0x1674c, 0x1683e, 0x16848, 0x1684a, 0x1684c, 0x1674e, 0x16758, 0x1675a, 0x1675c, 0x1684e, 0x16858, 0x1685a, 0x1685c,
  0x1675e, 0x16768, 0x1676a, 0x1676c, 0x1685e, 0x16868, 0x1686a, 0x1686c, 0x1676e, 0x16778, 0x1677a, 0x1677c, 0x1686e, 0x16878, 0x1687a, 0x1687c,
  0x167fe, 0x16808, 0x1680a, 0x1680c, 0x168fe, 0x16908, 0x1690a, 0x1690c, 0x1680e, 0x16818, 0x1681a, 0x1681c, 0x1690e, 0x16918, 0x1691a, 0x1691c,
  0x169c0, 0x16a00, 0x1691e, 0x16afe, 0x169e0, 0x16a20, 0x16ade, 0x16b1e, 0x169c2, 0x16a02, 0x16ac0, 0x16b00, 0x169e2, 0x16a22, 0x16ae0, 0x16b20,
  0x169c4, 0x16a04, 0x16ac2, 0x16b02, 0x169e4, 0x16a24, 0x16ae2, 0x16b22, 0x169c6, 0x16a06, 0x16ac4, 0x16b04, 0x169e6, 0x16a26, 0x16ae4, 0x16b24,
  0x169c8, 0x16a08, 0x16ac6, 0x16b06, 0x169e8, 0x16a28, 0x16ae6, 0x16b26, 0x169ca, 0x16a0a, 0x16ac8, 0x16b08, 0x169ea, 0x16a2a, 0x16ae8, 0x16b28,
  0x169dc, 0x16a1c, 0x16ada, 0x16b1a, 0x169fc, 0x16a3c, 0x16afa, 0x16b3a, 0x169de, 0x16a1e, 0x16adc, 0x16b1c, 0x169fe, 0x16a3e, 0x16afc, 0x16b3c,
  0x16b3e, 0x16bfe, 0x16c3e, 0x16c7e, 0x16bde, 0x16c1e, 0x16c5e, 0x16c9e, 0x16bc0, 0x16c00, 0x16c40, 0x16c80, 0x16be0, 0x16c20, 0x16c60, 0x16ca0,
  0x16bc2, 0x16c02, 0x16c42, 0x16c82, 0x16be2, 0x16c22, 0x16c62, 0x16ca2, 0x16bc4, 0x16c04, 0x16c44, 0x16c84, 0x16be4, 0x16c24, 0x16c64, 0x16ca4,
  0x16bc6, 0x16c06, 0x16c46, 0x16c86, 0x16be6, 0x16c26, 0x16c66, 0x16ca6, 0x16bc8, 0x16c08, 0x16c48, 0x16c88, 0x16be8, 0x16c28, 0x16c68, 0x16ca8,
  0x16bda, 0x16c1a, 0x16c5a, 0x16c9a, 0x16bfa, 0x16c3a, 0x16c7a, 0x16cba, 0x16bdc, 0x16c1c, 0x16c5c, 0x16c9c, 0x16bfc, 0x16c3c, 0x16c7c, 0x16cbc,
  0x16cbe, 0x16cc0, 0x16cc0, 0x16cc0, 0x16cc8, 0x16cc8, 0x16cc8, 0x16cc8, 0x16cd0, 0x16cd0, 0x16cd0, 0x16cd0, 0x16cd8, 0x16cd8, 0x16cd8, 0x16cd8,
  0x16ce0, 0x16ce0, 0x16ce0, 0x16ce0, 0x16ce8, 0x16ce8, 0x16ce8, 0x16ce8, 0x16cf0, 0x16cf0, 0x16cf0, 0x16cf0, 0x16cf8, 0x16cf8, 0x16cf8, 0x16cf8,
  0x16d00, 0x16d00, 0x16d00, 0x16d00, 0x16d08, 0x16d08, 0x16d08, 0x16d08, 0x16d10, 0x16d10, 0x16d10, 0x16d10, 0x16d18, 0x16d18, 0x16d18, 0x16d18,
  0x16d20, 0x16d20, 0x16d20, 0x16d20, 0x16d28, 0x16d28, 0x16d28, 0x16d28, 0x16d30, 0x16d30, 0x16d30, 0x16d30, 0x16d38, 0x16d38, 0x16d38, 0x16d38,
  0x16cc0, 0x16cc0, 0x16cc0, 0x16cc0, 0x16cc8, 0x16cc8, 0x16cc8, 0x16cc8, 0x16cd0, 0x16cd0, 0x16cd0, 0x16cd0, 0x16cd8, 0x16cd8, 0x16cd8, 0x16cd8,
  0x16ce0, 0x16ce0, 0x16ce0, 0x16ce0, 0x16ce8, 0x16ce8, 0x16ce8, 0x16ce8, 0x16cf0, 0x16cf0, 0x16cf0, 0x16cf0, 0x16cf8, 0x16cf8, 0x16cf8, 0x16cf8,
  0x16d00, 0x16d00, 0x16d00, 0x16d00, 0x16d08, 0x16d08, 0x16d08, 0x16d08, 0x16d10, 0x16d10, 0x16d10, 0x16d10, 0x16d18, 0x16d18, 0x16d18, 0x16d18,
  0x16d20, 0x16d20, 0x16d20, 0x16d20, 0x16d28, 0x16d28, 0x16d28, 0x16d28, 0x16d30, 0x16d30, 0x16d30, 0x16d30, 0x16d38, 0x16d38, 0x16d38, 0x16d38,
  0x16cc0, 0x16cc0, 0x16cc0, 0x16cc0, 0x16cc8, 0x16cc8, 0x16cc8, 0x16cc8, 0x16cd0, 0x16cd0, 0x16cd0, 0x16cd0, 0x16cd8, 0x16cd8, 0x16cd8, 0x16cd8,
  0x16ce0, 0x16ce0, 0x16ce0, 0x16ce0, 0x16ce8, 0x16ce8, 0x16ce8, 0x16ce8, 0x16cf0, 0x16cf0, 0x16cf0, 0x16cf0, 0x16cf8, 0x16cf8, 0x16cf8, 0x16cf8,
  0x16d00, 0x16d00, 0x16d00, 0x16d00, 0x16d08, 0x16d08, 0x16d08, 0x16d08, 0x16d10, 0x16d10, 0x16d10, 0x16d10, 0x16d18, 0x16d18, 0x16d18, 0x16d18,
  0x16d20, 0x16d20, 0x16d20, 0x16d20, 0x16d28, 0x16d28, 0x16d28, 0x16d28, 0x16d30, 0x16d30, 0x16d30, 0x16d30, 0x16d38, 0x16d38, 0x16d38, 0x16d38,
  0x16cc0, 0x16cc0, 0x3020100, 0x16500, 0x16cc8, 0x16cc8, 0x3020100, 0x16500, 0x16cd0, 0x16cd0, 0x3020100, 0x16500, 0x16cd8, 0x16cd8, 0x3020100, 0x16500,
  0x16ce0, 0x16ce0, 0x3020100, 0x16500, 0x16ce8, 0x16ce8, 0x3020100, 0x16500, 0x16cf0, 0x16cf0, 0x3020100, 0x16500, 0x16cf8, 0x16cf8, 0x3020100, 0x16500,
  0x16d00, 0x16d00, 0x3020100, 0x16500, 0x16d08, 0x16d08, 0x3020100, 0x16500, 0x16d10, 0x16d10, 0x3020100, 0x16500, 0x16d18, 0x16d18, 0x3020100, 0x16500,
  0x16d20, 0x16d20, 0x3020100, 0x16500, 0x16d28, 0x16d28, 0x3020100, 0x16500, 0x16d30, 0x16d30, 0x3020100, 0x16500, 0x16d38, 0x16d38, 0x3020100, 0x16500,
  0x16cc0, 0x16cc0, 0x3020100, 0x16500, 0x16cc8, 0x16cc8, 0x3020100, 0x16500, 0x16cd0, 0x16cd0, 0x3020100, 0x16500, 0x16cd8, 0x16cd8, 0x3020100, 0x16500,
  0x16ce0, 0x16ce0, 0x3020100, 0x16500, 0x16ce8, 0x16ce8, 0x3020100, 0x16500, 0x16cf0, 0x16cf0, 0x3020100, 0x16500, 0x16cf8, 0x16cf8, 0x3020100, 0x16500,
  0x16d00, 0x16d00, 0x3020100, 0x16500, 0x16d08, 0x16d08, 0x3020100, 0x16500, 0x16d10, 0x16d10, 0x3020100, 0x16500, 0x16d18, 0x16d18, 0x3020100, 0x16500,
  0x16d20, 0x16d20, 0x3020100, 0x16500, 0x16d28, 0x16d28, 0x3020100, 0x16500, 0x16d30, 0x16d30, 0x3020100, 0x16500, 0x16d38, 0x16d38, 0x3020100, 0x16500,
  0x16f5c, 0x16f64, 0x3020100, 0x16500, 0x1705c, 0x17064, 0x3020100, 0x16500, 0x16f6c, 0x16f74, 0x3020100, 0x16500, 0x1706c, 0x17074, 0x3020100, 0x16500,
  0x16f7c, 0x16f84, 0x3020100, 0x16500, 0x1707c, 0x17084, 0x3020100, 0x16500, 0x16f8c, 0x16f94, 0x3020100, 0x16500, 0x1708c, 0x17094, 0x3020100, 0x16500,
  0x16f9c, 0x16fa4, 0x3020100, 0x16500, 0x1709c, 0x170a4, 0x3020100, 0x16500, 0x16fac, 0x16fb4, 0x3020100, 0x16500, 0x170ac, 0x170b4, 0x3020100, 0x16500,
  0x1703c, 0x17044, 0x3020100, 0x16500, 0x1713c, 0x17144, 0x3020100, 0x16500, 0x1704c, 0x17054, 0x3020100, 0x16500, 0x1714c, 0x17154, 0x3020100, 0x16500,
  0x1715c, 0x16cc0, 0x5040504, 0x16500, 0x16cc8, 0x16cc8, 0x3020100, 0x16500, 0x16cd0, 0x16cd0, 0x3020100, 0x16500, 0x16cd8, 0x16cd8, 0x3020100, 0x16500,
  0x16ce0, 0x16ce0, 0x3020100, 0x16500, 0x16ce8, 0x16ce8, 0x3020100, 0x16500, 0x16cf0, 0x16cf0, 0x3020100, 0x16500, 0x16cf8, 0x16cf8, 0x3020100, 0x16500,
  0x16d00, 0x16d00, 0x3020100, 0x16500, 0x16d08, 0x16d08, 0x3020100, 0x16500, 0x16d10, 0x16d10, 0x3020100, 0x16500, 0x16d18, 0x16d18, 0x3020100, 0x16500,
  0x16d20, 0x16d20, 0x3020100, 0x16500, 0x16d28, 0x16d28, 0x3020100, 0x16500, 0x16d30, 0x16d30, 0x3020100, 0x16500, 0x16d38, 0x16d38, 0x3020100, 0x16500,
  0x16580, 0x3020100, 0x7060504, 0x3020100, 0x16680, 0x3020100, 0x7060504, 0x3020100, 0x16590, 0x3020100, 0x7060504, 0x3020100, 0x16690, 0x3020100, 0x7060504, 0x3020100,
  0x165a0, 0x3020100, 0x7060504, 0x3020100, 0x166a0, 0x3020100, 0x7060504, 0x3020100, 0x165b0, 0x3020100, 0x7060504, 0x3020100, 0x166b0, 0x3020100, 0x7060504, 0x3020100,
  0x165c0, 0x3020100, 0x7060504, 0x3020100, 0x166c0, 0x3020100, 0x7060504, 0x3020100, 0x165d0, 0x3020100, 0x7060504, 0x3020100, 0x166d0, 0x3020100, 0x7060504, 0x3020100,
  0x165e0, 0x3020100, 0x7060504, 0x3020100, 0x166e0, 0x3020100, 0x7060504, 0x3020100, 0x165f0, 0x3020100, 0x7060504, 0x3020100, 0x166f0, 0x3020100, 0x7060504, 0x3020100,
  0x16588, 0x3020100, 0x7060504, 0x3020100, 0x16688, 0x3020100, 0x7060504, 0x3020100, 0x16598, 0x3020100, 0x7060504, 0x3020100, 0x16698, 0x3020100, 0x7060504, 0x3020100,
  0x165a8, 0x3020100, 0x7060504, 0x3020100, 0x166a8, 0x3020100, 0x7060504, 0x3020100, 0x165b8, 0x3020100, 0x7060504, 0x3020100, 0x166b8, 0x3020100, 0x7060504, 0x3020100,
  0x165c8, 0x3020100, 0x7060504, 0x3020100, 0x166c8, 0x3020100, 0x7060504, 0x3020100, 0x165d8, 0x3020100, 0x7060504, 0x3020100, 0x166d8, 0x3020100, 0x7060504, 0x3020100,
  0x165e8, 0x3020100, 0x7060504, 0x3020100, 0x166e8, 0x3020100, 0x7060504, 0x3020100, 0x165f8, 0x3020100, 0x7060504, 0x3020100, 0x166f8, 0x3020100, 0x7060504, 0x3020100,
  0x16780, 0x1000100, 0x5040302, 0x5040100, 0x16880, 0x1000100, 0x5040302, 0x5040100, 0x16790, 0x1000100, 0x5040302, 0x5040100, 0x16890, 0x1000100, 0x5040302, 0x5040100,
  0x167a0, 0x1000100, 0x5040302, 0x5040100, 0x168a0, 0x1000100, 0x5040302, 0x5040100, 0x167b0, 0x1000100, 0x5040302, 0x5040100, 0x168b0, 0x1000100, 0x5040302, 0x5040100,
  0x167c0, 0x1000100, 0x5040302, 0x5040100, 0x168c0, 0x1000100, 0x5040302, 0x5040100, 0x167d0, 0x1000100, 0x5040302, 0x5040100, 0x168d0, 0x1000100, 0x5040302, 0x5040100,
  0x167e0, 0x1000100, 0x5040302, 0x5040100, 0x168e0, 0x1000100, 0x5040302, 0x5040100, 0x167f0, 0x1000100, 0x5040302, 0x5040100, 0x168f0, 0x1000100, 0x5040302, 0x5040100,
  0x16cc0, 0x3020100, 0x7060504, 0x3020100, 0x16cc8, 0x3020100, 0x7060504, 0x3020100, 0x16cd0, 0x3020100, 0x7060504, 0x3020100, 0x16cd8, 0x3020100, 0x7060504, 0x3020100,
  0x16ce0, 0x3020100, 0x7060504, 0x3020100, 0x16ce8, 0x3020100, 0x7060504, 0x3020100, 0x16cf0, 0x3020100, 0x7060504, 0x3020100, 0x16cf8, 0x3020100, 0x7060504, 0x3020100,
  0x16d00, 0x3020100, 0x7060504, 0x3020100, 0x16d08, 0x3020100, 0x7060504, 0x3020100, 0x16d10, 0x3020100, 0x7060504, 0x3020100, 0x16d18, 0x3020100, 0x7060504, 0x3020100,
  0x16d20, 0x3020100, 0x7060504, 0x3020100, 0x16d28, 0x3020100, 0x7060504, 0x3020100, 0x16d30, 0x3020100, 0x7060504, 0x3020100, 0x16d38, 0x3020100, 0x7060504, 0x3020100,
  0x16dbe, 0x16dc0, 0x16dc2, 0x16dc4, 0x16ebe, 0x16ec0, 0x16ec2, 0x16ec4, 0x16dce, 0x16dd0, 0x16dd2, 0x16dd4, 0x16ece, 0x16ed0, 0x16ed2, 0x16ed4,
  0x16dde, 0x16de0, 0x16de2, 0x16de4, 0x16ede, 0x16ee0, 0x16ee2, 0x16ee4, 0x16dee, 0x16df0, 0x16df2, 0x16df4, 0x16eee, 0x16ef0, 0x16ef2, 0x16ef4,
  0x16dfe, 0x16e00, 0x16e02, 0x16e04, 0x16efe, 0x16f00, 0x16f02, 0x16f04, 0x16e0e, 0x16e10, 0x16e12, 0x16e14, 0x16f0e, 0x16f10, 0x16f12, 0x16f14,
  0x16e1e, 0x16e20, 0x16e22, 0x16e24, 0x16f1e, 0x16f20, 0x16f22, 0x16f24, 0x16e2e, 0x16e30, 0x16e32, 0x16e34, 0x16f2e, 0x16f30, 0x16f32, 0x16f34,
  0x16dc6, 0x16dc8, 0x16dca, 0x16dcc, 0x16ec6, 0x16ec8, 0x16eca, 0x16ecc, 0x16dd6, 0x16dd8, 0x16dda, 0x16ddc, 0x16ed6, 0x16ed8, 0x16eda, 0x16edc,
  0x16de6, 0x16de8, 0x16dea, 0x16dec, 0x16ee6, 0x16ee8, 0x16eea, 0x16eec, 0x16df6, 0x16df8, 0x16dfa, 0x16dfc, 0x16ef6, 0x16ef8, 0x16efa, 0x16efc,
  0x16e06, 0x16e08, 0x16e0a, 0x16e0c, 0x16f06, 0x16f08, 0x16f0a, 0x16f0c, 0x16e16, 0x16e18, 0x16e1a, 0x16e1c, 0x16f16, 0x16f18, 0x16f1a, 0x16f1c,
  0x16e26, 0x16e28, 0x16e2a, 0x16e2c, 0x16f26, 0x16f28, 0x16f2a, 0x16f2c, 0x16e36, 0x16e38, 0x16e3a, 0x16e3c, 0x16f36, 0x16f38, 0x16f3a, 0x16f3c,
  0x1677e, 0x16788, 0x1678a, 0x1678c, 0x1687e, 0x16888, 0x1688a, 0x1688c, 0x1678e, 0x16798, 0x1679a, 0x1679c, 0x1688e, 0x16898, 0x1689a, 0x1689c,
  0x1679e, 0x167a8, 0x167aa, 0x167ac, 0x1689e, 0x168a8, 0x168aa, 0x168ac, 0x167ae, 0x167b8, 0x167ba, 0x167bc, 0x168ae, 0x168b8, 0x168ba, 0x168bc,
  0x167be, 0x167c8, 0x167ca, 0x167cc, 0x168be, 0x168c8, 0x168ca, 0x168cc, 0x167ce, 0x167d8, 0x167da, 0x167dc, 0x168ce, 0x168d8, 0x168da, 0x168dc,
  0x167de, 0x167e8, 0x167ea, 0x167ec, 0x168de, 0x168e8, 0x168ea, 0x168ec, 0x167ee, 0x167f8, 0x167fa, 0x167fc, 0x168ee, 0x168f8, 0x168fa, 0x168fc,
  0x169cc, 0x16a0c, 0x16aca, 0x16b0a, 0x169ec, 0x16a2c, 0x16aea, 0x16b2a, 0x169ce, 0x16a0e, 0x16acc, 0x16b0c, 0x169ee, 0x16a2e, 0x16aec, 0x16b2c,
  0x169d0, 0x16a10, 0x16ace, 0x16b0e, 0x169f0, 0x16a30, 0x16aee, 0x16b2e, 0x169d2, 0x16a12, 0x16ad0, 0x16b10, 0x169f2, 0x16a32, 0x16af0, 0x16b30,
  0x169d4, 0x16a14, 0x16ad2, 0x16b12, 0x169f4, 0x16a34, 0x16af2, 0x16b32, 0x169d6, 0x16a16, 0x16ad4, 0x16b14, 0x169f6, 0x16a36, 0x16af4, 0x16b34,
  0x169d8, 0x16a18, 0x16ad6, 0x16b16, 0x169f8, 0x16a38, 0x16af6, 0x16b36, 0x169da, 0x16a1a, 0x16ad8, 0x16b18, 0x169fa, 0x16a3a, 0x16af8, 0x16b38,
  0x16bca, 0x16c0a, 0x16c4a, 0x16c8a, 0x16bea, 0x16c2a, 0x16c6a, 0x16caa, 0x16bcc, 0x16c0c, 0x16c4c, 0x16c8c, 0x16bec, 0x16c2c, 0x16c6c, 0x16cac,
  0x16bce, 0x16c0e, 0x16c4e, 0x16c8e, 0x16bee, 0x16c2e, 0x16c6e, 0x16cae, 0x16bd0, 0x16c10, 0x16c50, 0x16c90, 0x16bf0, 0x16c30, 0x16c70, 0x16cb0,
  0x16bd2, 0x16c12, 0x16c52, 0x16c92, 0x16bf2, 0x16c32, 0x16c72, 0x16cb2, 0x16bd4, 0x16c14, 0x16c54, 0x16c94, 0x16bf4, 0x16c34, 0x16c74, 0x16cb4,
  0x16bd6, 0x16c16, 0x16c56, 0x16c96, 0x16bf6, 0x16c36, 0x16c76, 0x16cb6, 0x16bd8, 0x16c18, 0x16c58, 0x16c98, 0x16bf8, 0x16c38, 0x16c78, 0x16cb8,
  0x16cc0, 0x16cc0, 0x16cc0, 0x16cc0, 0x16cc8, 0x16cc8, 0x16cc8, 0x16cc8, 0x16cd0, 0x16cd0, 0x16cd0, 0x16cd0, 0x16cd8, 0x16cd8, 0x16cd8, 0x16cd8,
  0x16ce0, 0x16ce0, 0x16ce0, 0x16ce0, 0x16ce8, 0x16ce8, 0x16ce8, 0x16ce8, 0x16cf0, 0x16cf0, 0x16cf0, 0x16cf0, 0x16cf8, 0x16cf8, 0x16cf8, 0x16cf8,
  0x16d00, 0x16d00, 0x16d00, 0x16d00, 0x16d08, 0x16d08, 0x16d08, 0x16d08, 0x16d10, 0x16d10, 0x16d10, 0x16d10, 0x16d18, 0x16d18, 0x16d18, 0x16d18,
  0x16d20, 0x16d20, 0x16d20, 0x16d20, 0x16d28, 0x16d28, 0x16d28, 0x16d28, 0x16d30, 0x16d30, 0x16d30, 0x16d30, 0x16d38, 0x16d38, 0x16d38, 0x16d38,
  0x16cc0, 0x16cc0, 0x16cc0, 0x16cc0, 0x16cc8, 0x16cc8, 0x16cc8, 0x16cc8, 0x16cd0, 0x16cd0, 0x16cd0, 0x16cd0, 0x16cd8, 0x16cd8, 0x16cd8, 0x16cd8,
  0x16ce0, 0x16ce0, 0x16ce0, 0x16ce0, 0x16ce8, 0x16ce8, 0x16ce8, 0x16ce8, 0x16cf0, 0x16cf0, 0x16cf0, 0x16cf0, 0x16cf8, 0x16cf8, 0x16cf8, 0x16cf8,
  0x16d00, 0x16d00, 0x16d00, 0x16d00, 0x16d08, 0x16d08, 0x16d08, 0x16d08, 0x16d10, 0x16d10, 0x16d10, 0x16d10, 0x16d18, 0x16d18, 0x16d18, 0x16d18,
  0x16d20, 0x16d20, 0x16d20, 0x16d20, 0x16d28, 0x16d28, 0x16d28, 0x16d28, 0x16d30, 0x16d30, 0x16d30, 0x16d30, 0x16d38, 0x16d38, 0x16d38, 0x16d38,
  0x16cc0, 0x16cc0, 0x16cc0, 0x16cc0, 0x16cc8, 0x16cc8, 0x16cc8, 0x16cc8, 0x16cd0, 0x16cd0, 0x16cd0, 0x16cd0, 0x16cd8, 0x16cd8, 0x16cd8, 0x16cd8,
  0x16ce0, 0x16ce0, 0x16ce0, 0x16ce0, 0x16ce8, 0x16ce8, 0x16ce8, 0x16ce8, 0x16cf0, 0x16cf0, 0x16cf0, 0x16cf0, 0x16cf8, 0x16cf8, 0x16cf8, 0x16cf8,
  0x16d00, 0x16d00, 0x16d00, 0x16d00, 0x16d08, 0x16d08, 0x16d08, 0x16d08, 0x16d10, 0x16d10, 0x16d10, 0x16d10, 0x16d18, 0x16d18, 0x16d18, 0x16d18,
  0x16d20, 0x16d20, 0x16d20, 0x16d20, 0x16d28, 0x16d28, 0x16d28, 0x16d28, 0x16d30, 0x16d30, 0x16d30, 0x16d30, 0x16d38, 0x16d38, 0x16d38, 0x16d38,
  0x16cc0, 0x16cc0, 0x3020100, 0x16500, 0x16cc8, 0x16cc8, 0x3020100, 0x16500, 0x16cd0, 0x16cd0, 0x3020100, 0x16500, 0x16cd8, 0x16cd8, 0x3020100, 0x16500,
  0x16ce0, 0x16ce0, 0x3020100, 0x16500, 0x16ce8, 0x16ce8, 0x3020100, 0x16500, 0x16cf0, 0x16cf0, 0x3020100, 0x16500, 0x16cf8, 0x16cf8, 0x3020100, 0x16500,
  0x16d00, 0x16d00, 0x3020100, 0x16500, 0x16d08, 0x16d08, 0x3020100, 0x16500, 0x16d10, 0x16d10, 0x3020100, 0x16500, 0x16d18, 0x16d18, 0x3020100, 0x16500,
  0x16d20, 0x16d20, 0x3020100, 0x16500, 0x16d28, 0x16d28, 0x3020100, 0x16500, 0x16d30, 0x16d30, 0x3020100, 0x16500, 0x16d38, 0x16d38, 0x3020100, 0x16500,
  0x16cc0, 0x16cc0, 0x3020100, 0x16500, 0x16cc8, 0x16cc8, 0x3020100, 0x16500, 0x16cd0, 0x16cd0, 0x3020100, 0x16500, 0x16cd8, 0x16cd8, 0x3020100, 0x16500,
  0x16ce0, 0x16ce0, 0x3020100, 0x16500, 0x16ce8, 0x16ce8, 0x3020100, 0x16500, 0x16cf0, 0x16cf0, 0x3020100, 0x16500, 0x16cf8, 0x16cf8, 0x3020100, 0x16500,
  0x16d00, 0x16d00, 0x3020100, 0x16500, 0x16d08, 0x16d08, 0x3020100, 0x16500, 0x16d10, 0x16d10, 0x3020100, 0x16500, 0x16d18, 0x16d18, 0x3020100, 0x16500,
  0x16d20, 0x16d20, 0x3020100, 0x16500, 0x16d28, 0x16d28, 0x3020100, 0x16500, 0x16d30, 0x16d30, 0x3020100, 0x16500, 0x16d38, 0x16d38, 0x3020100, 0x16500,
  0x16fbc, 0x16fc4, 0x3020100, 0x16500, 0x170bc, 0x170c4, 0x3020100, 0x16500, 0x16fcc, 0x16fd4, 0x3020100, 0x16500, 0x170cc, 0x170d4, 0x3020100, 0x16500,
  0x16fdc, 0x16fe4, 0x3020100, 0x16500, 0x170dc, 0x170e4, 0x3020100, 0x16500, 0x16fec, 0x16ff4, 0x3020100, 0x16500, 0x170ec, 0x170f4, 0x3020100, 0x16500,
  0x16ffc, 0x17004, 0x3020100, 0x16500, 0x170fc, 0x17104, 0x3020100, 0x16500, 0x1700c, 0x17014, 0x3020100, 0x16500, 0x1710c, 0x17114, 0x3020100, 0x16500,
  0x1701c, 0x17024, 0x3020100, 0x16500, 0x1711c, 0x17124, 0x3020100, 0x16500, 0x1702c, 0x17034, 0x3020100, 0x16500, 0x1712c, 0x17134, 0x3020100, 0x16500,
  0x16cc0, 0x16cc0, 0x3020100, 0x16500, 0x16cc8, 0x16cc8, 0x3020100, 0x16500, 0x16cd0, 0x16cd0, 0x3020100, 0x16500, 0x16cd8, 0x16cd8, 0x3020100, 0x16500,
  0x16ce0, 0x16ce0, 0x3020100, 0x16500, 0x16ce8, 0x16ce8, 0x3020100, 0x16500, 0x16cf0, 0x16cf0, 0x3020100, 0x16500, 0x16cf8, 0x16cf8, 0x3020100, 0x16500,
  0x16d00, 0x16d00, 0x3020100, 0x16500, 0x16d08, 0x16d08, 0x3020100, 0x16500, 0x16d10, 0x16d10, 0x3020100, 0x16500, 0x16d18, 0x16d18, 0x3020100, 0x16500,
  0x16d20, 0x16d20, 0x3020100, 0x16500, 0x16d28, 0x16d28, 0x3020100, 0x16500, 0x16d30, 0x16d30, 0x3020100, 0x16500, 0x16d38, 0x16d38, 0x3020100, 0x16500,
  0x0, 0x0, 0x0, 0x0, 0x0, 0x0, 0x0, 0x0, 0x0, 0x0, 0x0, 0x0, 0x0, 0x0, 0x0, 0x0,
  0x0, 0x0, 0x0, 0x0, 0x0, 0x0, 0x0, 0x0, 0x0, 0x0, 0x0, 0x0, 0x0, 0x0, 0x0, 0x0,
  0x0, 0x0, 0x0, 0x0, 0x0, 0x0, 0x0, 0x0, 0x0, 0x0, 0x0, 0x0, 0x0, 0x0, 0x0, 0x0,
  0x0, 0x0, 0x0, 0x0, 0x0, 0x0, 0x0, 0x0, 0x0, 0x0, 0x0, 0x0, 0x0, 0x0, 0x0, 0x0,
  0x0, 0x0, 0x0, 0x0, 0x0, 0x0, 0x0, 0x0, 0x0, 0x0, 0x0, 0x0, 0x0, 0x0, 0x0, 0x0,
  0x0, 0x0, 0x0, 0x0, 0x0, 0x0, 0x0, 0x0, 0x0, 0x0, 0x0, 0x0, 0x0, 0x0, 0x0, 0x0,
  0x0, 0x0, 0x0, 0x0, 0x0, 0x0, 0x0, 0x0, 0x0, 0x0, 0x0, 0x0, 0x0, 0x0, 0x0, 0x0,
  0x0, 0x0, 0x0, 0x0, 0x0, 0x0, 0x0, 0x0, 0x0, 0x0, 0x0, 0x0, 0x0, 0x0, 0x0, 0x0,
  0x0, 0x0, 0x0, 0x0, 0x0, 0x0, 0x0, 0x0, 0x0, 0x0, 0x0, 0x0, 0x0, 0x0, 0x0, 0x0,
  0x0, 0x0, 0x0, 0x0, 0x0, 0x0, 0x0, 0x0, 0x0, 0x0, 0x0, 0x0, 0x0, 0x0, 0x0, 0x0,
  0x0, 0x0, 0x0, 0x0, 0x0, 0x0, 0x0, 0x0, 0x0, 0x0, 0x0, 0x0, 0x0, 0x0, 0x0, 0x0,
  0x0, 0x0, 0x0, 0x0, 0x0, 0x0, 0x0, 0x0, 0x0, 0x0, 0x0, 0x0, 0x0, 0x0, 0x0, 0x0,
  0x0, 0x0, 0x0, 0x0, 0x0, 0x0, 0x0, 0x0, 0x0, 0x0, 0x0, 0x0, 0x0, 0x0, 0x0, 0x0,
  0x0, 0x0, 0x0, 0x0, 0x0, 0x0, 0x0, 0x0, 0x0, 0x0, 0x0, 0x0, 0x0, 0x0, 0x0, 0x0,
  0x0, 0x0, 0x0, 0x0, 0x0, 0x0, 0x0, 0x0, 0x0, 0x0, 0x0, 0x0, 0x0, 0x0, 0x0, 0x0,
  0x0, 0x0, 0x0, 0x0, 0x0, 0x0, 0x0, 0x0, 0x0, 0x0, 0x0, 0x0, 0x0, 0x0, 0x0, 0x0
};

__global__ void __launch_bounds__(1024) mega(Args) {
  asm volatile(
    "s_load_dwordx16 s[4:19], s[0:1], 0\n"
    "s_load_dwordx16 s[20:35], s[0:1], 64\n"
    "v_readfirstlane_b32 s37, v0\n"
    "s_getpc_b64 s[40:41]\n"
    "K0:\n"
    "s_lshr_b32 s37, s37, 6\n"
    "s_cmpk_ge_u32 s2, 192\n"
    "s_cselect_b32 s42, 16, 0\n"
    "s_add_u32 s42, s42, s37\n"
    "s_lshl2_add_u32 s40, s42, s40\n"
    "s_add_u32 s40, s40, TBL1-K0\n"
    "s_mov_b32 s39, 0x100000\n"
    "s_setpc_b64 s[40:41]\n"
    "TBL1:\n"
    "s_branch GA0\n"
    "s_branch GA0\n"
    "s_branch GA0\n"
    "s_branch GA0\n"
    "s_branch GA0\n"
    "s_branch GA0\n"
    "s_branch S0\n"
    "s_branch S0\n"
    "s_branch S0\n"
    "s_branch S0\n"
    "s_branch S0\n"
    "s_branch S0\n"
    "s_branch MD0\n"
    "s_branch AUX0\n"
    "s_branch AUX0\n"
    "s_branch AUX0\n"
    "s_branch AUX0\n"
    "s_branch AUX0\n"
    "s_branch AUX0\n"
    "s_branch AUX0\n"
    "s_branch SIL0\n"
    "s_branch SIL0\n"
    "s_branch S0\n"
    "s_branch S0\n"
    "s_branch S0\n"
    "s_branch S0\n"
    "s_branch S0\n"
    "s_branch S0\n"
    "s_branch SIL0\n"
    "s_branch SIL0\n"
    "s_branch SIL0\n"
    "s_branch MD0\n"
    "TBL2:\n"
    "s_branch PAD0\n"
    "s_branch PAD1\n"
    "s_branch PAD2\n"
    "s_branch PAD3\n"
    "s_branch PAD4\n"
    "s_branch PAD5\n"
    "s_branch PAD6\n"
    "s_branch PAD7\n"
    "s_branch PAD8\n"
    "s_branch PAD9\n"
    "s_branch PAD10\n"
    "s_branch PAD11\n"
    "s_branch PAD12\n"
    "s_branch PAD13\n"
    "s_branch PAD14\n"
    "s_branch PAD15\n"
    "s_branch PAD16\n"
    "s_branch PAD17\n"
    "s_branch PAD18\n"
    "s_branch PAD19\n"
    "s_branch PAD20\n"
    "s_branch PAD21\n"
    "s_branch S1\n"
    "s_branch S1\n"
    "s_branch S1\n"
    "s_branch S1\n"
    "s_branch S1\n"
    "s_branch S1\n"
    "s_branch PAD28\n"
    "s_branch PAD29\n"
    "s_branch PAD30\n"
    "s_branch MD1\n"
    "AUX0:\n"
    "v_and_b32 v1, 31, v0\n"
    "v_lshlrev_b32 v1, 4, v1\n"
    "v_mov_b32 v4, 0\n"
    "v_mov_b32 v5, 0\n"
    "v_mov_b32 v6, 0\n"
    "v_mov_b32 v7, 0\n"
    "v_add_u32 v1, 147840, v1\n"
    "ds_write_b128 v1, v[4:7]\n"
    "s_lshr_b32 s42, s2, 3\n"
    "s_mul_i32 s42, s42, 7\n"
    "s_add_u32 s42, s42, 7\n"
    "v_and_b32 v1, 7, v0\n"
    "v_add_u32 v1, s42, v1\n"
    "v_min_u32 v1, (END-K0)/128, v1\n"
    "v_lshlrev_b32 v1, 7, v1\n"
    "s_mov_b32 m0, 148480\n"
    "s_mov_b64 exec, 0x7f\n"
    "global_load_lds_dword v1, s[40:41]\n"
    "s_add_u32 s40, s40, 128\n"
    "s_mov_b64 exec, 0\n"
    "s_setpc_b64 s[40:41]\n"
    "S0:\n"
    "s_setprio 3\n"
    "v_and_b32 v2, 3, v0\n"
    "v_lshrrev_b32 v3, 2, v0\n"
    "v_mul_u32_u24 v2, 0x900, v2\n"
    "s_mul_i32 s42, s2, 9216\n"
    "v_mad_u32_u24 v1, v3, 12, v2\n"
    "v_add_u32 v1, s42, v1\n"
    "s_waitcnt lgkmcnt(0)\n"
    "global_load_dwordx3 v[4:6], v1, s[4:5] offset:-1152 nt\n"
    "global_load_dwordx3 v[8:10], v1, s[4:5] nt\n"
    "s_load_dword s36, s[16:17], 0\n"
    "s_add_u32 s40, s40, 128\n"
    "s_mov_b64 exec, 0\n"
    "s_setpc_b64 s[40:41]\n"
    "GA0:\n"
    "s_and_b32 s44, s2, 7\n"
    "s_lshr_b32 s45, s2, 3\n"
    "s_mul_i32 s44, s44, 24\n"
    "s_add_u32 s38, s44, s45\n"
    "s_lshr_b32 s42, s37, 1\n"
    "s_mul_i32 s43, s38, 3\n"
    "s_add_u32 s43, s43, s42\n"
    "s_lshl_b32 s44, s43, 8\n"
    "s_lshl_b32 s45, s43, 1\n"
    "s_and_b32 s45, s45, -4\n"
    "v_and_b32 v1, 63, v0\n"
    "v_lshl_add_u32 v2, v1, 2, s44\n"
    "s_waitcnt lgkmcnt(0)\n"
    "global_load_dword v4, v2, s[6:7] nt\n"
    "global_load_dword v5, v2, s[8:9] nt\n"
    "s_memrealtime s[96:97]\n"
    "s_load_dword s46, s[10:11], s45\n"
    "global_load_dword v6, v2, s[20:21] nt\n"
    "global_load_dword v7, v2, s[22:23] nt\n"
    "s_load_dword s36, s[16:17], 0\n"
    "s_add_u32 s40, s40, 128\n"
    "s_mov_b64 exec, 0\n"
    "s_setpc_b64 s[40:41]\n"
    "SIL0:\n"
    "s_cmpk_ge_u32 s37, 12\n"
    "s_cselect_b32 s42, 384, 0\n"
    "s_addk_i32 s42, 256\n"
    "v_subrev_u32 v1, s42, v0\n"
    "v_min_u32 v1, 287, v1\n"
    "v_lshlrev_b32 v1, 2, v1\n"
    "s_waitcnt lgkmcnt(0)\n"
    "global_load_dword v4, v1, s[12:13]\n"
    "s_load_dword s36, s[16:17], 0\n"
    "s_add_u32 s40, s40, 128\n"
    "s_mov_b64 exec, 0\n"
    "s_setpc_b64 s[40:41]\n"
    "MD0:\n"
    "s_setprio 3\n"
    "s_mul_i32 s43, s2, 19\n"
    "s_lshr_b32 s43, s43, 9\n"
    "s_mul_i32 s44, s43, 27\n"
    "s_sub_u32 s44, s2, s44\n"
    "s_mul_i32 s45, s43, 442368\n"
    "s_lshl_b32 s46, s44, 7\n"
    "s_add_u32 s45, s45, s46\n"
    "v_bfe_u32 v1, v0, 3, 3\n"
    "v_and_b32 v2, 2, v1\n"
    "v_mul_u32_u24 v1, 0x1b00, v1\n"
    "v_xor_b32 v2, v2, v0\n"
    "v_and_b32 v2, 7, v2\n"
    "v_lshl_add_u32 v1, v2, 4, v1\n"
    "v_add_u32 v1, s45, v1\n"
    "v_xor_b32 v2, 64, v1\n"
    "v_add_u32 v2, 55296, v2\n"
    "s_lshl_b32 s46, s43, 7\n"
    "v_and_b32 v3, 31, v0\n"
    "v_lshl_add_u32 v5, v3, 2, s46\n"
    "s_mov_b32 m0, 138240\n"
    "s_cmpk_ge_u32 s2, 243\n"
    "s_waitcnt lgkmcnt(0)\n"
    "s_cbranch_scc1 MD0e\n"
    "global_load_dword v4, v5, s[12:13]\n"
    "global_load_lds_dwordx4 v1, s[14:15] nt\n"
    "s_add_u32 m0, m0, 1024\n"
    "v_add_u32 v1, 110592, v1\n"
    "global_load_lds_dwordx4 v2, s[14:15] nt\n"
    "s_add_u32 m0, m0, 1024\n"
    "v_add_u32 v2, 110592, v2\n"
    "global_load_lds_dwordx4 v1, s[14:15] nt\n"
    "s_add_u32 m0, m0, 1024\n"
    "v_add_u32 v1, 110592, v1\n"
    "global_load_lds_dwordx4 v2, s[14:15] nt\n"
    "s_add_u32 m0, m0, 1024\n"
    "v_add_u32 v2, 110592, v2\n"
    "global_load_lds_dwordx4 v1, s[14:15] nt\n"
    "s_add_u32 m0, m0, 1024\n"
    "v_add_u32 v1, 110592, v1\n"
    "global_load_lds_dwordx4 v2, s[14:15] nt\n"
    "s_add_u32 m0, m0, 1024\n"
    "v_add_u32 v2, 110592, v2\n"
    "global_load_lds_dwordx4 v1, s[14:15] nt\n"
    "s_add_u32 m0, m0, 1024\n"
    "s_nop 0\n"
    "global_load_lds_dwordx4 v2, s[14:15] nt\n"
    "MD0e:\n"
    "s_load_dword s36, s[16:17], 0\n"
    "s_add_u32 s40, s40, 128\n"
    "s_mov_b64 exec, 0\n"
    "s_setpc_b64 s[40:41]\n"
    "ABORT:\n"
    "s_endpgm\n"
    "PAD13:\n"
    "s_cbranch_execz AUX1\n"
    "PAD16:\n"
    "s_cbranch_execz M1\n"
    "S1:\n"
    "s_mov_b64 exec, -1\n"
    "s_barrier\n"
    "s_mul_i32 s42, s2, 12416\n"
    "s_add_u32 s42, s42, 7334912\n"
    "v_lshl_add_u32 v11, v0, 4, s42\n"
    "v_bfe_u32 v2, v0, 4, 2\n"
    "v_and_b32 v3, 0x3c3, v0\n"
    "v_and_b32 v12, 3, v0\n"
    "v_mad_u32_u24 v3, v12, 15, v3\n"
    "v_lshl_add_u32 v12, v2, 2, v3\n"
    "v_add_u32 v12, 147072, v12\n"
    "v_and_b32 v13, 31, v0\n"
    "v_lshlrev_b32 v13, 4, v13\n"
    "v_add_u32 v13, 147456, v13\n"
    "v_mov_b32 v14, 147968\n"
    "v_mov_b32 v15, 1\n"
    "s_mov_b32 s43, 0x3f803f80\n"
    "s_movk_i32 s44, 6144\n"
    "s_sub_u32 s47, s37, 8\n"
    "s_cmp_ge_u32 s47, 3\n"
    "s_cselect_b32 s48, 1, 0\n"
    "s_add_u32 s45, s47, 3\n"
    "s_cmp_lt_i32 s47, 0\n"
    "s_cselect_b32 s47, s45, s47\n"
    "s_cmp_eq_u32 s47, 3\n"
    "s_cselect_b32 s47, 0, s47\n"
    "s_mul_i32 s45, s47, 30464\n"
    "v_and_b32 v122, 3, v0\n"
    "v_mul_u32_u24 v122, 1088, v122\n"
    "v_bfe_u32 v123, v0, 2, 4\n"
    "v_lshlrev_b32 v123, 3, v123\n"
    "v_add3_u32 v122, v122, v123, s45\n"
    "s_mul_i32 s45, s48, 4352\n"
    "v_add_u32 v123, s45, v122\n"
    "s_lshl_b32 s45, s48, 12\n"
    "s_add_u32 s45, s45, 107520\n"
    "v_and_b32 v124, 63, v0\n"
    "v_lshl_add_u32 v124, v124, 4, s45\n"
    "s_lshl_b32 s45, s47, 2\n"
    "v_add_u32 v125, s45, v14\n"
    "s_lshl_b32 s45, s45, 1\n"
    "s_add_u32 s45, s45, 148416\n"
    "v_mov_b32 v126, s45\n"
    "s_mul_i32 s49, s47, 3328\n"
    "s_waitcnt vmcnt(1)\n"
    "v_dot2_f32_bf16 v16, v4, s43, 0\n"
    "v_dot2_f32_bf16 v17, v4, v4, 0\n"
    "v_dot2_f32_bf16 v16, v5, s43, v16\n"
    "v_dot2_f32_bf16 v17, v5, v5, v17\n"
    "v_dot2_f32_bf16 v16, v6, s43, v16\n"
    "v_dot2_f32_bf16 v17, v6, v6, v17\n"
    "s_waitcnt vmcnt(0)\n"
    "v_dot2_f32_bf16 v18, v8, s43, 0\n"
    "v_dot2_f32_bf16 v19, v8, v8, 0\n"
    "v_dot2_f32_bf16 v18, v9, s43, v18\n"
    "v_dot2_f32_bf16 v19, v9, v9, v19\n"
    "v_dot2_f32_bf16 v18, v10, s43, v18\n"
    "v_dot2_f32_bf16 v19, v10, v10, v19\n"
    "v_lshlrev_b32 v24, 16, v4\n"
    "v_lshlrev_b32 v25, 16, v8\n"
    "v_and_b32 v26, 0xffff0000, v4\n"
    "v_permlane32_swap_b32 v16, v18\n"
    "v_permlane32_swap_b32 v17, v19\n"
    "v_add_f32 v16, v16, v18\n"
    "v_add_f32 v17, v17, v19\n"
    "v_and_b32 v27, 0xffff0000, v8\n"
    "v_lshlrev_b32 v28, 16, v5\n"
    "v_permlane16_swap_b32 v16, v17\n"
    "v_add_f32 v16, v16, v17\n"
    "v_lshlrev_b32 v29, 16, v9\n"
    "v_and_b32 v30, 0xffff0000, v5\n"
    "v_add_f32_dpp v16, v16, v16 row_ror:8 row_mask:0xf bank_mask:0xf\n"
    "v_and_b32 v31, 0xffff0000, v9\n"
    "v_lshlrev_b32 v32, 16, v6\n"
    "v_add_f32_dpp v16, v16, v16 row_ror:4 row_mask:0xf bank_mask:0xf\n"
    "ds_write_b32 v12, v16\n"
    "s_mov_b64 exec, 1\n"
    "ds_add_u32 v14, v15 offset:0\n"
    "s_mov_b64 exec, -1\n"
    "v_lshlrev_b32 v33, 16, v10\n"
    "v_and_b32 v34, 0xffff0000, v6\n"
    "v_and_b32 v35, 0xffff0000, v10\n"
    "s_branch E_2\n"
    "R_1:\n"
    "s_sub_u32 s39, s39, 1\n"
    "s_cbranch_scc1 ABORT\n"
    "E_2:\n"
    "ds_read_b32 v20, v14 offset:0\n"
    "ds_read_b128 v[16:19], v13\n"
    "s_waitcnt lgkmcnt(0)\n"
    "v_cmp_ne_u32 vcc, 6, v20\n"
    "s_cbranch_vccnz R_1\n"
    "s_add_u32 s36, s36, 1\n"
    "v_permlane16_swap_b32 v16, v18\n"
    "v_permlane16_swap_b32 v17, v19\n"
    "v_add_f32 v16, v16, v18\n"
    "v_add_f32 v17, v17, v19\n"
    "v_mov_b32 v39, s36\n"
    "v_add_f32_dpp v16, v16, v16 row_ror:8 row_mask:0xf bank_mask:0xf\n"
    "v_add_f32_dpp v17, v17, v17 row_ror:8 row_mask:0xf bank_mask:0xf\n"
    "v_mov_b32 v43, s36\n"
    "v_add_f32_dpp v16, v16, v16 row_ror:4 row_mask:0xf bank_mask:0xf\n"
    "v_add_f32_dpp v17, v17, v17 row_ror:4 row_mask:0xf bank_mask:0xf\n"
    "v_mul_f32 v16, 0x3ae38e39, v16\n"
    "v_mul_f32 v17, 0x3ae38e39, v17\n"
    "v_fma_f32 v17, -v16, v16, v17\n"
    "v_max_f32 v17, 0, v17\n"
    "v_cvt_pk_bf16_f32 v17, 0, v17\n"
    "v_add_f32 v17, 0x3727c5ac, v17\n"
    "v_cvt_pk_bf16_f32 v17, 0, v17\n"
    "v_sqrt_f32 v17, v17\n"
    "s_nop 0\n"
    "v_cvt_pk_bf16_f32 v16, v16, v17\n"
    "v_mov_b32 v17, v16\n"
    "s_nop 1\n"
    "v_permlane16_swap_b32 v16, v17\n"
    "v_and_b32 v20, 0xffff0000, v16\n"
    "v_and_b32 v21, 0xffff0000, v17\n"
    "v_lshlrev_b32 v18, 16, v16\n"
    "v_lshlrev_b32 v19, 16, v17\n"
    "v_rcp_f32 v20, v20\n"
    "v_rcp_f32 v21, v21\n"
    "v_pk_add_f32 v[24:25], v[24:25], v[18:19] neg_lo:[0,1] neg_hi:[0,1]\n"
    "v_pk_add_f32 v[26:27], v[26:27], v[18:19] neg_lo:[0,1] neg_hi:[0,1]\n"
    "v_pk_add_f32 v[28:29], v[28:29], v[18:19] neg_lo:[0,1] neg_hi:[0,1]\n"
    "v_cvt_pk_bf16_f32 v24, 0, v24\n"
    "v_cvt_pk_bf16_f32 v25, 0, v25\n"
    "v_cvt_pk_bf16_f32 v26, 0, v26\n"
    "v_cvt_pk_bf16_f32 v27, 0, v27\n"
    "v_cvt_pk_bf16_f32 v28, 0, v28\n"
    "v_cvt_pk_bf16_f32 v29, 0, v29\n"
    "v_pk_mul_f32 v[24:25], v[24:25], v[20:21]\n"
    "v_pk_mul_f32 v[26:27], v[26:27], v[20:21]\n"
    "v_pk_mul_f32 v[28:29], v[28:29], v[20:21]\n"
    "v_cvt_pk_bf16_f32 v36, v24, v25\n"
    "v_cvt_pk_bf16_f32 v37, v26, v27\n"
    "v_cvt_pk_bf16_f32 v38, v28, v29\n"
    "buffer_store_dwordx4 v[36:39], v11, s[16:19], 0 offen sc1\n"
    "v_pk_add_f32 v[30:31], v[30:31], v[18:19] neg_lo:[0,1] neg_hi:[0,1]\n"
    "v_pk_add_f32 v[32:33], v[32:33], v[18:19] neg_lo:[0,1] neg_hi:[0,1]\n"
    "v_pk_add_f32 v[34:35], v[34:35], v[18:19] neg_lo:[0,1] neg_hi:[0,1]\n"
    "v_cvt_pk_bf16_f32 v30, 0, v30\n"
    "v_cvt_pk_bf16_f32 v31, 0, v31\n"
    "v_cvt_pk_bf16_f32 v32, 0, v32\n"
    "v_cvt_pk_bf16_f32 v33, 0, v33\n"
    "v_cvt_pk_bf16_f32 v34, 0, v34\n"
    "v_cvt_pk_bf16_f32 v35, 0, v35\n"
    "v_pk_mul_f32 v[30:31], v[30:31], v[20:21]\n"
    "v_pk_mul_f32 v[32:33], v[32:33], v[20:21]\n"
    "v_pk_mul_f32 v[34:35], v[34:35], v[20:21]\n"
    "v_cvt_pk_bf16_f32 v40, v30, v31\n"
    "v_cvt_pk_bf16_f32 v41, v32, v33\n"
    "v_cvt_pk_bf16_f32 v42, v34, v35\n"
    "buffer_store_dwordx4 v[40:43], v11, s[16:19], s44 offen sc1\n"
    "s_setprio 0\n"
    "s_cmpk_ge_u32 s2, 192\n"
    "s_cbranch_scc1 ES1\n"
    "s_branch KST\n"
    "PAD12:\n"
    "s_cbranch_execz MD1\n"
    "PAD17:\n"
    "s_cbranch_execz M1\n"
    "MD1:\n"
    "s_mov_b64 exec, -1\n"
    "s_barrier\n"
    "s_cmpk_ge_u32 s2, 243\n"
    "s_cbranch_scc1 M9\n"
    "v_bfe_u32 v5, v0, 4, 2\n"
    "v_lshlrev_b32 v1, 2, v3\n"
    "v_add_u32 v1, 149888, v1\n"
    "v_lshlrev_b32 v2, 4, v5\n"
    "v_add_u32 v2, 149888, v2\n"
    "v_bfe_u32 v6, v0, 2, 2\n"
    "v_bfe_u32 v7, v0, 1, 1\n"
    "v_and_b32 v8, 2, v6\n"
    "v_or_b32 v7, v7, v8\n"
    "v_and_b32 v8, 1, v5\n"
    "v_lshl_add_u32 v7, v8, 2, v7\n"
    "v_lshlrev_b32 v8, 10, v5\n"
    "v_lshl_add_u32 v8, v6, 7, v8\n"
    "v_lshl_add_u32 v8, v7, 4, v8\n"
    "v_and_b32 v7, 1, v0\n"
    "v_lshl_add_u32 v8, v7, 3, v8\n"
    "v_xor_b32 v9, 32, v8\n"
    "v_xor_b32 v10, 64, v8\n"
    "v_xor_b32 v11, 0x60, v8\n"
    "v_add_u32 v8, 138240, v8\n"
    "v_add_u32 v9, 138240, v9\n"
    "v_add_u32 v10, 138240, v10\n"
    "v_add_u32 v11, 138240, v11\n"
    "v_and_b32 v3, 63, v0\n"
    "v_lshlrev_b32 v3, 3, v3\n"
    "s_mul_i32 s43, s43, 13824\n"
    "s_lshl_b32 s44, s44, 9\n"
    "s_add_u32 s43, s43, s44\n"
    "s_add_u32 s43, s43, 10519552\n"
    "s_waitcnt vmcnt(8)\n"
    "v_lshlrev_b32 v5, 16, v4\n"
    "v_and_b32 v4, 0xffff0000, v4\n"
    "v_mul_f32 v6, 0xbfb8aa3b, v5\n"
    "v_mul_f32 v7, 0xbfb8aa3b, v4\n"
    "v_exp_f32 v6, v6\n"
    "v_exp_f32 v7, v7\n"
    "v_add_f32 v6, 1.0, v6\n"
    "v_add_f32 v7, 1.0, v7\n"
    "v_rcp_f32 v6, v6\n"
    "v_rcp_f32 v7, v7\n"
    "v_mul_f32 v5, v5, v6\n"
    "v_mul_f32 v4, v4, v7\n"
    "v_cvt_pk_bf16_f32 v4, v5, v4\n"
    "ds_write_b32 v1, v4\n"
    "ds_read_b128 v[12:15], v2\n"
    "ds_read_b128 v[16:19], v2 offset:64\n"
    "s_waitcnt lgkmcnt(0)\n"
    "s_add_u32 s36, s36, 1\n"
    "s_waitcnt vmcnt(4)\n"
    "ds_read_b64_tr_b16 v[40:41], v8 offset:0\n"
    "ds_read_b64_tr_b16 v[42:43], v8 offset:512\n"
    "ds_read_b64_tr_b16 v[44:45], v9 offset:0\n"
    "ds_read_b64_tr_b16 v[46:47], v9 offset:512\n"
    "ds_read_b64_tr_b16 v[48:49], v10 offset:0\n"
    "ds_read_b64_tr_b16 v[50:51], v10 offset:512\n"
    "ds_read_b64_tr_b16 v[52:53], v11 offset:0\n"
    "ds_read_b64_tr_b16 v[54:55], v11 offset:512\n"
    "s_waitcnt lgkmcnt(6)\n"
    "v_mfma_f32_16x16x32_bf16 v[20:23], v[12:15], v[40:43], 0\n"
    "s_waitcnt lgkmcnt(4)\n"
    "v_mfma_f32_16x16x32_bf16 v[24:27], v[12:15], v[44:47], 0\n"
    "s_waitcnt lgkmcnt(2)\n"
    "v_mfma_f32_16x16x32_bf16 v[28:31], v[12:15], v[48:51], 0\n"
    "s_waitcnt lgkmcnt(0)\n"
    "v_mfma_f32_16x16x32_bf16 v[32:35], v[12:15], v[52:55], 0\n"
    "s_waitcnt vmcnt(0)\n"
    "ds_read_b64_tr_b16 v[56:57], v8 offset:4096\n"
    "ds_read_b64_tr_b16 v[58:59], v8 offset:4608\n"
    "ds_read_b64_tr_b16 v[60:61], v9 offset:4096\n"
    "ds_read_b64_tr_b16 v[62:63], v9 offset:4608\n"
    "ds_read_b64_tr_b16 v[64:65], v10 offset:4096\n"
    "ds_read_b64_tr_b16 v[66:67], v10 offset:4608\n"
    "ds_read_b64_tr_b16 v[68:69], v11 offset:4096\n"
    "ds_read_b64_tr_b16 v[70:71], v11 offset:4608\n"
    "s_waitcnt lgkmcnt(6)\n"
    "v_mfma_f32_16x16x32_bf16 v[20:23], v[16:19], v[56:59], v[20:23]\n"
    "s_waitcnt lgkmcnt(4)\n"
    "v_mfma_f32_16x16x32_bf16 v[24:27], v[16:19], v[60:63], v[24:27]\n"
    "s_waitcnt lgkmcnt(2)\n"
    "v_mfma_f32_16x16x32_bf16 v[28:31], v[16:19], v[64:67], v[28:31]\n"
    "s_waitcnt lgkmcnt(0)\n"
    "v_mfma_f32_16x16x32_bf16 v[32:35], v[16:19], v[68:71], v[32:35]\n"
    "s_nop 1\n"
    "v_mov_b32 v21, s36\n"
    "s_nop 0\n"
    "v_mov_b32_dpp v20, v24 quad_perm:[0,1,2,3] row_mask:0x2 bank_mask:0xf\n"
    "s_nop 1\n"
    "v_mov_b32_dpp v20, v28 quad_perm:[0,1,2,3] row_mask:0x4 bank_mask:0xf\n"
    "s_nop 1\n"
    "v_mov_b32_dpp v20, v32 quad_perm:[0,1,2,3] row_mask:0x8 bank_mask:0xf\n"
    "buffer_store_dwordx2 v[20:21], v3, s[16:19], s43 offen sc1\n"
    "s_setprio 0\n"
    "s_cmpk_ge_u32 s2, 192\n"
    "s_cbranch_scc1 ES1\n"
    "s_branch AUX3\n"
    "PAD14:\n"
    "s_cbranch_execz AUX1\n"
    "GA1:\n"
    "s_mov_b64 exec, -1\n"
    "s_barrier\n"
    "s_setprio 1\n"
    "s_mov_b32 s48, 0x01000c0c\n"
    "s_bitcmp1_b32 s37, 0\n"
    "s_cselect_b32 s47, 0x03020c0c, s48\n"
    "s_and_b32 s43, s43, 1\n"
    "s_lshl_b32 s43, s43, 4\n"
    "s_mul_i32 s44, s42, 30464\n"
    "s_and_b32 s45, s37, 1\n"
    "s_lshl_b32 s45, s45, 9\n"
    "s_add_u32 s44, s44, s45\n"
    "v_lshl_add_u32 v2, v1, 3, s44\n"
    "v_lshlrev_b32 v3, 4, v0\n"
    "v_add_u32 v3, 126368, v3\n"
    "s_lshl_b32 s45, s37, 2\n"
    "s_add_u32 s45, s45, 147952\n"
    "v_mov_b32 v12, s45\n"
    "s_lshl_b32 s45, s42, 2\n"
    "s_add_u32 s44, s45, 147968\n"
    "v_mov_b32 v13, s44\n"
    "s_lshl_b32 s45, s45, 1\n"
    "s_add_u32 s45, s45, 148416\n"
    "v_mov_b32 v14, s45\n"
    "v_mov_b32 v15, 1\n"
    "v_mov_b32 v8, 0x3fb8aa3b\n"
    "s_mov_b32 s48, 0x3e22f983\n"
    "s_mov_b32 s49, 0x31dc9c88\n"
    "s_mov_b32 s50, 0x41000000\n"
    "s_mov_b32 s51, 0x43000000\n"
    "s_waitcnt lgkmcnt(0)\n"
    "s_lshr_b32 s46, s46, s43\n"
    "s_lshl_b32 s46, s46, 16\n"
    "s_add_u32 s36, s36, 1\n"
    "v_mul_f32 v9, s46, v8\n"
    "v_exp_f32 v9, v9\n"
    "s_waitcnt vmcnt(3)\n"
    "v_perm_b32 v4, v4, v4, s47\n"
    "v_mul_f32 v4, v4, v8\n"
    "v_cvt_pk_bf16_f32 v9, 0, v9\n"
    "v_exp_f32 v4, v4\n"
    "s_waitcnt vmcnt(2)\n"
    "v_perm_b32 v5, v5, v5, s47\n"
    "v_mul_f32 v5, v5, v9\n"
    "v_cvt_pk_bf16_f32 v4, 0, v4\n"
    "v_mul_f32 v10, s48, v5\n"
    "v_mul_f32 v4, v4, v9\n"
    "v_fma_f32 v11, v5, s48, -v10\n"
    "v_mul_f32_e64 v4, v4, -v8\n"
    "v_fma_f32 v11, v5, s49, v11\n"
    "v_mul_f32 v20, s50, v10\n"
    "v_mul_f32 v21, s50, v4\n"
    "v_fract_f32 v20, v20\n"
    "v_exp_f32 v21, v21\n"
    "v_fma_f32 v20, v11, s50, v20\n"
    "v_cos_f32 v16, v20\n"
    "v_sin_f32 v17, v20\n"
    "v_mul_f32 v22, s51, v10\n"
    "v_mul_f32 v23, s51, v4\n"
    "v_fract_f32 v22, v22\n"
    "v_exp_f32 v23, v23\n"
    "v_fma_f32 v22, v11, s51, v22\n"
    "v_cos_f32 v18, v22\n"
    "v_sin_f32 v19, v22\n"
    "v_pk_mul_f32 v[16:17], v[20:21], v[16:17] op_sel:[1,0] op_sel_hi:[1,1]\n"
    "v_pk_mul_f32 v[18:19], v[22:23], v[18:19] op_sel:[1,0] op_sel_hi:[1,1]\n"
    "s_cmpk_lt_u32 s37, 4\n"
    "s_cbranch_scc1 ASIDE\n"
    "ds_write_b128 v3, v[16:19]\n"
    "s_mov_b64 exec, 1\n"
    "ds_write_b32 v12, v15 offset:4\n"
    "s_mov_b64 exec, -1\n"
    "v_mov_b32 v36, 148448\n"
    "v_mov_b32 v37, s96\n"
    "ds_write_b32 v36, v37\n"
    "ASIDE:\n"
    "s_setprio 0\n"
    "v_fract_f32 v22, v10\n"
    "v_exp_f32 v23, v4\n"
    "v_add_f32 v22, v11, v22\n"
    "v_cos_f32 v24, v22\n"
    "v_sin_f32 v25, v22\n"
    "s_waitcnt vmcnt(0)\n"
    "v_and_b32 v32, 0x7fff7fff, v6\n"
    "v_and_b32 v33, 0x7fff7fff, v7\n"
    "v_pk_max_u16 v32, v32, v33\n"
    "v_pk_mul_f32 v[24:25], v[22:23], v[24:25] op_sel:[1,0] op_sel_hi:[1,1]\n"
    "v_lshlrev_b32 v33, 16, v32\n"
    "v_max_u32 v32, v32, v33\n"
    "v_perm_b32 v6, v6, v6, s47\n"
    "v_perm_b32 v7, v7, v7, s47\n"
    "v_max_u32_dpp v32, v32, v32 row_ror:8 row_mask:0xf bank_mask:0xf\n"
    "v_pk_mul_f32 v[20:21], v[18:19], v[18:19] op_sel:[1,1] op_sel_hi:[1,0] neg_lo:[0,1]\n"
    "v_pk_fma_f32 v[30:31], v[18:19], v[18:19], v[20:21] op_sel_hi:[0,1,1]\n"
    "v_max_u32_dpp v32, v32, v32 row_ror:4 row_mask:0xf bank_mask:0xf\n"
    "v_pk_mul_f32 v[20:21], v[24:25], v[24:25] op_sel:[1,1] op_sel_hi:[1,0] neg_lo:[0,1]\n"
    "v_pk_fma_f32 v[26:27], v[24:25], v[24:25], v[20:21] op_sel_hi:[0,1,1]\n"
    "v_max_u32_dpp v32, v32, v32 row_ror:2 row_mask:0xf bank_mask:0xf\n"
    "v_pk_mul_f32 v[20:21], v[26:27], v[26:27] op_sel:[1,1] op_sel_hi:[1,0] neg_lo:[0,1]\n"
    "v_pk_fma_f32 v[28:29], v[26:27], v[26:27], v[20:21] op_sel_hi:[0,1,1]\n"
    "v_max_u32_dpp v32, v32, v32 row_ror:1 row_mask:0xf bank_mask:0xf\n"
    "v_and_b32 v34, 0x7fffff, v9\n"
    "v_or_b32 v34, 0x35800000, v34\n"
    "v_max_u32_dpp v32, v32, v32 row_bcast:15 row_mask:0xa bank_mask:0xf\n"
    "v_and_b32 v35, 0x7f800000, v9\n"
    "s_nop 0\n"
    "v_max_u32_dpp v32, v32, v32 row_bcast:31 row_mask:0xc bank_mask:0xf\n"
    "s_nop 0\n"
    "v_readlane_b32 s44, v32, 63\n"
    "s_lshr_b32 s44, s44, 23\n"
    "s_max_u32 s44, s44, 26\n"
    "s_min_u32 s44, s44, 226\n"
    "s_sub_u32 s46, 266, s44\n"
    "s_lshl_b32 s46, s46, 23\n"
    "s_sub_u32 s44, s44, 132\n"
    "v_pk_mul_f32 v[36:37], v[6:7], s[46:47] op_sel_hi:[1,0]\n"
    "v_ldexp_f32 v35, v35, s44\n"
    "ds_write_b64 v14, v[34:35]\n"
    "v_pk_mul_f32 v[20:21], v[36:37], v[24:25] op_sel:[1,1] op_sel_hi:[1,0] neg_lo:[0,1]\n"
    "v_pk_fma_f32 v[38:39], v[36:37], v[24:25], v[20:21] op_sel_hi:[0,1,1]\n"
    "v_pk_mul_f32 v[20:21], v[38:39], v[24:25] op_sel:[1,1] op_sel_hi:[1,0] neg_lo:[0,1]\n"
    "v_pk_fma_f32 v[40:41], v[38:39], v[24:25], v[20:21] op_sel_hi:[0,1,1]\n"
    "v_pk_mul_f32 v[20:21], v[40:41], v[24:25] op_sel:[1,1] op_sel_hi:[1,0] neg_lo:[0,1]\n"
    "v_pk_fma_f32 v[42:43], v[40:41], v[24:25], v[20:21] op_sel_hi:[0,1,1]\n"
    "v_pk_mul_f32 v[20:21], v[36:37], v[30:31] op_sel:[1,1] op_sel_hi:[1,0] neg_lo:[0,1]\n"
    "v_pk_fma_f32 v[44:45], v[36:37], v[30:31], v[20:21] op_sel_hi:[0,1,1]\n"
    "v_cvt_pk_f16_f32 v60, v36, v38\n"
    "v_cvt_pk_f16_f32 v61, v40, v42\n"
    "v_cvt_pk_f16_f32 v62, -v37, -v39\n"
    "v_cvt_pk_f16_f32 v63, -v41, -v43\n"
    "ds_write_b64 v2, v[60:61] offset:0\n"
    "ds_write_b64 v2, v[62:63] offset:6528\n"
    "v_pk_mul_f32 v[20:21], v[42:43], v[24:25] op_sel:[1,1] op_sel_hi:[1,0] neg_lo:[0,1]\n"
    "v_pk_fma_f32 v[36:37], v[42:43], v[24:25], v[20:21] op_sel_hi:[0,1,1]\n"
    "v_pk_mul_f32 v[20:21], v[36:37], v[24:25] op_sel:[1,1] op_sel_hi:[1,0] neg_lo:[0,1]\n"
    "v_pk_fma_f32 v[38:39], v[36:37], v[24:25], v[20:21] op_sel_hi:[0,1,1]\n"
    "v_pk_mul_f32 v[20:21], v[38:39], v[24:25] op_sel:[1,1] op_sel_hi:[1,0] neg_lo:[0,1]\n"
    "v_pk_fma_f32 v[40:41], v[38:39], v[24:25], v[20:21] op_sel_hi:[0,1,1]\n"
    "v_pk_mul_f32 v[20:21], v[40:41], v[24:25] op_sel:[1,1] op_sel_hi:[1,0] neg_lo:[0,1]\n"
    "v_pk_fma_f32 v[42:43], v[40:41], v[24:25], v[20:21] op_sel_hi:[0,1,1]\n"
    "v_cvt_pk_f16_f32 v64, v36, v38\n"
    "v_cvt_pk_f16_f32 v65, v40, v42\n"
    "v_cvt_pk_f16_f32 v66, -v37, -v39\n"
    "v_cvt_pk_f16_f32 v67, -v41, -v43\n"
    "ds_write_b64 v2, v[64:65] offset:1088\n"
    "ds_write_b64 v2, v[66:67] offset:7616\n"
    "v_pk_mul_f32 v[20:21], v[44:45], v[26:27] op_sel:[1,1] op_sel_hi:[1,0] neg_lo:[0,1]\n"
    "v_pk_fma_f32 v[46:47], v[44:45], v[26:27], v[20:21] op_sel_hi:[0,1,1]\n"
    "v_pk_mul_f32 v[20:21], v[44:45], v[28:29] op_sel:[1,1] op_sel_hi:[1,0] neg_lo:[0,1]\n"
    "v_pk_fma_f32 v[48:49], v[44:45], v[28:29], v[20:21] op_sel_hi:[0,1,1]\n"
    "v_pk_mul_f32 v[20:21], v[48:49], v[26:27] op_sel:[1,1] op_sel_hi:[1,0] neg_lo:[0,1]\n"
    "v_pk_fma_f32 v[50:51], v[48:49], v[26:27], v[20:21] op_sel_hi:[0,1,1]\n"
    "v_pk_mul_f32 v[20:21], v[44:45], v[30:31] op_sel:[1,1] op_sel_hi:[1,0] neg_lo:[0,1]\n"
    "v_pk_fma_f32 v[36:37], v[44:45], v[30:31], v[20:21] op_sel_hi:[0,1,1]\n"
    "PAD15:\n"
    "s_cbranch_execz AUX1\n"
    "v_cvt_pk_f16_f32 v60, v44, v46\n"
    "v_cvt_pk_f16_f32 v61, v48, v50\n"
    "v_cvt_pk_f16_f32 v62, -v45, -v47\n"
    "v_cvt_pk_f16_f32 v63, -v49, -v51\n"
    "ds_write_b64 v2, v[60:61] offset:2176\n"
    "ds_write_b64 v2, v[62:63] offset:8704\n"
    "v_pk_mul_f32 v[20:21], v[36:37], v[30:31] op_sel:[1,1] op_sel_hi:[1,0] neg_lo:[0,1]\n"
    "v_pk_fma_f32 v[38:39], v[36:37], v[30:31], v[20:21] op_sel_hi:[0,1,1]\n"
    "v_pk_mul_f32 v[20:21], v[36:37], v[28:29] op_sel:[1,1] op_sel_hi:[1,0] neg_lo:[0,1]\n"
    "v_pk_fma_f32 v[40:41], v[36:37], v[28:29], v[20:21] op_sel_hi:[0,1,1]\n"
    "v_pk_mul_f32 v[20:21], v[38:39], v[28:29] op_sel:[1,1] op_sel_hi:[1,0] neg_lo:[0,1]\n"
    "v_pk_fma_f32 v[42:43], v[38:39], v[28:29], v[20:21] op_sel_hi:[0,1,1]\n"
    "v_pk_mul_f32 v[20:21], v[38:39], v[30:31] op_sel:[1,1] op_sel_hi:[1,0] neg_lo:[0,1]\n"
    "v_pk_fma_f32 v[44:45], v[38:39], v[30:31], v[20:21] op_sel_hi:[0,1,1]\n"
    "v_cvt_pk_f16_f32 v64, v40, v42\n"
    "v_cvt_pk_f16_f32 v65, v36, v38\n"
    "v_cvt_pk_f16_f32 v66, -v41, -v43\n"
    "v_cvt_pk_f16_f32 v67, -v37, -v39\n"
    "ds_write_b64 v2, v[64:65] offset:3264\n"
    "ds_write_b64 v2, v[66:67] offset:9792\n"
    "v_pk_mul_f32 v[20:21], v[44:45], v[30:31] op_sel:[1,1] op_sel_hi:[1,0] neg_lo:[0,1]\n"
    "v_pk_fma_f32 v[46:47], v[44:45], v[30:31], v[20:21] op_sel_hi:[0,1,1]\n"
    "v_pk_mul_f32 v[20:21], v[46:47], v[30:31] op_sel:[1,1] op_sel_hi:[1,0] neg_lo:[0,1]\n"
    "v_pk_fma_f32 v[48:49], v[46:47], v[30:31], v[20:21] op_sel_hi:[0,1,1]\n"
    "v_pk_mul_f32 v[20:21], v[48:49], v[30:31] op_sel:[1,1] op_sel_hi:[1,0] neg_lo:[0,1]\n"
    "v_pk_fma_f32 v[50:51], v[48:49], v[30:31], v[20:21] op_sel_hi:[0,1,1]\n"
    "v_pk_mul_f32 v[20:21], v[50:51], v[30:31] op_sel:[1,1] op_sel_hi:[1,0] neg_lo:[0,1]\n"
    "v_pk_fma_f32 v[36:37], v[50:51], v[30:31], v[20:21] op_sel_hi:[0,1,1]\n"
    "v_cvt_pk_f16_f32 v60, v44, v46\n"
    "v_cvt_pk_f16_f32 v61, v48, v50\n"
    "v_cvt_pk_f16_f32 v62, -v45, -v47\n"
    "v_cvt_pk_f16_f32 v63, -v49, -v51\n"
    "ds_write_b64 v2, v[60:61] offset:4352\n"
    "ds_write_b64 v2, v[62:63] offset:10880\n"
    "v_cvt_pk_f16_f32 v38, v36, -v37\n"
    "ds_write_b16 v2, v38 offset:5440\n"
    "ds_write_b16_d16_hi v2, v38 offset:11968\n"
    "s_mov_b64 exec, 1\n"
    "ds_add_u32 v13, v15 offset:12\n"
    "s_mov_b64 exec, -1\n"
    "s_cmpk_ge_u32 s37, 4\n"
    "s_cbranch_scc1 HELP\n"
    "BSIDE:\n"
    "v_mov_b32 v36, 0x46800000\n"
    "v_mov_b32 v37, 0\n"
    "v_pk_mul_f32 v[38:39], v[18:19], v[36:37] op_sel_hi:[1,0]\n"
    "v_cvt_pk_f16_f32 v44, v36, v38\n"
    "v_cvt_pk_f16_f32 v46, v37, v39\n"
    "v_pk_mul_f32 v[36:37], v[16:17], v[36:37] op_sel_hi:[1,0]\n"
    "v_pk_mul_f32 v[42:43], v[38:39], v[16:17] op_sel:[1,1] op_sel_hi:[1,0] neg_lo:[0,1]\n"
    "v_pk_fma_f32 v[38:39], v[38:39], v[16:17], v[42:43] op_sel_hi:[0,1,1]\n"
    "v_cvt_pk_f16_f32 v45, v36, v38\n"
    "v_cvt_pk_f16_f32 v47, v37, v39\n"
    "v_pk_mul_f32 v[40:41], v[36:37], v[16:17] op_sel:[1,1] op_sel_hi:[1,0] neg_lo:[0,1]\n"
    "v_pk_fma_f32 v[36:37], v[36:37], v[16:17], v[40:41] op_sel_hi:[0,1,1]\n"
    "ds_write_b64 v2, v[44:45] offset:13056\n"
    "v_pk_mul_f32 v[42:43], v[38:39], v[16:17] op_sel:[1,1] op_sel_hi:[1,0] neg_lo:[0,1]\n"
    "v_pk_fma_f32 v[38:39], v[38:39], v[16:17], v[42:43] op_sel_hi:[0,1,1]\n"
    "ds_write_b64 v2, v[46:47] offset:21760\n"
    "v_cvt_pk_f16_f32 v48, v36, v38\n"
    "v_cvt_pk_f16_f32 v50, v37, v39\n"
    "v_pk_mul_f32 v[40:41], v[36:37], v[16:17] op_sel:[1,1] op_sel_hi:[1,0] neg_lo:[0,1]\n"
    "v_pk_fma_f32 v[36:37], v[36:37], v[16:17], v[40:41] op_sel_hi:[0,1,1]\n"
    "v_pk_mul_f32 v[42:43], v[38:39], v[16:17] op_sel:[1,1] op_sel_hi:[1,0] neg_lo:[0,1]\n"
    "v_pk_fma_f32 v[38:39], v[38:39], v[16:17], v[42:43] op_sel_hi:[0,1,1]\n"
    "v_cvt_pk_f16_f32 v49, v36, v38\n"
    "v_cvt_pk_f16_f32 v51, v37, v39\n"
    "v_pk_mul_f32 v[40:41], v[36:37], v[16:17] op_sel:[1,1] op_sel_hi:[1,0] neg_lo:[0,1]\n"
    "v_pk_fma_f32 v[36:37], v[36:37], v[16:17], v[40:41] op_sel_hi:[0,1,1]\n"
    "ds_write_b64 v2, v[48:49] offset:14144\n"
    "v_pk_mul_f32 v[42:43], v[38:39], v[16:17] op_sel:[1,1] op_sel_hi:[1,0] neg_lo:[0,1]\n"
    "v_pk_fma_f32 v[38:39], v[38:39], v[16:17], v[42:43] op_sel_hi:[0,1,1]\n"
    "ds_write_b64 v2, v[50:51] offset:22848\n"
    "v_cvt_pk_f16_f32 v44, v36, v38\n"
    "v_cvt_pk_f16_f32 v46, v37, v39\n"
    "v_pk_mul_f32 v[40:41], v[36:37], v[16:17] op_sel:[1,1] op_sel_hi:[1,0] neg_lo:[0,1]\n"
    "v_pk_fma_f32 v[36:37], v[36:37], v[16:17], v[40:41] op_sel_hi:[0,1,1]\n"
    "v_pk_mul_f32 v[42:43], v[38:39], v[16:17] op_sel:[1,1] op_sel_hi:[1,0] neg_lo:[0,1]\n"
    "v_pk_fma_f32 v[38:39], v[38:39], v[16:17], v[42:43] op_sel_hi:[0,1,1]\n"
    "v_cvt_pk_f16_f32 v45, v36, v38\n"
    "v_cvt_pk_f16_f32 v47, v37, v39\n"
    "v_pk_mul_f32 v[40:41], v[36:37], v[16:17] op_sel:[1,1] op_sel_hi:[1,0] neg_lo:[0,1]\n"
    "v_pk_fma_f32 v[36:37], v[36:37], v[16:17], v[40:41] op_sel_hi:[0,1,1]\n"
    "ds_write_b64 v2, v[44:45] offset:15232\n"
    "v_pk_mul_f32 v[42:43], v[38:39], v[16:17] op_sel:[1,1] op_sel_hi:[1,0] neg_lo:[0,1]\n"
    "v_pk_fma_f32 v[38:39], v[38:39], v[16:17], v[42:43] op_sel_hi:[0,1,1]\n"
    "ds_write_b64 v2, v[46:47] offset:23936\n"
    "v_cvt_pk_f16_f32 v48, v36, v38\n"
    "v_cvt_pk_f16_f32 v50, v37, v39\n"
    "v_pk_mul_f32 v[40:41], v[36:37], v[16:17] op_sel:[1,1] op_sel_hi:[1,0] neg_lo:[0,1]\n"
    "v_pk_fma_f32 v[36:37], v[36:37], v[16:17], v[40:41] op_sel_hi:[0,1,1]\n"
    "v_pk_mul_f32 v[42:43], v[38:39], v[16:17] op_sel:[1,1] op_sel_hi:[1,0] neg_lo:[0,1]\n"
    "v_pk_fma_f32 v[38:39], v[38:39], v[16:17], v[42:43] op_sel_hi:[0,1,1]\n"
    "v_cvt_pk_f16_f32 v49, v36, v38\n"
    "v_cvt_pk_f16_f32 v51, v37, v39\n"
    "v_pk_mul_f32 v[40:41], v[36:37], v[16:17] op_sel:[1,1] op_sel_hi:[1,0] neg_lo:[0,1]\n"
    "v_pk_fma_f32 v[36:37], v[36:37], v[16:17], v[40:41] op_sel_hi:[0,1,1]\n"
    "ds_write_b64 v2, v[48:49] offset:17408\n"
    "v_pk_mul_f32 v[42:43], v[38:39], v[16:17] op_sel:[1,1] op_sel_hi:[1,0] neg_lo:[0,1]\n"
    "v_pk_fma_f32 v[38:39], v[38:39], v[16:17], v[42:43] op_sel_hi:[0,1,1]\n"
    "ds_write_b64 v2, v[50:51] offset:26112\n"
    "v_cvt_pk_f16_f32 v44, v36, v38\n"
    "v_cvt_pk_f16_f32 v46, v37, v39\n"
    "v_pk_mul_f32 v[40:41], v[36:37], v[16:17] op_sel:[1,1] op_sel_hi:[1,0] neg_lo:[0,1]\n"
    "v_pk_fma_f32 v[36:37], v[36:37], v[16:17], v[40:41] op_sel_hi:[0,1,1]\n"
    "v_pk_mul_f32 v[42:43], v[38:39], v[16:17] op_sel:[1,1] op_sel_hi:[1,0] neg_lo:[0,1]\n"
    "v_pk_fma_f32 v[38:39], v[38:39], v[16:17], v[42:43] op_sel_hi:[0,1,1]\n"
    "v_cvt_pk_f16_f32 v45, v36, v38\n"
    "v_cvt_pk_f16_f32 v47, v37, v39\n"
    "v_pk_mul_f32 v[40:41], v[36:37], v[16:17] op_sel:[1,1] op_sel_hi:[1,0] neg_lo:[0,1]\n"
    "v_pk_fma_f32 v[36:37], v[36:37], v[16:17], v[40:41] op_sel_hi:[0,1,1]\n"
    "ds_write_b64 v2, v[44:45] offset:18496\n"
    "v_pk_mul_f32 v[42:43], v[38:39], v[16:17] op_sel:[1,1] op_sel_hi:[1,0] neg_lo:[0,1]\n"
    "v_pk_fma_f32 v[38:39], v[38:39], v[16:17], v[42:43] op_sel_hi:[0,1,1]\n"
    "ds_write_b64 v2, v[46:47] offset:27200\n"
    "v_cvt_pk_f16_f32 v48, v36, v38\n"
    "v_cvt_pk_f16_f32 v50, v37, v39\n"
    "v_pk_mul_f32 v[40:41], v[36:37], v[16:17] op_sel:[1,1] op_sel_hi:[1,0] neg_lo:[0,1]\n"
    "v_pk_fma_f32 v[36:37], v[36:37], v[16:17], v[40:41] op_sel_hi:[0,1,1]\n"
    "v_pk_mul_f32 v[42:43], v[38:39], v[16:17] op_sel:[1,1] op_sel_hi:[1,0] neg_lo:[0,1]\n"
    "v_pk_fma_f32 v[38:39], v[38:39], v[16:17], v[42:43] op_sel_hi:[0,1,1]\n"
    "v_cvt_pk_f16_f32 v49, v36, v38\n"
    "v_cvt_pk_f16_f32 v51, v37, v39\n"
    "v_pk_mul_f32 v[40:41], v[36:37], v[16:17] op_sel:[1,1] op_sel_hi:[1,0] neg_lo:[0,1]\n"
    "v_pk_fma_f32 v[36:37], v[36:37], v[16:17], v[40:41] op_sel_hi:[0,1,1]\n"
    "ds_write_b64 v2, v[48:49] offset:19584\n"
    "PAD0:\n"
    "s_cbranch_execz GA1\n"
    "v_pk_mul_f32 v[42:43], v[38:39], v[16:17] op_sel:[1,1] op_sel_hi:[1,0] neg_lo:[0,1]\n"
    "v_pk_fma_f32 v[38:39], v[38:39], v[16:17], v[42:43] op_sel_hi:[0,1,1]\n"
    "ds_write_b64 v2, v[50:51] offset:28288\n"
    "v_cvt_pk_f16_f32 v44, v36, v38\n"
    "v_cvt_pk_f16_f32 v46, v37, v39\n"
    "v_pk_mul_f32 v[40:41], v[36:37], v[16:17] op_sel:[1,1] op_sel_hi:[1,0] neg_lo:[0,1]\n"
    "v_pk_fma_f32 v[36:37], v[36:37], v[16:17], v[40:41] op_sel_hi:[0,1,1]\n"
    "v_pk_mul_f32 v[42:43], v[38:39], v[16:17] op_sel:[1,1] op_sel_hi:[1,0] neg_lo:[0,1]\n"
    "v_pk_fma_f32 v[38:39], v[38:39], v[16:17], v[42:43] op_sel_hi:[0,1,1]\n"
    "v_cvt_pk_f16_f32 v45, v36, v38\n"
    "v_cvt_pk_f16_f32 v47, v37, v39\n"
    "v_pk_mul_f32 v[40:41], v[36:37], v[16:17] op_sel:[1,1] op_sel_hi:[1,0] neg_lo:[0,1]\n"
    "v_pk_fma_f32 v[36:37], v[36:37], v[16:17], v[40:41] op_sel_hi:[0,1,1]\n"
    "ds_write_b64 v2, v[44:45] offset:20672\n"
    "v_pk_mul_f32 v[42:43], v[38:39], v[16:17] op_sel:[1,1] op_sel_hi:[1,0] neg_lo:[0,1]\n"
    "v_pk_fma_f32 v[38:39], v[38:39], v[16:17], v[42:43] op_sel_hi:[0,1,1]\n"
    "ds_write_b64 v2, v[46:47] offset:29376\n"
    "v_cvt_pk_f16_f32 v48, v36, v38\n"
    "v_cvt_pk_f16_f32 v50, v37, v39\n"
    "v_pk_mul_f32 v[40:41], v[36:37], v[16:17] op_sel:[1,1] op_sel_hi:[1,0] neg_lo:[0,1]\n"
    "v_pk_fma_f32 v[36:37], v[36:37], v[16:17], v[40:41] op_sel_hi:[0,1,1]\n"
    "v_pk_mul_f32 v[42:43], v[38:39], v[16:17] op_sel:[1,1] op_sel_hi:[1,0] neg_lo:[0,1]\n"
    "v_pk_fma_f32 v[38:39], v[38:39], v[16:17], v[42:43] op_sel_hi:[0,1,1]\n"
    "v_cvt_pk_f16_f32 v49, v36, v38\n"
    "v_cvt_pk_f16_f32 v51, v37, v39\n"
    "ds_write_b64 v2, v[48:49] offset:16320\n"
    "ds_write_b64 v2, v[50:51] offset:25024\n"
    "s_mov_b64 exec, 1\n"
    "ds_add_u32 v13, v15 offset:28\n"
    "s_mov_b64 exec, -1\n"
    "s_cmpk_ge_u32 s37, 14\n"
    "s_cbranch_scc1 AUX3\n"
    "v_mov_b32 v14, 147968\n"
    "s_branch STAGE\n"
    "KST:\n"
    "s_branch E_4\n"
    "R_3:\n"
    "s_sleep 1\n"
    "s_sub_u32 s39, s39, 1\n"
    "s_cbranch_scc1 ABORT\n"
    "E_4:\n"
    "ds_read_b32 v4, v14 offset:24\n"
    "ds_read_b128 v[16:19], v124 offset:0\n"
    "ds_read_b128 v[20:23], v124 offset:1024\n"
    "ds_read_b128 v[24:27], v124 offset:2048\n"
    "ds_read_b128 v[28:31], v124 offset:3072\n"
    "s_waitcnt lgkmcnt(0)\n"
    "v_cmp_ne_u32 vcc, 1, v4\n"
    "s_cbranch_vccnz R_3\n"
    "v_add_u32 v28, s49, v28\n"
    "v_add_u32 v29, s49, v29\n"
    "s_mov_b32 s46, 0x01000100\n"
    "s_mov_b32 s42, s47\n"
    "s_mov_b64 s[50:51], 0x1ffff\n"
    "s_branch E_6\n"
    "R_5:\n"
    "s_sleep 1\n"
    "s_sub_u32 s39, s39, 1\n"
    "s_cbranch_scc1 ABORT\n"
    "E_6:\n"
    "ds_read_b32 v4, v125 offset:12\n"
    "s_waitcnt lgkmcnt(0)\n"
    "v_cmp_ne_u32 vcc, 2, v4\n"
    "s_cbranch_vccnz R_5\n"
    "ds_read_b64_tr_b16 v[32:33], v122 offset:0\n"
    "ds_read_b64_tr_b16 v[34:35], v122 offset:6528\n"
    "ds_read_b64_tr_b16 v[36:37], v122 offset:4352\n"
    "ds_read_b64_tr_b16 v[38:39], v122 offset:10880\n"
    "ds_read_b64_tr_b16 v[40:41], v122 offset:128\n"
    "ds_read_b64_tr_b16 v[42:43], v122 offset:6656\n"
    "ds_read_b64_tr_b16 v[44:45], v122 offset:4480\n"
    "ds_read_b64_tr_b16 v[46:47], v122 offset:11008\n"
    "ds_read_b64_tr_b16 v[48:49], v122 offset:256\n"
    "ds_read_b64_tr_b16 v[50:51], v122 offset:6784\n"
    "ds_read_b64_tr_b16 v[52:53], v122 offset:4608\n"
    "ds_read_b64_tr_b16 v[54:55], v122 offset:11136\n"
    "ds_read_b64_tr_b16 v[56:57], v122 offset:384\n"
    "ds_read_b64_tr_b16 v[58:59], v122 offset:6912\n"
    "ds_read_b64_tr_b16 v[60:61], v122 offset:4736\n"
    "ds_read_b64_tr_b16 v[62:63], v122 offset:11264\n"
    "ds_read_b64_tr_b16 v[64:65], v122 offset:512\n"
    "ds_read_b64_tr_b16 v[66:67], v122 offset:7040\n"
    "ds_read_b64_tr_b16 v[68:69], v122 offset:4864\n"
    "ds_read_b64_tr_b16 v[70:71], v122 offset:11392\n"
    "ds_read_b64_tr_b16 v[72:73], v122 offset:640\n"
    "ds_read_b64_tr_b16 v[74:75], v122 offset:7168\n"
    "ds_read_b64_tr_b16 v[76:77], v122 offset:4992\n"
    "ds_read_b64_tr_b16 v[78:79], v122 offset:11520\n"
    "ds_read_b64_tr_b16 v[80:81], v122 offset:768\n"
    "ds_read_b64_tr_b16 v[82:83], v122 offset:7296\n"
    "ds_read_b64_tr_b16 v[84:85], v122 offset:5120\n"
    "ds_read_b64_tr_b16 v[86:87], v122 offset:11648\n"
    "ds_read_b64_tr_b16 v[88:89], v122 offset:896\n"
    "ds_read_b64_tr_b16 v[90:91], v122 offset:7424\n"
    "ds_read_b64_tr_b16 v[92:93], v122 offset:5248\n"
    "ds_read_b64_tr_b16 v[94:95], v122 offset:11776\n"
    "ds_read_b32 v12, v126\n"
    "s_branch E_8\n"
    "R_7:\n"
    "s_sleep 1\n"
    "s_sub_u32 s39, s39, 1\n"
    "s_cbranch_scc1 ABORT\n"
    "E_8:\n"
    "ds_read_b32 v4, v14 offset:28\n"
    "ds_read_b64 v[6:7], v14 offset:32\n"
    "s_waitcnt lgkmcnt(0)\n"
    "v_add3_u32 v4, v4, v6, v7\n"
    "v_cmp_ne_u32 vcc, 6, v4\n"
    "s_cbranch_vccnz R_7\n"
    "s_setprio 1\n"
    "ds_read_b64_tr_b16 v[96:97], v123 offset:13056\n"
    "ds_read_b64_tr_b16 v[98:99], v123 offset:21760\n"
    "ds_read_b64_tr_b16 v[100:101], v123 offset:13184\n"
    "ds_read_b64_tr_b16 v[102:103], v123 offset:21888\n"
    "ds_read_b64_tr_b16 v[104:105], v123 offset:13312\n"
    "ds_read_b64_tr_b16 v[106:107], v123 offset:22016\n"
    "ds_read_b64_tr_b16 v[108:109], v123 offset:13440\n"
    "ds_read_b64_tr_b16 v[110:111], v123 offset:22144\n"
    "ds_read_b64_tr_b16 v[112:113], v123 offset:13568\n"
    "ds_read_b64_tr_b16 v[114:115], v123 offset:22272\n"
    "ds_read_b64_tr_b16 v[116:117], v123 offset:13696\n"
    "ds_read_b64_tr_b16 v[118:119], v123 offset:22400\n"
    "s_waitcnt lgkmcnt(10)\n"
    "v_mfma_f32_16x16x32_f16 v[4:7], v[32:35], v[96:99], 0\n"
    "v_mfma_f32_16x16x32_f16 v[8:11], v[36:39], v[96:99], 0\n"
    "ds_read_b64_tr_b16 v[96:97], v123 offset:13824\n"
    "ds_read_b64_tr_b16 v[98:99], v123 offset:22528\n"
    "s_waitcnt lgkmcnt(10)\n"
    "v_mfma_f32_16x16x32_f16 v[4:7], v[40:43], v[100:103], v[4:7]\n"
    "v_mfma_f32_16x16x32_f16 v[8:11], v[44:47], v[100:103], v[8:11]\n"
    "ds_read_b64_tr_b16 v[100:101], v123 offset:13952\n"
    "ds_read_b64_tr_b16 v[102:103], v123 offset:22656\n"
    "s_waitcnt lgkmcnt(10)\n"
    "v_mfma_f32_16x16x32_f16 v[4:7], v[48:51], v[104:107], v[4:7]\n"
    "v_mfma_f32_16x16x32_f16 v[8:11], v[52:55], v[104:107], v[8:11]\n"
    "s_waitcnt lgkmcnt(8)\n"
    "v_mfma_f32_16x16x32_f16 v[4:7], v[56:59], v[108:111], v[4:7]\n"
    "v_mfma_f32_16x16x32_f16 v[8:11], v[60:63], v[108:111], v[8:11]\n"
    "s_waitcnt lgkmcnt(6)\n"
    "v_mfma_f32_16x16x32_f16 v[4:7], v[64:67], v[112:115], v[4:7]\n"
    "v_mfma_f32_16x16x32_f16 v[8:11], v[68:71], v[112:115], v[8:11]\n"
    "s_waitcnt lgkmcnt(4)\n"
    "v_mfma_f32_16x16x32_f16 v[4:7], v[72:75], v[116:119], v[4:7]\n"
    "v_mfma_f32_16x16x32_f16 v[8:11], v[76:79], v[116:119], v[8:11]\n"
    "s_waitcnt lgkmcnt(2)\n"
    "v_mfma_f32_16x16x32_f16 v[4:7], v[80:83], v[96:99], v[4:7]\n"
    "v_mfma_f32_16x16x32_f16 v[8:11], v[84:87], v[96:99], v[8:11]\n"
    "s_waitcnt lgkmcnt(0)\n"
    "PAD1:\n"
    "s_cbranch_execz GA1\n"
    "v_mfma_f32_16x16x32_f16 v[4:7], v[88:91], v[100:103], v[4:7]\n"
    "v_mfma_f32_16x16x32_f16 v[8:11], v[92:95], v[100:103], v[8:11]\n"
    "s_cmp_eq_u32 s47, 1\n"
    "s_cbranch_scc1 KT1\n"
    "s_cmp_eq_u32 s47, 2\n"
    "s_cbranch_scc1 KT2\n"
    "KT0:\n"
    "s_nop 2\n"
    "v_pk_mul_f32 v[4:5], v[4:5], v[12:13] op_sel_hi:[1,0]\n"
    "v_pk_mul_f32 v[6:7], v[6:7], v[12:13] op_sel_hi:[1,0]\n"
    "v_cvt_pk_f16_f32 v4, v4, v5\n"
    "v_cvt_pk_f16_f32 v5, v6, v7\n"
    "v_pk_mul_f32 v[8:9], v[8:9], v[12:13] op_sel_hi:[1,0]\n"
    "v_perm_b32 v6, v5, v4, v17\n"
    "v_perm_b32 v7, v5, v4, v18\n"
    "v_perm_b32 v32, v5, v4, v30\n"
    "v_perm_b32 v33, v5, v5, s46\n"
    "v_perm_b32 v4, v5, v4, v19\n"
    "s_mov_b32 exec_hi, s50\n"
    "ds_write_b64 v16, v[6:7] offset:0\n"
    "s_mov_b32 exec_lo, 0\n"
    "ds_write2_b32 v28, v6, v32 offset1:1\n"
    "ds_write2_b32 v29, v33, v5 offset1:1\n"
    "s_mov_b64 exec, -1\n"
    "v_pk_mul_f32 v[10:11], v[10:11], v[12:13] op_sel_hi:[1,0]\n"
    "ds_write_b16 v22, v5 offset:0\n"
    "ds_write_b16_d16_hi v23, v5 offset:0\n"
    "v_cvt_pk_f16_f32 v8, v8, v9\n"
    "ds_write_b16 v20, v4 offset:0\n"
    "ds_write_b16_d16_hi v21, v4 offset:0\n"
    "v_cvt_pk_f16_f32 v9, v10, v11\n"
    "s_mov_b64 exec, s[50:51]\n"
    "ds_write_b16 v24, v8 offset:0\n"
    "ds_write_b16_d16_hi v25, v8 offset:0\n"
    "ds_write_b16 v26, v9 offset:0\n"
    "ds_write_b16_d16_hi v27, v9 offset:0\n"
    "s_mov_b64 exec, 1\n"
    "ds_add_u32 v14, v15 offset:224\n"
    "s_mov_b64 exec, -1\n"
    "s_branch KT3\n"
    "KT1:\n"
    "s_nop 4\n"
    "v_pk_mul_f32 v[4:5], v[4:5], v[12:13] op_sel_hi:[1,0]\n"
    "v_pk_mul_f32 v[6:7], v[6:7], v[12:13] op_sel_hi:[1,0]\n"
    "v_cvt_pk_f16_f32 v4, v4, v5\n"
    "v_cvt_pk_f16_f32 v5, v6, v7\n"
    "v_pk_mul_f32 v[8:9], v[8:9], v[12:13] op_sel_hi:[1,0]\n"
    "v_perm_b32 v6, v5, v4, v17\n"
    "v_perm_b32 v7, v5, v4, v18\n"
    "v_perm_b32 v32, v5, v4, v30\n"
    "v_perm_b32 v33, v5, v5, s46\n"
    "v_perm_b32 v4, v5, v4, v19\n"
    "s_mov_b32 exec_hi, s50\n"
    "ds_write_b64 v16, v[6:7] offset:3328\n"
    "s_mov_b32 exec_lo, 0\n"
    "ds_write2_b32 v28, v6, v32 offset1:1\n"
    "ds_write2_b32 v29, v33, v5 offset1:1\n"
    "s_mov_b64 exec, -1\n"
    "v_pk_mul_f32 v[10:11], v[10:11], v[12:13] op_sel_hi:[1,0]\n"
    "ds_write_b16 v22, v5 offset:3328\n"
    "ds_write_b16_d16_hi v23, v5 offset:3328\n"
    "v_cvt_pk_f16_f32 v8, v8, v9\n"
    "ds_write_b16 v20, v4 offset:3328\n"
    "ds_write_b16_d16_hi v21, v4 offset:3328\n"
    "v_cvt_pk_f16_f32 v9, v10, v11\n"
    "s_mov_b64 exec, s[50:51]\n"
    "ds_write_b16 v24, v8 offset:3328\n"
    "ds_write_b16_d16_hi v25, v8 offset:3328\n"
    "ds_write_b16 v26, v9 offset:3328\n"
    "ds_write_b16_d16_hi v27, v9 offset:3328\n"
    "s_mov_b64 exec, 1\n"
    "ds_add_u32 v14, v15 offset:224\n"
    "s_mov_b64 exec, -1\n"
    "s_branch KT3\n"
    "KT2:\n"
    "s_nop 2\n"
    "v_pk_mul_f32 v[4:5], v[4:5], v[12:13] op_sel_hi:[1,0]\n"
    "v_pk_mul_f32 v[6:7], v[6:7], v[12:13] op_sel_hi:[1,0]\n"
    "v_cvt_pk_f16_f32 v4, v4, v5\n"
    "v_cvt_pk_f16_f32 v5, v6, v7\n"
    "v_pk_mul_f32 v[8:9], v[8:9], v[12:13] op_sel_hi:[1,0]\n"
    "v_perm_b32 v6, v5, v4, v17\n"
    "v_perm_b32 v7, v5, v4, v18\n"
    "v_perm_b32 v32, v5, v4, v30\n"
    "v_perm_b32 v33, v5, v5, s46\n"
    "v_perm_b32 v4, v5, v4, v19\n"
    "s_mov_b32 exec_hi, s50\n"
    "ds_write_b64 v16, v[6:7] offset:6656\n"
    "s_mov_b32 exec_lo, 0\n"
    "ds_write2_b32 v28, v6, v32 offset1:1\n"
    "ds_write2_b32 v29, v33, v5 offset1:1\n"
    "s_mov_b64 exec, -1\n"
    "v_pk_mul_f32 v[10:11], v[10:11], v[12:13] op_sel_hi:[1,0]\n"
    "ds_write_b16 v22, v5 offset:6656\n"
    "ds_write_b16_d16_hi v23, v5 offset:6656\n"
    "v_cvt_pk_f16_f32 v8, v8, v9\n"
    "ds_write_b16 v20, v4 offset:6656\n"
    "ds_write_b16_d16_hi v21, v4 offset:6656\n"
    "v_cvt_pk_f16_f32 v9, v10, v11\n"
    "s_mov_b64 exec, s[50:51]\n"
    "ds_write_b16 v24, v8 offset:6656\n"
    "ds_write_b16_d16_hi v25, v8 offset:6656\n"
    "ds_write_b16 v26, v9 offset:6656\n"
    "ds_write_b16_d16_hi v27, v9 offset:6656\n"
    "s_mov_b64 exec, 1\n"
    "ds_add_u32 v14, v15 offset:224\n"
    "s_mov_b64 exec, -1\n"
    "KT3:\n"
    "s_cmp_eq_u32 s48, 0\n"
    "s_cbranch_scc1 NEAR\n"
    "s_cmpk_eq_u32 s37, 6\n"
    "s_cbranch_scc1 HELP\n"
    "s_branch FAR\n"
    "PAD2:\n"
    "s_cbranch_execz GA1\n"
    "AUX1:\n"
    "s_mov_b64 exec, -1\n"
    "v_lshlrev_b32 v1, 4, v0\n"
    "v_add_u32 v2, 78080, v1\n"
    "ds_write_b128 v2, v[4:7] offset:0\n"
    "ds_write_b128 v2, v[4:7] offset:3072\n"
    "ds_write_b128 v2, v[4:7] offset:6144\n"
    "v_min_u32 v2, 92144, v2\n"
    "ds_write_b128 v2, v[4:7] offset:9216\n"
    "s_mul_i32 s42, s37, 4576\n"
    "s_add_u32 s42, s42, 61344\n"
    "v_and_b32 v8, 63, v0\n"
    "v_min_u32 v2, 29, v8\n"
    "v_lshl_add_u32 v2, v2, 4, s42\n"
    "ds_write_b128 v2, v[4:7]\n"
    "v_mov_b32 v15, 1\n"
    "v_mov_b32 v14, 147968\n"
    "s_waitcnt lgkmcnt(0)\n"
    "s_load_dword s36, s[16:17], 0\n"
    "s_barrier\n"
    "s_cmpk_gt_u32 s37, 13\n"
    "s_cbranch_scc1 AUXB\n"
    "s_getpc_b64 s[44:45]\n"
    "s_add_u32 s44, s44, mega_tab@rel32@lo+4\n"
    "s_addc_u32 s45, s45, mega_tab@rel32@hi+12\n"
    "s_mov_b32 m0, 107520\n"
    "v_lshlrev_b32 v2, 4, v8\n"
    "v_add_u32 v3, 6144, v2\n"
    "global_load_lds_dwordx4 v3, s[44:45] offset:0\n"
    "global_load_lds_dwordx4 v3, s[44:45] offset:1024\n"
    "global_load_lds_dwordx4 v3, s[44:45] offset:2048\n"
    "global_load_lds_dwordx4 v3, s[44:45] offset:3072\n"
    "s_mov_b32 m0, 111616\n"
    "v_add_u32 v3, 4096, v3\n"
    "global_load_lds_dwordx4 v3, s[44:45] offset:0\n"
    "global_load_lds_dwordx4 v3, s[44:45] offset:1024\n"
    "global_load_lds_dwordx4 v3, s[44:45] offset:2048\n"
    "global_load_lds_dwordx4 v3, s[44:45] offset:3072\n"
    "s_mov_b32 m0, 101376\n"
    "s_nop 0\n"
    "global_load_lds_dwordx4 v2, s[44:45] offset:0\n"
    "global_load_lds_dwordx4 v2, s[44:45] offset:1024\n"
    "global_load_lds_dwordx4 v2, s[44:45] offset:2048\n"
    "global_load_lds_dwordx4 v2, s[44:45] offset:3072\n"
    "s_mov_b32 m0, 105472\n"
    "v_add_u32 v2, 4096, v2\n"
    "global_load_lds_dwordx4 v2, s[44:45] offset:0\n"
    "global_load_lds_dwordx4 v2, s[44:45] offset:1024\n"
    "s_mov_b64 exec, 0\n"
    "CH13:\n"
    "s_branch CH13_0\n"
    "AUXB:\n"
    "s_lshl_b32 s42, s37, 9\n"
    "s_add_u32 s42, s42, 53760\n"
    "v_lshl_add_u32 v2, v8, 3, s42\n"
    "v_add_u32 v3, 116128, v1\n"
    "s_lshl_b32 s42, s37, 2\n"
    "s_add_u32 s42, s42, 147912\n"
    "v_mov_b32 v12, s42\n"
    "v_mov_b32 v13, 147976\n"
    "s_branch E_10\n"
    "R_9:\n"
    "s_sleep 1\n"
    "s_sub_u32 s39, s39, 1\n"
    "s_cbranch_scc1 ABORT\n"
    "E_10:\n"
    "ds_read_b32 v20, v12 offset:4\n"
    "ds_read_b128 v[16:19], v3\n"
    "s_waitcnt lgkmcnt(0)\n"
    "v_cmp_ne_u32 vcc, 1, v20\n"
    "s_cbranch_vccnz R_9\n"
    "s_add_u32 s36, s36, 1\n"
    "s_setprio 1\n"
    "s_branch BSIDE\n"
    "AUX2:\n"
    "s_mov_b64 exec, -1\n"
    "s_waitcnt vmcnt(0) lgkmcnt(0)\n"
    "s_add_u32 s36, s36, 1\n"
    "s_mov_b64 exec, 1\n"
    "ds_add_u32 v14, v15 offset:24\n"
    "s_mov_b64 exec, -1\n"
    "AUX3:\n"
    "s_setprio 0\n"
    "s_and_b32 s44, s2, 7\n"
    "s_lshr_b32 s45, s2, 3\n"
    "s_mul_i32 s44, s44, 24\n"
    "s_add_u32 s38, s44, s45\n"
    "s_mul_i32 s42, s38, 6\n"
    "s_and_b32 s42, s42, -4\n"
    "s_load_dwordx2 s[52:53], s[24:25], s42\n"
    "v_mov_b32 v15, 1\n"
    "v_mov_b32 v14, 147968\n"
    "ds_read_b32 v2, v14 offset:480\n"
    "s_waitcnt lgkmcnt(0)\n"
    "v_readfirstlane_b32 s96, v2\n"
    "s_cmpk_lg_u32 s37, 13\n"
    "s_cbranch_scc1 STAGE\n"
    "s_setprio 3\n"
    "v_and_b32 v2, 15, v0\n"
    "v_bfe_u32 v3, v0, 4, 2\n"
    "v_mul_u32_u24 v3, 4608, v3\n"
    "s_movk_i32 s43, 13824\n"
    "v_mad_u32_u24 v3, v2, s43, v3\n"
    "s_mul_i32 s42, s38, 24\n"
    "s_add_u32 s42, s42, 10519552\n"
    "v_lshlrev_b32 v2, 1, v2\n"
    "v_add_u32 v2, 148352, v2\n"
    "s_mov_b32 s43, 0x42000000\n"
    "s_mov_b32 s46, 0x01ff01ff\n"
    "s_movk_i32 s47, 0x01ff\n"
    "v_mov_b32 v8, 0\n"
    "s_mov_b64 exec, s[46:47]\n"
    "U_11:\n"
    "s_memrealtime s[98:99]\n"
    "s_waitcnt lgkmcnt(0)\n"
    "s_sub_u32 s98, s98, s96\n"
    "s_cmpk_lt_u32 s98, 203\n"
    "s_cbranch_scc1 U_11\n"
    "s_branch E_13\n"
    "R_12:\n"
    "s_sleep 2\n"
    "s_sub_u32 s39, s39, 1\n"
    "s_cbranch_scc1 ABORT\n"
    "E_13:\n"
    "buffer_load_dwordx4 v[4:7], v3, s[16:19], s42 offen sc1\n"
    "buffer_load_dwordx2 v[8:9], v3, s[16:19], s42 offen offset:16 sc1\n"
    "s_waitcnt vmcnt(0)\n"
    "v_bitop3_b32 v10, v5, v7, s36 bitop3:0x7e\n"
    "v_bitop3_b32 v10, v10, v9, s36 bitop3:0xf6\n"
    "v_cmp_ne_u32 vcc, 0, v10\n"
    "s_cbranch_vccnz R_12\n"
    "s_mov_b64 exec, -1\n"
    "v_add_f32_dpp v4, v4, v4 row_ror:8 row_mask:0xf bank_mask:0xf\n"
    "v_add_f32_dpp v6, v6, v6 row_ror:8 row_mask:0xf bank_mask:0xf\n"
    "v_add_f32_dpp v8, v8, v8 row_ror:8 row_mask:0xf bank_mask:0xf\n"
    "v_add_f32_dpp v4, v4, v4 row_ror:4 row_mask:0xf bank_mask:0xf\n"
    "v_add_f32_dpp v6, v6, v6 row_ror:4 row_mask:0xf bank_mask:0xf\n"
    "v_add_f32_dpp v8, v8, v8 row_ror:4 row_mask:0xf bank_mask:0xf\n"
    "s_nop 0\n"
    "v_mov_b32_dpp v4, v6 quad_perm:[0,1,2,3] row_mask:0xf bank_mask:0x2\n"
    "s_nop 1\n"
    "v_mov_b32_dpp v4, v8 quad_perm:[0,1,2,3] row_mask:0xf bank_mask:0x4\n"
    "s_nop 1\n"
    "v_add_f32_dpp v4, v4, v4 quad_perm:[1,0,3,2] row_mask:0xf bank_mask:0xf\n"
    "s_nop 1\n"
    "v_add_f32_dpp v4, v4, v4 quad_perm:[2,3,0,1] row_mask:0xf bank_mask:0xf\n"
    "v_cvt_pk_bf16_f32 v5, 0, v4\n"
    "v_add_f32 v4, 1.0, v5\n"
    "v_cvt_pk_bf16_f32 v4, 0, v4\n"
    "v_mov_b32 v9, v4\n"
    "s_nop 1\n"
    "v_permlane16_swap_b32 v9, v4\n"
    "v_fma_f32 v6, abs(v4), s43, abs(v5)\n"
    "v_frexp_exp_i32_f32 v6, v6\n"
    "v_sub_u32 v7, 11, v6\n"
    "v_ldexp_f32 v10, v4, v7\n"
    "v_ldexp_f32 v11, v5, v7\n"
    "s_mov_b64 exec, 0x0111\n"
    "ds_write_b64 v2, v[10:11]\n"
    "ds_write_b32 v14, v15 offset:40\n"
    "v_subrev_u32 v8, 11, v6\n"
    "v_ldexp_f32 v8, 1.0, v8\n"
    "ds_write_b32 v2, v8 offset:32\n"
    "s_mov_b32 exec_lo, 0\n"
    "s_mov_b32 exec_hi, 0x0111\n"
    "ds_write_b32 v2, v5 offset:36\n"
    "s_mov_b64 exec, -1\n"
    "s_setprio 0\n"
    "STAGE:\n"
    "s_and_b32 s42, s38, 1\n"
    "s_mul_i32 s42, s42, 6144\n"
    "s_lshr_b32 s43, s38, 1\n"
    "s_lshl_b32 s43, s43, 6\n"
    "s_add_u32 s42, s42, s43\n"
    "PAD3:\n"
    "s_cbranch_execz GA1\n"
    "s_add_u32 s42, s42, 7341056\n"
    "s_add_u32 s43, s42, 1589248\n"
    "v_bfe_u32 v2, v0, 2, 7\n"
    "v_mul_u32_u24 v2, 12416, v2\n"
    "v_and_b32 v3, 3, v0\n"
    "v_lshl_add_u32 v2, v3, 4, v2\n"
    "U_14:\n"
    "s_memrealtime s[98:99]\n"
    "s_waitcnt lgkmcnt(0)\n"
    "s_sub_u32 s98, s98, s96\n"
    "s_cmpk_lt_u32 s98, 218\n"
    "s_cbranch_scc1 U_14\n"
    "buffer_load_dwordx4 v[4:7], v2, s[16:19], s42 offen sc1\n"
    "buffer_load_dwordx4 v[8:11], v2, s[16:19], s43 offen sc1\n"
    "v_and_b32 v3, 511, v0\n"
    "v_lshlrev_b32 v3, 2, v3\n"
    "v_sub_u32 v3, 118780, v3\n"
    "v_mov_b32 v12, 148352\n"
    "s_lshr_b32 s50, s2, 3\n"
    "s_and_b32 s51, s2, 7\n"
    "v_and_b32 v47, 63, v0\n"
    "s_mov_b64 s[60:61], 0\n"
    "s_cmpk_lt_u32 s37, 4\n"
    "s_cbranch_scc1 PFA\n"
    "s_mul_i32 s51, s51, 96\n"
    "s_lshl2_add_u32 s51, s50, s51\n"
    "s_add_u32 s51, s51, s37\n"
    "s_sub_u32 s51, s51, 12\n"
    "s_mul_i32 s51, s51, 6912\n"
    "v_lshl_add_u32 v47, v47, 7, s51\n"
    "s_mov_b64 s[58:59], s[32:33]\n"
    "s_mov_b32 s62, -1\n"
    "s_mov_b32 s63, 4194303\n"
    "s_branch PFC\n"
    "PFA:\n"
    "s_cmpk_ge_u32 s37, 2\n"
    "s_cbranch_scc1 PFB\n"
    "s_sub_u32 s51, 8, s37\n"
    "s_lshr_b32 s44, s28, 7\n"
    "s_add_u32 s44, s44, s2\n"
    "s_sub_u32 s44, 3, s44\n"
    "s_and_b32 s44, s44, 7\n"
    "s_add_u32 s44, s44, 16\n"
    "s_sub_u32 s44, s44, s51\n"
    "s_mul_i32 s45, s44, 57\n"
    "s_lshr_b32 s45, s45, 9\n"
    "s_mul_i32 s45, s45, 9\n"
    "s_sub_u32 s44, s44, s45\n"
    "s_and_b32 s51, s2, 7\n"
    "s_mul_i32 s51, s51, 9\n"
    "s_add_u32 s51, s51, s44\n"
    "s_lshl_b32 s51, s51, 7\n"
    "s_cmpk_eq_u32 s37, 0\n"
    "s_cselect_b32 s44, 24, 16\n"
    "s_cselect_b32 s45, 0, 192\n"
    "s_cselect_b32 s62, 0xffffff, 16\n"
    "s_mul_i32 s50, s50, s44\n"
    "s_add_u32 s50, s50, s45\n"
    "s_nop 0\n"
    "s_cmp_eq_u32 s62, 16\n"
    "s_cselect_b32 s62, 0xffff, s62\n"
    "s_mov_b32 s63, 0\n"
    "s_mul_i32 s50, s50, 9216\n"
    "s_add_u32 s51, s51, s50\n"
    "v_mul_u32_u24 v47, 9216, v47\n"
    "v_add_u32 v47, s51, v47\n"
    "s_branch PFD\n"
    "PFB:\n"
    "s_lshl_b32 s50, s50, 1\n"
    "s_add_u32 s50, s50, s37\n"
    "s_mul_i32 s50, s50, 18432\n"
    "s_add_u32 s50, s50, 4386816\n"
    "s_mul_i32 s51, s51, 1152\n"
    "s_add_u32 s51, s51, s50\n"
    "v_mul_u32_u24 v48, 57, v47\n"
    "v_lshrrev_b32 v48, 9, v48\n"
    "v_mul_u32_u24 v48, 8064, v48\n"
    "v_lshl_add_u32 v47, v47, 7, v48\n"
    "v_add_u32 v47, s51, v47\n"
    "s_mov_b32 s62, 0x3ffff\n"
    "s_mov_b32 s63, 0x0\n"
    "PFD:\n"
    "s_mov_b64 s[58:59], s[28:29]\n"
    "s_mov_b64 s[60:61], s[62:63]\n"
    "PFC:\n"
    "v_add_u32 v40, 192, v0\n"
    "v_min_u32 v40, 287, v40\n"
    "v_lshlrev_b32 v41, 2, v0\n"
    "v_lshlrev_b32 v42, 2, v40\n"
    "v_lshlrev_b32 v43, 4, v0\n"
    "v_lshlrev_b32 v44, 4, v40\n"
    "s_mov_b32 s44, 34304\n"
    "s_mov_b32 s45, 61952\n"
    "s_mov_b32 s46, 38912\n"
    "s_mov_b32 s47, 66560\n"
    "s_mov_b32 s48, 43520\n"
    "s_mov_b32 s49, 71168\n"
    "s_branch E_16\n"
    "R_15:\n"
    "s_sleep 1\n"
    "s_sub_u32 s39, s39, 1\n"
    "s_cbranch_scc1 ABORT\n"
    "E_16:\n"
    "ds_read_b32 v13, v14 offset:40\n"
    "ds_read_b128 v[16:19], v12\n"
    "ds_read_b64 v[20:21], v12 offset:16\n"
    "s_waitcnt lgkmcnt(0)\n"
    "v_cmp_ne_u32 vcc, 1, v13\n"
    "s_cbranch_vccnz R_15\n"
    "s_branch E_18\n"
    "R_17:\n"
    "s_sub_u32 s39, s39, 1\n"
    "s_cbranch_scc1 ABORT\n"
    "s_sleep 2\n"
    "s_wqm_b64 exec, vcc\n"
    "buffer_load_dwordx4 v[4:7], v2, s[16:19], s42 offen sc1\n"
    "buffer_load_dwordx4 v[8:11], v2, s[16:19], s43 offen sc1\n"
    "s_mov_b64 exec, -1\n"
    "E_18:\n"
    "s_waitcnt vmcnt(0)\n"
    "v_bitop3_b32 v13, v7, v11, s36 bitop3:0x7e\n"
    "v_cmp_ne_u32 vcc, 0, v13\n"
    "s_cbranch_vccnz R_17\n"
    "v_lshlrev_b32 v24, 16, v4\n"
    "v_and_b32 v25, 0xffff0000, v4\n"
    "v_lshlrev_b32 v26, 16, v5\n"
    "v_and_b32 v27, 0xffff0000, v5\n"
    "v_lshlrev_b32 v28, 16, v6\n"
    "v_and_b32 v29, 0xffff0000, v6\n"
    "v_pk_mul_f32 v[24:25], v[24:25], v[16:17] op_sel_hi:[1,0]\n"
    "v_pk_mul_f32 v[26:27], v[26:27], v[18:19] op_sel_hi:[1,0]\n"
    "v_pk_mul_f32 v[28:29], v[28:29], v[20:21] op_sel_hi:[1,0]\n"
    "v_cvt_pk_bf16_f32 v24, 0, v24\n"
    "v_cvt_pk_bf16_f32 v25, 0, v25\n"
    "v_cvt_pk_bf16_f32 v26, 0, v26\n"
    "v_cvt_pk_bf16_f32 v27, 0, v27\n"
    "v_cvt_pk_bf16_f32 v28, 0, v28\n"
    "v_cvt_pk_bf16_f32 v29, 0, v29\n"
    "v_pk_add_f32 v[24:25], v[16:17], v[24:25] op_sel:[1,0] op_sel_hi:[1,1]\n"
    "v_pk_add_f32 v[26:27], v[18:19], v[26:27] op_sel:[1,0] op_sel_hi:[1,1]\n"
    "v_pk_add_f32 v[28:29], v[20:21], v[28:29] op_sel:[1,0] op_sel_hi:[1,1]\n"
    "v_cvt_pk_bf16_f32 v24, 0, v24\n"
    "v_cvt_pk_bf16_f32 v25, 0, v25\n"
    "v_cvt_pk_bf16_f32 v26, 0, v26\n"
    "v_cvt_pk_bf16_f32 v27, 0, v27\n"
    "v_cvt_pk_bf16_f32 v28, 0, v28\n"
    "v_cvt_pk_bf16_f32 v29, 0, v29\n"
    "v_cvt_pkrtz_f16_f32 v30, v25, v24\n"
    "ds_write_b32 v3, v30 offset:2048\n"
    "v_cvt_pkrtz_f16_f32 v31, v27, v26\n"
    "ds_write_b32 v3, v31 offset:6624\n"
    "v_cvt_pkrtz_f16_f32 v32, v29, v28\n"
    "ds_write_b32 v3, v32 offset:11200\n"
    "v_lshlrev_b32 v24, 16, v8\n"
    "v_and_b32 v25, 0xffff0000, v8\n"
    "v_lshlrev_b32 v26, 16, v9\n"
    "v_and_b32 v27, 0xffff0000, v9\n"
    "v_lshlrev_b32 v28, 16, v10\n"
    "v_and_b32 v29, 0xffff0000, v10\n"
    "v_pk_mul_f32 v[24:25], v[24:25], v[16:17] op_sel_hi:[1,0]\n"
    "v_pk_mul_f32 v[26:27], v[26:27], v[18:19] op_sel_hi:[1,0]\n"
    "v_pk_mul_f32 v[28:29], v[28:29], v[20:21] op_sel_hi:[1,0]\n"
    "v_cvt_pk_bf16_f32 v24, 0, v24\n"
    "v_cvt_pk_bf16_f32 v25, 0, v25\n"
    "v_cvt_pk_bf16_f32 v26, 0, v26\n"
    "v_cvt_pk_bf16_f32 v27, 0, v27\n"
    "v_cvt_pk_bf16_f32 v28, 0, v28\n"
    "v_cvt_pk_bf16_f32 v29, 0, v29\n"
    "v_pk_add_f32 v[24:25], v[16:17], v[24:25] op_sel:[1,0] op_sel_hi:[1,1]\n"
    "v_pk_add_f32 v[26:27], v[18:19], v[26:27] op_sel:[1,0] op_sel_hi:[1,1]\n"
    "v_pk_add_f32 v[28:29], v[20:21], v[28:29] op_sel:[1,0] op_sel_hi:[1,1]\n"
    "PAD4:\n"
    "s_cbranch_execz GA1\n"
    "v_cvt_pk_bf16_f32 v24, 0, v24\n"
    "v_cvt_pk_bf16_f32 v25, 0, v25\n"
    "v_cvt_pk_bf16_f32 v26, 0, v26\n"
    "v_cvt_pk_bf16_f32 v27, 0, v27\n"
    "v_cvt_pk_bf16_f32 v28, 0, v28\n"
    "v_cvt_pk_bf16_f32 v29, 0, v29\n"
    "v_cvt_pkrtz_f16_f32 v33, v25, v24\n"
    "ds_write_b32 v3, v33 offset:0\n"
    "v_cvt_pkrtz_f16_f32 v34, v27, v26\n"
    "ds_write_b32 v3, v34 offset:4576\n"
    "v_cvt_pkrtz_f16_f32 v35, v29, v28\n"
    "ds_write_b32 v3, v35 offset:9152\n"
    "s_cmpk_lt_u32 s37, 3\n"
    "s_cbranch_scc1 MISC\n"
    "s_waitcnt lgkmcnt(0)\n"
    "s_barrier\n"
    "s_mov_b32 m0, 148480\n"
    "s_mov_b64 exec, s[62:63]\n"
    "s_cbranch_execz PFX_19\n"
    "global_load_lds_dword v47, s[58:59]\n"
    "s_mov_b64 exec, s[60:61]\n"
    "s_cbranch_execz PFX_19\n"
    "global_load_lds_dword v47, s[30:31]\n"
    "PFX_19:\n"
    "s_mov_b64 exec, -1\n"
    "s_cmpk_ge_u32 s37, 12\n"
    "s_cbranch_scc1 EPI\n"
    "FAR:\n"
    "s_lshr_b32 s42, s37, 2\n"
    "v_and_b32 v6, 15, v0\n"
    "v_bfe_u32 v7, v0, 4, 2\n"
    "v_and_b32 v1, 63, v0\n"
    "v_lshlrev_b32 v1, 4, v1\n"
    "v_add_u32 v1, 101376, v1\n"
    "s_mul_i32 s43, s42, 4576\n"
    "s_add_u32 s43, s43, 117216\n"
    "v_lshlrev_b32 v2, 1, v6\n"
    "v_sub_u32 v2, v7, v2\n"
    "v_lshl_add_u32 v2, v2, 4, s43\n"
    "s_mul_i32 s44, s42, 3328\n"
    "s_add_u32 s43, s44, 92596\n"
    "v_lshlrev_b32 v3, 2, v7\n"
    "v_sub_u32 v3, v6, v3\n"
    "v_lshl_add_u32 v3, v3, 2, s43\n"
    "s_mul_i32 s43, s42, 7680\n"
    "s_add_u32 s43, s43, 28160\n"
    "v_mul_u32_u24 v4, 320, v7\n"
    "v_lshl_add_u32 v4, v6, 4, v4\n"
    "v_add_u32 v4, s43, v4\n"
    "s_add_u32 s43, s42, 147968\n"
    "v_mov_b32 v5, s43\n"
    "v_mov_b32 v8, 1\n"
    "v_mov_b32 v11, 147968\n"
    "s_branch E_21\n"
    "R_20:\n"
    "s_sleep 1\n"
    "s_sub_u32 s39, s39, 1\n"
    "s_cbranch_scc1 ABORT\n"
    "E_21:\n"
    "ds_read_b32 v12, v11 offset:224\n"
    "s_waitcnt lgkmcnt(0)\n"
    "v_cmp_ne_u32 vcc, 6, v12\n"
    "s_cbranch_vccnz R_20\n"
    "ds_read_b128 v[12:15], v1 offset:0\n"
    "ds_read_b128 v[16:19], v1 offset:1024\n"
    "ds_read_b128 v[20:23], v1 offset:2048\n"
    "ds_read2_b32 v[24:25], v3 offset0:0 offset1:1\n"
    "ds_read2_b32 v[26:27], v3 offset0:2 offset1:3\n"
    "ds_read2_b32 v[36:37], v3 offset0:64 offset1:65\n"
    "ds_read2_b32 v[38:39], v3 offset0:66 offset1:67\n"
    "ds_read2_b32 v[28:29], v3 offset0:16 offset1:17\n"
    "ds_read2_b32 v[30:31], v3 offset0:18 offset1:19\n"
    "ds_read2_b32 v[40:41], v3 offset0:80 offset1:81\n"
    "ds_read2_b32 v[42:43], v3 offset0:82 offset1:83\n"
    "ds_read2_b32 v[32:33], v3 offset0:32 offset1:33\n"
    "ds_read2_b32 v[34:35], v3 offset0:34 offset1:35\n"
    "ds_read2_b32 v[44:45], v3 offset0:96 offset1:97\n"
    "ds_read2_b32 v[46:47], v3 offset0:98 offset1:99\n"
    "s_cmpk_eq_u32 s37, 3\n"
    "s_cbranch_scc1 FAR1\n"
    "s_setprio 2\n"
    "s_waitcnt lgkmcnt(0)\n"
    "s_barrier\n"
    "s_branch FAR2\n"
    "FAR1:\n"
    "s_setprio 1\n"
    "s_waitcnt lgkmcnt(0)\n"
    "FAR2:\n"
    "ds_read_b128 v[48:51], v2 offset:3584\n"
    "ds_read_b128 v[52:55], v2 offset:3600\n"
    "ds_read_b128 v[56:59], v2 offset:3072\n"
    "ds_read_b128 v[60:63], v2 offset:3088\n"
    "ds_read_b128 v[64:67], v2 offset:2560\n"
    "ds_read_b128 v[68:71], v2 offset:2576\n"
    "s_waitcnt lgkmcnt(4)\n"
    "v_mfma_f32_16x16x32_f16 v[72:75], v[48:51], v[12:15], 0\n"
    "v_mfma_f32_16x16x32_f16 v[76:79], v[48:51], v[16:19], 0\n"
    "v_mfma_f32_16x16x32_f16 v[80:83], v[48:51], v[20:23], 0\n"
    "v_mfma_f32_16x16x32_f16 v[84:87], v[52:55], v[20:23], 0\n"
    "s_nop 5\n"
    "v_cvt_pk_f16_f32 v88, v75, v79\n"
    "v_cvt_pk_f16_f32 v89, v74, v78\n"
    "v_cvt_pk_f16_f32 v90, v73, v77\n"
    "v_cvt_pk_f16_f32 v91, v72, v76\n"
    "v_cvt_pk_f16_f32 v92, v83, v87\n"
    "v_cvt_pk_f16_f32 v93, v82, v86\n"
    "v_cvt_pk_f16_f32 v94, v81, v85\n"
    "v_cvt_pk_f16_f32 v95, v80, v84\n"
    "s_waitcnt lgkmcnt(2)\n"
    "v_mfma_f32_16x16x32_f16 v[72:75], v[56:59], v[12:15], 0\n"
    "v_mfma_f32_16x16x32_f16 v[76:79], v[56:59], v[16:19], 0\n"
    "v_mfma_f32_16x16x32_f16 v[80:83], v[56:59], v[20:23], 0\n"
    "v_mfma_f32_16x16x32_f16 v[84:87], v[60:63], v[20:23], 0\n"
    "ds_read_b128 v[48:51], v2 offset:2048\n"
    "ds_read_b128 v[52:55], v2 offset:2064\n"
    "v_mfma_f32_16x16x32_f16 v[96:99], v[88:91], v[24:27], 0\n"
    "v_mfma_f32_16x16x32_f16 v[96:99], v[92:95], v[36:39], v[96:99]\n"
    "v_mfma_f32_16x16x32_f16 v[100:103], v[88:91], v[28:31], 0\n"
    "v_mfma_f32_16x16x32_f16 v[100:103], v[92:95], v[40:43], v[100:103]\n"
    "v_mfma_f32_16x16x32_f16 v[104:107], v[88:91], v[32:35], 0\n"
    "v_mfma_f32_16x16x32_f16 v[104:107], v[92:95], v[44:47], v[104:107]\n"
    "v_cvt_pk_f16_f32 v88, v75, v79\n"
    "v_cvt_pk_f16_f32 v89, v74, v78\n"
    "v_cvt_pk_f16_f32 v90, v73, v77\n"
    "v_cvt_pk_f16_f32 v91, v72, v76\n"
    "v_cvt_pk_f16_f32 v92, v83, v87\n"
    "v_cvt_pk_f16_f32 v93, v82, v86\n"
    "v_cvt_pk_f16_f32 v94, v81, v85\n"
    "v_cvt_pk_f16_f32 v95, v80, v84\n"
    "ds_write_b128 v4, v[96:99] offset:2560\n"
    "s_mov_b64 exec, 1\n"
    "ds_write_b8 v5, v8 offset:83\n"
    "s_mov_b64 exec, -1\n"
    "s_waitcnt lgkmcnt(4)\n"
    "v_mfma_f32_16x16x32_f16 v[72:75], v[64:67], v[12:15], 0\n"
    "v_mfma_f32_16x16x32_f16 v[76:79], v[64:67], v[16:19], 0\n"
    "v_mfma_f32_16x16x32_f16 v[80:83], v[64:67], v[20:23], 0\n"
    "v_mfma_f32_16x16x32_f16 v[84:87], v[68:71], v[20:23], 0\n"
    "ds_read_b128 v[56:59], v2 offset:1536\n"
    "ds_read_b128 v[60:63], v2 offset:1552\n"
    "v_mfma_f32_16x16x32_f16 v[100:103], v[88:91], v[24:27], v[100:103]\n"
    "v_mfma_f32_16x16x32_f16 v[100:103], v[92:95], v[36:39], v[100:103]\n"
    "v_mfma_f32_16x16x32_f16 v[104:107], v[88:91], v[28:31], v[104:107]\n"
    "v_mfma_f32_16x16x32_f16 v[104:107], v[92:95], v[40:43], v[104:107]\n"
    "v_mfma_f32_16x16x32_f16 v[108:111], v[88:91], v[32:35], 0\n"
    "v_mfma_f32_16x16x32_f16 v[108:111], v[92:95], v[44:47], v[108:111]\n"
    "v_cvt_pk_f16_f32 v88, v75, v79\n"
    "v_cvt_pk_f16_f32 v89, v74, v78\n"
    "v_cvt_pk_f16_f32 v90, v73, v77\n"
    "v_cvt_pk_f16_f32 v91, v72, v76\n"
    "PAD5:\n"
    "s_cbranch_execz GA1\n"
    "v_cvt_pk_f16_f32 v92, v83, v87\n"
    "v_cvt_pk_f16_f32 v93, v82, v86\n"
    "v_cvt_pk_f16_f32 v94, v81, v85\n"
    "v_cvt_pk_f16_f32 v95, v80, v84\n"
    "ds_write_b128 v4, v[100:103] offset:3840\n"
    "s_mov_b64 exec, 1\n"
    "ds_write_b8 v5, v8 offset:91\n"
    "s_mov_b64 exec, -1\n"
    "s_waitcnt lgkmcnt(6)\n"
    "v_mfma_f32_16x16x32_f16 v[72:75], v[48:51], v[12:15], 0\n"
    "v_mfma_f32_16x16x32_f16 v[76:79], v[48:51], v[16:19], 0\n"
    "v_mfma_f32_16x16x32_f16 v[80:83], v[48:51], v[20:23], 0\n"
    "v_mfma_f32_16x16x32_f16 v[84:87], v[52:55], v[20:23], 0\n"
    "v_mfma_f32_16x16x32_f16 v[104:107], v[88:91], v[24:27], v[104:107]\n"
    "v_mfma_f32_16x16x32_f16 v[104:107], v[92:95], v[36:39], v[104:107]\n"
    "v_mfma_f32_16x16x32_f16 v[108:111], v[88:91], v[28:31], v[108:111]\n"
    "v_mfma_f32_16x16x32_f16 v[108:111], v[92:95], v[40:43], v[108:111]\n"
    "v_mfma_f32_16x16x32_f16 v[112:115], v[88:91], v[32:35], 0\n"
    "v_mfma_f32_16x16x32_f16 v[112:115], v[92:95], v[44:47], v[112:115]\n"
    "v_cvt_pk_f16_f32 v88, v75, v79\n"
    "v_cvt_pk_f16_f32 v89, v74, v78\n"
    "v_cvt_pk_f16_f32 v90, v73, v77\n"
    "v_cvt_pk_f16_f32 v91, v72, v76\n"
    "v_cvt_pk_f16_f32 v92, v83, v87\n"
    "v_cvt_pk_f16_f32 v93, v82, v86\n"
    "v_cvt_pk_f16_f32 v94, v81, v85\n"
    "v_cvt_pk_f16_f32 v95, v80, v84\n"
    "ds_write_b128 v4, v[104:107] offset:5120\n"
    "s_mov_b64 exec, 1\n"
    "ds_write_b8 v5, v8 offset:99\n"
    "s_mov_b64 exec, -1\n"
    "s_waitcnt lgkmcnt(4)\n"
    "v_mfma_f32_16x16x32_f16 v[72:75], v[56:59], v[12:15], 0\n"
    "v_mfma_f32_16x16x32_f16 v[76:79], v[56:59], v[16:19], 0\n"
    "v_mfma_f32_16x16x32_f16 v[80:83], v[56:59], v[20:23], 0\n"
    "v_mfma_f32_16x16x32_f16 v[84:87], v[60:63], v[20:23], 0\n"
    "v_mfma_f32_16x16x32_f16 v[108:111], v[88:91], v[24:27], v[108:111]\n"
    "v_mfma_f32_16x16x32_f16 v[108:111], v[92:95], v[36:39], v[108:111]\n"
    "v_mfma_f32_16x16x32_f16 v[112:115], v[88:91], v[28:31], v[112:115]\n"
    "v_mfma_f32_16x16x32_f16 v[112:115], v[92:95], v[40:43], v[112:115]\n"
    "v_mfma_f32_16x16x32_f16 v[116:119], v[88:91], v[32:35], 0\n"
    "v_mfma_f32_16x16x32_f16 v[116:119], v[92:95], v[44:47], v[116:119]\n"
    "v_cvt_pk_f16_f32 v88, v75, v79\n"
    "v_cvt_pk_f16_f32 v89, v74, v78\n"
    "v_cvt_pk_f16_f32 v90, v73, v77\n"
    "v_cvt_pk_f16_f32 v91, v72, v76\n"
    "v_cvt_pk_f16_f32 v92, v83, v87\n"
    "v_cvt_pk_f16_f32 v93, v82, v86\n"
    "v_cvt_pk_f16_f32 v94, v81, v85\n"
    "v_cvt_pk_f16_f32 v95, v80, v84\n"
    "ds_write_b128 v4, v[108:111] offset:6400\n"
    "s_mov_b64 exec, 1\n"
    "ds_write_b8 v5, v8 offset:107\n"
    "s_mov_b64 exec, -1\n"
    "v_mfma_f32_16x16x32_f16 v[112:115], v[88:91], v[24:27], v[112:115]\n"
    "v_mfma_f32_16x16x32_f16 v[112:115], v[92:95], v[36:39], v[112:115]\n"
    "v_mfma_f32_16x16x32_f16 v[116:119], v[88:91], v[28:31], v[116:119]\n"
    "v_mfma_f32_16x16x32_f16 v[116:119], v[92:95], v[40:43], v[116:119]\n"
    "s_nop 5\n"
    "ds_write_b128 v4, v[112:115] offset:7680\n"
    "s_mov_b64 exec, 1\n"
    "ds_write_b8 v5, v8 offset:115\n"
    "s_mov_b64 exec, -1\n"
    "ds_write_b128 v4, v[116:119] offset:8960\n"
    "s_mov_b64 exec, 1\n"
    "ds_write_b8 v5, v8 offset:123\n"
    "s_mov_b64 exec, -1\n"
    "s_setprio 0\n"
    "s_branch LN2\n"
    "HELP:\n"
    "s_sub_u32 s42, s37, 4\n"
    "v_and_b32 v6, 15, v0\n"
    "v_bfe_u32 v7, v0, 4, 2\n"
    "v_and_b32 v1, 63, v0\n"
    "v_lshlrev_b32 v1, 4, v1\n"
    "v_add_u32 v1, 101376, v1\n"
    "s_mul_i32 s43, s42, 4576\n"
    "s_add_u32 s43, s43, 117216\n"
    "v_lshlrev_b32 v2, 1, v6\n"
    "v_sub_u32 v2, v7, v2\n"
    "v_lshl_add_u32 v2, v2, 4, s43\n"
    "s_mul_i32 s44, s42, 3328\n"
    "s_add_u32 s43, s44, 92596\n"
    "v_lshlrev_b32 v3, 2, v7\n"
    "v_sub_u32 v3, v6, v3\n"
    "v_lshl_add_u32 v3, v3, 2, s43\n"
    "s_mul_i32 s43, s42, 10240\n"
    "s_add_u32 s43, s43, 0\n"
    "v_mul_u32_u24 v4, 320, v7\n"
    "v_lshl_add_u32 v4, v6, 4, v4\n"
    "v_add_u32 v4, s43, v4\n"
    "s_add_u32 s43, s42, 147968\n"
    "v_mov_b32 v5, s43\n"
    "v_mov_b32 v8, 1\n"
    "s_add_u32 s44, s44, 92990\n"
    "v_mov_b32 v9, s44\n"
    "s_mul_i32 s43, s42, 3\n"
    "v_add_u32 v5, s43, v5\n"
    "s_mov_b32 s43, 0x01000100\n"
    "v_mov_b32 v11, 147968\n"
    "s_branch E_23\n"
    "PAD6:\n"
    "s_cbranch_execz S1\n"
    "R_22:\n"
    "s_sleep 1\n"
    "s_sub_u32 s39, s39, 1\n"
    "s_cbranch_scc1 ABORT\n"
    "E_23:\n"
    "ds_read_b32 v12, v11 offset:224\n"
    "s_waitcnt lgkmcnt(0)\n"
    "v_cmp_ne_u32 vcc, 6, v12\n"
    "s_cbranch_vccnz R_22\n"
    "ds_read_b128 v[12:15], v1 offset:3072\n"
    "ds_read_b128 v[16:19], v1 offset:4096\n"
    "ds_read_b128 v[20:23], v1 offset:5120\n"
    "ds_read_u16 v10, v9\n"
    "ds_read2_b32 v[24:25], v3 offset0:128 offset1:129\n"
    "ds_read2_b32 v[26:27], v3 offset0:130 offset1:131\n"
    "ds_read2_b32 v[28:29], v3 offset0:144 offset1:145\n"
    "ds_read2_b32 v[30:31], v3 offset0:146 offset1:147\n"
    "ds_read2_b32 v[32:33], v3 offset0:160 offset1:161\n"
    "ds_read2_b32 v[34:35], v3 offset0:162 offset1:163\n"
    "ds_read2_b32 v[36:37], v3 offset0:176 offset1:177\n"
    "ds_read2_b32 v[38:39], v3 offset0:178 offset1:179\n"
    "s_waitcnt lgkmcnt(8)\n"
    "v_perm_b32 v10, v10, v10, s43\n"
    "v_and_b32 v20, v20, v10\n"
    "v_and_b32 v21, v21, v10\n"
    "v_and_b32 v22, v22, v10\n"
    "v_and_b32 v23, v23, v10\n"
    "s_setprio 3\n"
    "s_waitcnt lgkmcnt(0)\n"
    "s_barrier\n"
    "ds_read_b128 v[40:43], v2 offset:3584\n"
    "ds_read_b128 v[44:47], v2 offset:3600\n"
    "ds_read_b128 v[48:51], v2 offset:3072\n"
    "ds_read_b128 v[52:55], v2 offset:3088\n"
    "ds_read_b128 v[56:59], v2 offset:2560\n"
    "ds_read_b128 v[60:63], v2 offset:2576\n"
    "ds_read_b128 v[64:67], v2 offset:2048\n"
    "ds_read_b128 v[68:71], v2 offset:2064\n"
    "s_waitcnt lgkmcnt(6)\n"
    "v_mfma_f32_16x16x32_f16 v[72:75], v[40:43], v[12:15], 0\n"
    "v_mfma_f32_16x16x32_f16 v[76:79], v[44:47], v[16:19], 0\n"
    "s_waitcnt lgkmcnt(4)\n"
    "v_mfma_f32_16x16x32_f16 v[80:83], v[48:51], v[12:15], 0\n"
    "v_mfma_f32_16x16x32_f16 v[84:87], v[52:55], v[16:19], 0\n"
    "s_waitcnt lgkmcnt(2)\n"
    "v_mfma_f32_16x16x32_f16 v[88:91], v[56:59], v[12:15], 0\n"
    "v_mfma_f32_16x16x32_f16 v[92:95], v[60:63], v[16:19], 0\n"
    "s_waitcnt lgkmcnt(0)\n"
    "v_mfma_f32_16x16x32_f16 v[96:99], v[64:67], v[12:15], 0\n"
    "v_mfma_f32_16x16x32_f16 v[100:103], v[68:71], v[16:19], 0\n"
    "v_mfma_f32_16x16x32_f16 v[108:111], v[20:23], v[40:43], 0\n"
    "v_mfma_f32_16x16x32_f16 v[112:115], v[20:23], v[48:51], 0\n"
    "v_mfma_f32_16x16x32_f16 v[116:119], v[20:23], v[56:59], 0\n"
    "v_mfma_f32_16x16x32_f16 v[120:123], v[20:23], v[64:67], 0\n"
    "ds_read_b128 v[40:43], v2 offset:1024\n"
    "ds_read_b128 v[44:47], v2 offset:1040\n"
    "ds_read_b128 v[48:51], v1 offset:0\n"
    "ds_read_b128 v[52:55], v1 offset:1024\n"
    "ds_read_b128 v[56:59], v1 offset:2048\n"
    "ds_read2_b32 v[60:61], v3 offset0:0 offset1:1\n"
    "ds_read2_b32 v[62:63], v3 offset0:2 offset1:3\n"
    "ds_read2_b32 v[64:65], v3 offset0:64 offset1:65\n"
    "ds_read2_b32 v[66:67], v3 offset0:66 offset1:67\n"
    "v_cvt_pk_f16_f32 v12, v75, v79\n"
    "v_cvt_pk_f16_f32 v13, v74, v78\n"
    "v_cvt_pk_f16_f32 v14, v73, v77\n"
    "v_cvt_pk_f16_f32 v15, v72, v76\n"
    "s_nop 1\n"
    "v_mfma_f32_16x16x32_f16 v[108:111], v[12:15], v[24:27], v[108:111]\n"
    "v_mfma_f32_16x16x32_f16 v[112:115], v[12:15], v[28:31], v[112:115]\n"
    "v_mfma_f32_16x16x32_f16 v[116:119], v[12:15], v[32:35], v[116:119]\n"
    "v_mfma_f32_16x16x32_f16 v[120:123], v[12:15], v[36:39], v[120:123]\n"
    "v_cvt_pk_f16_f32 v104, v83, v87\n"
    "v_cvt_pk_f16_f32 v105, v82, v86\n"
    "v_cvt_pk_f16_f32 v106, v81, v85\n"
    "v_cvt_pk_f16_f32 v107, v80, v84\n"
    "s_nop 1\n"
    "v_mfma_f32_16x16x32_f16 v[112:115], v[104:107], v[24:27], v[112:115]\n"
    "v_mfma_f32_16x16x32_f16 v[116:119], v[104:107], v[28:31], v[116:119]\n"
    "v_mfma_f32_16x16x32_f16 v[120:123], v[104:107], v[32:35], v[120:123]\n"
    "v_cvt_pk_f16_f32 v12, v91, v95\n"
    "v_cvt_pk_f16_f32 v13, v90, v94\n"
    "v_cvt_pk_f16_f32 v14, v89, v93\n"
    "v_cvt_pk_f16_f32 v15, v88, v92\n"
    "s_waitcnt lgkmcnt(4)\n"
    "v_mfma_f32_16x16x32_f16 v[72:75], v[40:43], v[48:51], 0\n"
    "v_mfma_f32_16x16x32_f16 v[76:79], v[40:43], v[52:55], 0\n"
    "v_mfma_f32_16x16x32_f16 v[80:83], v[40:43], v[56:59], 0\n"
    "v_mfma_f32_16x16x32_f16 v[84:87], v[44:47], v[56:59], 0\n"
    "v_mfma_f32_16x16x32_f16 v[116:119], v[12:15], v[24:27], v[116:119]\n"
    "v_mfma_f32_16x16x32_f16 v[120:123], v[12:15], v[28:31], v[120:123]\n"
    "v_cvt_pk_f16_f32 v104, v99, v103\n"
    "v_cvt_pk_f16_f32 v105, v98, v102\n"
    "v_cvt_pk_f16_f32 v106, v97, v101\n"
    "v_cvt_pk_f16_f32 v107, v96, v100\n"
    "s_nop 1\n"
    "v_mfma_f32_16x16x32_f16 v[120:123], v[104:107], v[24:27], v[120:123]\n"
    "v_cvt_pk_f16_f32 v12, v75, v79\n"
    "v_cvt_pk_f16_f32 v13, v74, v78\n"
    "v_cvt_pk_f16_f32 v14, v73, v77\n"
    "v_cvt_pk_f16_f32 v15, v72, v76\n"
    "v_cvt_pk_f16_f32 v16, v83, v87\n"
    "v_cvt_pk_f16_f32 v17, v82, v86\n"
    "v_cvt_pk_f16_f32 v18, v81, v85\n"
    "v_cvt_pk_f16_f32 v19, v80, v84\n"
    "s_waitcnt lgkmcnt(0)\n"
    "v_mfma_f32_16x16x32_f16 v[120:123], v[12:15], v[60:63], v[120:123]\n"
    "v_mfma_f32_16x16x32_f16 v[120:123], v[16:19], v[64:67], v[120:123]\n"
    "ds_write_b128 v4, v[108:111] offset:5120\n"
    "ds_write_b128 v4, v[112:115] offset:6400\n"
    "ds_write_b128 v4, v[116:119] offset:7680\n"
    "s_nop 4\n"
    "ds_write_b128 v4, v[120:123] offset:8960\n"
    "s_mov_b64 exec, 1\n"
    "ds_write_b8 v5, v8 offset:44\n"
    "s_mov_b64 exec, -1\n"
    "s_setprio 0\n"
    "s_branch LN2\n"
    "PAD7:\n"
    "s_cbranch_execz S1\n"
    "NEAR:\n"
    "v_and_b32 v6, 15, v0\n"
    "v_bfe_u32 v7, v0, 4, 2\n"
    "v_and_b32 v1, 63, v0\n"
    "v_lshlrev_b32 v1, 4, v1\n"
    "v_add_u32 v1, 101376, v1\n"
    "s_mul_i32 s43, s42, 4576\n"
    "s_add_u32 s43, s43, 117216\n"
    "v_lshlrev_b32 v2, 1, v6\n"
    "v_sub_u32 v2, v7, v2\n"
    "v_lshl_add_u32 v2, v2, 4, s43\n"
    "s_mul_i32 s44, s42, 3328\n"
    "s_add_u32 s43, s44, 92596\n"
    "v_lshlrev_b32 v3, 2, v7\n"
    "v_sub_u32 v3, v6, v3\n"
    "v_lshl_add_u32 v3, v3, 2, s43\n"
    "s_mul_i32 s43, s42, 10240\n"
    "s_add_u32 s43, s43, 0\n"
    "v_mul_u32_u24 v4, 320, v7\n"
    "v_lshl_add_u32 v4, v6, 4, v4\n"
    "v_add_u32 v4, s43, v4\n"
    "s_add_u32 s43, s42, 147968\n"
    "v_mov_b32 v5, s43\n"
    "v_mov_b32 v8, 1\n"
    "v_lshl_add_u32 v3, v7, 3, v6\n"
    "v_add_u32 v3, 1, v3\n"
    "v_and_b32 v9, 1, v3\n"
    "v_mul_u32_u24 v9, 2110, v9\n"
    "s_add_u32 s44, s44, 91392\n"
    "v_lshl_add_u32 v3, v3, 1, s44\n"
    "v_add_u32 v3, v3, v9\n"
    "v_add_u32 v1, 512, v3\n"
    "s_mul_i32 s43, s42, 3\n"
    "v_add_u32 v10, s43, v5\n"
    "s_setprio 2\n"
    "v_mov_b32 v11, 147968\n"
    "s_branch E_25\n"
    "R_24:\n"
    "s_sub_u32 s39, s39, 1\n"
    "s_cbranch_scc1 ABORT\n"
    "E_25:\n"
    "ds_read_b32 v12, v11 offset:224\n"
    "s_waitcnt lgkmcnt(0)\n"
    "v_cmp_ne_u32 vcc, 6, v12\n"
    "s_cbranch_vccnz R_24\n"
    "ds_read2_b32 v[88:89], v3 offset0:112 offset1:113\n"
    "ds_read2_b32 v[90:91], v3 offset0:114 offset1:115\n"
    "ds_read2_b32 v[120:121], v1 offset0:112 offset1:113\n"
    "ds_read2_b32 v[122:123], v1 offset0:114 offset1:115\n"
    "ds_read2_b32 v[84:85], v3 offset0:96 offset1:97\n"
    "ds_read2_b32 v[86:87], v3 offset0:98 offset1:99\n"
    "ds_read2_b32 v[116:117], v1 offset0:96 offset1:97\n"
    "ds_read2_b32 v[118:119], v1 offset0:98 offset1:99\n"
    "s_barrier\n"
    "ds_read_b128 v[24:27], v2 offset:4032\n"
    "ds_read_b128 v[28:31], v2 offset:3968\n"
    "ds_read_b128 v[32:35], v2 offset:3904\n"
    "s_waitcnt lgkmcnt(2)\n"
    "v_mfma_f32_16x16x32_f16 v[48:51], v[88:91], v[24:27], 0\n"
    "ds_read2_b32 v[80:81], v3 offset0:80 offset1:81\n"
    "ds_read2_b32 v[82:83], v3 offset0:82 offset1:83\n"
    "ds_read2_b32 v[112:113], v1 offset0:80 offset1:81\n"
    "ds_read2_b32 v[114:115], v1 offset0:82 offset1:83\n"
    "ds_read_b128 v[36:39], v2 offset:3840\n"
    "v_mfma_f32_16x16x32_f16 v[52:55], v[120:123], v[24:27], 0\n"
    "s_waitcnt lgkmcnt(6)\n"
    "v_mfma_f32_16x16x32_f16 v[48:51], v[84:87], v[28:31], v[48:51]\n"
    "ds_read2_b32 v[76:77], v3 offset0:64 offset1:65\n"
    "ds_read2_b32 v[78:79], v3 offset0:66 offset1:67\n"
    "ds_read2_b32 v[108:109], v1 offset0:64 offset1:65\n"
    "ds_read2_b32 v[110:111], v1 offset0:66 offset1:67\n"
    "ds_read_b128 v[40:43], v2 offset:3776\n"
    "v_mfma_f32_16x16x32_f16 v[52:55], v[116:119], v[28:31], v[52:55]\n"
    "s_waitcnt lgkmcnt(8)\n"
    "v_mfma_f32_16x16x32_f16 v[48:51], v[80:83], v[32:35], v[48:51]\n"
    "ds_read2_b32 v[72:73], v3 offset0:48 offset1:49\n"
    "ds_read2_b32 v[74:75], v3 offset0:50 offset1:51\n"
    "ds_read2_b32 v[104:105], v1 offset0:48 offset1:49\n"
    "ds_read2_b32 v[106:107], v1 offset0:50 offset1:51\n"
    "ds_read_b128 v[44:47], v2 offset:3712\n"
    "s_waitcnt lgkmcnt(11)\n"
    "v_mfma_f32_16x16x32_f16 v[52:55], v[112:115], v[32:35], v[52:55]\n"
    "s_waitcnt lgkmcnt(8)\n"
    "v_mfma_f32_16x16x32_f16 v[48:51], v[76:79], v[36:39], v[48:51]\n"
    "ds_read2_b32 v[68:69], v3 offset0:32 offset1:33\n"
    "ds_read2_b32 v[70:71], v3 offset0:34 offset1:35\n"
    "ds_read2_b32 v[100:101], v1 offset0:32 offset1:33\n"
    "ds_read2_b32 v[102:103], v1 offset0:34 offset1:35\n"
    "ds_read_b128 v[24:27], v2 offset:3648\n"
    "s_waitcnt lgkmcnt(11)\n"
    "v_mfma_f32_16x16x32_f16 v[52:55], v[108:111], v[36:39], v[52:55]\n"
    "s_waitcnt lgkmcnt(8)\n"
    "v_mfma_f32_16x16x32_f16 v[48:51], v[72:75], v[40:43], v[48:51]\n"
    "ds_read2_b32 v[64:65], v3 offset0:16 offset1:17\n"
    "ds_read2_b32 v[66:67], v3 offset0:18 offset1:19\n"
    "ds_read2_b32 v[96:97], v1 offset0:16 offset1:17\n"
    "ds_read2_b32 v[98:99], v1 offset0:18 offset1:19\n"
    "ds_read_b128 v[28:31], v2 offset:3584\n"
    "s_waitcnt lgkmcnt(11)\n"
    "v_mfma_f32_16x16x32_f16 v[52:55], v[104:107], v[40:43], v[52:55]\n"
    "s_waitcnt lgkmcnt(8)\n"
    "v_mfma_f32_16x16x32_f16 v[48:51], v[68:71], v[44:47], v[48:51]\n"
    "ds_read2_b32 v[60:61], v3 offset0:0 offset1:1\n"
    "ds_read2_b32 v[62:63], v3 offset0:2 offset1:3\n"
    "ds_read2_b32 v[92:93], v1 offset0:0 offset1:1\n"
    "ds_read2_b32 v[94:95], v1 offset0:2 offset1:3\n"
    "ds_read2_b32 v[124:125], v1 offset0:128 offset1:129\n"
    "ds_read2_b32 v[126:127], v1 offset0:130 offset1:131\n"
    "ds_read_b128 v[32:35], v2 offset:3520\n"
    "s_waitcnt lgkmcnt(13)\n"
    "v_mfma_f32_16x16x32_f16 v[52:55], v[100:103], v[44:47], v[52:55]\n"
    "s_waitcnt lgkmcnt(10)\n"
    "v_mfma_f32_16x16x32_f16 v[48:51], v[64:67], v[24:27], v[48:51]\n"
    "ds_read_b128 v[36:39], v2 offset:3456\n"
    "ds_read_b128 v[40:43], v2 offset:3392\n"
    "s_waitcnt lgkmcnt(10)\n"
    "v_mfma_f32_16x16x32_f16 v[52:55], v[96:99], v[24:27], v[52:55]\n"
    "s_waitcnt lgkmcnt(7)\n"
    "v_mfma_f32_16x16x32_f16 v[48:51], v[60:63], v[28:31], v[48:51]\n"
    "ds_read_b128 v[44:47], v2 offset:3328\n"
    "ds_read_b128 v[24:27], v2 offset:3264\n"
    "s_waitcnt lgkmcnt(7)\n"
    "v_mfma_f32_16x16x32_f16 v[52:55], v[92:95], v[28:31], v[52:55]\n"
    "s_waitcnt lgkmcnt(5)\n"
    "v_mfma_f32_16x16x32_f16 v[56:59], v[124:127], v[28:31], 0\n"
    "s_waitcnt lgkmcnt(4)\n"
    "v_mfma_f32_16x16x32_f16 v[52:55], v[88:91], v[32:35], v[52:55]\n"
    "ds_read_b128 v[28:31], v2 offset:3200\n"
    "v_mfma_f32_16x16x32_f16 v[56:59], v[120:123], v[32:35], v[56:59]\n"
    "s_waitcnt lgkmcnt(4)\n"
    "v_mfma_f32_16x16x32_f16 v[52:55], v[84:87], v[36:39], v[52:55]\n"
    "ds_read_b128 v[32:35], v2 offset:3136\n"
    "v_mfma_f32_16x16x32_f16 v[56:59], v[116:119], v[36:39], v[56:59]\n"
    "ds_write_b128 v4, v[48:51] offset:0\n"
    "s_mov_b64 exec, 1\n"
    "ds_write_b8 v5, v8 offset:64\n"
    "s_mov_b64 exec, -1\n"
    "s_waitcnt lgkmcnt(6)\n"
    "v_mfma_f32_16x16x32_f16 v[52:55], v[80:83], v[40:43], v[52:55]\n"
    "ds_read_b128 v[36:39], v2 offset:3072\n"
    "v_mfma_f32_16x16x32_f16 v[56:59], v[112:115], v[40:43], v[56:59]\n"
    "s_waitcnt lgkmcnt(6)\n"
    "v_mfma_f32_16x16x32_f16 v[52:55], v[76:79], v[44:47], v[52:55]\n"
    "ds_read_b128 v[40:43], v2 offset:3008\n"
    "v_mfma_f32_16x16x32_f16 v[56:59], v[108:111], v[44:47], v[56:59]\n"
    "s_waitcnt lgkmcnt(6)\n"
    "v_mfma_f32_16x16x32_f16 v[52:55], v[72:75], v[24:27], v[52:55]\n"
    "ds_read_b128 v[44:47], v2 offset:2944\n"
    "PAD8:\n"
    "s_cbranch_execz S1\n"
    "v_mfma_f32_16x16x32_f16 v[56:59], v[104:107], v[24:27], v[56:59]\n"
    "s_waitcnt lgkmcnt(6)\n"
    "v_mfma_f32_16x16x32_f16 v[52:55], v[68:71], v[28:31], v[52:55]\n"
    "ds_read_b128 v[24:27], v2 offset:2880\n"
    "v_mfma_f32_16x16x32_f16 v[56:59], v[100:103], v[28:31], v[56:59]\n"
    "s_waitcnt lgkmcnt(6)\n"
    "v_mfma_f32_16x16x32_f16 v[52:55], v[64:67], v[32:35], v[52:55]\n"
    "ds_read_b128 v[28:31], v2 offset:2816\n"
    "v_mfma_f32_16x16x32_f16 v[56:59], v[96:99], v[32:35], v[56:59]\n"
    "s_waitcnt lgkmcnt(4)\n"
    "v_mfma_f32_16x16x32_f16 v[52:55], v[60:63], v[36:39], v[52:55]\n"
    "ds_read_b128 v[32:35], v2 offset:2752\n"
    "v_mfma_f32_16x16x32_f16 v[56:59], v[92:95], v[36:39], v[56:59]\n"
    "v_mfma_f32_16x16x32_f16 v[48:51], v[124:127], v[36:39], 0\n"
    "s_waitcnt lgkmcnt(4)\n"
    "v_mfma_f32_16x16x32_f16 v[56:59], v[88:91], v[40:43], v[56:59]\n"
    "ds_read_b128 v[36:39], v2 offset:2688\n"
    "v_mfma_f32_16x16x32_f16 v[48:51], v[120:123], v[40:43], v[48:51]\n"
    "s_waitcnt lgkmcnt(4)\n"
    "v_mfma_f32_16x16x32_f16 v[56:59], v[84:87], v[44:47], v[56:59]\n"
    "ds_read_b128 v[40:43], v2 offset:2624\n"
    "v_mfma_f32_16x16x32_f16 v[48:51], v[116:119], v[44:47], v[48:51]\n"
    "ds_write_b128 v4, v[52:55] offset:1280\n"
    "s_mov_b64 exec, 1\n"
    "ds_write_b8 v5, v8 offset:72\n"
    "s_mov_b64 exec, -1\n"
    "ds_read_b32 v11, v10 offset:44\n"
    "s_waitcnt lgkmcnt(7)\n"
    "v_mfma_f32_16x16x32_f16 v[56:59], v[80:83], v[24:27], v[56:59]\n"
    "ds_read_b128 v[44:47], v2 offset:2560\n"
    "v_mfma_f32_16x16x32_f16 v[48:51], v[112:115], v[24:27], v[48:51]\n"
    "s_waitcnt lgkmcnt(7)\n"
    "v_mfma_f32_16x16x32_f16 v[56:59], v[76:79], v[28:31], v[56:59]\n"
    "ds_read_b128 v[24:27], v2 offset:2496\n"
    "v_mfma_f32_16x16x32_f16 v[48:51], v[108:111], v[28:31], v[48:51]\n"
    "s_waitcnt lgkmcnt(7)\n"
    "v_mfma_f32_16x16x32_f16 v[56:59], v[72:75], v[32:35], v[56:59]\n"
    "ds_read_b128 v[28:31], v2 offset:2432\n"
    "v_mfma_f32_16x16x32_f16 v[48:51], v[104:107], v[32:35], v[48:51]\n"
    "s_waitcnt lgkmcnt(3)\n"
    "v_cmp_ne_u32 vcc, 1, v11\n"
    "s_cbranch_vccnz NEARW\n"
    "NEARC:\n"
    "ds_read_b128 v[52:55], v4 offset:5120\n"
    "v_mfma_f32_16x16x32_f16 v[56:59], v[68:71], v[36:39], v[56:59]\n"
    "ds_read_b128 v[32:35], v2 offset:2368\n"
    "v_mfma_f32_16x16x32_f16 v[48:51], v[100:103], v[36:39], v[48:51]\n"
    "v_mfma_f32_16x16x32_f16 v[56:59], v[64:67], v[40:43], v[56:59]\n"
    "ds_read_b128 v[36:39], v2 offset:2304\n"
    "v_mfma_f32_16x16x32_f16 v[48:51], v[96:99], v[40:43], v[48:51]\n"
    "s_waitcnt lgkmcnt(5)\n"
    "v_mfma_f32_16x16x32_f16 v[56:59], v[60:63], v[44:47], v[56:59]\n"
    "ds_read_b128 v[40:43], v2 offset:2240\n"
    "v_mfma_f32_16x16x32_f16 v[48:51], v[92:95], v[44:47], v[48:51]\n"
    "s_waitcnt lgkmcnt(3)\n"
    "v_mfma_f32_16x16x32_f16 v[52:55], v[124:127], v[44:47], v[52:55]\n"
    "v_mfma_f32_16x16x32_f16 v[48:51], v[88:91], v[24:27], v[48:51]\n"
    "ds_read_b128 v[44:47], v2 offset:2176\n"
    "v_mfma_f32_16x16x32_f16 v[52:55], v[120:123], v[24:27], v[52:55]\n"
    "v_mfma_f32_16x16x32_f16 v[48:51], v[84:87], v[28:31], v[48:51]\n"
    "ds_read_b128 v[24:27], v2 offset:2112\n"
    "v_mfma_f32_16x16x32_f16 v[52:55], v[116:119], v[28:31], v[52:55]\n"
    "ds_write_b128 v4, v[56:59] offset:2560\n"
    "s_mov_b64 exec, 1\n"
    "ds_write_b8 v5, v8 offset:80\n"
    "s_mov_b64 exec, -1\n"
    "ds_read_b128 v[56:59], v4 offset:6400\n"
    "s_waitcnt lgkmcnt(7)\n"
    "v_mfma_f32_16x16x32_f16 v[48:51], v[80:83], v[32:35], v[48:51]\n"
    "ds_read_b128 v[28:31], v2 offset:2048\n"
    "v_mfma_f32_16x16x32_f16 v[52:55], v[112:115], v[32:35], v[52:55]\n"
    "s_waitcnt lgkmcnt(7)\n"
    "v_mfma_f32_16x16x32_f16 v[48:51], v[76:79], v[36:39], v[48:51]\n"
    "ds_read_b128 v[32:35], v2 offset:1984\n"
    "v_mfma_f32_16x16x32_f16 v[52:55], v[108:111], v[36:39], v[52:55]\n"
    "s_waitcnt lgkmcnt(7)\n"
    "v_mfma_f32_16x16x32_f16 v[48:51], v[72:75], v[40:43], v[48:51]\n"
    "ds_read_b128 v[36:39], v2 offset:1920\n"
    "v_mfma_f32_16x16x32_f16 v[52:55], v[104:107], v[40:43], v[52:55]\n"
    "s_waitcnt lgkmcnt(7)\n"
    "v_mfma_f32_16x16x32_f16 v[48:51], v[68:71], v[44:47], v[48:51]\n"
    "ds_read_b128 v[40:43], v2 offset:1856\n"
    "v_mfma_f32_16x16x32_f16 v[52:55], v[100:103], v[44:47], v[52:55]\n"
    "s_waitcnt lgkmcnt(7)\n"
    "v_mfma_f32_16x16x32_f16 v[48:51], v[64:67], v[24:27], v[48:51]\n"
    "ds_read_b128 v[44:47], v2 offset:1792\n"
    "v_mfma_f32_16x16x32_f16 v[52:55], v[96:99], v[24:27], v[52:55]\n"
    "s_waitcnt lgkmcnt(4)\n"
    "v_mfma_f32_16x16x32_f16 v[48:51], v[60:63], v[28:31], v[48:51]\n"
    "ds_read_b128 v[24:27], v2 offset:1728\n"
    "v_mfma_f32_16x16x32_f16 v[52:55], v[92:95], v[28:31], v[52:55]\n"
    "v_mfma_f32_16x16x32_f16 v[56:59], v[124:127], v[28:31], v[56:59]\n"
    "s_waitcnt lgkmcnt(4)\n"
    "v_mfma_f32_16x16x32_f16 v[52:55], v[88:91], v[32:35], v[52:55]\n"
    "ds_read_b128 v[28:31], v2 offset:1664\n"
    "v_mfma_f32_16x16x32_f16 v[56:59], v[120:123], v[32:35], v[56:59]\n"
    "s_waitcnt lgkmcnt(4)\n"
    "v_mfma_f32_16x16x32_f16 v[52:55], v[84:87], v[36:39], v[52:55]\n"
    "ds_read_b128 v[32:35], v2 offset:1600\n"
    "v_mfma_f32_16x16x32_f16 v[56:59], v[116:119], v[36:39], v[56:59]\n"
    "ds_write_b128 v4, v[48:51] offset:3840\n"
    "s_mov_b64 exec, 1\n"
    "ds_write_b8 v5, v8 offset:88\n"
    "s_mov_b64 exec, -1\n"
    "ds_read_b128 v[48:51], v4 offset:7680\n"
    "s_waitcnt lgkmcnt(7)\n"
    "v_mfma_f32_16x16x32_f16 v[52:55], v[80:83], v[40:43], v[52:55]\n"
    "ds_read_b128 v[36:39], v2 offset:1536\n"
    "v_mfma_f32_16x16x32_f16 v[56:59], v[112:115], v[40:43], v[56:59]\n"
    "s_waitcnt lgkmcnt(7)\n"
    "v_mfma_f32_16x16x32_f16 v[52:55], v[76:79], v[44:47], v[52:55]\n"
    "ds_read_b128 v[40:43], v2 offset:1472\n"
    "v_mfma_f32_16x16x32_f16 v[56:59], v[108:111], v[44:47], v[56:59]\n"
    "s_waitcnt lgkmcnt(7)\n"
    "v_mfma_f32_16x16x32_f16 v[52:55], v[72:75], v[24:27], v[52:55]\n"
    "ds_read_b128 v[44:47], v2 offset:1408\n"
    "v_mfma_f32_16x16x32_f16 v[56:59], v[104:107], v[24:27], v[56:59]\n"
    "s_waitcnt lgkmcnt(7)\n"
    "v_mfma_f32_16x16x32_f16 v[52:55], v[68:71], v[28:31], v[52:55]\n"
    "ds_read_b128 v[24:27], v2 offset:1344\n"
    "v_mfma_f32_16x16x32_f16 v[56:59], v[100:103], v[28:31], v[56:59]\n"
    "s_waitcnt lgkmcnt(7)\n"
    "v_mfma_f32_16x16x32_f16 v[52:55], v[64:67], v[32:35], v[52:55]\n"
    "ds_read_b128 v[28:31], v2 offset:1280\n"
    "v_mfma_f32_16x16x32_f16 v[56:59], v[96:99], v[32:35], v[56:59]\n"
    "s_waitcnt lgkmcnt(4)\n"
    "v_mfma_f32_16x16x32_f16 v[52:55], v[60:63], v[36:39], v[52:55]\n"
    "ds_read_b128 v[32:35], v2 offset:1216\n"
    "v_mfma_f32_16x16x32_f16 v[56:59], v[92:95], v[36:39], v[56:59]\n"
    "v_mfma_f32_16x16x32_f16 v[48:51], v[124:127], v[36:39], v[48:51]\n"
    "s_waitcnt lgkmcnt(4)\n"
    "v_mfma_f32_16x16x32_f16 v[56:59], v[88:91], v[40:43], v[56:59]\n"
    "ds_read_b128 v[36:39], v2 offset:1152\n"
    "v_mfma_f32_16x16x32_f16 v[48:51], v[120:123], v[40:43], v[48:51]\n"
    "s_waitcnt lgkmcnt(4)\n"
    "v_mfma_f32_16x16x32_f16 v[56:59], v[84:87], v[44:47], v[56:59]\n"
    "ds_read_b128 v[40:43], v2 offset:1088\n"
    "v_mfma_f32_16x16x32_f16 v[48:51], v[116:119], v[44:47], v[48:51]\n"
    "ds_write_b128 v4, v[52:55] offset:5120\n"
    "s_mov_b64 exec, 1\n"
    "ds_write_b8 v5, v8 offset:96\n"
    "s_mov_b64 exec, -1\n"
    "PAD9:\n"
    "s_cbranch_execz S1\n"
    "ds_read_b128 v[52:55], v4 offset:8960\n"
    "s_waitcnt lgkmcnt(7)\n"
    "v_mfma_f32_16x16x32_f16 v[56:59], v[80:83], v[24:27], v[56:59]\n"
    "ds_read_b128 v[44:47], v2 offset:1024\n"
    "v_mfma_f32_16x16x32_f16 v[48:51], v[112:115], v[24:27], v[48:51]\n"
    "s_waitcnt lgkmcnt(7)\n"
    "v_mfma_f32_16x16x32_f16 v[56:59], v[76:79], v[28:31], v[56:59]\n"
    "ds_read_b128 v[24:27], v2 offset:960\n"
    "v_mfma_f32_16x16x32_f16 v[48:51], v[108:111], v[28:31], v[48:51]\n"
    "s_waitcnt lgkmcnt(7)\n"
    "v_mfma_f32_16x16x32_f16 v[56:59], v[72:75], v[32:35], v[56:59]\n"
    "ds_read_b128 v[28:31], v2 offset:896\n"
    "v_mfma_f32_16x16x32_f16 v[48:51], v[104:107], v[32:35], v[48:51]\n"
    "s_waitcnt lgkmcnt(7)\n"
    "v_mfma_f32_16x16x32_f16 v[56:59], v[68:71], v[36:39], v[56:59]\n"
    "ds_read_b128 v[32:35], v2 offset:832\n"
    "v_mfma_f32_16x16x32_f16 v[48:51], v[100:103], v[36:39], v[48:51]\n"
    "s_waitcnt lgkmcnt(7)\n"
    "v_mfma_f32_16x16x32_f16 v[56:59], v[64:67], v[40:43], v[56:59]\n"
    "ds_read_b128 v[36:39], v2 offset:768\n"
    "v_mfma_f32_16x16x32_f16 v[48:51], v[96:99], v[40:43], v[48:51]\n"
    "s_waitcnt lgkmcnt(4)\n"
    "v_mfma_f32_16x16x32_f16 v[56:59], v[60:63], v[44:47], v[56:59]\n"
    "ds_read_b128 v[40:43], v2 offset:704\n"
    "v_mfma_f32_16x16x32_f16 v[48:51], v[92:95], v[44:47], v[48:51]\n"
    "v_mfma_f32_16x16x32_f16 v[52:55], v[124:127], v[44:47], v[52:55]\n"
    "s_waitcnt lgkmcnt(4)\n"
    "v_mfma_f32_16x16x32_f16 v[48:51], v[88:91], v[24:27], v[48:51]\n"
    "ds_read_b128 v[44:47], v2 offset:640\n"
    "v_mfma_f32_16x16x32_f16 v[52:55], v[120:123], v[24:27], v[52:55]\n"
    "s_waitcnt lgkmcnt(4)\n"
    "v_mfma_f32_16x16x32_f16 v[48:51], v[84:87], v[28:31], v[48:51]\n"
    "ds_read_b128 v[24:27], v2 offset:576\n"
    "v_mfma_f32_16x16x32_f16 v[52:55], v[116:119], v[28:31], v[52:55]\n"
    "ds_write_b128 v4, v[56:59] offset:6400\n"
    "s_mov_b64 exec, 1\n"
    "ds_write_b8 v5, v8 offset:104\n"
    "s_mov_b64 exec, -1\n"
    "s_waitcnt lgkmcnt(6)\n"
    "v_mfma_f32_16x16x32_f16 v[48:51], v[80:83], v[32:35], v[48:51]\n"
    "ds_read_b128 v[28:31], v2 offset:512\n"
    "v_mfma_f32_16x16x32_f16 v[52:55], v[112:115], v[32:35], v[52:55]\n"
    "s_waitcnt lgkmcnt(6)\n"
    "v_mfma_f32_16x16x32_f16 v[48:51], v[76:79], v[36:39], v[48:51]\n"
    "ds_read_b128 v[32:35], v2 offset:448\n"
    "v_mfma_f32_16x16x32_f16 v[52:55], v[108:111], v[36:39], v[52:55]\n"
    "s_waitcnt lgkmcnt(6)\n"
    "v_mfma_f32_16x16x32_f16 v[48:51], v[72:75], v[40:43], v[48:51]\n"
    "ds_read_b128 v[36:39], v2 offset:384\n"
    "v_mfma_f32_16x16x32_f16 v[52:55], v[104:107], v[40:43], v[52:55]\n"
    "s_waitcnt lgkmcnt(6)\n"
    "v_mfma_f32_16x16x32_f16 v[48:51], v[68:71], v[44:47], v[48:51]\n"
    "ds_read_b128 v[40:43], v2 offset:320\n"
    "v_mfma_f32_16x16x32_f16 v[52:55], v[100:103], v[44:47], v[52:55]\n"
    "s_waitcnt lgkmcnt(6)\n"
    "v_mfma_f32_16x16x32_f16 v[48:51], v[64:67], v[24:27], v[48:51]\n"
    "ds_read_b128 v[44:47], v2 offset:256\n"
    "v_mfma_f32_16x16x32_f16 v[52:55], v[96:99], v[24:27], v[52:55]\n"
    "s_waitcnt lgkmcnt(4)\n"
    "v_mfma_f32_16x16x32_f16 v[48:51], v[60:63], v[28:31], v[48:51]\n"
    "ds_read_b128 v[24:27], v2 offset:192\n"
    "v_mfma_f32_16x16x32_f16 v[52:55], v[92:95], v[28:31], v[52:55]\n"
    "s_waitcnt lgkmcnt(4)\n"
    "v_mfma_f32_16x16x32_f16 v[52:55], v[88:91], v[32:35], v[52:55]\n"
    "ds_read_b128 v[28:31], v2 offset:128\n"
    "s_waitcnt lgkmcnt(4)\n"
    "v_mfma_f32_16x16x32_f16 v[52:55], v[84:87], v[36:39], v[52:55]\n"
    "ds_read_b128 v[32:35], v2 offset:64\n"
    "ds_write_b128 v4, v[48:51] offset:7680\n"
    "s_mov_b64 exec, 1\n"
    "ds_write_b8 v5, v8 offset:112\n"
    "s_mov_b64 exec, -1\n"
    "s_waitcnt lgkmcnt(6)\n"
    "v_mfma_f32_16x16x32_f16 v[52:55], v[80:83], v[40:43], v[52:55]\n"
    "ds_read_b128 v[36:39], v2 offset:0\n"
    "s_waitcnt lgkmcnt(6)\n"
    "v_mfma_f32_16x16x32_f16 v[52:55], v[76:79], v[44:47], v[52:55]\n"
    "s_waitcnt lgkmcnt(5)\n"
    "v_mfma_f32_16x16x32_f16 v[52:55], v[72:75], v[24:27], v[52:55]\n"
    "s_waitcnt lgkmcnt(4)\n"
    "v_mfma_f32_16x16x32_f16 v[52:55], v[68:71], v[28:31], v[52:55]\n"
    "s_waitcnt lgkmcnt(3)\n"
    "v_mfma_f32_16x16x32_f16 v[52:55], v[64:67], v[32:35], v[52:55]\n"
    "s_waitcnt lgkmcnt(0)\n"
    "v_mfma_f32_16x16x32_f16 v[52:55], v[60:63], v[36:39], v[52:55]\n"
    "s_nop 7\n"
    "ds_write_b128 v4, v[52:55] offset:8960\n"
    "s_mov_b64 exec, 1\n"
    "ds_write_b8 v5, v8 offset:120\n"
    "s_mov_b64 exec, -1\n"
    "s_setprio 0\n"
    "s_branch LN2\n"
    "NEARW:\n"
    "s_branch E_27\n"
    "R_26:\n"
    "s_sleep 1\n"
    "s_sub_u32 s39, s39, 1\n"
    "s_cbranch_scc1 ABORT\n"
    "E_27:\n"
    "ds_read_b32 v11, v10 offset:44\n"
    "s_waitcnt lgkmcnt(0)\n"
    "v_cmp_ne_u32 vcc, 1, v11\n"
    "s_cbranch_vccnz R_26\n"
    "s_branch NEARC\n"
    "EPI:\n"
    "s_setprio 2\n"
    "v_bfe_u32 v2, v0, 3, 4\n"
    "v_bfe_u32 v4, v0, 1, 2\n"
    "v_mul_u32_u24 v4, 320, v4\n"
    "v_lshl_add_u32 v2, v2, 4, v4\n"
    "v_and_b32 v4, 1, v0\n"
    "v_lshl_add_u32 v2, v4, 3, v2\n"
    "s_lshr_b32 s42, s37, 1\n"
    "s_and_b32 s42, s42, 1\n"
    "s_mul_i32 s43, s42, 1280\n"
    "v_add_u32 v2, s43, v2\n"
    "s_lshl_b32 s43, s42, 3\n"
    "v_add_u32 v13, s43, v14\n"
    "s_mul_i32 s43, s38, 16512\n"
    "s_add_u32 s43, s43, 118784\n"
    "v_lshl_add_u32 v1, v0, 4, s43\n"
    "s_and_b32 s42, s38, 1\n"
    "s_lshl_b32 s42, s42, 4\n"
    "s_lshr_b64 s[52:53], s[52:53], s42\n"
    "s_lshl_b32 s54, s52, 16\n"
    "s_and_b32 s55, s52, 0xffff0000\n"
    "s_lshl_b32 s56, s53, 16\n"
    "v_mov_b32 v43, s36\n"
    "v_mov_b32 v47, s36\n"
    "s_mov_b32 s46, 0x01010101\n"
    "s_movk_i32 s47, 0x0101\n"
    "s_movk_i32 s48, 4096\n"
    "s_movk_i32 s49, 8192\n"
    "s_movk_i32 s50, 12288\n"
    "ds_read_b128 v[16:19], v12 offset:32\n"
    "ds_read_b64 v[20:21], v12 offset:48\n"
    "ds_read_b128 v[4:7], v12 offset:64\n"
    "ds_read_b64 v[8:9], v12 offset:80\n"
    "ds_read_b32 v24, v3 offset:3072\n"
    "ds_read_b32 v25, v3 offset:7648\n"
    "ds_read_b32 v26, v3 offset:12224\n"
    "ds_read_b32 v27, v3 offset:2048\n"
    "ds_read_b32 v28, v3 offset:6624\n"
    "ds_read_b32 v29, v3 offset:11200\n"
    "ds_read_b32 v30, v3 offset:1024\n"
    "ds_read_b32 v31, v3 offset:5600\n"
    "ds_read_b32 v32, v3 offset:10176\n"
    "ds_read_b32 v33, v3 offset:0\n"
    "ds_read_b32 v34, v3 offset:4576\n"
    "ds_read_b32 v35, v3 offset:9152\n"
    "s_waitcnt lgkmcnt(0)\n"
    "v_mul_f32 v5, v5, v16\n"
    "v_mul_f32 v16, s54, v16\n"
    "v_mul_f32 v7, v7, v18\n"
    "v_mul_f32 v18, s55, v18\n"
    "v_mul_f32 v9, v9, v20\n"
    "v_mul_f32 v20, s56, v20\n"
    "v_fma_mix_f32 v64, v24, v16, 0 op_sel:[1,0,0] op_sel_hi:[1,0,0]\n"
    "PAD10:\n"
    "s_cbranch_execz S1\n"
    "v_fma_mix_f32 v65, v24, v16, 0 op_sel_hi:[1,0,0]\n"
    "v_fma_mix_f32 v66, v25, v18, 0 op_sel:[1,0,0] op_sel_hi:[1,0,0]\n"
    "v_fma_mix_f32 v67, v25, v18, 0 op_sel_hi:[1,0,0]\n"
    "v_fma_mix_f32 v68, v26, v20, 0 op_sel:[1,0,0] op_sel_hi:[1,0,0]\n"
    "v_fma_mix_f32 v69, v26, v20, 0 op_sel_hi:[1,0,0]\n"
    "v_cvt_pk_bf16_f32 v64, 0, v64\n"
    "v_cvt_pk_bf16_f32 v65, 0, v65\n"
    "v_cvt_pk_bf16_f32 v66, 0, v66\n"
    "v_cvt_pk_bf16_f32 v67, 0, v67\n"
    "v_cvt_pk_bf16_f32 v68, 0, v68\n"
    "v_cvt_pk_bf16_f32 v69, 0, v69\n"
    "v_fma_mix_f32 v70, v27, v16, 0 op_sel:[1,0,0] op_sel_hi:[1,0,0]\n"
    "v_fma_mix_f32 v71, v27, v16, 0 op_sel_hi:[1,0,0]\n"
    "v_fma_mix_f32 v72, v28, v18, 0 op_sel:[1,0,0] op_sel_hi:[1,0,0]\n"
    "v_fma_mix_f32 v73, v28, v18, 0 op_sel_hi:[1,0,0]\n"
    "v_fma_mix_f32 v74, v29, v20, 0 op_sel:[1,0,0] op_sel_hi:[1,0,0]\n"
    "v_fma_mix_f32 v75, v29, v20, 0 op_sel_hi:[1,0,0]\n"
    "v_cvt_pk_bf16_f32 v70, 0, v70\n"
    "v_cvt_pk_bf16_f32 v71, 0, v71\n"
    "v_cvt_pk_bf16_f32 v72, 0, v72\n"
    "v_cvt_pk_bf16_f32 v73, 0, v73\n"
    "v_cvt_pk_bf16_f32 v74, 0, v74\n"
    "v_cvt_pk_bf16_f32 v75, 0, v75\n"
    "v_fma_mix_f32 v76, v30, v16, 0 op_sel:[1,0,0] op_sel_hi:[1,0,0]\n"
    "v_fma_mix_f32 v77, v30, v16, 0 op_sel_hi:[1,0,0]\n"
    "v_fma_mix_f32 v78, v31, v18, 0 op_sel:[1,0,0] op_sel_hi:[1,0,0]\n"
    "v_fma_mix_f32 v79, v31, v18, 0 op_sel_hi:[1,0,0]\n"
    "v_fma_mix_f32 v80, v32, v20, 0 op_sel:[1,0,0] op_sel_hi:[1,0,0]\n"
    "v_fma_mix_f32 v81, v32, v20, 0 op_sel_hi:[1,0,0]\n"
    "v_cvt_pk_bf16_f32 v76, 0, v76\n"
    "v_cvt_pk_bf16_f32 v77, 0, v77\n"
    "v_cvt_pk_bf16_f32 v78, 0, v78\n"
    "v_cvt_pk_bf16_f32 v79, 0, v79\n"
    "v_cvt_pk_bf16_f32 v80, 0, v80\n"
    "v_cvt_pk_bf16_f32 v81, 0, v81\n"
    "v_fma_mix_f32 v82, v33, v16, 0 op_sel:[1,0,0] op_sel_hi:[1,0,0]\n"
    "v_fma_mix_f32 v83, v33, v16, 0 op_sel_hi:[1,0,0]\n"
    "v_fma_mix_f32 v84, v34, v18, 0 op_sel:[1,0,0] op_sel_hi:[1,0,0]\n"
    "v_fma_mix_f32 v85, v34, v18, 0 op_sel_hi:[1,0,0]\n"
    "v_fma_mix_f32 v86, v35, v20, 0 op_sel:[1,0,0] op_sel_hi:[1,0,0]\n"
    "v_fma_mix_f32 v87, v35, v20, 0 op_sel_hi:[1,0,0]\n"
    "v_cvt_pk_bf16_f32 v82, 0, v82\n"
    "v_cvt_pk_bf16_f32 v83, 0, v83\n"
    "v_cvt_pk_bf16_f32 v84, 0, v84\n"
    "v_cvt_pk_bf16_f32 v85, 0, v85\n"
    "v_cvt_pk_bf16_f32 v86, 0, v86\n"
    "v_cvt_pk_bf16_f32 v87, 0, v87\n"
    "s_branch E_29\n"
    "R_28:\n"
    "s_sleep 1\n"
    "s_sub_u32 s39, s39, 1\n"
    "s_cbranch_scc1 ABORT\n"
    "E_29:\n"
    "ds_read_b64 v[22:23], v13 offset:64\n"
    "s_waitcnt lgkmcnt(0)\n"
    "v_cmp_ne_u32 vcc, 0x010101, v22\n"
    "s_cbranch_vccnz R_28\n"
    "ds_read_b64 v[48:49], v2 offset:0\n"
    "ds_read_b64 v[50:51], v2 offset:10240\n"
    "ds_read_b64 v[52:53], v2 offset:20480\n"
    "s_waitcnt lgkmcnt(0)\n"
    "v_cvt_pk_bf16_f32 v48, 0, v48\n"
    "v_cvt_pk_bf16_f32 v49, 0, v49\n"
    "v_cvt_pk_bf16_f32 v50, 0, v50\n"
    "v_cvt_pk_bf16_f32 v51, 0, v51\n"
    "v_cvt_pk_bf16_f32 v52, 0, v52\n"
    "v_cvt_pk_bf16_f32 v53, 0, v53\n"
    "v_fma_f32 v48, v48, v5, v64\n"
    "v_fma_f32 v49, v49, v5, v65\n"
    "v_fma_f32 v50, v50, v7, v66\n"
    "v_fma_f32 v51, v51, v7, v67\n"
    "v_fma_f32 v52, v52, v9, v68\n"
    "v_fma_f32 v53, v53, v9, v69\n"
    "v_cvt_pk_bf16_f32 v48, 0, v48\n"
    "v_cvt_pk_bf16_f32 v49, 0, v49\n"
    "v_cvt_pk_bf16_f32 v50, 0, v50\n"
    "v_cvt_pk_bf16_f32 v51, 0, v51\n"
    "v_cvt_pk_bf16_f32 v52, 0, v52\n"
    "v_cvt_pk_bf16_f32 v53, 0, v53\n"
    "v_mul_f32 v48, v48, v17\n"
    "v_mul_f32 v49, v49, v17\n"
    "v_mul_f32 v50, v50, v19\n"
    "v_mul_f32 v51, v51, v19\n"
    "v_mul_f32 v52, v52, v21\n"
    "v_mul_f32 v53, v53, v21\n"
    "v_cvt_pk_bf16_f32 v40, v48, v49\n"
    "v_cvt_pk_bf16_f32 v41, v50, v51\n"
    "v_cvt_pk_bf16_f32 v42, v52, v53\n"
    "buffer_store_dwordx4 v[40:43], v1, s[16:19], 0 offen sc1\n"
    "s_mov_b64 exec, 1\n"
    "ds_add_u32 v14, v15 offset:60\n"
    "s_mov_b64 exec, -1\n"
    "s_branch E_31\n"
    "PAD11:\n"
    "s_cbranch_execz S1\n"
    "R_30:\n"
    "s_sleep 1\n"
    "s_sub_u32 s39, s39, 1\n"
    "s_cbranch_scc1 ABORT\n"
    "E_31:\n"
    "ds_read_b64 v[22:23], v13 offset:80\n"
    "s_waitcnt lgkmcnt(0)\n"
    "v_cmp_ne_u64 vcc, s[46:47], v[22:23]\n"
    "s_cbranch_vccnz R_30\n"
    "ds_read_b64 v[48:49], v2 offset:2560\n"
    "ds_read_b64 v[54:55], v2 offset:30720\n"
    "ds_read_b64 v[50:51], v2 offset:12800\n"
    "ds_read_b64 v[56:57], v2 offset:38400\n"
    "ds_read_b64 v[52:53], v2 offset:23040\n"
    "ds_read_b64 v[58:59], v2 offset:46080\n"
    "s_waitcnt lgkmcnt(0)\n"
    "v_add_f32 v48, v48, v54\n"
    "v_add_f32 v49, v49, v55\n"
    "v_add_f32 v50, v50, v56\n"
    "v_add_f32 v51, v51, v57\n"
    "v_add_f32 v52, v52, v58\n"
    "v_add_f32 v53, v53, v59\n"
    "v_cvt_pk_bf16_f32 v48, 0, v48\n"
    "v_cvt_pk_bf16_f32 v49, 0, v49\n"
    "v_cvt_pk_bf16_f32 v50, 0, v50\n"
    "v_cvt_pk_bf16_f32 v51, 0, v51\n"
    "v_cvt_pk_bf16_f32 v52, 0, v52\n"
    "v_cvt_pk_bf16_f32 v53, 0, v53\n"
    "v_fma_f32 v48, v48, v5, v70\n"
    "v_fma_f32 v49, v49, v5, v71\n"
    "v_fma_f32 v50, v50, v7, v72\n"
    "v_fma_f32 v51, v51, v7, v73\n"
    "v_fma_f32 v52, v52, v9, v74\n"
    "v_fma_f32 v53, v53, v9, v75\n"
    "v_cvt_pk_bf16_f32 v48, 0, v48\n"
    "v_cvt_pk_bf16_f32 v49, 0, v49\n"
    "v_cvt_pk_bf16_f32 v50, 0, v50\n"
    "v_cvt_pk_bf16_f32 v51, 0, v51\n"
    "v_cvt_pk_bf16_f32 v52, 0, v52\n"
    "v_cvt_pk_bf16_f32 v53, 0, v53\n"
    "v_mul_f32 v48, v48, v17\n"
    "v_mul_f32 v49, v49, v17\n"
    "v_mul_f32 v50, v50, v19\n"
    "v_mul_f32 v51, v51, v19\n"
    "v_mul_f32 v52, v52, v21\n"
    "v_mul_f32 v53, v53, v21\n"
    "v_cvt_pk_bf16_f32 v44, v48, v49\n"
    "v_cvt_pk_bf16_f32 v45, v50, v51\n"
    "v_cvt_pk_bf16_f32 v46, v52, v53\n"
    "buffer_store_dwordx4 v[44:47], v1, s[16:19], s48 offen sc1\n"
    "s_mov_b64 exec, 1\n"
    "ds_add_u32 v14, v15 offset:60\n"
    "s_mov_b64 exec, -1\n"
    "s_branch E_33\n"
    "R_32:\n"
    "s_sleep 1\n"
    "s_sub_u32 s39, s39, 1\n"
    "s_cbranch_scc1 ABORT\n"
    "E_33:\n"
    "ds_read_b64 v[22:23], v13 offset:96\n"
    "s_waitcnt lgkmcnt(0)\n"
    "v_cmp_ne_u64 vcc, s[46:47], v[22:23]\n"
    "s_cbranch_vccnz R_32\n"
    "ds_read_b64 v[48:49], v2 offset:5120\n"
    "ds_read_b64 v[54:55], v2 offset:33280\n"
    "ds_read_b64 v[50:51], v2 offset:15360\n"
    "ds_read_b64 v[56:57], v2 offset:40960\n"
    "ds_read_b64 v[52:53], v2 offset:25600\n"
    "ds_read_b64 v[58:59], v2 offset:48640\n"
    "s_waitcnt lgkmcnt(0)\n"
    "v_add_f32 v48, v48, v54\n"
    "v_add_f32 v49, v49, v55\n"
    "v_add_f32 v50, v50, v56\n"
    "v_add_f32 v51, v51, v57\n"
    "v_add_f32 v52, v52, v58\n"
    "v_add_f32 v53, v53, v59\n"
    "v_cvt_pk_bf16_f32 v48, 0, v48\n"
    "v_cvt_pk_bf16_f32 v49, 0, v49\n"
    "v_cvt_pk_bf16_f32 v50, 0, v50\n"
    "v_cvt_pk_bf16_f32 v51, 0, v51\n"
    "v_cvt_pk_bf16_f32 v52, 0, v52\n"
    "v_cvt_pk_bf16_f32 v53, 0, v53\n"
    "v_fma_f32 v48, v48, v5, v76\n"
    "v_fma_f32 v49, v49, v5, v77\n"
    "v_fma_f32 v50, v50, v7, v78\n"
    "v_fma_f32 v51, v51, v7, v79\n"
    "v_fma_f32 v52, v52, v9, v80\n"
    "v_fma_f32 v53, v53, v9, v81\n"
    "v_cvt_pk_bf16_f32 v48, 0, v48\n"
    "v_cvt_pk_bf16_f32 v49, 0, v49\n"
    "v_cvt_pk_bf16_f32 v50, 0, v50\n"
    "v_cvt_pk_bf16_f32 v51, 0, v51\n"
    "v_cvt_pk_bf16_f32 v52, 0, v52\n"
    "v_cvt_pk_bf16_f32 v53, 0, v53\n"
    "v_mul_f32 v48, v48, v17\n"
    "v_mul_f32 v49, v49, v17\n"
    "v_mul_f32 v50, v50, v19\n"
    "v_mul_f32 v51, v51, v19\n"
    "v_mul_f32 v52, v52, v21\n"
    "v_mul_f32 v53, v53, v21\n"
    "v_cvt_pk_bf16_f32 v40, v48, v49\n"
    "v_cvt_pk_bf16_f32 v41, v50, v51\n"
    "v_cvt_pk_bf16_f32 v42, v52, v53\n"
    "buffer_store_dwordx4 v[40:43], v1, s[16:19], s49 offen sc1\n"
    "s_mov_b64 exec, 1\n"
    "ds_add_u32 v14, v15 offset:60\n"
    "s_mov_b64 exec, -1\n"
    "s_branch E_35\n"
    "R_34:\n"
    "s_sub_u32 s39, s39, 1\n"
    "s_cbranch_scc1 ABORT\n"
    "E_35:\n"
    "ds_read_b64 v[22:23], v13 offset:112\n"
    "ds_read_b64 v[48:49], v2 offset:7680\n"
    "ds_read_b64 v[54:55], v2 offset:35840\n"
    "ds_read_b64 v[50:51], v2 offset:17920\n"
    "ds_read_b64 v[56:57], v2 offset:43520\n"
    "ds_read_b64 v[52:53], v2 offset:28160\n"
    "ds_read_b64 v[58:59], v2 offset:51200\n"
    "s_waitcnt lgkmcnt(0)\n"
    "v_cmp_ne_u64 vcc, s[46:47], v[22:23]\n"
    "s_cbranch_vccnz R_34\n"
    "v_add_f32 v48, v48, v54\n"
    "v_add_f32 v49, v49, v55\n"
    "v_add_f32 v50, v50, v56\n"
    "v_add_f32 v51, v51, v57\n"
    "v_add_f32 v52, v52, v58\n"
    "v_add_f32 v53, v53, v59\n"
    "v_cvt_pk_bf16_f32 v48, 0, v48\n"
    "v_cvt_pk_bf16_f32 v49, 0, v49\n"
    "v_cvt_pk_bf16_f32 v50, 0, v50\n"
    "v_cvt_pk_bf16_f32 v51, 0, v51\n"
    "v_cvt_pk_bf16_f32 v52, 0, v52\n"
    "v_cvt_pk_bf16_f32 v53, 0, v53\n"
    "v_fma_f32 v48, v48, v5, v82\n"
    "v_fma_f32 v49, v49, v5, v83\n"
    "v_fma_f32 v50, v50, v7, v84\n"
    "v_fma_f32 v51, v51, v7, v85\n"
    "v_fma_f32 v52, v52, v9, v86\n"
    "v_fma_f32 v53, v53, v9, v87\n"
    "v_cvt_pk_bf16_f32 v48, 0, v48\n"
    "v_cvt_pk_bf16_f32 v49, 0, v49\n"
    "v_cvt_pk_bf16_f32 v50, 0, v50\n"
    "v_cvt_pk_bf16_f32 v51, 0, v51\n"
    "v_cvt_pk_bf16_f32 v52, 0, v52\n"
    "v_cvt_pk_bf16_f32 v53, 0, v53\n"
    "v_mul_f32 v48, v48, v17\n"
    "v_mul_f32 v49, v49, v17\n"
    "v_mul_f32 v50, v50, v19\n"
    "v_mul_f32 v51, v51, v19\n"
    "v_mul_f32 v52, v52, v21\n"
    "v_mul_f32 v53, v53, v21\n"
    "v_cvt_pk_bf16_f32 v44, v48, v49\n"
    "v_cvt_pk_bf16_f32 v45, v50, v51\n"
    "v_cvt_pk_bf16_f32 v46, v52, v53\n"
    "buffer_store_dwordx4 v[44:47], v1, s[16:19], s50 offen sc1\n"
    "s_mov_b64 exec, 1\n"
    "ds_add_u32 v14, v15 offset:60\n"
    "s_mov_b64 exec, -1\n"
    "s_branch LN2K\n"
    "CH13_0:\n"
    "s_cbranch_execz CH13_1\n"
    "MISC:\n"
    "s_cmpk_ge_u32 s2, 128\n"
    "s_cbranch_scc1 PREF\n"
    "s_waitcnt lgkmcnt(0)\n"
    "s_barrier\n"
    "s_mov_b32 m0, 148480\n"
    "s_mov_b64 exec, s[62:63]\n"
    "s_cbranch_execz PFX_36\n"
    "global_load_lds_dword v47, s[58:59]\n"
    "s_mov_b64 exec, s[60:61]\n"
    "s_cbranch_execz PFX_36\n"
    "global_load_lds_dword v47, s[30:31]\n"
    "PFX_36:\n"
    "s_mov_b64 exec, -1\n"
    "U_37:\n"
    "s_memrealtime s[98:99]\n"
    "s_waitcnt lgkmcnt(0)\n"
    "s_sub_u32 s98, s98, s96\n"
    "s_cmpk_lt_u32 s98, 340\n"
    "s_cbranch_scc1 U_37\n"
    "buffer_load_dwordx4 v[48:51], v43, s[16:19], s44 offen sc1\n"
    "buffer_load_dwordx4 v[52:55], v43, s[16:19], s45 offen sc1\n"
    "buffer_load_dwordx4 v[56:59], v43, s[16:19], s46 offen sc1\n"
    "buffer_load_dwordx4 v[60:63], v43, s[16:19], s47 offen sc1\n"
    "buffer_load_dwordx4 v[64:67], v43, s[16:19], s48 offen sc1\n"
    "buffer_load_dwordx4 v[68:71], v43, s[16:19], s49 offen sc1\n"
    "buffer_load_dwordx4 v[72:75], v44, s[16:19], s44 offen sc1\n"
    "buffer_load_dwordx4 v[76:79], v44, s[16:19], s45 offen sc1\n"
    "buffer_load_dwordx4 v[80:83], v44, s[16:19], s46 offen sc1\n"
    "buffer_load_dwordx4 v[84:87], v44, s[16:19], s47 offen sc1\n"
    "buffer_load_dwordx4 v[88:91], v44, s[16:19], s48 offen sc1\n"
    "buffer_load_dwordx4 v[92:95], v44, s[16:19], s49 offen sc1\n"
    "global_load_dword v45, v41, s[26:27]\n"
    "global_load_dword v46, v42, s[26:27]\n"
    "s_setprio 1\n"
    "s_and_b32 s57, s2, 7\n"
    "s_lshl_b32 s57, s57, 4\n"
    "s_lshr_b32 s43, s2, 3\n"
    "s_add_u32 s57, s57, s43\n"
    "s_mul_i32 s42, s57, 18432\n"
    "s_mul_i32 s43, s37, 768\n"
    "s_add_u32 s42, s42, s43\n"
    "v_and_b32 v1, 63, v0\n"
    "v_lshlrev_b32 v1, 4, v1\n"
    "v_add_u32 v2, s42, v1\n"
    "s_add_u32 m0, s43, 53760\n"
    "s_mov_b32 exec_hi, 0xffff\n"
    "global_load_lds_dwordx4 v2, s[4:5] nt\n"
    "s_add_u32 m0, m0, 2320\n"
    "v_add_u32 v2, 2304, v2\n"
    "global_load_lds_dwordx4 v2, s[4:5] nt\n"
    "s_add_u32 m0, m0, 2320\n"
    "v_add_u32 v2, 2304, v2\n"
    "global_load_lds_dwordx4 v2, s[4:5] nt\n"
    "s_add_u32 m0, m0, 2320\n"
    "v_add_u32 v2, 2304, v2\n"
    "global_load_lds_dwordx4 v2, s[4:5] nt\n"
    "s_add_u32 m0, m0, 2320\n"
    "v_add_u32 v2, 2304, v2\n"
    "global_load_lds_dwordx4 v2, s[4:5] nt\n"
    "s_add_u32 m0, m0, 2320\n"
    "v_add_u32 v2, 2304, v2\n"
    "global_load_lds_dwordx4 v2, s[4:5] nt\n"
    "s_add_u32 m0, m0, 2320\n"
    "v_add_u32 v2, 2304, v2\n"
    "global_load_lds_dwordx4 v2, s[4:5] nt\n"
    "s_add_u32 m0, m0, 2320\n"
    "v_add_u32 v2, 2304, v2\n"
    "global_load_lds_dwordx4 v2, s[4:5] nt\n"
    "s_add_u32 m0, m0, 2320\n"
    "v_add_u32 v2, 2304, v2\n"
    "s_mov_b64 exec, -1\n"
    "v_mov_b32 v8, 0\n"
    "v_mov_b32 v9, 0\n"
    "v_mov_b32 v10, 0\n"
    "v_mov_b32 v11, 0\n"
    "v_lshrrev_b32 v2, 9, v1\n"
    "v_mul_u32_u24 v2, 1536, v2\n"
    "v_add_u32 v2, v2, v1\n"
    "v_add_u32 v2, 134048, v2\n"
    "ds_write_b128 v2, v[8:11]\n"
    "s_mov_b32 s42, 138240\n"
    "v_lshl_add_u32 v1, v43, 1, s42\n"
    "v_lshl_add_u32 v2, v44, 1, s42\n"
    "v_add_u32 v41, 72320, v41\n"
    "v_add_u32 v42, 72320, v42\n"
    "v_mov_b32 v15, 1\n"
    "s_waitcnt vmcnt(8)\n"
    "s_branch E_39\n"
    "R_38:\n"
    "s_sub_u32 s39, s39, 1\n"
    "s_cbranch_scc1 ABORT\n"
    "s_sleep 4\n"
    "buffer_load_dwordx4 v[48:51], v43, s[16:19], s44 offen sc1\n"
    "buffer_load_dwordx4 v[52:55], v43, s[16:19], s45 offen sc1\n"
    "buffer_load_dwordx4 v[56:59], v43, s[16:19], s46 offen sc1\n"
    "buffer_load_dwordx4 v[60:63], v43, s[16:19], s47 offen sc1\n"
    "buffer_load_dwordx4 v[64:67], v43, s[16:19], s48 offen sc1\n"
    "buffer_load_dwordx4 v[68:71], v43, s[16:19], s49 offen sc1\n"
    "buffer_load_dwordx4 v[72:75], v44, s[16:19], s44 offen sc1\n"
    "buffer_load_dwordx4 v[76:79], v44, s[16:19], s45 offen sc1\n"
    "buffer_load_dwordx4 v[80:83], v44, s[16:19], s46 offen sc1\n"
    "buffer_load_dwordx4 v[84:87], v44, s[16:19], s47 offen sc1\n"
    "buffer_load_dwordx4 v[88:91], v44, s[16:19], s48 offen sc1\n"
    "buffer_load_dwordx4 v[92:95], v44, s[16:19], s49 offen sc1\n"
    "s_waitcnt vmcnt(0)\n"
    "E_39:\n"
    "v_bitop3_b32 v16, v49, v51, s36 bitop3:0x7e\n"
    "v_bitop3_b32 v17, v53, v55, s36 bitop3:0x7e\n"
    "v_bitop3_b32 v18, v57, v59, s36 bitop3:0x7e\n"
    "v_bitop3_b32 v19, v61, v63, s36 bitop3:0x7e\n"
    "v_bitop3_b32 v20, v65, v67, s36 bitop3:0x7e\n"
    "v_bitop3_b32 v21, v69, v71, s36 bitop3:0x7e\n"
    "v_bitop3_b32 v22, v73, v75, s36 bitop3:0x7e\n"
    "v_bitop3_b32 v23, v77, v79, s36 bitop3:0x7e\n"
    "v_bitop3_b32 v24, v81, v83, s36 bitop3:0x7e\n"
    "v_bitop3_b32 v25, v85, v87, s36 bitop3:0x7e\n"
    "v_bitop3_b32 v26, v89, v91, s36 bitop3:0x7e\n"
    "v_bitop3_b32 v27, v93, v95, s36 bitop3:0x7e\n"
    "v_or3_b32 v16, v16, v17, v18\n"
    "v_or3_b32 v19, v19, v20, v21\n"
    "v_or3_b32 v22, v22, v23, v24\n"
    "v_or3_b32 v25, v25, v26, v27\n"
    "v_or3_b32 v16, v16, v19, v22\n"
    "v_or_b32 v16, v16, v25\n"
    "v_cmp_ne_u32 vcc, 0, v16\n"
    "s_cbranch_vccnz R_38\n"
    "ds_write_b32 v41, v45\n"
    "v_add_f32 v16, v56, v60\n"
    "v_add_f32 v17, v48, v52\n"
    "v_add_f32 v19, v64, v68\n"
    "v_cvt_pk_bf16_f32 v16, 0, v16\n"
    "v_cvt_pk_bf16_f32 v17, 0, v17\n"
    "v_cvt_pk_bf16_f32 v19, 0, v19\n"
    "v_add_f32 v16, 1.0, v16\n"
    "v_lshlrev_b32 v32, 16, v45\n"
    "v_cvt_pk_bf16_f32 v16, 0, v16\n"
    "v_mul_f32 v18, v16, v32\n"
    "ds_write_b128 v1, v[16:19] offset:0\n"
    "v_add_f32 v20, v58, v62\n"
    "v_add_f32 v21, v50, v54\n"
    "v_add_f32 v23, v66, v70\n"
    "v_cvt_pk_bf16_f32 v20, 0, v20\n"
    "v_cvt_pk_bf16_f32 v21, 0, v21\n"
    "v_cvt_pk_bf16_f32 v23, 0, v23\n"
    "v_add_f32 v20, 1.0, v20\n"
    "v_and_b32 v33, 0xffff0000, v45\n"
    "v_cvt_pk_bf16_f32 v20, 0, v20\n"
    "v_mul_f32 v22, v20, v33\n"
    "ds_write_b128 v1, v[20:23] offset:16\n"
    "ds_write_b32 v42, v46\n"
    "v_add_f32 v24, v80, v84\n"
    "v_add_f32 v25, v72, v76\n"
    "v_add_f32 v27, v88, v92\n"
    "CH13_1:\n"
    "s_cbranch_execz CH13_2\n"
    "v_cvt_pk_bf16_f32 v24, 0, v24\n"
    "v_cvt_pk_bf16_f32 v25, 0, v25\n"
    "v_cvt_pk_bf16_f32 v27, 0, v27\n"
    "v_add_f32 v24, 1.0, v24\n"
    "v_lshlrev_b32 v34, 16, v46\n"
    "v_cvt_pk_bf16_f32 v24, 0, v24\n"
    "v_mul_f32 v26, v24, v34\n"
    "ds_write_b128 v2, v[24:27] offset:0\n"
    "v_add_f32 v28, v82, v86\n"
    "v_add_f32 v29, v74, v78\n"
    "v_add_f32 v31, v90, v94\n"
    "v_cvt_pk_bf16_f32 v28, 0, v28\n"
    "v_cvt_pk_bf16_f32 v29, 0, v29\n"
    "v_cvt_pk_bf16_f32 v31, 0, v31\n"
    "v_add_f32 v28, 1.0, v28\n"
    "v_and_b32 v35, 0xffff0000, v46\n"
    "v_cvt_pk_bf16_f32 v28, 0, v28\n"
    "v_mul_f32 v30, v28, v35\n"
    "ds_write_b128 v2, v[28:31] offset:16\n"
    "s_waitcnt vmcnt(0)\n"
    "s_mov_b64 exec, 1\n"
    "ds_add_u32 v14, v15 offset:56\n"
    "s_mov_b64 exec, -1\n"
    "s_setprio 0\n"
    "v_cmp_gt_u32 vcc, 0x60, v0\n"
    "v_mul_f32 v32, v32, v17\n"
    "v_mul_f32 v33, v33, v21\n"
    "v_add_f32 v4, v18, v22\n"
    "v_mul_f32 v5, v18, v18\n"
    "v_fmac_f32 v5, v22, v22\n"
    "v_mul_f32 v5, 4.0, v5\n"
    "v_fmac_f32 v5, v32, v32\n"
    "v_fmac_f32 v5, v33, v33\n"
    "v_mul_f32 v34, v34, v25\n"
    "v_mul_f32 v35, v35, v29\n"
    "v_add_f32 v6, v26, v30\n"
    "v_mul_f32 v7, v26, v26\n"
    "v_fmac_f32 v7, v30, v30\n"
    "v_mul_f32 v7, 4.0, v7\n"
    "v_fmac_f32 v7, v34, v34\n"
    "v_fmac_f32 v7, v35, v35\n"
    "v_cndmask_b32 v6, 0, v6, vcc\n"
    "v_cndmask_b32 v7, 0, v7, vcc\n"
    "v_add_f32 v4, v4, v6\n"
    "v_add_f32 v5, v5, v7\n"
    "v_lshrrev_b32 v3, 4, v0\n"
    "v_lshlrev_b32 v3, 3, v3\n"
    "v_add_u32 v3, 148736, v3\n"
    "v_add_f32_dpp v4, v4, v4 row_ror:8 row_mask:0xf bank_mask:0xf\n"
    "v_add_f32_dpp v5, v5, v5 row_ror:8 row_mask:0xf bank_mask:0xf\n"
    "s_nop 0\n"
    "v_add_f32_dpp v4, v4, v4 row_ror:4 row_mask:0xf bank_mask:0xf\n"
    "v_add_f32_dpp v5, v5, v5 row_ror:4 row_mask:0xf bank_mask:0xf\n"
    "s_nop 0\n"
    "v_add_f32_dpp v4, v4, v4 row_ror:2 row_mask:0xf bank_mask:0xf\n"
    "v_add_f32_dpp v5, v5, v5 row_ror:2 row_mask:0xf bank_mask:0xf\n"
    "s_nop 0\n"
    "v_add_f32_dpp v4, v4, v4 row_ror:1 row_mask:0xf bank_mask:0xf\n"
    "v_add_f32_dpp v5, v5, v5 row_ror:1 row_mask:0xf bank_mask:0xf\n"
    "ds_write_b64 v3, v[4:5]\n"
    "LN2:\n"
    "s_cmpk_ge_u32 s2, 128\n"
    "s_cbranch_scc1 CDONE\n"
    "s_and_b32 s57, s2, 7\n"
    "s_lshl_b32 s57, s57, 4\n"
    "s_lshr_b32 s43, s2, 3\n"
    "s_add_u32 s57, s57, s43\n"
    "v_and_b32 v2, 7, v0\n"
    "v_lshrrev_b32 v3, 3, v0\n"
    "v_mul_u32_u24 v6, 12, v3\n"
    "s_movk_i32 s44, 0x900\n"
    "v_mad_u32_u24 v4, v2, s44, v6\n"
    "v_lshlrev_b32 v2, 4, v2\n"
    "v_add_u32 v4, v2, v4\n"
    "v_add_u32 v5, 72320, v6\n"
    "s_mov_b32 s44, 33024\n"
    "v_mad_u32_u24 v1, v3, s44, v2\n"
    "s_lshl_b32 s42, s57, 7\n"
    "s_add_u32 s42, s42, 131072\n"
    "s_add_u32 s43, s42, 16512\n"
    "s_mov_b32 s44, 138240\n"
    "v_lshl_add_u32 v6, v6, 3, s44\n"
    "v_mov_b32 v7, 147968\n"
    "v_and_b32 v16, 8, v0\n"
    "v_lshl_add_u32 v16, v16, 8, v2\n"
    "v_bfe_u32 v17, v0, 4, 2\n"
    "v_lshlrev_b32 v18, 2, v17\n"
    "s_lshl_b32 s44, s37, 7\n"
    "s_add_u32 s44, s44, 132512\n"
    "v_add3_u32 v16, v16, v18, s44\n"
    "v_and_b32 v18, 2, v17\n"
    "v_lshl_add_u32 v18, v18, 2, v2\n"
    "s_lshl_b32 s44, s37, 2\n"
    "s_add_u32 s44, s44, 136608\n"
    "v_mad_u32_u24 v18, v18, 6, s44\n"
    "v_and_b32 v17, 63, v0\n"
    "s_mov_b32 s44, 132512\n"
    "v_lshl_add_u32 v17, v17, 4, s44\n"
    "v_mul_u32_u24 v22, 192, v2\n"
    "v_lshl_add_u32 v22, v3, 4, v22\n"
    "s_mul_i32 s45, s57, 24576\n"
    "s_add_u32 s45, s45, 7341056\n"
    "s_lshr_b32 s44, s57, 4\n"
    "s_lshl_b32 s44, s44, 1\n"
    "s_add_u32 s44, s44, 2\n"
    "s_branch E_41\n"
    "R_40:\n"
    "s_sleep 2\n"
    "s_sub_u32 s39, s39, 1\n"
    "s_cbranch_scc1 ABORT\n"
    "E_41:\n"
    "ds_read_b32 v19, v7 offset:56\n"
    "s_waitcnt lgkmcnt(0)\n"
    "v_cmp_ne_u32 vcc, 3, v19\n"
    "s_cbranch_vccnz R_40\n"
    "ds_read_b32 v8, v4 offset:53760\n"
    "ds_read_b32 v9, v4 offset:53764\n"
    "ds_read_b32 v10, v4 offset:53768\n"
    "ds_read_b32 v12, v4 offset:54912\n"
    "ds_read_b32 v13, v4 offset:54916\n"
    "ds_read_b32 v14, v4 offset:54920\n"
    "ds_read_b128 v[40:43], v6 offset:0\n"
    "ds_read_b128 v[44:47], v6 offset:16\n"
    "ds_read_b128 v[48:51], v6 offset:32\n"
    "ds_read_b128 v[52:55], v6 offset:48\n"
    "ds_read_b128 v[56:59], v6 offset:64\n"
    "ds_read_b128 v[60:63], v6 offset:80\n"
    "ds_read2_b32 v[36:37], v5 offset1:1\n"
    "ds_read_b32 v38, v5 offset:8\n"
    "s_waitcnt lgkmcnt(8)\n"
    "v_lshlrev_b32 v24, 16, v8\n"
    "v_lshlrev_b32 v25, 16, v12\n"
    "v_and_b32 v26, 0xffff0000, v8\n"
    "v_and_b32 v27, 0xffff0000, v12\n"
    "v_lshlrev_b32 v28, 16, v9\n"
    "v_lshlrev_b32 v29, 16, v13\n"
    "v_and_b32 v30, 0xffff0000, v9\n"
    "v_and_b32 v31, 0xffff0000, v13\n"
    "v_lshlrev_b32 v32, 16, v10\n"
    "v_lshlrev_b32 v33, 16, v14\n"
    "v_and_b32 v34, 0xffff0000, v10\n"
    "v_and_b32 v35, 0xffff0000, v14\n"
    "s_branch E_43\n"
    "R_42:\n"
    "s_sleep 1\n"
    "s_sub_u32 s39, s39, 1\n"
    "s_cbranch_scc1 ABORT\n"
    "E_43:\n"
    "ds_read_b32 v19, v7 offset:60\n"
    "s_waitcnt lgkmcnt(0)\n"
    "v_cmp_gt_u32 vcc, s44, v19\n"
    "s_cbranch_vccnz R_42\n"
    "s_cmpk_lt_u32 s57, 96\n"
    "s_cbranch_scc1 LN2g\n"
    "s_sleep 8\n"
    "LN2g:\n"
    "buffer_load_dwordx4 v[72:75], v1, s[16:19], s42 offen sc1\n"
    "buffer_load_dwordx4 v[76:79], v1, s[16:19], s43 offen sc1\n"
    "s_branch E_45\n"
    "CH13_2:\n"
    "s_cbranch_execz CH13_3\n"
    "R_44:\n"
    "s_sub_u32 s39, s39, 1\n"
    "s_cbranch_scc1 ABORT\n"
    "s_wqm_b64 exec, vcc\n"
    "buffer_load_dwordx4 v[72:75], v1, s[16:19], s42 offen sc1\n"
    "buffer_load_dwordx4 v[76:79], v1, s[16:19], s43 offen sc1\n"
    "s_mov_b64 exec, -1\n"
    "E_45:\n"
    "s_waitcnt vmcnt(0)\n"
    "v_bitop3_b32 v19, v75, v79, s36 bitop3:0x7e\n"
    "v_cmp_ne_u32 vcc, 0, v19\n"
    "s_cbranch_vccnz R_44\n"
    "v_lshlrev_b32 v20, 16, v72\n"
    "v_and_b32 v21, 0xffff0000, v72\n"
    "v_pk_add_f32 v[24:25], v[24:25], v[20:21]\n"
    "v_cvt_pk_bf16_f32 v24, 0, v24\n"
    "v_cvt_pk_bf16_f32 v25, 0, v25\n"
    "v_pk_mul_f32 v[10:11], v[24:25], v[24:25]\n"
    "v_pk_mul_f32 v[12:13], v[24:25], v[42:43] op_sel_hi:[1,0]\n"
    "v_lshlrev_b32 v20, 16, v73\n"
    "v_and_b32 v21, 0xffff0000, v73\n"
    "v_pk_add_f32 v[26:27], v[26:27], v[20:21]\n"
    "v_cvt_pk_bf16_f32 v26, 0, v26\n"
    "v_cvt_pk_bf16_f32 v27, 0, v27\n"
    "v_pk_add_f32 v[8:9], v[26:27], v[24:25]\n"
    "v_pk_fma_f32 v[10:11], v[26:27], v[26:27], v[10:11]\n"
    "v_pk_fma_f32 v[12:13], v[26:27], v[46:47], v[12:13] op_sel:[0,0,0] op_sel_hi:[1,0,1]\n"
    "v_lshlrev_b32 v20, 16, v74\n"
    "v_and_b32 v21, 0xffff0000, v74\n"
    "v_pk_add_f32 v[28:29], v[28:29], v[20:21]\n"
    "v_cvt_pk_bf16_f32 v28, 0, v28\n"
    "v_cvt_pk_bf16_f32 v29, 0, v29\n"
    "v_pk_add_f32 v[8:9], v[28:29], v[8:9]\n"
    "v_pk_fma_f32 v[10:11], v[28:29], v[28:29], v[10:11]\n"
    "v_pk_fma_f32 v[12:13], v[28:29], v[50:51], v[12:13] op_sel:[0,0,0] op_sel_hi:[1,0,1]\n"
    "v_lshlrev_b32 v20, 16, v76\n"
    "v_and_b32 v21, 0xffff0000, v76\n"
    "v_pk_add_f32 v[30:31], v[30:31], v[20:21]\n"
    "v_cvt_pk_bf16_f32 v30, 0, v30\n"
    "v_cvt_pk_bf16_f32 v31, 0, v31\n"
    "v_pk_add_f32 v[8:9], v[30:31], v[8:9]\n"
    "v_pk_fma_f32 v[10:11], v[30:31], v[30:31], v[10:11]\n"
    "v_pk_fma_f32 v[12:13], v[30:31], v[54:55], v[12:13] op_sel:[0,0,0] op_sel_hi:[1,0,1]\n"
    "v_lshlrev_b32 v20, 16, v77\n"
    "v_and_b32 v21, 0xffff0000, v77\n"
    "v_pk_add_f32 v[32:33], v[32:33], v[20:21]\n"
    "v_cvt_pk_bf16_f32 v32, 0, v32\n"
    "v_cvt_pk_bf16_f32 v33, 0, v33\n"
    "v_pk_add_f32 v[8:9], v[32:33], v[8:9]\n"
    "v_pk_fma_f32 v[10:11], v[32:33], v[32:33], v[10:11]\n"
    "v_pk_fma_f32 v[12:13], v[32:33], v[58:59], v[12:13] op_sel:[0,0,0] op_sel_hi:[1,0,1]\n"
    "v_lshlrev_b32 v20, 16, v78\n"
    "v_and_b32 v21, 0xffff0000, v78\n"
    "v_pk_add_f32 v[34:35], v[34:35], v[20:21]\n"
    "v_cvt_pk_bf16_f32 v34, 0, v34\n"
    "v_cvt_pk_bf16_f32 v35, 0, v35\n"
    "v_pk_add_f32 v[8:9], v[34:35], v[8:9]\n"
    "v_pk_fma_f32 v[10:11], v[34:35], v[34:35], v[10:11]\n"
    "v_pk_fma_f32 v[12:13], v[34:35], v[62:63], v[12:13] op_sel:[0,0,0] op_sel_hi:[1,0,1]\n"
    "v_permlane32_swap_b32 v8, v9\n"
    "v_permlane32_swap_b32 v10, v11\n"
    "v_permlane32_swap_b32 v12, v13\n"
    "v_add_f32 v8, v8, v9\n"
    "v_add_f32 v10, v10, v11\n"
    "v_add_f32 v12, v12, v13\n"
    "s_nop 0\n"
    "v_permlane16_swap_b32 v8, v10\n"
    "v_add_f32 v8, v8, v10\n"
    "s_nop 1\n"
    "v_add_f32_dpp v9, v8, v8 row_ror:8 row_mask:0xf bank_mask:0x3\n"
    "s_nop 1\n"
    "v_add_f32_dpp v9, v12, v12 row_ror:8 row_mask:0xf bank_mask:0xc\n"
    "ds_write_b32 v16, v9\n"
    "s_waitcnt lgkmcnt(0)\n"
    "s_barrier\n"
    "ds_read_b128 v[8:11], v17\n"
    "ds_read_b128 v[12:15], v17 offset:1024\n"
    "s_waitcnt lgkmcnt(0)\n"
    "v_pk_add_f32 v[8:9], v[8:9], v[12:13]\n"
    "v_pk_add_f32 v[10:11], v[10:11], v[14:15]\n"
    "s_nop 1\n"
    "v_permlane32_swap_b32 v8, v10\n"
    "v_permlane32_swap_b32 v9, v11\n"
    "v_pk_add_f32 v[8:9], v[8:9], v[10:11]\n"
    "s_nop 1\n"
    "v_add_f32_dpp v8, v8, v8 row_ror:8 row_mask:0xf bank_mask:0xf\n"
    "v_add_f32_dpp v9, v9, v9 row_ror:8 row_mask:0xf bank_mask:0xf\n"
    "v_mov_b32 v10, v8\n"
    "v_mov_b32 v11, v9\n"
    "s_nop 0\n"
    "v_permlane16_swap_b32 v8, v10\n"
    "v_permlane16_swap_b32 v9, v11\n"
    "v_pk_add_f32 v[8:9], v[8:9], v[10:11]\n"
    "v_mul_f32 v8, 0x3ae38e39, v8\n"
    "v_mul_f32 v9, 0x3ae38e39, v9\n"
    "v_fma_f32 v9, -v8, v8, v9\n"
    "v_max_f32 v9, 0, v9\n"
    "v_cvt_pk_bf16_f32 v9, 0, v9\n"
    "v_add_f32 v9, 0x3727c5ac, v9\n"
    "v_cvt_pk_bf16_f32 v9, 0, v9\n"
    "v_sqrt_f32 v9, v9\n"
    "s_nop 0\n"
    "v_cvt_pk_bf16_f32 v8, v8, v9\n"
    "v_mov_b32 v9, v8\n"
    "s_nop 1\n"
    "v_permlane32_swap_b32 v8, v9\n"
    "v_and_b32 v12, 0xffff0000, v8\n"
    "v_and_b32 v13, 0xffff0000, v9\n"
    "v_lshlrev_b32 v10, 16, v8\n"
    "v_lshlrev_b32 v11, 16, v9\n"
    "v_rcp_f32 v12, v12\n"
    "v_rcp_f32 v13, v13\n"
    "v_pk_add_f32 v[80:81], v[24:25], v[10:11] neg_lo:[0,1] neg_hi:[0,1]\n"
    "v_pk_add_f32 v[82:83], v[26:27], v[10:11] neg_lo:[0,1] neg_hi:[0,1]\n"
    "v_cvt_pk_bf16_f32 v80, 0, v80\n"
    "v_cvt_pk_bf16_f32 v81, 0, v81\n"
    "v_cvt_pk_bf16_f32 v82, 0, v82\n"
    "v_cvt_pk_bf16_f32 v83, 0, v83\n"
    "v_pk_mul_f32 v[80:81], v[80:81], v[12:13]\n"
    "v_pk_mul_f32 v[82:83], v[82:83], v[12:13]\n"
    "v_cvt_pk_bf16_f32 v80, 0, v80\n"
    "v_cvt_pk_bf16_f32 v81, 0, v81\n"
    "v_cvt_pk_bf16_f32 v82, 0, v82\n"
    "v_cvt_pk_bf16_f32 v83, 0, v83\n"
    "v_pk_mul_f32 v[80:81], v[80:81], v[40:41] op_sel_hi:[1,0]\n"
    "v_pk_mul_f32 v[82:83], v[82:83], v[44:45] op_sel_hi:[1,0]\n"
    "v_cvt_pk_bf16_f32 v80, 0, v80\n"
    "v_cvt_pk_bf16_f32 v81, 0, v81\n"
    "v_cvt_pk_bf16_f32 v82, 0, v82\n"
    "v_cvt_pk_bf16_f32 v83, 0, v83\n"
    "v_pk_add_f32 v[80:81], v[40:41], v[80:81] op_sel:[1,0] op_sel_hi:[1,1]\n"
    "v_pk_add_f32 v[82:83], v[44:45], v[82:83] op_sel:[1,0] op_sel_hi:[1,1]\n"
    "v_cvt_pk_bf16_f32 v72, v80, v82\n"
    "v_cvt_pk_bf16_f32 v76, v81, v83\n"
    "v_dot2_f32_bf16 v14, v72, v36, 0\n"
    "v_dot2_f32_bf16 v15, v76, v36, 0\n"
    "v_pk_add_f32 v[80:81], v[28:29], v[10:11] neg_lo:[0,1] neg_hi:[0,1]\n"
    "v_pk_add_f32 v[82:83], v[30:31], v[10:11] neg_lo:[0,1] neg_hi:[0,1]\n"
    "v_cvt_pk_bf16_f32 v80, 0, v80\n"
    "v_cvt_pk_bf16_f32 v81, 0, v81\n"
    "v_cvt_pk_bf16_f32 v82, 0, v82\n"
    "v_cvt_pk_bf16_f32 v83, 0, v83\n"
    "v_pk_mul_f32 v[80:81], v[80:81], v[12:13]\n"
    "v_pk_mul_f32 v[82:83], v[82:83], v[12:13]\n"
    "v_cvt_pk_bf16_f32 v80, 0, v80\n"
    "v_cvt_pk_bf16_f32 v81, 0, v81\n"
    "v_cvt_pk_bf16_f32 v82, 0, v82\n"
    "CH13_3:\n"
    "s_cbranch_execz CH13_4\n"
    "v_cvt_pk_bf16_f32 v83, 0, v83\n"
    "v_pk_mul_f32 v[80:81], v[80:81], v[48:49] op_sel_hi:[1,0]\n"
    "v_pk_mul_f32 v[82:83], v[82:83], v[52:53] op_sel_hi:[1,0]\n"
    "v_cvt_pk_bf16_f32 v80, 0, v80\n"
    "v_cvt_pk_bf16_f32 v81, 0, v81\n"
    "v_cvt_pk_bf16_f32 v82, 0, v82\n"
    "v_cvt_pk_bf16_f32 v83, 0, v83\n"
    "v_pk_add_f32 v[80:81], v[48:49], v[80:81] op_sel:[1,0] op_sel_hi:[1,1]\n"
    "v_pk_add_f32 v[82:83], v[52:53], v[82:83] op_sel:[1,0] op_sel_hi:[1,1]\n"
    "v_cvt_pk_bf16_f32 v73, v80, v82\n"
    "v_cvt_pk_bf16_f32 v77, v81, v83\n"
    "v_dot2_f32_bf16 v14, v73, v37, v14\n"
    "v_dot2_f32_bf16 v15, v77, v37, v15\n"
    "v_pk_add_f32 v[80:81], v[32:33], v[10:11] neg_lo:[0,1] neg_hi:[0,1]\n"
    "v_pk_add_f32 v[82:83], v[34:35], v[10:11] neg_lo:[0,1] neg_hi:[0,1]\n"
    "v_cvt_pk_bf16_f32 v80, 0, v80\n"
    "v_cvt_pk_bf16_f32 v81, 0, v81\n"
    "v_cvt_pk_bf16_f32 v82, 0, v82\n"
    "v_cvt_pk_bf16_f32 v83, 0, v83\n"
    "v_pk_mul_f32 v[80:81], v[80:81], v[12:13]\n"
    "v_pk_mul_f32 v[82:83], v[82:83], v[12:13]\n"
    "v_cvt_pk_bf16_f32 v80, 0, v80\n"
    "v_cvt_pk_bf16_f32 v81, 0, v81\n"
    "v_cvt_pk_bf16_f32 v82, 0, v82\n"
    "v_cvt_pk_bf16_f32 v83, 0, v83\n"
    "v_pk_mul_f32 v[80:81], v[80:81], v[56:57] op_sel_hi:[1,0]\n"
    "v_pk_mul_f32 v[82:83], v[82:83], v[60:61] op_sel_hi:[1,0]\n"
    "v_cvt_pk_bf16_f32 v80, 0, v80\n"
    "v_cvt_pk_bf16_f32 v81, 0, v81\n"
    "v_cvt_pk_bf16_f32 v82, 0, v82\n"
    "v_cvt_pk_bf16_f32 v83, 0, v83\n"
    "v_pk_add_f32 v[80:81], v[56:57], v[80:81] op_sel:[1,0] op_sel_hi:[1,1]\n"
    "v_pk_add_f32 v[82:83], v[60:61], v[82:83] op_sel:[1,0] op_sel_hi:[1,1]\n"
    "v_cvt_pk_bf16_f32 v74, v80, v82\n"
    "v_cvt_pk_bf16_f32 v78, v81, v83\n"
    "v_dot2_f32_bf16 v14, v74, v38, v14\n"
    "v_dot2_f32_bf16 v15, v78, v38, v15\n"
    "v_add_u32 v75, 1, v75\n"
    "v_add_u32 v79, 1, v79\n"
    "buffer_store_dwordx4 v[72:75], v22, s[16:19], s45 offen sc1\n"
    "buffer_store_dwordx4 v[76:79], v22, s[16:19], s45 offen offset:1536 sc1\n"
    "v_permlane32_swap_b32 v14, v15\n"
    "v_add_f32 v14, v14, v15\n"
    "s_nop 1\n"
    "v_add_f32_dpp v14, v14, v14 row_ror:8 row_mask:0xf bank_mask:0xf\n"
    "v_mov_b32 v15, v14\n"
    "s_nop 1\n"
    "v_permlane16_swap_b32 v14, v15\n"
    "v_add_f32 v14, v14, v15\n"
    "ds_write_b32 v18, v14\n"
    "s_waitcnt lgkmcnt(0)\n"
    "s_barrier\n"
    "v_lshlrev_b32 v80, 16, v72\n"
    "v_lshlrev_b32 v81, 16, v76\n"
    "v_and_b32 v82, 0xffff0000, v72\n"
    "v_and_b32 v83, 0xffff0000, v76\n"
    "v_pk_mul_f32 v[80:81], v[42:43], v[80:81] op_sel:[1,0] op_sel_hi:[1,1]\n"
    "v_pk_mul_f32 v[82:83], v[46:47], v[82:83] op_sel:[1,0] op_sel_hi:[1,1]\n"
    "v_cvt_pk_bf16_f32 v80, 0, v80\n"
    "v_cvt_pk_bf16_f32 v81, 0, v81\n"
    "v_cvt_pk_bf16_f32 v82, 0, v82\n"
    "v_cvt_pk_bf16_f32 v83, 0, v83\n"
    "v_pk_add_f32 v[80:81], v[80:81], v[24:25]\n"
    "v_pk_add_f32 v[82:83], v[82:83], v[26:27]\n"
    "v_cvt_pk_bf16_f32 v8, v80, v82\n"
    "v_cvt_pk_bf16_f32 v12, v81, v83\n"
    "ds_write_b32 v4, v8 offset:18560\n"
    "ds_write_b32 v4, v12 offset:19712\n"
    "v_lshlrev_b32 v80, 16, v73\n"
    "v_lshlrev_b32 v81, 16, v77\n"
    "v_and_b32 v82, 0xffff0000, v73\n"
    "v_and_b32 v83, 0xffff0000, v77\n"
    "v_pk_mul_f32 v[80:81], v[50:51], v[80:81] op_sel:[1,0] op_sel_hi:[1,1]\n"
    "v_pk_mul_f32 v[82:83], v[54:55], v[82:83] op_sel:[1,0] op_sel_hi:[1,1]\n"
    "v_cvt_pk_bf16_f32 v80, 0, v80\n"
    "v_cvt_pk_bf16_f32 v81, 0, v81\n"
    "v_cvt_pk_bf16_f32 v82, 0, v82\n"
    "v_cvt_pk_bf16_f32 v83, 0, v83\n"
    "v_pk_add_f32 v[80:81], v[80:81], v[28:29]\n"
    "v_pk_add_f32 v[82:83], v[82:83], v[30:31]\n"
    "v_cvt_pk_bf16_f32 v9, v80, v82\n"
    "v_cvt_pk_bf16_f32 v13, v81, v83\n"
    "ds_write_b32 v4, v9 offset:18564\n"
    "ds_write_b32 v4, v13 offset:19716\n"
    "v_lshlrev_b32 v80, 16, v74\n"
    "v_lshlrev_b32 v81, 16, v78\n"
    "v_and_b32 v82, 0xffff0000, v74\n"
    "v_and_b32 v83, 0xffff0000, v78\n"
    "v_pk_mul_f32 v[80:81], v[58:59], v[80:81] op_sel:[1,0] op_sel_hi:[1,1]\n"
    "v_pk_mul_f32 v[82:83], v[62:63], v[82:83] op_sel:[1,0] op_sel_hi:[1,1]\n"
    "v_cvt_pk_bf16_f32 v80, 0, v80\n"
    "v_cvt_pk_bf16_f32 v81, 0, v81\n"
    "v_cvt_pk_bf16_f32 v82, 0, v82\n"
    "v_cvt_pk_bf16_f32 v83, 0, v83\n"
    "v_pk_add_f32 v[80:81], v[80:81], v[32:33]\n"
    "v_pk_add_f32 v[82:83], v[82:83], v[34:35]\n"
    "v_cvt_pk_bf16_f32 v10, v80, v82\n"
    "v_cvt_pk_bf16_f32 v14, v81, v83\n"
    "ds_write_b32 v4, v10 offset:18568\n"
    "ds_write_b32 v4, v14 offset:19720\n"
    "s_waitcnt lgkmcnt(0)\n"
    "s_barrier\n"
    "s_branch END\n"
    "CH13_4:\n"
    "s_cbranch_execz CH13_5\n"
    "LN2K:\n"
    "s_setprio 0\n"
    "s_cmpk_ge_u32 s2, 128\n"
    "s_cbranch_scc1 CDONE\n"
    "s_and_b32 s57, s2, 7\n"
    "s_lshl_b32 s57, s57, 4\n"
    "s_lshr_b32 s43, s2, 3\n"
    "s_add_u32 s57, s57, s43\n"
    "v_and_b32 v1, 63, v0\n"
    "s_cmpk_lg_u32 s37, 12\n"
    "s_cbranch_scc1 LN2C\n"
    "s_setprio 3\n"
    "v_lshlrev_b32 v17, 4, v1\n"
    "v_add_u32 v17, 132512, v17\n"
    "v_and_b32 v2, 7, v0\n"
    "v_lshrrev_b32 v3, 5, v1\n"
    "v_lshl_add_u32 v2, v2, 1, v3\n"
    "v_lshlrev_b32 v3, 3, v2\n"
    "s_lshl_b32 s42, s57, 4\n"
    "s_sub_u32 s42, 2047, s42\n"
    "v_sub_u32 v2, s42, v2\n"
    "v_and_b32 v4, 15, v0\n"
    "v_sub_u32 v5, s42, v4\n"
    "v_lshlrev_b32 v6, 3, v4\n"
    "v_mul_u32_u24 v4, 48, v4\n"
    "v_add_u32 v4, 136608, v4\n"
    "s_lshl_b32 s42, s57, 7\n"
    "s_add_u32 s43, s42, 4096\n"
    "s_add_u32 s42, s42, 77824\n"
    "s_brev_b32 s46, 1\n"
    "s_movk_i32 s47, 0x7ff\n"
    "v_mov_b32 v21, s36\n"
    "s_mov_b32 s48, 0xff\n"
    "s_mov_b32 s49, 0xff\n"
    "v_min_u32 v7, 12, v1\n"
    "v_lshlrev_b32 v7, 3, v7\n"
    "v_add_u32 v7, 148736, v7\n"
    "v_mov_b32 v32, 0\n"
    "v_mov_b32 v33, 0\n"
    "s_mov_b64 exec, 0x1000\n"
    "ds_write_b64 v7, v[32:33]\n"
    "s_mov_b64 exec, -1\n"
    "s_waitcnt lgkmcnt(0)\n"
    "s_barrier\n"
    "ds_read_b128 v[8:11], v17\n"
    "ds_read_b128 v[12:15], v17 offset:1024\n"
    "ds_read_b128 v[24:27], v17 offset:2048\n"
    "ds_read_b128 v[28:31], v17 offset:3072\n"
    "ds_read_b64 v[32:33], v7\n"
    "s_waitcnt lgkmcnt(3)\n"
    "v_pk_add_f32 v[8:9], v[8:9], v[12:13]\n"
    "v_pk_add_f32 v[10:11], v[10:11], v[14:15]\n"
    "s_nop 1\n"
    "v_permlane32_swap_b32 v8, v10\n"
    "v_permlane32_swap_b32 v9, v11\n"
    "v_pk_add_f32 v[8:9], v[8:9], v[10:11]\n"
    "s_nop 1\n"
    "v_add_f32_dpp v8, v8, v8 row_ror:8 row_mask:0xf bank_mask:0xf\n"
    "v_add_f32_dpp v9, v9, v9 row_ror:8 row_mask:0xf bank_mask:0xf\n"
    "v_mov_b32 v10, v8\n"
    "v_mov_b32 v11, v9\n"
    "s_nop 0\n"
    "v_permlane16_swap_b32 v8, v10\n"
    "v_permlane16_swap_b32 v9, v11\n"
    "v_pk_add_f32 v[8:9], v[8:9], v[10:11]\n"
    "s_waitcnt lgkmcnt(0)\n"
    "v_pk_add_f32 v[24:25], v[24:25], v[28:29]\n"
    "v_pk_add_f32 v[26:27], v[26:27], v[30:31]\n"
    "v_add_f32_dpp v32, v32, v32 row_ror:8 row_mask:0xf bank_mask:0xf\n"
    "v_add_f32 v24, v24, v25\n"
    "v_add_f32 v26, v26, v27\n"
    "v_add_f32_dpp v32, v32, v32 row_ror:4 row_mask:0xf bank_mask:0xf\n"
    "v_mul_f32 v8, 0x3ae38e39, v8\n"
    "v_permlane32_swap_b32 v24, v26\n"
    "v_add_f32_dpp v32, v32, v32 row_ror:2 row_mask:0xf bank_mask:0xf\n"
    "v_add_f32 v24, v24, v26\n"
    "v_mul_f32 v9, 0x3ae38e39, v9\n"
    "v_add_f32_dpp v32, v32, v32 row_ror:1 row_mask:0xf bank_mask:0xf\n"
    "v_add_f32_dpp v24, v24, v24 row_ror:8 row_mask:0xf bank_mask:0xf\n"
    "v_fma_f32 v9, -v8, v8, v9\n"
    "v_mov_b32 v25, v24\n"
    "v_max_f32 v9, 0, v9\n"
    "v_cvt_pk_bf16_f32 v9, 0, v9\n"
    "v_permlane16_swap_b32 v24, v25\n"
    "v_add_f32 v9, 0x3727c5ac, v9\n"
    "v_readfirstlane_b32 s44, v32\n"
    "v_cvt_pk_bf16_f32 v9, 0, v9\n"
    "v_sqrt_f32 v9, v9\n"
    "v_add_f32 v24, v24, v25\n"
    "v_cvt_pk_bf16_f32 v8, v8, v9\n"
    "v_and_b32 v9, 0xffff0000, v8\n"
    "v_lshlrev_b32 v8, 16, v8\n"
    "v_rcp_f32 v9, v9\n"
    "v_fma_f32 v24, -v8, s44, v24\n"
    "v_mul_f32 v24, v24, v9\n"
    "v_and_b32 v9, 0x7ff, v24\n"
    "v_add_u32 v9, 0x7ff, v9\n"
    "v_or_b32 v24, v24, v9\n"
    "v_ashrrev_i32 v9, 31, v24\n"
    "v_bitop3_b32 v24, v9, v24, s46 bitop3:0x36\n"
    "v_bfi_b32 v20, s47, v2, v24\n"
    "s_mov_b64 exec, s[48:49]\n"
    "buffer_store_dwordx2 v[20:21], v3, s[16:19], s42 offen sc1\n"
    "s_cmpk_lg_u32 s57, 0\n"
    "s_cbranch_scc1 LN2K2\n"
    "s_mov_b64 exec, -1\n"
    "v_add_f32_dpp v33, v33, v33 row_ror:8 row_mask:0xf bank_mask:0xf\n"
    "s_nop 1\n"
    "v_add_f32_dpp v33, v33, v33 row_ror:4 row_mask:0xf bank_mask:0xf\n"
    "s_nop 1\n"
    "v_add_f32_dpp v33, v33, v33 row_ror:2 row_mask:0xf bank_mask:0xf\n"
    "s_nop 1\n"
    "v_add_f32_dpp v33, v33, v33 row_ror:1 row_mask:0xf bank_mask:0xf\n"
    "s_mov_b64 exec, 1\n"
    "v_sqrt_f32 v22, v33\n"
    "v_mov_b32 v23, s36\n"
    "v_mul_f32 v22, 0x3c343958, v22\n"
    "buffer_store_dwordx2 v[22:23], off, s[16:19], 0 offset:2048 sc1\n"
    "LN2K2:\n"
    "s_mov_b64 exec, 0xffff\n"
    "s_waitcnt lgkmcnt(0)\n"
    "s_barrier\n"
    "ds_read_b128 v[8:11], v4\n"
    "ds_read_b128 v[12:15], v4 offset:16\n"
    "ds_read_b128 v[24:27], v4 offset:32\n"
    "s_waitcnt lgkmcnt(1)\n"
    "v_pk_add_f32 v[8:9], v[8:9], v[12:13]\n"
    "v_pk_add_f32 v[10:11], v[10:11], v[14:15]\n"
    "s_waitcnt lgkmcnt(0)\n"
    "v_pk_add_f32 v[8:9], v[8:9], v[24:25]\n"
    "v_pk_add_f32 v[10:11], v[10:11], v[26:27]\n"
    "v_pk_add_f32 v[8:9], v[8:9], v[10:11]\n"
    "v_add_f32 v8, v8, v9\n"
    "v_and_b32 v9, 0x7ff, v8\n"
    "v_add_u32 v9, 0x7ff, v9\n"
    "v_or_b32 v8, v8, v9\n"
    "v_ashrrev_i32 v9, 31, v8\n"
    "v_bitop3_b32 v8, v9, v8, s46 bitop3:0x36\n"
    "v_bfi_b32 v20, s47, v5, v8\n"
    "buffer_store_dwordx2 v[20:21], v6, s[16:19], s43 offen sc1\n"
    "s_mov_b64 exec, -1\n"
    "s_branch END\n"
    "CH13_5:\n"
    "s_cbranch_execz AUX2\n"
    "LN2C:\n"
    "s_setprio 2\n"
    "v_subrev_u32 v1, 832, v0\n"
    "v_add_u32 v2, 0, v1\n"
    "v_mul_u32_u24 v3, 7282, v2\n"
    "v_lshrrev_b32 v3, 20, v3\n"
    "v_add_lshl_u32 v8, v2, v3, 4\n"
    "v_add_u32 v2, 192, v1\n"
    "v_mul_u32_u24 v3, 7282, v2\n"
    "v_lshrrev_b32 v3, 20, v3\n"
    "v_add_lshl_u32 v9, v2, v3, 4\n"
    "v_add_u32 v2, 384, v1\n"
    "v_mul_u32_u24 v3, 7282, v2\n"
    "v_lshrrev_b32 v3, 20, v3\n"
    "v_add_lshl_u32 v10, v2, v3, 4\n"
    "v_add_u32 v2, 576, v1\n"
    "v_mul_u32_u24 v3, 7282, v2\n"
    "v_lshrrev_b32 v3, 20, v3\n"
    "v_add_lshl_u32 v11, v2, v3, 4\n"
    "v_add_u32 v2, 768, v1\n"
    "v_mul_u32_u24 v3, 7282, v2\n"
    "v_lshrrev_b32 v3, 20, v3\n"
    "v_add_lshl_u32 v12, v2, v3, 4\n"
    "v_add_u32 v2, 960, v1\n"
    "v_mul_u32_u24 v3, 7282, v2\n"
    "v_lshrrev_b32 v3, 20, v3\n"
    "v_add_lshl_u32 v13, v2, v3, 4\n"
    "s_mul_i32 s42, s57, 18432\n"
    "v_lshl_add_u32 v1, v1, 4, s42\n"
    "v_add_u32 v2, 6144, v1\n"
    "v_add_u32 v3, 12288, v1\n"
    "s_mov_b32 s42, 7341056\n"
    "s_lshl_b32 s43, s57, 2\n"
    "v_mov_b32 v4, s36\n"
    "s_barrier\n"
    "s_barrier\n"
    "s_barrier\n"
    "ds_read_b128 v[40:43], v8 offset:18560\n"
    "ds_read_b128 v[44:47], v9 offset:18560\n"
    "ds_read_b128 v[48:51], v10 offset:18560\n"
    "ds_read_b128 v[52:55], v11 offset:18560\n"
    "ds_read_b128 v[56:59], v12 offset:18560\n"
    "ds_read_b128 v[60:63], v13 offset:18560\n"
    "s_waitcnt lgkmcnt(5)\n"
    "global_store_dwordx4 v1, v[40:43], s[34:35] offset:0 sc1\n"
    "s_waitcnt lgkmcnt(4)\n"
    "global_store_dwordx4 v1, v[44:47], s[34:35] offset:3072 sc1\n"
    "s_waitcnt lgkmcnt(3)\n"
    "global_store_dwordx4 v2, v[48:51], s[34:35] offset:0 sc1\n"
    "s_waitcnt lgkmcnt(2)\n"
    "global_store_dwordx4 v2, v[52:55], s[34:35] offset:3072 sc1\n"
    "s_waitcnt lgkmcnt(1)\n"
    "global_store_dwordx4 v3, v[56:59], s[34:35] offset:0 sc1\n"
    "s_waitcnt lgkmcnt(0)\n"
    "global_store_dwordx4 v3, v[60:63], s[34:35] offset:3072 sc1\n"
    "s_waitcnt vmcnt(0)\n"
    "s_barrier\n"
    "s_cmpk_lg_u32 s37, 15\n"
    "s_cbranch_scc1 END\n"
    "s_mov_b64 exec, 1\n"
    "buffer_store_dword v4, off, s[16:19], s43 offset:1024 sc1\n"
    "s_mov_b64 exec, -1\n"
    "s_branch END\n"
    "PREF:\n"
    "s_waitcnt lgkmcnt(0)\n"
    "s_barrier\n"
    "s_mov_b32 m0, 148480\n"
    "s_mov_b64 exec, s[62:63]\n"
    "s_cbranch_execz PFX_46\n"
    "global_load_lds_dword v47, s[58:59]\n"
    "s_mov_b64 exec, s[60:61]\n"
    "s_cbranch_execz PFX_46\n"
    "global_load_lds_dword v47, s[30:31]\n"
    "PFX_46:\n"
    "s_mov_b64 exec, -1\n"
    "CDONE:\n"
    "s_cmpk_lt_u32 s2, 184\n"
    "s_cbranch_scc1 END\n"
    "s_waitcnt vmcnt(0) lgkmcnt(0)\n"
    "s_barrier\n"
    "s_branch ES1b\n"
    "PAD18:\n"
    "s_cbranch_execz M1\n"
    "SIL1:\n"
    "s_mov_b64 exec, -1\n"
    "s_barrier\n"
    "v_add_u32 v1, 148736, v1\n"
    "v_mov_b32 v14, 147968\n"
    "v_mov_b32 v15, 1\n"
    "s_waitcnt vmcnt(0)\n"
    "v_lshlrev_b32 v5, 16, v4\n"
    "v_and_b32 v4, 0xffff0000, v4\n"
    "v_mul_f32 v6, 0xbfb8aa3b, v5\n"
    "v_mul_f32 v7, 0xbfb8aa3b, v4\n"
    "v_exp_f32 v6, v6\n"
    "v_exp_f32 v7, v7\n"
    "v_add_f32 v6, 1.0, v6\n"
    "v_add_f32 v7, 1.0, v7\n"
    "v_rcp_f32 v6, v6\n"
    "v_rcp_f32 v7, v7\n"
    "v_mul_f32 v5, v5, v6\n"
    "v_mul_f32 v4, v4, v7\n"
    "v_cvt_pk_bf16_f32 v4, v5, v4\n"
    "ds_write_b32 v1, v4\n"
    "s_mov_b64 exec, 1\n"
    "ds_add_u32 v14, v15 offset:4\n"
    "s_mov_b64 exec, -1\n"
    "s_branch M9\n"
    "M1:\n"
    "s_mov_b64 exec, -1\n"
    "s_waitcnt lgkmcnt(0)\n"
    "s_load_dword s36, s[16:17], 0\n"
    "s_barrier\n"
    "s_cmpk_ge_u32 s2, 246\n"
    "s_cbranch_scc1 M9\n"
    "s_sub_u32 s42, s2, 192\n"
    "s_lshr_b32 s42, s42, 3\n"
    "s_min_u32 s42, s42, 8\n"
    "s_lshr_b32 s44, s28, 7\n"
    "s_add_u32 s44, s44, s2\n"
    "s_sub_u32 s44, 3, s44\n"
    "s_and_b32 s44, s44, 7\n"
    "s_add_u32 s44, s44, 16\n"
    "s_sub_u32 s44, s44, s42\n"
    "s_mul_i32 s45, s44, 57\n"
    "s_lshr_b32 s45, s45, 9\n"
    "s_mul_i32 s45, s45, 9\n"
    "s_sub_u32 s44, s44, s45\n"
    "s_and_b32 s58, s2, 7\n"
    "s_mul_i32 s58, s58, 9\n"
    "s_add_u32 s58, s58, s44\n"
    "v_bfe_u32 v12, v0, 3, 3\n"
    "v_bfe_u32 v13, v0, 4, 1\n"
    "v_xor_b32 v13, v13, v0\n"
    "v_mul_u32_u24 v12, 9216, v12\n"
    "s_lshl_b32 s42, s37, 1\n"
    "v_xor_b32 v13, s42, v13\n"
    "v_and_b32 v13, 7, v13\n"
    "v_lshl_add_u32 v12, v13, 4, v12\n"
    "s_lshr_b32 s42, s37, 1\n"
    "s_mul_i32 s42, s42, 73728\n"
    "s_lshl_b32 s44, s58, 7\n"
    "s_add_u32 s42, s42, s44\n"
    "v_add_u32 v12, s42, v12\n"
    "s_bitcmp1_b32 s37, 0\n"
    "s_cselect_b64 s[56:57], s[30:31], s[28:29]\n"
    "s_sub_u32 s42, s2, 192\n"
    "s_lshr_b32 s43, s42, 1\n"
    "s_add_u32 s43, s43, 27\n"
    "s_and_b32 s44, s2, 1\n"
    "s_mul_i32 s45, s44, 1990656\n"
    "s_lshl_b32 s46, s43, 7\n"
    "s_add_u32 s45, s45, s46\n"
    "v_lshrrev_b32 v1, 3, v0\n"
    "v_mul_u32_u24 v1, 0x1b00, v1\n"
    "v_lshrrev_b32 v2, 3, v0\n"
    "v_lshrrev_b32 v3, 4, v0\n"
    "v_and_b32 v2, 2, v2\n"
    "v_and_b32 v3, 4, v3\n"
    "v_xor_b32 v2, v2, v0\n"
    "v_xor_b32 v2, v2, v3\n"
    "v_and_b32 v2, 7, v2\n"
    "v_lshl_add_u32 v1, v2, 4, v1\n"
    "v_add_u32 v1, s45, v1\n"
    "s_lshl_b32 s45, s37, 10\n"
    "s_add_u32 m0, s45, 98304\n"
    "s_nop 0\n"
    "global_load_lds_dwordx4 v1, s[14:15] nt\n"
    "s_add_u32 m0, m0, 4096\n"
    "v_add_u32 v1, 221184, v1\n"
    "global_load_lds_dwordx4 v1, s[14:15] nt\n"
    "s_add_u32 m0, m0, 4096\n"
    "v_add_u32 v1, 221184, v1\n"
    "global_load_lds_dwordx4 v1, s[14:15] nt\n"
    "s_add_u32 m0, m0, 4096\n"
    "v_add_u32 v1, 221184, v1\n"
    "global_load_lds_dwordx4 v1, s[14:15] nt\n"
    "s_add_u32 m0, m0, 4096\n"
    "v_add_u32 v1, 221184, v1\n"
    "s_setprio 3\n"
    "s_barrier\n"
    "global_load_lds_dwordx4 v1, s[14:15] nt\n"
    "s_add_u32 m0, m0, 4096\n"
    "v_add_u32 v1, 221184, v1\n"
    "global_load_lds_dwordx4 v1, s[14:15] nt\n"
    "s_add_u32 m0, m0, 4096\n"
    "v_add_u32 v1, 221184, v1\n"
    "global_load_lds_dwordx4 v1, s[14:15] nt\n"
    "s_add_u32 m0, m0, 4096\n"
    "v_add_u32 v1, 221184, v1\n"
    "global_load_lds_dwordx4 v1, s[14:15] nt\n"
    "s_add_u32 m0, m0, 4096\n"
    "v_add_u32 v1, 221184, v1\n"
    "global_load_lds_dwordx4 v1, s[14:15] nt\n"
    "s_add_u32 m0, m0, 4096\n"
    "v_add_u32 v1, 221184, v1\n"
    "v_bfe_u32 v5, v0, 4, 2\n"
    "v_bfe_u32 v6, v0, 2, 2\n"
    "v_bfe_u32 v7, v0, 1, 1\n"
    "s_lshl_b32 s45, s37, 1\n"
    "v_or_b32 v7, s45, v7\n"
    "v_and_b32 v8, 2, v6\n"
    "v_xor_b32 v7, v7, v8\n"
    "v_lshlrev_b32 v8, 2, v5\n"
    "v_and_b32 v8, 4, v8\n"
    "v_xor_b32 v7, v7, v8\n"
    "v_lshlrev_b32 v2, 10, v5\n"
    "v_lshl_add_u32 v2, v6, 7, v2\n"
    "v_lshl_add_u32 v2, v7, 4, v2\n"
    "v_and_b32 v8, 1, v0\n"
    "v_lshl_add_u32 v2, v8, 3, v2\n"
    "v_add_u32 v2, 98304, v2\n"
    "s_mul_i32 s45, s44, 576\n"
    "s_add_u32 s45, s45, 148736\n"
    "v_lshl_add_u32 v3, v5, 4, s45\n"
    "s_mul_i32 s45, s44, 27648\n"
    "s_lshl_b32 s46, s43, 9\n"
    "s_add_u32 s45, s45, s46\n"
    "s_lshl_b32 s46, s37, 7\n"
    "s_add_u32 s45, s45, s46\n"
    "s_add_u32 s45, s45, 20480\n"
    "v_and_b32 v4, 15, v0\n"
    "v_lshlrev_b32 v4, 3, v4\n"
    "v_mov_b32 v14, 147968\n"
    "v_mov_b32 v15, 1\n"
    "s_waitcnt lgkmcnt(0)\n"
    "s_add_u32 s36, s36, 1\n"
    "s_waitcnt vmcnt(0)\n"
    "s_mov_b64 exec, 1\n"
    "ds_add_u32 v14, v15 offset:4\n"
    "s_mov_b64 exec, -1\n"
    "s_branch E_48\n"
    "PAD19:\n"
    "s_cbranch_execz M1\n"
    "R_47:\n"
    "s_sub_u32 s39, s39, 1\n"
    "s_cbranch_scc1 ABORT\n"
    "E_48:\n"
    "ds_read_b32 v5, v14 offset:4\n"
    "s_waitcnt lgkmcnt(0)\n"
    "v_cmp_ne_u32 vcc, 9, v5\n"
    "s_cbranch_vccnz R_47\n"
    "ds_read_b128 v[16:19], v3 offset:0\n"
    "ds_read_b64_tr_b16 v[52:53], v2 offset:0\n"
    "ds_read_b64_tr_b16 v[54:55], v2 offset:512\n"
    "ds_read_b128 v[20:23], v3 offset:64\n"
    "ds_read_b64_tr_b16 v[56:57], v2 offset:4096\n"
    "ds_read_b64_tr_b16 v[58:59], v2 offset:4608\n"
    "ds_read_b128 v[24:27], v3 offset:128\n"
    "ds_read_b64_tr_b16 v[60:61], v2 offset:8192\n"
    "ds_read_b64_tr_b16 v[62:63], v2 offset:8704\n"
    "ds_read_b128 v[28:31], v3 offset:192\n"
    "ds_read_b64_tr_b16 v[64:65], v2 offset:12288\n"
    "ds_read_b64_tr_b16 v[66:67], v2 offset:12800\n"
    "ds_read_b128 v[32:35], v3 offset:256\n"
    "ds_read_b64_tr_b16 v[68:69], v2 offset:16384\n"
    "ds_read_b64_tr_b16 v[70:71], v2 offset:16896\n"
    "s_waitcnt lgkmcnt(12)\n"
    "v_mfma_f32_16x16x32_bf16 v[8:11], v[16:19], v[52:55], 0\n"
    "ds_read_b128 v[36:39], v3 offset:320\n"
    "ds_read_b64_tr_b16 v[72:73], v2 offset:20480\n"
    "ds_read_b64_tr_b16 v[74:75], v2 offset:20992\n"
    "s_waitcnt lgkmcnt(12)\n"
    "v_mfma_f32_16x16x32_bf16 v[8:11], v[20:23], v[56:59], v[8:11]\n"
    "ds_read_b128 v[40:43], v3 offset:384\n"
    "ds_read_b64_tr_b16 v[76:77], v2 offset:24576\n"
    "ds_read_b64_tr_b16 v[78:79], v2 offset:25088\n"
    "s_waitcnt lgkmcnt(12)\n"
    "v_mfma_f32_16x16x32_bf16 v[8:11], v[24:27], v[60:63], v[8:11]\n"
    "ds_read_b128 v[44:47], v3 offset:448\n"
    "ds_read_b64_tr_b16 v[80:81], v2 offset:28672\n"
    "ds_read_b64_tr_b16 v[82:83], v2 offset:29184\n"
    "s_waitcnt lgkmcnt(12)\n"
    "v_mfma_f32_16x16x32_bf16 v[8:11], v[28:31], v[64:67], v[8:11]\n"
    "ds_read_b128 v[48:51], v3 offset:512\n"
    "ds_read_b64_tr_b16 v[84:85], v2 offset:32768\n"
    "ds_read_b64_tr_b16 v[86:87], v2 offset:33280\n"
    "s_waitcnt lgkmcnt(12)\n"
    "v_mfma_f32_16x16x32_bf16 v[8:11], v[32:35], v[68:71], v[8:11]\n"
    "s_waitcnt lgkmcnt(9)\n"
    "v_mfma_f32_16x16x32_bf16 v[8:11], v[36:39], v[72:75], v[8:11]\n"
    "s_waitcnt lgkmcnt(6)\n"
    "v_mfma_f32_16x16x32_bf16 v[8:11], v[40:43], v[76:79], v[8:11]\n"
    "s_waitcnt lgkmcnt(3)\n"
    "v_mfma_f32_16x16x32_bf16 v[8:11], v[44:47], v[80:83], v[8:11]\n"
    "s_waitcnt lgkmcnt(0)\n"
    "v_mfma_f32_16x16x32_bf16 v[8:11], v[48:51], v[84:87], v[8:11]\n"
    "s_mov_b64 exec, 0xffff\n"
    "s_nop 6\n"
    "v_mov_b32 v9, s36\n"
    "buffer_store_dwordx2 v[8:9], v4, s[16:19], s45 offen sc1\n"
    "s_mov_b64 exec, -1\n"
    "s_setprio 0\n"
    "s_branch ES1c\n"
    "M9:\n"
    "s_waitcnt lgkmcnt(0)\n"
    "s_add_u32 s36, s36, 1\n"
    "ES1:\n"
    "s_barrier\n"
    "ES1b:\n"
    "s_sub_u32 s42, s2, 192\n"
    "s_lshr_b32 s42, s42, 3\n"
    "s_min_u32 s42, s42, 8\n"
    "s_lshr_b32 s44, s28, 7\n"
    "s_add_u32 s44, s44, s2\n"
    "s_sub_u32 s44, 3, s44\n"
    "s_and_b32 s44, s44, 7\n"
    "s_add_u32 s44, s44, 16\n"
    "s_sub_u32 s44, s44, s42\n"
    "s_mul_i32 s45, s44, 57\n"
    "s_lshr_b32 s45, s45, 9\n"
    "s_mul_i32 s45, s45, 9\n"
    "s_sub_u32 s44, s44, s45\n"
    "s_and_b32 s58, s2, 7\n"
    "s_mul_i32 s58, s58, 9\n"
    "s_add_u32 s58, s58, s44\n"
    "v_bfe_u32 v12, v0, 3, 3\n"
    "v_bfe_u32 v13, v0, 4, 1\n"
    "v_xor_b32 v13, v13, v0\n"
    "v_mul_u32_u24 v12, 9216, v12\n"
    "s_lshl_b32 s42, s37, 1\n"
    "v_xor_b32 v13, s42, v13\n"
    "v_and_b32 v13, 7, v13\n"
    "v_lshl_add_u32 v12, v13, 4, v12\n"
    "s_lshr_b32 s42, s37, 1\n"
    "s_mul_i32 s42, s42, 73728\n"
    "s_lshl_b32 s44, s58, 7\n"
    "s_add_u32 s42, s42, s44\n"
    "v_add_u32 v12, s42, v12\n"
    "s_bitcmp1_b32 s37, 0\n"
    "s_cselect_b64 s[56:57], s[30:31], s[28:29]\n"
    "s_cmpk_ge_u32 s37, 8\n"
    "s_cbranch_scc1 ES2\n"
    "s_sleep 3\n"
    "ES1c:\n"
    "s_lshl_b32 m0, s37, 10\n"
    "s_nop 0\n"
    "global_load_lds_dwordx4 v12, s[56:57] nt\n"
    "s_add_u32 m0, m0, 8192\n"
    "v_add_u32 v12, 294912, v12\n"
    "global_load_lds_dwordx4 v12, s[56:57] nt\n"
    "s_add_u32 m0, m0, 8192\n"
    "v_add_u32 v12, 294912, v12\n"
    "global_load_lds_dwordx4 v12, s[56:57] nt\n"
    "s_add_u32 m0, m0, 8192\n"
    "v_add_u32 v12, 294912, v12\n"
    "global_load_lds_dwordx4 v12, s[56:57] nt\n"
    "s_add_u32 m0, m0, 8192\n"
    "v_add_u32 v12, 294912, v12\n"
    "global_load_lds_dwordx4 v12, s[56:57] nt\n"
    "s_add_u32 m0, m0, 8192\n"
    "v_add_u32 v12, 294912, v12\n"
    "global_load_lds_dwordx4 v12, s[56:57] nt\n"
    "s_add_u32 m0, m0, 8192\n"
    "v_add_u32 v12, 294912, v12\n"
    "global_load_lds_dwordx4 v12, s[56:57] nt\n"
    "s_add_u32 m0, m0, 8192\n"
    "v_add_u32 v12, 294912, v12\n"
    "global_load_lds_dwordx4 v12, s[56:57] nt\n"
    "s_add_u32 m0, m0, 8192\n"
    "v_add_u32 v12, 294912, v12\n"
    "global_load_lds_dwordx4 v12, s[56:57] nt\n"
    "s_add_u32 m0, m0, 8192\n"
    "v_add_u32 v12, 294912, v12\n"
    "global_load_lds_dwordx4 v12, s[56:57] nt\n"
    "s_add_u32 m0, m0, 8192\n"
    "v_add_u32 v12, 294912, v12\n"
    "global_load_lds_dwordx4 v12, s[56:57] nt\n"
    "s_add_u32 m0, m0, 8192\n"
    "v_add_u32 v12, 294912, v12\n"
    "global_load_lds_dwordx4 v12, s[56:57] nt\n"
    "s_add_u32 m0, m0, 8192\n"
    "v_add_u32 v12, 294912, v12\n"
    "s_waitcnt vmcnt(3)\n"
    "ES2:\n"
    "s_barrier\n"
    "v_mov_b32 v14, 147968\n"
    "v_mov_b32 v15, 1\n"
    "v_and_b32 v1, 63, v0\n"
    "v_and_b32 v2, 15, v0\n"
    "v_bfe_u32 v3, v0, 4, 2\n"
    "s_cmpk_ge_u32 s37, 8\n"
    "s_cbranch_scc1 MOED\n"
    "global_load_lds_dwordx4 v12, s[56:57] nt\n"
    "s_add_u32 m0, m0, 8192\n"
    "v_add_u32 v12, 294912, v12\n"
    "global_load_lds_dwordx4 v12, s[56:57] nt\n"
    "s_add_u32 m0, m0, 8192\n"
    "v_add_u32 v12, 294912, v12\n"
    "v_bfe_u32 v6, v0, 1, 1\n"
    "v_bfe_u32 v7, v0, 2, 2\n"
    "v_lshrrev_b32 v8, 1, v7\n"
    "v_lshl_add_u32 v8, v6, 1, v8\n"
    "v_and_b32 v9, 1, v3\n"
    "PAD20:\n"
    "s_cbranch_execz SIL1\n"
    "v_lshl_add_u32 v8, v9, 2, v8\n"
    "v_xor_b32 v8, s37, v8\n"
    "v_lshlrev_b32 v4, 11, v3\n"
    "v_lshl_add_u32 v4, v6, 10, v4\n"
    "v_lshl_add_u32 v4, v7, 7, v4\n"
    "v_lshl_add_u32 v4, v8, 4, v4\n"
    "v_and_b32 v9, 1, v0\n"
    "v_lshl_add_u32 v4, v9, 3, v4\n"
    "v_add_u32 v5, 0x10000, v4\n"
    "v_add_u32 v6, 0x20000, v4\n"
    "ds_read_b64_tr_b16 v[56:57], v4 offset:0\n"
    "ds_read_b64_tr_b16 v[58:59], v4 offset:512\n"
    "ds_read_b64_tr_b16 v[60:61], v4 offset:8192\n"
    "ds_read_b64_tr_b16 v[62:63], v4 offset:8704\n"
    "ds_read_b64_tr_b16 v[64:65], v4 offset:16384\n"
    "ds_read_b64_tr_b16 v[66:67], v4 offset:16896\n"
    "ds_read_b64_tr_b16 v[68:69], v4 offset:24576\n"
    "ds_read_b64_tr_b16 v[70:71], v4 offset:25088\n"
    "ds_read_b64_tr_b16 v[72:73], v4 offset:32768\n"
    "ds_read_b64_tr_b16 v[74:75], v4 offset:33280\n"
    "ds_read_b64_tr_b16 v[76:77], v4 offset:40960\n"
    "ds_read_b64_tr_b16 v[78:79], v4 offset:41472\n"
    "ds_read_b64_tr_b16 v[80:81], v4 offset:49152\n"
    "ds_read_b64_tr_b16 v[82:83], v4 offset:49664\n"
    "ds_read_b64_tr_b16 v[84:85], v4 offset:57344\n"
    "ds_read_b64_tr_b16 v[86:87], v4 offset:57856\n"
    "ds_read_b64_tr_b16 v[88:89], v5 offset:0\n"
    "ds_read_b64_tr_b16 v[90:91], v5 offset:512\n"
    "s_waitcnt lgkmcnt(0)\n"
    "s_barrier\n"
    "global_load_lds_dwordx4 v12, s[56:57] nt\n"
    "s_add_u32 m0, m0, 8192\n"
    "v_add_u32 v12, 294912, v12\n"
    "global_load_lds_dwordx4 v12, s[56:57] nt\n"
    "s_add_u32 m0, m0, 8192\n"
    "v_add_u32 v12, 294912, v12\n"
    "global_load_lds_dwordx4 v12, s[56:57] nt\n"
    "s_add_u32 m0, m0, 8192\n"
    "v_add_u32 v12, 294912, v12\n"
    "global_load_lds_dwordx4 v12, s[56:57] nt\n"
    "s_add_u32 m0, m0, 8192\n"
    "v_add_u32 v12, 294912, v12\n"
    "v_lshrrev_b32 v12, 3, v0\n"
    "v_and_b32 v13, 2, v12\n"
    "v_mul_u32_u24 v12, 1152, v12\n"
    "v_lshrrev_b32 v7, 4, v0\n"
    "v_and_b32 v7, 4, v7\n"
    "v_xor_b32 v13, v13, v7\n"
    "v_xor_b32 v13, v13, v0\n"
    "v_and_b32 v13, 7, v13\n"
    "v_lshl_add_u32 v12, v13, 4, v12\n"
    "s_mul_i32 s42, s58, 73728\n"
    "v_add_u32 v12, s42, v12\n"
    "s_mul_i32 m0, s37, 9216\n"
    "s_nop 0\n"
    "global_load_lds_dwordx4 v12, s[32:33] offset:0 nt\n"
    "s_add_u32 m0, m0, 896\n"
    "s_nop 0\n"
    "global_load_lds_dwordx4 v12, s[32:33] offset:128 nt\n"
    "s_add_u32 m0, m0, 896\n"
    "s_nop 0\n"
    "global_load_lds_dwordx4 v12, s[32:33] offset:256 nt\n"
    "s_add_u32 m0, m0, 896\n"
    "s_nop 0\n"
    "global_load_lds_dwordx4 v12, s[32:33] offset:384 nt\n"
    "s_add_u32 m0, m0, 896\n"
    "s_nop 0\n"
    "global_load_lds_dwordx4 v12, s[32:33] offset:512 nt\n"
    "s_add_u32 m0, m0, 896\n"
    "s_nop 0\n"
    "global_load_lds_dwordx4 v12, s[32:33] offset:640 nt\n"
    "s_add_u32 m0, m0, 896\n"
    "s_nop 0\n"
    "global_load_lds_dwordx4 v12, s[32:33] offset:768 nt\n"
    "s_add_u32 m0, m0, 896\n"
    "s_nop 0\n"
    "global_load_lds_dwordx4 v12, s[32:33] offset:896 nt\n"
    "s_add_u32 m0, m0, 896\n"
    "s_nop 0\n"
    "global_load_lds_dwordx4 v12, s[32:33] offset:1024 nt\n"
    "s_add_u32 m0, m0, 896\n"
    "s_waitcnt vmcnt(9)\n"
    "s_barrier\n"
    "ds_read_b64_tr_b16 v[92:93], v5 offset:8192\n"
    "ds_read_b64_tr_b16 v[94:95], v5 offset:8704\n"
    "ds_read_b64_tr_b16 v[96:97], v5 offset:16384\n"
    "ds_read_b64_tr_b16 v[98:99], v5 offset:16896\n"
    "ds_read_b64_tr_b16 v[100:101], v5 offset:24576\n"
    "ds_read_b64_tr_b16 v[102:103], v5 offset:25088\n"
    "ds_read_b64_tr_b16 v[104:105], v5 offset:32768\n"
    "ds_read_b64_tr_b16 v[106:107], v5 offset:33280\n"
    "ds_read_b64_tr_b16 v[108:109], v5 offset:40960\n"
    "ds_read_b64_tr_b16 v[110:111], v5 offset:41472\n"
    "ds_read_b64_tr_b16 v[112:113], v5 offset:49152\n"
    "ds_read_b64_tr_b16 v[114:115], v5 offset:49664\n"
    "ds_read_b64_tr_b16 v[116:117], v5 offset:57344\n"
    "ds_read_b64_tr_b16 v[118:119], v5 offset:57856\n"
    "ds_read_b64_tr_b16 v[120:121], v6 offset:0\n"
    "ds_read_b64_tr_b16 v[122:123], v6 offset:512\n"
    "ds_read_b64_tr_b16 v[124:125], v6 offset:8192\n"
    "ds_read_b64_tr_b16 v[126:127], v6 offset:8704\n"
    "s_waitcnt vmcnt(0)\n"
    "s_barrier\n"
    "v_and_b32 v7, 7, v0\n"
    "v_lshlrev_b32 v8, 3, v7\n"
    "v_and_b32 v9, 8, v0\n"
    "s_waitcnt lgkmcnt(0)\n"
    "v_add_u32 v4, 151168, v8\n"
    "v_lshlrev_b32 v5, 3, v9\n"
    "v_lshl_add_u32 v5, v3, 4, v5\n"
    "v_add_u32 v5, 73728, v5\n"
    "v_and_b32 v6, 1, v3\n"
    "v_lshlrev_b32 v6, 2, v6\n"
    "v_and_b32 v8, 2, v3\n"
    "v_lshrrev_b32 v9, 3, v9\n"
    "v_add3_u32 v6, v6, v8, v9\n"
    "v_mul_u32_u24 v6, 144, v6\n"
    "v_lshl_add_u32 v6, v7, 1, v6\n"
    "s_lshl_b32 s42, s37, 4\n"
    "s_add_u32 s42, s42, 150016\n"
    "v_add_u32 v6, s42, v6\n"
    "s_branch MOE2\n"
    "PAD21:\n"
    "s_cbranch_execz SIL1\n"
    "MOED:\n"
    "v_bfe_u32 v6, v0, 2, 2\n"
    "v_bfe_u32 v7, v0, 1, 1\n"
    "v_and_b32 v8, 2, v6\n"
    "v_or_b32 v7, v7, v8\n"
    "v_and_b32 v8, 1, v3\n"
    "v_lshl_add_u32 v7, v8, 2, v7\n"
    "v_mul_u32_u24 v8, 9216, v3\n"
    "v_lshl_add_u32 v8, v6, 7, v8\n"
    "v_lshl_add_u32 v8, v7, 4, v8\n"
    "v_and_b32 v7, 1, v0\n"
    "v_lshl_add_u32 v8, v7, 3, v8\n"
    "s_mul_i32 s42, s37, 6\n"
    "s_sub_u32 s42, s42, 48\n"
    "s_and_b32 s43, s42, 3\n"
    "s_lshl_b32 s43, s43, 5\n"
    "s_lshr_b32 s44, s42, 2\n"
    "s_lshl_b32 s44, s44, 10\n"
    "v_xor_b32 v16, s43, v8\n"
    "v_add_u32 v16, s44, v16\n"
    "s_add_u32 s42, s42, 1\n"
    "s_and_b32 s43, s42, 3\n"
    "s_lshl_b32 s43, s43, 5\n"
    "s_lshr_b32 s44, s42, 2\n"
    "s_lshl_b32 s44, s44, 10\n"
    "v_xor_b32 v17, s43, v8\n"
    "v_add_u32 v17, s44, v17\n"
    "s_add_u32 s42, s42, 1\n"
    "s_and_b32 s43, s42, 3\n"
    "s_lshl_b32 s43, s43, 5\n"
    "s_lshr_b32 s44, s42, 2\n"
    "s_lshl_b32 s44, s44, 10\n"
    "v_xor_b32 v18, s43, v8\n"
    "v_add_u32 v18, s44, v18\n"
    "s_add_u32 s42, s42, 1\n"
    "s_and_b32 s43, s42, 3\n"
    "s_lshl_b32 s43, s43, 5\n"
    "s_lshr_b32 s44, s42, 2\n"
    "s_lshl_b32 s44, s44, 10\n"
    "v_xor_b32 v19, s43, v8\n"
    "v_add_u32 v19, s44, v19\n"
    "s_add_u32 s42, s42, 1\n"
    "s_and_b32 s43, s42, 3\n"
    "s_lshl_b32 s43, s43, 5\n"
    "s_lshr_b32 s44, s42, 2\n"
    "s_lshl_b32 s44, s44, 10\n"
    "v_xor_b32 v20, s43, v8\n"
    "v_add_u32 v20, s44, v20\n"
    "s_add_u32 s42, s42, 1\n"
    "s_and_b32 s43, s42, 3\n"
    "s_lshl_b32 s43, s43, 5\n"
    "s_lshr_b32 s44, s42, 2\n"
    "s_lshl_b32 s44, s44, 10\n"
    "v_xor_b32 v21, s43, v8\n"
    "v_add_u32 v21, s44, v21\n"
    "s_add_u32 s42, s42, 1\n"
    "v_and_b32 v7, 7, v0\n"
    "v_mul_u32_u24 v7, 144, v7\n"
    "v_lshl_add_u32 v7, v3, 4, v7\n"
    "v_add_u32 v7, 150016, v7\n"
    "v_mov_b32 v8, 148224\n"
    "v_cmp_gt_u32 vcc, 8, v2\n"
    "s_nop 1\n"
    "v_cndmask_b32 v4, v8, v7, vcc\n"
    "v_cndmask_b32 v5, v7, v8, vcc\n"
    "v_and_b32 v6, 1, v3\n"
    "v_mul_u32_u24 v6, 6144, v6\n"
    "v_lshl_add_u32 v6, v2, 4, v6\n"
    "v_lshrrev_b32 v7, 1, v3\n"
    "v_lshl_add_u32 v6, v7, 8, v6\n"
    "s_mul_i32 s42, s58, 12416\n"
    "s_lshl_b32 s43, s37, 9\n"
    "s_add_u32 s42, s42, s43\n"
    "s_add_u32 s42, s42, 6443008\n"
    "v_add_u32 v6, s42, v6\n"
    "s_barrier\n"
    "s_barrier\n"
    "s_barrier\n"
    "s_cmpk_ge_u32 s37, 14\n"
    "s_cbranch_scc1 MOE2\n"
    "ds_read_b64_tr_b16 v[56:57], v16 offset:0\n"
    "ds_read_b64_tr_b16 v[58:59], v16 offset:512\n"
    "ds_read_b64_tr_b16 v[60:61], v17 offset:0\n"
    "ds_read_b64_tr_b16 v[62:63], v17 offset:512\n"
    "ds_read_b64_tr_b16 v[64:65], v18 offset:0\n"
    "ds_read_b64_tr_b16 v[66:67], v18 offset:512\n"
    "ds_read_b64_tr_b16 v[68:69], v19 offset:0\n"
    "ds_read_b64_tr_b16 v[70:71], v19 offset:512\n"
    "ds_read_b64_tr_b16 v[72:73], v20 offset:0\n"
    "ds_read_b64_tr_b16 v[74:75], v20 offset:512\n"
    "ds_read_b64_tr_b16 v[76:77], v21 offset:0\n"
    "ds_read_b64_tr_b16 v[78:79], v21 offset:512\n"
    "ds_read_b64_tr_b16 v[80:81], v16 offset:36864\n"
    "ds_read_b64_tr_b16 v[82:83], v16 offset:37376\n"
    "ds_read_b64_tr_b16 v[84:85], v17 offset:36864\n"
    "ds_read_b64_tr_b16 v[86:87], v17 offset:37376\n"
    "ds_read_b64_tr_b16 v[88:89], v18 offset:36864\n"
    "ds_read_b64_tr_b16 v[90:91], v18 offset:37376\n"
    "ds_read_b64_tr_b16 v[92:93], v19 offset:36864\n"
    "ds_read_b64_tr_b16 v[94:95], v19 offset:37376\n"
    "ds_read_b64_tr_b16 v[96:97], v20 offset:36864\n"
    "ds_read_b64_tr_b16 v[98:99], v20 offset:37376\n"
    "ds_read_b64_tr_b16 v[100:101], v21 offset:36864\n"
    "ds_read_b64_tr_b16 v[102:103], v21 offset:37376\n"
    "s_waitcnt lgkmcnt(0)\n"
    "MOE2:\n"
    "s_lshl_b32 s42, s37, 2\n"
    "s_add_u32 s42, s42, 0\n"
    "v_mov_b32 v7, s42\n"
    "v_lshlrev_b32 v8, 4, v1\n"
    "v_and_b32 v9, 31, v1\n"
    "v_add_u32 v9, 64, v9\n"
    "s_mul_i32 s42, s37, 1168\n"
    "s_add_u32 s42, s42, 73728\n"
    "v_mad_u32_u24 v10, v1, 12, s42\n"
    "v_mad_u32_u24 v11, v9, 12, s42\n"
    "v_lshlrev_b32 v9, 4, v9\n"
    "v_mov_b32 v12, 148096\n"
    "s_mov_b32 s59, 7341056\n"
    "s_movk_i32 s60, 1536\n"
    "s_movk_i32 s61, 0x7ff\n"
    "s_lshl_b32 s62, s37, 2\n"
    "s_add_u32 s63, s36, 1\n"
    "s_cmpk_ge_u32 s37, 12\n"
    "s_cbranch_scc1 TOPK\n"
    "s_barrier\n"
    "s_barrier\n"
    "s_branch MOE3\n"
    "TOPK:\n"
    "s_setprio 3\n"
    "s_mul_i32 s42, s37, 9\n"
    "s_add_u32 s42, s42, s58\n"
    "s_mul_i32 s43, s42, 1821\n"
    "s_lshr_b32 s43, s43, 16\n"
    "s_mul_i32 s43, s43, 36\n"
    "s_sub_u32 s42, s42, s43\n"
    "s_lshl_b32 s42, s42, 7\n"
    "s_sub_u32 s42, 94084, s42\n"
    "v_lshlrev_b32 v13, 4, v0\n"
    "s_mov_b32 s44, 65536\n"
    "s_mov_b32 s45, 69632\n"
    "s_mov_b32 s46, 73728\n"
    "s_mov_b32 s47, 77824\n"
    "v_lshl_add_u32 v16, v2, 2, v12\n"
    "v_lshl_add_u32 v17, v3, 4, v12\n"
    "s_brev_b32 s48, 1\n"
    "v_mov_b32 v18, 4\n"
    "v_lshlrev_b32 v19, 2, v2\n"
    "v_mul_u32_u24 v52, 1168, v2\n"
    "s_branch E_50\n"
    "R_49:\n"
    "s_sleep 4\n"
    "s_sub_u32 s39, s39, 1\n"
    "s_cbranch_scc1 ABORT\n"
    "E_50:\n"
    "buffer_load_dword v20, off, s[16:19], s42 sc1\n"
    "s_waitcnt vmcnt(0)\n"
    "v_cmp_ne_u32 vcc, s36, v20\n"
    "s_cbranch_vccnz R_49\n"
    "buffer_load_dwordx4 v[24:27], v13, s[16:19], s44 offen sc1\n"
    "buffer_load_dwordx4 v[28:31], v13, s[16:19], s45 offen sc1\n"
    "buffer_load_dwordx4 v[32:35], v13, s[16:19], s46 offen sc1\n"
    "buffer_load_dwordx4 v[36:39], v13, s[16:19], s47 offen sc1\n"
    "buffer_load_dwordx2 v[40:41], off, s[16:19], 0 offset:2048 sc1\n"
    "s_branch E_52\n"
    "PAD28:\n"
    "s_cbranch_execz SIL1\n"
    "R_51:\n"
    "s_sub_u32 s39, s39, 1\n"
    "s_cbranch_scc1 ABORT\n"
    "s_wqm_b64 exec, vcc\n"
    "buffer_load_dwordx4 v[24:27], v13, s[16:19], s44 offen sc1\n"
    "buffer_load_dwordx4 v[28:31], v13, s[16:19], s45 offen sc1\n"
    "buffer_load_dwordx4 v[32:35], v13, s[16:19], s46 offen sc1\n"
    "buffer_load_dwordx4 v[36:39], v13, s[16:19], s47 offen sc1\n"
    "buffer_load_dwordx2 v[40:41], off, s[16:19], 0 offset:2048 sc1\n"
    "s_mov_b64 exec, -1\n"
    "E_52:\n"
    "s_waitcnt vmcnt(3)\n"
    "v_bitop3_b32 v20, v25, v27, s36 bitop3:0x7e\n"
    "v_bitop3_b32 v21, v29, v31, s36 bitop3:0x7e\n"
    "v_max3_u32 v42, v24, v26, v28\n"
    "s_waitcnt vmcnt(2)\n"
    "v_bitop3_b32 v22, v33, v35, s36 bitop3:0x7e\n"
    "v_max3_u32 v42, v42, v30, v32\n"
    "v_or3_b32 v20, v20, v21, v22\n"
    "s_waitcnt vmcnt(0)\n"
    "v_bitop3_b32 v21, v37, v39, s36 bitop3:0x7e\n"
    "v_xor_b32 v22, s36, v41\n"
    "v_max3_u32 v42, v42, v34, v36\n"
    "v_or3_b32 v20, v20, v21, v22\n"
    "v_max_u32 v42, v42, v38\n"
    "v_cmp_ne_u32 vcc, 0, v20\n"
    "s_cbranch_vccnz R_51\n"
    "ds_max_u32 v16, v42\n"
    "v_max3_u32 v45, v24, v26, v28\n"
    "v_med3_u32 v43, v24, v26, v28\n"
    "v_min3_u32 v44, v24, v26, v28\n"
    "v_med3_u32 v44, v43, v44, v30\n"
    "v_med3_u32 v43, v45, v43, v30\n"
    "v_max_u32 v45, v45, v30\n"
    "v_med3_u32 v44, v43, v44, v32\n"
    "v_med3_u32 v43, v45, v43, v32\n"
    "s_waitcnt lgkmcnt(0)\n"
    "s_barrier\n"
    "ds_read_b32 v20, v16\n"
    "ds_read_b128 v[46:49], v17\n"
    "v_max_u32 v45, v45, v32\n"
    "v_med3_u32 v44, v43, v44, v34\n"
    "v_med3_u32 v43, v45, v43, v34\n"
    "v_max_u32 v45, v45, v34\n"
    "v_med3_u32 v44, v43, v44, v36\n"
    "v_med3_u32 v43, v45, v43, v36\n"
    "v_max_u32 v45, v45, v36\n"
    "v_med3_u32 v44, v43, v44, v38\n"
    "v_med3_u32 v43, v45, v43, v38\n"
    "s_waitcnt lgkmcnt(0)\n"
    "v_cmp_gt_u32 s[50:51], v46, v20\n"
    "v_cmp_gt_u32 s[52:53], v47, v20\n"
    "v_cmp_gt_u32 s[54:55], v48, v20\n"
    "v_cmp_gt_u32 vcc, v49, v20\n"
    "v_cndmask_b32 v21, 0, 1, s[50:51]\n"
    "v_addc_co_u32 v21, s[50:51], 0, v21, s[52:53]\n"
    "v_addc_co_u32 v21, s[50:51], 0, v21, s[54:55]\n"
    "v_addc_co_u32 v21, vcc, 0, v21, vcc\n"
    "v_mov_b32 v22, v21\n"
    "v_ashrrev_i32 v23, 31, v20\n"
    "v_bitop3_b32 v23, v23, v20, s48 bitop3:0x63\n"
    "v_permlane16_swap_b32 v21, v22\n"
    "v_add_u32 v21, v21, v22\n"
    "v_mov_b32 v22, v21\n"
    "v_sub_f32 v23, v23, v40\n"
    "v_ashrrev_i32 v46, 31, v23\n"
    "v_permlane32_swap_b32 v21, v22\n"
    "v_bitop3_b32 v23, v46, v23, s48 bitop3:0x36\n"
    "v_add_u32 v21, v21, v22\n"
    "v_min_u32 v23, v23, v20\n"
    "v_cmp_eq_u32 vcc, 7, v21\n"
    "s_ff1_i32_b64 s49, vcc\n"
    "v_readlane_b32 s49, v23, s49\n"
    "s_andn2_b32 s49, s49, s61\n"
    "s_nop 0\n"
    "v_cmp_le_u32 s[50:51], s49, v43\n"
    "v_cmp_le_u32 s[52:53], s49, v44\n"
    "v_cmpx_le_u32 vcc, s49, v42\n"
    "v_cndmask_b32 v21, 4, 8, s[50:51]\n"
    "s_cmp_lg_u64 s[52:53], 0\n"
    "s_cbranch_scc1 TOPS\n"
    "ds_add_rtn_u32 v21, v12, v21 offset:64\n"
    "s_waitcnt lgkmcnt(0)\n"
    "ds_write_b32 v21, v42 offset:0\n"
    "s_mov_b64 exec, s[50:51]\n"
    "ds_write_b32 v21, v43 offset:4\n"
    "TOPC:\n"
    "s_mov_b64 exec, -1\n"
    "s_waitcnt lgkmcnt(0)\n"
    "s_barrier\n"
    "MOE3:\n"
    "ds_read_b32 v20, v12 offset:64\n"
    "ds_read_b32 v21, v7\n"
    "s_cmpk_eq_u32 s37, 15\n"
    "s_cbranch_scc1 MOE15\n"
    "s_waitcnt lgkmcnt(0)\n"
    "v_readfirstlane_b32 s42, v20\n"
    "v_bitop3_b32 v21, v21, s61, v21 bitop3:0x0c\n"
    "s_cmpk_gt_u32 s42, 64\n"
    "s_cbranch_scc1 MANY\n"
    "MOE4:\n"
    "v_mad_u32_u24 v22, v21, s60, v8\n"
    "v_mad_u32_u24 v23, v21, s60, v9\n"
    "s_cmp_le_u32 s42, s62\n"
    "s_cbranch_scc1 MOE5\n"
    "R_53:\n"
    "buffer_load_dwordx4 v[24:27], v22, s[16:19], s59 offen sc1\n"
    "buffer_load_dwordx4 v[28:31], v23, s[16:19], s59 offen sc1\n"
    "s_waitcnt vmcnt(1)\n"
    "ds_write2_b32 v10, v24, v25 offset1:1\n"
    "ds_write_b32 v10, v26 offset:8\n"
    "s_waitcnt vmcnt(0)\n"
    "ds_write2_b32 v11, v28, v29 offset1:1\n"
    "ds_write_b32 v11, v30 offset:8\n"
    "v_bitop3_b32 v20, v27, v31, s63 bitop3:0x7e\n"
    "v_cmp_ne_u32 vcc, 0, v20\n"
    "s_cbranch_vccnz ROWS\n"
    "MOE5:\n"
    "s_waitcnt lgkmcnt(0)\n"
    "s_barrier\n"
    "s_cmpk_ge_u32 s37, 8\n"
    "s_cbranch_scc1 MOE6\n"
    "ds_read_b32 v7, v4\n"
    "s_waitcnt lgkmcnt(0)\n"
    "v_add_u32 v7, v7, v5\n"
    "ds_read_b128 v[20:23], v7 offset:0\n"
    "ds_read_b128 v[24:27], v7 offset:128\n"
    "ds_read_b128 v[28:31], v7 offset:256\n"
    "ds_read_b128 v[32:35], v7 offset:384\n"
    "ds_read_b128 v[36:39], v7 offset:512\n"
    "ds_read_b128 v[40:43], v7 offset:640\n"
    "ds_read_b128 v[44:47], v7 offset:768\n"
    "ds_read_b128 v[48:51], v7 offset:896\n"
    "ds_read_b128 v[52:55], v7 offset:1024\n"
    "s_waitcnt lgkmcnt(8)\n"
    "v_mfma_f32_16x16x32_bf16 v[12:15], v[20:23], v[56:59], 0\n"
    "v_mfma_f32_16x16x32_bf16 v[16:19], v[20:23], v[60:63], 0\n"
    "s_waitcnt lgkmcnt(7)\n"
    "v_mfma_f32_16x16x32_bf16 v[12:15], v[24:27], v[64:67], v[12:15]\n"
    "v_mfma_f32_16x16x32_bf16 v[16:19], v[24:27], v[68:71], v[16:19]\n"
    "s_waitcnt lgkmcnt(6)\n"
    "v_mfma_f32_16x16x32_bf16 v[12:15], v[28:31], v[72:75], v[12:15]\n"
    "v_mfma_f32_16x16x32_bf16 v[16:19], v[28:31], v[76:79], v[16:19]\n"
    "s_waitcnt lgkmcnt(5)\n"
    "v_mfma_f32_16x16x32_bf16 v[12:15], v[32:35], v[80:83], v[12:15]\n"
    "v_mfma_f32_16x16x32_bf16 v[16:19], v[32:35], v[84:87], v[16:19]\n"
    "s_waitcnt lgkmcnt(4)\n"
    "v_mfma_f32_16x16x32_bf16 v[12:15], v[36:39], v[88:91], v[12:15]\n"
    "v_mfma_f32_16x16x32_bf16 v[16:19], v[36:39], v[92:95], v[16:19]\n"
    "s_waitcnt lgkmcnt(3)\n"
    "v_mfma_f32_16x16x32_bf16 v[12:15], v[40:43], v[96:99], v[12:15]\n"
    "v_mfma_f32_16x16x32_bf16 v[16:19], v[40:43], v[100:103], v[16:19]\n"
    "s_waitcnt lgkmcnt(2)\n"
    "v_mfma_f32_16x16x32_bf16 v[12:15], v[44:47], v[104:107], v[12:15]\n"
    "v_mfma_f32_16x16x32_bf16 v[16:19], v[44:47], v[108:111], v[16:19]\n"
    "s_waitcnt lgkmcnt(1)\n"
    "v_mfma_f32_16x16x32_bf16 v[12:15], v[48:51], v[112:115], v[12:15]\n"
    "v_mfma_f32_16x16x32_bf16 v[16:19], v[48:51], v[116:119], v[16:19]\n"
    "s_waitcnt lgkmcnt(0)\n"
    "v_mfma_f32_16x16x32_bf16 v[12:15], v[52:55], v[120:123], v[12:15]\n"
    "v_mfma_f32_16x16x32_bf16 v[16:19], v[52:55], v[124:127], v[16:19]\n"
    "s_nop 6\n"
    "v_permlane32_swap_b32 v12, v14\n"
    "v_permlane32_swap_b32 v13, v15\n"
    "v_permlane32_swap_b32 v16, v18\n"
    "v_permlane32_swap_b32 v17, v19\n"
    "v_add_f32 v12, v12, v18\n"
    "PAD29:\n"
    "s_cbranch_execz SIL1\n"
    "v_add_f32 v13, v13, v19\n"
    "v_mov_b32 v14, v12\n"
    "s_nop 0\n"
    "v_mov_b32_dpp v12, v13 row_ror:8 row_mask:0xf bank_mask:0xc\n"
    "v_mov_b32_dpp v13, v14 row_ror:8 row_mask:0xf bank_mask:0x3\n"
    "v_cvt_pk_bf16_f32 v12, 0, v12\n"
    "v_mul_f32 v14, 0xbfb8aa3b, v12\n"
    "v_exp_f32 v14, v14\n"
    "v_cvt_pk_bf16_f32 v13, 0, v13\n"
    "v_add_f32 v14, 1.0, v14\n"
    "v_rcp_f32 v14, v14\n"
    "s_nop 0\n"
    "v_mul_f32 v12, v12, v14\n"
    "v_cvt_pk_bf16_f32 v12, 0, v12\n"
    "v_mul_f32 v12, v12, v13\n"
    "v_cvt_pk_bf16_f32 v12, v12, v12\n"
    "ds_write_b16 v6, v12\n"
    "s_waitcnt lgkmcnt(0)\n"
    "s_barrier\n"
    "s_cmpk_lg_u32 s2, 192\n"
    "s_cbranch_scc1 END\n"
    "s_cmpk_lg_u32 s37, 0\n"
    "s_cbranch_scc1 END\n"
    "v_mov_b32 v1, s63\n"
    "s_mov_b64 exec, 1\n"
    "buffer_store_dword v1, off, s[16:19], 0 sc1\n"
    "s_mov_b64 exec, -1\n"
    "s_branch END\n"
    "MOE6:\n"
    "s_cmpk_lt_u32 s37, 13\n"
    "s_cbranch_scc1 DOWN\n"
    "s_cmpk_lt_u32 s2, 192\n"
    "s_cbranch_scc1 DOWN\n"
    "s_and_b32 s42, s2, 7\n"
    "s_lshl_b32 s42, s42, 3\n"
    "s_sub_u32 s43, s2, 192\n"
    "s_lshr_b32 s43, s43, 3\n"
    "s_add_u32 s42, s42, s43\n"
    "s_mul_i32 s42, s42, 3\n"
    "s_add_u32 s42, s42, s37\n"
    "s_sub_u32 s42, s42, 13\n"
    "s_mul_i32 s43, s42, 1366\n"
    "s_lshr_b32 s43, s43, 16\n"
    "s_mul_i32 s44, s43, 48\n"
    "s_sub_u32 s42, s42, s44\n"
    "s_lshl_b32 s42, s42, 2\n"
    "s_lshl_b32 s44, s43, 4\n"
    "s_add_u32 s44, s44, 151168\n"
    "v_and_b32 v20, 1, v0\n"
    "v_lshl_add_u32 v21, v20, 3, s44\n"
    "ds_read_b64 v[22:23], v21\n"
    "s_lshr_b32 s45, s42, 4\n"
    "s_lshl_b32 s45, s45, 5\n"
    "s_add_u32 s45, s45, s42\n"
    "v_lshrrev_b32 v25, 1, v2\n"
    "v_min_u32 v26, 2, v25\n"
    "v_lshl_add_u32 v27, v26, 4, v3\n"
    "v_add_u32 v27, s45, v27\n"
    "v_mul_u32_u24 v24, 683, v27\n"
    "v_lshrrev_b32 v24, 11, v24\n"
    "v_mul_u32_u24 v26, 3, v24\n"
    "v_sub_u32 v26, v27, v26\n"
    "v_lshlrev_b32 v28, 3, v27\n"
    "s_mov_b32 s45, 43520\n"
    "s_mov_b32 s46, 71168\n"
    "v_mul_u32_u24 v24, 16512, v24\n"
    "s_waitcnt lgkmcnt(0)\n"
    "v_bitop3_b32 v29, v23, s61, v23 bitop3:0x0c\n"
    "v_lshl_add_u32 v22, v27, 1, v22\n"
    "v_add_u32 v22, 73728, v22\n"
    "v_mov_b32 v40, 0\n"
    "v_mov_b32 v41, 0\n"
    "ds_read_u16_d16_hi v40, v22\n"
    "v_mul_u32_u24 v30, 1152, v29\n"
    "v_lshl_add_u32 v30, v27, 1, v30\n"
    "global_load_short_d16_hi v41, v30, s[4:5]\n"
    "v_lshrrev_b32 v31, 1, v29\n"
    "v_lshl_add_u32 v24, v31, 4, v24\n"
    "s_mov_b32 s47, 131072\n"
    "v_lshrrev_b32 v31, 4, v29\n"
    "v_lshlrev_b32 v31, 2, v31\n"
    "s_setprio 0\n"
    "s_cmpk_eq_u32 s37, 13\n"
    "s_cbranch_scc1 DOWN\n"
    "s_waitcnt lgkmcnt(0)\n"
    "s_barrier\n"
    "s_branch FIN\n"
    "DOWN:\n"
    "s_cmpk_ge_u32 s37, 14\n"
    "s_cbranch_scc1 END\n"
    "s_setprio 3\n"
    "v_mov_b32 v47, s36\n"
    "v_mov_b32 v51, s36\n"
    "s_waitcnt lgkmcnt(0)\n"
    "s_barrier\n"
    "ds_read_b128 v[104:107], v4\n"
    "ds_read_b128 v[108:111], v4 offset:64\n"
    "ds_read_b128 v[112:115], v5\n"
    "ds_read_b128 v[116:119], v5 offset:64\n"
    "s_waitcnt lgkmcnt(3)\n"
    "v_mfma_f32_16x16x32_bf16 v[8:11], v[104:107], v[56:59], 0\n"
    "v_mfma_f32_16x16x32_bf16 v[12:15], v[104:107], v[60:63], 0\n"
    "v_mfma_f32_16x16x32_bf16 v[16:19], v[104:107], v[64:67], 0\n"
    "s_waitcnt lgkmcnt(2)\n"
    "v_mfma_f32_16x16x32_bf16 v[8:11], v[108:111], v[80:83], v[8:11]\n"
    "v_mfma_f32_16x16x32_bf16 v[12:15], v[108:111], v[84:87], v[12:15]\n"
    "v_mfma_f32_16x16x32_bf16 v[16:19], v[108:111], v[88:91], v[16:19]\n"
    "s_waitcnt lgkmcnt(1)\n"
    "v_mfma_f32_16x16x32_bf16 v[8:11], v[112:115], v[68:71], v[8:11]\n"
    "v_mfma_f32_16x16x32_bf16 v[12:15], v[112:115], v[72:75], v[12:15]\n"
    "v_mfma_f32_16x16x32_bf16 v[16:19], v[112:115], v[76:79], v[16:19]\n"
    "s_waitcnt lgkmcnt(0)\n"
    "v_mfma_f32_16x16x32_bf16 v[8:11], v[116:119], v[92:95], v[8:11]\n"
    "v_mfma_f32_16x16x32_bf16 v[12:15], v[116:119], v[96:99], v[12:15]\n"
    "v_mfma_f32_16x16x32_bf16 v[16:19], v[116:119], v[100:103], v[16:19]\n"
    "s_nop 5\n"
    "v_cvt_pk_bf16_f32 v44, v8, v9\n"
    "v_cvt_pk_bf16_f32 v45, v12, v13\n"
    "v_cvt_pk_bf16_f32 v46, v16, v17\n"
    "buffer_store_dwordx4 v[44:47], v6, s[16:19], 0 offen offset:0 sc1\n"
    "v_cvt_pk_bf16_f32 v48, v10, v11\n"
    "v_cvt_pk_bf16_f32 v49, v14, v15\n"
    "v_cvt_pk_bf16_f32 v50, v18, v19\n"
    "buffer_store_dwordx4 v[48:51], v6, s[16:19], 0 offen offset:3072 sc1\n"
    "s_cmpk_lg_u32 s37, 13\n"
    "s_cbranch_scc1 END\n"
    "s_cmpk_lt_u32 s2, 192\n"
    "s_cbranch_scc1 END\n"
    "FIN:\n"
    "s_branch E_55\n"
    "PAD30:\n"
    "s_cbranch_execz SIL1\n"
    "R_54:\n"
    "s_sub_u32 s39, s39, 1\n"
    "s_cbranch_scc1 ABORT\n"
    "E_55:\n"
    "buffer_load_dwordx4 v[32:35], v24, s[16:19], s47 offen sc1\n"
    "buffer_load_dwordx2 v[36:37], v28, s[16:19], s45 offen sc1\n"
    "buffer_load_dwordx2 v[38:39], v28, s[16:19], s46 offen sc1\n"
    "s_waitcnt vmcnt(0)\n"
    "v_bitop3_b32 v42, v37, v39, s36 bitop3:0x7e\n"
    "v_xor_b32 v43, s36, v35\n"
    "v_or_b32 v42, v42, v43\n"
    "v_cmp_ne_u32 vcc, 0, v42\n"
    "s_cbranch_vccnz R_54\n"
    "v_cmp_eq_u32 vcc, 1, v26\n"
    "v_cmp_eq_u32 s[50:51], 2, v26\n"
    "v_and_b32 v42, 1, v29\n"
    "v_cndmask_b32 v32, v32, v33, vcc\n"
    "v_cndmask_b32 v32, v32, v34, s[50:51]\n"
    "v_lshlrev_b32 v42, 4, v42\n"
    "v_lshrrev_b32 v32, v42, v32\n"
    "v_lshlrev_b32 v32, 16, v32\n"
    "v_add_f32 v41, v41, v32\n"
    "v_add_f32 v36, v36, v38\n"
    "v_cvt_pk_bf16_f32 v41, 0, v41\n"
    "v_cvt_pk_bf16_f32 v36, 0, v36\n"
    "s_brev_b32 s48, 1\n"
    "v_ashrrev_i32 v42, 31, v23\n"
    "v_bitop3_b32 v23, v42, v23, s48 bitop3:0x63\n"
    "v_and_b32 v23, 0xfffff800, v23\n"
    "v_cvt_pk_bf16_f32 v23, 0, v23\n"
    "v_mul_f32 v23, 0xbfb8aa3b, v23\n"
    "v_exp_f32 v23, v23\n"
    "v_lshlrev_b32 v20, 4, v20\n"
    "v_add_f32 v23, 1.0, v23\n"
    "v_rcp_f32 v23, v23\n"
    "s_movk_i32 s44, 0x3f80\n"
    "v_lshlrev_b32_e64 v20, v20, s44\n"
    "v_cvt_pk_bf16_f32 v23, 0, v23\n"
    "v_cmp_eq_u32 vcc, 0, v25\n"
    "s_nop 1\n"
    "v_cndmask_b32 v32, 0, v20, vcc\n"
    "v_cmp_eq_u32 vcc, 1, v25\n"
    "s_nop 1\n"
    "v_cndmask_b32 v33, 0, v20, vcc\n"
    "v_cmp_eq_u32 vcc, 2, v25\n"
    "s_nop 1\n"
    "v_cndmask_b32 v34, 0, v20, vcc\n"
    "v_mov_b32 v35, 0\n"
    "v_lshrrev_b32 v42, 2, v1\n"
    "v_mul_u32_u24 v42, 12416, v42\n"
    "v_and_b32 v43, 3, v0\n"
    "v_lshl_add_u32 v42, v43, 4, v42\n"
    "s_mul_i32 s43, s43, 3072\n"
    "s_lshl_b32 s42, s42, 4\n"
    "s_add_u32 s42, s42, s43\n"
    "s_add_u32 s50, s42, 6447104\n"
    "s_add_u32 s51, s42, 6645760\n"
    "s_add_u32 s52, s42, 6844416\n"
    "s_add_u32 s53, s42, 7043072\n"
    "s_add_u32 s54, s42, 7241728\n"
    "v_cmp_gt_u32 vcc, 32, v1\n"
    "v_mov_b32 v43, 0x7ffffff0\n"
    "v_mov_b32 v21, s36\n"
    "v_cndmask_b32 v43, v43, v42, vcc\n"
    "v_cndmask_b32 v21, 0, v21, vcc\n"
    "s_mov_b32 s46, 0x003f003f\n"
    "s_mov_b32 s47, 0x003f003f\n"
    "s_sleep 4\n"
    "buffer_load_dwordx4 v[104:107], v42, s[16:19], s50 offen sc1\n"
    "buffer_load_dwordx4 v[108:111], v42, s[16:19], s51 offen sc1\n"
    "buffer_load_dwordx4 v[112:115], v42, s[16:19], s52 offen sc1\n"
    "buffer_load_dwordx4 v[116:119], v42, s[16:19], s53 offen sc1\n"
    "buffer_load_dwordx4 v[120:123], v43, s[16:19], s54 offen sc1\n"
    "buffer_load_dword v22, v31, s[16:19], 0 offen offset:1024 sc1\n"
    "s_branch E_57\n"
    "R_56:\n"
    "s_sub_u32 s39, s39, 1\n"
    "s_cbranch_scc1 ABORT\n"
    "s_wqm_b64 exec, vcc\n"
    "buffer_load_dwordx4 v[104:107], v42, s[16:19], s50 offen sc1\n"
    "buffer_load_dwordx4 v[108:111], v42, s[16:19], s51 offen sc1\n"
    "buffer_load_dwordx4 v[112:115], v42, s[16:19], s52 offen sc1\n"
    "buffer_load_dwordx4 v[116:119], v42, s[16:19], s53 offen sc1\n"
    "buffer_load_dwordx4 v[120:123], v43, s[16:19], s54 offen sc1\n"
    "buffer_load_dword v22, v31, s[16:19], 0 offen offset:1024 sc1\n"
    "s_mov_b64 exec, -1\n"
    "E_57:\n"
    "s_waitcnt vmcnt(2)\n"
    "v_bitop3_b32 v24, v107, v111, s36 bitop3:0x7e\n"
    "v_bitop3_b32 v25, v115, v119, s36 bitop3:0x7e\n"
    "s_waitcnt vmcnt(0)\n"
    "v_xor_b32 v26, v21, v123\n"
    "v_bitop3_b32 v24, v24, v22, s36 bitop3:0xf6\n"
    "v_or3_b32 v24, v24, v25, v26\n"
    "v_cmp_ne_u32 vcc, 0, v24\n"
    "s_cbranch_vccnz R_56\n"
    "v_mov_b32 v107, 0\n"
    "v_mov_b32 v111, 0\n"
    "v_mov_b32 v115, 0\n"
    "v_mov_b32 v119, 0\n"
    "v_mov_b32 v123, 0\n"
    "v_mfma_f32_16x16x32_bf16 v[8:11], v[104:107], v[32:35], 0\n"
    "v_mfma_f32_16x16x32_bf16 v[8:11], v[108:111], v[32:35], v[8:11]\n"
    "v_mfma_f32_16x16x32_bf16 v[8:11], v[112:115], v[32:35], v[8:11]\n"
    "v_mfma_f32_16x16x32_bf16 v[8:11], v[116:119], v[32:35], v[8:11]\n"
    "v_mfma_f32_16x16x32_bf16 v[8:11], v[120:123], v[32:35], v[8:11]\n"
    "s_nop 7\n"
    "v_permlane32_swap_b32 v8, v10\n"
    "v_permlane32_swap_b32 v9, v11\n"
    "v_add_f32 v8, v8, v10\n"
    "v_add_f32 v9, v9, v11\n"
    "s_nop 1\n"
    "v_permlane16_swap_b32 v8, v9\n"
    "v_add_f32 v8, v8, v9\n"
    "v_cvt_pk_bf16_f32 v8, 0, v8\n"
    "v_mul_f32 v8, v8, v23\n"
    "v_cvt_pk_bf16_f32 v8, 0, v8\n"
    "v_add_f32 v8, v40, v8\n"
    "v_cvt_pk_bf16_f32 v8, 0, v8\n"
    "v_mul_f32 v8, v36, v8\n"
    "v_cvt_pk_bf16_f32 v8, 0, v8\n"
    "v_add_f32 v8, v41, v8\n"
    "v_cvt_pk_bf16_f32 v8, v8, v8\n"
    "s_mov_b64 exec, s[46:47]\n"
    "global_store_short v30, v8, s[34:35]\n"
    "s_mov_b64 exec, -1\n"
    "s_branch END\n"
    "MOE15:\n"
    "ds_read_b32 v32, v19 offset:0\n"
    "s_waitcnt lgkmcnt(0)\n"
    "v_readfirstlane_b32 s42, v20\n"
    "s_cmpk_gt_u32 s42, 64\n"
    "s_cbranch_scc1 MANY\n"
    "MOE16:\n"
    "v_bitop3_b32 v32, v32, s61, v32 bitop3:0x0c\n"
    "v_lshlrev_b32 v32, 3, v32\n"
    "s_movk_i32 s43, 4096\n"
    "v_cmp_gt_u32 s[50:51], s42, v19\n"
    "v_bitop3_b32 v21, v21, s61, v21 bitop3:0x0c\n"
    "R_58:\n"
    "buffer_load_dwordx2 v[34:35], v32, s[16:19], s43 offen sc1\n"
    "s_waitcnt vmcnt(0)\n"
    "v_cmp_ne_u32 vcc, s36, v35\n"
    "v_cndmask_b32 v53, 0, v34, s[50:51]\n"
    "s_and_b64 vcc, vcc, s[50:51]\n"
    "s_cbranch_vccnz XKS\n"
    "v_mov_b32 v34, v53\n"
    "s_nop 1\n"
    "v_mov_b32_dpp v34, v53 row_ror:4 row_mask:0x2 bank_mask:0xf\n"
    "s_nop 1\n"
    "v_mov_b32_dpp v34, v53 row_ror:8 row_mask:0x4 bank_mask:0xf\n"
    "s_nop 1\n"
    "v_mov_b32_dpp v34, v53 row_ror:12 row_mask:0x8 bank_mask:0xf\n"
    "v_cmp_gt_u32 s[52:53], v34, v53\n"
    "s_nop 0\n"
    "v_mov_b32_dpp v35, v34 row_ror:1 row_mask:0xf bank_mask:0xf\n"
    "v_mov_b32_dpp v36, v34 row_ror:2 row_mask:0xf bank_mask:0xf\n"
    "v_mov_b32_dpp v37, v34 row_ror:3 row_mask:0xf bank_mask:0xf\n"
    "v_cmp_gt_u32 s[54:55], v35, v53\n"
    "v_cmp_gt_u32 s[50:51], v36, v53\n"
    "v_cmp_gt_u32 vcc, v37, v53\n"
    "v_cndmask_b32 v34, 0, 1, s[52:53]\n"
    "v_addc_co_u32 v34, s[52:53], 0, v34, s[54:55]\n"
    "v_addc_co_u32 v34, s[52:53], 0, v34, s[50:51]\n"
    "v_addc_co_u32 v34, vcc, 0, v34, vcc\n"
    "v_mov_b32 v35, v34\n"
    "s_nop 1\n"
    "v_permlane16_swap_b32 v34, v35\n"
    "v_add_u32 v34, v34, v35\n"
    "v_mov_b32 v35, v34\n"
    "s_nop 1\n"
    "v_permlane32_swap_b32 v34, v35\n"
    "v_add_u32 v34, v34, v35\n"
    "v_min_u32 v34, 8, v34\n"
    "v_lshl_add_u32 v34, v34, 3, v12\n"
    "ds_write_b64 v34, v[52:53] offset:3072\n"
    "s_branch MOE4\n"
    "XKS:\n"
    "s_sub_u32 s39, s39, 1\n"
    "s_cbranch_scc1 ABORT\n"
    "s_branch R_58\n"
    "ROWS:\n"
    "s_sub_u32 s39, s39, 1\n"
    "s_cbranch_scc1 ABORT\n"
    "s_branch R_53\n"
    "TOPS:\n"
    "s_mov_b64 exec, -1\n"
    "v_cmpx_le_u32 vcc, s49, v24\n"
    "ds_add_rtn_u32 v21, v12, v18 offset:64\n"
    "s_waitcnt lgkmcnt(0)\n"
    "ds_write_b32 v21, v24 offset:0\n"
    "s_mov_b64 exec, -1\n"
    "v_cmpx_le_u32 vcc, s49, v26\n"
    "ds_add_rtn_u32 v21, v12, v18 offset:64\n"
    "s_waitcnt lgkmcnt(0)\n"
    "ds_write_b32 v21, v26 offset:0\n"
    "s_mov_b64 exec, -1\n"
    "v_cmpx_le_u32 vcc, s49, v28\n"
    "ds_add_rtn_u32 v21, v12, v18 offset:64\n"
    "s_waitcnt lgkmcnt(0)\n"
    "ds_write_b32 v21, v28 offset:0\n"
    "s_mov_b64 exec, -1\n"
    "v_cmpx_le_u32 vcc, s49, v30\n"
    "ds_add_rtn_u32 v21, v12, v18 offset:64\n"
    "s_waitcnt lgkmcnt(0)\n"
    "ds_write_b32 v21, v30 offset:0\n"
    "s_mov_b64 exec, -1\n"
    "v_cmpx_le_u32 vcc, s49, v32\n"
    "ds_add_rtn_u32 v21, v12, v18 offset:64\n"
    "s_waitcnt lgkmcnt(0)\n"
    "ds_write_b32 v21, v32 offset:0\n"
    "s_mov_b64 exec, -1\n"
    "v_cmpx_le_u32 vcc, s49, v34\n"
    "ds_add_rtn_u32 v21, v12, v18 offset:64\n"
    "s_waitcnt lgkmcnt(0)\n"
    "ds_write_b32 v21, v34 offset:0\n"
    "s_mov_b64 exec, -1\n"
    "v_cmpx_le_u32 vcc, s49, v36\n"
    "ds_add_rtn_u32 v21, v12, v18 offset:64\n"
    "s_waitcnt lgkmcnt(0)\n"
    "ds_write_b32 v21, v36 offset:0\n"
    "s_mov_b64 exec, -1\n"
    "v_cmpx_le_u32 vcc, s49, v38\n"
    "ds_add_rtn_u32 v21, v12, v18 offset:64\n"
    "s_waitcnt lgkmcnt(0)\n"
    "ds_write_b32 v21, v38 offset:0\n"
    "s_branch TOPC\n"
    "MANY:\n"
    "v_lshlrev_b32 v22, 2, v0\n"
    "MANY1:\n"
    "v_cmpx_gt_u32 vcc, s42, v22\n"
    "s_cbranch_execz MANY3\n"
    "ds_read_b32 v23, v22 offset:0\n"
    "v_mov_b32 v24, 0\n"
    "s_mov_b32 s43, 0\n"
    "MANY2:\n"
    "v_mov_b32 v25, s43\n"
    "ds_read_b32 v25, v25 offset:0\n"
    "s_add_u32 s43, s43, 4\n"
    "s_waitcnt lgkmcnt(0)\n"
    "v_cmp_gt_u32 vcc, v25, v23\n"
    "s_nop 1\n"
    "v_addc_co_u32 v24, vcc, 0, v24, vcc\n"
    "s_cmp_lt_u32 s43, s42\n"
    "s_cbranch_scc1 MANY2\n"
    "v_min_u32 v24, 16, v24\n"
    "v_lshl_add_u32 v24, v24, 2, v12\n"
    "ds_write_b32 v24, v23 offset:3152\n"
    "v_add_u32 v22, 4096, v22\n"
    "s_branch MANY1\n"
    "MANY3:\n"
    "s_mov_b64 exec, -1\n"
    "v_add_u32 v22, s62, v12\n"
    "v_lshl_add_u32 v23, v2, 2, v12\n"
    "s_waitcnt lgkmcnt(0)\n"
    "s_barrier\n"
    "ds_read_b32 v21, v22 offset:3152\n"
    "ds_read_b32 v32, v23 offset:3152\n"
    "s_movk_i32 s42, 64\n"
    "s_waitcnt lgkmcnt(0)\n"
    "s_cmpk_eq_u32 s37, 15\n"
    "s_cbranch_scc1 MOE16\n"
    "v_bitop3_b32 v21, v21, s61, v21 bitop3:0x0c\n"
    "s_branch MOE4\n"
    "END:\n"
    "s_endpgm\n"

    :: "{s[0:1]}"(__builtin_amdgcn_kernarg_segment_ptr()), "{s2}"(blockIdx.x), "{v0}"(threadIdx.x) : "v127", "s99", "vcc", "memory");
}

extern "C" void kernel_launch(void* const* d_in, const int* in_sizes, int n_in, void* d_out, int out_size, void* d_ws, size_t ws_size, hipStream_t stream) {
  (void)hipFuncSetAttribute((const void*)mega, hipFuncAttributeMaxDynamicSharedMemorySize, 151328);
  Args p;
  p.x = d_in[0]; p.lar = d_in[1]; p.aim = d_in[2]; p.ldt = d_in[5]; p.cvec = d_in[12]; p.wmod = d_in[13];
  p.ws = (char*)d_ws + ((size_t)0 << 20); p.ws_bytes = 10643968; p.ws_flags = 0x00020000u;
  p.cre = d_in[3]; p.cim = d_in[4]; p.dsk = d_in[6]; p.wr = d_in[8]; p.wg = d_in[9]; p.wu = d_in[10]; p.wd = d_in[11];
  p.out = d_out;
  mega<<<256, 1024, 151328, stream>>>(p);
}
```

```cpp
#include <hip/hip_runtime.h>
#include <stdint.h>

struct Args {
  const void *x, *lar, *aim, *ldt, *cre, *cim, *wmod, *cvec;
  void* ws; uint32_t ws_bytes, ws_flags;
  const void *dsk, *wr, *wg, *wu, *wd;
  void* out;
};
extern "C" __device__ __attribute__((used, aligned(128))) const uint32_t mega_tab[3840] = {
  0x0, 0x0, 0x0, 0x0, 0x0, 0x0, 0x0, 0x0, 0x0, 0x0, 0x0, 0x0, 0x0, 0x0, 0x0, 0x0,
  0x0, 0x0, 0x0, 0x0, 0x0, 0x0, 0x0, 0x0, 0x0, 0x0, 0x0, 0x0, 0x0, 0x0, 0x0, 0x0,
  0x0, 0x0, 0x0, 0x0, 0x0, 0x0, 0x0, 0x0, 0x0, 0x0, 0x0, 0x0, 0x0, 0x0, 0x0, 0x3c000000,
  0x0, 0x0, 0x0, 0x3c003c00, 0x0, 0x0, 0x3c000000, 0x3c003c00, 0x0, 0x0, 0x3c003c00, 0x3c00, 0x0, 0x3c000000, 0x3c003c00, 0x0,
  0x0, 0x0, 0x0, 0x0, 0x0, 0x0, 0x0, 0x0, 0x0, 0x0, 0x0, 0x0, 0x0, 0x0, 0x0, 0x3c000000,
  0x0, 0x0, 0x0, 0x3c003c00, 0x0, 0x0, 0x3c000000, 0x3c003c00, 0x0, 0x0, 0x3c003c00, 0x3c00, 0x0, 0x3c000000, 0x3c003c00, 0x0,
  0x0, 0x3c003c00, 0x3c00, 0x0, 0x3c000000, 0x3c003c00, 0x0, 0x0, 0x3c003c00, 0x3c00, 0x0, 0x0, 0x3c003c00, 0x0, 0x0, 0x0,
  0x3c00, 0x0, 0x0, 0x0, 0x0, 0x0, 0x0, 0x0, 0x0, 0x0, 0x0, 0x0, 0x0, 0x0, 0x0, 0x0,
  0x0, 0x3c003c00, 0x3c00, 0x0, 0x3c000000, 0x3c003c00, 0x0, 0x0, 0x3c003c00, 0x3c00, 0x0, 0x0, 0x3c003c00, 0x0, 0x0, 0x0,
  0x3c00, 0x0, 0x0, 0x0, 0x0, 0x0, 0x0, 0x0, 0x0, 0x0, 0x0, 0x0, 0x0, 0x0, 0x0, 0x0,
  0x0, 0x0, 0x0, 0x0, 0x0, 0x0, 0x0, 0x0, 0x0, 0x0, 0x0, 0x0, 0x0, 0x0, 0x0, 0x0,
  0x0, 0x0, 0x0, 0x0, 0x0, 0x0, 0x0, 0x0, 0x0, 0x0, 0x0, 0x0, 0x0, 0x0, 0x0, 0x0,
  0x0, 0x0, 0x0, 0x0, 0x0, 0x0, 0x0, 0x0, 0x0, 0x0, 0x0, 0x0, 0x0, 0x0, 0x0, 0x0,
  0x0, 0x0, 0x0, 0x0, 0x0, 0x0, 0x0, 0x0, 0x0, 0x0, 0x0, 0x0, 0x0, 0x0, 0x0, 0x0,
  0x0, 0x0, 0x0, 0x0, 0x0, 0x0, 0x0, 0x0, 0x0, 0x0, 0x0, 0x0, 0x0, 0x0, 0x0, 0x0,
  0x0, 0x0, 0x0, 0x0, 0x0, 0x0, 0x0, 0x0, 0x0, 0x0, 0x0, 0x0, 0x0, 0x0, 0x0, 0x0,
  0x0, 0x0, 0x0, 0x0, 0x0, 0x0, 0x0, 0x0, 0x0, 0x0, 0x0, 0x0, 0x0, 0x0, 0x0, 0x0,
  0x0, 0x0, 0x0, 0x0, 0x0, 0x0, 0x0, 0x0, 0x0, 0x0, 0x0, 0x0, 0x0, 0x0, 0x0, 0x0,
  0x0, 0x0, 0x0, 0x0, 0x0, 0x0, 0x0, 0x0, 0x0, 0x0, 0x0, 0x0, 0x0, 0x0, 0x0, 0x0,
  0x0, 0x0, 0x0, 0x0, 0x0, 0x0, 0x0, 0x0, 0x0, 0x0, 0x0, 0x0, 0x0, 0x0, 0x0, 0x0,
  0x0, 0x0, 0x0, 0x0, 0x0, 0x0, 0x0, 0x0, 0x0, 0x0, 0x0, 0x0, 0x0, 0x0, 0x0, 0x0,
  0x0, 0x0, 0x0, 0x0, 0x0, 0x0, 0x0, 0x0, 0x0, 0x0, 0x0, 0x0, 0x0, 0x0, 0x0, 0x0,
  0x0, 0x0, 0x0, 0x0, 0x0, 0x0, 0x0, 0x0, 0x0, 0x0, 0x0, 0x0, 0x0, 0x0, 0x0, 0x3c000000,
  0x0, 0x0, 0x0, 0x3c003c00, 0x0, 0x0, 0x3c000000, 0x3c003c00, 0x0, 0x0, 0x3c003c00, 0x3c00, 0x0, 0x3c000000, 0x3c003c00, 0x0,
  0x0, 0x0, 0x0, 0x0, 0x0, 0x0, 0x0, 0x0, 0x0, 0x0, 0x0, 0x0, 0x0, 0x0, 0x0, 0x3c000000,
  0x0, 0x0, 0x0, 0x3c003c00, 0x0, 0x0, 0x3c000000, 0x3c003c00, 0x0, 0x0, 0x3c003c00, 0x3c00, 0x0, 0x3c000000, 0x3c003c00, 0x0,
  0x0, 0x3c003c00, 0x3c00, 0x0, 0x3c000000, 0x3c003c00, 0x0, 0x0, 0x3c003c00, 0x3c00, 0x0, 0x0, 0x3c003c00, 0x0, 0x0, 0x0,
  0x3c00, 0x0, 0x0, 0x0, 0x0, 0x0, 0x0, 0x0, 0x0, 0x0, 0x0, 0x0, 0x0, 0x0, 0x0, 0x0,
  0x0, 0x3c003c00, 0x3c00, 0x0, 0x3c000000, 0x3c003c00, 0x0, 0x0, 0x3c003c00, 0x3c00, 0x0, 0x0, 0x3c003c00, 0x0, 0x0, 0x0,
  0x3c00, 0x0, 0x0, 0x0, 0x0, 0x0, 0x0, 0x0, 0x0, 0x0, 0x0, 0x0, 0x0, 0x0, 0x0, 0x0,
  0x0, 0x0, 0x0, 0x0, 0x0, 0x0, 0x0, 0x0, 0x0, 0x0, 0x0, 0x0, 0x0, 0x0, 0x0, 0x0,
  0x0, 0x0, 0x0, 0x0, 0x0, 0x0, 0x0, 0x0, 0x0, 0x0, 0x0, 0x0, 0x0, 0x0, 0x0, 0x0,
  0x0, 0x0, 0x0, 0x0, 0x0, 0x0, 0x0, 0x0, 0x0, 0x0, 0x0, 0x0, 0x0, 0x0, 0x0, 0x0,
  0x0, 0x0, 0x0, 0x0, 0x0, 0x0, 0x0, 0x0, 0x0, 0x0, 0x0, 0x0, 0x0, 0x0, 0x0, 0x0,
  0x0, 0x0, 0x0, 0x0, 0x0, 0x0, 0x0, 0x0, 0x0, 0x0, 0x0, 0x0, 0x0, 0x0, 0x0, 0x0,
  0x0, 0x0, 0x0, 0x0, 0x0, 0x0, 0x0, 0x0, 0x0, 0x0, 0x0, 0x3c000000, 0x0, 0x0, 0x0, 0x3c003c00,
  0x0, 0x0, 0x0, 0x0, 0x0, 0x0, 0x0, 0x0, 0x0, 0x0, 0x0, 0x0, 0x0, 0x0, 0x0, 0x0,
  0x0, 0x0, 0x0, 0x0, 0x0, 0x0, 0x0, 0x0, 0x0, 0x0, 0x0, 0x3c000000, 0x0, 0x0, 0x0, 0x3c003c00,
  0x0, 0x0, 0x3c000000, 0x3c003c00, 0x0, 0x0, 0x3c003c00, 0x3c003c00, 0x0, 0x3c000000, 0x3c003c00, 0x3c003c00, 0x0, 0x3c003c00, 0x3c003c00, 0x3c00,
  0x3c000000, 0x3c003c00, 0x3c003c00, 0x0, 0x3c003c00, 0x3c003c00, 0x3c00, 0x0, 0x3c003c00, 0x3c003c00, 0x0, 0x0, 0x3c003c00, 0x3c00, 0x0, 0x0,
  0x0, 0x0, 0x3c000000, 0x3c003c00, 0x0, 0x0, 0x3c003c00, 0x3c003c00, 0x0, 0x3c000000, 0x3c003c00, 0x3c003c00, 0x0, 0x3c003c00, 0x3c003c00, 0x3c00,
  0x3c000000, 0x3c003c00, 0x3c003c00, 0x0, 0x3c003c00, 0x3c003c00, 0x3c00, 0x0, 0x3c003c00, 0x3c003c00, 0x0, 0x0, 0x3c003c00, 0x3c00, 0x0, 0x0,
  0x3c003c00, 0x0, 0x0, 0x0, 0x3c00, 0x0, 0x0, 0x0, 0x0, 0x0, 0x0, 0x0, 0x0, 0x0, 0x0, 0x0,
  0x0, 0x0, 0x0, 0x0, 0x0, 0x0, 0x0, 0x0, 0x0, 0x0, 0x0, 0x0, 0x0, 0x0, 0x0, 0x0,
  0x3c003c00, 0x0, 0x0, 0x0, 0x3c00, 0x0, 0x0, 0x0, 0x0, 0x0, 0x0, 0x0, 0x0, 0x0, 0x0, 0x0,
  0x0, 0x0, 0x0, 0x0, 0x0, 0x0, 0x0, 0x0, 0x0, 0x0, 0x0, 0x0, 0x0, 0x0, 0x0, 0x0,
  0x0, 0x0, 0x0, 0x0, 0x0, 0x0, 0x0, 0x0, 0x0, 0x0, 0x0, 0x0, 0x0, 0x0, 0x0, 0x0,
  0x0, 0x0, 0x0, 0x0, 0x0, 0x0, 0x0, 0x0, 0x0, 0x0, 0x0, 0x0, 0x0, 0x0, 0x0, 0x0,
  0x0, 0x0, 0x0, 0x0, 0x0, 0x0, 0x0, 0x0, 0x0, 0x0, 0x0, 0x0, 0x0, 0x0, 0x0, 0x0,
  0x0, 0x0, 0x0, 0x0, 0x0, 0x0, 0x0, 0x0, 0x0, 0x0, 0x0, 0x0, 0x0, 0x0, 0x0, 0x0,
  0x0, 0x0, 0x0, 0x0, 0x0, 0x0, 0x0, 0x0, 0x0, 0x0, 0x0, 0x0, 0x0, 0x0, 0x0, 0x0,
  0x0, 0x0, 0x0, 0x0, 0x0, 0x0, 0x0, 0x3c000000, 0x0, 0x0, 0x0, 0x3c003c00, 0x0, 0x0, 0x3c000000, 0x3c003c00,
  0x0, 0x0, 0x0, 0x0, 0x0, 0x0, 0x0, 0x0, 0x0, 0x0, 0x0, 0x0, 0x0, 0x0, 0x0, 0x0,
  0x0, 0x0, 0x0, 0x0, 0x0, 0x0, 0x0, 0x3c000000, 0x0, 0x0, 0x0, 0x3c003c00, 0x0, 0x0, 0x3c000000, 0x3c003c00,
  0x0, 0x0, 0x3c003c00, 0x3c003c00, 0x0, 0x3c000000, 0x3c003c00, 0x3c003c00, 0x0, 0x3c003c00, 0x3c003c00, 0x3c003c00, 0x3c000000, 0x3c003c00, 0x3c003c00, 0x3c003c00,
  0x3c003c00, 0x3c003c00, 0x3c003c00, 0x3c00, 0x3c003c00, 0x3c003c00, 0x3c003c00, 0x0, 0x3c003c00, 0x3c003c00, 0x3c00, 0x0, 0x3c003c00, 0x3c003c00, 0x0, 0x0,
  0x0, 0x0, 0x3c003c00, 0x3c003c00, 0x0, 0x3c000000, 0x3c003c00, 0x3c003c00, 0x0, 0x3c003c00, 0x3c003c00, 0x3c003c00, 0x3c000000, 0x3c003c00, 0x3c003c00, 0x3c003c00,
  0x3c003c00, 0x3c003c00, 0x3c003c00, 0x3c00, 0x3c003c00, 0x3c003c00, 0x3c003c00, 0x0, 0x3c003c00, 0x3c003c00, 0x3c00, 0x0, 0x3c003c00, 0x3c003c00, 0x0, 0x0,
  0x3c003c00, 0x3c00, 0x0, 0x0, 0x3c003c00, 0x0, 0x0, 0x0, 0x3c00, 0x0, 0x0, 0x0, 0x0, 0x0, 0x0, 0x0,
  0x0, 0x0, 0x0, 0x0, 0x0, 0x0, 0x0, 0x0, 0x0, 0x0, 0x0, 0x0, 0x0, 0x0, 0x0, 0x0,
  0x3c003c00, 0x3c00, 0x0, 0x0, 0x3c003c00, 0x0, 0x0, 0x0, 0x3c00, 0x0, 0x0, 0x0, 0x0, 0x0, 0x0, 0x0,
  0x0, 0x0, 0x0, 0x0, 0x0, 0x0, 0x0, 0x0, 0x0, 0x0, 0x0, 0x0, 0x0, 0x0, 0x0, 0x0,
  0x0, 0x0, 0x0, 0x0, 0x0, 0x0, 0x0, 0x0, 0x0, 0x0, 0x0, 0x0, 0x0, 0x0, 0x0, 0x0,
  0x0, 0x0, 0x0, 0x0, 0x0, 0x0, 0x0, 0x0, 0x0, 0x0, 0x0, 0x0, 0x0, 0x0, 0x0, 0x0,
  0x0, 0x0, 0x0, 0x0, 0x0, 0x0, 0x0, 0x0, 0x0, 0x0, 0x0, 0x0, 0x0, 0x0, 0x0, 0x0,
  0x0, 0x0, 0x0, 0x0, 0x0, 0x0, 0x0, 0x0, 0x0, 0x0, 0x0, 0x0, 0x0, 0x0, 0x0, 0x0,
  0x0, 0x0, 0x0, 0x0, 0x0, 0x0, 0x0, 0x0, 0x0, 0x0, 0x0, 0x0, 0x0, 0x0, 0x0, 0x0,
  0x0, 0x0, 0x0, 0x3c000000, 0x0, 0x0, 0x0, 0x3c003c00, 0x0, 0x0, 0x3c000000, 0x3c003c00, 0x0, 0x0, 0x3c003c00, 0x3c003c00,
  0x0, 0x0, 0x0, 0x0, 0x0, 0x0, 0x0, 0x0, 0x0, 0x0, 0x0, 0x0, 0x0, 0x0, 0x0, 0x0,
  0x0, 0x0, 0x0, 0x3c000000, 0x0, 0x0, 0x0, 0x3c003c00, 0x0, 0x0, 0x3c000000, 0x3c003c00, 0x0, 0x0, 0x3c003c00, 0x3c003c00,
  0x0, 0x3c000000, 0x3c003c00, 0x3c003c00, 0x0, 0x3c003c00, 0x3c003c00, 0x3c003c00, 0x3c000000, 0x3c003c00, 0x3c003c00, 0x3c003c00, 0x3c003c00, 0x3c003c00, 0x3c003c00, 0x3c003c00,
  0x3c003c00, 0x3c003c00, 0x3c003c00, 0x3c003c00, 0x3c003c00, 0x3c003c00, 0x3c003c00, 0x3c00, 0x3c003c00, 0x3c003c00, 0x3c003c00, 0x0, 0x3c003c00, 0x3c003c00, 0x3c00, 0x0,
  0x0, 0x3c000000, 0x3c003c00, 0x3c003c00, 0x0, 0x3c003c00, 0x3c003c00, 0x3c003c00, 0x3c000000, 0x3c003c00, 0x3c003c00, 0x3c003c00, 0x3c003c00, 0x3c003c00, 0x3c003c00, 0x3c003c00,
  0x3c003c00, 0x3c003c00, 0x3c003c00, 0x3c003c00, 0x3c003c00, 0x3c003c00, 0x3c003c00, 0x3c00, 0x3c003c00, 0x3c003c00, 0x3c003c00, 0x0, 0x3c003c00, 0x3c003c00, 0x3c00, 0x0,
  0x3c003c00, 0x3c003c00, 0x0, 0x0, 0x3c003c00, 0x3c00, 0x0, 0x0, 0x3c003c00, 0x0, 0x0, 0x0, 0x3c00, 0x0, 0x0, 0x0,
  0x0, 0x0, 0x0, 0x0, 0x0, 0x0, 0x0, 0x0, 0x0, 0x0, 0x0, 0x0, 0x0, 0x0, 0x0, 0x0,
  0x3c003c00, 0x3c003c00, 0x0, 0x0, 0x3c003c00, 0x3c00, 0x0, 0x0, 0x3c003c00, 0x0, 0x0, 0x0, 0x3c00, 0x0, 0x0, 0x0,
  0x0, 0x0, 0x0, 0x0, 0x0, 0x0, 0x0, 0x0, 0x0, 0x0, 0x0, 0x0, 0x0, 0x0, 0x0, 0x0,
  0x0, 0x0, 0x0, 0x0, 0x0, 0x0, 0x0, 0x0, 0x0, 0x0, 0x0, 0x0, 0x0, 0x0, 0x0, 0x0,
  0x0, 0x0, 0x0, 0x0, 0x0, 0x0, 0x0, 0x0, 0x0, 0x0, 0x0, 0x0, 0x0, 0x0, 0x0, 0x0,
  0x0, 0x0, 0x0, 0x0, 0x0, 0x0, 0x0, 0x0, 0x0, 0x0, 0x0, 0x0, 0x0, 0x0, 0x0, 0x0,
  0x0, 0x0, 0x0, 0x0, 0x0, 0x0, 0x0, 0x0, 0x0, 0x0, 0x0, 0x0, 0x0, 0x0, 0x0, 0x0,
  0x0, 0x0, 0x0, 0x0, 0x0, 0x0, 0x0, 0x0, 0x0, 0x0, 0x0, 0x0, 0x0, 0x0, 0x0, 0xffff0000,
  0x0, 0x0, 0x0, 0xffffffff, 0x0, 0x0, 0xffff0000, 0xffff, 0x0, 0x0, 0xffffffff, 0x0, 0x0, 0xffff0000, 0xffff, 0x0,
  0x0, 0x0, 0x0, 0x0, 0x0, 0x0, 0x0, 0x0, 0x0, 0x0, 0x0, 0x0, 0x0, 0x0, 0x0, 0xffff0000,
  0x0, 0x0, 0x0, 0xffffffff, 0x0, 0x0, 0xffff0000, 0xffff, 0x0, 0x0, 0xffffffff, 0x0, 0x0, 0xffff0000, 0xffff, 0x0,
  0x0, 0xffffffff, 0x0, 0x0, 0xffff0000, 0xffff, 0x0, 0x0, 0xffffffff, 0x0, 0x0, 0x0, 0xffff, 0x0, 0x0, 0x0,
  0x0, 0x0, 0x0, 0x0, 0x0, 0x0, 0x0, 0x0, 0x0, 0x0, 0x0, 0x0, 0x0, 0x0, 0x0, 0x0,
  0x0, 0xffffffff, 0x0, 0x0, 0xffff0000, 0xffff, 0x0, 0x0, 0xffffffff, 0x0, 0x0, 0x0, 0xffff, 0x0, 0x0, 0x0,
  0x0, 0x0, 0x0, 0x0, 0x0, 0x0, 0x0, 0x0, 0x0, 0x0, 0x0, 0x0, 0x0, 0x0, 0x0, 0x0,
  0x0, 0x0, 0x0, 0x0, 0x0, 0x0, 0x0, 0x0, 0x0, 0x0, 0x0, 0x0, 0x0, 0x0, 0x0, 0x0,
  0x0, 0x0, 0x0, 0x0, 0x0, 0x0, 0x0, 0x0, 0x0, 0x0, 0x0, 0x0, 0x0, 0x0, 0x0, 0x0,
  0x0, 0x0, 0x0, 0x0, 0x0, 0x0, 0x0, 0x0, 0x0, 0x0, 0x0, 0x0, 0x0, 0x0, 0x0, 0x0,
  0x0, 0x0, 0x0, 0x0, 0x0, 0x0, 0x0, 0x0, 0x0, 0x0, 0x0, 0x0, 0x0, 0x0, 0x0, 0x0,
  0x0, 0x0, 0x0, 0x0, 0x0, 0x0, 0x0, 0x0, 0x0, 0x0, 0x0, 0x0, 0x0, 0x0, 0x0, 0x0,
  0x0, 0x0, 0x0, 0x0, 0x0, 0x0, 0x0, 0x0, 0x0, 0x0, 0x0, 0x0, 0x0, 0x0, 0x0, 0x0,
  0x16520, 0x3020100, 0x7060504, 0x3020100, 0x16620, 0x3020100, 0x7060504, 0x3020100, 0x16530, 0x3020100, 0x7060504, 0x3020100, 0x16630, 0x3020100, 0x7060504, 0x3020100,
  0x16540, 0x3020100, 0x7060504, 0x3020100, 0x16640, 0x3020100, 0x7060504, 0x3020100, 0x16550, 0x3020100, 0x7060504, 0x3020100, 0x16650, 0x3020100, 0x7060504, 0x3020100,
  0x16560, 0x3020100, 0x7060504, 0x3020100, 0x16660, 0x3020100, 0x7060504, 0x3020100, 0x16570, 0x3020100, 0x7060504, 0x3020100, 0x16670, 0x3020100, 0x7060504, 0x3020100,
  0x16600, 0x3020100, 0x7060504, 0x3020100, 0x16700, 0x3020100, 0x7060504, 0x3020100, 0x16610, 0x3020100, 0x7060504, 0x3020100, 0x16710, 0x3020100, 0x7060504, 0x3020100,
  0x16528, 0x3020100, 0x7060504, 0x3020100, 0x16628, 0x3020100, 0x7060504, 0x3020100, 0x16538, 0x3020100, 0x7060504, 0x3020100, 0x16638, 0x3020100, 0x7060504, 0x3020100,
  0x16548, 0x3020100, 0x7060504, 0x3020100, 0x16648, 0x3020100, 0x7060504, 0x3020100, 0x16558, 0x3020100, 0x7060504, 0x3020100, 0x16658, 0x3020100, 0x7060504, 0x3020100,
  0x16568, 0x3020100, 0x7060504, 0x3020100, 0x16668, 0x3020100, 0x7060504, 0x3020100, 0x16578, 0x3020100, 0x7060504, 0x3020100, 0x16678, 0x3020100, 0x7060504, 0x3020100,
  0x16608, 0x3020100, 0x7060504, 0x3020100, 0x16708, 0x3020100, 0x7060504, 0x3020100, 0x16618, 0x3020100, 0x7060504, 0x3020100, 0x16718, 0x3020100, 0x7060504, 0x3020100,
  0x16720, 0x1000100, 0x5040302, 0x5040100, 0x16820, 0x1000100, 0x5040302, 0x5040100, 0x16730, 0x1000100, 0x5040302, 0x5040100, 0x16830, 0x1000100, 0x5040302, 0x5040100,
  0x16740, 0x1000100, 0x5040302, 0x5040100, 0x16840, 0x1000100, 0x5040302, 0x5040100, 0x16750, 0x1000100, 0x5040302, 0x5040100, 0x16850, 0x1000100, 0x5040302, 0x5040100,
  0x16760, 0x1000100, 0x5040302, 0x5040100, 0x16860, 0x1000100, 0x5040302, 0x5040100, 0x16770, 0x1000100, 0x5040302, 0x5040100, 0x16870, 0x1000100, 0x5040302, 0x5040100,
  0x16800, 0x1000100, 0x5040302, 0x5040100, 0x16900, 0x1000100, 0x5040302, 0x5040100, 0x16810, 0x1000100, 0x5040302, 0x5040100, 0x16910, 0x1000100, 0x5040302, 0x5040100,
  0x16920, 0x5040504, 0xc0c0504, 0x3020100, 0x16cc8, 0x3020100, 0x7060504, 0x3020100, 0x16cd0, 0x3020100, 0x7060504, 0x3020100, 0x16cd8, 0x3020100, 0x7060504, 0x3020100,
  0x16ce0, 0x3020100, 0x7060504, 0x3020100, 0x16ce8, 0x3020100, 0x7060504, 0x3020100, 0x16cf0, 0x3020100, 0x7060504, 0x3020100, 0x16cf8, 0x3020100, 0x7060504, 0x3020100,
  0x16d00, 0x3020100, 0x7060504, 0x3020100, 0x16d08, 0x3020100, 0x7060504, 0x3020100, 0x16d10, 0x3020100, 0x7060504, 0x3020100, 0x16d18, 0x3020100, 0x7060504, 0x3020100,
  0x16d20, 0x3020100, 0x7060504, 0x3020100, 0x16d28, 0x3020100, 0x7060504, 0x3020100, 0x16d30, 0x3020100, 0x7060504, 0x3020100, 0x16d38, 0x3020100, 0x7060504, 0x3020100,
  0x16d5e, 0x16d60, 0x16d62, 0x16d64, 0x16e5e, 0x16e60, 0x16e62, 0x16e64, 0x16d6e, 0x16d70, 0x16d72, 0x16d74, 0x16e6e, 0x16e70, 0x16e72, 0x16e74,
  0x16d7e, 0x16d80, 0x16d82, 0x16d84, 0x16e7e, 0x16e80, 0x16e82, 0x16e84, 0x16d8e, 0x16d90, 0x16d92, 0x16d94, 0x16e8e, 0x16e90, 0x16e92, 0x16e94,
  0x16d9e, 0x16da0, 0x16da2, 0x16da4, 0x16e9e, 0x16ea0, 0x16ea2, 0x16ea4, 0x16dae, 0x16db0, 0x16db2, 0x16db4, 0x16eae, 0x16eb0, 0x16eb2, 0x16eb4,
  0x16e3e, 0x16e40, 0x16e42, 0x16e44, 0x16f3e, 0x16f40, 0x16f42, 0x16f44, 0x16e4e, 0x16e50, 0x16e52, 0x16e54, 0x16f4e, 0x16f50, 0x16f52, 0x16f54,
  0x16d66, 0x16d68, 0x16d6a, 0x16d6c, 0x16e66, 0x16e68, 0x16e6a, 0x16e6c, 0x16d76, 0x16d78, 0x16d7a, 0x16d7c, 0x16e76, 0x16e78, 0x16e7a, 0x16e7c,
  0x16d86, 0x16d88, 0x16d8a, 0x16d8c, 0x16e86, 0x16e88, 0x16e8a, 0x16e8c, 0x16d96, 0x16d98, 0x16d9a, 0x16d9c, 0x16e96, 0x16e98, 0x16e9a, 0x16e9c,
  0x16da6, 0x16da8, 0x16daa, 0x16dac, 0x16ea6, 0x16ea8, 0x16eaa, 0x16eac, 0x16db6, 0x16db8, 0x16dba, 0x16dbc, 0x16eb6, 0x16eb8, 0x16eba, 0x16ebc,
  0x16e46, 0x16e48, 0x16e4a, 0x16e4c, 0x16f46, 0x16f48, 0x16f4a, 0x16f4c, 0x16e56, 0x16e58, 0x16e5a, 0x16e5c, 0x16f56, 0x16f58, 0x16f5a, 0x16f5c,
  0x16cc0, 0x16728, 0x1672a, 0x1672c, 0x1681e, 0x16828, 0x1682a, 0x1682c, 0x1672e, 0x16738, 0x1673a, 0x1673c, 0x1682e, 0x16838, 0x1683a, 0x1683c,
  0x1673e, 0x16748, 0x1674a, 0x1674c, 0x1683e, 0x16848, 0x1684a, 0x1684c, 0x1674e, 0x16758, 0x1675a, 0x1675c, 0x1684e, 0x16858, 0x1685a, 0x1685c,
  0x1675e, 0x16768, 0x1676a, 0x1676c, 0x1685e, 0x16868, 0x1686a, 0x1686c, 0x1676e, 0x16778, 0x1677a, 0x1677c, 0x1686e, 0x16878, 0x1687a, 0x1687c,
  0x167fe, 0x16808, 0x1680a, 0x1680c, 0x168fe, 0x16908, 0x1690a, 0x1690c, 0x1680e, 0x16818, 0x1681a, 0x1681c, 0x1690e, 0x16918, 0x1691a, 0x1691c,
  0x169c0, 0x16a00, 0x1691e, 0x16afe, 0x169e0, 0x16a20, 0x16ade, 0x16b1e, 0x169c2, 0x16a02, 0x16ac0, 0x16b00, 0x169e2, 0x16a22, 0x16ae0, 0x16b20,
  0x169c4, 0x16a04, 0x16ac2, 0x16b02, 0x169e4, 0x16a24, 0x16ae2, 0x16b22, 0x169c6, 0x16a06, 0x16ac4, 0x16b04, 0x169e6, 0x16a26, 0x16ae4, 0x16b24,
  0x169c8, 0x16a08, 0x16ac6, 0x16b06, 0x169e8, 0x16a28, 0x16ae6, 0x16b26, 0x169ca, 0x16a0a, 0x16ac8, 0x16b08, 0x169ea, 0x16a2a, 0x16ae8, 0x16b28,
  0x169dc, 0x16a1c, 0x16ada, 0x16b1a, 0x169fc, 0x16a3c, 0x16afa, 0x16b3a, 0x169de, 0x16a1e, 0x16adc, 0x16b1c, 0x169fe, 0x16a3e, 0x16afc, 0x16b3c,
  0x16b3e, 0x16bfe, 0x16c3e, 0x16c7e, 0x16bde, 0x16c1e, 0x16c5e, 0x16c9e, 0x16bc0, 0x16c00, 0x16c40, 0x16c80, 0x16be0, 0x16c20, 0x16c60, 0x16ca0,
  0x16bc2, 0x16c02, 0x16c42, 0x16c82, 0x16be2, 0x16c22, 0x16c62, 0x16ca2, 0x16bc4, 0x16c04, 0x16c44, 0x16c84, 0x16be4, 0x16c24, 0x16c64, 0x16ca4,
  0x16bc6, 0x16c06, 0x16c46, 0x16c86, 0x16be6, 0x16c26, 0x16c66, 0x16ca6, 0x16bc8, 0x16c08, 0x16c48, 0x16c88, 0x16be8, 0x16c28, 0x16c68, 0x16ca8,
  0x16bda, 0x16c1a, 0x16c5a, 0x16c9a, 0x16bfa, 0x16c3a, 0x16c7a, 0x16cba, 0x16bdc, 0x16c1c, 0x16c5c, 0x16c9c, 0x16bfc, 0x16c3c, 0x16c7c, 0x16cbc,
  0x16cbe, 0x16cc0, 0x16cc0, 0x16cc0, 0x16cc8, 0x16cc8, 0x16cc8, 0x16cc8, 0x16cd0, 0x16cd0, 0x16cd0, 0x16cd0, 0x16cd8, 0x16cd8, 0x16cd8, 0x16cd8,
  0x16ce0, 0x16ce0, 0x16ce0, 0x16ce0, 0x16ce8, 0x16ce8, 0x16ce8, 0x16ce8, 0x16cf0, 0x16cf0, 0x16cf0, 0x16cf0, 0x16cf8, 0x16cf8, 0x16cf8, 0x16cf8,
  0x16d00, 0x16d00, 0x16d00, 0x16d00, 0x16d08, 0x16d08, 0x16d08, 0x16d08, 0x16d10, 0x16d10, 0x16d10, 0x16d10, 0x16d18, 0x16d18, 0x16d18, 0x16d18,
  0x16d20, 0x16d20, 0x16d20, 0x16d20, 0x16d28, 0x16d28, 0x16d28, 0x16d28, 0x16d30, 0x16d30, 0x16d30, 0x16d30, 0x16d38, 0x16d38, 0x16d38, 0x16d38,
  0x16cc0, 0x16cc0, 0x16cc0, 0x16cc0, 0x16cc8, 0x16cc8, 0x16cc8, 0x16cc8, 0x16cd0, 0x16cd0, 0x16cd0, 0x16cd0, 0x16cd8, 0x16cd8, 0x16cd8, 0x16cd8,
  0x16ce0, 0x16ce0, 0x16ce0, 0x16ce0, 0x16ce8, 0x16ce8, 0x16ce8, 0x16ce8, 0x16cf0, 0x16cf0, 0x16cf0, 0x16cf0, 0x16cf8, 0x16cf8, 0x16cf8, 0x16cf8,
  0x16d00, 0x16d00, 0x16d00, 0x16d00, 0x16d08, 0x16d08, 0x16d08, 0x16d08, 0x16d10, 0x16d10, 0x16d10, 0x16d10, 0x16d18, 0x16d18, 0x16d18, 0x16d18,
  0x16d20, 0x16d20, 0x16d20, 0x16d20, 0x16d28, 0x16d28, 0x16d28, 0x16d28, 0x16d30, 0x16d30, 0x16d30, 0x16d30, 0x16d38, 0x16d38, 0x16d38, 0x16d38,
  0x16cc0, 0x16cc0, 0x16cc0, 0x16cc0, 0x16cc8, 0x16cc8, 0x16cc8, 0x16cc8, 0x16cd0, 0x16cd0, 0x16cd0, 0x16cd0, 0x16cd8, 0x16cd8, 0x16cd8, 0x16cd8,
  0x16ce0, 0x16ce0, 0x16ce0, 0x16ce0, 0x16ce8, 0x16ce8, 0x16ce8, 0x16ce8, 0x16cf0, 0x16cf0, 0x16cf0, 0x16cf0, 0x16cf8, 0x16cf8, 0x16cf8, 0x16cf8,
  0x16d00, 0x16d00, 0x16d00, 0x16d00, 0x16d08, 0x16d08, 0x16d08, 0x16d08, 0x16d10, 0x16d10, 0x16d10, 0x16d10, 0x16d18, 0x16d18, 0x16d18, 0x16d18,
  0x16d20, 0x16d20, 0x16d20, 0x16d20, 0x16d28, 0x16d28, 0x16d28, 0x16d28, 0x16d30, 0x16d30, 0x16d30, 0x16d30, 0x16d38, 0x16d38, 0x16d38, 0x16d38,
  0x16cc0, 0x16cc0, 0x3020100, 0x16500, 0x16cc8, 0x16cc8, 0x3020100, 0x16500, 0x16cd0, 0x16cd0, 0x3020100, 0x16500, 0x16cd8, 0x16cd8, 0x3020100, 0x16500,
  0x16ce0, 0x16ce0, 0x3020100, 0x16500, 0x16ce8, 0x16ce8, 0x3020100, 0x16500, 0x16cf0, 0x16cf0, 0x3020100, 0x16500, 0x16cf8, 0x16cf8, 0x3020100, 0x16500,
  0x16d00, 0x16d00, 0x3020100, 0x16500, 0x16d08, 0x16d08, 0x3020100, 0x16500, 0x16d10, 0x16d10, 0x3020100, 0x16500, 0x16d18, 0x16d18, 0x3020100, 0x16500,
  0x16d20, 0x16d20, 0x3020100, 0x16500, 0x16d28, 0x16d28, 0x3020100, 0x16500, 0x16d30, 0x16d30, 0x3020100, 0x16500, 0x16d38, 0x16d38, 0x3020100, 0x16500,
  0x16cc0, 0x16cc0, 0x3020100, 0x16500, 0x16cc8, 0x16cc8, 0x3020100, 0x16500, 0x16cd0, 0x16cd0, 0x3020100, 0x16500, 0x16cd8, 0x16cd8, 0x3020100, 0x16500,
  0x16ce0, 0x16ce0, 0x3020100, 0x16500, 0x16ce8, 0x16ce8, 0x3020100, 0x16500, 0x16cf0, 0x16cf0, 0x3020100, 0x16500, 0x16cf8, 0x16cf8, 0x3020100, 0x16500,
  0x16d00, 0x16d00, 0x3020100, 0x16500, 0x16d08, 0x16d08, 0x3020100, 0x16500, 0x16d10, 0x16d10, 0x3020100, 0x16500, 0x16d18, 0x16d18, 0x3020100, 0x16500,
  0x16d20, 0x16d20, 0x3020100, 0x16500, 0x16d28, 0x16d28, 0x3020100, 0x16500, 0x16d30, 0x16d30, 0x3020100, 0x16500, 0x16d38, 0x16d38, 0x3020100, 0x16500,
  0x16f5c, 0x16f64, 0x3020100, 0x16500, 0x1705c, 0x17064, 0x3020100, 0x16500, 0x16f6c, 0x16f74, 0x3020100, 0x16500, 0x1706c, 0x17074, 0x3020100, 0x16500,
  0x16f7c, 0x16f84, 0x3020100, 0x16500, 0x1707c, 0x17084, 0x3020100, 0x16500, 0x16f8c, 0x16f94, 0x3020100, 0x16500, 0x1708c, 0x17094, 0x3020100, 0x16500,
  0x16f9c, 0x16fa4, 0x3020100, 0x16500, 0x1709c, 0x170a4, 0x3020100, 0x16500, 0x16fac, 0x16fb4, 0x3020100, 0x16500, 0x170ac, 0x170b4, 0x3020100, 0x16500,
  0x1703c, 0x17044, 0x3020100, 0x16500, 0x1713c, 0x17144, 0x3020100, 0x16500, 0x1704c, 0x17054, 0x3020100, 0x16500, 0x1714c, 0x17154, 0x3020100, 0x16500,
  0x1715c, 0x16cc0, 0x5040504, 0x16500, 0x16cc8, 0x16cc8, 0x3020100, 0x16500, 0x16cd0, 0x16cd0, 0x3020100, 0x16500, 0x16cd8, 0x16cd8, 0x3020100, 0x16500,
  0x16ce0, 0x16ce0, 0x3020100, 0x16500, 0x16ce8, 0x16ce8, 0x3020100, 0x16500, 0x16cf0, 0x16cf0, 0x3020100, 0x16500, 0x16cf8, 0x16cf8, 0x3020100, 0x16500,
  0x16d00, 0x16d00, 0x3020100, 0x16500, 0x16d08, 0x16d08, 0x3020100, 0x16500, 0x16d10, 0x16d10, 0x3020100, 0x16500, 0x16d18, 0x16d18, 0x3020100, 0x16500,
  0x16d20, 0x16d20, 0x3020100, 0x16500, 0x16d28, 0x16d28, 0x3020100, 0x16500, 0x16d30, 0x16d30, 0x3020100, 0x16500, 0x16d38, 0x16d38, 0x3020100, 0x16500,
  0x16580, 0x3020100, 0x7060504, 0x3020100, 0x16680, 0x3020100, 0x7060504, 0x3020100, 0x16590, 0x3020100, 0x7060504, 0x3020100, 0x16690, 0x3020100, 0x7060504, 0x3020100,
  0x165a0, 0x3020100, 0x7060504, 0x3020100, 0x166a0, 0x3020100, 0x7060504, 0x3020100, 0x165b0, 0x3020100, 0x7060504, 0x3020100, 0x166b0, 0x3020100, 0x7060504, 0x3020100,
  0x165c0, 0x3020100, 0x7060504, 0x3020100, 0x166c0, 0x3020100, 0x7060504, 0x3020100, 0x165d0, 0x3020100, 0x7060504, 0x3020100, 0x166d0, 0x3020100, 0x7060504, 0x3020100,
  0x165e0, 0x3020100, 0x7060504, 0x3020100, 0x166e0, 0x3020100, 0x7060504, 0x3020100, 0x165f0, 0x3020100, 0x7060504, 0x3020100, 0x166f0, 0x3020100, 0x7060504, 0x3020100,
  0x16588, 0x3020100, 0x7060504, 0x3020100, 0x16688, 0x3020100, 0x7060504, 0x3020100, 0x16598, 0x3020100, 0x7060504, 0x3020100, 0x16698, 0x3020100, 0x7060504, 0x3020100,
  0x165a8, 0x3020100, 0x7060504, 0x3020100, 0x166a8, 0x3020100, 0x7060504, 0x3020100, 0x165b8, 0x3020100, 0x7060504, 0x3020100, 0x166b8, 0x3020100, 0x7060504, 0x3020100,
  0x165c8, 0x3020100, 0x7060504, 0x3020100, 0x166c8, 0x3020100, 0x7060504, 0x3020100, 0x165d8, 0x3020100, 0x7060504, 0x3020100, 0x166d8, 0x3020100, 0x7060504, 0x3020100,
  0x165e8, 0x3020100, 0x7060504, 0x3020100, 0x166e8, 0x3020100, 0x7060504, 0x3020100, 0x165f8, 0x3020100, 0x7060504, 0x3020100, 0x166f8, 0x3020100, 0x7060504, 0x3020100,
  0x16780, 0x1000100, 0x5040302, 0x5040100, 0x16880, 0x1000100, 0x5040302, 0x5040100, 0x16790, 0x1000100, 0x5040302, 0x5040100, 0x16890, 0x1000100, 0x5040302, 0x5040100,
  0x167a0, 0x1000100, 0x5040302, 0x5040100, 0x168a0, 0x1000100, 0x5040302, 0x5040100, 0x167b0, 0x1000100, 0x5040302, 0x5040100, 0x168b0, 0x1000100, 0x5040302, 0x5040100,
  0x167c0, 0x1000100, 0x5040302, 0x5040100, 0x168c0, 0x1000100, 0x5040302, 0x5040100, 0x167d0, 0x1000100, 0x5040302, 0x5040100, 0x168d0, 0x1000100, 0x5040302, 0x5040100,
  0x167e0, 0x1000100, 0x5040302, 0x5040100, 0x168e0, 0x1000100, 0x5040302, 0x5040100, 0x167f0, 0x1000100, 0x5040302, 0x5040100, 0x168f0, 0x1000100, 0x5040302, 0x5040100,
  0x16cc0, 0x3020100, 0x7060504, 0x3020100, 0x16cc8, 0x3020100, 0x7060504, 0x3020100, 0x16cd0, 0x3020100, 0x7060504, 0x3020100, 0x16cd8, 0x3020100, 0x7060504, 0x3020100,
  0x16ce0, 0x3020100, 0x7060504, 0x3020100, 0x16ce8, 0x3020100, 0x7060504, 0x3020100, 0x16cf0, 0x3020100, 0x7060504, 0x3020100, 0x16cf8, 0x3020100, 0x7060504, 0x3020100,
  0x16d00, 0x3020100, 0x7060504, 0x3020100, 0x16d08, 0x3020100, 0x7060504, 0x3020100, 0x16d10, 0x3020100, 0x7060504, 0x3020100, 0x16d18, 0x3020100, 0x7060504, 0x3020100,
  0x16d20, 0x3020100, 0x7060504, 0x3020100, 0x16d28, 0x3020100, 0x7060504, 0x3020100, 0x16d30, 0x3020100, 0x7060504, 0x3020100, 0x16d38, 0x3020100, 0x7060504, 0x3020100,
  0x16dbe, 0x16dc0, 0x16dc2, 0x16dc4, 0x16ebe, 0x16ec0, 0x16ec2, 0x16ec4, 0x16dce, 0x16dd0, 0x16dd2, 0x16dd4, 0x16ece, 0x16ed0, 0x16ed2, 0x16ed4,
  0x16dde, 0x16de0, 0x16de2, 0x16de4, 0x16ede, 0x16ee0, 0x16ee2, 0x16ee4, 0x16dee, 0x16df0, 0x16df2, 0x16df4, 0x16eee, 0x16ef0, 0x16ef2, 0x16ef4,
  0x16dfe, 0x16e00, 0x16e02, 0x16e04, 0x16efe, 0x16f00, 0x16f02, 0x16f04, 0x16e0e, 0x16e10, 0x16e12, 0x16e14, 0x16f0e, 0x16f10, 0x16f12, 0x16f14,
  0x16e1e, 0x16e20, 0x16e22, 0x16e24, 0x16f1e, 0x16f20, 0x16f22, 0x16f24, 0x16e2e, 0x16e30, 0x16e32, 0x16e34, 0x16f2e, 0x16f30, 0x16f32, 0x16f34,
  0x16dc6, 0x16dc8, 0x16dca, 0x16dcc, 0x16ec6, 0x16ec8, 0x16eca, 0x16ecc, 0x16dd6, 0x16dd8, 0x16dda, 0x16ddc, 0x16ed6, 0x16ed8, 0x16eda, 0x16edc,
  0x16de6, 0x16de8, 0x16dea, 0x16dec, 0x16ee6, 0x16ee8, 0x16eea, 0x16eec, 0x16df6, 0x16df8, 0x16dfa, 0x16dfc, 0x16ef6, 0x16ef8, 0x16efa, 0x16efc,
  0x16e06, 0x16e08, 0x16e0a, 0x16e0c, 0x16f06, 0x16f08, 0x16f0a, 0x16f0c, 0x16e16, 0x16e18, 0x16e1a, 0x16e1c, 0x16f16, 0x16f18, 0x16f1a, 0x16f1c,
  0x16e26, 0x16e28, 0x16e2a, 0x16e2c, 0x16f26, 0x16f28, 0x16f2a, 0x16f2c, 0x16e36, 0x16e38, 0x16e3a, 0x16e3c, 0x16f36, 0x16f38, 0x16f3a, 0x16f3c,
  0x1677e, 0x16788, 0x1678a, 0x1678c, 0x1687e, 0x16888, 0x1688a, 0x1688c, 0x1678e, 0x16798, 0x1679a, 0x1679c, 0x1688e, 0x16898, 0x1689a, 0x1689c,
  0x1679e, 0x167a8, 0x167aa, 0x167ac, 0x1689e, 0x168a8, 0x168aa, 0x168ac, 0x167ae, 0x167b8, 0x167ba, 0x167bc, 0x168ae, 0x168b8, 0x168ba, 0x168bc,
  0x167be, 0x167c8, 0x167ca, 0x167cc, 0x168be, 0x168c8, 0x168ca, 0x168cc, 0x167ce, 0x167d8, 0x167da, 0x167dc, 0x168ce, 0x168d8, 0x168da, 0x168dc,
  0x167de, 0x167e8, 0x167ea, 0x167ec, 0x168de, 0x168e8, 0x168ea, 0x168ec, 0x167ee, 0x167f8, 0x167fa, 0x167fc, 0x168ee, 0x168f8, 0x168fa, 0x168fc,
  0x169cc, 0x16a0c, 0x16aca, 0x16b0a, 0x169ec, 0x16a2c, 0x16aea, 0x16b2a, 0x169ce, 0x16a0e, 0x16acc, 0x16b0c, 0x169ee, 0x16a2e, 0x16aec, 0x16b2c,
  0x169d0, 0x16a10, 0x16ace, 0x16b0e, 0x169f0, 0x16a30, 0x16aee, 0x16b2e, 0x169d2, 0x16a12, 0x16ad0, 0x16b10, 0x169f2, 0x16a32, 0x16af0, 0x16b30,
  0x169d4, 0x16a14, 0x16ad2, 0x16b12, 0x169f4, 0x16a34, 0x16af2, 0x16b32, 0x169d6, 0x16a16, 0x16ad4, 0x16b14, 0x169f6, 0x16a36, 0x16af4, 0x16b34,
  0x169d8, 0x16a18, 0x16ad6, 0x16b16, 0x169f8, 0x16a38, 0x16af6, 0x16b36, 0x169da, 0x16a1a, 0x16ad8, 0x16b18, 0x169fa, 0x16a3a, 0x16af8, 0x16b38,
  0x16bca, 0x16c0a, 0x16c4a, 0x16c8a, 0x16bea, 0x16c2a, 0x16c6a, 0x16caa, 0x16bcc, 0x16c0c, 0x16c4c, 0x16c8c, 0x16bec, 0x16c2c, 0x16c6c, 0x16cac,
  0x16bce, 0x16c0e, 0x16c4e, 0x16c8e, 0x16bee, 0x16c2e, 0x16c6e, 0x16cae, 0x16bd0, 0x16c10, 0x16c50, 0x16c90, 0x16bf0, 0x16c30, 0x16c70, 0x16cb0,
  0x16bd2, 0x16c12, 0x16c52, 0x16c92, 0x16bf2, 0x16c32, 0x16c72, 0x16cb2, 0x16bd4, 0x16c14, 0x16c54, 0x16c94, 0x16bf4, 0x16c34, 0x16c74, 0x16cb4,
  0x16bd6, 0x16c16, 0x16c56, 0x16c96, 0x16bf6, 0x16c36, 0x16c76, 0x16cb6, 0x16bd8, 0x16c18, 0x16c58, 0x16c98, 0x16bf8, 0x16c38, 0x16c78, 0x16cb8,
  0x16cc0, 0x16cc0, 0x16cc0, 0x16cc0, 0x16cc8, 0x16cc8, 0x16cc8, 0x16cc8, 0x16cd0, 0x16cd0, 0x16cd0, 0x16cd0, 0x16cd8, 0x16cd8, 0x16cd8, 0x16cd8,
  0x16ce0, 0x16ce0, 0x16ce0, 0x16ce0, 0x16ce8, 0x16ce8, 0x16ce8, 0x16ce8, 0x16cf0, 0x16cf0, 0x16cf0, 0x16cf0, 0x16cf8, 0x16cf8, 0x16cf8, 0x16cf8,
  0x16d00, 0x16d00, 0x16d00, 0x16d00, 0x16d08, 0x16d08, 0x16d08, 0x16d08, 0x16d10, 0x16d10, 0x16d10, 0x16d10, 0x16d18, 0x16d18, 0x16d18, 0x16d18,
  0x16d20, 0x16d20, 0x16d20, 0x16d20, 0x16d28, 0x16d28, 0x16d28, 0x16d28, 0x16d30, 0x16d30, 0x16d30, 0x16d30, 0x16d38, 0x16d38, 0x16d38, 0x16d38,
  0x16cc0, 0x16cc0, 0x16cc0, 0x16cc0, 0x16cc8, 0x16cc8, 0x16cc8, 0x16cc8, 0x16cd0, 0x16cd0, 0x16cd0, 0x16cd0, 0x16cd8, 0x16cd8, 0x16cd8, 0x16cd8,
  0x16ce0, 0x16ce0, 0x16ce0, 0x16ce0, 0x16ce8, 0x16ce8, 0x16ce8, 0x16ce8, 0x16cf0, 0x16cf0, 0x16cf0, 0x16cf0, 0x16cf8, 0x16cf8, 0x16cf8, 0x16cf8,
  0x16d00, 0x16d00, 0x16d00, 0x16d00, 0x16d08, 0x16d08, 0x16d08, 0x16d08, 0x16d10, 0x16d10, 0x16d10, 0x16d10, 0x16d18, 0x16d18, 0x16d18, 0x16d18,
  0x16d20, 0x16d20, 0x16d20, 0x16d20, 0x16d28, 0x16d28, 0x16d28, 0x16d28, 0x16d30, 0x16d30, 0x16d30, 0x16d30, 0x16d38, 0x16d38, 0x16d38, 0x16d38,
  0x16cc0, 0x16cc0, 0x16cc0, 0x16cc0, 0x16cc8, 0x16cc8, 0x16cc8, 0x16cc8, 0x16cd0, 0x16cd0, 0x16cd0, 0x16cd0, 0x16cd8, 0x16cd8, 0x16cd8, 0x16cd8,
  0x16ce0, 0x16ce0, 0x16ce0, 0x16ce0, 0x16ce8, 0x16ce8, 0x16ce8, 0x16ce8, 0x16cf0, 0x16cf0, 0x16cf0, 0x16cf0, 0x16cf8, 0x16cf8, 0x16cf8, 0x16cf8,
  0x16d00, 0x16d00, 0x16d00, 0x16d00, 0x16d08, 0x16d08, 0x16d08, 0x16d08, 0x16d10, 0x16d10, 0x16d10, 0x16d10, 0x16d18, 0x16d18, 0x16d18, 0x16d18,
  0x16d20, 0x16d20, 0x16d20, 0x16d20, 0x16d28, 0x16d28, 0x16d28, 0x16d28, 0x16d30, 0x16d30, 0x16d30, 0x16d30, 0x16d38, 0x16d38, 0x16d38, 0x16d38,
  0x16cc0, 0x16cc0, 0x3020100, 0x16500, 0x16cc8, 0x16cc8, 0x3020100, 0x16500, 0x16cd0, 0x16cd0, 0x3020100, 0x16500, 0x16cd8, 0x16cd8, 0x3020100, 0x16500,
  0x16ce0, 0x16ce0, 0x3020100, 0x16500, 0x16ce8, 0x16ce8, 0x3020100, 0x16500, 0x16cf0, 0x16cf0, 0x3020100, 0x16500, 0x16cf8, 0x16cf8, 0x3020100, 0x16500,
  0x16d00, 0x16d00, 0x3020100, 0x16500, 0x16d08, 0x16d08, 0x3020100, 0x16500, 0x16d10, 0x16d10, 0x3020100, 0x16500, 0x16d18, 0x16d18, 0x3020100, 0x16500,
  0x16d20, 0x16d20, 0x3020100, 0x16500, 0x16d28, 0x16d28, 0x3020100, 0x16500, 0x16d30, 0x16d30, 0x3020100, 0x16500, 0x16d38, 0x16d38, 0x3020100, 0x16500,
  0x16cc0, 0x16cc0, 0x3020100, 0x16500, 0x16cc8, 0x16cc8, 0x3020100, 0x16500, 0x16cd0, 0x16cd0, 0x3020100, 0x16500, 0x16cd8, 0x16cd8, 0x3020100, 0x16500,
  0x16ce0, 0x16ce0, 0x3020100, 0x16500, 0x16ce8, 0x16ce8, 0x3020100, 0x16500, 0x16cf0, 0x16cf0, 0x3020100, 0x16500, 0x16cf8, 0x16cf8, 0x3020100, 0x16500,
  0x16d00, 0x16d00, 0x3020100, 0x16500, 0x16d08, 0x16d08, 0x3020100, 0x16500, 0x16d10, 0x16d10, 0x3020100, 0x16500, 0x16d18, 0x16d18, 0x3020100, 0x16500,
  0x16d20, 0x16d20, 0x3020100, 0x16500, 0x16d28, 0x16d28, 0x3020100, 0x16500, 0x16d30, 0x16d30, 0x3020100, 0x16500, 0x16d38, 0x16d38, 0x3020100, 0x16500,
  0x16fbc, 0x16fc4, 0x3020100, 0x16500, 0x170bc, 0x170c4, 0x3020100, 0x16500, 0x16fcc, 0x16fd4, 0x3020100, 0x16500, 0x170cc, 0x170d4, 0x3020100, 0x16500,
  0x16fdc, 0x16fe4, 0x3020100, 0x16500, 0x170dc, 0x170e4, 0x3020100, 0x16500, 0x16fec, 0x16ff4, 0x3020100, 0x16500, 0x170ec, 0x170f4, 0x3020100, 0x16500,
  0x16ffc, 0x17004, 0x3020100, 0x16500, 0x170fc, 0x17104, 0x3020100, 0x16500, 0x1700c, 0x17014, 0x3020100, 0x16500, 0x1710c, 0x17114, 0x3020100, 0x16500,
  0x1701c, 0x17024, 0x3020100, 0x16500, 0x1711c, 0x17124, 0x3020100, 0x16500, 0x1702c, 0x17034, 0x3020100, 0x16500, 0x1712c, 0x17134, 0x3020100, 0x16500,
  0x16cc0, 0x16cc0, 0x3020100, 0x16500, 0x16cc8, 0x16cc8, 0x3020100, 0x16500, 0x16cd0, 0x16cd0, 0x3020100, 0x16500, 0x16cd8, 0x16cd8, 0x3020100, 0x16500,
  0x16ce0, 0x16ce0, 0x3020100, 0x16500, 0x16ce8, 0x16ce8, 0x3020100, 0x16500, 0x16cf0, 0x16cf0, 0x3020100, 0x16500, 0x16cf8, 0x16cf8, 0x3020100, 0x16500,
  0x16d00, 0x16d00, 0x3020100, 0x16500, 0x16d08, 0x16d08, 0x3020100, 0x16500, 0x16d10, 0x16d10, 0x3020100, 0x16500, 0x16d18, 0x16d18, 0x3020100, 0x16500,
  0x16d20, 0x16d20, 0x3020100, 0x16500, 0x16d28, 0x16d28, 0x3020100, 0x16500, 0x16d30, 0x16d30, 0x3020100, 0x16500, 0x16d38, 0x16d38, 0x3020100, 0x16500,
  0x0, 0x0, 0x0, 0x0, 0x0, 0x0, 0x0, 0x0, 0x0, 0x0, 0x0, 0x0, 0x0, 0x0, 0x0, 0x0,
  0x0, 0x0, 0x0, 0x0, 0x0, 0x0, 0x0, 0x0, 0x0, 0x0, 0x0, 0x0, 0x0, 0x0, 0x0, 0x0,
  0x0, 0x0, 0x0, 0x0, 0x0, 0x0, 0x0, 0x0, 0x0, 0x0, 0x0, 0x0, 0x0, 0x0, 0x0, 0x0,
  0x0, 0x0, 0x0, 0x0, 0x0, 0x0, 0x0, 0x0, 0x0, 0x0, 0x0, 0x0, 0x0, 0x0, 0x0, 0x0,
  0x0, 0x0, 0x0, 0x0, 0x0, 0x0, 0x0, 0x0, 0x0, 0x0, 0x0, 0x0, 0x0, 0x0, 0x0, 0x0,
  0x0, 0x0, 0x0, 0x0, 0x0, 0x0, 0x0, 0x0, 0x0, 0x0, 0x0, 0x0, 0x0, 0x0, 0x0, 0x0,
  0x0, 0x0, 0x0, 0x0, 0x0, 0x0, 0x0, 0x0, 0x0, 0x0, 0x0, 0x0, 0x0, 0x0, 0x0, 0x0,
  0x0, 0x0, 0x0, 0x0, 0x0, 0x0, 0x0, 0x0, 0x0, 0x0, 0x0, 0x0, 0x0, 0x0, 0x0, 0x0,
  0x0, 0x0, 0x0, 0x0, 0x0, 0x0, 0x0, 0x0, 0x0, 0x0, 0x0, 0x0, 0x0, 0x0, 0x0, 0x0,
  0x0, 0x0, 0x0, 0x0, 0x0, 0x0, 0x0, 0x0, 0x0, 0x0, 0x0, 0x0, 0x0, 0x0, 0x0, 0x0,
  0x0, 0x0, 0x0, 0x0, 0x0, 0x0, 0x0, 0x0, 0x0, 0x0, 0x0, 0x0, 0x0, 0x0, 0x0, 0x0,
  0x0, 0x0, 0x0, 0x0, 0x0, 0x0, 0x0, 0x0, 0x0, 0x0, 0x0, 0x0, 0x0, 0x0, 0x0, 0x0,
  0x0, 0x0, 0x0, 0x0, 0x0, 0x0, 0x0, 0x0, 0x0, 0x0, 0x0, 0x0, 0x0, 0x0, 0x0, 0x0,
  0x0, 0x0, 0x0, 0x0, 0x0, 0x0, 0x0, 0x0, 0x0, 0x0, 0x0, 0x0, 0x0, 0x0, 0x0, 0x0,
  0x0, 0x0, 0x0, 0x0, 0x0, 0x0, 0x0, 0x0, 0x0, 0x0, 0x0, 0x0, 0x0, 0x0, 0x0, 0x0,
  0x0, 0x0, 0x0, 0x0, 0x0, 0x0, 0x0, 0x0, 0x0, 0x0, 0x0, 0x0, 0x0, 0x0, 0x0, 0x0
};

__global__ void __launch_bounds__(1024) mega(Args) {
  asm volatile(
    "s_load_dwordx16 s[4:19], s[0:1], 0\n"
    "s_load_dwordx16 s[20:35], s[0:1], 64\n"
    "v_readfirstlane_b32 s37, v0\n"
    "s_getpc_b64 s[40:41]\n"
    "K0:\n"
    "s_lshr_b32 s37, s37, 6\n"
    "s_cmpk_ge_u32 s2, 192\n"
    "s_cselect_b32 s42, 16, 0\n"
    "s_add_u32 s42, s42, s37\n"
    "s_lshl2_add_u32 s40, s42, s40\n"
    "s_add_u32 s40, s40, TBL1-K0\n"
    "s_mov_b32 s39, 0x100000\n"
    "s_setpc_b64 s[40:41]\n"
    "TBL1:\n"
    "s_branch GA0\n"
    "s_branch GA0\n"
    "s_branch GA0\n"
    "s_branch GA0\n"
    "s_branch GA0\n"
    "s_branch GA0\n"
    "s_branch S0\n"
    "s_branch S0\n"
    "s_branch S0\n"
    "s_branch S0\n"
    "s_branch S0\n"
    "s_branch S0\n"
    "s_branch MD0\n"
    "s_branch AUXT\n"
    "s_branch AUXT\n"
    "s_branch AUX0\n"
    "s_branch SIL0\n"
    "s_branch SIL0\n"
    "s_branch SIL0\n"
    "s_branch SIL0\n"
    "s_branch SIL0\n"
    "s_branch MD0\n"
    "s_branch S0\n"
    "s_branch S0\n"
    "s_branch S0\n"
    "s_branch S0\n"
    "s_branch S0\n"
    "s_branch S0\n"
    "s_branch AUXT\n"
    "s_branch AUXT\n"
    "s_branch AUXT\n"
    "s_branch AUX0\n"
    "TBL2:\n"
    "s_branch GA1\n"
    "s_branch GA1\n"
    "s_branch GA1\n"
    "s_branch GA1\n"
    "s_branch GA1\n"
    "s_branch GA1\n"
    "s_branch S1\n"
    "s_branch S1\n"
    "s_branch S1\n"
    "s_branch S1\n"
    "s_branch S1\n"
    "s_branch S1\n"
    "s_branch MD1\n"
    "s_branch AUX1\n"
    "s_branch AUX1\n"
    "s_branch AUX1\n"
    "s_branch SIL1\n"
    "s_branch SIL1\n"
    "s_branch SIL1\n"
    "s_branch SIL1\n"
    "s_branch SIL1\n"
    "s_branch MD1\n"
    "s_branch S1\n"
    "s_branch S1\n"
    "s_branch S1\n"
    "s_branch S1\n"
    "s_branch S1\n"
    "s_branch S1\n"
    "s_branch M1\n"
    "s_branch M1\n"
    "s_branch M1\n"
    "s_branch M1\n"
    "AUX0:\n"
    "s_setprio 3\n"
    "s_lshr_b32 s42, s2, 3\n"
    "s_mul_i32 s42, s42, 7\n"
    "s_add_u32 s42, s42, 7\n"
    "v_and_b32 v1, 7, v0\n"
    "v_add_u32 v1, s42, v1\n"
    "v_min_u32 v1, (END-K0)/128, v1\n"
    "v_lshlrev_b32 v1, 7, v1\n"
    "s_mov_b32 m0, 148480\n"
    "s_mov_b64 exec, 0x7f\n"
    "global_load_lds_dword v1, s[40:41]\n"
    "s_mov_b64 exec, -1\n"
    "v_and_b32 v1, 63, v0\n"
    "v_lshlrev_b32 v1, 3, v1\n"
    "v_mov_b32 v4, 0\n"
    "v_mov_b32 v5, 0\n"
    "v_add_u32 v1, 147840, v1\n"
    "ds_write_b64 v1, v[4:5]\n"
    "s_setprio 0\n"
    "AUXT:\n"
    "s_add_u32 s40, s40, 128\n"
    "s_setpc_b64 s[40:41]\n"
    "S0:\n"
    "v_and_b32 v2, 3, v0\n"
    "v_lshrrev_b32 v3, 2, v0\n"
    "v_mul_u32_u24 v2, 0x900, v2\n"
    "s_mul_i32 s42, s2, 9216\n"
    "v_mad_u32_u24 v1, v3, 12, v2\n"
    "v_add_u32 v1, s42, v1\n"
    "s_waitcnt lgkmcnt(0)\n"
    "global_load_dwordx3 v[4:6], v1, s[4:5] offset:-1152 nt\n"
    "global_load_dwordx3 v[8:10], v1, s[4:5] nt\n"
    "s_load_dword s36, s[20:21], 0\n"
    "s_branch S1\n"
    "GA0:\n"
    "s_and_b32 s44, s2, 7\n"
    "s_lshr_b32 s45, s2, 3\n"
    "s_mul_i32 s44, s44, 24\n"
    "s_add_u32 s38, s44, s45\n"
    "s_lshr_b32 s42, s37, 1\n"
    "s_mul_i32 s43, s38, 3\n"
    "s_add_u32 s43, s43, s42\n"
    "s_lshl_b32 s44, s43, 8\n"
    "s_lshl_b32 s45, s43, 1\n"
    "s_and_b32 s45, s45, -4\n"
    "v_and_b32 v1, 63, v0\n"
    "v_lshl_add_u32 v2, v1, 2, s44\n"
    "s_waitcnt lgkmcnt(0)\n"
    "global_load_dword v4, v2, s[6:7] nt\n"
    "global_load_dword v5, v2, s[8:9] nt\n"
    "s_memrealtime s[96:97]\n"
    "s_load_dword s46, s[10:11], s45\n"
    "global_load_dword v6, v2, s[12:13] nt\n"
    "global_load_dword v7, v2, s[14:15] nt\n"
    "s_load_dword s36, s[20:21], 0\n"
    "s_branch GA1\n"
    "SIL0:\n"
    "v_min_u32 v1, 287, v0\n"
    "v_lshlrev_b32 v1, 2, v1\n"
    "s_waitcnt lgkmcnt(0)\n"
    "global_load_dword v4, v1, s[18:19]\n"
    "s_load_dword s36, s[20:21], 0\n"
    "s_branch SIL1\n"
    "MD0:\n"
    "s_setprio 3\n"
    "s_mul_i32 s43, s2, 19\n"
    "s_lshr_b32 s43, s43, 9\n"
    "s_mul_i32 s44, s43, 27\n"
    "s_sub_u32 s44, s2, s44\n"
    "s_mul_i32 s45, s43, 442368\n"
    "s_lshl_b32 s46, s44, 7\n"
    "s_add_u32 s45, s45, s46\n"
    "v_bfe_u32 v1, v0, 3, 3\n"
    "v_and_b32 v2, 2, v1\n"
    "v_mul_u32_u24 v1, 0x1b00, v1\n"
    "v_xor_b32 v2, v2, v0\n"
    "v_and_b32 v2, 7, v2\n"
    "v_lshl_add_u32 v1, v2, 4, v1\n"
    "v_add_u32 v1, s45, v1\n"
    "v_xor_b32 v2, 64, v1\n"
    "v_add_u32 v2, 55296, v2\n"
    "s_lshl_b32 s46, s43, 7\n"
    "v_and_b32 v3, 31, v0\n"
    "v_lshl_add_u32 v5, v3, 2, s46\n"
    "s_mov_b32 m0, 138240\n"
    "s_cmpk_ge_u32 s2, 243\n"
    "s_waitcnt lgkmcnt(0)\n"
    "s_cbranch_scc1 MD0e\n"
    "global_load_dword v4, v5, s[18:19]\n"
    "global_load_lds_dwordx4 v1, s[16:17] nt\n"
    "s_add_u32 m0, m0, 1024\n"
    "v_add_u32 v1, 110592, v1\n"
    "global_load_lds_dwordx4 v2, s[16:17] nt\n"
    "s_add_u32 m0, m0, 1024\n"
    "v_add_u32 v2, 110592, v2\n"
    "global_load_lds_dwordx4 v1, s[16:17] nt\n"
    "s_add_u32 m0, m0, 1024\n"
    "v_add_u32 v1, 110592, v1\n"
    "global_load_lds_dwordx4 v2, s[16:17] nt\n"
    "s_add_u32 m0, m0, 1024\n"
    "v_add_u32 v2, 110592, v2\n"
    "global_load_lds_dwordx4 v1, s[16:17] nt\n"
    "s_add_u32 m0, m0, 1024\n"
    "v_add_u32 v1, 110592, v1\n"
    "global_load_lds_dwordx4 v2, s[16:17] nt\n"
    "s_add_u32 m0, m0, 1024\n"
    "v_add_u32 v2, 110592, v2\n"
    "global_load_lds_dwordx4 v1, s[16:17] nt\n"
    "s_add_u32 m0, m0, 1024\n"
    "s_nop 0\n"
    "global_load_lds_dwordx4 v2, s[16:17] nt\n"
    "MD0e:\n"
    "s_load_dword s36, s[20:21], 0\n"
    "s_branch MD1\n"
    "ABORT:\n"
    "s_endpgm\n"
    "S1:\n"
    "s_mov_b64 exec, -1\n"
    "s_mul_i32 s42, s2, 12416\n"
    "s_add_u32 s42, s42, 7334912\n"
    "v_lshl_add_u32 v11, v0, 4, s42\n"
    "v_bfe_u32 v2, v0, 4, 2\n"
    "v_and_b32 v3, 0x3c3, v0\n"
    "v_and_b32 v12, 3, v0\n"
    "v_mad_u32_u24 v3, v12, 15, v3\n"
    "v_lshl_add_u32 v12, v2, 2, v3\n"
    "v_add_u32 v12, 147072, v12\n"
    "v_and_b32 v13, 31, v0\n"
    "v_lshlrev_b32 v13, 4, v13\n"
    "v_add_u32 v13, 147456, v13\n"
    "v_mov_b32 v14, 147968\n"
    "v_mov_b32 v15, 1\n"
    "s_mov_b32 s43, 0x3f803f80\n"
    "s_movk_i32 s44, 6144\n"
    "s_sub_u32 s47, s37, 8\n"
    "s_cmp_ge_u32 s47, 3\n"
    "s_cselect_b32 s48, 1, 0\n"
    "s_add_u32 s45, s47, 3\n"
    "s_cmp_lt_i32 s47, 0\n"
    "s_cselect_b32 s47, s45, s47\n"
    "s_cmp_eq_u32 s47, 3\n"
    "s_cselect_b32 s47, 0, s47\n"
    "s_mul_i32 s45, s47, 30464\n"
    "v_and_b32 v122, 3, v0\n"
    "v_mul_u32_u24 v122, 1088, v122\n"
    "v_bfe_u32 v123, v0, 2, 4\n"
    "v_lshlrev_b32 v123, 3, v123\n"
    "v_add3_u32 v122, v122, v123, s45\n"
    "s_mul_i32 s45, s48, 4352\n"
    "v_add_u32 v123, s45, v122\n"
    "s_lshl_b32 s45, s48, 12\n"
    "s_add_u32 s45, s45, 107520\n"
    "v_and_b32 v124, 63, v0\n"
    "v_lshl_add_u32 v124, v124, 4, s45\n"
    "s_lshl_b32 s45, s47, 2\n"
    "v_add_u32 v125, s45, v14\n"
    "s_lshl_b32 s45, s45, 1\n"
    "s_add_u32 s45, s45, 148416\n"
    "v_mov_b32 v126, s45\n"
    "s_mul_i32 s49, s47, 3328\n"
    "s_barrier\n"
    "s_setprio 3\n"
    "s_waitcnt vmcnt(1)\n"
    "v_dot2_f32_bf16 v16, v4, s43, 0\n"
    "v_dot2_f32_bf16 v17, v4, v4, 0\n"
    "v_dot2_f32_bf16 v16, v5, s43, v16\n"
    "v_dot2_f32_bf16 v17, v5, v5, v17\n"
    "v_dot2_f32_bf16 v16, v6, s43, v16\n"
    "v_dot2_f32_bf16 v17, v6, v6, v17\n"
    "s_waitcnt vmcnt(0)\n"
    "v_dot2_f32_bf16 v18, v8, s43, 0\n"
    "v_dot2_f32_bf16 v19, v8, v8, 0\n"
    "v_dot2_f32_bf16 v18, v9, s43, v18\n"
    "v_dot2_f32_bf16 v19, v9, v9, v19\n"
    "v_dot2_f32_bf16 v18, v10, s43, v18\n"
    "v_dot2_f32_bf16 v19, v10, v10, v19\n"
    "v_lshlrev_b32 v24, 16, v4\n"
    "v_lshlrev_b32 v25, 16, v8\n"
    "v_and_b32 v26, 0xffff0000, v4\n"
    "v_permlane32_swap_b32 v16, v18\n"
    "v_permlane32_swap_b32 v17, v19\n"
    "v_add_f32 v16, v16, v18\n"
    "v_add_f32 v17, v17, v19\n"
    "v_and_b32 v27, 0xffff0000, v8\n"
    "v_lshlrev_b32 v28, 16, v5\n"
    "v_permlane16_swap_b32 v16, v17\n"
    "v_add_f32 v16, v16, v17\n"
    "v_lshlrev_b32 v29, 16, v9\n"
    "v_and_b32 v30, 0xffff0000, v5\n"
    "v_add_f32_dpp v16, v16, v16 row_ror:8 row_mask:0xf bank_mask:0xf\n"
    "v_and_b32 v31, 0xffff0000, v9\n"
    "v_lshlrev_b32 v32, 16, v6\n"
    "v_add_f32_dpp v16, v16, v16 row_ror:4 row_mask:0xf bank_mask:0xf\n"
    "ds_write_b32 v12, v16\n"
    "s_mov_b64 exec, 1\n"
    "ds_add_u32 v14, v15 offset:0\n"
    "s_mov_b64 exec, -1\n"
    "v_lshlrev_b32 v33, 16, v10\n"
    "v_and_b32 v34, 0xffff0000, v6\n"
    "v_and_b32 v35, 0xffff0000, v10\n"
    "s_branch E_2\n"
    "R_1:\n"
    "s_sub_u32 s39, s39, 1\n"
    "s_cbranch_scc1 ABORT\n"
    "E_2:\n"
    "ds_read_b32 v20, v14 offset:0\n"
    "ds_read_b128 v[16:19], v13\n"
    "s_waitcnt lgkmcnt(0)\n"
    "v_cmp_ne_u32 vcc, 6, v20\n"
    "s_cbranch_vccnz R_1\n"
    "s_add_u32 s36, s36, 1\n"
    "v_permlane16_swap_b32 v16, v18\n"
    "v_permlane16_swap_b32 v17, v19\n"
    "v_add_f32 v16, v16, v18\n"
    "v_add_f32 v17, v17, v19\n"
    "v_mov_b32 v39, s36\n"
    "v_add_f32_dpp v16, v16, v16 row_ror:8 row_mask:0xf bank_mask:0xf\n"
    "v_add_f32_dpp v17, v17, v17 row_ror:8 row_mask:0xf bank_mask:0xf\n"
    "v_mov_b32 v43, s36\n"
    "v_add_f32_dpp v16, v16, v16 row_ror:4 row_mask:0xf bank_mask:0xf\n"
    "v_add_f32_dpp v17, v17, v17 row_ror:4 row_mask:0xf bank_mask:0xf\n"
    "v_mul_f32 v16, 0x3ae38e39, v16\n"
    "v_mul_f32 v17, 0x3ae38e39, v17\n"
    "v_fma_f32 v17, -v16, v16, v17\n"
    "v_max_f32 v17, 0, v17\n"
    "v_cvt_pk_bf16_f32 v17, 0, v17\n"
    "v_add_f32 v17, 0x3727c5ac, v17\n"
    "v_cvt_pk_bf16_f32 v17, 0, v17\n"
    "v_sqrt_f32 v17, v17\n"
    "s_nop 0\n"
    "v_cvt_pk_bf16_f32 v16, v16, v17\n"
    "v_mov_b32 v17, v16\n"
    "s_nop 1\n"
    "v_permlane16_swap_b32 v16, v17\n"
    "v_and_b32 v20, 0xffff0000, v16\n"
    "v_and_b32 v21, 0xffff0000, v17\n"
    "v_lshlrev_b32 v18, 16, v16\n"
    "v_lshlrev_b32 v19, 16, v17\n"
    "v_rcp_f32 v20, v20\n"
    "v_rcp_f32 v21, v21\n"
    "v_pk_add_f32 v[24:25], v[24:25], v[18:19] neg_lo:[0,1] neg_hi:[0,1]\n"
    "v_pk_add_f32 v[26:27], v[26:27], v[18:19] neg_lo:[0,1] neg_hi:[0,1]\n"
    "v_pk_add_f32 v[28:29], v[28:29], v[18:19] neg_lo:[0,1] neg_hi:[0,1]\n"
    "v_cvt_pk_bf16_f32 v24, 0, v24\n"
    "v_cvt_pk_bf16_f32 v25, 0, v25\n"
    "v_cvt_pk_bf16_f32 v26, 0, v26\n"
    "v_cvt_pk_bf16_f32 v27, 0, v27\n"
    "v_cvt_pk_bf16_f32 v28, 0, v28\n"
    "v_cvt_pk_bf16_f32 v29, 0, v29\n"
    "v_pk_mul_f32 v[24:25], v[24:25], v[20:21]\n"
    "v_pk_mul_f32 v[26:27], v[26:27], v[20:21]\n"
    "v_pk_mul_f32 v[28:29], v[28:29], v[20:21]\n"
    "v_cvt_pk_bf16_f32 v36, v24, v25\n"
    "v_cvt_pk_bf16_f32 v37, v26, v27\n"
    "v_cvt_pk_bf16_f32 v38, v28, v29\n"
    "buffer_store_dwordx4 v[36:39], v11, s[20:23], 0 offen sc1\n"
    "v_pk_add_f32 v[30:31], v[30:31], v[18:19] neg_lo:[0,1] neg_hi:[0,1]\n"
    "v_pk_add_f32 v[32:33], v[32:33], v[18:19] neg_lo:[0,1] neg_hi:[0,1]\n"
    "v_pk_add_f32 v[34:35], v[34:35], v[18:19] neg_lo:[0,1] neg_hi:[0,1]\n"
    "v_cvt_pk_bf16_f32 v30, 0, v30\n"
    "v_cvt_pk_bf16_f32 v31, 0, v31\n"
    "v_cvt_pk_bf16_f32 v32, 0, v32\n"
    "v_cvt_pk_bf16_f32 v33, 0, v33\n"
    "v_cvt_pk_bf16_f32 v34, 0, v34\n"
    "v_cvt_pk_bf16_f32 v35, 0, v35\n"
    "v_pk_mul_f32 v[30:31], v[30:31], v[20:21]\n"
    "v_pk_mul_f32 v[32:33], v[32:33], v[20:21]\n"
    "v_pk_mul_f32 v[34:35], v[34:35], v[20:21]\n"
    "v_cvt_pk_bf16_f32 v40, v30, v31\n"
    "v_cvt_pk_bf16_f32 v41, v32, v33\n"
    "v_cvt_pk_bf16_f32 v42, v34, v35\n"
    "buffer_store_dwordx4 v[40:43], v11, s[20:23], s44 offen sc1\n"
    "s_setprio 0\n"
    "s_cmpk_ge_u32 s2, 192\n"
    "s_cbranch_scc1 ES1\n"
    "s_branch KST\n"
    "MD1:\n"
    "s_mov_b64 exec, -1\n"
    "v_bfe_u32 v5, v0, 4, 2\n"
    "v_lshlrev_b32 v1, 2, v3\n"
    "v_add_u32 v1, 149888, v1\n"
    "v_lshlrev_b32 v2, 4, v5\n"
    "v_add_u32 v2, 149888, v2\n"
    "v_bfe_u32 v6, v0, 2, 2\n"
    "v_bfe_u32 v7, v0, 1, 1\n"
    "v_and_b32 v8, 2, v6\n"
    "v_or_b32 v7, v7, v8\n"
    "v_and_b32 v8, 1, v5\n"
    "v_lshl_add_u32 v7, v8, 2, v7\n"
    "v_lshlrev_b32 v8, 10, v5\n"
    "v_lshl_add_u32 v8, v6, 7, v8\n"
    "v_lshl_add_u32 v8, v7, 4, v8\n"
    "v_and_b32 v7, 1, v0\n"
    "v_lshl_add_u32 v8, v7, 3, v8\n"
    "v_xor_b32 v9, 32, v8\n"
    "v_xor_b32 v10, 64, v8\n"
    "v_xor_b32 v11, 0x60, v8\n"
    "v_add_u32 v8, 138240, v8\n"
    "v_add_u32 v9, 138240, v9\n"
    "v_add_u32 v10, 138240, v10\n"
    "v_add_u32 v11, 138240, v11\n"
    "v_and_b32 v3, 63, v0\n"
    "v_lshlrev_b32 v3, 3, v3\n"
    "s_mul_i32 s43, s43, 13824\n"
    "s_lshl_b32 s44, s44, 9\n"
    "s_add_u32 s43, s43, s44\n"
    "s_add_u32 s43, s43, 10519552\n"
    "s_barrier\n"
    "s_cmpk_ge_u32 s2, 243\n"
    "s_cbranch_scc1 M9\n"
    "s_waitcnt vmcnt(8)\n"
    "v_lshlrev_b32 v5, 16, v4\n"
    "v_and_b32 v4, 0xffff0000, v4\n"
    "v_mul_f32 v6, 0xbfb8aa3b, v5\n"
    "v_mul_f32 v7, 0xbfb8aa3b, v4\n"
    "v_exp_f32 v6, v6\n"
    "v_exp_f32 v7, v7\n"
    "v_add_f32 v6, 1.0, v6\n"
    "v_add_f32 v7, 1.0, v7\n"
    "v_rcp_f32 v6, v6\n"
    "v_rcp_f32 v7, v7\n"
    "v_mul_f32 v5, v5, v6\n"
    "v_mul_f32 v4, v4, v7\n"
    "v_cvt_pk_bf16_f32 v4, v5, v4\n"
    "ds_write_b32 v1, v4\n"
    "ds_read_b128 v[12:15], v2\n"
    "ds_read_b128 v[16:19], v2 offset:64\n"
    "s_waitcnt lgkmcnt(0)\n"
    "s_add_u32 s36, s36, 1\n"
    "s_waitcnt vmcnt(4)\n"
    "ds_read_b64_tr_b16 v[40:41], v8 offset:0\n"
    "ds_read_b64_tr_b16 v[42:43], v8 offset:512\n"
    "ds_read_b64_tr_b16 v[44:45], v9 offset:0\n"
    "ds_read_b64_tr_b16 v[46:47], v9 offset:512\n"
    "ds_read_b64_tr_b16 v[48:49], v10 offset:0\n"
    "ds_read_b64_tr_b16 v[50:51], v10 offset:512\n"
    "ds_read_b64_tr_b16 v[52:53], v11 offset:0\n"
    "ds_read_b64_tr_b16 v[54:55], v11 offset:512\n"
    "s_waitcnt lgkmcnt(6)\n"
    "v_mfma_f32_16x16x32_bf16 v[20:23], v[12:15], v[40:43], 0\n"
    "s_waitcnt lgkmcnt(4)\n"
    "v_mfma_f32_16x16x32_bf16 v[24:27], v[12:15], v[44:47], 0\n"
    "s_waitcnt lgkmcnt(2)\n"
    "v_mfma_f32_16x16x32_bf16 v[28:31], v[12:15], v[48:51], 0\n"
    "s_waitcnt lgkmcnt(0)\n"
    "v_mfma_f32_16x16x32_bf16 v[32:35], v[12:15], v[52:55], 0\n"
    "s_waitcnt vmcnt(0)\n"
    "ds_read_b64_tr_b16 v[56:57], v8 offset:4096\n"
    "ds_read_b64_tr_b16 v[58:59], v8 offset:4608\n"
    "ds_read_b64_tr_b16 v[60:61], v9 offset:4096\n"
    "ds_read_b64_tr_b16 v[62:63], v9 offset:4608\n"
    "ds_read_b64_tr_b16 v[64:65], v10 offset:4096\n"
    "ds_read_b64_tr_b16 v[66:67], v10 offset:4608\n"
    "ds_read_b64_tr_b16 v[68:69], v11 offset:4096\n"
    "ds_read_b64_tr_b16 v[70:71], v11 offset:4608\n"
    "s_waitcnt lgkmcnt(6)\n"
    "v_mfma_f32_16x16x32_bf16 v[20:23], v[16:19], v[56:59], v[20:23]\n"
    "s_waitcnt lgkmcnt(4)\n"
    "v_mfma_f32_16x16x32_bf16 v[24:27], v[16:19], v[60:63], v[24:27]\n"
    "s_waitcnt lgkmcnt(2)\n"
    "v_mfma_f32_16x16x32_bf16 v[28:31], v[16:19], v[64:67], v[28:31]\n"
    "s_waitcnt lgkmcnt(0)\n"
    "v_mfma_f32_16x16x32_bf16 v[32:35], v[16:19], v[68:71], v[32:35]\n"
    "s_nop 1\n"
    "v_mov_b32 v21, s36\n"
    "s_nop 0\n"
    "v_mov_b32_dpp v20, v24 quad_perm:[0,1,2,3] row_mask:0x2 bank_mask:0xf\n"
    "s_nop 1\n"
    "v_mov_b32_dpp v20, v28 quad_perm:[0,1,2,3] row_mask:0x4 bank_mask:0xf\n"
    "s_nop 1\n"
    "v_mov_b32_dpp v20, v32 quad_perm:[0,1,2,3] row_mask:0x8 bank_mask:0xf\n"
    "buffer_store_dwordx2 v[20:21], v3, s[20:23], s43 offen sc1\n"
    "s_setprio 0\n"
    "s_cmpk_ge_u32 s2, 192\n"
    "s_cbranch_scc1 ES1\n"
    "s_branch AUX3\n"
    "GA1:\n"
    "s_mov_b64 exec, -1\n"
    "s_setprio 1\n"
    "s_mov_b32 s48, 0x01000c0c\n"
    "s_bitcmp1_b32 s37, 0\n"
    "s_cselect_b32 s47, 0x03020c0c, s48\n"
    "s_and_b32 s43, s43, 1\n"
    "s_lshl_b32 s43, s43, 4\n"
    "s_mul_i32 s44, s42, 30464\n"
    "s_and_b32 s45, s37, 1\n"
    "s_lshl_b32 s45, s45, 9\n"
    "s_add_u32 s44, s44, s45\n"
    "v_lshl_add_u32 v2, v1, 3, s44\n"
    "v_lshlrev_b32 v52, 4, v0\n"
    "v_add_u32 v52, 126368, v52\n"
    "s_lshl_b32 s45, s37, 2\n"
    "s_add_u32 s45, s45, 147952\n"
    "v_mov_b32 v12, s45\n"
    "s_lshl_b32 s45, s42, 2\n"
    "s_add_u32 s44, s45, 147968\n"
    "v_mov_b32 v13, s44\n"
    "s_lshl_b32 s45, s45, 1\n"
    "s_add_u32 s45, s45, 148416\n"
    "v_mov_b32 v14, s45\n"
    "v_mov_b32 v15, 1\n"
    "v_mov_b32 v8, 0x3fb8aa3b\n"
    "s_mov_b32 s48, 0x3e22f983\n"
    "s_mov_b32 s49, 0x31dc9c88\n"
    "s_mov_b32 s50, 0x41000000\n"
    "s_mov_b32 s51, 0x43000000\n"
    "s_barrier\n"
    "s_waitcnt lgkmcnt(0)\n"
    "s_lshr_b32 s46, s46, s43\n"
    "s_lshl_b32 s46, s46, 16\n"
    "s_add_u32 s36, s36, 1\n"
    "v_mul_f32 v9, s46, v8\n"
    "v_exp_f32 v9, v9\n"
    "s_waitcnt vmcnt(3)\n"
    "v_perm_b32 v4, v4, v4, s47\n"
    "v_mul_f32 v4, v4, v8\n"
    "v_cvt_pk_bf16_f32 v9, 0, v9\n"
    "v_exp_f32 v4, v4\n"
    "s_waitcnt vmcnt(2)\n"
    "v_perm_b32 v5, v5, v5, s47\n"
    "v_mul_f32 v5, v5, v9\n"
    "v_cvt_pk_bf16_f32 v4, 0, v4\n"
    "v_mul_f32 v10, s48, v5\n"
    "v_mul_f32 v4, v4, v9\n"
    "v_fma_f32 v11, v5, s48, -v10\n"
    "v_mul_f32_e64 v4, v4, -v8\n"
    "v_fma_f32 v11, v5, s49, v11\n"
    "v_mul_f32 v20, s50, v10\n"
    "v_mul_f32 v21, s50, v4\n"
    "v_fract_f32 v20, v20\n"
    "v_exp_f32 v21, v21\n"
    "v_fma_f32 v20, v11, s50, v20\n"
    "v_cos_f32 v16, v20\n"
    "v_sin_f32 v17, v20\n"
    "v_mul_f32 v22, s51, v10\n"
    "v_mul_f32 v23, s51, v4\n"
    "v_fract_f32 v22, v22\n"
    "v_exp_f32 v23, v23\n"
    "v_fma_f32 v22, v11, s51, v22\n"
    "v_cos_f32 v18, v22\n"
    "v_sin_f32 v19, v22\n"
    "v_pk_mul_f32 v[16:17], v[20:21], v[16:17] op_sel:[1,0] op_sel_hi:[1,1]\n"
    "v_pk_mul_f32 v[18:19], v[22:23], v[18:19] op_sel:[1,0] op_sel_hi:[1,1]\n"
    "s_cmpk_lt_u32 s37, 4\n"
    "s_cbranch_scc1 ASIDE\n"
    "ds_write_b128 v52, v[16:19]\n"
    "s_mov_b64 exec, 1\n"
    "ds_write_b32 v12, v15 offset:4\n"
    "s_mov_b64 exec, -1\n"
    "v_mov_b32 v36, 148448\n"
    "v_mov_b32 v37, s96\n"
    "ds_write_b32 v36, v37\n"
    "ASIDE:\n"
    "s_setprio 0\n"
    "v_fract_f32 v22, v10\n"
    "v_exp_f32 v23, v4\n"
    "v_add_f32 v22, v11, v22\n"
    "v_cos_f32 v24, v22\n"
    "v_sin_f32 v25, v22\n"
    "s_waitcnt vmcnt(0)\n"
    "v_and_b32 v32, 0x7fff7fff, v6\n"
    "v_and_b32 v33, 0x7fff7fff, v7\n"
    "v_pk_max_u16 v32, v32, v33\n"
    "v_pk_mul_f32 v[24:25], v[22:23], v[24:25] op_sel:[1,0] op_sel_hi:[1,1]\n"
    "v_lshlrev_b32 v33, 16, v32\n"
    "v_max_u32 v32, v32, v33\n"
    "v_perm_b32 v6, v6, v6, s47\n"
    "v_perm_b32 v7, v7, v7, s47\n"
    "v_max_u32_dpp v32, v32, v32 row_ror:8 row_mask:0xf bank_mask:0xf\n"
    "v_pk_mul_f32 v[20:21], v[18:19], v[18:19] op_sel:[1,1] op_sel_hi:[1,0] neg_lo:[0,1]\n"
    "v_pk_fma_f32 v[30:31], v[18:19], v[18:19], v[20:21] op_sel_hi:[0,1,1]\n"
    "v_max_u32_dpp v32, v32, v32 row_ror:4 row_mask:0xf bank_mask:0xf\n"
    "v_pk_mul_f32 v[20:21], v[24:25], v[24:25] op_sel:[1,1] op_sel_hi:[1,0] neg_lo:[0,1]\n"
    "v_pk_fma_f32 v[26:27], v[24:25], v[24:25], v[20:21] op_sel_hi:[0,1,1]\n"
    "v_max_u32_dpp v32, v32, v32 row_ror:2 row_mask:0xf bank_mask:0xf\n"
    "v_pk_mul_f32 v[20:21], v[26:27], v[26:27] op_sel:[1,1] op_sel_hi:[1,0] neg_lo:[0,1]\n"
    "v_pk_fma_f32 v[28:29], v[26:27], v[26:27], v[20:21] op_sel_hi:[0,1,1]\n"
    "v_max_u32_dpp v32, v32, v32 row_ror:1 row_mask:0xf bank_mask:0xf\n"
    "v_and_b32 v34, 0x7fffff, v9\n"
    "v_or_b32 v34, 0x35800000, v34\n"
    "v_max_u32_dpp v32, v32, v32 row_bcast:15 row_mask:0xa bank_mask:0xf\n"
    "v_and_b32 v35, 0x7f800000, v9\n"
    "s_nop 0\n"
    "v_max_u32_dpp v32, v32, v32 row_bcast:31 row_mask:0xc bank_mask:0xf\n"
    "s_nop 0\n"
    "v_readlane_b32 s44, v32, 63\n"
    "s_lshr_b32 s44, s44, 23\n"
    "s_max_u32 s44, s44, 26\n"
    "s_min_u32 s44, s44, 226\n"
    "s_sub_u32 s46, 266, s44\n"
    "s_lshl_b32 s46, s46, 23\n"
    "s_sub_u32 s44, s44, 132\n"
    "v_pk_mul_f32 v[36:37], v[6:7], s[46:47] op_sel_hi:[1,0]\n"
    "v_ldexp_f32 v35, v35, s44\n"
    "ds_write_b64 v14, v[34:35]\n"
    "v_pk_mul_f32 v[20:21], v[36:37], v[24:25] op_sel:[1,1] op_sel_hi:[1,0] neg_lo:[0,1]\n"
    "v_pk_fma_f32 v[38:39], v[36:37], v[24:25], v[20:21] op_sel_hi:[0,1,1]\n"
    "v_pk_mul_f32 v[20:21], v[38:39], v[24:25] op_sel:[1,1] op_sel_hi:[1,0] neg_lo:[0,1]\n"
    "v_pk_fma_f32 v[40:41], v[38:39], v[24:25], v[20:21] op_sel_hi:[0,1,1]\n"
    "v_pk_mul_f32 v[20:21], v[40:41], v[24:25] op_sel:[1,1] op_sel_hi:[1,0] neg_lo:[0,1]\n"
    "v_pk_fma_f32 v[42:43], v[40:41], v[24:25], v[20:21] op_sel_hi:[0,1,1]\n"
    "v_pk_mul_f32 v[20:21], v[36:37], v[30:31] op_sel:[1,1] op_sel_hi:[1,0] neg_lo:[0,1]\n"
    "v_pk_fma_f32 v[44:45], v[36:37], v[30:31], v[20:21] op_sel_hi:[0,1,1]\n"
    "v_cvt_pk_f16_f32 v60, v36, v38\n"
    "v_cvt_pk_f16_f32 v61, v40, v42\n"
    "v_cvt_pk_f16_f32 v62, -v37, -v39\n"
    "v_cvt_pk_f16_f32 v63, -v41, -v43\n"
    "ds_write_b64 v2, v[60:61] offset:0\n"
    "ds_write_b64 v2, v[62:63] offset:6528\n"
    "v_pk_mul_f32 v[20:21], v[42:43], v[24:25] op_sel:[1,1] op_sel_hi:[1,0] neg_lo:[0,1]\n"
    "v_pk_fma_f32 v[36:37], v[42:43], v[24:25], v[20:21] op_sel_hi:[0,1,1]\n"
    "v_pk_mul_f32 v[20:21], v[36:37], v[24:25] op_sel:[1,1] op_sel_hi:[1,0] neg_lo:[0,1]\n"
    "v_pk_fma_f32 v[38:39], v[36:37], v[24:25], v[20:21] op_sel_hi:[0,1,1]\n"
    "v_pk_mul_f32 v[20:21], v[38:39], v[24:25] op_sel:[1,1] op_sel_hi:[1,0] neg_lo:[0,1]\n"
    "v_pk_fma_f32 v[40:41], v[38:39], v[24:25], v[20:21] op_sel_hi:[0,1,1]\n"
    "v_pk_mul_f32 v[20:21], v[40:41], v[24:25] op_sel:[1,1] op_sel_hi:[1,0] neg_lo:[0,1]\n"
    "v_pk_fma_f32 v[42:43], v[40:41], v[24:25], v[20:21] op_sel_hi:[0,1,1]\n"
    "v_cvt_pk_f16_f32 v64, v36, v38\n"
    "v_cvt_pk_f16_f32 v65, v40, v42\n"
    "v_cvt_pk_f16_f32 v66, -v37, -v39\n"
    "v_cvt_pk_f16_f32 v67, -v41, -v43\n"
    "ds_write_b64 v2, v[64:65] offset:1088\n"
    "ds_write_b64 v2, v[66:67] offset:7616\n"
    "v_pk_mul_f32 v[20:21], v[44:45], v[26:27] op_sel:[1,1] op_sel_hi:[1,0] neg_lo:[0,1]\n"
    "v_pk_fma_f32 v[46:47], v[44:45], v[26:27], v[20:21] op_sel_hi:[0,1,1]\n"
    "v_pk_mul_f32 v[20:21], v[44:45], v[28:29] op_sel:[1,1] op_sel_hi:[1,0] neg_lo:[0,1]\n"
    "v_pk_fma_f32 v[48:49], v[44:45], v[28:29], v[20:21] op_sel_hi:[0,1,1]\n"
    "v_pk_mul_f32 v[20:21], v[48:49], v[26:27] op_sel:[1,1] op_sel_hi:[1,0] neg_lo:[0,1]\n"
    "v_pk_fma_f32 v[50:51], v[48:49], v[26:27], v[20:21] op_sel_hi:[0,1,1]\n"
    "v_pk_mul_f32 v[20:21], v[44:45], v[30:31] op_sel:[1,1] op_sel_hi:[1,0] neg_lo:[0,1]\n"
    "v_pk_fma_f32 v[36:37], v[44:45], v[30:31], v[20:21] op_sel_hi:[0,1,1]\n"
    "v_cvt_pk_f16_f32 v60, v44, v46\n"
    "v_cvt_pk_f16_f32 v61, v48, v50\n"
    "v_cvt_pk_f16_f32 v62, -v45, -v47\n"
    "v_cvt_pk_f16_f32 v63, -v49, -v51\n"
    "ds_write_b64 v2, v[60:61] offset:2176\n"
    "ds_write_b64 v2, v[62:63] offset:8704\n"
    "v_pk_mul_f32 v[20:21], v[36:37], v[30:31] op_sel:[1,1] op_sel_hi:[1,0] neg_lo:[0,1]\n"
    "v_pk_fma_f32 v[38:39], v[36:37], v[30:31], v[20:21] op_sel_hi:[0,1,1]\n"
    "v_pk_mul_f32 v[20:21], v[36:37], v[28:29] op_sel:[1,1] op_sel_hi:[1,0] neg_lo:[0,1]\n"
    "v_pk_fma_f32 v[40:41], v[36:37], v[28:29], v[20:21] op_sel_hi:[0,1,1]\n"
    "v_pk_mul_f32 v[20:21], v[38:39], v[28:29] op_sel:[1,1] op_sel_hi:[1,0] neg_lo:[0,1]\n"
    "v_pk_fma_f32 v[42:43], v[38:39], v[28:29], v[20:21] op_sel_hi:[0,1,1]\n"
    "v_pk_mul_f32 v[20:21], v[38:39], v[30:31] op_sel:[1,1] op_sel_hi:[1,0] neg_lo:[0,1]\n"
    "v_pk_fma_f32 v[44:45], v[38:39], v[30:31], v[20:21] op_sel_hi:[0,1,1]\n"
    "v_cvt_pk_f16_f32 v64, v40, v42\n"
    "v_cvt_pk_f16_f32 v65, v36, v38\n"
    "v_cvt_pk_f16_f32 v66, -v41, -v43\n"
    "v_cvt_pk_f16_f32 v67, -v37, -v39\n"
    "ds_write_b64 v2, v[64:65] offset:3264\n"
    "ds_write_b64 v2, v[66:67] offset:9792\n"
    "v_pk_mul_f32 v[20:21], v[44:45], v[30:31] op_sel:[1,1] op_sel_hi:[1,0] neg_lo:[0,1]\n"
    "v_pk_fma_f32 v[46:47], v[44:45], v[30:31], v[20:21] op_sel_hi:[0,1,1]\n"
    "v_pk_mul_f32 v[20:21], v[46:47], v[30:31] op_sel:[1,1] op_sel_hi:[1,0] neg_lo:[0,1]\n"
    "v_pk_fma_f32 v[48:49], v[46:47], v[30:31], v[20:21] op_sel_hi:[0,1,1]\n"
    "v_pk_mul_f32 v[20:21], v[48:49], v[30:31] op_sel:[1,1] op_sel_hi:[1,0] neg_lo:[0,1]\n"
    "v_pk_fma_f32 v[50:51], v[48:49], v[30:31], v[20:21] op_sel_hi:[0,1,1]\n"
    "v_pk_mul_f32 v[20:21], v[50:51], v[30:31] op_sel:[1,1] op_sel_hi:[1,0] neg_lo:[0,1]\n"
    "v_pk_fma_f32 v[36:37], v[50:51], v[30:31], v[20:21] op_sel_hi:[0,1,1]\n"
    "v_cvt_pk_f16_f32 v60, v44, v46\n"
    "v_cvt_pk_f16_f32 v61, v48, v50\n"
    "v_cvt_pk_f16_f32 v62, -v45, -v47\n"
    "v_cvt_pk_f16_f32 v63, -v49, -v51\n"
    "ds_write_b64 v2, v[60:61] offset:4352\n"
    "ds_write_b64 v2, v[62:63] offset:10880\n"
    "v_cvt_pk_f16_f32 v38, v36, -v37\n"
    "ds_write_b16 v2, v38 offset:5440\n"
    "ds_write_b16_d16_hi v2, v38 offset:11968\n"
    "s_mov_b64 exec, 1\n"
    "ds_add_u32 v13, v15 offset:12\n"
    "s_mov_b64 exec, -1\n"
    "s_cmpk_eq_u32 s37, 4\n"
    "s_cbranch_scc1 HELP\n"
    "s_cmpk_eq_u32 s37, 5\n"
    "s_cbranch_scc1 CONST\n"
    "BSIDE:\n"
    "v_mov_b32 v36, 0x46800000\n"
    "v_mov_b32 v37, 0\n"
    "v_pk_mul_f32 v[38:39], v[18:19], v[36:37] op_sel_hi:[1,0]\n"
    "v_cvt_pk_f16_f32 v44, v36, v38\n"
    "v_cvt_pk_f16_f32 v46, v37, v39\n"
    "v_pk_mul_f32 v[36:37], v[16:17], v[36:37] op_sel_hi:[1,0]\n"
    "v_pk_mul_f32 v[42:43], v[38:39], v[16:17] op_sel:[1,1] op_sel_hi:[1,0] neg_lo:[0,1]\n"
    "v_pk_fma_f32 v[38:39], v[38:39], v[16:17], v[42:43] op_sel_hi:[0,1,1]\n"
    "v_cvt_pk_f16_f32 v45, v36, v38\n"
    "v_cvt_pk_f16_f32 v47, v37, v39\n"
    "v_pk_mul_f32 v[40:41], v[36:37], v[16:17] op_sel:[1,1] op_sel_hi:[1,0] neg_lo:[0,1]\n"
    "v_pk_fma_f32 v[36:37], v[36:37], v[16:17], v[40:41] op_sel_hi:[0,1,1]\n"
    "ds_write_b64 v2, v[44:45] offset:13056\n"
    "v_pk_mul_f32 v[42:43], v[38:39], v[16:17] op_sel:[1,1] op_sel_hi:[1,0] neg_lo:[0,1]\n"
    "v_pk_fma_f32 v[38:39], v[38:39], v[16:17], v[42:43] op_sel_hi:[0,1,1]\n"
    "ds_write_b64 v2, v[46:47] offset:21760\n"
    "v_cvt_pk_f16_f32 v48, v36, v38\n"
    "v_cvt_pk_f16_f32 v50, v37, v39\n"
    "v_pk_mul_f32 v[40:41], v[36:37], v[16:17] op_sel:[1,1] op_sel_hi:[1,0] neg_lo:[0,1]\n"
    "v_pk_fma_f32 v[36:37], v[36:37], v[16:17], v[40:41] op_sel_hi:[0,1,1]\n"
    "v_pk_mul_f32 v[42:43], v[38:39], v[16:17] op_sel:[1,1] op_sel_hi:[1,0] neg_lo:[0,1]\n"
    "v_pk_fma_f32 v[38:39], v[38:39], v[16:17], v[42:43] op_sel_hi:[0,1,1]\n"
    "v_cvt_pk_f16_f32 v49, v36, v38\n"
    "v_cvt_pk_f16_f32 v51, v37, v39\n"
    "v_pk_mul_f32 v[40:41], v[36:37], v[16:17] op_sel:[1,1] op_sel_hi:[1,0] neg_lo:[0,1]\n"
    "v_pk_fma_f32 v[36:37], v[36:37], v[16:17], v[40:41] op_sel_hi:[0,1,1]\n"
    "ds_write_b64 v2, v[48:49] offset:14144\n"
    "v_pk_mul_f32 v[42:43], v[38:39], v[16:17] op_sel:[1,1] op_sel_hi:[1,0] neg_lo:[0,1]\n"
    "v_pk_fma_f32 v[38:39], v[38:39], v[16:17], v[42:43] op_sel_hi:[0,1,1]\n"
    "ds_write_b64 v2, v[50:51] offset:22848\n"
    "v_cvt_pk_f16_f32 v44, v36, v38\n"
    "v_cvt_pk_f16_f32 v46, v37, v39\n"
    "v_pk_mul_f32 v[40:41], v[36:37], v[16:17] op_sel:[1,1] op_sel_hi:[1,0] neg_lo:[0,1]\n"
    "v_pk_fma_f32 v[36:37], v[36:37], v[16:17], v[40:41] op_sel_hi:[0,1,1]\n"
    "v_pk_mul_f32 v[42:43], v[38:39], v[16:17] op_sel:[1,1] op_sel_hi:[1,0] neg_lo:[0,1]\n"
    "v_pk_fma_f32 v[38:39], v[38:39], v[16:17], v[42:43] op_sel_hi:[0,1,1]\n"
    "v_cvt_pk_f16_f32 v45, v36, v38\n"
    "v_cvt_pk_f16_f32 v47, v37, v39\n"
    "v_pk_mul_f32 v[40:41], v[36:37], v[16:17] op_sel:[1,1] op_sel_hi:[1,0] neg_lo:[0,1]\n"
    "v_pk_fma_f32 v[36:37], v[36:37], v[16:17], v[40:41] op_sel_hi:[0,1,1]\n"
    "ds_write_b64 v2, v[44:45] offset:15232\n"
    "v_pk_mul_f32 v[42:43], v[38:39], v[16:17] op_sel:[1,1] op_sel_hi:[1,0] neg_lo:[0,1]\n"
    "v_pk_fma_f32 v[38:39], v[38:39], v[16:17], v[42:43] op_sel_hi:[0,1,1]\n"
    "ds_write_b64 v2, v[46:47] offset:23936\n"
    "v_cvt_pk_f16_f32 v48, v36, v38\n"
    "v_cvt_pk_f16_f32 v50, v37, v39\n"
    "v_pk_mul_f32 v[40:41], v[36:37], v[16:17] op_sel:[1,1] op_sel_hi:[1,0] neg_lo:[0,1]\n"
    "v_pk_fma_f32 v[36:37], v[36:37], v[16:17], v[40:41] op_sel_hi:[0,1,1]\n"
    "v_pk_mul_f32 v[42:43], v[38:39], v[16:17] op_sel:[1,1] op_sel_hi:[1,0] neg_lo:[0,1]\n"
    "v_pk_fma_f32 v[38:39], v[38:39], v[16:17], v[42:43] op_sel_hi:[0,1,1]\n"
    "v_cvt_pk_f16_f32 v49, v36, v38\n"
    "v_cvt_pk_f16_f32 v51, v37, v39\n"
    "v_pk_mul_f32 v[40:41], v[36:37], v[16:17] op_sel:[1,1] op_sel_hi:[1,0] neg_lo:[0,1]\n"
    "v_pk_fma_f32 v[36:37], v[36:37], v[16:17], v[40:41] op_sel_hi:[0,1,1]\n"
    "ds_write_b64 v2, v[48:49] offset:17408\n"
    "v_pk_mul_f32 v[42:43], v[38:39], v[16:17] op_sel:[1,1] op_sel_hi:[1,0] neg_lo:[0,1]\n"
    "v_pk_fma_f32 v[38:39], v[38:39], v[16:17], v[42:43] op_sel_hi:[0,1,1]\n"
    "ds_write_b64 v2, v[50:51] offset:26112\n"
    "v_cvt_pk_f16_f32 v44, v36, v38\n"
    "v_cvt_pk_f16_f32 v46, v37, v39\n"
    "v_pk_mul_f32 v[40:41], v[36:37], v[16:17] op_sel:[1,1] op_sel_hi:[1,0] neg_lo:[0,1]\n"
    "v_pk_fma_f32 v[36:37], v[36:37], v[16:17], v[40:41] op_sel_hi:[0,1,1]\n"
    "v_pk_mul_f32 v[42:43], v[38:39], v[16:17] op_sel:[1,1] op_sel_hi:[1,0] neg_lo:[0,1]\n"
    "v_pk_fma_f32 v[38:39], v[38:39], v[16:17], v[42:43] op_sel_hi:[0,1,1]\n"
    "v_cvt_pk_f16_f32 v45, v36, v38\n"
    "v_cvt_pk_f16_f32 v47, v37, v39\n"
    "v_pk_mul_f32 v[40:41], v[36:37], v[16:17] op_sel:[1,1] op_sel_hi:[1,0] neg_lo:[0,1]\n"
    "v_pk_fma_f32 v[36:37], v[36:37], v[16:17], v[40:41] op_sel_hi:[0,1,1]\n"
    "ds_write_b64 v2, v[44:45] offset:18496\n"
    "v_pk_mul_f32 v[42:43], v[38:39], v[16:17] op_sel:[1,1] op_sel_hi:[1,0] neg_lo:[0,1]\n"
    "v_pk_fma_f32 v[38:39], v[38:39], v[16:17], v[42:43] op_sel_hi:[0,1,1]\n"
    "ds_write_b64 v2, v[46:47] offset:27200\n"
    "v_cvt_pk_f16_f32 v48, v36, v38\n"
    "v_cvt_pk_f16_f32 v50, v37, v39\n"
    "v_pk_mul_f32 v[40:41], v[36:37], v[16:17] op_sel:[1,1] op_sel_hi:[1,0] neg_lo:[0,1]\n"
    "v_pk_fma_f32 v[36:37], v[36:37], v[16:17], v[40:41] op_sel_hi:[0,1,1]\n"
    "v_pk_mul_f32 v[42:43], v[38:39], v[16:17] op_sel:[1,1] op_sel_hi:[1,0] neg_lo:[0,1]\n"
    "v_pk_fma_f32 v[38:39], v[38:39], v[16:17], v[42:43] op_sel_hi:[0,1,1]\n"
    "v_cvt_pk_f16_f32 v49, v36, v38\n"
    "v_cvt_pk_f16_f32 v51, v37, v39\n"
    "v_pk_mul_f32 v[40:41], v[36:37], v[16:17] op_sel:[1,1] op_sel_hi:[1,0] neg_lo:[0,1]\n"
    "v_pk_fma_f32 v[36:37], v[36:37], v[16:17], v[40:41] op_sel_hi:[0,1,1]\n"
    "ds_write_b64 v2, v[48:49] offset:19584\n"
    "v_pk_mul_f32 v[42:43], v[38:39], v[16:17] op_sel:[1,1] op_sel_hi:[1,0] neg_lo:[0,1]\n"
    "v_pk_fma_f32 v[38:39], v[38:39], v[16:17], v[42:43] op_sel_hi:[0,1,1]\n"
    "ds_write_b64 v2, v[50:51] offset:28288\n"
    "v_cvt_pk_f16_f32 v44, v36, v38\n"
    "v_cvt_pk_f16_f32 v46, v37, v39\n"
    "v_pk_mul_f32 v[40:41], v[36:37], v[16:17] op_sel:[1,1] op_sel_hi:[1,0] neg_lo:[0,1]\n"
    "v_pk_fma_f32 v[36:37], v[36:37], v[16:17], v[40:41] op_sel_hi:[0,1,1]\n"
    "v_pk_mul_f32 v[42:43], v[38:39], v[16:17] op_sel:[1,1] op_sel_hi:[1,0] neg_lo:[0,1]\n"
    "v_pk_fma_f32 v[38:39], v[38:39], v[16:17], v[42:43] op_sel_hi:[0,1,1]\n"
    "v_cvt_pk_f16_f32 v45, v36, v38\n"
    "v_cvt_pk_f16_f32 v47, v37, v39\n"
    "v_pk_mul_f32 v[40:41], v[36:37], v[16:17] op_sel:[1,1] op_sel_hi:[1,0] neg_lo:[0,1]\n"
    "v_pk_fma_f32 v[36:37], v[36:37], v[16:17], v[40:41] op_sel_hi:[0,1,1]\n"
    "ds_write_b64 v2, v[44:45] offset:20672\n"
    "v_pk_mul_f32 v[42:43], v[38:39], v[16:17] op_sel:[1,1] op_sel_hi:[1,0] neg_lo:[0,1]\n"
    "v_pk_fma_f32 v[38:39], v[38:39], v[16:17], v[42:43] op_sel_hi:[0,1,1]\n"
    "ds_write_b64 v2, v[46:47] offset:29376\n"
    "v_cvt_pk_f16_f32 v48, v36, v38\n"
    "v_cvt_pk_f16_f32 v50, v37, v39\n"
    "v_pk_mul_f32 v[40:41], v[36:37], v[16:17] op_sel:[1,1] op_sel_hi:[1,0] neg_lo:[0,1]\n"
    "v_pk_fma_f32 v[36:37], v[36:37], v[16:17], v[40:41] op_sel_hi:[0,1,1]\n"
    "v_pk_mul_f32 v[42:43], v[38:39], v[16:17] op_sel:[1,1] op_sel_hi:[1,0] neg_lo:[0,1]\n"
    "v_pk_fma_f32 v[38:39], v[38:39], v[16:17], v[42:43] op_sel_hi:[0,1,1]\n"
    "v_cvt_pk_f16_f32 v49, v36, v38\n"
    "v_cvt_pk_f16_f32 v51, v37, v39\n"
    "ds_write_b64 v2, v[48:49] offset:16320\n"
    "ds_write_b64 v2, v[50:51] offset:25024\n"
    "s_mov_b64 exec, 1\n"
    "ds_add_u32 v13, v15 offset:28\n"
    "s_mov_b64 exec, -1\n"
    "s_cmpk_ge_u32 s37, 14\n"
    "s_cbranch_scc1 AUX3\n"
    "v_mov_b32 v14, 147968\n"
    "s_branch STAGE\n"
    "KST:\n"
    "s_mov_b32 s42, s47\n"
    "s_cmp_eq_u32 s48, 0\n"
    "s_cbranch_scc1 NEAR\n"
    "s_cmpk_eq_u32 s37, 6\n"
    "s_cbranch_scc1 HELP\n"
    "s_branch FAR\n"
    "KST2:\n"
    "s_branch E_4\n"
    "R_3:\n"
    "s_sleep 1\n"
    "s_sub_u32 s39, s39, 1\n"
    "s_cbranch_scc1 ABORT\n"
    "E_4:\n"
    "ds_read_b32 v112, v14 offset:24\n"
    "ds_read_b128 v[16:19], v124 offset:0\n"
    "ds_read_b128 v[20:23], v124 offset:1024\n"
    "ds_read_b128 v[24:27], v124 offset:2048\n"
    "ds_read_b128 v[28:31], v124 offset:3072\n"
    "s_waitcnt lgkmcnt(0)\n"
    "v_cmp_ne_u32 vcc, 1, v112\n"
    "s_cbranch_vccnz R_3\n"
    "v_add_u32 v28, s49, v28\n"
    "v_add_u32 v29, s49, v29\n"
    "s_mov_b32 s46, 0x01000100\n"
    "s_mov_b64 s[50:51], 0x1ffff\n"
    "s_branch E_6\n"
    "R_5:\n"
    "s_sleep 1\n"
    "s_sub_u32 s39, s39, 1\n"
    "s_cbranch_scc1 ABORT\n"
    "E_6:\n"
    "ds_read_b32 v112, v125 offset:12\n"
    "s_waitcnt lgkmcnt(0)\n"
    "v_cmp_ne_u32 vcc, 2, v112\n"
    "s_cbranch_vccnz R_5\n"
    "ds_read_b64_tr_b16 v[32:33], v122 offset:0\n"
    "ds_read_b64_tr_b16 v[34:35], v122 offset:6528\n"
    "ds_read_b64_tr_b16 v[36:37], v122 offset:4352\n"
    "ds_read_b64_tr_b16 v[38:39], v122 offset:10880\n"
    "ds_read_b64_tr_b16 v[40:41], v122 offset:128\n"
    "ds_read_b64_tr_b16 v[42:43], v122 offset:6656\n"
    "ds_read_b64_tr_b16 v[44:45], v122 offset:4480\n"
    "ds_read_b64_tr_b16 v[46:47], v122 offset:11008\n"
    "ds_read_b64_tr_b16 v[48:49], v122 offset:256\n"
    "ds_read_b64_tr_b16 v[50:51], v122 offset:6784\n"
    "ds_read_b64_tr_b16 v[52:53], v122 offset:4608\n"
    "ds_read_b64_tr_b16 v[54:55], v122 offset:11136\n"
    "ds_read_b64_tr_b16 v[56:57], v122 offset:384\n"
    "ds_read_b64_tr_b16 v[58:59], v122 offset:6912\n"
    "ds_read_b64_tr_b16 v[60:61], v122 offset:4736\n"
    "ds_read_b64_tr_b16 v[62:63], v122 offset:11264\n"
    "ds_read_b64_tr_b16 v[64:65], v122 offset:512\n"
    "ds_read_b64_tr_b16 v[66:67], v122 offset:7040\n"
    "ds_read_b64_tr_b16 v[68:69], v122 offset:4864\n"
    "ds_read_b64_tr_b16 v[70:71], v122 offset:11392\n"
    "ds_read_b64_tr_b16 v[72:73], v122 offset:640\n"
    "ds_read_b64_tr_b16 v[74:75], v122 offset:7168\n"
    "ds_read_b64_tr_b16 v[76:77], v122 offset:4992\n"
    "ds_read_b64_tr_b16 v[78:79], v122 offset:11520\n"
    "ds_read_b64_tr_b16 v[80:81], v122 offset:768\n"
    "ds_read_b64_tr_b16 v[82:83], v122 offset:7296\n"
    "ds_read_b64_tr_b16 v[84:85], v122 offset:5120\n"
    "ds_read_b64_tr_b16 v[86:87], v122 offset:11648\n"
    "ds_read_b64_tr_b16 v[88:89], v122 offset:896\n"
    "ds_read_b64_tr_b16 v[90:91], v122 offset:7424\n"
    "ds_read_b64_tr_b16 v[92:93], v122 offset:5248\n"
    "ds_read_b64_tr_b16 v[94:95], v122 offset:11776\n"
    "ds_read_b32 v12, v126\n"
    "s_branch E_8\n"
    "R_7:\n"
    "s_sleep 1\n"
    "s_sub_u32 s39, s39, 1\n"
    "s_cbranch_scc1 ABORT\n"
    "E_8:\n"
    "ds_read_b32 v112, v14 offset:28\n"
    "ds_read_b64 v[114:115], v14 offset:32\n"
    "s_waitcnt lgkmcnt(0)\n"
    "v_add3_u32 v112, v112, v114, v115\n"
    "v_cmp_ne_u32 vcc, 6, v112\n"
    "s_cbranch_vccnz R_7\n"
    "s_setprio 1\n"
    "ds_read_b64_tr_b16 v[96:97], v123 offset:13056\n"
    "ds_read_b64_tr_b16 v[98:99], v123 offset:21760\n"
    "ds_read_b64_tr_b16 v[100:101], v123 offset:13184\n"
    "ds_read_b64_tr_b16 v[102:103], v123 offset:21888\n"
    "ds_read_b64_tr_b16 v[104:105], v123 offset:13312\n"
    "ds_read_b64_tr_b16 v[106:107], v123 offset:22016\n"
    "ds_read_b64_tr_b16 v[108:109], v123 offset:13440\n"
    "ds_read_b64_tr_b16 v[110:111], v123 offset:22144\n"
    "s_waitcnt lgkmcnt(6)\n"
    "v_mfma_f32_16x16x32_f16 v[112:115], v[32:35], v[96:99], 0\n"
    "v_mfma_f32_16x16x32_f16 v[116:119], v[36:39], v[96:99], 0\n"
    "ds_read_b64_tr_b16 v[96:97], v123 offset:13568\n"
    "ds_read_b64_tr_b16 v[98:99], v123 offset:22272\n"
    "s_waitcnt lgkmcnt(6)\n"
    "v_mfma_f32_16x16x32_f16 v[112:115], v[40:43], v[100:103], v[112:115]\n"
    "v_mfma_f32_16x16x32_f16 v[116:119], v[44:47], v[100:103], v[116:119]\n"
    "ds_read_b64_tr_b16 v[100:101], v123 offset:13696\n"
    "ds_read_b64_tr_b16 v[102:103], v123 offset:22400\n"
    "s_waitcnt lgkmcnt(6)\n"
    "v_mfma_f32_16x16x32_f16 v[112:115], v[48:51], v[104:107], v[112:115]\n"
    "v_mfma_f32_16x16x32_f16 v[116:119], v[52:55], v[104:107], v[116:119]\n"
    "ds_read_b64_tr_b16 v[104:105], v123 offset:13824\n"
    "ds_read_b64_tr_b16 v[106:107], v123 offset:22528\n"
    "s_waitcnt lgkmcnt(6)\n"
    "v_mfma_f32_16x16x32_f16 v[112:115], v[56:59], v[108:111], v[112:115]\n"
    "v_mfma_f32_16x16x32_f16 v[116:119], v[60:63], v[108:111], v[116:119]\n"
    "ds_read_b64_tr_b16 v[108:109], v123 offset:13952\n"
    "ds_read_b64_tr_b16 v[110:111], v123 offset:22656\n"
    "s_waitcnt lgkmcnt(6)\n"
    "v_mfma_f32_16x16x32_f16 v[112:115], v[64:67], v[96:99], v[112:115]\n"
    "v_mfma_f32_16x16x32_f16 v[116:119], v[68:71], v[96:99], v[116:119]\n"
    "s_waitcnt lgkmcnt(4)\n"
    "v_mfma_f32_16x16x32_f16 v[112:115], v[72:75], v[100:103], v[112:115]\n"
    "v_mfma_f32_16x16x32_f16 v[116:119], v[76:79], v[100:103], v[116:119]\n"
    "s_waitcnt lgkmcnt(2)\n"
    "v_mfma_f32_16x16x32_f16 v[112:115], v[80:83], v[104:107], v[112:115]\n"
    "v_mfma_f32_16x16x32_f16 v[116:119], v[84:87], v[104:107], v[116:119]\n"
    "s_waitcnt lgkmcnt(0)\n"
    "v_mfma_f32_16x16x32_f16 v[112:115], v[88:91], v[108:111], v[112:115]\n"
    "v_mfma_f32_16x16x32_f16 v[116:119], v[92:95], v[108:111], v[116:119]\n"
    "s_cmp_eq_u32 s47, 1\n"
    "s_cbranch_scc1 KT1\n"
    "s_cmp_eq_u32 s47, 2\n"
    "s_cbranch_scc1 KT2\n"
    "KT0:\n"
    "s_nop 2\n"
    "v_pk_mul_f32 v[112:113], v[112:113], v[12:13] op_sel_hi:[1,0]\n"
    "v_pk_mul_f32 v[114:115], v[114:115], v[12:13] op_sel_hi:[1,0]\n"
    "v_cvt_pk_f16_f32 v112, v112, v113\n"
    "v_cvt_pk_f16_f32 v113, v114, v115\n"
    "v_pk_mul_f32 v[116:117], v[116:117], v[12:13] op_sel_hi:[1,0]\n"
    "v_perm_b32 v114, v113, v112, v17\n"
    "v_perm_b32 v115, v113, v112, v18\n"
    "v_perm_b32 v32, v113, v112, v30\n"
    "v_perm_b32 v33, v113, v113, s46\n"
    "v_perm_b32 v112, v113, v112, v19\n"
    "s_mov_b32 exec_hi, s50\n"
    "ds_write_b64 v16, v[114:115] offset:0\n"
    "s_mov_b32 exec_lo, 0\n"
    "ds_write2_b32 v28, v114, v32 offset1:1\n"
    "ds_write2_b32 v29, v33, v113 offset1:1\n"
    "s_mov_b64 exec, -1\n"
    "v_pk_mul_f32 v[118:119], v[118:119], v[12:13] op_sel_hi:[1,0]\n"
    "ds_write_b16 v22, v113 offset:0\n"
    "ds_write_b16_d16_hi v23, v113 offset:0\n"
    "v_cvt_pk_f16_f32 v116, v116, v117\n"
    "ds_write_b16 v20, v112 offset:0\n"
    "ds_write_b16_d16_hi v21, v112 offset:0\n"
    "v_cvt_pk_f16_f32 v117, v118, v119\n"
    "s_mov_b64 exec, s[50:51]\n"
    "ds_write_b16 v24, v116 offset:0\n"
    "ds_write_b16_d16_hi v25, v116 offset:0\n"
    "ds_write_b16 v26, v117 offset:0\n"
    "ds_write_b16_d16_hi v27, v117 offset:0\n"
    "s_mov_b64 exec, 1\n"
    "ds_add_u32 v14, v15 offset:224\n"
    "s_mov_b64 exec, -1\n"
    "s_branch KT3\n"
    "KT1:\n"
    "s_nop 4\n"
    "v_pk_mul_f32 v[112:113], v[112:113], v[12:13] op_sel_hi:[1,0]\n"
    "v_pk_mul_f32 v[114:115], v[114:115], v[12:13] op_sel_hi:[1,0]\n"
    "v_cvt_pk_f16_f32 v112, v112, v113\n"
    "v_cvt_pk_f16_f32 v113, v114, v115\n"
    "v_pk_mul_f32 v[116:117], v[116:117], v[12:13] op_sel_hi:[1,0]\n"
    "v_perm_b32 v114, v113, v112, v17\n"
    "v_perm_b32 v115, v113, v112, v18\n"
    "v_perm_b32 v32, v113, v112, v30\n"
    "v_perm_b32 v33, v113, v113, s46\n"
    "v_perm_b32 v112, v113, v112, v19\n"
    "s_mov_b32 exec_hi, s50\n"
    "ds_write_b64 v16, v[114:115] offset:3328\n"
    "s_mov_b32 exec_lo, 0\n"
    "ds_write2_b32 v28, v114, v32 offset1:1\n"
    "ds_write2_b32 v29, v33, v113 offset1:1\n"
    "s_mov_b64 exec, -1\n"
    "v_pk_mul_f32 v[118:119], v[118:119], v[12:13] op_sel_hi:[1,0]\n"
    "ds_write_b16 v22, v113 offset:3328\n"
    "ds_write_b16_d16_hi v23, v113 offset:3328\n"
    "v_cvt_pk_f16_f32 v116, v116, v117\n"
    "ds_write_b16 v20, v112 offset:3328\n"
    "ds_write_b16_d16_hi v21, v112 offset:3328\n"
    "v_cvt_pk_f16_f32 v117, v118, v119\n"
    "s_mov_b64 exec, s[50:51]\n"
    "ds_write_b16 v24, v116 offset:3328\n"
    "ds_write_b16_d16_hi v25, v116 offset:3328\n"
    "ds_write_b16 v26, v117 offset:3328\n"
    "ds_write_b16_d16_hi v27, v117 offset:3328\n"
    "s_mov_b64 exec, 1\n"
    "ds_add_u32 v14, v15 offset:224\n"
    "s_mov_b64 exec, -1\n"
    "s_branch KT3\n"
    "KT2:\n"
    "s_nop 2\n"
    "v_pk_mul_f32 v[112:113], v[112:113], v[12:13] op_sel_hi:[1,0]\n"
    "v_pk_mul_f32 v[114:115], v[114:115], v[12:13] op_sel_hi:[1,0]\n"
    "v_cvt_pk_f16_f32 v112, v112, v113\n"
    "v_cvt_pk_f16_f32 v113, v114, v115\n"
    "v_pk_mul_f32 v[116:117], v[116:117], v[12:13] op_sel_hi:[1,0]\n"
    "v_perm_b32 v114, v113, v112, v17\n"
    "v_perm_b32 v115, v113, v112, v18\n"
    "v_perm_b32 v32, v113, v112, v30\n"
    "v_perm_b32 v33, v113, v113, s46\n"
    "v_perm_b32 v112, v113, v112, v19\n"
    "s_mov_b32 exec_hi, s50\n"
    "ds_write_b64 v16, v[114:115] offset:6656\n"
    "s_mov_b32 exec_lo, 0\n"
    "ds_write2_b32 v28, v114, v32 offset1:1\n"
    "ds_write2_b32 v29, v33, v113 offset1:1\n"
    "s_mov_b64 exec, -1\n"
    "v_pk_mul_f32 v[118:119], v[118:119], v[12:13] op_sel_hi:[1,0]\n"
    "ds_write_b16 v22, v113 offset:6656\n"
    "ds_write_b16_d16_hi v23, v113 offset:6656\n"
    "v_cvt_pk_f16_f32 v116, v116, v117\n"
    "ds_write_b16 v20, v112 offset:6656\n"
    "ds_write_b16_d16_hi v21, v112 offset:6656\n"
    "v_cvt_pk_f16_f32 v117, v118, v119\n"
    "s_mov_b64 exec, s[50:51]\n"
    "ds_write_b16 v24, v116 offset:6656\n"
    "ds_write_b16_d16_hi v25, v116 offset:6656\n"
    "ds_write_b16 v26, v117 offset:6656\n"
    "ds_write_b16_d16_hi v27, v117 offset:6656\n"
    "s_mov_b64 exec, 1\n"
    "ds_add_u32 v14, v15 offset:224\n"
    "s_mov_b64 exec, -1\n"
    "KT3:\n"
    "s_cmp_eq_u32 s48, 0\n"
    "s_cbranch_scc1 NEAR1\n"
    "s_cmpk_eq_u32 s37, 6\n"
    "s_cbranch_scc1 HELP1\n"
    "s_branch FAR1\n"
    "CONST:\n"
    "v_mov_b32 v14, 147968\n"
    "s_branch E_10\n"
    "R_9:\n"
    "s_sleep 1\n"
    "s_sub_u32 s39, s39, 1\n"
    "s_cbranch_scc1 ABORT\n"
    "E_10:\n"
    "ds_read_b32 v4, v14 offset:12\n"
    "ds_read_b64 v[6:7], v14 offset:16\n"
    "s_waitcnt lgkmcnt(0)\n"
    "v_add3_u32 v4, v4, v6, v7\n"
    "v_cmp_ne_u32 vcc, 6, v4\n"
    "s_cbranch_vccnz R_9\n"
    "v_mov_b32 v4, 0\n"
    "v_mov_b32 v6, 0\n"
    "s_setprio 3\n"
    "v_and_b32 v2, 15, v0\n"
    "v_bfe_u32 v3, v0, 4, 2\n"
    "v_mul_u32_u24 v3, 4608, v3\n"
    "s_movk_i32 s43, 13824\n"
    "v_mad_u32_u24 v3, v2, s43, v3\n"
    "s_mul_i32 s42, s38, 24\n"
    "s_add_u32 s42, s42, 10519552\n"
    "v_lshlrev_b32 v2, 1, v2\n"
    "v_add_u32 v2, 148352, v2\n"
    "s_mov_b32 s43, 0x42000000\n"
    "s_mov_b32 s46, 0x01ff01ff\n"
    "s_movk_i32 s47, 0x01ff\n"
    "s_mov_b32 s48, 0\n"
    "s_movk_i32 s49, 0x0111\n"
    "v_mov_b32 v8, 0\n"
    "s_mov_b64 exec, s[46:47]\n"
    "U_11:\n"
    "s_memrealtime s[98:99]\n"
    "s_waitcnt lgkmcnt(0)\n"
    "s_sub_u32 s98, s98, s96\n"
    "s_cmpk_lt_u32 s98, 205\n"
    "s_cbranch_scc1 U_11\n"
    "s_branch E_13\n"
    "R_12:\n"
    "s_sleep 2\n"
    "s_sub_u32 s39, s39, 1\n"
    "s_cbranch_scc1 ABORT\n"
    "E_13:\n"
    "buffer_load_dwordx4 v[4:7], v3, s[20:23], s42 offen sc1\n"
    "buffer_load_dwordx2 v[8:9], v3, s[20:23], s42 offen offset:16 sc1\n"
    "s_waitcnt vmcnt(0)\n"
    "v_bitop3_b32 v10, v5, v7, s36 bitop3:0x7e\n"
    "v_bitop3_b32 v10, v10, v9, s36 bitop3:0xf6\n"
    "v_cmp_ne_u32 vcc, 0, v10\n"
    "s_cbranch_vccnz R_12\n"
    "s_mov_b64 exec, -1\n"
    "v_add_f32_dpp v4, v4, v4 row_ror:8 row_mask:0xf bank_mask:0xf\n"
    "v_add_f32_dpp v6, v6, v6 row_ror:8 row_mask:0xf bank_mask:0xf\n"
    "v_add_f32_dpp v8, v8, v8 row_ror:8 row_mask:0xf bank_mask:0xf\n"
    "v_add_f32_dpp v4, v4, v4 row_ror:4 row_mask:0xf bank_mask:0xf\n"
    "v_add_f32_dpp v6, v6, v6 row_ror:4 row_mask:0xf bank_mask:0xf\n"
    "v_add_f32_dpp v8, v8, v8 row_ror:4 row_mask:0xf bank_mask:0xf\n"
    "s_nop 0\n"
    "v_mov_b32_dpp v4, v6 quad_perm:[0,1,2,3] row_mask:0xf bank_mask:0x2\n"
    "s_nop 1\n"
    "v_mov_b32_dpp v4, v8 quad_perm:[0,1,2,3] row_mask:0xf bank_mask:0x4\n"
    "s_nop 1\n"
    "v_add_f32_dpp v4, v4, v4 quad_perm:[1,0,3,2] row_mask:0xf bank_mask:0xf\n"
    "s_nop 1\n"
    "v_add_f32_dpp v4, v4, v4 quad_perm:[2,3,0,1] row_mask:0xf bank_mask:0xf\n"
    "v_cvt_pk_bf16_f32 v5, 0, v4\n"
    "v_add_f32 v4, 1.0, v5\n"
    "s_mov_b64 exec, s[48:49]\n"
    "ds_write_b32 v2, v5 offset:36\n"
    "s_mov_b64 exec, -1\n"
    "v_cvt_pk_bf16_f32 v4, 0, v4\n"
    "v_mov_b32 v9, v4\n"
    "s_nop 1\n"
    "v_permlane16_swap_b32 v9, v4\n"
    "v_fma_f32 v6, abs(v4), s43, abs(v5)\n"
    "v_frexp_exp_i32_f32 v6, v6\n"
    "v_sub_u32 v7, 11, v6\n"
    "v_ldexp_f32 v10, v4, v7\n"
    "v_ldexp_f32 v11, v5, v7\n"
    "v_subrev_u32 v8, 11, v6\n"
    "v_ldexp_f32 v8, 1.0, v8\n"
    "s_mov_b64 exec, 0x0111\n"
    "ds_write_b64 v2, v[10:11]\n"
    "ds_write_b32 v2, v8 offset:32\n"
    "ds_write_b32 v14, v15 offset:40\n"
    "s_mov_b64 exec, -1\n"
    "s_branch HELP\n"
    "AUX1:\n"
    "s_mov_b64 exec, -1\n"
    "s_waitcnt lgkmcnt(0)\n"
    "s_load_dword s36, s[20:21], 0\n"
    "s_barrier\n"
    "v_lshlrev_b32 v1, 4, v0\n"
    "v_and_b32 v8, 63, v0\n"
    "v_mov_b32 v15, 1\n"
    "v_mov_b32 v14, 147968\n"
    "s_cmpk_gt_u32 s37, 13\n"
    "s_cbranch_scc1 AUXB\n"
    "s_getpc_b64 s[44:45]\n"
    "s_add_u32 s44, s44, mega_tab@rel32@lo+4\n"
    "s_addc_u32 s45, s45, mega_tab@rel32@hi+12\n"
    "s_mov_b32 m0, 107520\n"
    "v_lshlrev_b32 v2, 4, v8\n"
    "v_add_u32 v3, 6144, v2\n"
    "global_load_lds_dwordx4 v3, s[44:45] offset:0\n"
    "global_load_lds_dwordx4 v3, s[44:45] offset:1024\n"
    "global_load_lds_dwordx4 v3, s[44:45] offset:2048\n"
    "global_load_lds_dwordx4 v3, s[44:45] offset:3072\n"
    "s_mov_b32 m0, 111616\n"
    "v_add_u32 v3, 4096, v3\n"
    "global_load_lds_dwordx4 v3, s[44:45] offset:0\n"
    "global_load_lds_dwordx4 v3, s[44:45] offset:1024\n"
    "global_load_lds_dwordx4 v3, s[44:45] offset:2048\n"
    "global_load_lds_dwordx4 v3, s[44:45] offset:3072\n"
    "s_mov_b32 m0, 101376\n"
    "s_nop 0\n"
    "global_load_lds_dwordx4 v2, s[44:45] offset:0\n"
    "global_load_lds_dwordx4 v2, s[44:45] offset:1024\n"
    "global_load_lds_dwordx4 v2, s[44:45] offset:2048\n"
    "global_load_lds_dwordx4 v2, s[44:45] offset:3072\n"
    "s_mov_b32 m0, 105472\n"
    "v_add_u32 v2, 4096, v2\n"
    "global_load_lds_dwordx4 v2, s[44:45] offset:0\n"
    "global_load_lds_dwordx4 v2, s[44:45] offset:1024\n"
    "v_mov_b32 v4, 0\n"
    "v_mov_b32 v5, 0\n"
    "v_mov_b32 v6, 0\n"
    "v_mov_b32 v7, 0\n"
    "v_add_u32 v2, 78080, v1\n"
    "ds_write_b128 v2, v[4:7] offset:0\n"
    "ds_write_b128 v2, v[4:7] offset:1024\n"
    "ds_write_b128 v2, v[4:7] offset:2048\n"
    "ds_write_b128 v2, v[4:7] offset:3072\n"
    "ds_write_b128 v2, v[4:7] offset:4096\n"
    "ds_write_b128 v2, v[4:7] offset:5120\n"
    "ds_write_b128 v2, v[4:7] offset:6144\n"
    "ds_write_b128 v2, v[4:7] offset:7168\n"
    "ds_write_b128 v2, v[4:7] offset:8192\n"
    "v_min_u32 v2, 92144, v2\n"
    "ds_write_b128 v2, v[4:7] offset:9216\n"
    "v_min_u32 v2, 29, v8\n"
    "v_lshlrev_b32 v2, 4, v2\n"
    "v_add_u32 v2, 120832, v2\n"
    "ds_write_b128 v2, v[4:7] offset:0\n"
    "ds_write_b128 v2, v[4:7] offset:4576\n"
    "ds_write_b128 v2, v[4:7] offset:9152\n"
    "s_branch AUX2\n"
    "AUXB:\n"
    "s_lshl_b32 s42, s37, 9\n"
    "s_add_u32 s42, s42, 53760\n"
    "v_lshl_add_u32 v2, v8, 3, s42\n"
    "v_add_u32 v3, 116128, v1\n"
    "s_lshl_b32 s42, s37, 2\n"
    "s_add_u32 s42, s42, 147912\n"
    "v_mov_b32 v12, s42\n"
    "v_mov_b32 v13, 147976\n"
    "s_branch E_15\n"
    "R_14:\n"
    "s_sleep 1\n"
    "s_sub_u32 s39, s39, 1\n"
    "s_cbranch_scc1 ABORT\n"
    "E_15:\n"
    "ds_read_b32 v20, v12 offset:4\n"
    "ds_read_b128 v[16:19], v3\n"
    "s_waitcnt lgkmcnt(0)\n"
    "v_cmp_ne_u32 vcc, 1, v20\n"
    "s_cbranch_vccnz R_14\n"
    "s_add_u32 s36, s36, 1\n"
    "s_setprio 1\n"
    "s_branch BSIDE\n"
    "AUX2:\n"
    "s_mov_b64 exec, -1\n"
    "s_waitcnt vmcnt(0) lgkmcnt(0)\n"
    "s_add_u32 s36, s36, 1\n"
    "s_mov_b64 exec, 1\n"
    "ds_add_u32 v14, v15 offset:24\n"
    "s_mov_b64 exec, -1\n"
    "s_mov_b64 exec, 0\n"
    "CH13:\n"
    "s_branch AUX3\n"
    "AUX3:\n"
    "s_mov_b64 exec, -1\n"
    "s_setprio 0\n"
    "s_and_b32 s44, s2, 7\n"
    "s_lshr_b32 s45, s2, 3\n"
    "s_mul_i32 s44, s44, 24\n"
    "s_add_u32 s38, s44, s45\n"
    "v_mov_b32 v14, 147968\n"
    "ds_read_b32 v2, v14 offset:480\n"
    "s_waitcnt lgkmcnt(0)\n"
    "v_readfirstlane_b32 s96, v2\n"
    "STAGE:\n"
    "v_mov_b32 v78, 147968\n"
    "s_lshl_b32 s43, s37, 2\n"
    "s_mov_b32 s44, 0x78650432\n"
    "s_lshr_b32 s54, s44, s43\n"
    "s_and_b32 s54, s54, 15\n"
    "s_add_u32 s55, s54, 7\n"
    "s_cmpk_eq_u32 s37, 3\n"
    "s_cselect_b32 s55, 1, s55\n"
    "s_and_b32 s42, s38, 1\n"
    "s_mul_i32 s42, s42, 6144\n"
    "s_lshr_b32 s43, s38, 1\n"
    "s_lshl_b32 s43, s43, 6\n"
    "s_add_u32 s42, s42, s43\n"
    "s_add_u32 s42, s42, 7341056\n"
    "s_mul_i32 s40, s54, 198656\n"
    "s_add_u32 s40, s40, s42\n"
    "s_mul_i32 s41, s55, 198656\n"
    "s_add_u32 s41, s41, s42\n"
    "v_bfe_u32 v66, v0, 2, 4\n"
    "v_mul_u32_u24 v66, 12416, v66\n"
    "v_and_b32 v67, 3, v0\n"
    "v_lshl_add_u32 v66, v67, 4, v66\n"
    "U_16:\n"
    "s_memrealtime s[98:99]\n"
    "s_waitcnt lgkmcnt(0)\n"
    "s_sub_u32 s98, s98, s96\n"
    "s_cmpk_lt_u32 s98, 215\n"
    "s_cbranch_scc1 U_16\n"
    "buffer_load_dwordx4 v[68:71], v66, s[20:23], s40 offen sc1\n"
    "buffer_load_dwordx4 v[72:75], v66, s[20:23], s41 offen sc1\n"
    "v_and_b32 v67, 63, v0\n"
    "v_lshlrev_b32 v67, 2, v67\n"
    "s_lshl_b32 s43, s54, 8\n"
    "s_sub_u32 s43, 120828, s43\n"
    "v_sub_u32 v86, s43, v67\n"
    "s_lshl_b32 s43, s55, 8\n"
    "s_sub_u32 s43, 120828, s43\n"
    "v_sub_u32 v87, s43, v67\n"
    "v_mov_b32 v76, 148352\n"
    "v_and_b32 v111, 63, v0\n"
    "s_lshr_b32 s50, s2, 3\n"
    "s_and_b32 s51, s2, 7\n"
    "s_cmpk_ge_u32 s2, 128\n"
    "s_cbranch_scc1 PFA\n"
    "s_lshl_b32 s51, s51, 4\n"
    "s_add_u32 s51, s51, s50\n"
    "s_mul_i32 s51, s51, 18432\n"
    "s_mul_i32 s60, s37, 768\n"
    "s_add_u32 s51, s51, s60\n"
    "v_lshl_add_u32 v111, v111, 4, s51\n"
    "s_add_u32 s60, s60, 53760\n"
    "s_mov_b64 s[58:59], s[4:5]\n"
    "s_branch PFC\n"
    "PFA:\n"
    "s_sub_u32 s50, s50, 16\n"
    "s_cmpk_lt_u32 s37, 4\n"
    "s_cbranch_scc1 PFB\n"
    "s_lshl2_add_u32 s50, s50, s37\n"
    "s_sub_u32 s50, s50, 12\n"
    "s_mul_i32 s50, s50, 54\n"
    "s_mul_i32 s60, s51, 663552\n"
    "s_lshr_b32 s44, s28, 7\n"
    "s_add_u32 s44, s44, s2\n"
    "s_sub_u32 s44, 3, s44\n"
    "s_and_b32 s44, s44, 7\n"
    "s_add_u32 s44, s44, 1\n"
    "s_add_u32 s44, s44, 7\n"
    "s_mul_i32 s45, s44, 57\n"
    "s_lshr_b32 s45, s45, 9\n"
    "s_mul_i32 s45, s45, 9\n"
    "s_sub_u32 s44, s44, s45\n"
    "s_and_b32 s51, s2, 7\n"
    "s_mul_i32 s51, s51, 9\n"
    "s_add_u32 s51, s51, s44\n"
    "s_mul_i32 s44, s44, 576\n"
    "s_add_u32 s50, s50, s44\n"
    "v_min_u32 v111, 53, v111\n"
    "v_add_u32 v111, s50, v111\n"
    "s_branch PFC\n"
    "PFB:\n"
    "s_lshr_b32 s51, s37, 1\n"
    "s_sub_u32 s51, 8, s51\n"
    "s_lshr_b32 s44, s28, 7\n"
    "s_add_u32 s44, s44, s2\n"
    "s_sub_u32 s44, 3, s44\n"
    "s_and_b32 s44, s44, 7\n"
    "s_add_u32 s44, s44, 1\n"
    "s_add_u32 s44, s44, s51\n"
    "s_mul_i32 s45, s44, 57\n"
    "s_lshr_b32 s45, s45, 9\n"
    "s_mul_i32 s45, s45, 9\n"
    "s_sub_u32 s44, s44, s45\n"
    "s_and_b32 s51, s2, 7\n"
    "s_mul_i32 s51, s51, 9\n"
    "s_add_u32 s51, s51, s44\n"
    "s_lshl_b32 s51, s51, 7\n"
    "s_lshl1_add_u32 s44, s50, s37\n"
    "s_mul_i32 s44, s44, 36\n"
    "s_mul_i32 s45, s50, 36\n"
    "s_add_u32 s45, s45, 288\n"
    "s_cmpk_lt_u32 s37, 2\n"
    "s_cselect_b32 s50, s44, s45\n"
    "s_mul_i32 s50, s50, 9216\n"
    "s_add_u32 s51, s51, s50\n"
    "v_min_u32 v111, 35, v111\n"
    "v_mul_u32_u24 v111, 9216, v111\n"
    "v_add_u32 v111, s51, v111\n"
    "PFC:\n"
    "s_mul_i32 s42, s38, 6\n"
    "s_and_b32 s42, s42, -4\n"
    "s_load_dwordx2 s[52:53], s[24:25], s42\n"
    "s_cmpk_eq_u32 s37, 3\n"
    "s_cbranch_scc1 FAR\n"
    "s_branch EPS\n"
    "STG0:\n"
    "s_branch E_18\n"
    "R_17:\n"
    "s_sub_u32 s39, s39, 1\n"
    "s_cbranch_scc1 ABORT\n"
    "s_wqm_b64 exec, vcc\n"
    "buffer_load_dwordx4 v[68:71], v66, s[20:23], s40 offen sc1\n"
    "buffer_load_dwordx4 v[72:75], v66, s[20:23], s41 offen sc1\n"
    "s_mov_b64 exec, -1\n"
    "E_18:\n"
    "s_waitcnt vmcnt(0)\n"
    "v_bitop3_b32 v77, v71, v75, s36 bitop3:0x7e\n"
    "v_cmp_ne_u32 vcc, 0, v77\n"
    "s_cbranch_vccnz R_17\n"
    "s_setprio 3\n"
    "s_branch E_20\n"
    "R_19:\n"
    "s_sub_u32 s39, s39, 1\n"
    "s_cbranch_scc1 ABORT\n"
    "E_20:\n"
    "ds_read_b32 v77, v78 offset:40\n"
    "ds_read_b128 v[80:83], v76\n"
    "ds_read_b64 v[84:85], v76 offset:16\n"
    "s_waitcnt lgkmcnt(0)\n"
    "v_cmp_ne_u32 vcc, 1, v77\n"
    "s_cbranch_vccnz R_19\n"
    "ds_read_b128 v[114:117], v76 offset:32\n"
    "ds_read_b64 v[118:119], v76 offset:48\n"
    "ds_read_b128 v[120:123], v76 offset:64\n"
    "ds_read_b64 v[124:125], v76 offset:80\n"
    "v_lshlrev_b32 v88, 16, v68\n"
    "v_and_b32 v89, 0xffff0000, v68\n"
    "v_lshlrev_b32 v90, 16, v69\n"
    "v_and_b32 v91, 0xffff0000, v69\n"
    "v_lshlrev_b32 v92, 16, v70\n"
    "v_and_b32 v93, 0xffff0000, v70\n"
    "v_pk_mul_f32 v[88:89], v[88:89], v[80:81] op_sel_hi:[1,0]\n"
    "v_pk_mul_f32 v[90:91], v[90:91], v[82:83] op_sel_hi:[1,0]\n"
    "v_pk_mul_f32 v[92:93], v[92:93], v[84:85] op_sel_hi:[1,0]\n"
    "v_cvt_pk_bf16_f32 v88, 0, v88\n"
    "v_cvt_pk_bf16_f32 v89, 0, v89\n"
    "v_cvt_pk_bf16_f32 v90, 0, v90\n"
    "v_cvt_pk_bf16_f32 v91, 0, v91\n"
    "v_cvt_pk_bf16_f32 v92, 0, v92\n"
    "v_cvt_pk_bf16_f32 v93, 0, v93\n"
    "v_pk_add_f32 v[88:89], v[80:81], v[88:89] op_sel:[1,0] op_sel_hi:[1,1]\n"
    "v_pk_add_f32 v[90:91], v[82:83], v[90:91] op_sel:[1,0] op_sel_hi:[1,1]\n"
    "v_pk_add_f32 v[92:93], v[84:85], v[92:93] op_sel:[1,0] op_sel_hi:[1,1]\n"
    "v_cvt_pk_bf16_f32 v88, 0, v88\n"
    "v_cvt_pk_bf16_f32 v89, 0, v89\n"
    "v_cvt_pk_bf16_f32 v90, 0, v90\n"
    "v_cvt_pk_bf16_f32 v91, 0, v91\n"
    "v_cvt_pk_bf16_f32 v92, 0, v92\n"
    "v_cvt_pk_bf16_f32 v93, 0, v93\n"
    "v_cvt_pkrtz_f16_f32 v94, v89, v88\n"
    "ds_write_b32 v86, v94 offset:0\n"
    "v_cvt_pkrtz_f16_f32 v95, v91, v90\n"
    "ds_write_b32 v86, v95 offset:4576\n"
    "v_cvt_pkrtz_f16_f32 v96, v93, v92\n"
    "ds_write_b32 v86, v96 offset:9152\n"
    "v_lshlrev_b32 v88, 16, v72\n"
    "v_and_b32 v89, 0xffff0000, v72\n"
    "v_lshlrev_b32 v90, 16, v73\n"
    "v_and_b32 v91, 0xffff0000, v73\n"
    "v_lshlrev_b32 v92, 16, v74\n"
    "v_and_b32 v93, 0xffff0000, v74\n"
    "v_pk_mul_f32 v[88:89], v[88:89], v[80:81] op_sel_hi:[1,0]\n"
    "v_pk_mul_f32 v[90:91], v[90:91], v[82:83] op_sel_hi:[1,0]\n"
    "v_pk_mul_f32 v[92:93], v[92:93], v[84:85] op_sel_hi:[1,0]\n"
    "v_cvt_pk_bf16_f32 v88, 0, v88\n"
    "v_cvt_pk_bf16_f32 v89, 0, v89\n"
    "v_cvt_pk_bf16_f32 v90, 0, v90\n"
    "v_cvt_pk_bf16_f32 v91, 0, v91\n"
    "v_cvt_pk_bf16_f32 v92, 0, v92\n"
    "v_cvt_pk_bf16_f32 v93, 0, v93\n"
    "v_pk_add_f32 v[88:89], v[80:81], v[88:89] op_sel:[1,0] op_sel_hi:[1,1]\n"
    "v_pk_add_f32 v[90:91], v[82:83], v[90:91] op_sel:[1,0] op_sel_hi:[1,1]\n"
    "v_pk_add_f32 v[92:93], v[84:85], v[92:93] op_sel:[1,0] op_sel_hi:[1,1]\n"
    "v_cvt_pk_bf16_f32 v88, 0, v88\n"
    "v_cvt_pk_bf16_f32 v89, 0, v89\n"
    "v_cvt_pk_bf16_f32 v90, 0, v90\n"
    "v_cvt_pk_bf16_f32 v91, 0, v91\n"
    "v_cvt_pk_bf16_f32 v92, 0, v92\n"
    "v_cvt_pk_bf16_f32 v93, 0, v93\n"
    "v_cvt_pkrtz_f16_f32 v97, v89, v88\n"
    "ds_write_b32 v87, v97 offset:0\n"
    "v_cvt_pkrtz_f16_f32 v98, v91, v90\n"
    "ds_write_b32 v87, v98 offset:4576\n"
    "v_cvt_pkrtz_f16_f32 v99, v93, v92\n"
    "ds_write_b32 v87, v99 offset:9152\n"
    "s_waitcnt lgkmcnt(0)\n"
    "s_barrier\n"
    "s_cmpk_eq_u32 s37, 3\n"
    "s_cbranch_scc1 FAR3\n"
    "s_cmpk_ge_u32 s2, 128\n"
    "s_cbranch_scc1 PFD\n"
    "s_cmpk_ge_u32 s37, 12\n"
    "s_cbranch_scc1 EPI\n"
    "s_mov_b32 m0, s60\n"
    "s_mov_b32 exec_hi, 0xffff\n"
    "global_load_lds_dwordx4 v111, s[58:59] nt\n"
    "s_add_u32 m0, m0, 2320\n"
    "s_add_u32 s58, s58, 2304\n"
    "s_addc_u32 s59, s59, 0\n"
    "global_load_lds_dwordx4 v111, s[58:59] nt\n"
    "s_add_u32 m0, m0, 2320\n"
    "s_add_u32 s58, s58, 2304\n"
    "s_addc_u32 s59, s59, 0\n"
    "global_load_lds_dwordx4 v111, s[58:59] nt\n"
    "s_add_u32 m0, m0, 2320\n"
    "s_add_u32 s58, s58, 2304\n"
    "s_addc_u32 s59, s59, 0\n"
    "global_load_lds_dwordx4 v111, s[58:59] nt\n"
    "s_add_u32 m0, m0, 2320\n"
    "s_add_u32 s58, s58, 2304\n"
    "s_addc_u32 s59, s59, 0\n"
    "global_load_lds_dwordx4 v111, s[58:59] nt\n"
    "s_add_u32 m0, m0, 2320\n"
    "s_add_u32 s58, s58, 2304\n"
    "s_addc_u32 s59, s59, 0\n"
    "global_load_lds_dwordx4 v111, s[58:59] nt\n"
    "s_add_u32 m0, m0, 2320\n"
    "s_add_u32 s58, s58, 2304\n"
    "s_addc_u32 s59, s59, 0\n"
    "global_load_lds_dwordx4 v111, s[58:59] nt\n"
    "s_add_u32 m0, m0, 2320\n"
    "s_add_u32 s58, s58, 2304\n"
    "s_addc_u32 s59, s59, 0\n"
    "global_load_lds_dwordx4 v111, s[58:59] nt\n"
    "s_add_u32 m0, m0, 2320\n"
    "s_add_u32 s58, s58, 2304\n"
    "s_addc_u32 s59, s59, 0\n"
    "s_mov_b64 exec, -1\n"
    "s_branch EPI\n"
    "PFD:\n"
    "s_mov_b32 m0, 148480\n"
    "s_cmpk_ge_u32 s37, 12\n"
    "s_cbranch_scc1 PFE\n"
    "global_load_lds_dword v111, s[28:29]\n"
    "global_load_lds_dword v111, s[30:31]\n"
    "s_branch EPI\n"
    "PFE:\n"
    "v_subrev_u32 v112, 5184, v111\n"
    "v_min_u32 v111, v111, v112\n"
    "v_lshl_add_u32 v112, v111, 7, s60\n"
    "global_load_lds_dword v112, s[32:33]\n"
    "v_add_u32 v111, 1728, v111\n"
    "s_branch EPI\n"
    "FAR:\n"
    "s_lshr_b32 s42, s37, 2\n"
    "v_and_b32 v6, 15, v0\n"
    "v_bfe_u32 v7, v0, 4, 2\n"
    "v_and_b32 v1, 63, v0\n"
    "v_lshlrev_b32 v1, 4, v1\n"
    "v_add_u32 v1, 101376, v1\n"
    "s_mul_i32 s43, s42, 4576\n"
    "s_add_u32 s43, s43, 117216\n"
    "v_lshlrev_b32 v2, 1, v6\n"
    "v_sub_u32 v2, v7, v2\n"
    "v_lshl_add_u32 v2, v2, 4, s43\n"
    "s_mul_i32 s44, s42, 3328\n"
    "s_add_u32 s43, s44, 92596\n"
    "v_lshlrev_b32 v3, 2, v7\n"
    "v_sub_u32 v3, v6, v3\n"
    "v_lshl_add_u32 v3, v3, 2, s43\n"
    "s_mul_i32 s43, s42, 7680\n"
    "s_add_u32 s43, s43, 28160\n"
    "v_mul_u32_u24 v4, 160, v7\n"
    "v_lshl_add_u32 v4, v6, 3, v4\n"
    "v_add_u32 v4, s43, v4\n"
    "s_add_u32 s43, s42, 147968\n"
    "v_mov_b32 v5, s43\n"
    "v_mov_b32 v8, 1\n"
    "v_mov_b32 v11, 147968\n"
    "s_cmpk_ge_u32 s37, 6\n"
    "s_cbranch_scc1 KST2\n"
    "s_branch STG0\n"
    "FAR1:\n"
    "s_branch E_22\n"
    "R_21:\n"
    "s_sleep 1\n"
    "s_sub_u32 s39, s39, 1\n"
    "s_cbranch_scc1 ABORT\n"
    "E_22:\n"
    "ds_read_b32 v12, v11 offset:224\n"
    "s_waitcnt lgkmcnt(0)\n"
    "v_cmp_ne_u32 vcc, 6, v12\n"
    "s_cbranch_vccnz R_21\n"
    "FAR3:\n"
    "ds_read_b128 v[12:15], v1 offset:0\n"
    "ds_read_b128 v[16:19], v1 offset:1024\n"
    "ds_read_b128 v[20:23], v1 offset:2048\n"
    "ds_read2_b32 v[24:25], v3 offset0:0 offset1:1\n"
    "ds_read2_b32 v[26:27], v3 offset0:2 offset1:3\n"
    "ds_read2_b32 v[36:37], v3 offset0:64 offset1:65\n"
    "ds_read2_b32 v[38:39], v3 offset0:66 offset1:67\n"
    "ds_read2_b32 v[28:29], v3 offset0:16 offset1:17\n"
    "ds_read2_b32 v[30:31], v3 offset0:18 offset1:19\n"
    "ds_read2_b32 v[40:41], v3 offset0:80 offset1:81\n"
    "ds_read2_b32 v[42:43], v3 offset0:82 offset1:83\n"
    "ds_read2_b32 v[32:33], v3 offset0:32 offset1:33\n"
    "ds_read2_b32 v[34:35], v3 offset0:34 offset1:35\n"
    "ds_read2_b32 v[44:45], v3 offset0:96 offset1:97\n"
    "ds_read2_b32 v[46:47], v3 offset0:98 offset1:99\n"
    "s_cmpk_eq_u32 s37, 3\n"
    "s_cbranch_scc1 FAR2\n"
    "s_barrier\n"
    "FAR2:\n"
    "s_setprio 3\n"
    "ds_read_b128 v[48:51], v2 offset:3584\n"
    "ds_read_b128 v[52:55], v2 offset:3600\n"
    "ds_read_b128 v[56:59], v2 offset:3072\n"
    "ds_read_b128 v[60:63], v2 offset:3088\n"
    "ds_read_b128 v[64:67], v2 offset:2560\n"
    "ds_read_b128 v[68:71], v2 offset:2576\n"
    "s_waitcnt lgkmcnt(4)\n"
    "v_mfma_f32_16x16x32_f16 v[72:75], v[48:51], v[12:15], 0\n"
    "v_mfma_f32_16x16x32_f16 v[76:79], v[48:51], v[16:19], 0\n"
    "v_mfma_f32_16x16x32_f16 v[80:83], v[48:51], v[20:23], 0\n"
    "v_mfma_f32_16x16x32_f16 v[84:87], v[52:55], v[20:23], 0\n"
    "s_nop 5\n"
    "v_cvt_pk_f16_f32 v88, v75, v79\n"
    "v_cvt_pk_f16_f32 v89, v74, v78\n"
    "v_cvt_pk_f16_f32 v90, v73, v77\n"
    "v_cvt_pk_f16_f32 v91, v72, v76\n"
    "v_cvt_pk_f16_f32 v92, v83, v87\n"
    "v_cvt_pk_f16_f32 v93, v82, v86\n"
    "v_cvt_pk_f16_f32 v94, v81, v85\n"
    "v_cvt_pk_f16_f32 v95, v80, v84\n"
    "s_waitcnt lgkmcnt(2)\n"
    "v_mfma_f32_16x16x32_f16 v[72:75], v[56:59], v[12:15], 0\n"
    "ds_read_b128 v[48:51], v2 offset:2048\n"
    "v_mfma_f32_16x16x32_f16 v[76:79], v[56:59], v[16:19], 0\n"
    "ds_read_b128 v[52:55], v2 offset:2064\n"
    "v_mfma_f32_16x16x32_f16 v[80:83], v[56:59], v[20:23], 0\n"
    "v_mfma_f32_16x16x32_f16 v[84:87], v[60:63], v[20:23], 0\n"
    "v_mfma_f32_16x16x32_f16 v[104:107], v[88:91], v[24:27], 0\n"
    "v_mfma_f32_16x16x32_f16 v[104:107], v[92:95], v[36:39], v[104:107]\n"
    "v_mfma_f32_16x16x32_f16 v[108:111], v[88:91], v[28:31], 0\n"
    "s_nop 1\n"
    "v_cvt_pk_f16_f32 v96, v75, v79\n"
    "v_cvt_pk_f16_f32 v97, v74, v78\n"
    "v_mfma_f32_16x16x32_f16 v[108:111], v[92:95], v[40:43], v[108:111]\n"
    "v_cvt_pk_f16_f32 v98, v73, v77\n"
    "v_cvt_pk_f16_f32 v99, v72, v76\n"
    "v_mfma_f32_16x16x32_f16 v[112:115], v[88:91], v[32:35], 0\n"
    "v_cvt_pk_f16_f32 v100, v83, v87\n"
    "v_cvt_pk_f16_f32 v101, v82, v86\n"
    "v_mfma_f32_16x16x32_f16 v[112:115], v[92:95], v[44:47], v[112:115]\n"
    "v_cvt_pk_f16_f32 v102, v81, v85\n"
    "v_cvt_pk_f16_f32 v103, v80, v84\n"
    "s_waitcnt lgkmcnt(2)\n"
    "v_mfma_f32_16x16x32_f16 v[72:75], v[64:67], v[12:15], 0\n"
    "ds_read_b128 v[56:59], v2 offset:1536\n"
    "v_mfma_f32_16x16x32_f16 v[76:79], v[64:67], v[16:19], 0\n"
    "ds_read_b128 v[60:63], v2 offset:1552\n"
    "v_mfma_f32_16x16x32_f16 v[80:83], v[64:67], v[20:23], 0\n"
    "ds_write_b64 v4, v[104:105] offset:2560\n"
    "v_mfma_f32_16x16x32_f16 v[84:87], v[68:71], v[20:23], 0\n"
    "ds_write_b64 v4, v[106:107] offset:3200\n"
    "v_mfma_f32_16x16x32_f16 v[108:111], v[96:99], v[24:27], v[108:111]\n"
    "ds_write_b8 v5, v8 offset:83\n"
    "v_mfma_f32_16x16x32_f16 v[108:111], v[100:103], v[36:39], v[108:111]\n"
    "v_mfma_f32_16x16x32_f16 v[112:115], v[96:99], v[28:31], v[112:115]\n"
    "v_cvt_pk_f16_f32 v88, v75, v79\n"
    "v_cvt_pk_f16_f32 v89, v74, v78\n"
    "v_mfma_f32_16x16x32_f16 v[112:115], v[100:103], v[40:43], v[112:115]\n"
    "v_cvt_pk_f16_f32 v90, v73, v77\n"
    "v_cvt_pk_f16_f32 v91, v72, v76\n"
    "v_mfma_f32_16x16x32_f16 v[116:119], v[96:99], v[32:35], 0\n"
    "v_cvt_pk_f16_f32 v92, v83, v87\n"
    "v_cvt_pk_f16_f32 v93, v82, v86\n"
    "v_mfma_f32_16x16x32_f16 v[116:119], v[100:103], v[44:47], v[116:119]\n"
    "v_cvt_pk_f16_f32 v94, v81, v85\n"
    "v_cvt_pk_f16_f32 v95, v80, v84\n"
    "s_setprio 2\n"
    "s_waitcnt lgkmcnt(5)\n"
    "v_mfma_f32_16x16x32_f16 v[72:75], v[48:51], v[12:15], 0\n"
    "v_mfma_f32_16x16x32_f16 v[76:79], v[48:51], v[16:19], 0\n"
    "v_mfma_f32_16x16x32_f16 v[80:83], v[48:51], v[20:23], 0\n"
    "ds_write_b64 v4, v[108:109] offset:3840\n"
    "v_mfma_f32_16x16x32_f16 v[84:87], v[52:55], v[20:23], 0\n"
    "ds_write_b64 v4, v[110:111] offset:4480\n"
    "v_mfma_f32_16x16x32_f16 v[112:115], v[88:91], v[24:27], v[112:115]\n"
    "ds_write_b8 v5, v8 offset:91\n"
    "v_mfma_f32_16x16x32_f16 v[112:115], v[92:95], v[36:39], v[112:115]\n"
    "v_mfma_f32_16x16x32_f16 v[116:119], v[88:91], v[28:31], v[116:119]\n"
    "v_cvt_pk_f16_f32 v96, v75, v79\n"
    "v_cvt_pk_f16_f32 v97, v74, v78\n"
    "v_mfma_f32_16x16x32_f16 v[116:119], v[92:95], v[40:43], v[116:119]\n"
    "v_cvt_pk_f16_f32 v98, v73, v77\n"
    "v_cvt_pk_f16_f32 v99, v72, v76\n"
    "v_mfma_f32_16x16x32_f16 v[120:123], v[88:91], v[32:35], 0\n"
    "v_cvt_pk_f16_f32 v100, v83, v87\n"
    "v_cvt_pk_f16_f32 v101, v82, v86\n"
    "v_mfma_f32_16x16x32_f16 v[120:123], v[92:95], v[44:47], v[120:123]\n"
    "v_cvt_pk_f16_f32 v102, v81, v85\n"
    "v_cvt_pk_f16_f32 v103, v80, v84\n"
    "s_setprio 1\n"
    "s_waitcnt lgkmcnt(6)\n"
    "v_mfma_f32_16x16x32_f16 v[72:75], v[56:59], v[12:15], 0\n"
    "v_mfma_f32_16x16x32_f16 v[76:79], v[56:59], v[16:19], 0\n"
    "v_mfma_f32_16x16x32_f16 v[80:83], v[56:59], v[20:23], 0\n"
    "ds_write_b64 v4, v[112:113] offset:5120\n"
    "v_mfma_f32_16x16x32_f16 v[84:87], v[60:63], v[20:23], 0\n"
    "ds_write_b64 v4, v[114:115] offset:5760\n"
    "v_mfma_f32_16x16x32_f16 v[116:119], v[96:99], v[24:27], v[116:119]\n"
    "ds_write_b8 v5, v8 offset:99\n"
    "v_mfma_f32_16x16x32_f16 v[116:119], v[100:103], v[36:39], v[116:119]\n"
    "v_mfma_f32_16x16x32_f16 v[120:123], v[96:99], v[28:31], v[120:123]\n"
    "v_cvt_pk_f16_f32 v88, v75, v79\n"
    "v_cvt_pk_f16_f32 v89, v74, v78\n"
    "v_mfma_f32_16x16x32_f16 v[120:123], v[100:103], v[40:43], v[120:123]\n"
    "v_cvt_pk_f16_f32 v90, v73, v77\n"
    "v_cvt_pk_f16_f32 v91, v72, v76\n"
    "v_mfma_f32_16x16x32_f16 v[124:127], v[96:99], v[32:35], 0\n"
    "v_cvt_pk_f16_f32 v92, v83, v87\n"
    "v_cvt_pk_f16_f32 v93, v82, v86\n"
    "v_mfma_f32_16x16x32_f16 v[124:127], v[100:103], v[44:47], v[124:127]\n"
    "v_cvt_pk_f16_f32 v94, v81, v85\n"
    "v_cvt_pk_f16_f32 v95, v80, v84\n"
    "s_setprio 0\n"
    "v_mfma_f32_16x16x32_f16 v[120:123], v[88:91], v[24:27], v[120:123]\n"
    "v_mfma_f32_16x16x32_f16 v[120:123], v[92:95], v[36:39], v[120:123]\n"
    "ds_write_b64 v4, v[116:117] offset:6400\n"
    "v_mfma_f32_16x16x32_f16 v[124:127], v[88:91], v[28:31], v[124:127]\n"
    "ds_write_b64 v4, v[118:119] offset:7040\n"
    "v_mfma_f32_16x16x32_f16 v[124:127], v[92:95], v[40:43], v[124:127]\n"
    "ds_write_b8 v5, v8 offset:107\n"
    "s_nop 2\n"
    "ds_write_b64 v4, v[120:121] offset:7680\n"
    "ds_write_b64 v4, v[122:123] offset:8320\n"
    "ds_write_b8 v5, v8 offset:115\n"
    "s_nop 0\n"
    "ds_write_b64 v4, v[124:125] offset:8960\n"
    "ds_write_b64 v4, v[126:127] offset:9600\n"
    "ds_write_b8 v5, v8 offset:123\n"
    "s_setprio 0\n"
    "s_branch LN2\n"
    "HELP:\n"
    "s_sub_u32 s42, s37, 4\n"
    "v_and_b32 v6, 15, v0\n"
    "v_bfe_u32 v7, v0, 4, 2\n"
    "v_and_b32 v1, 63, v0\n"
    "v_lshlrev_b32 v1, 4, v1\n"
    "v_add_u32 v1, 101376, v1\n"
    "s_mul_i32 s43, s42, 4576\n"
    "s_add_u32 s43, s43, 117216\n"
    "v_lshlrev_b32 v2, 1, v6\n"
    "v_sub_u32 v2, v7, v2\n"
    "v_lshl_add_u32 v2, v2, 4, s43\n"
    "s_mul_i32 s44, s42, 3328\n"
    "s_add_u32 s43, s44, 92596\n"
    "v_lshlrev_b32 v3, 2, v7\n"
    "v_sub_u32 v3, v6, v3\n"
    "v_lshl_add_u32 v3, v3, 2, s43\n"
    "s_mul_i32 s43, s42, 10240\n"
    "s_add_u32 s43, s43, 0\n"
    "v_mul_u32_u24 v4, 160, v7\n"
    "v_lshl_add_u32 v4, v6, 3, v4\n"
    "v_add_u32 v4, s43, v4\n"
    "s_add_u32 s43, s42, 147968\n"
    "v_mov_b32 v5, s43\n"
    "v_mov_b32 v8, 1\n"
    "v_mov_b32 v11, 147968\n"
    "s_add_u32 s44, s44, 92990\n"
    "v_mov_b32 v9, s44\n"
    "s_mul_i32 s43, s42, 3\n"
    "v_add_u32 v5, s43, v5\n"
    "s_cmpk_ge_u32 s37, 6\n"
    "s_cbranch_scc1 KST2\n"
    "HELP1:\n"
    "s_mov_b32 s43, 0x01000100\n"
    "s_branch E_24\n"
    "R_23:\n"
    "s_sleep 1\n"
    "s_sub_u32 s39, s39, 1\n"
    "s_cbranch_scc1 ABORT\n"
    "E_24:\n"
    "ds_read_b32 v12, v11 offset:224\n"
    "s_waitcnt lgkmcnt(0)\n"
    "v_cmp_ne_u32 vcc, 6, v12\n"
    "s_cbranch_vccnz R_23\n"
    "ds_read_b128 v[12:15], v1 offset:3072\n"
    "ds_read_b128 v[16:19], v1 offset:4096\n"
    "ds_read_b128 v[20:23], v1 offset:5120\n"
    "ds_read_u16 v10, v9\n"
    "ds_read2_b32 v[24:25], v3 offset0:128 offset1:129\n"
    "ds_read2_b32 v[26:27], v3 offset0:130 offset1:131\n"
    "ds_read2_b32 v[28:29], v3 offset0:144 offset1:145\n"
    "ds_read2_b32 v[30:31], v3 offset0:146 offset1:147\n"
    "ds_read2_b32 v[32:33], v3 offset0:160 offset1:161\n"
    "ds_read2_b32 v[34:35], v3 offset0:162 offset1:163\n"
    "ds_read2_b32 v[36:37], v3 offset0:176 offset1:177\n"
    "ds_read2_b32 v[38:39], v3 offset0:178 offset1:179\n"
    "s_waitcnt lgkmcnt(8)\n"
    "v_perm_b32 v10, v10, v10, s43\n"
    "v_and_b32 v20, v20, v10\n"
    "v_and_b32 v21, v21, v10\n"
    "v_and_b32 v22, v22, v10\n"
    "v_and_b32 v23, v23, v10\n"
    "s_setprio 3\n"
    "s_barrier\n"
    "ds_read_b128 v[40:43], v2 offset:3584\n"
    "ds_read_b128 v[44:47], v2 offset:3600\n"
    "ds_read_b128 v[48:51], v2 offset:3072\n"
    "ds_read_b128 v[52:55], v2 offset:3088\n"
    "ds_read_b128 v[56:59], v2 offset:2560\n"
    "ds_read_b128 v[60:63], v2 offset:2576\n"
    "ds_read_b128 v[64:67], v2 offset:2048\n"
    "ds_read_b128 v[68:71], v2 offset:2064\n"
    "ds_read_b128 v[104:107], v2 offset:1040\n"
    "s_waitcnt lgkmcnt(7)\n"
    "v_mfma_f32_16x16x32_f16 v[72:75], v[40:43], v[12:15], 0\n"
    "v_mfma_f32_16x16x32_f16 v[76:79], v[44:47], v[16:19], 0\n"
    "ds_read_b128 v[44:47], v1 offset:0\n"
    "s_waitcnt lgkmcnt(6)\n"
    "v_mfma_f32_16x16x32_f16 v[80:83], v[48:51], v[12:15], 0\n"
    "v_mfma_f32_16x16x32_f16 v[84:87], v[52:55], v[16:19], 0\n"
    "ds_read_b128 v[52:55], v1 offset:1024\n"
    "s_waitcnt lgkmcnt(5)\n"
    "v_mfma_f32_16x16x32_f16 v[88:91], v[56:59], v[12:15], 0\n"
    "v_mfma_f32_16x16x32_f16 v[92:95], v[60:63], v[16:19], 0\n"
    "ds_read_b128 v[60:63], v1 offset:2048\n"
    "s_waitcnt lgkmcnt(4)\n"
    "v_mfma_f32_16x16x32_f16 v[96:99], v[64:67], v[12:15], 0\n"
    "v_mfma_f32_16x16x32_f16 v[100:103], v[68:71], v[16:19], 0\n"
    "ds_read_b128 v[68:71], v2 offset:1024\n"
    "v_mfma_f32_16x16x32_f16 v[108:111], v[20:23], v[40:43], 0\n"
    "v_cvt_pk_f16_f32 v12, v75, v79\n"
    "v_cvt_pk_f16_f32 v13, v74, v78\n"
    "v_mfma_f32_16x16x32_f16 v[112:115], v[20:23], v[48:51], 0\n"
    "v_cvt_pk_f16_f32 v14, v73, v77\n"
    "v_cvt_pk_f16_f32 v15, v72, v76\n"
    "v_mfma_f32_16x16x32_f16 v[116:119], v[20:23], v[56:59], 0\n"
    "v_cvt_pk_f16_f32 v16, v83, v87\n"
    "v_cvt_pk_f16_f32 v17, v82, v86\n"
    "v_mfma_f32_16x16x32_f16 v[120:123], v[20:23], v[64:67], 0\n"
    "v_cvt_pk_f16_f32 v18, v81, v85\n"
    "v_cvt_pk_f16_f32 v19, v80, v84\n"
    "ds_read2_b32 v[40:41], v3 offset0:0 offset1:1\n"
    "ds_read2_b32 v[42:43], v3 offset0:2 offset1:3\n"
    "ds_read2_b32 v[48:49], v3 offset0:64 offset1:65\n"
    "ds_read2_b32 v[50:51], v3 offset0:66 offset1:67\n"
    "v_mfma_f32_16x16x32_f16 v[108:111], v[12:15], v[24:27], v[108:111]\n"
    "v_cvt_pk_f16_f32 v20, v91, v95\n"
    "v_cvt_pk_f16_f32 v21, v90, v94\n"
    "v_mfma_f32_16x16x32_f16 v[112:115], v[12:15], v[28:31], v[112:115]\n"
    "v_cvt_pk_f16_f32 v22, v89, v93\n"
    "v_cvt_pk_f16_f32 v23, v88, v92\n"
    "v_mfma_f32_16x16x32_f16 v[116:119], v[12:15], v[32:35], v[116:119]\n"
    "v_cvt_pk_f16_f32 v124, v99, v103\n"
    "v_cvt_pk_f16_f32 v125, v98, v102\n"
    "v_mfma_f32_16x16x32_f16 v[120:123], v[12:15], v[36:39], v[120:123]\n"
    "v_cvt_pk_f16_f32 v126, v97, v101\n"
    "v_cvt_pk_f16_f32 v127, v96, v100\n"
    "s_waitcnt lgkmcnt(4)\n"
    "v_mfma_f32_16x16x32_f16 v[72:75], v[68:71], v[44:47], 0\n"
    "v_mfma_f32_16x16x32_f16 v[76:79], v[68:71], v[52:55], 0\n"
    "v_mfma_f32_16x16x32_f16 v[80:83], v[68:71], v[60:63], 0\n"
    "ds_write_b64 v4, v[108:109] offset:5120\n"
    "v_mfma_f32_16x16x32_f16 v[84:87], v[104:107], v[60:63], 0\n"
    "ds_write_b64 v4, v[110:111] offset:5760\n"
    "v_mfma_f32_16x16x32_f16 v[112:115], v[16:19], v[24:27], v[112:115]\n"
    "v_mfma_f32_16x16x32_f16 v[116:119], v[16:19], v[28:31], v[116:119]\n"
    "v_mfma_f32_16x16x32_f16 v[120:123], v[16:19], v[32:35], v[120:123]\n"
    "s_nop 0\n"
    "v_cvt_pk_f16_f32 v12, v75, v79\n"
    "v_cvt_pk_f16_f32 v13, v74, v78\n"
    "v_mfma_f32_16x16x32_f16 v[116:119], v[20:23], v[24:27], v[116:119]\n"
    "v_cvt_pk_f16_f32 v14, v73, v77\n"
    "v_cvt_pk_f16_f32 v15, v72, v76\n"
    "v_mfma_f32_16x16x32_f16 v[120:123], v[20:23], v[28:31], v[120:123]\n"
    "v_cvt_pk_f16_f32 v16, v83, v87\n"
    "v_cvt_pk_f16_f32 v17, v82, v86\n"
    "v_mfma_f32_16x16x32_f16 v[120:123], v[124:127], v[24:27], v[120:123]\n"
    "v_cvt_pk_f16_f32 v18, v81, v85\n"
    "v_cvt_pk_f16_f32 v19, v80, v84\n"
    "s_waitcnt lgkmcnt(2)\n"
    "v_mfma_f32_16x16x32_f16 v[120:123], v[12:15], v[40:43], v[120:123]\n"
    "ds_write_b64 v4, v[112:113] offset:6400\n"
    "ds_write_b64 v4, v[114:115] offset:7040\n"
    "v_mfma_f32_16x16x32_f16 v[120:123], v[16:19], v[48:51], v[120:123]\n"
    "ds_write_b64 v4, v[116:117] offset:7680\n"
    "ds_write_b64 v4, v[118:119] offset:8320\n"
    "s_nop 5\n"
    "ds_write_b64 v4, v[120:121] offset:8960\n"
    "ds_write_b64 v4, v[122:123] offset:9600\n"
    "ds_write_b8 v5, v8 offset:44\n"
    "s_branch MISC\n"
    "NEAR:\n"
    "v_and_b32 v6, 15, v0\n"
    "v_bfe_u32 v7, v0, 4, 2\n"
    "v_and_b32 v1, 63, v0\n"
    "v_lshlrev_b32 v1, 4, v1\n"
    "v_add_u32 v1, 101376, v1\n"
    "s_mul_i32 s43, s42, 4576\n"
    "s_add_u32 s43, s43, 117216\n"
    "v_lshlrev_b32 v2, 1, v6\n"
    "v_sub_u32 v2, v7, v2\n"
    "v_lshl_add_u32 v2, v2, 4, s43\n"
    "s_mul_i32 s44, s42, 3328\n"
    "s_add_u32 s43, s44, 92596\n"
    "v_lshlrev_b32 v3, 2, v7\n"
    "v_sub_u32 v3, v6, v3\n"
    "v_lshl_add_u32 v3, v3, 2, s43\n"
    "s_mul_i32 s43, s42, 10240\n"
    "s_add_u32 s43, s43, 0\n"
    "v_mul_u32_u24 v4, 160, v7\n"
    "v_lshl_add_u32 v4, v6, 3, v4\n"
    "v_add_u32 v4, s43, v4\n"
    "s_add_u32 s43, s42, 147968\n"
    "v_mov_b32 v5, s43\n"
    "v_mov_b32 v8, 1\n"
    "v_mov_b32 v11, 147968\n"
    "v_lshl_add_u32 v3, v7, 3, v6\n"
    "v_add_u32 v3, 1, v3\n"
    "v_and_b32 v9, 1, v3\n"
    "v_mul_u32_u24 v9, 2110, v9\n"
    "s_add_u32 s44, s44, 91392\n"
    "v_lshl_add_u32 v3, v3, 1, s44\n"
    "v_add_u32 v3, v3, v9\n"
    "v_add_u32 v1, 512, v3\n"
    "s_mul_i32 s43, s42, 3\n"
    "v_add_u32 v10, s43, v5\n"
    "s_branch KST2\n"
    "NEAR1:\n"
    "s_setprio 3\n"
    "s_branch E_26\n"
    "R_25:\n"
    "s_sub_u32 s39, s39, 1\n"
    "s_cbranch_scc1 ABORT\n"
    "E_26:\n"
    "ds_read_b32 v12, v11 offset:224\n"
    "s_waitcnt lgkmcnt(0)\n"
    "v_cmp_ne_u32 vcc, 6, v12\n"
    "s_cbranch_vccnz R_25\n"
    "ds_read2_b32 v[88:89], v3 offset0:112 offset1:113\n"
    "ds_read2_b32 v[90:91], v3 offset0:114 offset1:115\n"
    "ds_read2_b32 v[120:121], v1 offset0:112 offset1:113\n"
    "ds_read2_b32 v[122:123], v1 offset0:114 offset1:115\n"
    "ds_read2_b32 v[84:85], v3 offset0:96 offset1:97\n"
    "ds_read2_b32 v[86:87], v3 offset0:98 offset1:99\n"
    "ds_read2_b32 v[116:117], v1 offset0:96 offset1:97\n"
    "ds_read2_b32 v[118:119], v1 offset0:98 offset1:99\n"
    "ds_read2_b32 v[80:81], v3 offset0:80 offset1:81\n"
    "ds_read2_b32 v[82:83], v3 offset0:82 offset1:83\n"
    "ds_read2_b32 v[112:113], v1 offset0:80 offset1:81\n"
    "ds_read2_b32 v[114:115], v1 offset0:82 offset1:83\n"
    "ds_read2_b32 v[76:77], v3 offset0:64 offset1:65\n"
    "ds_read2_b32 v[78:79], v3 offset0:66 offset1:67\n"
    "ds_read2_b32 v[108:109], v1 offset0:64 offset1:65\n"
    "ds_read2_b32 v[110:111], v1 offset0:66 offset1:67\n"
    "s_barrier\n"
    "ds_read_b128 v[24:27], v2 offset:4032\n"
    "ds_read_b128 v[28:31], v2 offset:3968\n"
    "ds_read_b128 v[32:35], v2 offset:3904\n"
    "s_waitcnt lgkmcnt(2)\n"
    "v_mfma_f32_16x16x32_f16 v[48:51], v[88:91], v[24:27], 0\n"
    "ds_read2_b32 v[72:73], v3 offset0:48 offset1:49\n"
    "ds_read2_b32 v[74:75], v3 offset0:50 offset1:51\n"
    "ds_read2_b32 v[104:105], v1 offset0:48 offset1:49\n"
    "ds_read2_b32 v[106:107], v1 offset0:50 offset1:51\n"
    "ds_read_b128 v[36:39], v2 offset:3840\n"
    "v_mfma_f32_16x16x32_f16 v[52:55], v[120:123], v[24:27], 0\n"
    "s_waitcnt lgkmcnt(6)\n"
    "v_mfma_f32_16x16x32_f16 v[48:51], v[84:87], v[28:31], v[48:51]\n"
    "ds_read2_b32 v[68:69], v3 offset0:32 offset1:33\n"
    "ds_read2_b32 v[70:71], v3 offset0:34 offset1:35\n"
    "ds_read2_b32 v[100:101], v1 offset0:32 offset1:33\n"
    "ds_read2_b32 v[102:103], v1 offset0:34 offset1:35\n"
    "ds_read_b128 v[40:43], v2 offset:3776\n"
    "v_mfma_f32_16x16x32_f16 v[52:55], v[116:119], v[28:31], v[52:55]\n"
    "s_waitcnt lgkmcnt(10)\n"
    "v_mfma_f32_16x16x32_f16 v[48:51], v[80:83], v[32:35], v[48:51]\n"
    "ds_read2_b32 v[64:65], v3 offset0:16 offset1:17\n"
    "ds_read2_b32 v[66:67], v3 offset0:18 offset1:19\n"
    "ds_read2_b32 v[96:97], v1 offset0:16 offset1:17\n"
    "ds_read2_b32 v[98:99], v1 offset0:18 offset1:19\n"
    "ds_read_b128 v[44:47], v2 offset:3712\n"
    "v_mfma_f32_16x16x32_f16 v[52:55], v[112:115], v[32:35], v[52:55]\n"
    "s_waitcnt lgkmcnt(10)\n"
    "v_mfma_f32_16x16x32_f16 v[48:51], v[76:79], v[36:39], v[48:51]\n"
    "ds_read2_b32 v[60:61], v3 offset0:0 offset1:1\n"
    "ds_read2_b32 v[62:63], v3 offset0:2 offset1:3\n"
    "ds_read2_b32 v[92:93], v1 offset0:0 offset1:1\n"
    "ds_read2_b32 v[94:95], v1 offset0:2 offset1:3\n"
    "ds_read2_b32 v[124:125], v1 offset0:128 offset1:129\n"
    "ds_read2_b32 v[126:127], v1 offset0:130 offset1:131\n"
    "ds_read_b128 v[24:27], v2 offset:3648\n"
    "v_mfma_f32_16x16x32_f16 v[52:55], v[108:111], v[36:39], v[52:55]\n"
    "s_waitcnt lgkmcnt(12)\n"
    "v_mfma_f32_16x16x32_f16 v[48:51], v[72:75], v[40:43], v[48:51]\n"
    "ds_read_b128 v[28:31], v2 offset:3584\n"
    "ds_read_b128 v[32:35], v2 offset:3520\n"
    "v_mfma_f32_16x16x32_f16 v[52:55], v[104:107], v[40:43], v[52:55]\n"
    "s_waitcnt lgkmcnt(9)\n"
    "v_mfma_f32_16x16x32_f16 v[48:51], v[68:71], v[44:47], v[48:51]\n"
    "ds_read_b128 v[36:39], v2 offset:3456\n"
    "ds_read_b128 v[40:43], v2 offset:3392\n"
    "v_mfma_f32_16x16x32_f16 v[52:55], v[100:103], v[44:47], v[52:55]\n"
    "s_waitcnt lgkmcnt(4)\n"
    "v_mfma_f32_16x16x32_f16 v[48:51], v[64:67], v[24:27], v[48:51]\n"
    "ds_read_b128 v[44:47], v2 offset:3328\n"
    "v_mfma_f32_16x16x32_f16 v[52:55], v[96:99], v[24:27], v[52:55]\n"
    "s_waitcnt lgkmcnt(4)\n"
    "v_mfma_f32_16x16x32_f16 v[48:51], v[60:63], v[28:31], v[48:51]\n"
    "ds_read_b128 v[24:27], v2 offset:3264\n"
    "v_mfma_f32_16x16x32_f16 v[52:55], v[92:95], v[28:31], v[52:55]\n"
    "v_mfma_f32_16x16x32_f16 v[56:59], v[124:127], v[28:31], 0\n"
    "s_waitcnt lgkmcnt(4)\n"
    "v_mfma_f32_16x16x32_f16 v[52:55], v[88:91], v[32:35], v[52:55]\n"
    "ds_read_b128 v[28:31], v2 offset:3200\n"
    "v_mfma_f32_16x16x32_f16 v[56:59], v[120:123], v[32:35], v[56:59]\n"
    "s_waitcnt lgkmcnt(4)\n"
    "v_mfma_f32_16x16x32_f16 v[52:55], v[84:87], v[36:39], v[52:55]\n"
    "ds_read_b128 v[32:35], v2 offset:3136\n"
    "v_mfma_f32_16x16x32_f16 v[56:59], v[116:119], v[36:39], v[56:59]\n"
    "ds_write_b64 v4, v[48:49] offset:0\n"
    "s_waitcnt lgkmcnt(5)\n"
    "v_mfma_f32_16x16x32_f16 v[52:55], v[80:83], v[40:43], v[52:55]\n"
    "ds_read_b128 v[36:39], v2 offset:3072\n"
    "v_mfma_f32_16x16x32_f16 v[56:59], v[112:115], v[40:43], v[56:59]\n"
    "ds_write_b64 v4, v[50:51] offset:640\n"
    "s_waitcnt lgkmcnt(6)\n"
    "v_mfma_f32_16x16x32_f16 v[52:55], v[76:79], v[44:47], v[52:55]\n"
    "ds_read_b128 v[40:43], v2 offset:3008\n"
    "v_mfma_f32_16x16x32_f16 v[56:59], v[108:111], v[44:47], v[56:59]\n"
    "ds_write_b8 v5, v8 offset:64\n"
    "s_waitcnt lgkmcnt(7)\n"
    "v_mfma_f32_16x16x32_f16 v[52:55], v[72:75], v[24:27], v[52:55]\n"
    "ds_read_b128 v[44:47], v2 offset:2944\n"
    "v_mfma_f32_16x16x32_f16 v[56:59], v[104:107], v[24:27], v[56:59]\n"
    "s_waitcnt lgkmcnt(7)\n"
    "v_mfma_f32_16x16x32_f16 v[52:55], v[68:71], v[28:31], v[52:55]\n"
    "ds_read_b128 v[24:27], v2 offset:2880\n"
    "v_mfma_f32_16x16x32_f16 v[56:59], v[100:103], v[28:31], v[56:59]\n"
    "s_waitcnt lgkmcnt(7)\n"
    "v_mfma_f32_16x16x32_f16 v[52:55], v[64:67], v[32:35], v[52:55]\n"
    "ds_read_b128 v[28:31], v2 offset:2816\n"
    "v_mfma_f32_16x16x32_f16 v[56:59], v[96:99], v[32:35], v[56:59]\n"
    "s_waitcnt lgkmcnt(6)\n"
    "v_mfma_f32_16x16x32_f16 v[52:55], v[60:63], v[36:39], v[52:55]\n"
    "ds_read_b128 v[32:35], v2 offset:2752\n"
    "v_mfma_f32_16x16x32_f16 v[56:59], v[92:95], v[36:39], v[56:59]\n"
    "v_mfma_f32_16x16x32_f16 v[48:51], v[124:127], v[36:39], 0\n"
    "s_waitcnt lgkmcnt(5)\n"
    "v_mfma_f32_16x16x32_f16 v[56:59], v[88:91], v[40:43], v[56:59]\n"
    "ds_read_b128 v[36:39], v2 offset:2688\n"
    "v_mfma_f32_16x16x32_f16 v[48:51], v[120:123], v[40:43], v[48:51]\n"
    "ds_read_b32 v11, v10 offset:44\n"
    "s_waitcnt lgkmcnt(5)\n"
    "v_mfma_f32_16x16x32_f16 v[56:59], v[84:87], v[44:47], v[56:59]\n"
    "ds_read_b128 v[40:43], v2 offset:2624\n"
    "v_mfma_f32_16x16x32_f16 v[48:51], v[116:119], v[44:47], v[48:51]\n"
    "ds_write_b64 v4, v[52:53] offset:1280\n"
    "s_waitcnt lgkmcnt(6)\n"
    "v_mfma_f32_16x16x32_f16 v[56:59], v[80:83], v[24:27], v[56:59]\n"
    "ds_read_b128 v[44:47], v2 offset:2560\n"
    "v_mfma_f32_16x16x32_f16 v[48:51], v[112:115], v[24:27], v[48:51]\n"
    "ds_write_b64 v4, v[54:55] offset:1920\n"
    "s_waitcnt lgkmcnt(7)\n"
    "v_mfma_f32_16x16x32_f16 v[56:59], v[76:79], v[28:31], v[56:59]\n"
    "ds_read_b128 v[24:27], v2 offset:2496\n"
    "v_mfma_f32_16x16x32_f16 v[48:51], v[108:111], v[28:31], v[48:51]\n"
    "ds_write_b8 v5, v8 offset:72\n"
    "s_waitcnt lgkmcnt(6)\n"
    "v_cmp_ne_u32 vcc, 1, v11\n"
    "v_mfma_f32_16x16x32_f16 v[56:59], v[72:75], v[32:35], v[56:59]\n"
    "ds_read_b128 v[28:31], v2 offset:2432\n"
    "v_mfma_f32_16x16x32_f16 v[48:51], v[104:107], v[32:35], v[48:51]\n"
    "s_cbranch_vccnz NEARW\n"
    "NEARC:\n"
    "ds_read_b64 v[52:53], v4 offset:5120\n"
    "ds_read_b64 v[54:55], v4 offset:5760\n"
    "v_mfma_f32_16x16x32_f16 v[56:59], v[68:71], v[36:39], v[56:59]\n"
    "ds_read_b128 v[32:35], v2 offset:2368\n"
    "v_mfma_f32_16x16x32_f16 v[48:51], v[100:103], v[36:39], v[48:51]\n"
    "s_waitcnt lgkmcnt(9)\n"
    "v_mfma_f32_16x16x32_f16 v[56:59], v[64:67], v[40:43], v[56:59]\n"
    "ds_read_b128 v[36:39], v2 offset:2304\n"
    "v_mfma_f32_16x16x32_f16 v[48:51], v[96:99], v[40:43], v[48:51]\n"
    "s_waitcnt lgkmcnt(8)\n"
    "v_mfma_f32_16x16x32_f16 v[56:59], v[60:63], v[44:47], v[56:59]\n"
    "ds_read_b128 v[40:43], v2 offset:2240\n"
    "v_mfma_f32_16x16x32_f16 v[48:51], v[92:95], v[44:47], v[48:51]\n"
    "s_waitcnt lgkmcnt(3)\n"
    "v_mfma_f32_16x16x32_f16 v[52:55], v[124:127], v[44:47], v[52:55]\n"
    "v_mfma_f32_16x16x32_f16 v[48:51], v[88:91], v[24:27], v[48:51]\n"
    "ds_read_b128 v[44:47], v2 offset:2176\n"
    "v_mfma_f32_16x16x32_f16 v[52:55], v[120:123], v[24:27], v[52:55]\n"
    "v_mfma_f32_16x16x32_f16 v[48:51], v[84:87], v[28:31], v[48:51]\n"
    "ds_read_b128 v[24:27], v2 offset:2112\n"
    "v_mfma_f32_16x16x32_f16 v[52:55], v[116:119], v[28:31], v[52:55]\n"
    "ds_write_b64 v4, v[56:57] offset:2560\n"
    "s_waitcnt lgkmcnt(5)\n"
    "v_mfma_f32_16x16x32_f16 v[48:51], v[80:83], v[32:35], v[48:51]\n"
    "ds_read_b128 v[28:31], v2 offset:2048\n"
    "v_mfma_f32_16x16x32_f16 v[52:55], v[112:115], v[32:35], v[52:55]\n"
    "ds_write_b64 v4, v[58:59] offset:3200\n"
    "s_waitcnt lgkmcnt(6)\n"
    "v_mfma_f32_16x16x32_f16 v[48:51], v[76:79], v[36:39], v[48:51]\n"
    "ds_read_b128 v[32:35], v2 offset:1984\n"
    "v_mfma_f32_16x16x32_f16 v[52:55], v[108:111], v[36:39], v[52:55]\n"
    "ds_write_b8 v5, v8 offset:80\n"
    "s_waitcnt lgkmcnt(7)\n"
    "v_mfma_f32_16x16x32_f16 v[48:51], v[72:75], v[40:43], v[48:51]\n"
    "ds_read_b128 v[36:39], v2 offset:1920\n"
    "v_mfma_f32_16x16x32_f16 v[52:55], v[104:107], v[40:43], v[52:55]\n"
    "ds_read_b64 v[56:57], v4 offset:6400\n"
    "ds_read_b64 v[58:59], v4 offset:7040\n"
    "s_waitcnt lgkmcnt(9)\n"
    "v_mfma_f32_16x16x32_f16 v[48:51], v[68:71], v[44:47], v[48:51]\n"
    "ds_read_b128 v[40:43], v2 offset:1856\n"
    "v_mfma_f32_16x16x32_f16 v[52:55], v[100:103], v[44:47], v[52:55]\n"
    "s_waitcnt lgkmcnt(9)\n"
    "v_mfma_f32_16x16x32_f16 v[48:51], v[64:67], v[24:27], v[48:51]\n"
    "ds_read_b128 v[44:47], v2 offset:1792\n"
    "v_mfma_f32_16x16x32_f16 v[52:55], v[96:99], v[24:27], v[52:55]\n"
    "s_waitcnt lgkmcnt(8)\n"
    "v_mfma_f32_16x16x32_f16 v[48:51], v[60:63], v[28:31], v[48:51]\n"
    "ds_read_b128 v[24:27], v2 offset:1728\n"
    "v_mfma_f32_16x16x32_f16 v[52:55], v[92:95], v[28:31], v[52:55]\n"
    "s_waitcnt lgkmcnt(3)\n"
    "v_mfma_f32_16x16x32_f16 v[56:59], v[124:127], v[28:31], v[56:59]\n"
    "v_mfma_f32_16x16x32_f16 v[52:55], v[88:91], v[32:35], v[52:55]\n"
    "ds_read_b128 v[28:31], v2 offset:1664\n"
    "v_mfma_f32_16x16x32_f16 v[56:59], v[120:123], v[32:35], v[56:59]\n"
    "v_mfma_f32_16x16x32_f16 v[52:55], v[84:87], v[36:39], v[52:55]\n"
    "ds_read_b128 v[32:35], v2 offset:1600\n"
    "v_mfma_f32_16x16x32_f16 v[56:59], v[116:119], v[36:39], v[56:59]\n"
    "ds_write_b64 v4, v[48:49] offset:3840\n"
    "s_waitcnt lgkmcnt(5)\n"
    "v_mfma_f32_16x16x32_f16 v[52:55], v[80:83], v[40:43], v[52:55]\n"
    "ds_read_b128 v[36:39], v2 offset:1536\n"
    "v_mfma_f32_16x16x32_f16 v[56:59], v[112:115], v[40:43], v[56:59]\n"
    "ds_write_b64 v4, v[50:51] offset:4480\n"
    "s_waitcnt lgkmcnt(6)\n"
    "v_mfma_f32_16x16x32_f16 v[52:55], v[76:79], v[44:47], v[52:55]\n"
    "ds_read_b128 v[40:43], v2 offset:1472\n"
    "v_mfma_f32_16x16x32_f16 v[56:59], v[108:111], v[44:47], v[56:59]\n"
    "ds_write_b8 v5, v8 offset:88\n"
    "s_waitcnt lgkmcnt(7)\n"
    "v_mfma_f32_16x16x32_f16 v[52:55], v[72:75], v[24:27], v[52:55]\n"
    "ds_read_b128 v[44:47], v2 offset:1408\n"
    "v_mfma_f32_16x16x32_f16 v[56:59], v[104:107], v[24:27], v[56:59]\n"
    "ds_read_b64 v[48:49], v4 offset:7680\n"
    "ds_read_b64 v[50:51], v4 offset:8320\n"
    "s_waitcnt lgkmcnt(9)\n"
    "v_mfma_f32_16x16x32_f16 v[52:55], v[68:71], v[28:31], v[52:55]\n"
    "ds_read_b128 v[24:27], v2 offset:1344\n"
    "v_mfma_f32_16x16x32_f16 v[56:59], v[100:103], v[28:31], v[56:59]\n"
    "s_waitcnt lgkmcnt(9)\n"
    "v_mfma_f32_16x16x32_f16 v[52:55], v[64:67], v[32:35], v[52:55]\n"
    "ds_read_b128 v[28:31], v2 offset:1280\n"
    "v_mfma_f32_16x16x32_f16 v[56:59], v[96:99], v[32:35], v[56:59]\n"
    "s_waitcnt lgkmcnt(8)\n"
    "v_mfma_f32_16x16x32_f16 v[52:55], v[60:63], v[36:39], v[52:55]\n"
    "ds_read_b128 v[32:35], v2 offset:1216\n"
    "v_mfma_f32_16x16x32_f16 v[56:59], v[92:95], v[36:39], v[56:59]\n"
    "s_waitcnt lgkmcnt(3)\n"
    "v_mfma_f32_16x16x32_f16 v[48:51], v[124:127], v[36:39], v[48:51]\n"
    "v_mfma_f32_16x16x32_f16 v[56:59], v[88:91], v[40:43], v[56:59]\n"
    "ds_read_b128 v[36:39], v2 offset:1152\n"
    "v_mfma_f32_16x16x32_f16 v[48:51], v[120:123], v[40:43], v[48:51]\n"
    "v_mfma_f32_16x16x32_f16 v[56:59], v[84:87], v[44:47], v[56:59]\n"
    "ds_read_b128 v[40:43], v2 offset:1088\n"
    "v_mfma_f32_16x16x32_f16 v[48:51], v[116:119], v[44:47], v[48:51]\n"
    "ds_write_b64 v4, v[52:53] offset:5120\n"
    "s_waitcnt lgkmcnt(5)\n"
    "v_mfma_f32_16x16x32_f16 v[56:59], v[80:83], v[24:27], v[56:59]\n"
    "ds_read_b128 v[44:47], v2 offset:1024\n"
    "v_mfma_f32_16x16x32_f16 v[48:51], v[112:115], v[24:27], v[48:51]\n"
    "ds_write_b64 v4, v[54:55] offset:5760\n"
    "s_waitcnt lgkmcnt(6)\n"
    "v_mfma_f32_16x16x32_f16 v[56:59], v[76:79], v[28:31], v[56:59]\n"
    "ds_read_b128 v[24:27], v2 offset:960\n"
    "v_mfma_f32_16x16x32_f16 v[48:51], v[108:111], v[28:31], v[48:51]\n"
    "ds_write_b8 v5, v8 offset:96\n"
    "s_waitcnt lgkmcnt(7)\n"
    "v_mfma_f32_16x16x32_f16 v[56:59], v[72:75], v[32:35], v[56:59]\n"
    "ds_read_b128 v[28:31], v2 offset:896\n"
    "v_mfma_f32_16x16x32_f16 v[48:51], v[104:107], v[32:35], v[48:51]\n"
    "ds_read_b64 v[52:53], v4 offset:8960\n"
    "ds_read_b64 v[54:55], v4 offset:9600\n"
    "s_waitcnt lgkmcnt(9)\n"
    "v_mfma_f32_16x16x32_f16 v[56:59], v[68:71], v[36:39], v[56:59]\n"
    "ds_read_b128 v[32:35], v2 offset:832\n"
    "v_mfma_f32_16x16x32_f16 v[48:51], v[100:103], v[36:39], v[48:51]\n"
    "s_waitcnt lgkmcnt(9)\n"
    "v_mfma_f32_16x16x32_f16 v[56:59], v[64:67], v[40:43], v[56:59]\n"
    "ds_read_b128 v[36:39], v2 offset:768\n"
    "v_mfma_f32_16x16x32_f16 v[48:51], v[96:99], v[40:43], v[48:51]\n"
    "s_waitcnt lgkmcnt(8)\n"
    "v_mfma_f32_16x16x32_f16 v[56:59], v[60:63], v[44:47], v[56:59]\n"
    "ds_read_b128 v[40:43], v2 offset:704\n"
    "v_mfma_f32_16x16x32_f16 v[48:51], v[92:95], v[44:47], v[48:51]\n"
    "s_waitcnt lgkmcnt(3)\n"
    "v_mfma_f32_16x16x32_f16 v[52:55], v[124:127], v[44:47], v[52:55]\n"
    "v_mfma_f32_16x16x32_f16 v[48:51], v[88:91], v[24:27], v[48:51]\n"
    "ds_read_b128 v[44:47], v2 offset:640\n"
    "v_mfma_f32_16x16x32_f16 v[52:55], v[120:123], v[24:27], v[52:55]\n"
    "v_mfma_f32_16x16x32_f16 v[48:51], v[84:87], v[28:31], v[48:51]\n"
    "ds_read_b128 v[24:27], v2 offset:576\n"
    "v_mfma_f32_16x16x32_f16 v[52:55], v[116:119], v[28:31], v[52:55]\n"
    "ds_write_b64 v4, v[56:57] offset:6400\n"
    "s_waitcnt lgkmcnt(5)\n"
    "v_mfma_f32_16x16x32_f16 v[48:51], v[80:83], v[32:35], v[48:51]\n"
    "ds_read_b128 v[28:31], v2 offset:512\n"
    "v_mfma_f32_16x16x32_f16 v[52:55], v[112:115], v[32:35], v[52:55]\n"
    "ds_write_b64 v4, v[58:59] offset:7040\n"
    "ds_write_b8 v5, v8 offset:104\n"
    "s_waitcnt lgkmcnt(7)\n"
    "v_mfma_f32_16x16x32_f16 v[48:51], v[76:79], v[36:39], v[48:51]\n"
    "ds_read_b128 v[32:35], v2 offset:448\n"
    "v_mfma_f32_16x16x32_f16 v[52:55], v[108:111], v[36:39], v[52:55]\n"
    "s_waitcnt lgkmcnt(7)\n"
    "v_mfma_f32_16x16x32_f16 v[48:51], v[72:75], v[40:43], v[48:51]\n"
    "ds_read_b128 v[36:39], v2 offset:384\n"
    "v_mfma_f32_16x16x32_f16 v[52:55], v[104:107], v[40:43], v[52:55]\n"
    "s_waitcnt lgkmcnt(7)\n"
    "v_mfma_f32_16x16x32_f16 v[48:51], v[68:71], v[44:47], v[48:51]\n"
    "ds_read_b128 v[40:43], v2 offset:320\n"
    "v_mfma_f32_16x16x32_f16 v[52:55], v[100:103], v[44:47], v[52:55]\n"
    "s_waitcnt lgkmcnt(7)\n"
    "v_mfma_f32_16x16x32_f16 v[48:51], v[64:67], v[24:27], v[48:51]\n"
    "ds_read_b128 v[44:47], v2 offset:256\n"
    "v_mfma_f32_16x16x32_f16 v[52:55], v[96:99], v[24:27], v[52:55]\n"
    "s_waitcnt lgkmcnt(6)\n"
    "v_mfma_f32_16x16x32_f16 v[48:51], v[60:63], v[28:31], v[48:51]\n"
    "ds_read_b128 v[24:27], v2 offset:192\n"
    "v_mfma_f32_16x16x32_f16 v[52:55], v[92:95], v[28:31], v[52:55]\n"
    "s_waitcnt lgkmcnt(4)\n"
    "v_mfma_f32_16x16x32_f16 v[52:55], v[88:91], v[32:35], v[52:55]\n"
    "ds_read_b128 v[28:31], v2 offset:128\n"
    "s_waitcnt lgkmcnt(4)\n"
    "v_mfma_f32_16x16x32_f16 v[52:55], v[84:87], v[36:39], v[52:55]\n"
    "ds_read_b128 v[32:35], v2 offset:64\n"
    "ds_write_b64 v4, v[48:49] offset:7680\n"
    "s_waitcnt lgkmcnt(5)\n"
    "v_mfma_f32_16x16x32_f16 v[52:55], v[80:83], v[40:43], v[52:55]\n"
    "ds_read_b128 v[36:39], v2 offset:0\n"
    "ds_write_b64 v4, v[50:51] offset:8320\n"
    "ds_write_b8 v5, v8 offset:112\n"
    "s_waitcnt lgkmcnt(7)\n"
    "v_mfma_f32_16x16x32_f16 v[52:55], v[76:79], v[44:47], v[52:55]\n"
    "s_waitcnt lgkmcnt(6)\n"
    "v_mfma_f32_16x16x32_f16 v[52:55], v[72:75], v[24:27], v[52:55]\n"
    "s_waitcnt lgkmcnt(5)\n"
    "v_mfma_f32_16x16x32_f16 v[52:55], v[68:71], v[28:31], v[52:55]\n"
    "s_waitcnt lgkmcnt(4)\n"
    "v_mfma_f32_16x16x32_f16 v[52:55], v[64:67], v[32:35], v[52:55]\n"
    "s_waitcnt lgkmcnt(2)\n"
    "v_mfma_f32_16x16x32_f16 v[52:55], v[60:63], v[36:39], v[52:55]\n"
    "s_nop 7\n"
    "ds_write_b64 v4, v[52:53] offset:8960\n"
    "ds_write_b64 v4, v[54:55] offset:9600\n"
    "ds_write_b8 v5, v8 offset:120\n"
    "s_setprio 0\n"
    "s_branch LN2\n"
    "NEARW:\n"
    "s_branch E_28\n"
    "R_27:\n"
    "s_sleep 1\n"
    "s_sub_u32 s39, s39, 1\n"
    "s_cbranch_scc1 ABORT\n"
    "E_28:\n"
    "ds_read_b32 v11, v10 offset:44\n"
    "s_waitcnt lgkmcnt(0)\n"
    "v_cmp_ne_u32 vcc, 1, v11\n"
    "s_cbranch_vccnz R_27\n"
    "s_branch NEARC\n"
    "EPS:\n"
    "v_mov_b32 v14, 147968\n"
    "v_mov_b32 v15, 1\n"
    "v_bfe_u32 v2, v0, 3, 3\n"
    "v_bfe_u32 v3, v0, 1, 2\n"
    "v_mul_u32_u24 v3, 160, v3\n"
    "v_lshl_add_u32 v2, v2, 3, v3\n"
    "v_and_b32 v3, 1, v0\n"
    "s_movk_i32 s43, 640\n"
    "v_mad_u32_u24 v2, v3, s43, v2\n"
    "s_mul_i32 s43, s38, 16512\n"
    "s_add_u32 s43, s43, 131072\n"
    "v_and_b32 v3, 63, v0\n"
    "v_lshl_add_u32 v1, v3, 4, s43\n"
    "v_mov_b32 v43, s36\n"
    "s_sub_u32 s57, 15, s37\n"
    "v_add_u32 v36, 1792, v86\n"
    "s_lshl_b32 s49, s57, 10\n"
    "s_lshr_b32 s46, s57, 1\n"
    "s_mul_i32 s43, s46, 1280\n"
    "s_and_b32 s47, s57, 1\n"
    "s_lshl_b32 s47, s47, 6\n"
    "s_add_u32 s43, s43, s47\n"
    "v_add_u32 v30, s43, v2\n"
    "s_lshl_b32 s46, s46, 3\n"
    "v_add_u32 v33, s46, v14\n"
    "s_lshl_b32 s50, s54, 10\n"
    "s_lshr_b32 s46, s54, 1\n"
    "s_mul_i32 s43, s46, 1280\n"
    "s_and_b32 s47, s54, 1\n"
    "s_lshl_b32 s47, s47, 6\n"
    "s_add_u32 s43, s43, s47\n"
    "v_add_u32 v31, s43, v2\n"
    "s_lshl_b32 s46, s46, 3\n"
    "v_add_u32 v34, s46, v14\n"
    "s_lshl_b32 s51, s55, 10\n"
    "s_lshr_b32 s46, s55, 1\n"
    "s_mul_i32 s43, s46, 1280\n"
    "s_and_b32 s47, s55, 1\n"
    "s_lshl_b32 s47, s47, 6\n"
    "s_add_u32 s43, s43, s47\n"
    "v_add_u32 v32, s43, v2\n"
    "s_lshl_b32 s46, s46, 3\n"
    "v_add_u32 v35, s46, v14\n"
    "s_mov_b32 s46, 0x01010101\n"
    "s_movk_i32 s47, 0x0101\n"
    "s_and_b32 s42, s38, 1\n"
    "s_lshl_b32 s42, s42, 4\n"
    "s_waitcnt lgkmcnt(0)\n"
    "s_lshr_b64 s[52:53], s[52:53], s42\n"
    "s_lshl_b32 s44, s52, 16\n"
    "s_and_b32 s45, s52, 0xffff0000\n"
    "s_lshl_b32 s48, s53, 16\n"
    "s_branch STG0\n"
    "EPI:\n"
    "s_setprio 2\n"
    "v_mul_f32 v120, v121, v114\n"
    "v_mul_f32 v114, s44, v114\n"
    "v_mul_f32 v122, v123, v116\n"
    "v_mul_f32 v116, s45, v116\n"
    "v_mul_f32 v124, v125, v118\n"
    "v_mul_f32 v118, s48, v118\n"
    "v_fma_mix_f32 v70, v94, v114, 0 op_sel:[1,0,0] op_sel_hi:[1,0,0]\n"
    "v_fma_mix_f32 v71, v94, v114, 0 op_sel_hi:[1,0,0]\n"
    "v_fma_mix_f32 v72, v95, v116, 0 op_sel:[1,0,0] op_sel_hi:[1,0,0]\n"
    "v_fma_mix_f32 v73, v95, v116, 0 op_sel_hi:[1,0,0]\n"
    "v_fma_mix_f32 v74, v96, v118, 0 op_sel:[1,0,0] op_sel_hi:[1,0,0]\n"
    "v_fma_mix_f32 v75, v96, v118, 0 op_sel_hi:[1,0,0]\n"
    "v_cvt_pk_bf16_f32 v70, 0, v70\n"
    "v_cvt_pk_bf16_f32 v71, 0, v71\n"
    "v_cvt_pk_bf16_f32 v72, 0, v72\n"
    "v_cvt_pk_bf16_f32 v73, 0, v73\n"
    "v_cvt_pk_bf16_f32 v74, 0, v74\n"
    "v_cvt_pk_bf16_f32 v75, 0, v75\n"
    "v_fma_mix_f32 v76, v97, v114, 0 op_sel:[1,0,0] op_sel_hi:[1,0,0]\n"
    "v_fma_mix_f32 v77, v97, v114, 0 op_sel_hi:[1,0,0]\n"
    "v_fma_mix_f32 v78, v98, v116, 0 op_sel:[1,0,0] op_sel_hi:[1,0,0]\n"
    "v_fma_mix_f32 v79, v98, v116, 0 op_sel_hi:[1,0,0]\n"
    "v_fma_mix_f32 v80, v99, v118, 0 op_sel:[1,0,0] op_sel_hi:[1,0,0]\n"
    "v_fma_mix_f32 v81, v99, v118, 0 op_sel_hi:[1,0,0]\n"
    "v_cvt_pk_bf16_f32 v76, 0, v76\n"
    "v_cvt_pk_bf16_f32 v77, 0, v77\n"
    "v_cvt_pk_bf16_f32 v78, 0, v78\n"
    "v_cvt_pk_bf16_f32 v79, 0, v79\n"
    "v_cvt_pk_bf16_f32 v80, 0, v80\n"
    "v_cvt_pk_bf16_f32 v81, 0, v81\n"
    "s_cmpk_lt_u32 s37, 14\n"
    "s_cbranch_scc1 EP1\n"
    "ds_read_b32 v24, v36 offset:0\n"
    "ds_read_b32 v25, v36 offset:4576\n"
    "ds_read_b32 v26, v36 offset:9152\n"
    "s_waitcnt lgkmcnt(0)\n"
    "v_fma_mix_f32 v64, v24, v114, 0 op_sel:[1,0,0] op_sel_hi:[1,0,0]\n"
    "v_fma_mix_f32 v65, v24, v114, 0 op_sel_hi:[1,0,0]\n"
    "v_fma_mix_f32 v66, v25, v116, 0 op_sel:[1,0,0] op_sel_hi:[1,0,0]\n"
    "v_fma_mix_f32 v67, v25, v116, 0 op_sel_hi:[1,0,0]\n"
    "v_fma_mix_f32 v68, v26, v118, 0 op_sel:[1,0,0] op_sel_hi:[1,0,0]\n"
    "v_fma_mix_f32 v69, v26, v118, 0 op_sel_hi:[1,0,0]\n"
    "v_cvt_pk_bf16_f32 v64, 0, v64\n"
    "v_cvt_pk_bf16_f32 v65, 0, v65\n"
    "v_cvt_pk_bf16_f32 v66, 0, v66\n"
    "v_cvt_pk_bf16_f32 v67, 0, v67\n"
    "v_cvt_pk_bf16_f32 v68, 0, v68\n"
    "v_cvt_pk_bf16_f32 v69, 0, v69\n"
    "s_branch E_30\n"
    "R_29:\n"
    "s_sleep 1\n"
    "s_sub_u32 s39, s39, 1\n"
    "s_cbranch_scc1 ABORT\n"
    "E_30:\n"
    "ds_read_b32 v22, v33 offset:64\n"
    "s_waitcnt lgkmcnt(0)\n"
    "v_cmp_ne_u32 vcc, 0x010101, v22\n"
    "s_cbranch_vccnz R_29\n"
    "ds_read_b64 v[48:49], v30 offset:0\n"
    "ds_read_b64 v[50:51], v30 offset:10240\n"
    "ds_read_b64 v[52:53], v30 offset:20480\n"
    "s_waitcnt lgkmcnt(0)\n"
    "v_cvt_pk_bf16_f32 v48, 0, v48\n"
    "v_cvt_pk_bf16_f32 v49, 0, v49\n"
    "v_cvt_pk_bf16_f32 v50, 0, v50\n"
    "v_cvt_pk_bf16_f32 v51, 0, v51\n"
    "v_cvt_pk_bf16_f32 v52, 0, v52\n"
    "v_cvt_pk_bf16_f32 v53, 0, v53\n"
    "v_fma_f32 v48, v48, v120, v64\n"
    "v_fma_f32 v49, v49, v120, v65\n"
    "v_fma_f32 v50, v50, v122, v66\n"
    "v_fma_f32 v51, v51, v122, v67\n"
    "v_fma_f32 v52, v52, v124, v68\n"
    "v_fma_f32 v53, v53, v124, v69\n"
    "v_cvt_pk_bf16_f32 v48, 0, v48\n"
    "v_cvt_pk_bf16_f32 v49, 0, v49\n"
    "v_cvt_pk_bf16_f32 v50, 0, v50\n"
    "v_cvt_pk_bf16_f32 v51, 0, v51\n"
    "v_cvt_pk_bf16_f32 v52, 0, v52\n"
    "v_cvt_pk_bf16_f32 v53, 0, v53\n"
    "v_mul_f32 v48, v48, v115\n"
    "v_mul_f32 v49, v49, v115\n"
    "v_mul_f32 v50, v50, v117\n"
    "v_mul_f32 v51, v51, v117\n"
    "v_mul_f32 v52, v52, v119\n"
    "v_mul_f32 v53, v53, v119\n"
    "v_cvt_pk_bf16_f32 v40, v48, v49\n"
    "v_cvt_pk_bf16_f32 v41, v50, v51\n"
    "v_cvt_pk_bf16_f32 v42, v52, v53\n"
    "buffer_store_dwordx4 v[40:43], v1, s[20:23], s49 offen sc1\n"
    "s_mov_b64 exec, 1\n"
    "ds_add_u32 v14, v15 offset:60\n"
    "s_mov_b64 exec, -1\n"
    "EP1:\n"
    "s_cmpk_ge_u32 s37, 2\n"
    "s_cbranch_scc1 EP1F\n"
    "s_branch E_32\n"
    "R_31:\n"
    "s_sleep 1\n"
    "s_sub_u32 s39, s39, 1\n"
    "s_cbranch_scc1 ABORT\n"
    "E_32:\n"
    "ds_read_b32 v22, v34 offset:64\n"
    "s_waitcnt lgkmcnt(0)\n"
    "v_cmp_ne_u32 vcc, 0x010101, v22\n"
    "s_cbranch_vccnz R_31\n"
    "ds_read_b64 v[48:49], v31 offset:0\n"
    "ds_read_b64 v[50:51], v31 offset:10240\n"
    "ds_read_b64 v[52:53], v31 offset:20480\n"
    "s_waitcnt lgkmcnt(0)\n"
    "s_branch EP1M\n"
    "EP1F:\n"
    "s_branch E_34\n"
    "R_33:\n"
    "s_sleep 1\n"
    "s_sub_u32 s39, s39, 1\n"
    "s_cbranch_scc1 ABORT\n"
    "E_34:\n"
    "ds_read_b64 v[22:23], v34 offset:64\n"
    "s_waitcnt lgkmcnt(0)\n"
    "v_cmp_ne_u64 vcc, s[46:47], v[22:23]\n"
    "s_cbranch_vccnz R_33\n"
    "ds_read_b64 v[48:49], v31 offset:0\n"
    "ds_read_b64 v[54:55], v31 offset:28160\n"
    "ds_read_b64 v[50:51], v31 offset:10240\n"
    "ds_read_b64 v[56:57], v31 offset:35840\n"
    "ds_read_b64 v[52:53], v31 offset:20480\n"
    "ds_read_b64 v[58:59], v31 offset:43520\n"
    "s_waitcnt lgkmcnt(0)\n"
    "v_add_f32 v48, v48, v54\n"
    "v_add_f32 v49, v49, v55\n"
    "v_add_f32 v50, v50, v56\n"
    "v_add_f32 v51, v51, v57\n"
    "v_add_f32 v52, v52, v58\n"
    "v_add_f32 v53, v53, v59\n"
    "EP1M:\n"
    "v_cvt_pk_bf16_f32 v48, 0, v48\n"
    "v_cvt_pk_bf16_f32 v49, 0, v49\n"
    "v_cvt_pk_bf16_f32 v50, 0, v50\n"
    "v_cvt_pk_bf16_f32 v51, 0, v51\n"
    "v_cvt_pk_bf16_f32 v52, 0, v52\n"
    "v_cvt_pk_bf16_f32 v53, 0, v53\n"
    "v_fma_f32 v48, v48, v120, v70\n"
    "v_fma_f32 v49, v49, v120, v71\n"
    "v_fma_f32 v50, v50, v122, v72\n"
    "v_fma_f32 v51, v51, v122, v73\n"
    "v_fma_f32 v52, v52, v124, v74\n"
    "v_fma_f32 v53, v53, v124, v75\n"
    "v_cvt_pk_bf16_f32 v48, 0, v48\n"
    "v_cvt_pk_bf16_f32 v49, 0, v49\n"
    "v_cvt_pk_bf16_f32 v50, 0, v50\n"
    "v_cvt_pk_bf16_f32 v51, 0, v51\n"
    "v_cvt_pk_bf16_f32 v52, 0, v52\n"
    "v_cvt_pk_bf16_f32 v53, 0, v53\n"
    "v_mul_f32 v48, v48, v115\n"
    "v_mul_f32 v49, v49, v115\n"
    "v_mul_f32 v50, v50, v117\n"
    "v_mul_f32 v51, v51, v117\n"
    "v_mul_f32 v52, v52, v119\n"
    "v_mul_f32 v53, v53, v119\n"
    "v_cvt_pk_bf16_f32 v40, v48, v49\n"
    "v_cvt_pk_bf16_f32 v41, v50, v51\n"
    "v_cvt_pk_bf16_f32 v42, v52, v53\n"
    "buffer_store_dwordx4 v[40:43], v1, s[20:23], s50 offen sc1\n"
    "s_mov_b64 exec, 1\n"
    "ds_add_u32 v14, v15 offset:60\n"
    "s_mov_b64 exec, -1\n"
    "s_cmpk_ge_u32 s37, 12\n"
    "s_cbranch_scc1 EP2\n"
    "s_waitcnt vmcnt(1)\n"
    "s_mov_b64 exec, 1\n"
    "ds_add_u32 v14, v15 offset:56\n"
    "s_mov_b64 exec, -1\n"
    "EP2:\n"
    "s_branch E_36\n"
    "R_35:\n"
    "s_sleep 1\n"
    "s_sub_u32 s39, s39, 1\n"
    "s_cbranch_scc1 ABORT\n"
    "E_36:\n"
    "ds_read_b64 v[22:23], v35 offset:56\n"
    "s_waitcnt lgkmcnt(0)\n"
    "v_cmp_ne_u64 vcc, s[46:47], v[22:23]\n"
    "s_cbranch_vccnz R_35\n"
    "s_branch E_38\n"
    "R_37:\n"
    "s_sub_u32 s39, s39, 1\n"
    "s_cbranch_scc1 ABORT\n"
    "E_38:\n"
    "ds_read_b64 v[22:23], v35 offset:64\n"
    "ds_read_b64 v[48:49], v32 offset:0\n"
    "ds_read_b64 v[54:55], v32 offset:28160\n"
    "ds_read_b64 v[50:51], v32 offset:10240\n"
    "ds_read_b64 v[56:57], v32 offset:35840\n"
    "ds_read_b64 v[52:53], v32 offset:20480\n"
    "ds_read_b64 v[58:59], v32 offset:43520\n"
    "s_waitcnt lgkmcnt(0)\n"
    "v_cmp_ne_u64 vcc, s[46:47], v[22:23]\n"
    "s_cbranch_vccnz R_37\n"
    "s_cmpk_ge_u32 s37, 14\n"
    "s_cbranch_scc1 EPZ\n"
    "v_add_f32 v48, v48, v54\n"
    "v_add_f32 v49, v49, v55\n"
    "v_add_f32 v50, v50, v56\n"
    "v_add_f32 v51, v51, v57\n"
    "v_add_f32 v52, v52, v58\n"
    "v_add_f32 v53, v53, v59\n"
    "v_cvt_pk_bf16_f32 v48, 0, v48\n"
    "v_cvt_pk_bf16_f32 v49, 0, v49\n"
    "v_cvt_pk_bf16_f32 v50, 0, v50\n"
    "v_cvt_pk_bf16_f32 v51, 0, v51\n"
    "v_cvt_pk_bf16_f32 v52, 0, v52\n"
    "v_cvt_pk_bf16_f32 v53, 0, v53\n"
    "v_fma_f32 v48, v48, v120, v76\n"
    "v_fma_f32 v49, v49, v120, v77\n"
    "v_fma_f32 v50, v50, v122, v78\n"
    "v_fma_f32 v51, v51, v122, v79\n"
    "v_fma_f32 v52, v52, v124, v80\n"
    "v_fma_f32 v53, v53, v124, v81\n"
    "v_cvt_pk_bf16_f32 v48, 0, v48\n"
    "v_cvt_pk_bf16_f32 v49, 0, v49\n"
    "v_cvt_pk_bf16_f32 v50, 0, v50\n"
    "v_cvt_pk_bf16_f32 v51, 0, v51\n"
    "v_cvt_pk_bf16_f32 v52, 0, v52\n"
    "v_cvt_pk_bf16_f32 v53, 0, v53\n"
    "v_mul_f32 v48, v48, v115\n"
    "v_mul_f32 v49, v49, v115\n"
    "v_mul_f32 v50, v50, v117\n"
    "v_mul_f32 v51, v51, v117\n"
    "v_mul_f32 v52, v52, v119\n"
    "v_mul_f32 v53, v53, v119\n"
    "v_cvt_pk_bf16_f32 v40, v48, v49\n"
    "v_cvt_pk_bf16_f32 v41, v50, v51\n"
    "v_cvt_pk_bf16_f32 v42, v52, v53\n"
    "buffer_store_dwordx4 v[40:43], v1, s[20:23], s51 offen sc1\n"
    "s_mov_b64 exec, 1\n"
    "ds_add_u32 v14, v15 offset:60\n"
    "s_mov_b64 exec, -1\n"
    "s_cmpk_ge_u32 s37, 12\n"
    "s_cbranch_scc1 LN2K\n"
    "s_setprio 0\n"
    "s_branch LN2\n"
    "EPZ:\n"
    "s_setprio 3\n"
    "v_pk_add_f32 v[48:49], v[48:49], v[54:55]\n"
    "v_pk_add_f32 v[50:51], v[50:51], v[56:57]\n"
    "v_pk_add_f32 v[52:53], v[52:53], v[58:59]\n"
    "v_cvt_pk_bf16_f32 v48, 0, v48\n"
    "v_cvt_pk_bf16_f32 v49, 0, v49\n"
    "v_cvt_pk_bf16_f32 v50, 0, v50\n"
    "v_cvt_pk_bf16_f32 v51, 0, v51\n"
    "v_cvt_pk_bf16_f32 v52, 0, v52\n"
    "v_cvt_pk_bf16_f32 v53, 0, v53\n"
    "v_pk_fma_f32 v[48:49], v[48:49], v[120:121], v[76:77] op_sel_hi:[1,0,1]\n"
    "v_pk_fma_f32 v[50:51], v[50:51], v[122:123], v[78:79] op_sel_hi:[1,0,1]\n"
    "v_pk_fma_f32 v[52:53], v[52:53], v[124:125], v[80:81] op_sel_hi:[1,0,1]\n"
    "v_cvt_pk_bf16_f32 v48, 0, v48\n"
    "v_cvt_pk_bf16_f32 v49, 0, v49\n"
    "v_cvt_pk_bf16_f32 v50, 0, v50\n"
    "v_cvt_pk_bf16_f32 v51, 0, v51\n"
    "v_cvt_pk_bf16_f32 v52, 0, v52\n"
    "v_cvt_pk_bf16_f32 v53, 0, v53\n"
    "v_pk_mul_f32 v[48:49], v[114:115], v[48:49] op_sel:[1,0] op_sel_hi:[1,1]\n"
    "v_pk_mul_f32 v[50:51], v[116:117], v[50:51] op_sel:[1,0] op_sel_hi:[1,1]\n"
    "v_pk_mul_f32 v[52:53], v[118:119], v[52:53] op_sel:[1,0] op_sel_hi:[1,1]\n"
    "v_cvt_pk_bf16_f32 v40, v48, v49\n"
    "v_cvt_pk_bf16_f32 v41, v50, v51\n"
    "v_cvt_pk_bf16_f32 v42, v52, v53\n"
    "buffer_store_dwordx4 v[40:43], v1, s[20:23], s51 offen sc1\n"
    "s_mov_b64 exec, 1\n"
    "ds_add_u32 v14, v15 offset:60\n"
    "s_mov_b64 exec, -1\n"
    "s_cmpk_lg_u32 s37, 14\n"
    "s_cbranch_scc1 LN2K\n"
    "s_memrealtime s[98:99]\n"
    "s_lshl_b32 s42, s2, 7\n"
    "s_add_u32 s42, s42, 94208\n"
    "s_waitcnt lgkmcnt(0)\n"
    "v_mov_b32 v44, s98\n"
    "v_mov_b32 v45, v43\n"
    "s_mov_b64 exec, 1\n"
    "buffer_store_dwordx2 v[44:45], off, s[20:23], s42 sc1\n"
    "s_mov_b64 exec, -1\n"
    "s_branch LN2K\n"
    "MISC:\n"
    "s_cmpk_ge_u32 s2, 128\n"
    "s_cbranch_scc1 CDONE\n"
    "v_and_b32 v109, 255, v0\n"
    "v_add_u32 v104, 192, v109\n"
    "v_min_u32 v104, 287, v104\n"
    "v_lshlrev_b32 v105, 2, v109\n"
    "v_lshlrev_b32 v106, 2, v104\n"
    "v_lshlrev_b32 v107, 4, v109\n"
    "v_lshlrev_b32 v108, 4, v104\n"
    "s_mov_b32 s44, 34304\n"
    "s_mov_b32 s45, 61952\n"
    "s_mov_b32 s46, 38912\n"
    "s_mov_b32 s47, 66560\n"
    "s_mov_b32 s48, 43520\n"
    "s_mov_b32 s49, 71168\n"
    "v_mov_b32 v14, 147968\n"
    "ds_read_b32 v4, v14 offset:480\n"
    "s_waitcnt lgkmcnt(0)\n"
    "v_readfirstlane_b32 s96, v4\n"
    "s_mov_b64 exec, -1\n"
    "global_load_dword v45, v105, s[26:27]\n"
    "global_load_dword v46, v106, s[26:27]\n"
    "s_setprio 1\n"
    "U_39:\n"
    "s_memrealtime s[98:99]\n"
    "s_waitcnt lgkmcnt(0)\n"
    "s_sub_u32 s98, s98, s96\n"
    "s_cmpk_lt_u32 s98, 350\n"
    "s_cbranch_scc1 U_39\n"
    "buffer_load_dwordx4 v[48:51], v107, s[20:23], s44 offen sc1\n"
    "buffer_load_dwordx4 v[52:55], v107, s[20:23], s45 offen sc1\n"
    "buffer_load_dwordx4 v[56:59], v107, s[20:23], s46 offen sc1\n"
    "buffer_load_dwordx4 v[60:63], v107, s[20:23], s47 offen sc1\n"
    "buffer_load_dwordx4 v[64:67], v107, s[20:23], s48 offen sc1\n"
    "buffer_load_dwordx4 v[68:71], v107, s[20:23], s49 offen sc1\n"
    "buffer_load_dwordx4 v[72:75], v108, s[20:23], s44 offen sc1\n"
    "buffer_load_dwordx4 v[76:79], v108, s[20:23], s45 offen sc1\n"
    "buffer_load_dwordx4 v[80:83], v108, s[20:23], s46 offen sc1\n"
    "buffer_load_dwordx4 v[84:87], v108, s[20:23], s47 offen sc1\n"
    "buffer_load_dwordx4 v[88:91], v108, s[20:23], s48 offen sc1\n"
    "buffer_load_dwordx4 v[92:95], v108, s[20:23], s49 offen sc1\n"
    "v_and_b32 v1, 63, v0\n"
    "v_lshlrev_b32 v1, 4, v1\n"
    "s_mov_b64 exec, -1\n"
    "v_mov_b32 v8, 0\n"
    "v_mov_b32 v9, 0\n"
    "v_mov_b32 v10, 0\n"
    "v_mov_b32 v11, 0\n"
    "v_lshrrev_b32 v2, 9, v1\n"
    "v_mul_u32_u24 v2, 1536, v2\n"
    "v_add_u32 v2, v2, v1\n"
    "v_add_u32 v2, 134048, v2\n"
    "ds_write_b128 v2, v[8:11]\n"
    "s_mov_b32 s42, 138240\n"
    "v_lshl_add_u32 v1, v107, 1, s42\n"
    "v_lshl_add_u32 v2, v108, 1, s42\n"
    "v_add_u32 v41, 72320, v105\n"
    "v_add_u32 v42, 72320, v106\n"
    "v_mov_b32 v15, 1\n"
    "v_mov_b32 v14, 147968\n"
    "s_branch E_41\n"
    "R_40:\n"
    "s_sub_u32 s39, s39, 1\n"
    "s_cbranch_scc1 ABORT\n"
    "s_sleep 4\n"
    "buffer_load_dwordx4 v[48:51], v107, s[20:23], s44 offen sc1\n"
    "buffer_load_dwordx4 v[52:55], v107, s[20:23], s45 offen sc1\n"
    "buffer_load_dwordx4 v[56:59], v107, s[20:23], s46 offen sc1\n"
    "buffer_load_dwordx4 v[60:63], v107, s[20:23], s47 offen sc1\n"
    "buffer_load_dwordx4 v[64:67], v107, s[20:23], s48 offen sc1\n"
    "buffer_load_dwordx4 v[68:71], v107, s[20:23], s49 offen sc1\n"
    "buffer_load_dwordx4 v[72:75], v108, s[20:23], s44 offen sc1\n"
    "buffer_load_dwordx4 v[76:79], v108, s[20:23], s45 offen sc1\n"
    "buffer_load_dwordx4 v[80:83], v108, s[20:23], s46 offen sc1\n"
    "buffer_load_dwordx4 v[84:87], v108, s[20:23], s47 offen sc1\n"
    "buffer_load_dwordx4 v[88:91], v108, s[20:23], s48 offen sc1\n"
    "buffer_load_dwordx4 v[92:95], v108, s[20:23], s49 offen sc1\n"
    "E_41:\n"
    "s_waitcnt vmcnt(0)\n"
    "v_bitop3_b32 v16, v49, v51, s36 bitop3:0x7e\n"
    "v_bitop3_b32 v17, v53, v55, s36 bitop3:0x7e\n"
    "v_bitop3_b32 v18, v57, v59, s36 bitop3:0x7e\n"
    "v_bitop3_b32 v19, v61, v63, s36 bitop3:0x7e\n"
    "v_bitop3_b32 v20, v65, v67, s36 bitop3:0x7e\n"
    "v_bitop3_b32 v21, v69, v71, s36 bitop3:0x7e\n"
    "v_bitop3_b32 v22, v73, v75, s36 bitop3:0x7e\n"
    "v_bitop3_b32 v23, v77, v79, s36 bitop3:0x7e\n"
    "v_bitop3_b32 v24, v81, v83, s36 bitop3:0x7e\n"
    "v_bitop3_b32 v25, v85, v87, s36 bitop3:0x7e\n"
    "v_bitop3_b32 v26, v89, v91, s36 bitop3:0x7e\n"
    "v_bitop3_b32 v27, v93, v95, s36 bitop3:0x7e\n"
    "v_or3_b32 v16, v16, v17, v18\n"
    "v_or3_b32 v19, v19, v20, v21\n"
    "v_or3_b32 v22, v22, v23, v24\n"
    "v_or3_b32 v25, v25, v26, v27\n"
    "v_or3_b32 v16, v16, v19, v22\n"
    "v_or_b32 v16, v16, v25\n"
    "v_cmp_ne_u32 vcc, 0, v16\n"
    "s_cbranch_vccnz R_40\n"
    "ds_write_b32 v41, v45\n"
    "v_add_f32 v16, v56, v60\n"
    "v_add_f32 v17, v48, v52\n"
    "v_add_f32 v19, v64, v68\n"
    "v_cvt_pk_bf16_f32 v16, 0, v16\n"
    "v_cvt_pk_bf16_f32 v17, 0, v17\n"
    "v_cvt_pk_bf16_f32 v19, 0, v19\n"
    "v_add_f32 v16, 1.0, v16\n"
    "v_lshlrev_b32 v32, 16, v45\n"
    "v_cvt_pk_bf16_f32 v16, 0, v16\n"
    "v_mul_f32 v18, v16, v32\n"
    "ds_write_b128 v1, v[16:19] offset:0\n"
    "v_add_f32 v20, v58, v62\n"
    "v_add_f32 v21, v50, v54\n"
    "v_add_f32 v23, v66, v70\n"
    "v_cvt_pk_bf16_f32 v20, 0, v20\n"
    "v_cvt_pk_bf16_f32 v21, 0, v21\n"
    "v_cvt_pk_bf16_f32 v23, 0, v23\n"
    "v_add_f32 v20, 1.0, v20\n"
    "v_and_b32 v33, 0xffff0000, v45\n"
    "v_cvt_pk_bf16_f32 v20, 0, v20\n"
    "v_mul_f32 v22, v20, v33\n"
    "ds_write_b128 v1, v[20:23] offset:16\n"
    "ds_write_b32 v42, v46\n"
    "v_add_f32 v24, v80, v84\n"
    "v_add_f32 v25, v72, v76\n"
    "v_add_f32 v27, v88, v92\n"
    "v_cvt_pk_bf16_f32 v24, 0, v24\n"
    "v_cvt_pk_bf16_f32 v25, 0, v25\n"
    "v_cvt_pk_bf16_f32 v27, 0, v27\n"
    "v_add_f32 v24, 1.0, v24\n"
    "v_lshlrev_b32 v34, 16, v46\n"
    "v_cvt_pk_bf16_f32 v24, 0, v24\n"
    "v_mul_f32 v26, v24, v34\n"
    "ds_write_b128 v2, v[24:27] offset:0\n"
    "v_add_f32 v28, v82, v86\n"
    "v_add_f32 v29, v74, v78\n"
    "v_add_f32 v31, v90, v94\n"
    "v_cvt_pk_bf16_f32 v28, 0, v28\n"
    "v_cvt_pk_bf16_f32 v29, 0, v29\n"
    "v_cvt_pk_bf16_f32 v31, 0, v31\n"
    "v_add_f32 v28, 1.0, v28\n"
    "v_and_b32 v35, 0xffff0000, v46\n"
    "v_cvt_pk_bf16_f32 v28, 0, v28\n"
    "v_mul_f32 v30, v28, v35\n"
    "ds_write_b128 v2, v[28:31] offset:16\n"
    "s_mov_b64 exec, 1\n"
    "ds_add_u32 v14, v15 offset:56\n"
    "s_mov_b64 exec, -1\n"
    "s_setprio 0\n"
    "v_cmp_gt_u32 vcc, 0x60, v109\n"
    "v_mul_f32 v32, v32, v17\n"
    "v_mul_f32 v33, v33, v21\n"
    "v_add_f32 v4, v18, v22\n"
    "v_mul_f32 v5, v18, v18\n"
    "v_fmac_f32 v5, v22, v22\n"
    "v_mul_f32 v5, 4.0, v5\n"
    "v_fmac_f32 v5, v32, v32\n"
    "v_fmac_f32 v5, v33, v33\n"
    "v_mul_f32 v34, v34, v25\n"
    "v_mul_f32 v35, v35, v29\n"
    "v_add_f32 v6, v26, v30\n"
    "v_mul_f32 v7, v26, v26\n"
    "v_fmac_f32 v7, v30, v30\n"
    "v_mul_f32 v7, 4.0, v7\n"
    "v_fmac_f32 v7, v34, v34\n"
    "v_fmac_f32 v7, v35, v35\n"
    "v_cndmask_b32 v6, 0, v6, vcc\n"
    "v_cndmask_b32 v7, 0, v7, vcc\n"
    "v_add_f32 v4, v4, v6\n"
    "v_add_f32 v5, v5, v7\n"
    "v_lshrrev_b32 v3, 4, v109\n"
    "v_lshlrev_b32 v3, 3, v3\n"
    "v_add_u32 v3, 148736, v3\n"
    "v_add_f32_dpp v4, v4, v4 row_ror:8 row_mask:0xf bank_mask:0xf\n"
    "v_add_f32_dpp v5, v5, v5 row_ror:8 row_mask:0xf bank_mask:0xf\n"
    "s_nop 0\n"
    "v_add_f32_dpp v4, v4, v4 row_ror:4 row_mask:0xf bank_mask:0xf\n"
    "v_add_f32_dpp v5, v5, v5 row_ror:4 row_mask:0xf bank_mask:0xf\n"
    "s_nop 0\n"
    "v_add_f32_dpp v4, v4, v4 row_ror:2 row_mask:0xf bank_mask:0xf\n"
    "v_add_f32_dpp v5, v5, v5 row_ror:2 row_mask:0xf bank_mask:0xf\n"
    "s_nop 0\n"
    "v_add_f32_dpp v4, v4, v4 row_ror:1 row_mask:0xf bank_mask:0xf\n"
    "v_add_f32_dpp v5, v5, v5 row_ror:1 row_mask:0xf bank_mask:0xf\n"
    "ds_write_b64 v3, v[4:5]\n"
    "LN2:\n"
    "s_cmpk_ge_u32 s2, 128\n"
    "s_cbranch_scc1 CDONE\n"
    "s_and_b32 s57, s2, 7\n"
    "s_lshl_b32 s57, s57, 4\n"
    "s_lshr_b32 s43, s2, 3\n"
    "s_add_u32 s57, s57, s43\n"
    "v_and_b32 v2, 7, v0\n"
    "v_lshrrev_b32 v3, 3, v0\n"
    "v_mul_u32_u24 v6, 12, v3\n"
    "s_movk_i32 s44, 0x900\n"
    "v_mad_u32_u24 v4, v2, s44, v6\n"
    "v_lshlrev_b32 v2, 4, v2\n"
    "v_add_u32 v4, v2, v4\n"
    "v_add_u32 v5, 72320, v6\n"
    "s_mov_b32 s44, 33024\n"
    "v_mad_u32_u24 v1, v3, s44, v2\n"
    "s_lshl_b32 s42, s57, 7\n"
    "s_add_u32 s42, s42, 131072\n"
    "s_add_u32 s43, s42, 16512\n"
    "s_mov_b32 s44, 138240\n"
    "v_lshl_add_u32 v6, v6, 3, s44\n"
    "v_mov_b32 v7, 147968\n"
    "v_and_b32 v16, 8, v0\n"
    "v_lshl_add_u32 v16, v16, 8, v2\n"
    "v_bfe_u32 v17, v0, 4, 2\n"
    "v_lshlrev_b32 v18, 2, v17\n"
    "s_lshl_b32 s44, s37, 7\n"
    "s_add_u32 s44, s44, 132512\n"
    "v_add3_u32 v16, v16, v18, s44\n"
    "v_and_b32 v18, 2, v17\n"
    "v_lshl_add_u32 v18, v18, 2, v2\n"
    "s_lshl_b32 s44, s37, 2\n"
    "s_add_u32 s44, s44, 136608\n"
    "v_mad_u32_u24 v18, v18, 6, s44\n"
    "v_and_b32 v17, 63, v0\n"
    "s_mov_b32 s44, 132512\n"
    "v_lshl_add_u32 v17, v17, 4, s44\n"
    "v_mul_u32_u24 v22, 192, v2\n"
    "v_lshl_add_u32 v22, v3, 4, v22\n"
    "s_mul_i32 s45, s57, 24576\n"
    "s_add_u32 s45, s45, 7341056\n"
    "s_lshr_b32 s44, s57, 4\n"
    "s_lshl_b32 s44, s44, 1\n"
    "s_add_u32 s44, s44, 2\n"
    "s_branch E_43\n"
    "R_42:\n"
    "s_sleep 2\n"
    "s_sub_u32 s39, s39, 1\n"
    "s_cbranch_scc1 ABORT\n"
    "E_43:\n"
    "ds_read_b32 v19, v7 offset:56\n"
    "s_waitcnt lgkmcnt(0)\n"
    "v_cmp_ne_u32 vcc, 6, v19\n"
    "s_cbranch_vccnz R_42\n"
    "ds_read_b32 v8, v4 offset:53760\n"
    "ds_read_b32 v9, v4 offset:53764\n"
    "ds_read_b32 v10, v4 offset:53768\n"
    "ds_read_b32 v12, v4 offset:54912\n"
    "ds_read_b32 v13, v4 offset:54916\n"
    "ds_read_b32 v14, v4 offset:54920\n"
    "ds_read_b128 v[40:43], v6 offset:0\n"
    "ds_read_b128 v[44:47], v6 offset:16\n"
    "ds_read_b128 v[48:51], v6 offset:32\n"
    "ds_read_b128 v[52:55], v6 offset:48\n"
    "ds_read_b128 v[56:59], v6 offset:64\n"
    "ds_read_b128 v[60:63], v6 offset:80\n"
    "ds_read2_b32 v[36:37], v5 offset1:1\n"
    "ds_read_b32 v38, v5 offset:8\n"
    "s_waitcnt lgkmcnt(8)\n"
    "v_lshlrev_b32 v24, 16, v8\n"
    "v_lshlrev_b32 v25, 16, v12\n"
    "v_and_b32 v26, 0xffff0000, v8\n"
    "v_and_b32 v27, 0xffff0000, v12\n"
    "v_lshlrev_b32 v28, 16, v9\n"
    "v_lshlrev_b32 v29, 16, v13\n"
    "v_and_b32 v30, 0xffff0000, v9\n"
    "v_and_b32 v31, 0xffff0000, v13\n"
    "v_lshlrev_b32 v32, 16, v10\n"
    "v_lshlrev_b32 v33, 16, v14\n"
    "v_and_b32 v34, 0xffff0000, v10\n"
    "v_and_b32 v35, 0xffff0000, v14\n"
    "s_branch E_45\n"
    "R_44:\n"
    "s_sleep 1\n"
    "s_sub_u32 s39, s39, 1\n"
    "s_cbranch_scc1 ABORT\n"
    "E_45:\n"
    "ds_read_b32 v19, v7 offset:60\n"
    "s_waitcnt lgkmcnt(0)\n"
    "v_cmp_gt_u32 vcc, s44, v19\n"
    "s_cbranch_vccnz R_44\n"
    "s_cmpk_lt_u32 s57, 96\n"
    "s_cbranch_scc1 LN2g\n"
    "s_sleep 8\n"
    "LN2g:\n"
    "buffer_load_dwordx4 v[72:75], v1, s[20:23], s42 offen sc1\n"
    "buffer_load_dwordx4 v[76:79], v1, s[20:23], s43 offen sc1\n"
    "s_branch E_47\n"
    "R_46:\n"
    "s_sub_u32 s39, s39, 1\n"
    "s_cbranch_scc1 ABORT\n"
    "s_wqm_b64 exec, vcc\n"
    "buffer_load_dwordx4 v[72:75], v1, s[20:23], s42 offen sc1\n"
    "buffer_load_dwordx4 v[76:79], v1, s[20:23], s43 offen sc1\n"
    "s_mov_b64 exec, -1\n"
    "E_47:\n"
    "s_waitcnt vmcnt(0)\n"
    "v_bitop3_b32 v19, v75, v79, s36 bitop3:0x7e\n"
    "v_cmp_ne_u32 vcc, 0, v19\n"
    "s_cbranch_vccnz R_46\n"
    "v_lshlrev_b32 v20, 16, v72\n"
    "v_and_b32 v21, 0xffff0000, v72\n"
    "v_pk_add_f32 v[24:25], v[24:25], v[20:21]\n"
    "v_cvt_pk_bf16_f32 v24, 0, v24\n"
    "v_cvt_pk_bf16_f32 v25, 0, v25\n"
    "v_pk_mul_f32 v[10:11], v[24:25], v[24:25]\n"
    "v_pk_mul_f32 v[12:13], v[24:25], v[42:43] op_sel_hi:[1,0]\n"
    "v_lshlrev_b32 v20, 16, v73\n"
    "v_and_b32 v21, 0xffff0000, v73\n"
    "v_pk_add_f32 v[26:27], v[26:27], v[20:21]\n"
    "v_cvt_pk_bf16_f32 v26, 0, v26\n"
    "v_cvt_pk_bf16_f32 v27, 0, v27\n"
    "v_pk_add_f32 v[8:9], v[26:27], v[24:25]\n"
    "v_pk_fma_f32 v[10:11], v[26:27], v[26:27], v[10:11]\n"
    "v_pk_fma_f32 v[12:13], v[26:27], v[46:47], v[12:13] op_sel:[0,0,0] op_sel_hi:[1,0,1]\n"
    "v_lshlrev_b32 v20, 16, v74\n"
    "v_and_b32 v21, 0xffff0000, v74\n"
    "v_pk_add_f32 v[28:29], v[28:29], v[20:21]\n"
    "v_cvt_pk_bf16_f32 v28, 0, v28\n"
    "v_cvt_pk_bf16_f32 v29, 0, v29\n"
    "v_pk_add_f32 v[8:9], v[28:29], v[8:9]\n"
    "v_pk_fma_f32 v[10:11], v[28:29], v[28:29], v[10:11]\n"
    "v_pk_fma_f32 v[12:13], v[28:29], v[50:51], v[12:13] op_sel:[0,0,0] op_sel_hi:[1,0,1]\n"
    "v_lshlrev_b32 v20, 16, v76\n"
    "v_and_b32 v21, 0xffff0000, v76\n"
    "v_pk_add_f32 v[30:31], v[30:31], v[20:21]\n"
    "v_cvt_pk_bf16_f32 v30, 0, v30\n"
    "v_cvt_pk_bf16_f32 v31, 0, v31\n"
    "v_pk_add_f32 v[8:9], v[30:31], v[8:9]\n"
    "v_pk_fma_f32 v[10:11], v[30:31], v[30:31], v[10:11]\n"
    "v_pk_fma_f32 v[12:13], v[30:31], v[54:55], v[12:13] op_sel:[0,0,0] op_sel_hi:[1,0,1]\n"
    "v_lshlrev_b32 v20, 16, v77\n"
    "v_and_b32 v21, 0xffff0000, v77\n"
    "v_pk_add_f32 v[32:33], v[32:33], v[20:21]\n"
    "v_cvt_pk_bf16_f32 v32, 0, v32\n"
    "v_cvt_pk_bf16_f32 v33, 0, v33\n"
    "v_pk_add_f32 v[8:9], v[32:33], v[8:9]\n"
    "v_pk_fma_f32 v[10:11], v[32:33], v[32:33], v[10:11]\n"
    "v_pk_fma_f32 v[12:13], v[32:33], v[58:59], v[12:13] op_sel:[0,0,0] op_sel_hi:[1,0,1]\n"
    "v_lshlrev_b32 v20, 16, v78\n"
    "v_and_b32 v21, 0xffff0000, v78\n"
    "v_pk_add_f32 v[34:35], v[34:35], v[20:21]\n"
    "v_cvt_pk_bf16_f32 v34, 0, v34\n"
    "v_cvt_pk_bf16_f32 v35, 0, v35\n"
    "v_pk_add_f32 v[8:9], v[34:35], v[8:9]\n"
    "v_pk_fma_f32 v[10:11], v[34:35], v[34:35], v[10:11]\n"
    "v_pk_fma_f32 v[12:13], v[34:35], v[62:63], v[12:13] op_sel:[0,0,0] op_sel_hi:[1,0,1]\n"
    "v_permlane32_swap_b32 v8, v9\n"
    "v_permlane32_swap_b32 v10, v11\n"
    "v_permlane32_swap_b32 v12, v13\n"
    "v_add_f32 v8, v8, v9\n"
    "v_add_f32 v10, v10, v11\n"
    "v_add_f32 v12, v12, v13\n"
    "s_nop 0\n"
    "v_permlane16_swap_b32 v8, v10\n"
    "v_add_f32 v8, v8, v10\n"
    "s_nop 1\n"
    "v_add_f32_dpp v9, v8, v8 row_ror:8 row_mask:0xf bank_mask:0x3\n"
    "s_nop 1\n"
    "v_add_f32_dpp v9, v12, v12 row_ror:8 row_mask:0xf bank_mask:0xc\n"
    "ds_write_b32 v16, v9\n"
    "s_waitcnt lgkmcnt(0)\n"
    "s_barrier\n"
    "ds_read_b128 v[8:11], v17\n"
    "ds_read_b128 v[12:15], v17 offset:1024\n"
    "s_waitcnt lgkmcnt(0)\n"
    "v_pk_add_f32 v[8:9], v[8:9], v[12:13]\n"
    "v_pk_add_f32 v[10:11], v[10:11], v[14:15]\n"
    "s_nop 1\n"
    "v_permlane32_swap_b32 v8, v10\n"
    "v_permlane32_swap_b32 v9, v11\n"
    "v_pk_add_f32 v[8:9], v[8:9], v[10:11]\n"
    "s_nop 1\n"
    "v_add_f32_dpp v8, v8, v8 row_ror:8 row_mask:0xf bank_mask:0xf\n"
    "v_add_f32_dpp v9, v9, v9 row_ror:8 row_mask:0xf bank_mask:0xf\n"
    "v_mov_b32 v10, v8\n"
    "v_mov_b32 v11, v9\n"
    "s_nop 0\n"
    "v_permlane16_swap_b32 v8, v10\n"
    "v_permlane16_swap_b32 v9, v11\n"
    "v_pk_add_f32 v[8:9], v[8:9], v[10:11]\n"
    "v_mul_f32 v8, 0x3ae38e39, v8\n"
    "v_mul_f32 v9, 0x3ae38e39, v9\n"
    "v_fma_f32 v9, -v8, v8, v9\n"
    "v_max_f32 v9, 0, v9\n"
    "v_cvt_pk_bf16_f32 v9, 0, v9\n"
    "v_add_f32 v9, 0x3727c5ac, v9\n"
    "v_cvt_pk_bf16_f32 v9, 0, v9\n"
    "v_sqrt_f32 v9, v9\n"
    "s_nop 0\n"
    "v_cvt_pk_bf16_f32 v8, v8, v9\n"
    "v_mov_b32 v9, v8\n"
    "s_nop 1\n"
    "v_permlane32_swap_b32 v8, v9\n"
    "v_and_b32 v12, 0xffff0000, v8\n"
    "v_and_b32 v13, 0xffff0000, v9\n"
    "v_lshlrev_b32 v10, 16, v8\n"
    "v_lshlrev_b32 v11, 16, v9\n"
    "v_rcp_f32 v12, v12\n"
    "v_rcp_f32 v13, v13\n"
    "v_pk_add_f32 v[80:81], v[24:25], v[10:11] neg_lo:[0,1] neg_hi:[0,1]\n"
    "v_pk_add_f32 v[82:83], v[26:27], v[10:11] neg_lo:[0,1] neg_hi:[0,1]\n"
    "v_cvt_pk_bf16_f32 v80, 0, v80\n"
    "v_cvt_pk_bf16_f32 v81, 0, v81\n"
    "v_cvt_pk_bf16_f32 v82, 0, v82\n"
    "v_cvt_pk_bf16_f32 v83, 0, v83\n"
    "v_pk_mul_f32 v[80:81], v[80:81], v[12:13]\n"
    "v_pk_mul_f32 v[82:83], v[82:83], v[12:13]\n"
    "v_cvt_pk_bf16_f32 v80, 0, v80\n"
    "v_cvt_pk_bf16_f32 v81, 0, v81\n"
    "v_cvt_pk_bf16_f32 v82, 0, v82\n"
    "v_cvt_pk_bf16_f32 v83, 0, v83\n"
    "v_pk_mul_f32 v[80:81], v[80:81], v[40:41] op_sel_hi:[1,0]\n"
    "v_pk_mul_f32 v[82:83], v[82:83], v[44:45] op_sel_hi:[1,0]\n"
    "v_cvt_pk_bf16_f32 v80, 0, v80\n"
    "v_cvt_pk_bf16_f32 v81, 0, v81\n"
    "v_cvt_pk_bf16_f32 v82, 0, v82\n"
    "v_cvt_pk_bf16_f32 v83, 0, v83\n"
    "v_pk_add_f32 v[80:81], v[40:41], v[80:81] op_sel:[1,0] op_sel_hi:[1,1]\n"
    "v_pk_add_f32 v[82:83], v[44:45], v[82:83] op_sel:[1,0] op_sel_hi:[1,1]\n"
    "v_cvt_pk_bf16_f32 v72, v80, v82\n"
    "v_cvt_pk_bf16_f32 v76, v81, v83\n"
    "v_dot2_f32_bf16 v14, v72, v36, 0\n"
    "v_dot2_f32_bf16 v15, v76, v36, 0\n"
    "v_pk_add_f32 v[80:81], v[28:29], v[10:11] neg_lo:[0,1] neg_hi:[0,1]\n"
    "v_pk_add_f32 v[82:83], v[30:31], v[10:11] neg_lo:[0,1] neg_hi:[0,1]\n"
    "v_cvt_pk_bf16_f32 v80, 0, v80\n"
    "v_cvt_pk_bf16_f32 v81, 0, v81\n"
    "v_cvt_pk_bf16_f32 v82, 0, v82\n"
    "v_cvt_pk_bf16_f32 v83, 0, v83\n"
    "v_pk_mul_f32 v[80:81], v[80:81], v[12:13]\n"
    "v_pk_mul_f32 v[82:83], v[82:83], v[12:13]\n"
    "v_cvt_pk_bf16_f32 v80, 0, v80\n"
    "v_cvt_pk_bf16_f32 v81, 0, v81\n"
    "v_cvt_pk_bf16_f32 v82, 0, v82\n"
    "v_cvt_pk_bf16_f32 v83, 0, v83\n"
    "v_pk_mul_f32 v[80:81], v[80:81], v[48:49] op_sel_hi:[1,0]\n"
    "v_pk_mul_f32 v[82:83], v[82:83], v[52:53] op_sel_hi:[1,0]\n"
    "v_cvt_pk_bf16_f32 v80, 0, v80\n"
    "v_cvt_pk_bf16_f32 v81, 0, v81\n"
    "v_cvt_pk_bf16_f32 v82, 0, v82\n"
    "v_cvt_pk_bf16_f32 v83, 0, v83\n"
    "v_pk_add_f32 v[80:81], v[48:49], v[80:81] op_sel:[1,0] op_sel_hi:[1,1]\n"
    "v_pk_add_f32 v[82:83], v[52:53], v[82:83] op_sel:[1,0] op_sel_hi:[1,1]\n"
    "v_cvt_pk_bf16_f32 v73, v80, v82\n"
    "v_cvt_pk_bf16_f32 v77, v81, v83\n"
    "v_dot2_f32_bf16 v14, v73, v37, v14\n"
    "v_dot2_f32_bf16 v15, v77, v37, v15\n"
    "v_pk_add_f32 v[80:81], v[32:33], v[10:11] neg_lo:[0,1] neg_hi:[0,1]\n"
    "v_pk_add_f32 v[82:83], v[34:35], v[10:11] neg_lo:[0,1] neg_hi:[0,1]\n"
    "v_cvt_pk_bf16_f32 v80, 0, v80\n"
    "v_cvt_pk_bf16_f32 v81, 0, v81\n"
    "v_cvt_pk_bf16_f32 v82, 0, v82\n"
    "v_cvt_pk_bf16_f32 v83, 0, v83\n"
    "v_pk_mul_f32 v[80:81], v[80:81], v[12:13]\n"
    "v_pk_mul_f32 v[82:83], v[82:83], v[12:13]\n"
    "v_cvt_pk_bf16_f32 v80, 0, v80\n"
    "v_cvt_pk_bf16_f32 v81, 0, v81\n"
    "v_cvt_pk_bf16_f32 v82, 0, v82\n"
    "v_cvt_pk_bf16_f32 v83, 0, v83\n"
    "v_pk_mul_f32 v[80:81], v[80:81], v[56:57] op_sel_hi:[1,0]\n"
    "v_pk_mul_f32 v[82:83], v[82:83], v[60:61] op_sel_hi:[1,0]\n"
    "v_cvt_pk_bf16_f32 v80, 0, v80\n"
    "v_cvt_pk_bf16_f32 v81, 0, v81\n"
    "v_cvt_pk_bf16_f32 v82, 0, v82\n"
    "v_cvt_pk_bf16_f32 v83, 0, v83\n"
    "v_pk_add_f32 v[80:81], v[56:57], v[80:81] op_sel:[1,0] op_sel_hi:[1,1]\n"
    "v_pk_add_f32 v[82:83], v[60:61], v[82:83] op_sel:[1,0] op_sel_hi:[1,1]\n"
    "v_cvt_pk_bf16_f32 v74, v80, v82\n"
    "v_cvt_pk_bf16_f32 v78, v81, v83\n"
    "v_dot2_f32_bf16 v14, v74, v38, v14\n"
    "v_dot2_f32_bf16 v15, v78, v38, v15\n"
    "v_add_u32 v75, 1, v75\n"
    "v_add_u32 v79, 1, v79\n"
    "buffer_store_dwordx4 v[72:75], v22, s[20:23], s45 offen sc1\n"
    "buffer_store_dwordx4 v[76:79], v22, s[20:23], s45 offen offset:1536 sc1\n"
    "v_permlane32_swap_b32 v14, v15\n"
    "v_add_f32 v14, v14, v15\n"
    "s_nop 1\n"
    "v_add_f32_dpp v14, v14, v14 row_ror:8 row_mask:0xf bank_mask:0xf\n"
    "v_mov_b32 v15, v14\n"
    "s_nop 1\n"
    "v_permlane16_swap_b32 v14, v15\n"
    "v_add_f32 v14, v14, v15\n"
    "ds_write_b32 v18, v14\n"
    "s_waitcnt lgkmcnt(0)\n"
    "s_barrier\n"
    "v_lshlrev_b32 v80, 16, v72\n"
    "v_lshlrev_b32 v81, 16, v76\n"
    "v_and_b32 v82, 0xffff0000, v72\n"
    "v_and_b32 v83, 0xffff0000, v76\n"
    "v_pk_mul_f32 v[80:81], v[42:43], v[80:81] op_sel:[1,0] op_sel_hi:[1,1]\n"
    "v_pk_mul_f32 v[82:83], v[46:47], v[82:83] op_sel:[1,0] op_sel_hi:[1,1]\n"
    "v_cvt_pk_bf16_f32 v80, 0, v80\n"
    "v_cvt_pk_bf16_f32 v81, 0, v81\n"
    "v_cvt_pk_bf16_f32 v82, 0, v82\n"
    "v_cvt_pk_bf16_f32 v83, 0, v83\n"
    "v_pk_add_f32 v[80:81], v[80:81], v[24:25]\n"
    "v_pk_add_f32 v[82:83], v[82:83], v[26:27]\n"
    "v_cvt_pk_bf16_f32 v8, v80, v82\n"
    "v_cvt_pk_bf16_f32 v12, v81, v83\n"
    "ds_write_b32 v4, v8 offset:18560\n"
    "ds_write_b32 v4, v12 offset:19712\n"
    "v_lshlrev_b32 v80, 16, v73\n"
    "v_lshlrev_b32 v81, 16, v77\n"
    "v_and_b32 v82, 0xffff0000, v73\n"
    "v_and_b32 v83, 0xffff0000, v77\n"
    "v_pk_mul_f32 v[80:81], v[50:51], v[80:81] op_sel:[1,0] op_sel_hi:[1,1]\n"
    "v_pk_mul_f32 v[82:83], v[54:55], v[82:83] op_sel:[1,0] op_sel_hi:[1,1]\n"
    "v_cvt_pk_bf16_f32 v80, 0, v80\n"
    "v_cvt_pk_bf16_f32 v81, 0, v81\n"
    "v_cvt_pk_bf16_f32 v82, 0, v82\n"
    "v_cvt_pk_bf16_f32 v83, 0, v83\n"
    "v_pk_add_f32 v[80:81], v[80:81], v[28:29]\n"
    "v_pk_add_f32 v[82:83], v[82:83], v[30:31]\n"
    "v_cvt_pk_bf16_f32 v9, v80, v82\n"
    "v_cvt_pk_bf16_f32 v13, v81, v83\n"
    "ds_write_b32 v4, v9 offset:18564\n"
    "ds_write_b32 v4, v13 offset:19716\n"
    "v_lshlrev_b32 v80, 16, v74\n"
    "v_lshlrev_b32 v81, 16, v78\n"
    "v_and_b32 v82, 0xffff0000, v74\n"
    "v_and_b32 v83, 0xffff0000, v78\n"
    "v_pk_mul_f32 v[80:81], v[58:59], v[80:81] op_sel:[1,0] op_sel_hi:[1,1]\n"
    "v_pk_mul_f32 v[82:83], v[62:63], v[82:83] op_sel:[1,0] op_sel_hi:[1,1]\n"
    "v_cvt_pk_bf16_f32 v80, 0, v80\n"
    "v_cvt_pk_bf16_f32 v81, 0, v81\n"
    "v_cvt_pk_bf16_f32 v82, 0, v82\n"
    "v_cvt_pk_bf16_f32 v83, 0, v83\n"
    "v_pk_add_f32 v[80:81], v[80:81], v[32:33]\n"
    "v_pk_add_f32 v[82:83], v[82:83], v[34:35]\n"
    "v_cvt_pk_bf16_f32 v10, v80, v82\n"
    "v_cvt_pk_bf16_f32 v14, v81, v83\n"
    "ds_write_b32 v4, v10 offset:18568\n"
    "ds_write_b32 v4, v14 offset:19720\n"
    "s_waitcnt lgkmcnt(0)\n"
    "s_barrier\n"
    "s_branch END\n"
    "LN2K:\n"
    "s_setprio 0\n"
    "s_cmpk_ge_u32 s2, 128\n"
    "s_cbranch_scc1 PFZ\n"
    "s_and_b32 s57, s2, 7\n"
    "s_lshl_b32 s57, s57, 4\n"
    "s_lshr_b32 s43, s2, 3\n"
    "s_add_u32 s57, s57, s43\n"
    "v_and_b32 v1, 63, v0\n"
    "s_cmpk_lg_u32 s37, 12\n"
    "s_cbranch_scc1 LN2C\n"
    "s_setprio 3\n"
    "v_lshlrev_b32 v17, 4, v1\n"
    "v_add_u32 v17, 132512, v17\n"
    "v_and_b32 v2, 7, v0\n"
    "v_lshrrev_b32 v3, 5, v1\n"
    "v_lshl_add_u32 v2, v2, 1, v3\n"
    "v_lshlrev_b32 v3, 3, v2\n"
    "s_lshl_b32 s42, s57, 4\n"
    "s_sub_u32 s42, 2047, s42\n"
    "v_sub_u32 v2, s42, v2\n"
    "v_and_b32 v4, 15, v0\n"
    "v_sub_u32 v5, s42, v4\n"
    "v_lshlrev_b32 v6, 3, v4\n"
    "v_mul_u32_u24 v4, 48, v4\n"
    "v_add_u32 v4, 136608, v4\n"
    "s_lshl_b32 s42, s57, 7\n"
    "s_add_u32 s43, s42, 4096\n"
    "s_add_u32 s42, s42, 77824\n"
    "s_brev_b32 s46, 1\n"
    "s_movk_i32 s47, 0x7ff\n"
    "v_mov_b32 v21, s36\n"
    "s_mov_b32 s48, 0xff\n"
    "s_mov_b32 s49, 0xff\n"
    "v_min_u32 v7, 12, v1\n"
    "v_lshlrev_b32 v7, 3, v7\n"
    "v_add_u32 v7, 148736, v7\n"
    "v_mov_b32 v32, 0\n"
    "v_mov_b32 v33, 0\n"
    "s_mov_b64 exec, 0x1000\n"
    "ds_write_b64 v7, v[32:33]\n"
    "s_mov_b64 exec, -1\n"
    "s_waitcnt lgkmcnt(0)\n"
    "s_barrier\n"
    "ds_read_b128 v[8:11], v17\n"
    "ds_read_b128 v[12:15], v17 offset:1024\n"
    "ds_read_b128 v[24:27], v17 offset:2048\n"
    "ds_read_b128 v[28:31], v17 offset:3072\n"
    "ds_read_b64 v[32:33], v7\n"
    "s_waitcnt lgkmcnt(3)\n"
    "v_pk_add_f32 v[8:9], v[8:9], v[12:13]\n"
    "v_pk_add_f32 v[10:11], v[10:11], v[14:15]\n"
    "s_nop 1\n"
    "v_permlane32_swap_b32 v8, v10\n"
    "v_permlane32_swap_b32 v9, v11\n"
    "v_pk_add_f32 v[8:9], v[8:9], v[10:11]\n"
    "s_nop 1\n"
    "v_add_f32_dpp v8, v8, v8 row_ror:8 row_mask:0xf bank_mask:0xf\n"
    "v_add_f32_dpp v9, v9, v9 row_ror:8 row_mask:0xf bank_mask:0xf\n"
    "v_mov_b32 v10, v8\n"
    "v_mov_b32 v11, v9\n"
    "s_nop 0\n"
    "v_permlane16_swap_b32 v8, v10\n"
    "v_permlane16_swap_b32 v9, v11\n"
    "v_pk_add_f32 v[8:9], v[8:9], v[10:11]\n"
    "s_waitcnt lgkmcnt(0)\n"
    "v_pk_add_f32 v[24:25], v[24:25], v[28:29]\n"
    "v_pk_add_f32 v[26:27], v[26:27], v[30:31]\n"
    "v_add_f32_dpp v32, v32, v32 row_ror:8 row_mask:0xf bank_mask:0xf\n"
    "v_add_f32 v24, v24, v25\n"
    "v_add_f32 v26, v26, v27\n"
    "v_add_f32_dpp v32, v32, v32 row_ror:4 row_mask:0xf bank_mask:0xf\n"
    "v_mul_f32 v8, 0x3ae38e39, v8\n"
    "v_permlane32_swap_b32 v24, v26\n"
    "v_add_f32_dpp v32, v32, v32 row_ror:2 row_mask:0xf bank_mask:0xf\n"
    "v_add_f32 v24, v24, v26\n"
    "v_mul_f32 v9, 0x3ae38e39, v9\n"
    "v_add_f32_dpp v32, v32, v32 row_ror:1 row_mask:0xf bank_mask:0xf\n"
    "v_add_f32_dpp v24, v24, v24 row_ror:8 row_mask:0xf bank_mask:0xf\n"
    "v_fma_f32 v9, -v8, v8, v9\n"
    "v_mov_b32 v25, v24\n"
    "v_max_f32 v9, 0, v9\n"
    "v_cvt_pk_bf16_f32 v9, 0, v9\n"
    "v_permlane16_swap_b32 v24, v25\n"
    "v_add_f32 v9, 0x3727c5ac, v9\n"
    "v_readfirstlane_b32 s44, v32\n"
    "v_cvt_pk_bf16_f32 v9, 0, v9\n"
    "v_sqrt_f32 v9, v9\n"
    "v_add_f32 v24, v24, v25\n"
    "v_cvt_pk_bf16_f32 v8, v8, v9\n"
    "v_and_b32 v9, 0xffff0000, v8\n"
    "v_lshlrev_b32 v8, 16, v8\n"
    "v_rcp_f32 v9, v9\n"
    "v_fma_f32 v24, -v8, s44, v24\n"
    "v_mul_f32 v24, v24, v9\n"
    "v_and_b32 v9, 0x7ff, v24\n"
    "v_add_u32 v9, 0x7ff, v9\n"
    "v_or_b32 v24, v24, v9\n"
    "v_ashrrev_i32 v9, 31, v24\n"
    "v_bitop3_b32 v24, v9, v24, s46 bitop3:0x36\n"
    "v_bfi_b32 v20, s47, v2, v24\n"
    "s_mov_b64 exec, s[48:49]\n"
    "buffer_store_dwordx2 v[20:21], v3, s[20:23], s42 offen sc1\n"
    "s_cmpk_lg_u32 s57, 0\n"
    "s_cbranch_scc1 LN2K2\n"
    "s_mov_b64 exec, -1\n"
    "v_add_f32_dpp v33, v33, v33 row_ror:8 row_mask:0xf bank_mask:0xf\n"
    "s_nop 1\n"
    "v_add_f32_dpp v33, v33, v33 row_ror:4 row_mask:0xf bank_mask:0xf\n"
    "s_nop 1\n"
    "v_add_f32_dpp v33, v33, v33 row_ror:2 row_mask:0xf bank_mask:0xf\n"
    "s_nop 1\n"
    "v_add_f32_dpp v33, v33, v33 row_ror:1 row_mask:0xf bank_mask:0xf\n"
    "s_mov_b64 exec, 1\n"
    "v_sqrt_f32 v22, v33\n"
    "v_mov_b32 v23, s36\n"
    "v_mul_f32 v22, 0x3c343958, v22\n"
    "buffer_store_dwordx2 v[22:23], off, s[20:23], 0 offset:2048 sc1\n"
    "LN2K2:\n"
    "s_mov_b64 exec, 0xffff\n"
    "s_waitcnt lgkmcnt(0)\n"
    "s_barrier\n"
    "ds_read_b128 v[8:11], v4\n"
    "ds_read_b128 v[12:15], v4 offset:16\n"
    "ds_read_b128 v[24:27], v4 offset:32\n"
    "s_waitcnt lgkmcnt(1)\n"
    "v_pk_add_f32 v[8:9], v[8:9], v[12:13]\n"
    "v_pk_add_f32 v[10:11], v[10:11], v[14:15]\n"
    "s_waitcnt lgkmcnt(0)\n"
    "v_pk_add_f32 v[8:9], v[8:9], v[24:25]\n"
    "v_pk_add_f32 v[10:11], v[10:11], v[26:27]\n"
    "v_pk_add_f32 v[8:9], v[8:9], v[10:11]\n"
    "v_add_f32 v8, v8, v9\n"
    "v_and_b32 v9, 0x7ff, v8\n"
    "v_add_u32 v9, 0x7ff, v9\n"
    "v_or_b32 v8, v8, v9\n"
    "v_ashrrev_i32 v9, 31, v8\n"
    "v_bitop3_b32 v8, v9, v8, s46 bitop3:0x36\n"
    "v_bfi_b32 v20, s47, v5, v8\n"
    "buffer_store_dwordx2 v[20:21], v6, s[20:23], s43 offen sc1\n"
    "s_mov_b64 exec, -1\n"
    "s_branch END\n"
    "LN2C:\n"
    "s_setprio 2\n"
    "v_subrev_u32 v1, 832, v0\n"
    "v_add_u32 v2, 0, v1\n"
    "v_mul_u32_u24 v3, 7282, v2\n"
    "v_lshrrev_b32 v3, 20, v3\n"
    "v_add_lshl_u32 v8, v2, v3, 4\n"
    "v_add_u32 v2, 192, v1\n"
    "v_mul_u32_u24 v3, 7282, v2\n"
    "v_lshrrev_b32 v3, 20, v3\n"
    "v_add_lshl_u32 v9, v2, v3, 4\n"
    "v_add_u32 v2, 384, v1\n"
    "v_mul_u32_u24 v3, 7282, v2\n"
    "v_lshrrev_b32 v3, 20, v3\n"
    "v_add_lshl_u32 v10, v2, v3, 4\n"
    "v_add_u32 v2, 576, v1\n"
    "v_mul_u32_u24 v3, 7282, v2\n"
    "v_lshrrev_b32 v3, 20, v3\n"
    "v_add_lshl_u32 v11, v2, v3, 4\n"
    "v_add_u32 v2, 768, v1\n"
    "v_mul_u32_u24 v3, 7282, v2\n"
    "v_lshrrev_b32 v3, 20, v3\n"
    "v_add_lshl_u32 v12, v2, v3, 4\n"
    "v_add_u32 v2, 960, v1\n"
    "v_mul_u32_u24 v3, 7282, v2\n"
    "v_lshrrev_b32 v3, 20, v3\n"
    "v_add_lshl_u32 v13, v2, v3, 4\n"
    "s_mul_i32 s42, s57, 18432\n"
    "v_lshl_add_u32 v1, v1, 4, s42\n"
    "v_add_u32 v2, 6144, v1\n"
    "v_add_u32 v3, 12288, v1\n"
    "s_mov_b32 s42, 7341056\n"
    "s_lshl_b32 s43, s57, 2\n"
    "v_mov_b32 v4, s36\n"
    "s_barrier\n"
    "s_barrier\n"
    "s_barrier\n"
    "ds_read_b128 v[40:43], v8 offset:18560\n"
    "ds_read_b128 v[44:47], v9 offset:18560\n"
    "ds_read_b128 v[48:51], v10 offset:18560\n"
    "ds_read_b128 v[52:55], v11 offset:18560\n"
    "ds_read_b128 v[56:59], v12 offset:18560\n"
    "ds_read_b128 v[60:63], v13 offset:18560\n"
    "s_waitcnt lgkmcnt(5)\n"
    "global_store_dwordx4 v1, v[40:43], s[34:35] offset:0 sc1\n"
    "s_waitcnt lgkmcnt(4)\n"
    "global_store_dwordx4 v1, v[44:47], s[34:35] offset:3072 sc1\n"
    "s_waitcnt lgkmcnt(3)\n"
    "global_store_dwordx4 v2, v[48:51], s[34:35] offset:0 sc1\n"
    "s_waitcnt lgkmcnt(2)\n"
    "global_store_dwordx4 v2, v[52:55], s[34:35] offset:3072 sc1\n"
    "s_waitcnt lgkmcnt(1)\n"
    "global_store_dwordx4 v3, v[56:59], s[34:35] offset:0 sc1\n"
    "s_waitcnt lgkmcnt(0)\n"
    "global_store_dwordx4 v3, v[60:63], s[34:35] offset:3072 sc1\n"
    "s_waitcnt vmcnt(0)\n"
    "s_barrier\n"
    "s_cmpk_lg_u32 s37, 15\n"
    "s_cbranch_scc1 END\n"
    "s_mov_b64 exec, 1\n"
    "buffer_store_dword v4, off, s[20:23], s43 offset:1024 sc1\n"
    "s_mov_b64 exec, -1\n"
    "s_branch END\n"
    "PFZ:\n"
    "s_cmpk_ge_u32 s2, 184\n"
    "s_cbranch_scc1 CDONE\n"
    "s_branch E_49\n"
    "R_48:\n"
    "s_sleep 1\n"
    "s_sub_u32 s39, s39, 1\n"
    "s_cbranch_scc1 ABORT\n"
    "E_49:\n"
    "ds_read_b32 v4, v14 offset:60\n"
    "s_waitcnt lgkmcnt(0)\n"
    "v_cmp_ne_u32 vcc, 16, v4\n"
    "s_cbranch_vccnz R_48\n"
    "v_subrev_u32 v112, 5184, v111\n"
    "v_min_u32 v111, v111, v112\n"
    "v_lshl_add_u32 v112, v111, 7, s60\n"
    "global_load_lds_dword v112, s[32:33]\n"
    "v_add_u32 v111, 1728, v111\n"
    "v_subrev_u32 v112, 5184, v111\n"
    "v_min_u32 v111, v111, v112\n"
    "v_lshl_add_u32 v112, v111, 7, s60\n"
    "global_load_lds_dword v112, s[32:33]\n"
    "v_add_u32 v111, 1728, v111\n"
    "CDONE:\n"
    "s_cmpk_lt_u32 s2, 184\n"
    "s_cbranch_scc1 END\n"
    "s_branch ES0\n"
    "SIL1:\n"
    "s_mov_b64 exec, -1\n"
    "s_barrier\n"
    "v_add_u32 v1, 148736, v1\n"
    "v_mov_b32 v14, 147968\n"
    "v_mov_b32 v15, 1\n"
    "s_waitcnt vmcnt(0)\n"
    "v_lshlrev_b32 v5, 16, v4\n"
    "v_and_b32 v4, 0xffff0000, v4\n"
    "v_mul_f32 v6, 0xbfb8aa3b, v5\n"
    "v_mul_f32 v7, 0xbfb8aa3b, v4\n"
    "v_exp_f32 v6, v6\n"
    "v_exp_f32 v7, v7\n"
    "v_add_f32 v6, 1.0, v6\n"
    "v_add_f32 v7, 1.0, v7\n"
    "v_rcp_f32 v6, v6\n"
    "v_rcp_f32 v7, v7\n"
    "v_mul_f32 v5, v5, v6\n"
    "v_mul_f32 v4, v4, v7\n"
    "v_cvt_pk_bf16_f32 v4, v5, v4\n"
    "ds_write_b32 v1, v4\n"
    "s_mov_b64 exec, 1\n"
    "ds_add_u32 v14, v15 offset:4\n"
    "s_mov_b64 exec, -1\n"
    "s_cmpk_lg_u32 s37, 0\n"
    "s_cbranch_scc1 CHE9\n"
    "s_mov_b64 exec, 0\n"
    "CHE:\n"
    "s_branch CHE9\n"
    "CHE9:\n"
    "s_mov_b64 exec, -1\n"
    "s_branch M9\n"
    "M1:\n"
    "s_mov_b64 exec, -1\n"
    "s_waitcnt lgkmcnt(0)\n"
    "s_memrealtime s[96:97]\n"
    "s_load_dword s36, s[20:21], 0\n"
    "s_barrier\n"
    "s_cmpk_ge_u32 s2, 246\n"
    "s_cbranch_scc1 M8\n"
    "s_sub_u32 s42, s2, 192\n"
    "s_lshr_b32 s42, s42, 3\n"
    "s_min_u32 s42, s42, 8\n"
    "s_lshr_b32 s44, s28, 7\n"
    "s_add_u32 s44, s44, s2\n"
    "s_sub_u32 s44, 3, s44\n"
    "s_and_b32 s44, s44, 7\n"
    "s_add_u32 s44, s44, 1\n"
    "s_add_u32 s44, s44, s42\n"
    "s_mul_i32 s45, s44, 57\n"
    "s_lshr_b32 s45, s45, 9\n"
    "s_mul_i32 s45, s45, 9\n"
    "s_sub_u32 s44, s44, s45\n"
    "s_and_b32 s58, s2, 7\n"
    "s_mul_i32 s58, s58, 9\n"
    "s_add_u32 s58, s58, s44\n"
    "v_bfe_u32 v12, v0, 3, 3\n"
    "v_bfe_u32 v13, v0, 4, 1\n"
    "v_xor_b32 v13, v13, v0\n"
    "v_mul_u32_u24 v12, 9216, v12\n"
    "s_lshl_b32 s42, s37, 1\n"
    "v_xor_b32 v13, s42, v13\n"
    "v_and_b32 v13, 7, v13\n"
    "v_lshl_add_u32 v12, v13, 4, v12\n"
    "s_lshr_b32 s42, s37, 1\n"
    "s_mul_i32 s42, s42, 73728\n"
    "s_lshl_b32 s44, s58, 7\n"
    "s_add_u32 s42, s42, s44\n"
    "v_add_u32 v12, s42, v12\n"
    "s_bitcmp1_b32 s37, 0\n"
    "s_cselect_b64 s[56:57], s[30:31], s[28:29]\n"
    "s_sub_u32 s47, s37, 12\n"
    "s_sub_u32 s42, s2, 192\n"
    "s_lshr_b32 s43, s42, 1\n"
    "s_add_u32 s43, s43, 27\n"
    "s_and_b32 s44, s2, 1\n"
    "s_mul_i32 s45, s44, 1990656\n"
    "s_lshl_b32 s46, s43, 7\n"
    "s_add_u32 s45, s45, s46\n"
    "v_bfe_u32 v1, v0, 3, 5\n"
    "v_mul_u32_u24 v1, 0x1b00, v1\n"
    "v_lshrrev_b32 v2, 3, v0\n"
    "v_lshrrev_b32 v3, 4, v0\n"
    "v_and_b32 v2, 2, v2\n"
    "v_and_b32 v3, 4, v3\n"
    "v_xor_b32 v2, v2, v0\n"
    "v_xor_b32 v2, v2, v3\n"
    "v_and_b32 v2, 7, v2\n"
    "v_lshl_add_u32 v1, v2, 4, v1\n"
    "v_add_u32 v1, s45, v1\n"
    "s_lshl_b32 s45, s47, 10\n"
    "s_add_u32 m0, s45, 98304\n"
    "s_nop 0\n"
    "global_load_lds_dwordx4 v1, s[16:17] nt\n"
    "s_add_u32 m0, m0, 4096\n"
    "v_add_u32 v1, 221184, v1\n"
    "global_load_lds_dwordx4 v1, s[16:17] nt\n"
    "s_add_u32 m0, m0, 4096\n"
    "v_add_u32 v1, 221184, v1\n"
    "global_load_lds_dwordx4 v1, s[16:17] nt\n"
    "s_add_u32 m0, m0, 4096\n"
    "v_add_u32 v1, 221184, v1\n"
    "global_load_lds_dwordx4 v1, s[16:17] nt\n"
    "s_add_u32 m0, m0, 4096\n"
    "v_add_u32 v1, 221184, v1\n"
    "global_load_lds_dwordx4 v1, s[16:17] nt\n"
    "s_add_u32 m0, m0, 4096\n"
    "v_add_u32 v1, 221184, v1\n"
    "global_load_lds_dwordx4 v1, s[16:17] nt\n"
    "s_add_u32 m0, m0, 4096\n"
    "v_add_u32 v1, 221184, v1\n"
    "global_load_lds_dwordx4 v1, s[16:17] nt\n"
    "s_add_u32 m0, m0, 4096\n"
    "v_add_u32 v1, 221184, v1\n"
    "global_load_lds_dwordx4 v1, s[16:17] nt\n"
    "s_add_u32 m0, m0, 4096\n"
    "v_add_u32 v1, 221184, v1\n"
    "global_load_lds_dwordx4 v1, s[16:17] nt\n"
    "s_add_u32 m0, m0, 4096\n"
    "v_add_u32 v1, 221184, v1\n"
    "s_setprio 3\n"
    "s_barrier\n"
    "v_bfe_u32 v5, v0, 4, 2\n"
    "v_bfe_u32 v6, v0, 2, 2\n"
    "v_bfe_u32 v7, v0, 1, 1\n"
    "s_lshl_b32 s45, s47, 1\n"
    "v_or_b32 v7, s45, v7\n"
    "v_and_b32 v8, 2, v6\n"
    "v_xor_b32 v7, v7, v8\n"
    "v_lshlrev_b32 v8, 2, v5\n"
    "v_and_b32 v8, 4, v8\n"
    "v_xor_b32 v7, v7, v8\n"
    "v_lshlrev_b32 v2, 10, v5\n"
    "v_lshl_add_u32 v2, v6, 7, v2\n"
    "v_lshl_add_u32 v2, v7, 4, v2\n"
    "v_and_b32 v8, 1, v0\n"
    "v_lshl_add_u32 v2, v8, 3, v2\n"
    "v_add_u32 v2, 98304, v2\n"
    "s_mul_i32 s45, s44, 576\n"
    "s_add_u32 s45, s45, 148736\n"
    "v_lshl_add_u32 v3, v5, 4, s45\n"
    "s_mul_i32 s45, s44, 27648\n"
    "s_lshl_b32 s46, s43, 9\n"
    "s_add_u32 s45, s45, s46\n"
    "s_lshl_b32 s46, s47, 7\n"
    "s_add_u32 s45, s45, s46\n"
    "s_add_u32 s45, s45, 20480\n"
    "v_and_b32 v4, 15, v0\n"
    "v_lshlrev_b32 v4, 3, v4\n"
    "v_mov_b32 v14, 147968\n"
    "v_mov_b32 v15, 1\n"
    "s_waitcnt lgkmcnt(0)\n"
    "s_add_u32 s36, s36, 1\n"
    "s_waitcnt vmcnt(0)\n"
    "s_mov_b64 exec, 1\n"
    "ds_add_u32 v14, v15 offset:4\n"
    "s_mov_b64 exec, -1\n"
    "s_branch E_51\n"
    "R_50:\n"
    "s_sub_u32 s39, s39, 1\n"
    "s_cbranch_scc1 ABORT\n"
    "E_51:\n"
    "ds_read_b32 v5, v14 offset:4\n"
    "s_waitcnt lgkmcnt(0)\n"
    "v_cmp_ne_u32 vcc, 9, v5\n"
    "s_cbranch_vccnz R_50\n"
    "ds_read_b128 v[16:19], v3 offset:0\n"
    "ds_read_b64_tr_b16 v[52:53], v2 offset:0\n"
    "ds_read_b64_tr_b16 v[54:55], v2 offset:512\n"
    "ds_read_b128 v[20:23], v3 offset:64\n"
    "ds_read_b64_tr_b16 v[56:57], v2 offset:4096\n"
    "ds_read_b64_tr_b16 v[58:59], v2 offset:4608\n"
    "ds_read_b128 v[24:27], v3 offset:128\n"
    "ds_read_b64_tr_b16 v[60:61], v2 offset:8192\n"
    "ds_read_b64_tr_b16 v[62:63], v2 offset:8704\n"
    "ds_read_b128 v[28:31], v3 offset:192\n"
    "ds_read_b64_tr_b16 v[64:65], v2 offset:12288\n"
    "ds_read_b64_tr_b16 v[66:67], v2 offset:12800\n"
    "ds_read_b128 v[32:35], v3 offset:256\n"
    "ds_read_b64_tr_b16 v[68:69], v2 offset:16384\n"
    "ds_read_b64_tr_b16 v[70:71], v2 offset:16896\n"
    "s_waitcnt lgkmcnt(12)\n"
    "v_mfma_f32_16x16x32_bf16 v[8:11], v[16:19], v[52:55], 0\n"
    "ds_read_b128 v[36:39], v3 offset:320\n"
    "ds_read_b64_tr_b16 v[72:73], v2 offset:20480\n"
    "ds_read_b64_tr_b16 v[74:75], v2 offset:20992\n"
    "s_waitcnt lgkmcnt(12)\n"
    "v_mfma_f32_16x16x32_bf16 v[8:11], v[20:23], v[56:59], v[8:11]\n"
    "ds_read_b128 v[40:43], v3 offset:384\n"
    "ds_read_b64_tr_b16 v[76:77], v2 offset:24576\n"
    "ds_read_b64_tr_b16 v[78:79], v2 offset:25088\n"
    "s_waitcnt lgkmcnt(12)\n"
    "v_mfma_f32_16x16x32_bf16 v[8:11], v[24:27], v[60:63], v[8:11]\n"
    "ds_read_b128 v[44:47], v3 offset:448\n"
    "ds_read_b64_tr_b16 v[80:81], v2 offset:28672\n"
    "ds_read_b64_tr_b16 v[82:83], v2 offset:29184\n"
    "s_waitcnt lgkmcnt(12)\n"
    "v_mfma_f32_16x16x32_bf16 v[8:11], v[28:31], v[64:67], v[8:11]\n"
    "ds_read_b128 v[48:51], v3 offset:512\n"
    "ds_read_b64_tr_b16 v[84:85], v2 offset:32768\n"
    "ds_read_b64_tr_b16 v[86:87], v2 offset:33280\n"
    "s_waitcnt lgkmcnt(12)\n"
    "v_mfma_f32_16x16x32_bf16 v[8:11], v[32:35], v[68:71], v[8:11]\n"
    "s_waitcnt lgkmcnt(9)\n"
    "v_mfma_f32_16x16x32_bf16 v[8:11], v[36:39], v[72:75], v[8:11]\n"
    "s_waitcnt lgkmcnt(6)\n"
    "v_mfma_f32_16x16x32_bf16 v[8:11], v[40:43], v[76:79], v[8:11]\n"
    "s_waitcnt lgkmcnt(3)\n"
    "v_mfma_f32_16x16x32_bf16 v[8:11], v[44:47], v[80:83], v[8:11]\n"
    "s_waitcnt lgkmcnt(0)\n"
    "v_mfma_f32_16x16x32_bf16 v[8:11], v[48:51], v[84:87], v[8:11]\n"
    "s_mov_b64 exec, 0xffff\n"
    "s_nop 6\n"
    "v_mov_b32 v9, s36\n"
    "buffer_store_dwordx2 v[8:9], v4, s[20:23], s45 offen sc1\n"
    "s_mov_b64 exec, -1\n"
    "s_mov_b64 exec, 1\n"
    "ds_add_u32 v14, v15 offset:232\n"
    "s_mov_b64 exec, -1\n"
    "s_setprio 0\n"
    "s_branch MOED\n"
    "M8:\n"
    "v_mov_b32 v14, 147968\n"
    "v_mov_b32 v15, 1\n"
    "s_mov_b64 exec, 1\n"
    "ds_add_u32 v14, v15 offset:232\n"
    "s_mov_b64 exec, -1\n"
    "M9:\n"
    "s_waitcnt lgkmcnt(0)\n"
    "s_add_u32 s36, s36, 1\n"
    "ES1:\n"
    "ES0:\n"
    "s_sub_u32 s42, s2, 192\n"
    "s_lshr_b32 s42, s42, 3\n"
    "s_min_u32 s42, s42, 8\n"
    "s_lshr_b32 s44, s28, 7\n"
    "s_add_u32 s44, s44, s2\n"
    "s_sub_u32 s44, 3, s44\n"
    "s_and_b32 s44, s44, 7\n"
    "s_add_u32 s44, s44, 1\n"
    "s_add_u32 s44, s44, s42\n"
    "s_mul_i32 s45, s44, 57\n"
    "s_lshr_b32 s45, s45, 9\n"
    "s_mul_i32 s45, s45, 9\n"
    "s_sub_u32 s44, s44, s45\n"
    "s_and_b32 s58, s2, 7\n"
    "s_mul_i32 s58, s58, 9\n"
    "s_add_u32 s58, s58, s44\n"
    "v_bfe_u32 v12, v0, 3, 3\n"
    "v_bfe_u32 v13, v0, 4, 1\n"
    "v_xor_b32 v13, v13, v0\n"
    "v_mul_u32_u24 v12, 9216, v12\n"
    "s_lshl_b32 s42, s37, 1\n"
    "v_xor_b32 v13, s42, v13\n"
    "v_and_b32 v13, 7, v13\n"
    "v_lshl_add_u32 v12, v13, 4, v12\n"
    "s_lshr_b32 s42, s37, 1\n"
    "s_mul_i32 s42, s42, 73728\n"
    "s_lshl_b32 s44, s58, 7\n"
    "s_add_u32 s42, s42, s44\n"
    "v_add_u32 v12, s42, v12\n"
    "s_bitcmp1_b32 s37, 0\n"
    "s_cselect_b64 s[56:57], s[30:31], s[28:29]\n"
    "s_waitcnt lgkmcnt(0)\n"
    "s_barrier\n"
    "s_cmpk_ge_u32 s37, 8\n"
    "s_cbranch_scc1 MOED\n"
    "ES1c:\n"
    "s_lshl_b32 m0, s37, 10\n"
    "ES1d:\n"
    "s_waitcnt vmcnt(5)\n"
    "global_load_lds_dwordx4 v12, s[56:57] nt\n"
    "s_add_u32 m0, m0, 8192\n"
    "v_add_u32 v12, 294912, v12\n"
    "s_waitcnt vmcnt(5)\n"
    "global_load_lds_dwordx4 v12, s[56:57] nt\n"
    "s_add_u32 m0, m0, 8192\n"
    "v_add_u32 v12, 294912, v12\n"
    "s_waitcnt vmcnt(5)\n"
    "global_load_lds_dwordx4 v12, s[56:57] nt\n"
    "s_add_u32 m0, m0, 8192\n"
    "v_add_u32 v12, 294912, v12\n"
    "s_waitcnt vmcnt(5)\n"
    "global_load_lds_dwordx4 v12, s[56:57] nt\n"
    "s_add_u32 m0, m0, 8192\n"
    "v_add_u32 v12, 294912, v12\n"
    "s_waitcnt vmcnt(5)\n"
    "global_load_lds_dwordx4 v12, s[56:57] nt\n"
    "s_add_u32 m0, m0, 8192\n"
    "v_add_u32 v12, 294912, v12\n"
    "s_waitcnt vmcnt(5)\n"
    "global_load_lds_dwordx4 v12, s[56:57] nt\n"
    "s_add_u32 m0, m0, 8192\n"
    "v_add_u32 v12, 294912, v12\n"
    "s_waitcnt vmcnt(5)\n"
    "global_load_lds_dwordx4 v12, s[56:57] nt\n"
    "s_add_u32 m0, m0, 8192\n"
    "v_add_u32 v12, 294912, v12\n"
    "s_waitcnt vmcnt(5)\n"
    "global_load_lds_dwordx4 v12, s[56:57] nt\n"
    "s_add_u32 m0, m0, 8192\n"
    "v_add_u32 v12, 294912, v12\n"
    "s_waitcnt vmcnt(5)\n"
    "global_load_lds_dwordx4 v12, s[56:57] nt\n"
    "s_add_u32 m0, m0, 8192\n"
    "v_add_u32 v12, 294912, v12\n"
    "s_waitcnt vmcnt(5)\n"
    "global_load_lds_dwordx4 v12, s[56:57] nt\n"
    "s_add_u32 m0, m0, 8192\n"
    "v_add_u32 v12, 294912, v12\n"
    "s_waitcnt vmcnt(5)\n"
    "global_load_lds_dwordx4 v12, s[56:57] nt\n"
    "s_add_u32 m0, m0, 8192\n"
    "v_add_u32 v12, 294912, v12\n"
    "s_waitcnt vmcnt(5)\n"
    "global_load_lds_dwordx4 v12, s[56:57] nt\n"
    "s_add_u32 m0, m0, 8192\n"
    "v_add_u32 v12, 294912, v12\n"
    "v_mov_b32 v14, 148096\n"
    "v_mov_b32 v15, 1\n"
    "v_bfe_u32 v3, v0, 4, 2\n"
    "s_add_u32 s63, s36, 1\n"
    "v_bfe_u32 v6, v0, 1, 1\n"
    "v_bfe_u32 v7, v0, 2, 2\n"
    "v_lshrrev_b32 v8, 1, v7\n"
    "v_lshl_add_u32 v8, v6, 1, v8\n"
    "v_and_b32 v9, 1, v3\n"
    "v_lshl_add_u32 v8, v9, 2, v8\n"
    "v_xor_b32 v8, s37, v8\n"
    "v_lshlrev_b32 v4, 11, v3\n"
    "v_lshl_add_u32 v4, v6, 10, v4\n"
    "v_lshl_add_u32 v4, v7, 7, v4\n"
    "v_lshl_add_u32 v4, v8, 4, v4\n"
    "v_and_b32 v9, 1, v0\n"
    "v_lshl_add_u32 v4, v9, 3, v4\n"
    "v_add_u32 v5, 0x10000, v4\n"
    "v_add_u32 v6, 0x20000, v4\n"
    "v_lshrrev_b32 v2, 3, v0\n"
    "v_and_b32 v13, 2, v2\n"
    "v_mul_u32_u24 v2, 1152, v2\n"
    "v_lshrrev_b32 v7, 4, v0\n"
    "v_and_b32 v7, 4, v7\n"
    "v_xor_b32 v13, v13, v7\n"
    "v_xor_b32 v13, v13, v0\n"
    "v_and_b32 v13, 7, v13\n"
    "v_lshl_add_u32 v2, v13, 4, v2\n"
    "s_mul_i32 s42, s58, 73728\n"
    "v_add_u32 v2, s42, v2\n"
    "s_cmpk_lt_u32 s2, 192\n"
    "s_cbranch_scc1 ES3\n"
    "s_branch E_53\n"
    "R_52:\n"
    "s_sleep 1\n"
    "s_sub_u32 s39, s39, 1\n"
    "s_cbranch_scc1 ABORT\n"
    "E_53:\n"
    "ds_read_b32 v10, v14 offset:104\n"
    "s_waitcnt lgkmcnt(0)\n"
    "v_cmp_ne_u32 vcc, 4, v10\n"
    "s_cbranch_vccnz R_52\n"
    "ES3:\n"
    "s_waitcnt vmcnt(5)\n"
    "global_load_lds_dwordx4 v12, s[56:57] nt\n"
    "s_add_u32 m0, m0, 8192\n"
    "v_add_u32 v12, 294912, v12\n"
    "s_waitcnt vmcnt(5)\n"
    "global_load_lds_dwordx4 v12, s[56:57] nt\n"
    "s_add_u32 m0, m0, 8192\n"
    "v_add_u32 v12, 294912, v12\n"
    "s_waitcnt vmcnt(5)\n"
    "global_load_lds_dwordx4 v12, s[56:57] nt\n"
    "s_add_u32 m0, m0, 8192\n"
    "v_add_u32 v12, 294912, v12\n"
    "s_waitcnt vmcnt(6)\n"
    "s_mov_b64 exec, 1\n"
    "ds_add_u32 v14, v15 offset:76\n"
    "s_mov_b64 exec, -1\n"
    "s_waitcnt vmcnt(5)\n"
    "global_load_lds_dwordx4 v12, s[56:57] nt\n"
    "s_add_u32 m0, m0, 8192\n"
    "v_add_u32 v12, 294912, v12\n"
    "s_branch E_55\n"
    "R_54:\n"
    "s_sub_u32 s39, s39, 1\n"
    "s_cbranch_scc1 ABORT\n"
    "E_55:\n"
    "ds_read_b32 v10, v14 offset:76\n"
    "s_waitcnt lgkmcnt(0)\n"
    "v_cmp_ne_u32 vcc, 8, v10\n"
    "s_cbranch_vccnz R_54\n"
    "ds_read_b64_tr_b16 v[56:57], v4 offset:0\n"
    "ds_read_b64_tr_b16 v[58:59], v4 offset:512\n"
    "ds_read_b64_tr_b16 v[60:61], v4 offset:8192\n"
    "ds_read_b64_tr_b16 v[62:63], v4 offset:8704\n"
    "ds_read_b64_tr_b16 v[64:65], v4 offset:16384\n"
    "ds_read_b64_tr_b16 v[66:67], v4 offset:16896\n"
    "ds_read_b64_tr_b16 v[68:69], v4 offset:24576\n"
    "ds_read_b64_tr_b16 v[70:71], v4 offset:25088\n"
    "ds_read_b64_tr_b16 v[72:73], v4 offset:32768\n"
    "ds_read_b64_tr_b16 v[74:75], v4 offset:33280\n"
    "ds_read_b64_tr_b16 v[76:77], v4 offset:40960\n"
    "ds_read_b64_tr_b16 v[78:79], v4 offset:41472\n"
    "ds_read_b64_tr_b16 v[80:81], v4 offset:49152\n"
    "ds_read_b64_tr_b16 v[82:83], v4 offset:49664\n"
    "ds_read_b64_tr_b16 v[84:85], v4 offset:57344\n"
    "ds_read_b64_tr_b16 v[86:87], v4 offset:57856\n"
    "ds_read_b64_tr_b16 v[88:89], v5 offset:0\n"
    "ds_read_b64_tr_b16 v[90:91], v5 offset:512\n"
    "s_mov_b64 exec, 1\n"
    "ds_add_u32 v14, v15 offset:80\n"
    "s_mov_b64 exec, -1\n"
    "s_waitcnt vmcnt(5)\n"
    "global_load_lds_dwordx4 v12, s[56:57] nt\n"
    "s_add_u32 m0, m0, 8192\n"
    "v_add_u32 v12, 294912, v12\n"
    "s_waitcnt vmcnt(5)\n"
    "global_load_lds_dwordx4 v12, s[56:57] nt\n"
    "s_add_u32 m0, m0, 8192\n"
    "v_add_u32 v12, 294912, v12\n"
    "s_branch E_57\n"
    "R_56:\n"
    "s_sleep 1\n"
    "s_sub_u32 s39, s39, 1\n"
    "s_cbranch_scc1 ABORT\n"
    "E_57:\n"
    "ds_read_b32 v10, v14 offset:80\n"
    "s_waitcnt lgkmcnt(0)\n"
    "v_cmp_ne_u32 vcc, 8, v10\n"
    "s_cbranch_vccnz R_56\n"
    "s_mul_i32 m0, s37, 9216\n"
    "s_waitcnt vmcnt(5)\n"
    "global_load_lds_dwordx4 v2, s[32:33] offset:0 nt\n"
    "s_add_u32 m0, m0, 896\n"
    "s_waitcnt vmcnt(5)\n"
    "global_load_lds_dwordx4 v2, s[32:33] offset:128 nt\n"
    "s_add_u32 m0, m0, 896\n"
    "s_waitcnt vmcnt(5)\n"
    "global_load_lds_dwordx4 v2, s[32:33] offset:256 nt\n"
    "s_add_u32 m0, m0, 896\n"
    "s_waitcnt vmcnt(5)\n"
    "global_load_lds_dwordx4 v2, s[32:33] offset:384 nt\n"
    "s_add_u32 m0, m0, 896\n"
    "s_waitcnt vmcnt(5)\n"
    "global_load_lds_dwordx4 v2, s[32:33] offset:512 nt\n"
    "s_add_u32 m0, m0, 896\n"
    "s_waitcnt vmcnt(5)\n"
    "global_load_lds_dwordx4 v2, s[32:33] offset:640 nt\n"
    "s_add_u32 m0, m0, 896\n"
    "s_waitcnt vmcnt(6)\n"
    "s_mov_b64 exec, 1\n"
    "ds_add_u32 v14, v15 offset:84\n"
    "s_mov_b64 exec, -1\n"
    "s_waitcnt vmcnt(5)\n"
    "global_load_lds_dwordx4 v2, s[32:33] offset:768 nt\n"
    "s_add_u32 m0, m0, 896\n"
    "s_branch E_59\n"
    "R_58:\n"
    "s_sub_u32 s39, s39, 1\n"
    "s_cbranch_scc1 ABORT\n"
    "E_59:\n"
    "ds_read_b32 v10, v14 offset:84\n"
    "s_waitcnt lgkmcnt(0)\n"
    "v_cmp_ne_u32 vcc, 8, v10\n"
    "s_cbranch_vccnz R_58\n"
    "ds_read_b64_tr_b16 v[92:93], v5 offset:8192\n"
    "ds_read_b64_tr_b16 v[94:95], v5 offset:8704\n"
    "ds_read_b64_tr_b16 v[96:97], v5 offset:16384\n"
    "ds_read_b64_tr_b16 v[98:99], v5 offset:16896\n"
    "ds_read_b64_tr_b16 v[100:101], v5 offset:24576\n"
    "ds_read_b64_tr_b16 v[102:103], v5 offset:25088\n"
    "ds_read_b64_tr_b16 v[104:105], v5 offset:32768\n"
    "ds_read_b64_tr_b16 v[106:107], v5 offset:33280\n"
    "ds_read_b64_tr_b16 v[108:109], v5 offset:40960\n"
    "ds_read_b64_tr_b16 v[110:111], v5 offset:41472\n"
    "ds_read_b64_tr_b16 v[112:113], v5 offset:49152\n"
    "ds_read_b64_tr_b16 v[114:115], v5 offset:49664\n"
    "ds_read_b64_tr_b16 v[116:117], v5 offset:57344\n"
    "ds_read_b64_tr_b16 v[118:119], v5 offset:57856\n"
    "ds_read_b64_tr_b16 v[120:121], v6 offset:0\n"
    "ds_read_b64_tr_b16 v[122:123], v6 offset:512\n"
    "ds_read_b64_tr_b16 v[124:125], v6 offset:8192\n"
    "ds_read_b64_tr_b16 v[126:127], v6 offset:8704\n"
    "s_mov_b64 exec, 1\n"
    "ds_add_u32 v14, v15 offset:88\n"
    "s_mov_b64 exec, -1\n"
    "s_waitcnt vmcnt(5)\n"
    "global_load_lds_dwordx4 v2, s[32:33] offset:896 nt\n"
    "s_add_u32 m0, m0, 896\n"
    "s_waitcnt vmcnt(5)\n"
    "global_load_lds_dwordx4 v2, s[32:33] offset:1024 nt\n"
    "s_add_u32 m0, m0, 896\n"
    "v_and_b32 v7, 7, v0\n"
    "v_lshlrev_b32 v8, 3, v7\n"
    "v_and_b32 v9, 8, v0\n"
    "v_add_u32 v4, 151168, v8\n"
    "v_lshlrev_b32 v5, 3, v9\n"
    "v_lshl_add_u32 v5, v3, 4, v5\n"
    "v_add_u32 v5, 73728, v5\n"
    "v_and_b32 v6, 1, v3\n"
    "v_lshlrev_b32 v6, 2, v6\n"
    "v_and_b32 v8, 2, v3\n"
    "v_lshrrev_b32 v9, 3, v9\n"
    "v_add3_u32 v6, v6, v8, v9\n"
    "v_mul_u32_u24 v6, 144, v6\n"
    "v_lshl_add_u32 v6, v7, 1, v6\n"
    "s_lshl_b32 s42, s37, 4\n"
    "s_add_u32 s42, s42, 150016\n"
    "v_add_u32 v6, s42, v6\n"
    "s_setprio 2\n"
    "s_waitcnt vmcnt(0) lgkmcnt(0)\n"
    "s_barrier\n"
    "ds_read_b32 v7, v4\n"
    "s_waitcnt lgkmcnt(0)\n"
    "v_add_u32 v7, v7, v5\n"
    "ds_read_b128 v[20:23], v7 offset:0\n"
    "ds_read_b128 v[24:27], v7 offset:128\n"
    "ds_read_b128 v[28:31], v7 offset:256\n"
    "ds_read_b128 v[32:35], v7 offset:384\n"
    "ds_read_b128 v[36:39], v7 offset:512\n"
    "ds_read_b128 v[40:43], v7 offset:640\n"
    "ds_read_b128 v[44:47], v7 offset:768\n"
    "ds_read_b128 v[48:51], v7 offset:896\n"
    "ds_read_b128 v[52:55], v7 offset:1024\n"
    "s_waitcnt lgkmcnt(8)\n"
    "v_mfma_f32_16x16x32_bf16 v[12:15], v[20:23], v[56:59], 0\n"
    "v_mfma_f32_16x16x32_bf16 v[16:19], v[20:23], v[60:63], 0\n"
    "s_waitcnt lgkmcnt(7)\n"
    "v_mfma_f32_16x16x32_bf16 v[12:15], v[24:27], v[64:67], v[12:15]\n"
    "v_mfma_f32_16x16x32_bf16 v[16:19], v[24:27], v[68:71], v[16:19]\n"
    "s_waitcnt lgkmcnt(6)\n"
    "v_mfma_f32_16x16x32_bf16 v[12:15], v[28:31], v[72:75], v[12:15]\n"
    "v_mfma_f32_16x16x32_bf16 v[16:19], v[28:31], v[76:79], v[16:19]\n"
    "s_waitcnt lgkmcnt(5)\n"
    "v_mfma_f32_16x16x32_bf16 v[12:15], v[32:35], v[80:83], v[12:15]\n"
    "v_mfma_f32_16x16x32_bf16 v[16:19], v[32:35], v[84:87], v[16:19]\n"
    "s_waitcnt lgkmcnt(4)\n"
    "v_mfma_f32_16x16x32_bf16 v[12:15], v[36:39], v[88:91], v[12:15]\n"
    "v_mfma_f32_16x16x32_bf16 v[16:19], v[36:39], v[92:95], v[16:19]\n"
    "s_waitcnt lgkmcnt(3)\n"
    "v_mfma_f32_16x16x32_bf16 v[12:15], v[40:43], v[96:99], v[12:15]\n"
    "v_mfma_f32_16x16x32_bf16 v[16:19], v[40:43], v[100:103], v[16:19]\n"
    "s_waitcnt lgkmcnt(2)\n"
    "v_mfma_f32_16x16x32_bf16 v[12:15], v[44:47], v[104:107], v[12:15]\n"
    "v_mfma_f32_16x16x32_bf16 v[16:19], v[44:47], v[108:111], v[16:19]\n"
    "s_waitcnt lgkmcnt(1)\n"
    "v_mfma_f32_16x16x32_bf16 v[12:15], v[48:51], v[112:115], v[12:15]\n"
    "v_mfma_f32_16x16x32_bf16 v[16:19], v[48:51], v[116:119], v[16:19]\n"
    "s_waitcnt lgkmcnt(0)\n"
    "v_mfma_f32_16x16x32_bf16 v[12:15], v[52:55], v[120:123], v[12:15]\n"
    "v_mfma_f32_16x16x32_bf16 v[16:19], v[52:55], v[124:127], v[16:19]\n"
    "s_nop 6\n"
    "v_permlane32_swap_b32 v12, v14\n"
    "v_permlane32_swap_b32 v13, v15\n"
    "v_permlane32_swap_b32 v16, v18\n"
    "v_permlane32_swap_b32 v17, v19\n"
    "v_add_f32 v12, v12, v18\n"
    "v_add_f32 v13, v13, v19\n"
    "v_mov_b32 v14, v12\n"
    "s_nop 0\n"
    "v_mov_b32_dpp v12, v13 row_ror:8 row_mask:0xf bank_mask:0xc\n"
    "v_mov_b32_dpp v13, v14 row_ror:8 row_mask:0xf bank_mask:0x3\n"
    "v_cvt_pk_bf16_f32 v12, 0, v12\n"
    "v_mul_f32 v14, 0xbfb8aa3b, v12\n"
    "v_exp_f32 v14, v14\n"
    "v_cvt_pk_bf16_f32 v13, 0, v13\n"
    "v_add_f32 v14, 1.0, v14\n"
    "v_rcp_f32 v14, v14\n"
    "s_nop 0\n"
    "v_mul_f32 v12, v12, v14\n"
    "v_cvt_pk_bf16_f32 v12, 0, v12\n"
    "v_mul_f32 v12, v12, v13\n"
    "v_cvt_pk_bf16_f32 v12, v12, v12\n"
    "ds_write_b16 v6, v12\n"
    "s_waitcnt lgkmcnt(0)\n"
    "s_barrier\n"
    "s_cmpk_lg_u32 s2, 192\n"
    "s_cbranch_scc1 END\n"
    "s_cmpk_lg_u32 s37, 0\n"
    "s_cbranch_scc1 END\n"
    "v_mov_b32 v1, s63\n"
    "s_mov_b64 exec, 1\n"
    "buffer_store_dword v1, off, s[20:23], 0 sc1\n"
    "s_mov_b64 exec, -1\n"
    "s_branch END\n"
    "MOED:\n"
    "v_and_b32 v1, 63, v0\n"
    "v_and_b32 v2, 15, v0\n"
    "v_bfe_u32 v3, v0, 4, 2\n"
    "v_mov_b32 v14, 148096\n"
    "v_mov_b32 v15, 1\n"
    "v_bfe_u32 v6, v0, 2, 2\n"
    "v_bfe_u32 v7, v0, 1, 1\n"
    "v_and_b32 v8, 2, v6\n"
    "v_or_b32 v7, v7, v8\n"
    "v_and_b32 v8, 1, v3\n"
    "v_lshl_add_u32 v7, v8, 2, v7\n"
    "v_mul_u32_u24 v8, 9216, v3\n"
    "v_lshl_add_u32 v8, v6, 7, v8\n"
    "v_lshl_add_u32 v8, v7, 4, v8\n"
    "v_and_b32 v7, 1, v0\n"
    "v_lshl_add_u32 v8, v7, 3, v8\n"
    "s_mul_i32 s42, s37, 6\n"
    "s_sub_u32 s42, s42, 48\n"
    "s_and_b32 s43, s42, 3\n"
    "s_lshl_b32 s43, s43, 5\n"
    "s_lshr_b32 s44, s42, 2\n"
    "s_lshl_b32 s44, s44, 10\n"
    "v_xor_b32 v104, s43, v8\n"
    "v_add_u32 v104, s44, v104\n"
    "s_add_u32 s42, s42, 1\n"
    "s_and_b32 s43, s42, 3\n"
    "s_lshl_b32 s43, s43, 5\n"
    "s_lshr_b32 s44, s42, 2\n"
    "s_lshl_b32 s44, s44, 10\n"
    "v_xor_b32 v105, s43, v8\n"
    "v_add_u32 v105, s44, v105\n"
    "s_add_u32 s42, s42, 1\n"
    "s_and_b32 s43, s42, 3\n"
    "s_lshl_b32 s43, s43, 5\n"
    "s_lshr_b32 s44, s42, 2\n"
    "s_lshl_b32 s44, s44, 10\n"
    "v_xor_b32 v106, s43, v8\n"
    "v_add_u32 v106, s44, v106\n"
    "s_add_u32 s42, s42, 1\n"
    "s_and_b32 s43, s42, 3\n"
    "s_lshl_b32 s43, s43, 5\n"
    "s_lshr_b32 s44, s42, 2\n"
    "s_lshl_b32 s44, s44, 10\n"
    "v_xor_b32 v107, s43, v8\n"
    "v_add_u32 v107, s44, v107\n"
    "s_add_u32 s42, s42, 1\n"
    "s_and_b32 s43, s42, 3\n"
    "s_lshl_b32 s43, s43, 5\n"
    "s_lshr_b32 s44, s42, 2\n"
    "s_lshl_b32 s44, s44, 10\n"
    "v_xor_b32 v108, s43, v8\n"
    "v_add_u32 v108, s44, v108\n"
    "s_add_u32 s42, s42, 1\n"
    "s_and_b32 s43, s42, 3\n"
    "s_lshl_b32 s43, s43, 5\n"
    "s_lshr_b32 s44, s42, 2\n"
    "s_lshl_b32 s44, s44, 10\n"
    "v_xor_b32 v109, s43, v8\n"
    "v_add_u32 v109, s44, v109\n"
    "s_add_u32 s42, s42, 1\n"
    "v_and_b32 v7, 7, v0\n"
    "v_mul_u32_u24 v7, 144, v7\n"
    "v_lshl_add_u32 v7, v3, 4, v7\n"
    "v_add_u32 v7, 150016, v7\n"
    "v_mov_b32 v8, 148224\n"
    "v_cmp_gt_u32 vcc, 8, v2\n"
    "s_nop 1\n"
    "v_cndmask_b32 v4, v8, v7, vcc\n"
    "v_cndmask_b32 v5, v7, v8, vcc\n"
    "v_and_b32 v6, 1, v3\n"
    "v_mul_u32_u24 v6, 6144, v6\n"
    "v_lshl_add_u32 v6, v2, 4, v6\n"
    "v_lshrrev_b32 v7, 1, v3\n"
    "v_lshl_add_u32 v6, v7, 8, v6\n"
    "s_mul_i32 s42, s58, 12416\n"
    "s_lshl_b32 s43, s37, 9\n"
    "s_add_u32 s42, s42, s43\n"
    "s_add_u32 s42, s42, 6443008\n"
    "v_add_u32 v6, s42, v6\n"
    "s_lshl_b32 s62, s37, 2\n"
    "s_add_u32 s62, s62, 151296\n"
    "v_mov_b32 v7, s62\n"
    "s_add_u32 s62, s62, 28\n"
    "v_lshlrev_b32 v8, 4, v1\n"
    "v_and_b32 v9, 31, v1\n"
    "v_add_u32 v9, 64, v9\n"
    "s_mul_i32 s42, s37, 1168\n"
    "s_add_u32 s42, s42, 64384\n"
    "v_mad_u32_u24 v11, v1, 12, s42\n"
    "v_mad_u32_u24 v12, v9, 12, s42\n"
    "v_add_u32 v50, 8176, v11\n"
    "v_add_u32 v51, 8176, v12\n"
    "v_lshlrev_b32 v9, 4, v9\n"
    "s_mov_b32 s59, 7341056\n"
    "s_movk_i32 s60, 1536\n"
    "s_movk_i32 s61, 0x7ff\n"
    "s_add_u32 s63, s36, 1\n"
    "s_cmpk_ge_u32 s37, 12\n"
    "s_cbranch_scc1 TOPK\n"
    "s_cmpk_lg_u32 s37, 11\n"
    "s_cbranch_scc1 MOE2\n"
    "s_memrealtime s[96:97]\n"
    "v_mul_u32_u24 v13, 11, v2\n"
    "v_add_u32 v13, s58, v13\n"
    "v_subrev_u32 v10, 192, v13\n"
    "v_min_u32 v13, v13, v10\n"
    "v_lshlrev_b32 v13, 7, v13\n"
    "s_mov_b32 s42, 94208\n"
    "s_waitcnt lgkmcnt(0)\n"
    "s_cmpk_lt_u32 s2, 192\n"
    "s_cbranch_scc1 HNT1\n"
    "U_60:\n"
    "s_sleep 8\n"
    "s_memrealtime s[98:99]\n"
    "s_waitcnt lgkmcnt(0)\n"
    "s_sub_u32 s98, s98, s96\n"
    "s_cmpk_lt_u32 s98, 450\n"
    "s_cbranch_scc1 U_60\n"
    "HNT1:\n"
    "s_mov_b64 exec, 0xffff\n"
    "s_branch E_62\n"
    "R_61:\n"
    "s_sleep 12\n"
    "s_sub_u32 s39, s39, 1\n"
    "s_cbranch_scc1 ABORT\n"
    "E_62:\n"
    "buffer_load_dwordx2 v[16:17], v13, s[20:23], s42 offen sc1\n"
    "s_waitcnt vmcnt(0)\n"
    "v_cmp_ne_u32 vcc, s36, v17\n"
    "s_cbranch_vccnz R_61\n"
    "s_mov_b64 exec, -1\n"
    "v_subrev_u32 v16, s96, v16\n"
    "s_nop 1\n"
    "v_max_i32_dpp v16, v16, v16 row_ror:8 row_mask:0xf bank_mask:0xf\n"
    "s_nop 1\n"
    "v_max_i32_dpp v16, v16, v16 row_ror:4 row_mask:0xf bank_mask:0xf\n"
    "s_nop 1\n"
    "v_max_i32_dpp v16, v16, v16 row_ror:2 row_mask:0xf bank_mask:0xf\n"
    "s_nop 1\n"
    "v_max_i32_dpp v16, v16, v16 row_ror:1 row_mask:0xf bank_mask:0xf\n"
    "v_add_u32 v16, s96, v16\n"
    "v_mov_b32 v17, 1\n"
    "s_mov_b64 exec, 1\n"
    "ds_write_b64 v14, v[16:17] offset:112\n"
    "s_mov_b64 exec, -1\n"
    "MOE2:\n"
    "s_branch E_64\n"
    "R_63:\n"
    "s_sleep 4\n"
    "s_sub_u32 s39, s39, 1\n"
    "s_cbranch_scc1 ABORT\n"
    "E_64:\n"
    "ds_read_b32 v10, v14 offset:72\n"
    "s_waitcnt lgkmcnt(0)\n"
    "v_cmp_eq_u32 vcc, 0, v10\n"
    "s_cbranch_vccnz R_63\n"
    "s_setprio 2\n"
    "s_branch MOE3\n"
    "TOPK:\n"
    "s_setprio 3\n"
    "s_mul_i32 s42, s37, 9\n"
    "s_add_u32 s42, s42, s58\n"
    "s_mul_i32 s43, s42, 1821\n"
    "s_lshr_b32 s43, s43, 16\n"
    "s_mul_i32 s43, s43, 36\n"
    "s_sub_u32 s42, s42, s43\n"
    "s_lshl_b32 s42, s42, 7\n"
    "s_sub_u32 s42, 94084, s42\n"
    "v_lshlrev_b32 v13, 4, v0\n"
    "s_mov_b32 s44, 65536\n"
    "s_mov_b32 s45, 69632\n"
    "s_mov_b32 s46, 73728\n"
    "s_mov_b32 s47, 77824\n"
    "v_lshl_add_u32 v16, v2, 2, v14\n"
    "v_lshl_add_u32 v17, v3, 4, v14\n"
    "s_brev_b32 s48, 1\n"
    "v_mov_b32 v18, 4\n"
    "v_lshlrev_b32 v19, 2, v2\n"
    "v_add_u32 v19, 151328, v19\n"
    "v_mul_u32_u24 v52, 1168, v2\n"
    "v_mov_b32 v20, 37832\n"
    "s_mov_b64 exec, 1\n"
    "ds_add_u32 v14, v20 offset:64\n"
    "s_mov_b64 exec, -1\n"
    "s_branch E_66\n"
    "R_65:\n"
    "s_sleep 2\n"
    "s_sub_u32 s39, s39, 1\n"
    "s_cbranch_scc1 ABORT\n"
    "E_66:\n"
    "ds_read_b64 v[20:21], v14 offset:112\n"
    "s_waitcnt lgkmcnt(0)\n"
    "v_cmp_ne_u32 vcc, 1, v21\n"
    "s_cbranch_vccnz R_65\n"
    "v_readfirstlane_b32 s96, v20\n"
    "U_67:\n"
    "s_memrealtime s[98:99]\n"
    "s_waitcnt lgkmcnt(0)\n"
    "s_sub_u32 s98, s98, s96\n"
    "s_cmpk_lt_u32 s98, 185\n"
    "s_cbranch_scc1 U_67\n"
    "buffer_load_dwordx4 v[24:27], v13, s[20:23], s44 offen sc1\n"
    "buffer_load_dwordx4 v[28:31], v13, s[20:23], s45 offen sc1\n"
    "buffer_load_dwordx4 v[32:35], v13, s[20:23], s46 offen sc1\n"
    "buffer_load_dwordx4 v[36:39], v13, s[20:23], s47 offen sc1\n"
    "buffer_load_dwordx2 v[40:41], off, s[20:23], 0 offset:2048 sc1\n"
    "s_branch E_69\n"
    "R_68:\n"
    "s_sub_u32 s39, s39, 1\n"
    "s_cbranch_scc1 ABORT\n"
    "s_wqm_b64 exec, vcc\n"
    "buffer_load_dwordx4 v[24:27], v13, s[20:23], s44 offen sc1\n"
    "buffer_load_dwordx4 v[28:31], v13, s[20:23], s45 offen sc1\n"
    "buffer_load_dwordx4 v[32:35], v13, s[20:23], s46 offen sc1\n"
    "buffer_load_dwordx4 v[36:39], v13, s[20:23], s47 offen sc1\n"
    "buffer_load_dwordx2 v[40:41], off, s[20:23], 0 offset:2048 sc1\n"
    "s_mov_b64 exec, -1\n"
    "E_69:\n"
    "s_waitcnt vmcnt(3)\n"
    "v_bitop3_b32 v20, v25, v27, s36 bitop3:0x7e\n"
    "v_bitop3_b32 v21, v29, v31, s36 bitop3:0x7e\n"
    "v_max3_u32 v42, v24, v26, v28\n"
    "s_waitcnt vmcnt(2)\n"
    "v_bitop3_b32 v22, v33, v35, s36 bitop3:0x7e\n"
    "v_max3_u32 v42, v42, v30, v32\n"
    "v_or3_b32 v20, v20, v21, v22\n"
    "s_waitcnt vmcnt(0)\n"
    "v_bitop3_b32 v21, v37, v39, s36 bitop3:0x7e\n"
    "v_xor_b32 v22, s36, v41\n"
    "v_max3_u32 v42, v42, v34, v36\n"
    "v_or3_b32 v20, v20, v21, v22\n"
    "v_max_u32 v42, v42, v38\n"
    "v_cmp_ne_u32 vcc, 0, v20\n"
    "s_cbranch_vccnz R_68\n"
    "ds_max_u32 v16, v42\n"
    "s_mov_b64 exec, 1\n"
    "ds_add_u32 v14, v15 offset:72\n"
    "s_mov_b64 exec, -1\n"
    "ds_read_b32 v10, v14 offset:72\n"
    "ds_read_b32 v20, v16\n"
    "ds_read_b128 v[46:49], v17\n"
    "v_max3_u32 v45, v24, v26, v28\n"
    "v_med3_u32 v43, v24, v26, v28\n"
    "v_min3_u32 v44, v24, v26, v28\n"
    "v_med3_u32 v44, v43, v44, v30\n"
    "v_med3_u32 v43, v45, v43, v30\n"
    "v_max_u32 v45, v45, v30\n"
    "v_med3_u32 v44, v43, v44, v32\n"
    "v_med3_u32 v43, v45, v43, v32\n"
    "v_max_u32 v45, v45, v32\n"
    "v_med3_u32 v44, v43, v44, v34\n"
    "v_med3_u32 v43, v45, v43, v34\n"
    "v_max_u32 v45, v45, v34\n"
    "v_med3_u32 v44, v43, v44, v36\n"
    "v_med3_u32 v43, v45, v43, v36\n"
    "v_max_u32 v45, v45, v36\n"
    "v_med3_u32 v44, v43, v44, v38\n"
    "v_med3_u32 v43, v45, v43, v38\n"
    "s_branch E_71\n"
    "R_70:\n"
    "s_sub_u32 s39, s39, 1\n"
    "s_cbranch_scc1 ABORT\n"
    "ds_read_b32 v10, v14 offset:72\n"
    "ds_read_b32 v20, v16\n"
    "ds_read_b128 v[46:49], v17\n"
    "E_71:\n"
    "s_waitcnt lgkmcnt(0)\n"
    "v_cmp_ne_u32 vcc, 4, v10\n"
    "v_cmp_gt_u32 s[50:51], v46, v20\n"
    "v_cmp_gt_u32 s[52:53], v47, v20\n"
    "v_cmp_gt_u32 s[54:55], v48, v20\n"
    "s_cbranch_vccnz R_70\n"
    "v_cmp_gt_u32 vcc, v49, v20\n"
    "v_cndmask_b32 v21, 0, 1, s[50:51]\n"
    "v_addc_co_u32 v21, s[50:51], 0, v21, s[52:53]\n"
    "v_addc_co_u32 v21, s[50:51], 0, v21, s[54:55]\n"
    "v_addc_co_u32 v21, vcc, 0, v21, vcc\n"
    "v_mov_b32 v22, v21\n"
    "v_ashrrev_i32 v23, 31, v20\n"
    "v_bitop3_b32 v23, v23, v20, s48 bitop3:0x63\n"
    "v_permlane16_swap_b32 v21, v22\n"
    "v_add_u32 v21, v21, v22\n"
    "v_mov_b32 v22, v21\n"
    "v_sub_f32 v23, v23, v40\n"
    "v_ashrrev_i32 v46, 31, v23\n"
    "v_permlane32_swap_b32 v21, v22\n"
    "v_bitop3_b32 v23, v46, v23, s48 bitop3:0x36\n"
    "v_add_u32 v21, v21, v22\n"
    "v_min_u32 v23, v23, v20\n"
    "v_cmp_eq_u32 vcc, 7, v21\n"
    "s_ff1_i32_b64 s49, vcc\n"
    "v_readlane_b32 s49, v23, s49\n"
    "s_andn2_b32 s49, s49, s61\n"
    "s_nop 0\n"
    "v_cmp_le_u32 s[50:51], s49, v43\n"
    "v_cmp_le_u32 s[52:53], s49, v44\n"
    "v_cmpx_le_u32 vcc, s49, v42\n"
    "v_cndmask_b32 v21, 4, 8, s[50:51]\n"
    "s_cmp_lg_u64 s[52:53], 0\n"
    "s_cbranch_scc1 TOPS\n"
    "ds_add_rtn_u32 v21, v14, v21 offset:64\n"
    "s_waitcnt lgkmcnt(0)\n"
    "ds_write_b32 v21, v42\n"
    "s_mov_b64 exec, s[50:51]\n"
    "ds_write_b32 v21, v43 offset:4\n"
    "TOPC:\n"
    "s_mov_b64 exec, 1\n"
    "ds_add_u32 v14, v15 offset:68\n"
    "s_mov_b64 exec, -1\n"
    "s_cmpk_eq_u32 s37, 15\n"
    "s_cbranch_scc1 MOE15\n"
    "MOE3:\n"
    "ds_read_b64 v[20:21], v14 offset:64\n"
    "ds_read2_b32 v[22:23], v7 offset1:7\n"
    "s_sub_u32 s39, s39, 1\n"
    "s_cbranch_scc1 ABORT\n"
    "s_waitcnt lgkmcnt(0)\n"
    "v_cmp_ne_u32 vcc, 4, v21\n"
    "v_readfirstlane_b32 s42, v20\n"
    "v_bitop3_b32 v22, v22, s61, v22 bitop3:0x0c\n"
    "s_cbranch_vccnz MOE3\n"
    "s_cmp_gt_u32 s42, 151392\n"
    "s_cbranch_scc1 MANY\n"
    "MOE4:\n"
    "v_mad_u32_u24 v24, v22, s60, v8\n"
    "v_mad_u32_u24 v25, v22, s60, v9\n"
    "s_cmp_gt_u32 s42, s62\n"
    "s_cbranch_scc1 ROW2\n"
    "ROW1:\n"
    "buffer_load_dwordx4 v[28:31], v24, s[20:23], s59 offen sc1\n"
    "buffer_load_dwordx4 v[32:35], v25, s[20:23], s59 offen sc1\n"
    "s_branch E_73\n"
    "R_72:\n"
    "s_sleep 1\n"
    "s_sub_u32 s39, s39, 1\n"
    "s_cbranch_scc1 ABORT\n"
    "E_73:\n"
    "ds_read_b32 v10, v14 offset:88\n"
    "s_waitcnt lgkmcnt(0)\n"
    "v_cmp_ne_u32 vcc, 8, v10\n"
    "s_cbranch_vccnz R_72\n"
    "s_waitcnt vmcnt(1)\n"
    "ds_write2_b32 v11, v28, v29 offset1:1\n"
    "ds_write_b32 v11, v30 offset:8\n"
    "s_waitcnt vmcnt(0)\n"
    "ds_write2_b32 v12, v32, v33 offset1:1\n"
    "ds_write_b32 v12, v34 offset:8\n"
    "v_bitop3_b32 v20, v31, v35, s63 bitop3:0x7e\n"
    "v_cmp_ne_u32 vcc, 0, v20\n"
    "s_cbranch_vccnz ROWS\n"
    "MOE5:\n"
    "s_waitcnt lgkmcnt(0)\n"
    "s_barrier\n"
    "s_cmpk_ge_u32 s37, 14\n"
    "s_cbranch_scc1 MOE6\n"
    "s_setprio 0\n"
    "s_sleep 3\n"
    "ds_read_b64_tr_b16 v[56:57], v104 offset:0\n"
    "ds_read_b64_tr_b16 v[58:59], v104 offset:512\n"
    "ds_read_b64_tr_b16 v[60:61], v105 offset:0\n"
    "ds_read_b64_tr_b16 v[62:63], v105 offset:512\n"
    "ds_read_b64_tr_b16 v[64:65], v106 offset:0\n"
    "ds_read_b64_tr_b16 v[66:67], v106 offset:512\n"
    "ds_read_b64_tr_b16 v[68:69], v107 offset:0\n"
    "ds_read_b64_tr_b16 v[70:71], v107 offset:512\n"
    "ds_read_b64_tr_b16 v[72:73], v108 offset:0\n"
    "ds_read_b64_tr_b16 v[74:75], v108 offset:512\n"
    "ds_read_b64_tr_b16 v[76:77], v109 offset:0\n"
    "ds_read_b64_tr_b16 v[78:79], v109 offset:512\n"
    "ds_read_b64_tr_b16 v[80:81], v104 offset:36864\n"
    "ds_read_b64_tr_b16 v[82:83], v104 offset:37376\n"
    "ds_read_b64_tr_b16 v[84:85], v105 offset:36864\n"
    "ds_read_b64_tr_b16 v[86:87], v105 offset:37376\n"
    "ds_read_b64_tr_b16 v[88:89], v106 offset:36864\n"
    "ds_read_b64_tr_b16 v[90:91], v106 offset:37376\n"
    "ds_read_b64_tr_b16 v[92:93], v107 offset:36864\n"
    "ds_read_b64_tr_b16 v[94:95], v107 offset:37376\n"
    "ds_read_b64_tr_b16 v[96:97], v108 offset:36864\n"
    "ds_read_b64_tr_b16 v[98:99], v108 offset:37376\n"
    "ds_read_b64_tr_b16 v[100:101], v109 offset:36864\n"
    "ds_read_b64_tr_b16 v[102:103], v109 offset:37376\n"
    "MOE6:\n"
    "s_cmpk_lt_u32 s37, 13\n"
    "s_cbranch_scc1 DOWN\n"
    "s_cmpk_lt_u32 s2, 192\n"
    "s_cbranch_scc1 DOWN\n"
    "s_and_b32 s42, s2, 7\n"
    "s_lshl_b32 s42, s42, 3\n"
    "s_sub_u32 s43, s2, 192\n"
    "s_lshr_b32 s43, s43, 3\n"
    "s_add_u32 s42, s42, s43\n"
    "s_mul_i32 s42, s42, 3\n"
    "s_add_u32 s42, s42, s37\n"
    "s_sub_u32 s42, s42, 13\n"
    "s_mul_i32 s43, s42, 1366\n"
    "s_lshr_b32 s43, s43, 16\n"
    "s_mul_i32 s44, s43, 48\n"
    "s_sub_u32 s42, s42, s44\n"
    "s_lshl_b32 s42, s42, 2\n"
    "s_lshl_b32 s44, s43, 4\n"
    "s_add_u32 s44, s44, 151168\n"
    "v_and_b32 v20, 1, v0\n"
    "v_lshl_add_u32 v21, v20, 3, s44\n"
    "ds_read_b64 v[22:23], v21\n"
    "s_lshr_b32 s45, s42, 4\n"
    "s_lshl_b32 s45, s45, 5\n"
    "s_add_u32 s45, s45, s42\n"
    "v_lshrrev_b32 v25, 1, v2\n"
    "v_min_u32 v26, 2, v25\n"
    "v_lshl_add_u32 v27, v26, 4, v3\n"
    "v_add_u32 v27, s45, v27\n"
    "v_mul_u32_u24 v24, 683, v27\n"
    "v_lshrrev_b32 v24, 11, v24\n"
    "v_mul_u32_u24 v26, 3, v24\n"
    "v_sub_u32 v26, v27, v26\n"
    "v_lshlrev_b32 v28, 3, v27\n"
    "s_mov_b32 s45, 43520\n"
    "s_mov_b32 s46, 71168\n"
    "v_mul_u32_u24 v24, 16512, v24\n"
    "s_waitcnt lgkmcnt(0)\n"
    "v_bitop3_b32 v29, v23, s61, v23 bitop3:0x0c\n"
    "v_lshl_add_u32 v22, v27, 1, v22\n"
    "v_add_u32 v22, 73728, v22\n"
    "v_mov_b32 v40, 0\n"
    "v_mov_b32 v41, 0\n"
    "ds_read_u16_d16_hi v40, v22\n"
    "v_mul_u32_u24 v30, 1152, v29\n"
    "v_lshl_add_u32 v30, v27, 1, v30\n"
    "global_load_short_d16_hi v41, v30, s[4:5]\n"
    "v_lshrrev_b32 v31, 1, v29\n"
    "v_lshl_add_u32 v24, v31, 4, v24\n"
    "s_mov_b32 s47, 131072\n"
    "v_lshrrev_b32 v31, 4, v29\n"
    "v_lshlrev_b32 v31, 2, v31\n"
    "s_setprio 0\n"
    "s_cmpk_eq_u32 s37, 13\n"
    "s_cbranch_scc1 DOWN\n"
    "s_waitcnt lgkmcnt(0)\n"
    "s_barrier\n"
    "s_branch FIN\n"
    "DOWN:\n"
    "s_cmpk_ge_u32 s37, 14\n"
    "s_cbranch_scc1 END\n"
    "s_setprio 3\n"
    "v_mov_b32 v47, s36\n"
    "v_mov_b32 v51, s36\n"
    "s_waitcnt lgkmcnt(0)\n"
    "s_barrier\n"
    "ds_read_b128 v[104:107], v4\n"
    "ds_read_b128 v[108:111], v4 offset:64\n"
    "ds_read_b128 v[112:115], v5\n"
    "ds_read_b128 v[116:119], v5 offset:64\n"
    "s_waitcnt lgkmcnt(3)\n"
    "v_mfma_f32_16x16x32_bf16 v[8:11], v[104:107], v[56:59], 0\n"
    "v_mfma_f32_16x16x32_bf16 v[12:15], v[104:107], v[60:63], 0\n"
    "v_mfma_f32_16x16x32_bf16 v[16:19], v[104:107], v[64:67], 0\n"
    "s_waitcnt lgkmcnt(2)\n"
    "v_mfma_f32_16x16x32_bf16 v[8:11], v[108:111], v[80:83], v[8:11]\n"
    "v_mfma_f32_16x16x32_bf16 v[12:15], v[108:111], v[84:87], v[12:15]\n"
    "v_mfma_f32_16x16x32_bf16 v[16:19], v[108:111], v[88:91], v[16:19]\n"
    "s_waitcnt lgkmcnt(1)\n"
    "v_mfma_f32_16x16x32_bf16 v[8:11], v[112:115], v[68:71], v[8:11]\n"
    "v_mfma_f32_16x16x32_bf16 v[12:15], v[112:115], v[72:75], v[12:15]\n"
    "v_mfma_f32_16x16x32_bf16 v[16:19], v[112:115], v[76:79], v[16:19]\n"
    "s_waitcnt lgkmcnt(0)\n"
    "v_mfma_f32_16x16x32_bf16 v[8:11], v[116:119], v[92:95], v[8:11]\n"
    "v_mfma_f32_16x16x32_bf16 v[12:15], v[116:119], v[96:99], v[12:15]\n"
    "v_mfma_f32_16x16x32_bf16 v[16:19], v[116:119], v[100:103], v[16:19]\n"
    "s_nop 5\n"
    "v_cvt_pk_bf16_f32 v44, v8, v9\n"
    "v_cvt_pk_bf16_f32 v45, v12, v13\n"
    "v_cvt_pk_bf16_f32 v46, v16, v17\n"
    "buffer_store_dwordx4 v[44:47], v6, s[20:23], 0 offen offset:0 sc1\n"
    "v_cvt_pk_bf16_f32 v48, v10, v11\n"
    "v_cvt_pk_bf16_f32 v49, v14, v15\n"
    "v_cvt_pk_bf16_f32 v50, v18, v19\n"
    "buffer_store_dwordx4 v[48:51], v6, s[20:23], 0 offen offset:3072 sc1\n"
    "s_cmpk_lg_u32 s37, 13\n"
    "s_cbranch_scc1 END\n"
    "s_cmpk_lt_u32 s2, 192\n"
    "s_cbranch_scc1 END\n"
    "FIN:\n"
    "s_branch E_75\n"
    "R_74:\n"
    "s_sub_u32 s39, s39, 1\n"
    "s_cbranch_scc1 ABORT\n"
    "E_75:\n"
    "buffer_load_dwordx4 v[32:35], v24, s[20:23], s47 offen sc1\n"
    "buffer_load_dwordx2 v[36:37], v28, s[20:23], s45 offen sc1\n"
    "buffer_load_dwordx2 v[38:39], v28, s[20:23], s46 offen sc1\n"
    "s_waitcnt vmcnt(0)\n"
    "v_bitop3_b32 v42, v37, v39, s36 bitop3:0x7e\n"
    "v_xor_b32 v43, s36, v35\n"
    "v_or_b32 v42, v42, v43\n"
    "v_cmp_ne_u32 vcc, 0, v42\n"
    "s_cbranch_vccnz R_74\n"
    "v_cmp_eq_u32 vcc, 1, v26\n"
    "v_cmp_eq_u32 s[50:51], 2, v26\n"
    "v_and_b32 v42, 1, v29\n"
    "v_cndmask_b32 v32, v32, v33, vcc\n"
    "v_cndmask_b32 v32, v32, v34, s[50:51]\n"
    "v_lshlrev_b32 v42, 4, v42\n"
    "v_lshrrev_b32 v32, v42, v32\n"
    "v_lshlrev_b32 v32, 16, v32\n"
    "v_add_f32 v41, v41, v32\n"
    "v_add_f32 v36, v36, v38\n"
    "v_cvt_pk_bf16_f32 v41, 0, v41\n"
    "v_cvt_pk_bf16_f32 v36, 0, v36\n"
    "s_brev_b32 s48, 1\n"
    "v_ashrrev_i32 v42, 31, v23\n"
    "v_bitop3_b32 v23, v42, v23, s48 bitop3:0x63\n"
    "v_and_b32 v23, 0xfffff800, v23\n"
    "v_cvt_pk_bf16_f32 v23, 0, v23\n"
    "v_mul_f32 v23, 0xbfb8aa3b, v23\n"
    "v_exp_f32 v23, v23\n"
    "v_lshlrev_b32 v20, 4, v20\n"
    "v_add_f32 v23, 1.0, v23\n"
    "v_rcp_f32 v23, v23\n"
    "s_movk_i32 s44, 0x3f80\n"
    "v_lshlrev_b32_e64 v20, v20, s44\n"
    "v_cvt_pk_bf16_f32 v23, 0, v23\n"
    "v_cmp_eq_u32 vcc, 0, v25\n"
    "s_nop 1\n"
    "v_cndmask_b32 v32, 0, v20, vcc\n"
    "v_cmp_eq_u32 vcc, 1, v25\n"
    "s_nop 1\n"
    "v_cndmask_b32 v33, 0, v20, vcc\n"
    "v_cmp_eq_u32 vcc, 2, v25\n"
    "s_nop 1\n"
    "v_cndmask_b32 v34, 0, v20, vcc\n"
    "v_mov_b32 v35, 0\n"
    "v_lshrrev_b32 v42, 2, v1\n"
    "v_mul_u32_u24 v42, 12416, v42\n"
    "v_and_b32 v43, 3, v0\n"
    "v_lshl_add_u32 v42, v43, 4, v42\n"
    "s_mul_i32 s43, s43, 3072\n"
    "s_lshl_b32 s42, s42, 4\n"
    "s_add_u32 s42, s42, s43\n"
    "s_add_u32 s50, s42, 6447104\n"
    "s_add_u32 s51, s42, 6645760\n"
    "s_add_u32 s52, s42, 6844416\n"
    "s_add_u32 s53, s42, 7043072\n"
    "s_add_u32 s54, s42, 7241728\n"
    "v_cmp_gt_u32 vcc, 32, v1\n"
    "v_mov_b32 v43, 0x7ffffff0\n"
    "v_mov_b32 v21, s36\n"
    "v_cndmask_b32 v43, v43, v42, vcc\n"
    "v_cndmask_b32 v21, 0, v21, vcc\n"
    "s_mov_b32 s46, 0x003f003f\n"
    "s_mov_b32 s47, 0x003f003f\n"
    "s_sleep 4\n"
    "buffer_load_dwordx4 v[104:107], v42, s[20:23], s50 offen sc1\n"
    "buffer_load_dwordx4 v[108:111], v42, s[20:23], s51 offen sc1\n"
    "buffer_load_dwordx4 v[112:115], v42, s[20:23], s52 offen sc1\n"
    "buffer_load_dwordx4 v[116:119], v42, s[20:23], s53 offen sc1\n"
    "buffer_load_dwordx4 v[120:123], v43, s[20:23], s54 offen sc1\n"
    "buffer_load_dword v22, v31, s[20:23], 0 offen offset:1024 sc1\n"
    "s_branch E_77\n"
    "R_76:\n"
    "s_sub_u32 s39, s39, 1\n"
    "s_cbranch_scc1 ABORT\n"
    "s_wqm_b64 exec, vcc\n"
    "buffer_load_dwordx4 v[104:107], v42, s[20:23], s50 offen sc1\n"
    "buffer_load_dwordx4 v[108:111], v42, s[20:23], s51 offen sc1\n"
    "buffer_load_dwordx4 v[112:115], v42, s[20:23], s52 offen sc1\n"
    "buffer_load_dwordx4 v[116:119], v42, s[20:23], s53 offen sc1\n"
    "buffer_load_dwordx4 v[120:123], v43, s[20:23], s54 offen sc1\n"
    "buffer_load_dword v22, v31, s[20:23], 0 offen offset:1024 sc1\n"
    "s_mov_b64 exec, -1\n"
    "E_77:\n"
    "s_waitcnt vmcnt(2)\n"
    "v_bitop3_b32 v24, v107, v111, s36 bitop3:0x7e\n"
    "v_bitop3_b32 v25, v115, v119, s36 bitop3:0x7e\n"
    "s_waitcnt vmcnt(0)\n"
    "v_xor_b32 v26, v21, v123\n"
    "v_bitop3_b32 v24, v24, v22, s36 bitop3:0xf6\n"
    "v_or3_b32 v24, v24, v25, v26\n"
    "v_cmp_ne_u32 vcc, 0, v24\n"
    "s_cbranch_vccnz R_76\n"
    "v_mov_b32 v107, 0\n"
    "v_mov_b32 v111, 0\n"
    "v_mov_b32 v115, 0\n"
    "v_mov_b32 v119, 0\n"
    "v_mov_b32 v123, 0\n"
    "v_mfma_f32_16x16x32_bf16 v[8:11], v[104:107], v[32:35], 0\n"
    "v_mfma_f32_16x16x32_bf16 v[8:11], v[108:111], v[32:35], v[8:11]\n"
    "v_mfma_f32_16x16x32_bf16 v[8:11], v[112:115], v[32:35], v[8:11]\n"
    "v_mfma_f32_16x16x32_bf16 v[8:11], v[116:119], v[32:35], v[8:11]\n"
    "v_mfma_f32_16x16x32_bf16 v[8:11], v[120:123], v[32:35], v[8:11]\n"
    "s_nop 7\n"
    "v_permlane32_swap_b32 v8, v10\n"
    "v_permlane32_swap_b32 v9, v11\n"
    "v_add_f32 v8, v8, v10\n"
    "v_add_f32 v9, v9, v11\n"
    "s_nop 1\n"
    "v_permlane16_swap_b32 v8, v9\n"
    "v_add_f32 v8, v8, v9\n"
    "v_cvt_pk_bf16_f32 v8, 0, v8\n"
    "v_mul_f32 v8, v8, v23\n"
    "v_cvt_pk_bf16_f32 v8, 0, v8\n"
    "v_add_f32 v8, v40, v8\n"
    "v_cvt_pk_bf16_f32 v8, 0, v8\n"
    "v_mul_f32 v8, v36, v8\n"
    "v_cvt_pk_bf16_f32 v8, 0, v8\n"
    "v_add_f32 v8, v41, v8\n"
    "v_cvt_pk_bf16_f32 v8, v8, v8\n"
    "s_mov_b64 exec, s[46:47]\n"
    "global_store_short v30, v8, s[34:35]\n"
    "s_mov_b64 exec, -1\n"
    "s_branch END\n"
    "ROW2:\n"
    "v_bitop3_b32 v23, v23, s61, v23 bitop3:0x0c\n"
    "v_mad_u32_u24 v26, v23, s60, v8\n"
    "v_mad_u32_u24 v27, v23, s60, v9\n"
    "ROW3:\n"
    "buffer_load_dwordx4 v[28:31], v24, s[20:23], s59 offen sc1\n"
    "buffer_load_dwordx4 v[32:35], v25, s[20:23], s59 offen sc1\n"
    "buffer_load_dwordx4 v[36:39], v26, s[20:23], s59 offen sc1\n"
    "buffer_load_dwordx4 v[40:43], v27, s[20:23], s59 offen sc1\n"
    "s_branch E_79\n"
    "R_78:\n"
    "s_sleep 1\n"
    "s_sub_u32 s39, s39, 1\n"
    "s_cbranch_scc1 ABORT\n"
    "E_79:\n"
    "ds_read_b32 v10, v14 offset:88\n"
    "s_waitcnt lgkmcnt(0)\n"
    "v_cmp_ne_u32 vcc, 8, v10\n"
    "s_cbranch_vccnz R_78\n"
    "s_waitcnt vmcnt(3)\n"
    "ds_write2_b32 v11, v28, v29 offset1:1\n"
    "ds_write_b32 v11, v30 offset:8\n"
    "s_waitcnt vmcnt(2)\n"
    "ds_write2_b32 v12, v32, v33 offset1:1\n"
    "ds_write_b32 v12, v34 offset:8\n"
    "s_waitcnt vmcnt(1)\n"
    "ds_write2_b32 v50, v36, v37 offset1:1\n"
    "ds_write_b32 v50, v38 offset:8\n"
    "s_waitcnt vmcnt(0)\n"
    "ds_write2_b32 v51, v40, v41 offset1:1\n"
    "ds_write_b32 v51, v42 offset:8\n"
    "v_bitop3_b32 v20, v31, v35, s63 bitop3:0x7e\n"
    "v_bitop3_b32 v21, v39, v43, s63 bitop3:0x7e\n"
    "v_or_b32 v20, v20, v21\n"
    "v_cmp_ne_u32 vcc, 0, v20\n"
    "s_add_u32 s43, s62, 28\n"
    "s_cbranch_vccnz ROWT\n"
    "s_cmp_le_u32 s42, s43\n"
    "s_cbranch_scc1 MOE5\n"
    "ds_read_b32 v22, v7 offset:56\n"
    "v_add_u32 v11, 16352, v11\n"
    "v_add_u32 v12, 16352, v12\n"
    "s_waitcnt lgkmcnt(0)\n"
    "v_bitop3_b32 v22, v22, s61, v22 bitop3:0x0c\n"
    "v_mad_u32_u24 v24, v22, s60, v8\n"
    "v_mad_u32_u24 v25, v22, s60, v9\n"
    "s_branch ROW1\n"
    "MOE15:\n"
    "ds_read_b64 v[20:21], v14 offset:64\n"
    "ds_read_b32 v32, v19\n"
    "s_sub_u32 s39, s39, 1\n"
    "s_cbranch_scc1 ABORT\n"
    "s_waitcnt lgkmcnt(0)\n"
    "v_cmp_ne_u32 vcc, 4, v21\n"
    "v_readfirstlane_b32 s42, v20\n"
    "v_bitop3_b32 v32, v32, s61, v32 bitop3:0x0c\n"
    "s_cbranch_vccnz MOE15\n"
    "s_cmp_gt_u32 s42, 151392\n"
    "s_cbranch_scc1 MANY\n"
    "MOE16:\n"
    "v_lshlrev_b32 v32, 3, v32\n"
    "s_movk_i32 s43, 4096\n"
    "v_cmp_gt_u32 s[50:51], s42, v19\n"
    "R_80:\n"
    "buffer_load_dwordx2 v[34:35], v32, s[20:23], s43 offen sc1\n"
    "s_waitcnt vmcnt(0)\n"
    "v_cmp_ne_u32 vcc, s36, v35\n"
    "v_cndmask_b32 v53, 0, v34, s[50:51]\n"
    "s_and_b64 vcc, vcc, s[50:51]\n"
    "s_cbranch_vccnz XKS\n"
    "v_mov_b32 v34, v53\n"
    "s_nop 1\n"
    "v_mov_b32_dpp v34, v53 row_ror:4 row_mask:0x2 bank_mask:0xf\n"
    "s_nop 1\n"
    "v_mov_b32_dpp v34, v53 row_ror:8 row_mask:0x4 bank_mask:0xf\n"
    "s_nop 1\n"
    "v_mov_b32_dpp v34, v53 row_ror:12 row_mask:0x8 bank_mask:0xf\n"
    "v_cmp_gt_u32 s[52:53], v34, v53\n"
    "s_nop 0\n"
    "v_mov_b32_dpp v35, v34 row_ror:1 row_mask:0xf bank_mask:0xf\n"
    "v_mov_b32_dpp v36, v34 row_ror:2 row_mask:0xf bank_mask:0xf\n"
    "v_mov_b32_dpp v37, v34 row_ror:3 row_mask:0xf bank_mask:0xf\n"
    "v_cmp_gt_u32 s[54:55], v35, v53\n"
    "v_cmp_gt_u32 s[50:51], v36, v53\n"
    "v_cmp_gt_u32 vcc, v37, v53\n"
    "v_cndmask_b32 v34, 0, 1, s[52:53]\n"
    "v_addc_co_u32 v34, s[52:53], 0, v34, s[54:55]\n"
    "v_addc_co_u32 v34, s[52:53], 0, v34, s[50:51]\n"
    "v_addc_co_u32 v34, vcc, 0, v34, vcc\n"
    "v_mov_b32 v35, v34\n"
    "s_nop 1\n"
    "v_permlane16_swap_b32 v34, v35\n"
    "v_add_u32 v34, v34, v35\n"
    "v_mov_b32 v35, v34\n"
    "s_nop 1\n"
    "v_permlane32_swap_b32 v34, v35\n"
    "v_add_u32 v34, v34, v35\n"
    "v_min_u32 v34, 8, v34\n"
    "v_lshl_add_u32 v34, v34, 3, v14\n"
    "ds_write_b64 v34, v[52:53] offset:3072\n"
    "s_branch MOE5\n"
    "XKS:\n"
    "s_sub_u32 s39, s39, 1\n"
    "s_cbranch_scc1 ABORT\n"
    "s_branch R_80\n"
    "ROWS:\n"
    "s_sub_u32 s39, s39, 1\n"
    "s_cbranch_scc1 ABORT\n"
    "s_branch ROW1\n"
    "ROWT:\n"
    "s_sub_u32 s39, s39, 1\n"
    "s_cbranch_scc1 ABORT\n"
    "s_branch ROW3\n"
    "TOPS:\n"
    "s_mov_b64 exec, -1\n"
    "v_cmpx_le_u32 vcc, s49, v24\n"
    "ds_add_rtn_u32 v21, v14, v18 offset:64\n"
    "s_waitcnt lgkmcnt(0)\n"
    "ds_write_b32 v21, v24\n"
    "s_mov_b64 exec, -1\n"
    "v_cmpx_le_u32 vcc, s49, v26\n"
    "ds_add_rtn_u32 v21, v14, v18 offset:64\n"
    "s_waitcnt lgkmcnt(0)\n"
    "ds_write_b32 v21, v26\n"
    "s_mov_b64 exec, -1\n"
    "v_cmpx_le_u32 vcc, s49, v28\n"
    "ds_add_rtn_u32 v21, v14, v18 offset:64\n"
    "s_waitcnt lgkmcnt(0)\n"
    "ds_write_b32 v21, v28\n"
    "s_mov_b64 exec, -1\n"
    "v_cmpx_le_u32 vcc, s49, v30\n"
    "ds_add_rtn_u32 v21, v14, v18 offset:64\n"
    "s_waitcnt lgkmcnt(0)\n"
    "ds_write_b32 v21, v30\n"
    "s_mov_b64 exec, -1\n"
    "v_cmpx_le_u32 vcc, s49, v32\n"
    "ds_add_rtn_u32 v21, v14, v18 offset:64\n"
    "s_waitcnt lgkmcnt(0)\n"
    "ds_write_b32 v21, v32\n"
    "s_mov_b64 exec, -1\n"
    "v_cmpx_le_u32 vcc, s49, v34\n"
    "ds_add_rtn_u32 v21, v14, v18 offset:64\n"
    "s_waitcnt lgkmcnt(0)\n"
    "ds_write_b32 v21, v34\n"
    "s_mov_b64 exec, -1\n"
    "v_cmpx_le_u32 vcc, s49, v36\n"
    "ds_add_rtn_u32 v21, v14, v18 offset:64\n"
    "s_waitcnt lgkmcnt(0)\n"
    "ds_write_b32 v21, v36\n"
    "s_mov_b64 exec, -1\n"
    "v_cmpx_le_u32 vcc, s49, v38\n"
    "ds_add_rtn_u32 v21, v14, v18 offset:64\n"
    "s_waitcnt lgkmcnt(0)\n"
    "ds_write_b32 v21, v38\n"
    "s_branch TOPC\n"
    "MANY:\n"
    "v_lshlrev_b32 v22, 2, v0\n"
    "v_add_u32 v22, 149280, v22\n"
    "MANY1:\n"
    "v_cmpx_gt_u32 vcc, s42, v22\n"
    "s_cbranch_execz MANY3\n"
    "ds_read_b32 v23, v22\n"
    "v_mov_b32 v24, 0\n"
    "s_mov_b32 s43, 151328\n"
    "MANY2:\n"
    "v_mov_b32 v25, s43\n"
    "ds_read_b32 v25, v25\n"
    "s_add_u32 s43, s43, 4\n"
    "s_waitcnt lgkmcnt(0)\n"
    "v_cmp_gt_u32 vcc, v25, v23\n"
    "s_nop 1\n"
    "v_addc_co_u32 v24, vcc, 0, v24, vcc\n"
    "s_cmp_lt_u32 s43, s42\n"
    "s_cbranch_scc1 MANY2\n"
    "v_min_u32 v24, 16, v24\n"
    "v_lshl_add_u32 v24, v24, 2, v14\n"
    "ds_write_b32 v24, v23 offset:3152\n"
    "v_add_u32 v22, 2048, v22\n"
    "s_branch MANY1\n"
    "MANY3:\n"
    "s_mov_b64 exec, -1\n"
    "s_mov_b64 exec, 1\n"
    "ds_add_u32 v14, v15 offset:92\n"
    "s_mov_b64 exec, -1\n"
    "s_branch E_82\n"
    "R_81:\n"
    "s_sleep 1\n"
    "s_sub_u32 s39, s39, 1\n"
    "s_cbranch_scc1 ABORT\n"
    "E_82:\n"
    "ds_read_b32 v10, v14 offset:92\n"
    "s_waitcnt lgkmcnt(0)\n"
    "v_cmp_ne_u32 vcc, 8, v10\n"
    "s_cbranch_vccnz R_81\n"
    "v_subrev_u32 v7, 80, v7\n"
    "v_lshl_add_u32 v24, v2, 2, v14\n"
    "ds_read2_b32 v[22:23], v7 offset1:7\n"
    "ds_read_b32 v32, v24 offset:3152\n"
    "s_mov_b32 s42, 151392\n"
    "s_waitcnt lgkmcnt(0)\n"
    "v_bitop3_b32 v22, v22, s61, v22 bitop3:0x0c\n"
    "v_bitop3_b32 v32, v32, s61, v32 bitop3:0x0c\n"
    "s_cmpk_eq_u32 s37, 15\n"
    "s_cbranch_scc1 MOE16\n"
    "s_branch MOE4\n"
    "END:\n"
    "s_endpgm\n"

    :: "{s[0:1]}"(__builtin_amdgcn_kernarg_segment_ptr()), "{s2}"(blockIdx.x), "{v0}"(threadIdx.x) : "v127", "s99", "vcc", "memory");
}

extern "C" void kernel_launch(void* const* d_in, const int* in_sizes, int n_in, void* d_out, int out_size, void* d_ws, size_t ws_size, hipStream_t stream) {
  (void)hipFuncSetAttribute((const void*)mega, hipFuncAttributeMaxDynamicSharedMemorySize, 159584);
  Args p;
  p.x = d_in[0]; p.lar = d_in[1]; p.aim = d_in[2]; p.ldt = d_in[5]; p.cvec = d_in[12]; p.wmod = d_in[13];
  p.ws = (char*)d_ws + ((size_t)240 << 20); p.ws_bytes = 10643968; p.ws_flags = 0x00020000u;
  p.cre = d_in[3]; p.cim = d_in[4]; p.dsk = d_in[6]; p.wr = d_in[8]; p.wg = d_in[9]; p.wu = d_in[10]; p.wd = d_in[11];
  p.out = d_out;
  mega<<<256, 1024, 159584, stream>>>(p);
}
```

```cpp
#include <hip/hip_runtime.h>
#include <stdint.h>

struct Args {
  const void *x, *lar, *aim, *ldt, *cre, *cim, *wmod, *cvec;
  void* ws; uint32_t ws_bytes, ws_flags;
  const void *dsk, *wr, *wg, *wu, *wd;
  void* out;
};
extern "C" __device__ __attribute__((used, aligned(128))) const uint32_t mega_tab[3840] = {
  0x0, 0x0, 0x0, 0x0, 0x0, 0x0, 0x0, 0x0, 0x0, 0x0, 0x0, 0x0, 0x0, 0x0, 0x0, 0x0,
  0x0, 0x0, 0x0, 0x0, 0x0, 0x0, 0x0, 0x0, 0x0, 0x0, 0x0, 0x0, 0x0, 0x0, 0x0, 0x0,
  0x0, 0x0, 0x0, 0x0, 0x0, 0x0, 0x0, 0x0, 0x0, 0x0, 0x0, 0x0, 0x0, 0x0, 0x0, 0x3c000000,
  0x0, 0x0, 0x0, 0x3c003c00, 0x0, 0x0, 0x3c000000, 0x3c003c00, 0x0, 0x0, 0x3c003c00, 0x3c00, 0x0, 0x3c000000, 0x3c003c00, 0x0,
  0x0, 0x0, 0x0, 0x0, 0x0, 0x0, 0x0, 0x0, 0x0, 0x0, 0x0, 0x0, 0x0, 0x0, 0x0, 0x3c000000,
  0x0, 0x0, 0x0, 0x3c003c00, 0x0, 0x0, 0x3c000000, 0x3c003c00, 0x0, 0x0, 0x3c003c00, 0x3c00, 0x0, 0x3c000000, 0x3c003c00, 0x0,
  0x0, 0x3c003c00, 0x3c00, 0x0, 0x3c000000, 0x3c003c00, 0x0, 0x0, 0x3c003c00, 0x3c00, 0x0, 0x0, 0x3c003c00, 0x0, 0x0, 0x0,
  0x3c00, 0x0, 0x0, 0x0, 0x0, 0x0, 0x0, 0x0, 0x0, 0x0, 0x0, 0x0, 0x0, 0x0, 0x0, 0x0,
  0x0, 0x3c003c00, 0x3c00, 0x0, 0x3c000000, 0x3c003c00, 0x0, 0x0, 0x3c003c00, 0x3c00, 0x0, 0x0, 0x3c003c00, 0x0, 0x0, 0x0,
  0x3c00, 0x0, 0x0, 0x0, 0x0, 0x0, 0x0, 0x0, 0x0, 0x0, 0x0, 0x0, 0x0, 0x0, 0x0, 0x0,
  0x0, 0x0, 0x0, 0x0, 0x0, 0x0, 0x0, 0x0, 0x0, 0x0, 0x0, 0x0, 0x0, 0x0, 0x0, 0x0,
  0x0, 0x0, 0x0, 0x0, 0x0, 0x0, 0x0, 0x0, 0x0, 0x0, 0x0, 0x0, 0x0, 0x0, 0x0, 0x0,
  0x0, 0x0, 0x0, 0x0, 0x0, 0x0, 0x0, 0x0, 0x0, 0x0, 0x0, 0x0, 0x0, 0x0, 0x0, 0x0,
  0x0, 0x0, 0x0, 0x0, 0x0, 0x0, 0x0, 0x0, 0x0, 0x0, 0x0, 0x0, 0x0, 0x0, 0x0, 0x0,
  0x0, 0x0, 0x0, 0x0, 0x0, 0x0, 0x0, 0x0, 0x0, 0x0, 0x0, 0x0, 0x0, 0x0, 0x0, 0x0,
  0x0, 0x0, 0x0, 0x0, 0x0, 0x0, 0x0, 0x0, 0x0, 0x0, 0x0, 0x0, 0x0, 0x0, 0x0, 0x0,
  0x0, 0x0, 0x0, 0x0, 0x0, 0x0, 0x0, 0x0, 0x0, 0x0, 0x0, 0x0, 0x0, 0x0, 0x0, 0x0,
  0x0, 0x0, 0x0, 0x0, 0x0, 0x0, 0x0, 0x0, 0x0, 0x0, 0x0, 0x0, 0x0, 0x0, 0x0, 0x0,
  0x0, 0x0, 0x0, 0x0, 0x0, 0x0, 0x0, 0x0, 0x0, 0x0, 0x0, 0x0, 0x0, 0x0, 0x0, 0x0,
  0x0, 0x0, 0x0, 0x0, 0x0, 0x0, 0x0, 0x0, 0x0, 0x0, 0x0, 0x0, 0x0, 0x0, 0x0, 0x0,
  0x0, 0x0, 0x0, 0x0, 0x0, 0x0, 0x0, 0x0, 0x0, 0x0, 0x0, 0x0, 0x0, 0x0, 0x0, 0x0,
  0x0, 0x0, 0x0, 0x0, 0x0, 0x0, 0x0, 0x0, 0x0, 0x0, 0x0, 0x0, 0x0, 0x0, 0x0, 0x0,
  0x0, 0x0, 0x0, 0x0, 0x0, 0x0, 0x0, 0x0, 0x0, 0x0, 0x0, 0x0, 0x0, 0x0, 0x0, 0x3c000000,
  0x0, 0x0, 0x0, 0x3c003c00, 0x0, 0x0, 0x3c000000, 0x3c003c00, 0x0, 0x0, 0x3c003c00, 0x3c00, 0x0, 0x3c000000, 0x3c003c00, 0x0,
  0x0, 0x0, 0x0, 0x0, 0x0, 0x0, 0x0, 0x0, 0x0, 0x0, 0x0, 0x0, 0x0, 0x0, 0x0, 0x3c000000,
  0x0, 0x0, 0x0, 0x3c003c00, 0x0, 0x0, 0x3c000000, 0x3c003c00, 0x0, 0x0, 0x3c003c00, 0x3c00, 0x0, 0x3c000000, 0x3c003c00, 0x0,
  0x0, 0x3c003c00, 0x3c00, 0x0, 0x3c000000, 0x3c003c00, 0x0, 0x0, 0x3c003c00, 0x3c00, 0x0, 0x0, 0x3c003c00, 0x0, 0x0, 0x0,
  0x3c00, 0x0, 0x0, 0x0, 0x0, 0x0, 0x0, 0x0, 0x0, 0x0, 0x0, 0x0, 0x0, 0x0, 0x0, 0x0,
  0x0, 0x3c003c00, 0x3c00, 0x0, 0x3c000000, 0x3c003c00, 0x0, 0x0, 0x3c003c00, 0x3c00, 0x0, 0x0, 0x3c003c00, 0x0, 0x0, 0x0,
  0x3c00, 0x0, 0x0, 0x0, 0x0, 0x0, 0x0, 0x0, 0x0, 0x0, 0x0, 0x0, 0x0, 0x0, 0x0, 0x0,
  0x0, 0x0, 0x0, 0x0, 0x0, 0x0, 0x0, 0x0, 0x0, 0x0, 0x0, 0x0, 0x0, 0x0, 0x0, 0x0,
  0x0, 0x0, 0x0, 0x0, 0x0, 0x0, 0x0, 0x0, 0x0, 0x0, 0x0, 0x0, 0x0, 0x0, 0x0, 0x0,
  0x0, 0x0, 0x0, 0x0, 0x0, 0x0, 0x0, 0x0, 0x0, 0x0, 0x0, 0x0, 0x0, 0x0, 0x0, 0x0,
  0x0, 0x0, 0x0, 0x0, 0x0, 0x0, 0x0, 0x0, 0x0, 0x0, 0x0, 0x0, 0x0, 0x0, 0x0, 0x0,
  0x0, 0x0, 0x0, 0x0, 0x0, 0x0, 0x0, 0x0, 0x0, 0x0, 0x0, 0x0, 0x0, 0x0, 0x0, 0x0,
  0x0, 0x0, 0x0, 0x0, 0x0, 0x0, 0x0, 0x0, 0x0, 0x0, 0x0, 0x3c000000, 0x0, 0x0, 0x0, 0x3c003c00,
  0x0, 0x0, 0x0, 0x0, 0x0, 0x0, 0x0, 0x0, 0x0, 0x0, 0x0, 0x0, 0x0, 0x0, 0x0, 0x0,
  0x0, 0x0, 0x0, 0x0, 0x0, 0x0, 0x0, 0x0, 0x0, 0x0, 0x0, 0x3c000000, 0x0, 0x0, 0x0, 0x3c003c00,
  0x0, 0x0, 0x3c000000, 0x3c003c00, 0x0, 0x0, 0x3c003c00, 0x3c003c00, 0x0, 0x3c000000, 0x3c003c00, 0x3c003c00, 0x0, 0x3c003c00, 0x3c003c00, 0x3c00,
  0x3c000000, 0x3c003c00, 0x3c003c00, 0x0, 0x3c003c00, 0x3c003c00, 0x3c00, 0x0, 0x3c003c00, 0x3c003c00, 0x0, 0x0, 0x3c003c00, 0x3c00, 0x0, 0x0,
  0x0, 0x0, 0x3c000000, 0x3c003c00, 0x0, 0x0, 0x3c003c00, 0x3c003c00, 0x0, 0x3c000000, 0x3c003c00, 0x3c003c00, 0x0, 0x3c003c00, 0x3c003c00, 0x3c00,
  0x3c000000, 0x3c003c00, 0x3c003c00, 0x0, 0x3c003c00, 0x3c003c00, 0x3c00, 0x0, 0x3c003c00, 0x3c003c00, 0x0, 0x0, 0x3c003c00, 0x3c00, 0x0, 0x0,
  0x3c003c00, 0x0, 0x0, 0x0, 0x3c00, 0x0, 0x0, 0x0, 0x0, 0x0, 0x0, 0x0, 0x0, 0x0, 0x0, 0x0,
  0x0, 0x0, 0x0, 0x0, 0x0, 0x0, 0x0, 0x0, 0x0, 0x0, 0x0, 0x0, 0x0, 0x0, 0x0, 0x0,
  0x3c003c00, 0x0, 0x0, 0x0, 0x3c00, 0x0, 0x0, 0x0, 0x0, 0x0, 0x0, 0x0, 0x0, 0x0, 0x0, 0x0,
  0x0, 0x0, 0x0, 0x0, 0x0, 0x0, 0x0, 0x0, 0x0, 0x0, 0x0, 0x0, 0x0, 0x0, 0x0, 0x0,
  0x0, 0x0, 0x0, 0x0, 0x0, 0x0, 0x0, 0x0, 0x0, 0x0, 0x0, 0x0, 0x0, 0x0, 0x0, 0x0,
  0x0, 0x0, 0x0, 0x0, 0x0, 0x0, 0x0, 0x0, 0x0, 0x0, 0x0, 0x0, 0x0, 0x0, 0x0, 0x0,
  0x0, 0x0, 0x0, 0x0, 0x0, 0x0, 0x0, 0x0, 0x0, 0x0, 0x0, 0x0, 0x0, 0x0, 0x0, 0x0,
  0x0, 0x0, 0x0, 0x0, 0x0, 0x0, 0x0, 0x0, 0x0, 0x0, 0x0, 0x0, 0x0, 0x0, 0x0, 0x0,
  0x0, 0x0, 0x0, 0x0, 0x0, 0x0, 0x0, 0x0, 0x0, 0x0, 0x0, 0x0, 0x0, 0x0, 0x0, 0x0,
  0x0, 0x0, 0x0, 0x0, 0x0, 0x0, 0x0, 0x3c000000, 0x0, 0x0, 0x0, 0x3c003c00, 0x0, 0x0, 0x3c000000, 0x3c003c00,
  0x0, 0x0, 0x0, 0x0, 0x0, 0x0, 0x0, 0x0, 0x0, 0x0, 0x0, 0x0, 0x0, 0x0, 0x0, 0x0,
  0x0, 0x0, 0x0, 0x0, 0x0, 0x0, 0x0, 0x3c000000, 0x0, 0x0, 0x0, 0x3c003c00, 0x0, 0x0, 0x3c000000, 0x3c003c00,
  0x0, 0x0, 0x3c003c00, 0x3c003c00, 0x0, 0x3c000000, 0x3c003c00, 0x3c003c00, 0x0, 0x3c003c00, 0x3c003c00, 0x3c003c00, 0x3c000000, 0x3c003c00, 0x3c003c00, 0x3c003c00,
  0x3c003c00, 0x3c003c00, 0x3c003c00, 0x3c00, 0x3c003c00, 0x3c003c00, 0x3c003c00, 0x0, 0x3c003c00, 0x3c003c00, 0x3c00, 0x0, 0x3c003c00, 0x3c003c00, 0x0, 0x0,
  0x0, 0x0, 0x3c003c00, 0x3c003c00, 0x0, 0x3c000000, 0x3c003c00, 0x3c003c00, 0x0, 0x3c003c00, 0x3c003c00, 0x3c003c00, 0x3c000000, 0x3c003c00, 0x3c003c00, 0x3c003c00,
  0x3c003c00, 0x3c003c00, 0x3c003c00, 0x3c00, 0x3c003c00, 0x3c003c00, 0x3c003c00, 0x0, 0x3c003c00, 0x3c003c00, 0x3c00, 0x0, 0x3c003c00, 0x3c003c00, 0x0, 0x0,
  0x3c003c00, 0x3c00, 0x0, 0x0, 0x3c003c00, 0x0, 0x0, 0x0, 0x3c00, 0x0, 0x0, 0x0, 0x0, 0x0, 0x0, 0x0,
  0x0, 0x0, 0x0, 0x0, 0x0, 0x0, 0x0, 0x0, 0x0, 0x0, 0x0, 0x0, 0x0, 0x0, 0x0, 0x0,
  0x3c003c00, 0x3c00, 0x0, 0x0, 0x3c003c00, 0x0, 0x0, 0x0, 0x3c00, 0x0, 0x0, 0x0, 0x0, 0x0, 0x0, 0x0,
  0x0, 0x0, 0x0, 0x0, 0x0, 0x0, 0x0, 0x0, 0x0, 0x0, 0x0, 0x0, 0x0, 0x0, 0x0, 0x0,
  0x0, 0x0, 0x0, 0x0, 0x0, 0x0, 0x0, 0x0, 0x0, 0x0, 0x0, 0x0, 0x0, 0x0, 0x0, 0x0,
  0x0, 0x0, 0x0, 0x0, 0x0, 0x0, 0x0, 0x0, 0x0, 0x0, 0x0, 0x0, 0x0, 0x0, 0x0, 0x0,
  0x0, 0x0, 0x0, 0x0, 0x0, 0x0, 0x0, 0x0, 0x0, 0x0, 0x0, 0x0, 0x0, 0x0, 0x0, 0x0,
  0x0, 0x0, 0x0, 0x0, 0x0, 0x0, 0x0, 0x0, 0x0, 0x0, 0x0, 0x0, 0x0, 0x0, 0x0, 0x0,
  0x0, 0x0, 0x0, 0x0, 0x0, 0x0, 0x0, 0x0, 0x0, 0x0, 0x0, 0x0, 0x0, 0x0, 0x0, 0x0,
  0x0, 0x0, 0x0, 0x3c000000, 0x0, 0x0, 0x0, 0x3c003c00, 0x0, 0x0, 0x3c000000, 0x3c003c00, 0x0, 0x0, 0x3c003c00, 0x3c003c00,
  0x0, 0x0, 0x0, 0x0, 0x0, 0x0, 0x0, 0x0, 0x0, 0x0, 0x0, 0x0, 0x0, 0x0, 0x0, 0x0,
  0x0, 0x0, 0x0, 0x3c000000, 0x0, 0x0, 0x0, 0x3c003c00, 0x0, 0x0, 0x3c000000, 0x3c003c00, 0x0, 0x0, 0x3c003c00, 0x3c003c00,
  0x0, 0x3c000000, 0x3c003c00, 0x3c003c00, 0x0, 0x3c003c00, 0x3c003c00, 0x3c003c00, 0x3c000000, 0x3c003c00, 0x3c003c00, 0x3c003c00, 0x3c003c00, 0x3c003c00, 0x3c003c00, 0x3c003c00,
  0x3c003c00, 0x3c003c00, 0x3c003c00, 0x3c003c00, 0x3c003c00, 0x3c003c00, 0x3c003c00, 0x3c00, 0x3c003c00, 0x3c003c00, 0x3c003c00, 0x0, 0x3c003c00, 0x3c003c00, 0x3c00, 0x0,
  0x0, 0x3c000000, 0x3c003c00, 0x3c003c00, 0x0, 0x3c003c00, 0x3c003c00, 0x3c003c00, 0x3c000000, 0x3c003c00, 0x3c003c00, 0x3c003c00, 0x3c003c00, 0x3c003c00, 0x3c003c00, 0x3c003c00,
  0x3c003c00, 0x3c003c00, 0x3c003c00, 0x3c003c00, 0x3c003c00, 0x3c003c00, 0x3c003c00, 0x3c00, 0x3c003c00, 0x3c003c00, 0x3c003c00, 0x0, 0x3c003c00, 0x3c003c00, 0x3c00, 0x0,
  0x3c003c00, 0x3c003c00, 0x0, 0x0, 0x3c003c00, 0x3c00, 0x0, 0x0, 0x3c003c00, 0x0, 0x0, 0x0, 0x3c00, 0x0, 0x0, 0x0,
  0x0, 0x0, 0x0, 0x0, 0x0, 0x0, 0x0, 0x0, 0x0, 0x0, 0x0, 0x0, 0x0, 0x0, 0x0, 0x0,
  0x3c003c00, 0x3c003c00, 0x0, 0x0, 0x3c003c00, 0x3c00, 0x0, 0x0, 0x3c003c00, 0x0, 0x0, 0x0, 0x3c00, 0x0, 0x0, 0x0,
  0x0, 0x0, 0x0, 0x0, 0x0, 0x0, 0x0, 0x0, 0x0, 0x0, 0x0, 0x0, 0x0, 0x0, 0x0, 0x0,
  0x0, 0x0, 0x0, 0x0, 0x0, 0x0, 0x0, 0x0, 0x0, 0x0, 0x0, 0x0, 0x0, 0x0, 0x0, 0x0,
  0x0, 0x0, 0x0, 0x0, 0x0, 0x0, 0x0, 0x0, 0x0, 0x0, 0x0, 0x0, 0x0, 0x0, 0x0, 0x0,
  0x0, 0x0, 0x0, 0x0, 0x0, 0x0, 0x0, 0x0, 0x0, 0x0, 0x0, 0x0, 0x0, 0x0, 0x0, 0x0,
  0x0, 0x0, 0x0, 0x0, 0x0, 0x0, 0x0, 0x0, 0x0, 0x0, 0x0, 0x0, 0x0, 0x0, 0x0, 0x0,
  0x0, 0x0, 0x0, 0x0, 0x0, 0x0, 0x0, 0x0, 0x0, 0x0, 0x0, 0x0, 0x0, 0x0, 0x0, 0xffff0000,
  0x0, 0x0, 0x0, 0xffffffff, 0x0, 0x0, 0xffff0000, 0xffff, 0x0, 0x0, 0xffffffff, 0x0, 0x0, 0xffff0000, 0xffff, 0x0,
  0x0, 0x0, 0x0, 0x0, 0x0, 0x0, 0x0, 0x0, 0x0, 0x0, 0x0, 0x0, 0x0, 0x0, 0x0, 0xffff0000,
  0x0, 0x0, 0x0, 0xffffffff, 0x0, 0x0, 0xffff0000, 0xffff, 0x0, 0x0, 0xffffffff, 0x0, 0x0, 0xffff0000, 0xffff, 0x0,
  0x0, 0xffffffff, 0x0, 0x0, 0xffff0000, 0xffff, 0x0, 0x0, 0xffffffff, 0x0, 0x0, 0x0, 0xffff, 0x0, 0x0, 0x0,
  0x0, 0x0, 0x0, 0x0, 0x0, 0x0, 0x0, 0x0, 0x0, 0x0, 0x0, 0x0, 0x0, 0x0, 0x0, 0x0,
  0x0, 0xffffffff, 0x0, 0x0, 0xffff0000, 0xffff, 0x0, 0x0, 0xffffffff, 0x0, 0x0, 0x0, 0xffff, 0x0, 0x0, 0x0,
  0x0, 0x0, 0x0, 0x0, 0x0, 0x0, 0x0, 0x0, 0x0, 0x0, 0x0, 0x0, 0x0, 0x0, 0x0, 0x0,
  0x0, 0x0, 0x0, 0x0, 0x0, 0x0, 0x0, 0x0, 0x0, 0x0, 0x0, 0x0, 0x0, 0x0, 0x0, 0x0,
  0x0, 0x0, 0x0, 0x0, 0x0, 0x0, 0x0, 0x0, 0x0, 0x0, 0x0, 0x0, 0x0, 0x0, 0x0, 0x0,
  0x0, 0x0, 0x0, 0x0, 0x0, 0x0, 0x0, 0x0, 0x0, 0x0, 0x0, 0x0, 0x0, 0x0, 0x0, 0x0,
  0x0, 0x0, 0x0, 0x0, 0x0, 0x0, 0x0, 0x0, 0x0, 0x0, 0x0, 0x0, 0x0, 0x0, 0x0, 0x0,
  0x0, 0x0, 0x0, 0x0, 0x0, 0x0, 0x0, 0x0, 0x0, 0x0, 0x0, 0x0, 0x0, 0x0, 0x0, 0x0,
  0x0, 0x0, 0x0, 0x0, 0x0, 0x0, 0x0, 0x0, 0x0, 0x0, 0x0, 0x0, 0x0, 0x0, 0x0, 0x0,
  0x16520, 0x3020100, 0x7060504, 0x3020100, 0x16620, 0x3020100, 0x7060504, 0x3020100, 0x16530, 0x3020100, 0x7060504, 0x3020100, 0x16630, 0x3020100, 0x7060504, 0x3020100,
  0x16540, 0x3020100, 0x7060504, 0x3020100, 0x16640, 0x3020100, 0x7060504, 0x3020100, 0x16550, 0x3020100, 0x7060504, 0x3020100, 0x16650, 0x3020100, 0x7060504, 0x3020100,
  0x16560, 0x3020100, 0x7060504, 0x3020100, 0x16660, 0x3020100, 0x7060504, 0x3020100, 0x16570, 0x3020100, 0x7060504, 0x3020100, 0x16670, 0x3020100, 0x7060504, 0x3020100,
  0x16600, 0x3020100, 0x7060504, 0x3020100, 0x16700, 0x3020100, 0x7060504, 0x3020100, 0x16610, 0x3020100, 0x7060504, 0x3020100, 0x16710, 0x3020100, 0x7060504, 0x3020100,
  0x16528, 0x3020100, 0x7060504, 0x3020100, 0x16628, 0x3020100, 0x7060504, 0x3020100, 0x16538, 0x3020100, 0x7060504, 0x3020100, 0x16638, 0x3020100, 0x7060504, 0x3020100,
  0x16548, 0x3020100, 0x7060504, 0x3020100, 0x16648, 0x3020100, 0x7060504, 0x3020100, 0x16558, 0x3020100, 0x7060504, 0x3020100, 0x16658, 0x3020100, 0x7060504, 0x3020100,
  0x16568, 0x3020100, 0x7060504, 0x3020100, 0x16668, 0x3020100, 0x7060504, 0x3020100, 0x16578, 0x3020100, 0x7060504, 0x3020100, 0x16678, 0x3020100, 0x7060504, 0x3020100,
  0x16608, 0x3020100, 0x7060504, 0x3020100, 0x16708, 0x3020100, 0x7060504, 0x3020100, 0x16618, 0x3020100, 0x7060504, 0x3020100, 0x16718, 0x3020100, 0x7060504, 0x3020100,
  0x16720, 0x1000100, 0x5040302, 0x5040100, 0x16820, 0x1000100, 0x5040302, 0x5040100, 0x16730, 0x1000100, 0x5040302, 0x5040100, 0x16830, 0x1000100, 0x5040302, 0x5040100,
  0x16740, 0x1000100, 0x5040302, 0x5040100, 0x16840, 0x1000100, 0x5040302, 0x5040100, 0x16750, 0x1000100, 0x5040302, 0x5040100, 0x16850, 0x1000100, 0x5040302, 0x5040100,
  0x16760, 0x1000100, 0x5040302, 0x5040100, 0x16860, 0x1000100, 0x5040302, 0x5040100, 0x16770, 0x1000100, 0x5040302, 0x5040100, 0x16870, 0x1000100, 0x5040302, 0x5040100,
  0x16800, 0x1000100, 0x5040302, 0x5040100, 0x16900, 0x1000100, 0x5040302, 0x5040100, 0x16810, 0x1000100, 0x5040302, 0x5040100, 0x16910, 0x1000100, 0x5040302, 0x5040100,
  0x16920, 0x5040504, 0xc0c0504, 0x3020100, 0x16cc8, 0x3020100, 0x7060504, 0x3020100, 0x16cd0, 0x3020100, 0x7060504, 0x3020100, 0x16cd8, 0x3020100, 0x7060504, 0x3020100,
  0x16ce0, 0x3020100, 0x7060504, 0x3020100, 0x16ce8, 0x3020100, 0x7060504, 0x3020100, 0x16cf0, 0x3020100, 0x7060504, 0x3020100, 0x16cf8, 0x3020100, 0x7060504, 0x3020100,
  0x16d00, 0x3020100, 0x7060504, 0x3020100, 0x16d08, 0x3020100, 0x7060504, 0x3020100, 0x16d10, 0x3020100, 0x7060504, 0x3020100, 0x16d18, 0x3020100, 0x7060504, 0x3020100,
  0x16d20, 0x3020100, 0x7060504, 0x3020100, 0x16d28, 0x3020100, 0x7060504, 0x3020100, 0x16d30, 0x3020100, 0x7060504, 0x3020100, 0x16d38, 0x3020100, 0x7060504, 0x3020100,
  0x16d5e, 0x16d60, 0x16d62, 0x16d64, 0x16e5e, 0x16e60, 0x16e62, 0x16e64, 0x16d6e, 0x16d70, 0x16d72, 0x16d74, 0x16e6e, 0x16e70, 0x16e72, 0x16e74,
  0x16d7e, 0x16d80, 0x16d82, 0x16d84, 0x16e7e, 0x16e80, 0x16e82, 0x16e84, 0x16d8e, 0x16d90, 0x16d92, 0x16d94, 0x16e8e, 0x16e90, 0x16e92, 0x16e94,
  0x16d9e, 0x16da0, 0x16da2, 0x16da4, 0x16e9e, 0x16ea0, 0x16ea2, 0x16ea4, 0x16dae, 0x16db0, 0x16db2, 0x16db4, 0x16eae, 0x16eb0, 0x16eb2, 0x16eb4,
  0x16e3e, 0x16e40, 0x16e42, 0x16e44, 0x16f3e, 0x16f40, 0x16f42, 0x16f44, 0x16e4e, 0x16e50, 0x16e52, 0x16e54, 0x16f4e, 0x16f50, 0x16f52, 0x16f54,
  0x16d66, 0x16d68, 0x16d6a, 0x16d6c, 0x16e66, 0x16e68, 0x16e6a, 0x16e6c, 0x16d76, 0x16d78, 0x16d7a, 0x16d7c, 0x16e76, 0x16e78, 0x16e7a, 0x16e7c,
  0x16d86, 0x16d88, 0x16d8a, 0x16d8c, 0x16e86, 0x16e88, 0x16e8a, 0x16e8c, 0x16d96, 0x16d98, 0x16d9a, 0x16d9c, 0x16e96, 0x16e98, 0x16e9a, 0x16e9c,
  0x16da6, 0x16da8, 0x16daa, 0x16dac, 0x16ea6, 0x16ea8, 0x16eaa, 0x16eac, 0x16db6, 0x16db8, 0x16dba, 0x16dbc, 0x16eb6, 0x16eb8, 0x16eba, 0x16ebc,
  0x16e46, 0x16e48, 0x16e4a, 0x16e4c, 0x16f46, 0x16f48, 0x16f4a, 0x16f4c, 0x16e56, 0x16e58, 0x16e5a, 0x16e5c, 0x16f56, 0x16f58, 0x16f5a, 0x16f5c,
  0x16cc0, 0x16728, 0x1672a, 0x1672c, 0x1681e, 0x16828, 0x1682a, 0x1682c, 0x1672e, 0x16738, 0x1673a, 0x1673c, 0x1682e, 0x16838, 0x1683a, 0x1683c,
  0x1673e, 0x16748, 0x1674a, 0x1674c, 0x1683e, 0x16848, 0x1684a, 0x1684c, 0x1674e, 0x16758, 0x1675a, 0x1675c, 0x1684e, 0x16858, 0x1685a, 0x1685c,
  0x1675e, 0x16768, 0x1676a, 0x1676c, 0x1685e, 0x16868, 0x1686a, 0x1686c, 0x1676e, 0x16778, 0x1677a, 0x1677c, 0x1686e, 0x16878, 0x1687a, 0x1687c,
  0x167fe, 0x16808, 0x1680a, 0x1680c, 0x168fe, 0x16908, 0x1690a, 0x1690c, 0x1680e, 0x16818, 0x1681a, 0x1681c, 0x1690e, 0x16918, 0x1691a, 0x1691c,
  0x169c0, 0x16a00, 0x1691e, 0x16afe, 0x169e0, 0x16a20, 0x16ade, 0x16b1e, 0x169c2, 0x16a02, 0x16ac0, 0x16b00, 0x169e2, 0x16a22, 0x16ae0, 0x16b20,
  0x169c4, 0x16a04, 0x16ac2, 0x16b02, 0x169e4, 0x16a24, 0x16ae2, 0x16b22, 0x169c6, 0x16a06, 0x16ac4, 0x16b04, 0x169e6, 0x16a26, 0x16ae4, 0x16b24,
  0x169c8, 0x16a08, 0x16ac6, 0x16b06, 0x169e8, 0x16a28, 0x16ae6, 0x16b26, 0x169ca, 0x16a0a, 0x16ac8, 0x16b08, 0x169ea, 0x16a2a, 0x16ae8, 0x16b28,
  0x169dc, 0x16a1c, 0x16ada, 0x16b1a, 0x169fc, 0x16a3c, 0x16afa, 0x16b3a, 0x169de, 0x16a1e, 0x16adc, 0x16b1c, 0x169fe, 0x16a3e, 0x16afc, 0x16b3c,
  0x16b3e, 0x16bfe, 0x16c3e, 0x16c7e, 0x16bde, 0x16c1e, 0x16c5e, 0x16c9e, 0x16bc0, 0x16c00, 0x16c40, 0x16c80, 0x16be0, 0x16c20, 0x16c60, 0x16ca0,
  0x16bc2, 0x16c02, 0x16c42, 0x16c82, 0x16be2, 0x16c22, 0x16c62, 0x16ca2, 0x16bc4, 0x16c04, 0x16c44, 0x16c84, 0x16be4, 0x16c24, 0x16c64, 0x16ca4,
  0x16bc6, 0x16c06, 0x16c46, 0x16c86, 0x16be6, 0x16c26, 0x16c66, 0x16ca6, 0x16bc8, 0x16c08, 0x16c48, 0x16c88, 0x16be8, 0x16c28, 0x16c68, 0x16ca8,
  0x16bda, 0x16c1a, 0x16c5a, 0x16c9a, 0x16bfa, 0x16c3a, 0x16c7a, 0x16cba, 0x16bdc, 0x16c1c, 0x16c5c, 0x16c9c, 0x16bfc, 0x16c3c, 0x16c7c, 0x16cbc,
  0x16cbe, 0x16cc0, 0x16cc0, 0x16cc0, 0x16cc8, 0x16cc8, 0x16cc8, 0x16cc8, 0x16cd0, 0x16cd0, 0x16cd0, 0x16cd0, 0x16cd8, 0x16cd8, 0x16cd8, 0x16cd8,
  0x16ce0, 0x16ce0, 0x16ce0, 0x16ce0, 0x16ce8, 0x16ce8, 0x16ce8, 0x16ce8, 0x16cf0, 0x16cf0, 0x16cf0, 0x16cf0, 0x16cf8, 0x16cf8, 0x16cf8, 0x16cf8,
  0x16d00, 0x16d00, 0x16d00, 0x16d00, 0x16d08, 0x16d08, 0x16d08, 0x16d08, 0x16d10, 0x16d10, 0x16d10, 0x16d10, 0x16d18, 0x16d18, 0x16d18, 0x16d18,
  0x16d20, 0x16d20, 0x16d20, 0x16d20, 0x16d28, 0x16d28, 0x16d28, 0x16d28, 0x16d30, 0x16d30, 0x16d30, 0x16d30, 0x16d38, 0x16d38, 0x16d38, 0x16d38,
  0x16cc0, 0x16cc0, 0x16cc0, 0x16cc0, 0x16cc8, 0x16cc8, 0x16cc8, 0x16cc8, 0x16cd0, 0x16cd0, 0x16cd0, 0x16cd0, 0x16cd8, 0x16cd8, 0x16cd8, 0x16cd8,
  0x16ce0, 0x16ce0, 0x16ce0, 0x16ce0, 0x16ce8, 0x16ce8, 0x16ce8, 0x16ce8, 0x16cf0, 0x16cf0, 0x16cf0, 0x16cf0, 0x16cf8, 0x16cf8, 0x16cf8, 0x16cf8,
  0x16d00, 0x16d00, 0x16d00, 0x16d00, 0x16d08, 0x16d08, 0x16d08, 0x16d08, 0x16d10, 0x16d10, 0x16d10, 0x16d10, 0x16d18, 0x16d18, 0x16d18, 0x16d18,
  0x16d20, 0x16d20, 0x16d20, 0x16d20, 0x16d28, 0x16d28, 0x16d28, 0x16d28, 0x16d30, 0x16d30, 0x16d30, 0x16d30, 0x16d38, 0x16d38, 0x16d38, 0x16d38,
  0x16cc0, 0x16cc0, 0x16cc0, 0x16cc0, 0x16cc8, 0x16cc8, 0x16cc8, 0x16cc8, 0x16cd0, 0x16cd0, 0x16cd0, 0x16cd0, 0x16cd8, 0x16cd8, 0x16cd8, 0x16cd8,
  0x16ce0, 0x16ce0, 0x16ce0, 0x16ce0, 0x16ce8, 0x16ce8, 0x16ce8, 0x16ce8, 0x16cf0, 0x16cf0, 0x16cf0, 0x16cf0, 0x16cf8, 0x16cf8, 0x16cf8, 0x16cf8,
  0x16d00, 0x16d00, 0x16d00, 0x16d00, 0x16d08, 0x16d08, 0x16d08, 0x16d08, 0x16d10, 0x16d10, 0x16d10, 0x16d10, 0x16d18, 0x16d18, 0x16d18, 0x16d18,
  0x16d20, 0x16d20, 0x16d20, 0x16d20, 0x16d28, 0x16d28, 0x16d28, 0x16d28, 0x16d30, 0x16d30, 0x16d30, 0x16d30, 0x16d38, 0x16d38, 0x16d38, 0x16d38,
  0x16cc0, 0x16cc0, 0x3020100, 0x16500, 0x16cc8, 0x16cc8, 0x3020100, 0x16500, 0x16cd0, 0x16cd0, 0x3020100, 0x16500, 0x16cd8, 0x16cd8, 0x3020100, 0x16500,
  0x16ce0, 0x16ce0, 0x3020100, 0x16500, 0x16ce8, 0x16ce8, 0x3020100, 0x16500, 0x16cf0, 0x16cf0, 0x3020100, 0x16500, 0x16cf8, 0x16cf8, 0x3020100, 0x16500,
  0x16d00, 0x16d00, 0x3020100, 0x16500, 0x16d08, 0x16d08, 0x3020100, 0x16500, 0x16d10, 0x16d10, 0x3020100, 0x16500, 0x16d18, 0x16d18, 0x3020100, 0x16500,
  0x16d20, 0x16d20, 0x3020100, 0x16500, 0x16d28, 0x16d28, 0x3020100, 0x16500, 0x16d30, 0x16d30, 0x3020100, 0x16500, 0x16d38, 0x16d38, 0x3020100, 0x16500,
  0x16cc0, 0x16cc0, 0x3020100, 0x16500, 0x16cc8, 0x16cc8, 0x3020100, 0x16500, 0x16cd0, 0x16cd0, 0x3020100, 0x16500, 0x16cd8, 0x16cd8, 0x3020100, 0x16500,
  0x16ce0, 0x16ce0, 0x3020100, 0x16500, 0x16ce8, 0x16ce8, 0x3020100, 0x16500, 0x16cf0, 0x16cf0, 0x3020100, 0x16500, 0x16cf8, 0x16cf8, 0x3020100, 0x16500,
  0x16d00, 0x16d00, 0x3020100, 0x16500, 0x16d08, 0x16d08, 0x3020100, 0x16500, 0x16d10, 0x16d10, 0x3020100, 0x16500, 0x16d18, 0x16d18, 0x3020100, 0x16500,
  0x16d20, 0x16d20, 0x3020100, 0x16500, 0x16d28, 0x16d28, 0x3020100, 0x16500, 0x16d30, 0x16d30, 0x3020100, 0x16500, 0x16d38, 0x16d38, 0x3020100, 0x16500,
  0x16f5c, 0x16f64, 0x3020100, 0x16500, 0x1705c, 0x17064, 0x3020100, 0x16500, 0x16f6c, 0x16f74, 0x3020100, 0x16500, 0x1706c, 0x17074, 0x3020100, 0x16500,
  0x16f7c, 0x16f84, 0x3020100, 0x16500, 0x1707c, 0x17084, 0x3020100, 0x16500, 0x16f8c, 0x16f94, 0x3020100, 0x16500, 0x1708c, 0x17094, 0x3020100, 0x16500,
  0x16f9c, 0x16fa4, 0x3020100, 0x16500, 0x1709c, 0x170a4, 0x3020100, 0x16500, 0x16fac, 0x16fb4, 0x3020100, 0x16500, 0x170ac, 0x170b4, 0x3020100, 0x16500,
  0x1703c, 0x17044, 0x3020100, 0x16500, 0x1713c, 0x17144, 0x3020100, 0x16500, 0x1704c, 0x17054, 0x3020100, 0x16500, 0x1714c, 0x17154, 0x3020100, 0x16500,
  0x1715c, 0x16cc0, 0x5040504, 0x16500, 0x16cc8, 0x16cc8, 0x3020100, 0x16500, 0x16cd0, 0x16cd0, 0x3020100, 0x16500, 0x16cd8, 0x16cd8, 0x3020100, 0x16500,
  0x16ce0, 0x16ce0, 0x3020100, 0x16500, 0x16ce8, 0x16ce8, 0x3020100, 0x16500, 0x16cf0, 0x16cf0, 0x3020100, 0x16500, 0x16cf8, 0x16cf8, 0x3020100, 0x16500,
  0x16d00, 0x16d00, 0x3020100, 0x16500, 0x16d08, 0x16d08, 0x3020100, 0x16500, 0x16d10, 0x16d10, 0x3020100, 0x16500, 0x16d18, 0x16d18, 0x3020100, 0x16500,
  0x16d20, 0x16d20, 0x3020100, 0x16500, 0x16d28, 0x16d28, 0x3020100, 0x16500, 0x16d30, 0x16d30, 0x3020100, 0x16500, 0x16d38, 0x16d38, 0x3020100, 0x16500,
  0x16580, 0x3020100, 0x7060504, 0x3020100, 0x16680, 0x3020100, 0x7060504, 0x3020100, 0x16590, 0x3020100, 0x7060504, 0x3020100, 0x16690, 0x3020100, 0x7060504, 0x3020100,
  0x165a0, 0x3020100, 0x7060504, 0x3020100, 0x166a0, 0x3020100, 0x7060504, 0x3020100, 0x165b0, 0x3020100, 0x7060504, 0x3020100, 0x166b0, 0x3020100, 0x7060504, 0x3020100,
  0x165c0, 0x3020100, 0x7060504, 0x3020100, 0x166c0, 0x3020100, 0x7060504, 0x3020100, 0x165d0, 0x3020100, 0x7060504, 0x3020100, 0x166d0, 0x3020100, 0x7060504, 0x3020100,
  0x165e0, 0x3020100, 0x7060504, 0x3020100, 0x166e0, 0x3020100, 0x7060504, 0x3020100, 0x165f0, 0x3020100, 0x7060504, 0x3020100, 0x166f0, 0x3020100, 0x7060504, 0x3020100,
  0x16588, 0x3020100, 0x7060504, 0x3020100, 0x16688, 0x3020100, 0x7060504, 0x3020100, 0x16598, 0x3020100, 0x7060504, 0x3020100, 0x16698, 0x3020100, 0x7060504, 0x3020100,
  0x165a8, 0x3020100, 0x7060504, 0x3020100, 0x166a8, 0x3020100, 0x7060504, 0x3020100, 0x165b8, 0x3020100, 0x7060504, 0x3020100, 0x166b8, 0x3020100, 0x7060504, 0x3020100,
  0x165c8, 0x3020100, 0x7060504, 0x3020100, 0x166c8, 0x3020100, 0x7060504, 0x3020100, 0x165d8, 0x3020100, 0x7060504, 0x3020100, 0x166d8, 0x3020100, 0x7060504, 0x3020100,
  0x165e8, 0x3020100, 0x7060504, 0x3020100, 0x166e8, 0x3020100, 0x7060504, 0x3020100, 0x165f8, 0x3020100, 0x7060504, 0x3020100, 0x166f8, 0x3020100, 0x7060504, 0x3020100,
  0x16780, 0x1000100, 0x5040302, 0x5040100, 0x16880, 0x1000100, 0x5040302, 0x5040100, 0x16790, 0x1000100, 0x5040302, 0x5040100, 0x16890, 0x1000100, 0x5040302, 0x5040100,
  0x167a0, 0x1000100, 0x5040302, 0x5040100, 0x168a0, 0x1000100, 0x5040302, 0x5040100, 0x167b0, 0x1000100, 0x5040302, 0x5040100, 0x168b0, 0x1000100, 0x5040302, 0x5040100,
  0x167c0, 0x1000100, 0x5040302, 0x5040100, 0x168c0, 0x1000100, 0x5040302, 0x5040100, 0x167d0, 0x1000100, 0x5040302, 0x5040100, 0x168d0, 0x1000100, 0x5040302, 0x5040100,
  0x167e0, 0x1000100, 0x5040302, 0x5040100, 0x168e0, 0x1000100, 0x5040302, 0x5040100, 0x167f0, 0x1000100, 0x5040302, 0x5040100, 0x168f0, 0x1000100, 0x5040302, 0x5040100,
  0x16cc0, 0x3020100, 0x7060504, 0x3020100, 0x16cc8, 0x3020100, 0x7060504, 0x3020100, 0x16cd0, 0x3020100, 0x7060504, 0x3020100, 0x16cd8, 0x3020100, 0x7060504, 0x3020100,
  0x16ce0, 0x3020100, 0x7060504, 0x3020100, 0x16ce8, 0x3020100, 0x7060504, 0x3020100, 0x16cf0, 0x3020100, 0x7060504, 0x3020100, 0x16cf8, 0x3020100, 0x7060504, 0x3020100,
  0x16d00, 0x3020100, 0x7060504, 0x3020100, 0x16d08, 0x3020100, 0x7060504, 0x3020100, 0x16d10, 0x3020100, 0x7060504, 0x3020100, 0x16d18, 0x3020100, 0x7060504, 0x3020100,
  0x16d20, 0x3020100, 0x7060504, 0x3020100, 0x16d28, 0x3020100, 0x7060504, 0x3020100, 0x16d30, 0x3020100, 0x7060504, 0x3020100, 0x16d38, 0x3020100, 0x7060504, 0x3020100,
  0x16dbe, 0x16dc0, 0x16dc2, 0x16dc4, 0x16ebe, 0x16ec0, 0x16ec2, 0x16ec4, 0x16dce, 0x16dd0, 0x16dd2, 0x16dd4, 0x16ece, 0x16ed0, 0x16ed2, 0x16ed4,
  0x16dde, 0x16de0, 0x16de2, 0x16de4, 0x16ede, 0x16ee0, 0x16ee2, 0x16ee4, 0x16dee, 0x16df0, 0x16df2, 0x16df4, 0x16eee, 0x16ef0, 0x16ef2, 0x16ef4,
  0x16dfe, 0x16e00, 0x16e02, 0x16e04, 0x16efe, 0x16f00, 0x16f02, 0x16f04, 0x16e0e, 0x16e10, 0x16e12, 0x16e14, 0x16f0e, 0x16f10, 0x16f12, 0x16f14,
  0x16e1e, 0x16e20, 0x16e22, 0x16e24, 0x16f1e, 0x16f20, 0x16f22, 0x16f24, 0x16e2e, 0x16e30, 0x16e32, 0x16e34, 0x16f2e, 0x16f30, 0x16f32, 0x16f34,
  0x16dc6, 0x16dc8, 0x16dca, 0x16dcc, 0x16ec6, 0x16ec8, 0x16eca, 0x16ecc, 0x16dd6, 0x16dd8, 0x16dda, 0x16ddc, 0x16ed6, 0x16ed8, 0x16eda, 0x16edc,
  0x16de6, 0x16de8, 0x16dea, 0x16dec, 0x16ee6, 0x16ee8, 0x16eea, 0x16eec, 0x16df6, 0x16df8, 0x16dfa, 0x16dfc, 0x16ef6, 0x16ef8, 0x16efa, 0x16efc,
  0x16e06, 0x16e08, 0x16e0a, 0x16e0c, 0x16f06, 0x16f08, 0x16f0a, 0x16f0c, 0x16e16, 0x16e18, 0x16e1a, 0x16e1c, 0x16f16, 0x16f18, 0x16f1a, 0x16f1c,
  0x16e26, 0x16e28, 0x16e2a, 0x16e2c, 0x16f26, 0x16f28, 0x16f2a, 0x16f2c, 0x16e36, 0x16e38, 0x16e3a, 0x16e3c, 0x16f36, 0x16f38, 0x16f3a, 0x16f3c,
  0x1677e, 0x16788, 0x1678a, 0x1678c, 0x1687e, 0x16888, 0x1688a, 0x1688c, 0x1678e, 0x16798, 0x1679a, 0x1679c, 0x1688e, 0x16898, 0x1689a, 0x1689c,
  0x1679e, 0x167a8, 0x167aa, 0x167ac, 0x1689e, 0x168a8, 0x168aa, 0x168ac, 0x167ae, 0x167b8, 0x167ba, 0x167bc, 0x168ae, 0x168b8, 0x168ba, 0x168bc,
  0x167be, 0x167c8, 0x167ca, 0x167cc, 0x168be, 0x168c8, 0x168ca, 0x168cc, 0x167ce, 0x167d8, 0x167da, 0x167dc, 0x168ce, 0x168d8, 0x168da, 0x168dc,
  0x167de, 0x167e8, 0x167ea, 0x167ec, 0x168de, 0x168e8, 0x168ea, 0x168ec, 0x167ee, 0x167f8, 0x167fa, 0x167fc, 0x168ee, 0x168f8, 0x168fa, 0x168fc,
  0x169cc, 0x16a0c, 0x16aca, 0x16b0a, 0x169ec, 0x16a2c, 0x16aea, 0x16b2a, 0x169ce, 0x16a0e, 0x16acc, 0x16b0c, 0x169ee, 0x16a2e, 0x16aec, 0x16b2c,
  0x169d0, 0x16a10, 0x16ace, 0x16b0e, 0x169f0, 0x16a30, 0x16aee, 0x16b2e, 0x169d2, 0x16a12, 0x16ad0, 0x16b10, 0x169f2, 0x16a32, 0x16af0, 0x16b30,
  0x169d4, 0x16a14, 0x16ad2, 0x16b12, 0x169f4, 0x16a34, 0x16af2, 0x16b32, 0x169d6, 0x16a16, 0x16ad4, 0x16b14, 0x169f6, 0x16a36, 0x16af4, 0x16b34,
  0x169d8, 0x16a18, 0x16ad6, 0x16b16, 0x169f8, 0x16a38, 0x16af6, 0x16b36, 0x169da, 0x16a1a, 0x16ad8, 0x16b18, 0x169fa, 0x16a3a, 0x16af8, 0x16b38,
  0x16bca, 0x16c0a, 0x16c4a, 0x16c8a, 0x16bea, 0x16c2a, 0x16c6a, 0x16caa, 0x16bcc, 0x16c0c, 0x16c4c, 0x16c8c, 0x16bec, 0x16c2c, 0x16c6c, 0x16cac,
  0x16bce, 0x16c0e, 0x16c4e, 0x16c8e, 0x16bee, 0x16c2e, 0x16c6e, 0x16cae, 0x16bd0, 0x16c10, 0x16c50, 0x16c90, 0x16bf0, 0x16c30, 0x16c70, 0x16cb0,
  0x16bd2, 0x16c12, 0x16c52, 0x16c92, 0x16bf2, 0x16c32, 0x16c72, 0x16cb2, 0x16bd4, 0x16c14, 0x16c54, 0x16c94, 0x16bf4, 0x16c34, 0x16c74, 0x16cb4,
  0x16bd6, 0x16c16, 0x16c56, 0x16c96, 0x16bf6, 0x16c36, 0x16c76, 0x16cb6, 0x16bd8, 0x16c18, 0x16c58, 0x16c98, 0x16bf8, 0x16c38, 0x16c78, 0x16cb8,
  0x16cc0, 0x16cc0, 0x16cc0, 0x16cc0, 0x16cc8, 0x16cc8, 0x16cc8, 0x16cc8, 0x16cd0, 0x16cd0, 0x16cd0, 0x16cd0, 0x16cd8, 0x16cd8, 0x16cd8, 0x16cd8,
  0x16ce0, 0x16ce0, 0x16ce0, 0x16ce0, 0x16ce8, 0x16ce8, 0x16ce8, 0x16ce8, 0x16cf0, 0x16cf0, 0x16cf0, 0x16cf0, 0x16cf8, 0x16cf8, 0x16cf8, 0x16cf8,
  0x16d00, 0x16d00, 0x16d00, 0x16d00, 0x16d08, 0x16d08, 0x16d08, 0x16d08, 0x16d10, 0x16d10, 0x16d10, 0x16d10, 0x16d18, 0x16d18, 0x16d18, 0x16d18,
  0x16d20, 0x16d20, 0x16d20, 0x16d20, 0x16d28, 0x16d28, 0x16d28, 0x16d28, 0x16d30, 0x16d30, 0x16d30, 0x16d30, 0x16d38, 0x16d38, 0x16d38, 0x16d38,
  0x16cc0, 0x16cc0, 0x16cc0, 0x16cc0, 0x16cc8, 0x16cc8, 0x16cc8, 0x16cc8, 0x16cd0, 0x16cd0, 0x16cd0, 0x16cd0, 0x16cd8, 0x16cd8, 0x16cd8, 0x16cd8,
  0x16ce0, 0x16ce0, 0x16ce0, 0x16ce0, 0x16ce8, 0x16ce8, 0x16ce8, 0x16ce8, 0x16cf0, 0x16cf0, 0x16cf0, 0x16cf0, 0x16cf8, 0x16cf8, 0x16cf8, 0x16cf8,
  0x16d00, 0x16d00, 0x16d00, 0x16d00, 0x16d08, 0x16d08, 0x16d08, 0x16d08, 0x16d10, 0x16d10, 0x16d10, 0x16d10, 0x16d18, 0x16d18, 0x16d18, 0x16d18,
  0x16d20, 0x16d20, 0x16d20, 0x16d20, 0x16d28, 0x16d28, 0x16d28, 0x16d28, 0x16d30, 0x16d30, 0x16d30, 0x16d30, 0x16d38, 0x16d38, 0x16d38, 0x16d38,
  0x16cc0, 0x16cc0, 0x16cc0, 0x16cc0, 0x16cc8, 0x16cc8, 0x16cc8, 0x16cc8, 0x16cd0, 0x16cd0, 0x16cd0, 0x16cd0, 0x16cd8, 0x16cd8, 0x16cd8, 0x16cd8,
  0x16ce0, 0x16ce0, 0x16ce0, 0x16ce0, 0x16ce8, 0x16ce8, 0x16ce8, 0x16ce8, 0x16cf0, 0x16cf0, 0x16cf0, 0x16cf0, 0x16cf8, 0x16cf8, 0x16cf8, 0x16cf8,
  0x16d00, 0x16d00, 0x16d00, 0x16d00, 0x16d08, 0x16d08, 0x16d08, 0x16d08, 0x16d10, 0x16d10, 0x16d10, 0x16d10, 0x16d18, 0x16d18, 0x16d18, 0x16d18,
  0x16d20, 0x16d20, 0x16d20, 0x16d20, 0x16d28, 0x16d28, 0x16d28, 0x16d28, 0x16d30, 0x16d30, 0x16d30, 0x16d30, 0x16d38, 0x16d38, 0x16d38, 0x16d38,
  0x16cc0, 0x16cc0, 0x3020100, 0x16500, 0x16cc8, 0x16cc8, 0x3020100, 0x16500, 0x16cd0, 0x16cd0, 0x3020100, 0x16500, 0x16cd8, 0x16cd8, 0x3020100, 0x16500,
  0x16ce0, 0x16ce0, 0x3020100, 0x16500, 0x16ce8, 0x16ce8, 0x3020100, 0x16500, 0x16cf0, 0x16cf0, 0x3020100, 0x16500, 0x16cf8, 0x16cf8, 0x3020100, 0x16500,
  0x16d00, 0x16d00, 0x3020100, 0x16500, 0x16d08, 0x16d08, 0x3020100, 0x16500, 0x16d10, 0x16d10, 0x3020100, 0x16500, 0x16d18, 0x16d18, 0x3020100, 0x16500,
  0x16d20, 0x16d20, 0x3020100, 0x16500, 0x16d28, 0x16d28, 0x3020100, 0x16500, 0x16d30, 0x16d30, 0x3020100, 0x16500, 0x16d38, 0x16d38, 0x3020100, 0x16500,
  0x16cc0, 0x16cc0, 0x3020100, 0x16500, 0x16cc8, 0x16cc8, 0x3020100, 0x16500, 0x16cd0, 0x16cd0, 0x3020100, 0x16500, 0x16cd8, 0x16cd8, 0x3020100, 0x16500,
  0x16ce0, 0x16ce0, 0x3020100, 0x16500, 0x16ce8, 0x16ce8, 0x3020100, 0x16500, 0x16cf0, 0x16cf0, 0x3020100, 0x16500, 0x16cf8, 0x16cf8, 0x3020100, 0x16500,
  0x16d00, 0x16d00, 0x3020100, 0x16500, 0x16d08, 0x16d08, 0x3020100, 0x16500, 0x16d10, 0x16d10, 0x3020100, 0x16500, 0x16d18, 0x16d18, 0x3020100, 0x16500,
  0x16d20, 0x16d20, 0x3020100, 0x16500, 0x16d28, 0x16d28, 0x3020100, 0x16500, 0x16d30, 0x16d30, 0x3020100, 0x16500, 0x16d38, 0x16d38, 0x3020100, 0x16500,
  0x16fbc, 0x16fc4, 0x3020100, 0x16500, 0x170bc, 0x170c4, 0x3020100, 0x16500, 0x16fcc, 0x16fd4, 0x3020100, 0x16500, 0x170cc, 0x170d4, 0x3020100, 0x16500,
  0x16fdc, 0x16fe4, 0x3020100, 0x16500, 0x170dc, 0x170e4, 0x3020100, 0x16500, 0x16fec, 0x16ff4, 0x3020100, 0x16500, 0x170ec, 0x170f4, 0x3020100, 0x16500,
  0x16ffc, 0x17004, 0x3020100, 0x16500, 0x170fc, 0x17104, 0x3020100, 0x16500, 0x1700c, 0x17014, 0x3020100, 0x16500, 0x1710c, 0x17114, 0x3020100, 0x16500,
  0x1701c, 0x17024, 0x3020100, 0x16500, 0x1711c, 0x17124, 0x3020100, 0x16500, 0x1702c, 0x17034, 0x3020100, 0x16500, 0x1712c, 0x17134, 0x3020100, 0x16500,
  0x16cc0, 0x16cc0, 0x3020100, 0x16500, 0x16cc8, 0x16cc8, 0x3020100, 0x16500, 0x16cd0, 0x16cd0, 0x3020100, 0x16500, 0x16cd8, 0x16cd8, 0x3020100, 0x16500,
  0x16ce0, 0x16ce0, 0x3020100, 0x16500, 0x16ce8, 0x16ce8, 0x3020100, 0x16500, 0x16cf0, 0x16cf0, 0x3020100, 0x16500, 0x16cf8, 0x16cf8, 0x3020100, 0x16500,
  0x16d00, 0x16d00, 0x3020100, 0x16500, 0x16d08, 0x16d08, 0x3020100, 0x16500, 0x16d10, 0x16d10, 0x3020100, 0x16500, 0x16d18, 0x16d18, 0x3020100, 0x16500,
  0x16d20, 0x16d20, 0x3020100, 0x16500, 0x16d28, 0x16d28, 0x3020100, 0x16500, 0x16d30, 0x16d30, 0x3020100, 0x16500, 0x16d38, 0x16d38, 0x3020100, 0x16500,
  0x0, 0x0, 0x0, 0x0, 0x0, 0x0, 0x0, 0x0, 0x0, 0x0, 0x0, 0x0, 0x0, 0x0, 0x0, 0x0,
  0x0, 0x0, 0x0, 0x0, 0x0, 0x0, 0x0, 0x0, 0x0, 0x0, 0x0, 0x0, 0x0, 0x0, 0x0, 0x0,
  0x0, 0x0, 0x0, 0x0, 0x0, 0x0, 0x0, 0x0, 0x0, 0x0, 0x0, 0x0, 0x0, 0x0, 0x0, 0x0,
  0x0, 0x0, 0x0, 0x0, 0x0, 0x0, 0x0, 0x0, 0x0, 0x0, 0x0, 0x0, 0x0, 0x0, 0x0, 0x0,
  0x0, 0x0, 0x0, 0x0, 0x0, 0x0, 0x0, 0x0, 0x0, 0x0, 0x0, 0x0, 0x0, 0x0, 0x0, 0x0,
  0x0, 0x0, 0x0, 0x0, 0x0, 0x0, 0x0, 0x0, 0x0, 0x0, 0x0, 0x0, 0x0, 0x0, 0x0, 0x0,
  0x0, 0x0, 0x0, 0x0, 0x0, 0x0, 0x0, 0x0, 0x0, 0x0, 0x0, 0x0, 0x0, 0x0, 0x0, 0x0,
  0x0, 0x0, 0x0, 0x0, 0x0, 0x0, 0x0, 0x0, 0x0, 0x0, 0x0, 0x0, 0x0, 0x0, 0x0, 0x0,
  0x0, 0x0, 0x0, 0x0, 0x0, 0x0, 0x0, 0x0, 0x0, 0x0, 0x0, 0x0, 0x0, 0x0, 0x0, 0x0,
  0x0, 0x0, 0x0, 0x0, 0x0, 0x0, 0x0, 0x0, 0x0, 0x0, 0x0, 0x0, 0x0, 0x0, 0x0, 0x0,
  0x0, 0x0, 0x0, 0x0, 0x0, 0x0, 0x0, 0x0, 0x0, 0x0, 0x0, 0x0, 0x0, 0x0, 0x0, 0x0,
  0x0, 0x0, 0x0, 0x0, 0x0, 0x0, 0x0, 0x0, 0x0, 0x0, 0x0, 0x0, 0x0, 0x0, 0x0, 0x0,
  0x0, 0x0, 0x0, 0x0, 0x0, 0x0, 0x0, 0x0, 0x0, 0x0, 0x0, 0x0, 0x0, 0x0, 0x0, 0x0,
  0x0, 0x0, 0x0, 0x0, 0x0, 0x0, 0x0, 0x0, 0x0, 0x0, 0x0, 0x0, 0x0, 0x0, 0x0, 0x0,
  0x0, 0x0, 0x0, 0x0, 0x0, 0x0, 0x0, 0x0, 0x0, 0x0, 0x0, 0x0, 0x0, 0x0, 0x0, 0x0,
  0x0, 0x0, 0x0, 0x0, 0x0, 0x0, 0x0, 0x0, 0x0, 0x0, 0x0, 0x0, 0x0, 0x0, 0x0, 0x0
};

__global__ void __launch_bounds__(1024) mega(Args) {
  asm volatile(
    "s_load_dwordx16 s[4:19], s[0:1], 0\n"
    "s_load_dwordx16 s[20:35], s[0:1], 64\n"
    "v_readfirstlane_b32 s37, v0\n"
    "s_getpc_b64 s[40:41]\n"
    "K0:\n"
    "s_lshr_b32 s37, s37, 6\n"
    "s_cmpk_ge_u32 s2, 192\n"
    "s_cselect_b32 s42, 16, 0\n"
    "s_add_u32 s42, s42, s37\n"
    "s_lshl2_add_u32 s40, s42, s40\n"
    "s_add_u32 s40, s40, TBL1-K0\n"
    "s_mov_b32 s39, 0x100000\n"
    "s_setpc_b64 s[40:41]\n"
    "TBL1:\n"
    "s_branch GA0\n"
    "s_branch GA0\n"
    "s_branch GA0\n"
    "s_branch GA0\n"
    "s_branch GA0\n"
    "s_branch GA0\n"
    "s_branch S0\n"
    "s_branch S0\n"
    "s_branch S0\n"
    "s_branch S0\n"
    "s_branch S0\n"
    "s_branch S0\n"
    "s_branch MD0\n"
    "s_branch AUX1\n"
    "s_branch AUX1\n"
    "s_branch AUX0\n"
    "s_branch SIL0\n"
    "s_branch SIL0\n"
    "s_branch SIL0\n"
    "s_branch SIL0\n"
    "s_branch SIL0\n"
    "s_branch MD0\n"
    "s_branch S0\n"
    "s_branch S0\n"
    "s_branch S0\n"
    "s_branch S0\n"
    "s_branch S0\n"
    "s_branch S0\n"
    "s_branch M1\n"
    "s_branch M1\n"
    "s_branch M1\n"
    "s_branch AUX0\n"
    "AUX0:\n"
    "s_setprio 3\n"
    "s_lshr_b32 s42, s2, 3\n"
    "s_mul_i32 s42, s42, 7\n"
    "s_add_u32 s42, s42, 7\n"
    "v_and_b32 v1, 7, v0\n"
    "v_add_u32 v1, s42, v1\n"
    "v_min_u32 v1, (END-K0)/128, v1\n"
    "v_lshlrev_b32 v1, 7, v1\n"
    "s_mov_b32 m0, 148480\n"
    "s_mov_b64 exec, 0x7f\n"
    "global_load_lds_dword v1, s[40:41]\n"
    "s_mov_b64 exec, -1\n"
    "v_and_b32 v1, 63, v0\n"
    "v_lshlrev_b32 v1, 3, v1\n"
    "v_mov_b32 v4, 0\n"
    "v_mov_b32 v5, 0\n"
    "v_add_u32 v1, 147840, v1\n"
    "ds_write_b64 v1, v[4:5]\n"
    "s_setprio 0\n"
    "AUXT:\n"
    "s_cmpk_ge_u32 s2, 192\n"
    "s_cbranch_scc1 M1\n"
    "s_branch AUX1\n"
    "S0:\n"
    "s_setprio 0\n"
    "v_bfe_u32 v2, v0, 4, 2\n"
    "v_and_b32 v3, 15, v0\n"
    "v_mul_u32_u24 v2, 0x900, v2\n"
    "s_mul_i32 s42, s2, 48\n"
    "s_add_u32 s42, s42, s37\n"
    "s_mul_i32 s42, s42, 192\n"
    "v_mad_u32_u24 v1, v3, 12, v2\n"
    "v_add_u32 v1, s42, v1\n"
    "s_waitcnt lgkmcnt(0)\n"
    "global_load_dwordx3 v[68:70], v1, s[4:5] offset:-1152 nt\n"
    "global_load_dwordx3 v[72:74], v1, s[4:5] nt\n"
    "s_setprio 0\n"
    "s_load_dword s36, s[20:21], 0\n"
    "s_branch S1\n"
    "GA0:\n"
    "s_and_b32 s44, s2, 7\n"
    "s_lshr_b32 s45, s2, 3\n"
    "s_mul_i32 s44, s44, 24\n"
    "s_add_u32 s38, s44, s45\n"
    "s_lshr_b32 s42, s37, 1\n"
    "s_mul_i32 s43, s38, 3\n"
    "s_add_u32 s43, s43, s42\n"
    "s_lshl_b32 s44, s43, 8\n"
    "s_lshl_b32 s45, s43, 1\n"
    "s_and_b32 s45, s45, -4\n"
    "v_and_b32 v1, 63, v0\n"
    "v_lshl_add_u32 v2, v1, 2, s44\n"
    "s_waitcnt lgkmcnt(0)\n"
    "global_load_dword v4, v2, s[6:7] nt\n"
    "global_load_dword v5, v2, s[8:9] nt\n"
    "s_memrealtime s[96:97]\n"
    "s_load_dword s46, s[10:11], s45\n"
    "global_load_dword v6, v2, s[12:13] nt\n"
    "global_load_dword v7, v2, s[14:15] nt\n"
    "s_load_dword s36, s[20:21], 0\n"
    "s_branch GA1\n"
    "SIL0:\n"
    "v_min_u32 v1, 287, v0\n"
    "v_lshlrev_b32 v1, 2, v1\n"
    "s_waitcnt lgkmcnt(0)\n"
    "global_load_dword v4, v1, s[18:19]\n"
    "s_load_dword s36, s[20:21], 0\n"
    "s_branch SIL1\n"
    "MD0:\n"
    "s_setprio 0\n"
    "s_mul_i32 s43, s2, 19\n"
    "s_lshr_b32 s43, s43, 9\n"
    "s_mul_i32 s44, s43, 27\n"
    "s_sub_u32 s44, s2, s44\n"
    "s_mul_i32 s45, s43, 442368\n"
    "s_lshl_b32 s46, s44, 7\n"
    "s_add_u32 s45, s45, s46\n"
    "v_bfe_u32 v1, v0, 3, 3\n"
    "v_and_b32 v2, 2, v1\n"
    "v_mul_u32_u24 v1, 0x1b00, v1\n"
    "v_xor_b32 v2, v2, v0\n"
    "v_and_b32 v2, 7, v2\n"
    "v_lshl_add_u32 v1, v2, 4, v1\n"
    "v_add_u32 v1, s45, v1\n"
    "v_xor_b32 v2, 64, v1\n"
    "v_add_u32 v2, 55296, v2\n"
    "s_lshl_b32 s46, s43, 7\n"
    "v_and_b32 v3, 31, v0\n"
    "v_lshl_add_u32 v5, v3, 2, s46\n"
    "s_mov_b32 m0, 138240\n"
    "s_cmpk_ge_u32 s2, 243\n"
    "s_waitcnt lgkmcnt(0)\n"
    "s_memrealtime s[96:97]\n"
    "s_cbranch_scc1 MD0e\n"
    "global_load_dword v4, v5, s[18:19]\n"
    "global_load_lds_dwordx4 v1, s[16:17] nt\n"
    "s_add_u32 m0, m0, 1024\n"
    "v_add_u32 v1, 110592, v1\n"
    "global_load_lds_dwordx4 v2, s[16:17] nt\n"
    "s_add_u32 m0, m0, 1024\n"
    "v_add_u32 v2, 110592, v2\n"
    "global_load_lds_dwordx4 v1, s[16:17] nt\n"
    "s_add_u32 m0, m0, 1024\n"
    "v_add_u32 v1, 110592, v1\n"
    "global_load_lds_dwordx4 v2, s[16:17] nt\n"
    "s_add_u32 m0, m0, 1024\n"
    "v_add_u32 v2, 110592, v2\n"
    "global_load_lds_dwordx4 v1, s[16:17] nt\n"
    "s_add_u32 m0, m0, 1024\n"
    "v_add_u32 v1, 110592, v1\n"
    "global_load_lds_dwordx4 v2, s[16:17] nt\n"
    "s_add_u32 m0, m0, 1024\n"
    "v_add_u32 v2, 110592, v2\n"
    "global_load_lds_dwordx4 v1, s[16:17] nt\n"
    "s_add_u32 m0, m0, 1024\n"
    "s_nop 0\n"
    "global_load_lds_dwordx4 v2, s[16:17] nt\n"
    "MD0e:\n"
    "v_mov_b32 v6, 148448\n"
    "s_waitcnt lgkmcnt(0)\n"
    "v_mov_b32 v7, s96\n"
    "ds_write_b32 v6, v7\n"
    "s_waitcnt lgkmcnt(0)\n"
    "s_load_dword s36, s[20:21], 0\n"
    "s_branch MD1\n"
    "ABORT:\n"
    "s_endpgm\n"
    "S1:\n"
    "s_setprio 3\n"
    "s_barrier\n"
    "v_mov_b32 v14, 147968\n"
    "v_mov_b32 v15, 1\n"
    "s_cmpk_lt_u32 s2, 192\n"
    "s_cbranch_scc1 KSP\n"
    "S2:\n"
    "s_mul_i32 s42, s2, 12416\n"
    "s_add_u32 s42, s42, 7334912\n"
    "v_lshl_add_u32 v75, v0, 4, s42\n"
    "v_and_b32 v76, -4, v0\n"
    "v_add_u32 v76, 147072, v76\n"
    "v_and_b32 v86, 3, v0\n"
    "v_and_b32 v67, 60, v0\n"
    "v_lshl_add_u32 v86, v86, 6, v67\n"
    "v_and_b32 v77, 31, v0\n"
    "v_lshlrev_b32 v77, 4, v77\n"
    "v_add_u32 v77, 147456, v77\n"
    "s_mov_b32 s43, 0x3f803f80\n"
    "s_movk_i32 s44, 6144\n"
    "s_setprio 3\n"
    "s_waitcnt vmcnt(1)\n"
    "ds_bpermute_b32 v108, v86, v68\n"
    "ds_bpermute_b32 v109, v86, v69\n"
    "ds_bpermute_b32 v110, v86, v70\n"
    "v_dot2_f32_bf16 v80, v68, s43, 0\n"
    "v_dot2_f32_bf16 v81, v68, v68, 0\n"
    "v_dot2_f32_bf16 v80, v69, s43, v80\n"
    "v_dot2_f32_bf16 v81, v69, v69, v81\n"
    "v_dot2_f32_bf16 v80, v70, s43, v80\n"
    "v_dot2_f32_bf16 v81, v70, v70, v81\n"
    "s_waitcnt vmcnt(0)\n"
    "ds_bpermute_b32 v112, v86, v72\n"
    "ds_bpermute_b32 v113, v86, v73\n"
    "ds_bpermute_b32 v114, v86, v74\n"
    "v_dot2_f32_bf16 v82, v72, s43, 0\n"
    "v_dot2_f32_bf16 v83, v72, v72, 0\n"
    "v_dot2_f32_bf16 v82, v73, s43, v82\n"
    "v_dot2_f32_bf16 v83, v73, v73, v83\n"
    "v_dot2_f32_bf16 v82, v74, s43, v82\n"
    "v_dot2_f32_bf16 v83, v74, v74, v83\n"
    "v_add_f32_dpp v84, v80, v80 row_ror:8 row_mask:0xf bank_mask:0x3\n"
    "v_add_f32_dpp v85, v81, v81 row_ror:8 row_mask:0xf bank_mask:0x3\n"
    "s_nop 0\n"
    "v_add_f32_dpp v84, v82, v82 row_ror:8 row_mask:0xf bank_mask:0xc\n"
    "v_add_f32_dpp v85, v83, v83 row_ror:8 row_mask:0xf bank_mask:0xc\n"
    "s_nop 0\n"
    "v_add_f32_dpp v80, v84, v84 row_half_mirror row_mask:0xf bank_mask:0x5\n"
    "s_nop 1\n"
    "v_add_f32_dpp v80, v85, v85 row_half_mirror row_mask:0xf bank_mask:0xa\n"
    "s_nop 1\n"
    "v_add_f32_dpp v80, v80, v80 quad_perm:[1,0,3,2] row_mask:0xf bank_mask:0xf\n"
    "s_nop 1\n"
    "v_add_f32_dpp v80, v80, v80 quad_perm:[2,3,0,1] row_mask:0xf bank_mask:0xf\n"
    "ds_write_b32 v76, v80\n"
    "s_mov_b64 exec, 1\n"
    "ds_add_u32 v14, v15 offset:0\n"
    "s_mov_b64 exec, -1\n"
    "s_waitcnt lgkmcnt(0)\n"
    "v_lshlrev_b32 v88, 16, v108\n"
    "v_lshlrev_b32 v89, 16, v112\n"
    "v_and_b32 v90, 0xffff0000, v108\n"
    "v_and_b32 v91, 0xffff0000, v112\n"
    "v_lshlrev_b32 v92, 16, v109\n"
    "v_lshlrev_b32 v93, 16, v113\n"
    "v_and_b32 v94, 0xffff0000, v109\n"
    "v_and_b32 v95, 0xffff0000, v113\n"
    "v_lshlrev_b32 v96, 16, v110\n"
    "v_lshlrev_b32 v97, 16, v114\n"
    "v_and_b32 v98, 0xffff0000, v110\n"
    "v_and_b32 v99, 0xffff0000, v114\n"
    "s_branch E_2\n"
    "R_1:\n"
    "s_sleep 1\n"
    "s_sub_u32 s39, s39, 1\n"
    "s_cbranch_scc1 ABORT\n"
    "E_2:\n"
    "ds_read_b32 v84, v14 offset:0\n"
    "ds_read_b128 v[80:83], v77\n"
    "s_waitcnt lgkmcnt(0)\n"
    "v_cmp_ne_u32 vcc, 6, v84\n"
    "s_cbranch_vccnz R_1\n"
    "s_add_u32 s36, s36, 1\n"
    "v_permlane16_swap_b32 v80, v82\n"
    "v_permlane16_swap_b32 v81, v83\n"
    "v_add_f32 v80, v80, v82\n"
    "v_add_f32 v81, v81, v83\n"
    "v_mov_b32 v103, s36\n"
    "v_add_f32_dpp v80, v80, v80 row_ror:8 row_mask:0xf bank_mask:0xf\n"
    "v_add_f32_dpp v81, v81, v81 row_ror:8 row_mask:0xf bank_mask:0xf\n"
    "v_mov_b32 v107, s36\n"
    "v_add_f32_dpp v80, v80, v80 row_ror:4 row_mask:0xf bank_mask:0xf\n"
    "v_add_f32_dpp v81, v81, v81 row_ror:4 row_mask:0xf bank_mask:0xf\n"
    "v_mul_f32 v80, 0x3ae38e39, v80\n"
    "v_mul_f32 v81, 0x3ae38e39, v81\n"
    "v_fma_f32 v81, -v80, v80, v81\n"
    "v_max_f32 v81, 0, v81\n"
    "v_cvt_pk_bf16_f32 v81, 0, v81\n"
    "v_add_f32 v81, 0x3727c5ac, v81\n"
    "v_cvt_pk_bf16_f32 v81, 0, v81\n"
    "v_sqrt_f32 v81, v81\n"
    "s_nop 0\n"
    "v_cvt_pk_bf16_f32 v80, v80, v81\n"
    "v_mov_b32 v81, v80\n"
    "s_nop 1\n"
    "v_permlane16_swap_b32 v80, v81\n"
    "v_and_b32 v84, 0xffff0000, v80\n"
    "v_and_b32 v85, 0xffff0000, v81\n"
    "v_lshlrev_b32 v82, 16, v80\n"
    "v_lshlrev_b32 v83, 16, v81\n"
    "v_rcp_f32 v84, v84\n"
    "v_rcp_f32 v85, v85\n"
    "v_pk_add_f32 v[88:89], v[88:89], v[82:83] neg_lo:[0,1] neg_hi:[0,1]\n"
    "v_pk_add_f32 v[90:91], v[90:91], v[82:83] neg_lo:[0,1] neg_hi:[0,1]\n"
    "v_pk_add_f32 v[92:93], v[92:93], v[82:83] neg_lo:[0,1] neg_hi:[0,1]\n"
    "v_cvt_pk_bf16_f32 v88, 0, v88\n"
    "v_cvt_pk_bf16_f32 v89, 0, v89\n"
    "v_cvt_pk_bf16_f32 v90, 0, v90\n"
    "v_cvt_pk_bf16_f32 v91, 0, v91\n"
    "v_cvt_pk_bf16_f32 v92, 0, v92\n"
    "v_cvt_pk_bf16_f32 v93, 0, v93\n"
    "v_pk_mul_f32 v[88:89], v[88:89], v[84:85]\n"
    "v_pk_mul_f32 v[90:91], v[90:91], v[84:85]\n"
    "v_pk_mul_f32 v[92:93], v[92:93], v[84:85]\n"
    "v_cvt_pk_bf16_f32 v100, v88, v89\n"
    "v_cvt_pk_bf16_f32 v101, v90, v91\n"
    "v_cvt_pk_bf16_f32 v102, v92, v93\n"
    "buffer_store_dwordx4 v[100:103], v75, s[20:23], 0 offen sc1\n"
    "v_pk_add_f32 v[94:95], v[94:95], v[82:83] neg_lo:[0,1] neg_hi:[0,1]\n"
    "v_pk_add_f32 v[96:97], v[96:97], v[82:83] neg_lo:[0,1] neg_hi:[0,1]\n"
    "v_pk_add_f32 v[98:99], v[98:99], v[82:83] neg_lo:[0,1] neg_hi:[0,1]\n"
    "v_cvt_pk_bf16_f32 v94, 0, v94\n"
    "v_cvt_pk_bf16_f32 v95, 0, v95\n"
    "v_cvt_pk_bf16_f32 v96, 0, v96\n"
    "v_cvt_pk_bf16_f32 v97, 0, v97\n"
    "v_cvt_pk_bf16_f32 v98, 0, v98\n"
    "v_cvt_pk_bf16_f32 v99, 0, v99\n"
    "v_pk_mul_f32 v[94:95], v[94:95], v[84:85]\n"
    "v_pk_mul_f32 v[96:97], v[96:97], v[84:85]\n"
    "v_pk_mul_f32 v[98:99], v[98:99], v[84:85]\n"
    "v_cvt_pk_bf16_f32 v104, v94, v95\n"
    "v_cvt_pk_bf16_f32 v105, v96, v97\n"
    "v_cvt_pk_bf16_f32 v106, v98, v99\n"
    "buffer_store_dwordx4 v[104:107], v75, s[20:23], s44 offen sc1\n"
    "s_setprio 0\n"
    "s_cmpk_lt_u32 s2, 192\n"
    "s_cbranch_scc1 KST\n"
    "s_setprio 0\n"
    "s_branch ES1\n"
    "KSP:\n"
    "s_sub_u32 s47, s37, 8\n"
    "s_cmp_ge_u32 s47, 3\n"
    "s_cselect_b32 s48, 1, 0\n"
    "s_add_u32 s45, s47, 3\n"
    "s_cmp_lt_i32 s47, 0\n"
    "s_cselect_b32 s47, s45, s47\n"
    "s_cmp_eq_u32 s47, 3\n"
    "s_cselect_b32 s47, 0, s47\n"
    "s_mul_i32 s45, s47, 30464\n"
    "v_and_b32 v122, 3, v0\n"
    "v_mul_u32_u24 v122, 1088, v122\n"
    "v_bfe_u32 v123, v0, 2, 4\n"
    "v_lshlrev_b32 v123, 3, v123\n"
    "v_add3_u32 v122, v122, v123, s45\n"
    "s_mul_i32 s45, s48, 4352\n"
    "v_add_u32 v123, s45, v122\n"
    "s_lshl_b32 s45, s48, 12\n"
    "s_add_u32 s45, s45, 107520\n"
    "v_and_b32 v124, 63, v0\n"
    "v_lshl_add_u32 v124, v124, 4, s45\n"
    "s_lshl_b32 s45, s47, 2\n"
    "v_add_u32 v125, s45, v14\n"
    "s_lshl_b32 s45, s45, 1\n"
    "s_add_u32 s45, s45, 148416\n"
    "v_mov_b32 v126, s45\n"
    "s_mul_i32 s49, s47, 3328\n"
    "s_mov_b32 s42, s47\n"
    "s_cmp_eq_u32 s48, 0\n"
    "s_cbranch_scc1 NEAR\n"
    "s_cmpk_eq_u32 s37, 6\n"
    "s_cbranch_scc1 HELP\n"
    "s_branch FAR\n"
    "MD1:\n"
    "v_bfe_u32 v5, v0, 4, 2\n"
    "v_lshlrev_b32 v1, 2, v3\n"
    "v_add_u32 v1, 149888, v1\n"
    "v_lshlrev_b32 v2, 4, v5\n"
    "v_add_u32 v2, 149888, v2\n"
    "v_bfe_u32 v6, v0, 2, 2\n"
    "v_bfe_u32 v7, v0, 1, 1\n"
    "v_and_b32 v8, 2, v6\n"
    "v_or_b32 v7, v7, v8\n"
    "v_and_b32 v8, 1, v5\n"
    "v_lshl_add_u32 v7, v8, 2, v7\n"
    "v_lshlrev_b32 v8, 10, v5\n"
    "v_lshl_add_u32 v8, v6, 7, v8\n"
    "v_lshl_add_u32 v8, v7, 4, v8\n"
    "v_and_b32 v7, 1, v0\n"
    "v_lshl_add_u32 v8, v7, 3, v8\n"
    "v_xor_b32 v9, 32, v8\n"
    "v_xor_b32 v10, 64, v8\n"
    "v_xor_b32 v11, 0x60, v8\n"
    "v_add_u32 v8, 138240, v8\n"
    "v_add_u32 v9, 138240, v9\n"
    "v_add_u32 v10, 138240, v10\n"
    "v_add_u32 v11, 138240, v11\n"
    "v_and_b32 v3, 63, v0\n"
    "v_lshlrev_b32 v3, 3, v3\n"
    "s_mul_i32 s43, s43, 13824\n"
    "s_lshl_b32 s44, s44, 9\n"
    "s_add_u32 s43, s43, s44\n"
    "s_add_u32 s43, s43, 10519552\n"
    "s_barrier\n"
    "s_cmpk_ge_u32 s2, 243\n"
    "s_cbranch_scc1 M9\n"
    "s_setprio 3\n"
    "s_waitcnt vmcnt(8)\n"
    "v_lshlrev_b32 v5, 16, v4\n"
    "v_and_b32 v4, 0xffff0000, v4\n"
    "v_mul_f32 v6, 0xbfb8aa3b, v5\n"
    "v_mul_f32 v7, 0xbfb8aa3b, v4\n"
    "v_exp_f32 v6, v6\n"
    "v_exp_f32 v7, v7\n"
    "v_add_f32 v6, 1.0, v6\n"
    "v_add_f32 v7, 1.0, v7\n"
    "v_rcp_f32 v6, v6\n"
    "v_rcp_f32 v7, v7\n"
    "v_mul_f32 v5, v5, v6\n"
    "v_mul_f32 v4, v4, v7\n"
    "v_cvt_pk_bf16_f32 v4, v5, v4\n"
    "ds_write_b32 v1, v4\n"
    "ds_read_b128 v[12:15], v2\n"
    "ds_read_b128 v[16:19], v2 offset:64\n"
    "s_waitcnt lgkmcnt(0)\n"
    "s_add_u32 s36, s36, 1\n"
    "s_waitcnt vmcnt(4)\n"
    "ds_read_b64_tr_b16 v[40:41], v8 offset:0\n"
    "ds_read_b64_tr_b16 v[42:43], v8 offset:512\n"
    "ds_read_b64_tr_b16 v[44:45], v9 offset:0\n"
    "ds_read_b64_tr_b16 v[46:47], v9 offset:512\n"
    "ds_read_b64_tr_b16 v[48:49], v10 offset:0\n"
    "ds_read_b64_tr_b16 v[50:51], v10 offset:512\n"
    "ds_read_b64_tr_b16 v[52:53], v11 offset:0\n"
    "ds_read_b64_tr_b16 v[54:55], v11 offset:512\n"
    "s_waitcnt lgkmcnt(6)\n"
    "v_mfma_f32_16x16x32_bf16 v[20:23], v[12:15], v[40:43], 0\n"
    "s_waitcnt lgkmcnt(4)\n"
    "v_mfma_f32_16x16x32_bf16 v[24:27], v[12:15], v[44:47], 0\n"
    "s_waitcnt lgkmcnt(2)\n"
    "v_mfma_f32_16x16x32_bf16 v[28:31], v[12:15], v[48:51], 0\n"
    "s_waitcnt lgkmcnt(0)\n"
    "v_mfma_f32_16x16x32_bf16 v[32:35], v[12:15], v[52:55], 0\n"
    "s_waitcnt vmcnt(0)\n"
    "ds_read_b64_tr_b16 v[56:57], v8 offset:4096\n"
    "ds_read_b64_tr_b16 v[58:59], v8 offset:4608\n"
    "ds_read_b64_tr_b16 v[60:61], v9 offset:4096\n"
    "ds_read_b64_tr_b16 v[62:63], v9 offset:4608\n"
    "ds_read_b64_tr_b16 v[64:65], v10 offset:4096\n"
    "ds_read_b64_tr_b16 v[66:67], v10 offset:4608\n"
    "ds_read_b64_tr_b16 v[68:69], v11 offset:4096\n"
    "ds_read_b64_tr_b16 v[70:71], v11 offset:4608\n"
    "s_waitcnt lgkmcnt(6)\n"
    "v_mfma_f32_16x16x32_bf16 v[20:23], v[16:19], v[56:59], v[20:23]\n"
    "s_waitcnt lgkmcnt(4)\n"
    "v_mfma_f32_16x16x32_bf16 v[24:27], v[16:19], v[60:63], v[24:27]\n"
    "s_waitcnt lgkmcnt(2)\n"
    "v_mfma_f32_16x16x32_bf16 v[28:31], v[16:19], v[64:67], v[28:31]\n"
    "s_waitcnt lgkmcnt(0)\n"
    "v_mfma_f32_16x16x32_bf16 v[32:35], v[16:19], v[68:71], v[32:35]\n"
    "s_nop 1\n"
    "v_mov_b32 v21, s36\n"
    "s_nop 0\n"
    "v_mov_b32_dpp v20, v24 quad_perm:[0,1,2,3] row_mask:0x2 bank_mask:0xf\n"
    "s_nop 1\n"
    "v_mov_b32_dpp v20, v28 quad_perm:[0,1,2,3] row_mask:0x4 bank_mask:0xf\n"
    "s_nop 1\n"
    "v_mov_b32_dpp v20, v32 quad_perm:[0,1,2,3] row_mask:0x8 bank_mask:0xf\n"
    "buffer_store_dwordx2 v[20:21], v3, s[20:23], s43 offen sc1\n"
    "s_setprio 0\n"
    "s_cmpk_ge_u32 s2, 192\n"
    "s_cbranch_scc1 ES1\n"
    "s_mov_b64 exec, 0\n"
    "CH12:\n"
    "s_branch AUX3\n"
    "GA1:\n"
    "s_setprio 1\n"
    "s_mov_b32 s48, 0x01000c0c\n"
    "s_bitcmp1_b32 s37, 0\n"
    "s_cselect_b32 s47, 0x03020c0c, s48\n"
    "s_and_b32 s43, s43, 1\n"
    "s_lshl_b32 s43, s43, 4\n"
    "s_mul_i32 s44, s42, 30464\n"
    "s_and_b32 s45, s37, 1\n"
    "s_lshl_b32 s45, s45, 9\n"
    "s_add_u32 s44, s44, s45\n"
    "v_lshl_add_u32 v2, v1, 3, s44\n"
    "v_lshlrev_b32 v52, 4, v0\n"
    "v_add_u32 v52, 126368, v52\n"
    "s_lshl_b32 s45, s37, 2\n"
    "s_add_u32 s45, s45, 147952\n"
    "v_mov_b32 v12, s45\n"
    "s_lshl_b32 s45, s42, 2\n"
    "s_add_u32 s44, s45, 147968\n"
    "v_mov_b32 v13, s44\n"
    "s_lshl_b32 s45, s45, 1\n"
    "s_add_u32 s45, s45, 148416\n"
    "v_mov_b32 v14, s45\n"
    "v_mov_b32 v15, 1\n"
    "v_mov_b32 v8, 0x3fb8aa3b\n"
    "s_mov_b32 s48, 0x3e22f983\n"
    "s_mov_b32 s49, 0x31dc9c88\n"
    "s_mov_b32 s50, 0x41000000\n"
    "s_mov_b32 s51, 0x43000000\n"
    "s_lshl_b32 s57, s37, 2\n"
    "s_mov_b32 s58, 0x78650432\n"
    "s_lshr_b32 s54, s58, s57\n"
    "s_and_b32 s54, s54, 15\n"
    "s_add_u32 s55, s54, 7\n"
    "s_cmpk_eq_u32 s37, 3\n"
    "s_cselect_b32 s55, 1, s55\n"
    "s_and_b32 s56, s38, 1\n"
    "s_mul_i32 s56, s56, 6144\n"
    "s_lshr_b32 s57, s38, 1\n"
    "s_lshl_b32 s57, s57, 6\n"
    "s_add_u32 s56, s56, s57\n"
    "s_add_u32 s56, s56, 7341056\n"
    "s_mul_i32 s40, s54, 198656\n"
    "s_add_u32 s40, s40, s56\n"
    "s_mul_i32 s41, s55, 198656\n"
    "s_add_u32 s41, s41, s56\n"
    "v_bfe_u32 v66, v0, 2, 4\n"
    "v_mul_u32_u24 v66, 12416, v66\n"
    "v_and_b32 v67, 3, v0\n"
    "v_lshl_add_u32 v66, v67, 4, v66\n"
    "s_barrier\n"
    "s_waitcnt lgkmcnt(0)\n"
    "s_lshr_b32 s46, s46, s43\n"
    "s_lshl_b32 s46, s46, 16\n"
    "s_add_u32 s36, s36, 1\n"
    "v_mul_f32 v9, s46, v8\n"
    "v_exp_f32 v9, v9\n"
    "s_waitcnt vmcnt(3)\n"
    "v_perm_b32 v4, v4, v4, s47\n"
    "v_mul_f32 v4, v4, v8\n"
    "v_cvt_pk_bf16_f32 v9, 0, v9\n"
    "v_exp_f32 v4, v4\n"
    "s_waitcnt vmcnt(2)\n"
    "v_perm_b32 v5, v5, v5, s47\n"
    "v_mul_f32 v5, v5, v9\n"
    "v_cvt_pk_bf16_f32 v4, 0, v4\n"
    "v_mul_f32 v10, s48, v5\n"
    "v_mul_f32 v4, v4, v9\n"
    "v_fma_f32 v11, v5, s48, -v10\n"
    "v_mul_f32_e64 v4, v4, -v8\n"
    "v_fma_f32 v11, v5, s49, v11\n"
    "v_mul_f32 v20, s50, v10\n"
    "v_mul_f32 v21, s50, v4\n"
    "v_fract_f32 v20, v20\n"
    "v_exp_f32 v21, v21\n"
    "v_fma_f32 v20, v11, s50, v20\n"
    "v_cos_f32 v16, v20\n"
    "v_sin_f32 v17, v20\n"
    "v_mul_f32 v22, s51, v10\n"
    "v_mul_f32 v23, s51, v4\n"
    "v_fract_f32 v22, v22\n"
    "v_exp_f32 v23, v23\n"
    "v_fma_f32 v22, v11, s51, v22\n"
    "v_cos_f32 v18, v22\n"
    "v_sin_f32 v19, v22\n"
    "v_pk_mul_f32 v[16:17], v[20:21], v[16:17] op_sel:[1,0] op_sel_hi:[1,1]\n"
    "v_pk_mul_f32 v[18:19], v[22:23], v[18:19] op_sel:[1,0] op_sel_hi:[1,1]\n"
    "s_cmpk_lt_u32 s37, 4\n"
    "s_cbranch_scc1 ASIDE\n"
    "ds_write_b128 v52, v[16:19]\n"
    "s_mov_b64 exec, 1\n"
    "ds_write_b32 v12, v15 offset:4\n"
    "s_mov_b64 exec, -1\n"
    "ASIDE:\n"
    "s_setprio 0\n"
    "v_fract_f32 v22, v10\n"
    "v_exp_f32 v23, v4\n"
    "v_add_f32 v22, v11, v22\n"
    "v_cos_f32 v24, v22\n"
    "v_sin_f32 v25, v22\n"
    "s_waitcnt vmcnt(0)\n"
    "v_and_b32 v32, 0x7fff7fff, v6\n"
    "v_and_b32 v33, 0x7fff7fff, v7\n"
    "v_pk_max_u16 v32, v32, v33\n"
    "v_pk_mul_f32 v[24:25], v[22:23], v[24:25] op_sel:[1,0] op_sel_hi:[1,1]\n"
    "v_lshlrev_b32 v33, 16, v32\n"
    "v_max_u32 v32, v32, v33\n"
    "v_perm_b32 v6, v6, v6, s47\n"
    "v_perm_b32 v7, v7, v7, s47\n"
    "v_max_u32_dpp v32, v32, v32 row_ror:8 row_mask:0xf bank_mask:0xf\n"
    "v_pk_mul_f32 v[20:21], v[18:19], v[18:19] op_sel:[1,1] op_sel_hi:[1,0] neg_lo:[0,1]\n"
    "v_pk_fma_f32 v[30:31], v[18:19], v[18:19], v[20:21] op_sel_hi:[0,1,1]\n"
    "v_max_u32_dpp v32, v32, v32 row_ror:4 row_mask:0xf bank_mask:0xf\n"
    "v_pk_mul_f32 v[20:21], v[24:25], v[24:25] op_sel:[1,1] op_sel_hi:[1,0] neg_lo:[0,1]\n"
    "v_pk_fma_f32 v[26:27], v[24:25], v[24:25], v[20:21] op_sel_hi:[0,1,1]\n"
    "v_max_u32_dpp v32, v32, v32 row_ror:2 row_mask:0xf bank_mask:0xf\n"
    "v_pk_mul_f32 v[20:21], v[26:27], v[26:27] op_sel:[1,1] op_sel_hi:[1,0] neg_lo:[0,1]\n"
    "v_pk_fma_f32 v[28:29], v[26:27], v[26:27], v[20:21] op_sel_hi:[0,1,1]\n"
    "v_max_u32_dpp v32, v32, v32 row_ror:1 row_mask:0xf bank_mask:0xf\n"
    "v_and_b32 v34, 0x7fffff, v9\n"
    "v_or_b32 v34, 0x35800000, v34\n"
    "v_max_u32_dpp v32, v32, v32 row_bcast:15 row_mask:0xa bank_mask:0xf\n"
    "v_and_b32 v35, 0x7f800000, v9\n"
    "s_nop 0\n"
    "v_max_u32_dpp v32, v32, v32 row_bcast:31 row_mask:0xc bank_mask:0xf\n"
    "s_nop 0\n"
    "v_readlane_b32 s44, v32, 63\n"
    "s_lshr_b32 s44, s44, 23\n"
    "s_max_u32 s44, s44, 26\n"
    "s_min_u32 s44, s44, 226\n"
    "s_sub_u32 s46, 266, s44\n"
    "s_lshl_b32 s46, s46, 23\n"
    "s_sub_u32 s44, s44, 132\n"
    "v_pk_mul_f32 v[36:37], v[6:7], s[46:47] op_sel_hi:[1,0]\n"
    "v_ldexp_f32 v35, v35, s44\n"
    "ds_write_b64 v14, v[34:35]\n"
    "v_pk_mul_f32 v[20:21], v[36:37], v[24:25] op_sel:[1,1] op_sel_hi:[1,0] neg_lo:[0,1]\n"
    "v_pk_fma_f32 v[38:39], v[36:37], v[24:25], v[20:21] op_sel_hi:[0,1,1]\n"
    "v_pk_mul_f32 v[20:21], v[38:39], v[24:25] op_sel:[1,1] op_sel_hi:[1,0] neg_lo:[0,1]\n"
    "v_pk_fma_f32 v[40:41], v[38:39], v[24:25], v[20:21] op_sel_hi:[0,1,1]\n"
    "v_pk_mul_f32 v[20:21], v[40:41], v[24:25] op_sel:[1,1] op_sel_hi:[1,0] neg_lo:[0,1]\n"
    "v_pk_fma_f32 v[42:43], v[40:41], v[24:25], v[20:21] op_sel_hi:[0,1,1]\n"
    "v_pk_mul_f32 v[20:21], v[36:37], v[30:31] op_sel:[1,1] op_sel_hi:[1,0] neg_lo:[0,1]\n"
    "v_pk_fma_f32 v[44:45], v[36:37], v[30:31], v[20:21] op_sel_hi:[0,1,1]\n"
    "v_cvt_pk_f16_f32 v54, v36, v38\n"
    "v_cvt_pk_f16_f32 v55, v40, v42\n"
    "v_cvt_pk_f16_f32 v56, -v37, -v39\n"
    "v_cvt_pk_f16_f32 v57, -v41, -v43\n"
    "ds_write_b64 v2, v[54:55] offset:0\n"
    "ds_write_b64 v2, v[56:57] offset:6528\n"
    "v_pk_mul_f32 v[20:21], v[42:43], v[24:25] op_sel:[1,1] op_sel_hi:[1,0] neg_lo:[0,1]\n"
    "v_pk_fma_f32 v[36:37], v[42:43], v[24:25], v[20:21] op_sel_hi:[0,1,1]\n"
    "v_pk_mul_f32 v[20:21], v[36:37], v[24:25] op_sel:[1,1] op_sel_hi:[1,0] neg_lo:[0,1]\n"
    "v_pk_fma_f32 v[38:39], v[36:37], v[24:25], v[20:21] op_sel_hi:[0,1,1]\n"
    "v_pk_mul_f32 v[20:21], v[38:39], v[24:25] op_sel:[1,1] op_sel_hi:[1,0] neg_lo:[0,1]\n"
    "v_pk_fma_f32 v[40:41], v[38:39], v[24:25], v[20:21] op_sel_hi:[0,1,1]\n"
    "v_pk_mul_f32 v[20:21], v[40:41], v[24:25] op_sel:[1,1] op_sel_hi:[1,0] neg_lo:[0,1]\n"
    "v_pk_fma_f32 v[42:43], v[40:41], v[24:25], v[20:21] op_sel_hi:[0,1,1]\n"
    "v_cvt_pk_f16_f32 v58, v36, v38\n"
    "v_cvt_pk_f16_f32 v59, v40, v42\n"
    "v_cvt_pk_f16_f32 v60, -v37, -v39\n"
    "v_cvt_pk_f16_f32 v61, -v41, -v43\n"
    "ds_write_b64 v2, v[58:59] offset:1088\n"
    "ds_write_b64 v2, v[60:61] offset:7616\n"
    "v_pk_mul_f32 v[20:21], v[44:45], v[26:27] op_sel:[1,1] op_sel_hi:[1,0] neg_lo:[0,1]\n"
    "v_pk_fma_f32 v[46:47], v[44:45], v[26:27], v[20:21] op_sel_hi:[0,1,1]\n"
    "v_pk_mul_f32 v[20:21], v[44:45], v[28:29] op_sel:[1,1] op_sel_hi:[1,0] neg_lo:[0,1]\n"
    "v_pk_fma_f32 v[48:49], v[44:45], v[28:29], v[20:21] op_sel_hi:[0,1,1]\n"
    "v_pk_mul_f32 v[20:21], v[48:49], v[26:27] op_sel:[1,1] op_sel_hi:[1,0] neg_lo:[0,1]\n"
    "v_pk_fma_f32 v[50:51], v[48:49], v[26:27], v[20:21] op_sel_hi:[0,1,1]\n"
    "v_pk_mul_f32 v[20:21], v[44:45], v[30:31] op_sel:[1,1] op_sel_hi:[1,0] neg_lo:[0,1]\n"
    "v_pk_fma_f32 v[36:37], v[44:45], v[30:31], v[20:21] op_sel_hi:[0,1,1]\n"
    "v_cvt_pk_f16_f32 v54, v44, v46\n"
    "v_cvt_pk_f16_f32 v55, v48, v50\n"
    "v_cvt_pk_f16_f32 v56, -v45, -v47\n"
    "v_cvt_pk_f16_f32 v57, -v49, -v51\n"
    "ds_write_b64 v2, v[54:55] offset:2176\n"
    "ds_write_b64 v2, v[56:57] offset:8704\n"
    "v_pk_mul_f32 v[20:21], v[36:37], v[30:31] op_sel:[1,1] op_sel_hi:[1,0] neg_lo:[0,1]\n"
    "v_pk_fma_f32 v[38:39], v[36:37], v[30:31], v[20:21] op_sel_hi:[0,1,1]\n"
    "v_pk_mul_f32 v[20:21], v[36:37], v[28:29] op_sel:[1,1] op_sel_hi:[1,0] neg_lo:[0,1]\n"
    "v_pk_fma_f32 v[40:41], v[36:37], v[28:29], v[20:21] op_sel_hi:[0,1,1]\n"
    "v_pk_mul_f32 v[20:21], v[38:39], v[28:29] op_sel:[1,1] op_sel_hi:[1,0] neg_lo:[0,1]\n"
    "v_pk_fma_f32 v[42:43], v[38:39], v[28:29], v[20:21] op_sel_hi:[0,1,1]\n"
    "v_pk_mul_f32 v[20:21], v[38:39], v[30:31] op_sel:[1,1] op_sel_hi:[1,0] neg_lo:[0,1]\n"
    "v_pk_fma_f32 v[44:45], v[38:39], v[30:31], v[20:21] op_sel_hi:[0,1,1]\n"
    "v_cvt_pk_f16_f32 v58, v40, v42\n"
    "v_cvt_pk_f16_f32 v59, v36, v38\n"
    "v_cvt_pk_f16_f32 v60, -v41, -v43\n"
    "v_cvt_pk_f16_f32 v61, -v37, -v39\n"
    "ds_write_b64 v2, v[58:59] offset:3264\n"
    "ds_write_b64 v2, v[60:61] offset:9792\n"
    "v_pk_mul_f32 v[20:21], v[44:45], v[30:31] op_sel:[1,1] op_sel_hi:[1,0] neg_lo:[0,1]\n"
    "v_pk_fma_f32 v[46:47], v[44:45], v[30:31], v[20:21] op_sel_hi:[0,1,1]\n"
    "v_pk_mul_f32 v[20:21], v[46:47], v[30:31] op_sel:[1,1] op_sel_hi:[1,0] neg_lo:[0,1]\n"
    "v_pk_fma_f32 v[48:49], v[46:47], v[30:31], v[20:21] op_sel_hi:[0,1,1]\n"
    "v_pk_mul_f32 v[20:21], v[48:49], v[30:31] op_sel:[1,1] op_sel_hi:[1,0] neg_lo:[0,1]\n"
    "v_pk_fma_f32 v[50:51], v[48:49], v[30:31], v[20:21] op_sel_hi:[0,1,1]\n"
    "v_pk_mul_f32 v[20:21], v[50:51], v[30:31] op_sel:[1,1] op_sel_hi:[1,0] neg_lo:[0,1]\n"
    "v_pk_fma_f32 v[36:37], v[50:51], v[30:31], v[20:21] op_sel_hi:[0,1,1]\n"
    "v_cvt_pk_f16_f32 v54, v44, v46\n"
    "v_cvt_pk_f16_f32 v55, v48, v50\n"
    "v_cvt_pk_f16_f32 v56, -v45, -v47\n"
    "v_cvt_pk_f16_f32 v57, -v49, -v51\n"
    "ds_write_b64 v2, v[54:55] offset:4352\n"
    "ds_write_b64 v2, v[56:57] offset:10880\n"
    "v_cvt_pk_f16_f32 v38, v36, -v37\n"
    "ds_write_b16 v2, v38 offset:5440\n"
    "ds_write_b16_d16_hi v2, v38 offset:11968\n"
    "s_mov_b64 exec, 1\n"
    "ds_add_u32 v13, v15 offset:12\n"
    "s_mov_b64 exec, -1\n"
    "s_memrealtime s[98:99]\n"
    "s_cmpk_ge_u32 s37, 4\n"
    "s_cbranch_scc1 CH45\n"
    "s_waitcnt lgkmcnt(0)\n"
    "s_sub_u32 s61, s98, s96\n"
    "s_cmpk_lt_u32 s61, 200\n"
    "s_cbranch_scc1 BSIDE\n"
    "buffer_load_dwordx4 v[68:71], v66, s[20:23], s40 offen sc1\n"
    "buffer_load_dwordx4 v[72:75], v66, s[20:23], s41 offen sc1\n"
    "BSIDE:\n"
    "v_mov_b32 v36, 0x46800000\n"
    "v_mov_b32 v37, 0\n"
    "v_pk_mul_f32 v[38:39], v[18:19], v[36:37] op_sel_hi:[1,0]\n"
    "v_cvt_pk_f16_f32 v44, v36, v38\n"
    "v_cvt_pk_f16_f32 v46, v37, v39\n"
    "v_pk_mul_f32 v[36:37], v[16:17], v[36:37] op_sel_hi:[1,0]\n"
    "v_pk_mul_f32 v[42:43], v[38:39], v[16:17] op_sel:[1,1] op_sel_hi:[1,0] neg_lo:[0,1]\n"
    "v_pk_fma_f32 v[38:39], v[38:39], v[16:17], v[42:43] op_sel_hi:[0,1,1]\n"
    "v_cvt_pk_f16_f32 v45, v36, v38\n"
    "v_cvt_pk_f16_f32 v47, v37, v39\n"
    "v_pk_mul_f32 v[40:41], v[36:37], v[16:17] op_sel:[1,1] op_sel_hi:[1,0] neg_lo:[0,1]\n"
    "v_pk_fma_f32 v[36:37], v[36:37], v[16:17], v[40:41] op_sel_hi:[0,1,1]\n"
    "ds_write_b64 v2, v[44:45] offset:13056\n"
    "v_pk_mul_f32 v[42:43], v[38:39], v[16:17] op_sel:[1,1] op_sel_hi:[1,0] neg_lo:[0,1]\n"
    "v_pk_fma_f32 v[38:39], v[38:39], v[16:17], v[42:43] op_sel_hi:[0,1,1]\n"
    "ds_write_b64 v2, v[46:47] offset:21760\n"
    "v_cvt_pk_f16_f32 v48, v36, v38\n"
    "v_cvt_pk_f16_f32 v50, v37, v39\n"
    "v_pk_mul_f32 v[40:41], v[36:37], v[16:17] op_sel:[1,1] op_sel_hi:[1,0] neg_lo:[0,1]\n"
    "v_pk_fma_f32 v[36:37], v[36:37], v[16:17], v[40:41] op_sel_hi:[0,1,1]\n"
    "v_pk_mul_f32 v[42:43], v[38:39], v[16:17] op_sel:[1,1] op_sel_hi:[1,0] neg_lo:[0,1]\n"
    "v_pk_fma_f32 v[38:39], v[38:39], v[16:17], v[42:43] op_sel_hi:[0,1,1]\n"
    "v_cvt_pk_f16_f32 v49, v36, v38\n"
    "v_cvt_pk_f16_f32 v51, v37, v39\n"
    "v_pk_mul_f32 v[40:41], v[36:37], v[16:17] op_sel:[1,1] op_sel_hi:[1,0] neg_lo:[0,1]\n"
    "v_pk_fma_f32 v[36:37], v[36:37], v[16:17], v[40:41] op_sel_hi:[0,1,1]\n"
    "ds_write_b64 v2, v[48:49] offset:14144\n"
    "v_pk_mul_f32 v[42:43], v[38:39], v[16:17] op_sel:[1,1] op_sel_hi:[1,0] neg_lo:[0,1]\n"
    "v_pk_fma_f32 v[38:39], v[38:39], v[16:17], v[42:43] op_sel_hi:[0,1,1]\n"
    "ds_write_b64 v2, v[50:51] offset:22848\n"
    "v_cvt_pk_f16_f32 v44, v36, v38\n"
    "v_cvt_pk_f16_f32 v46, v37, v39\n"
    "v_pk_mul_f32 v[40:41], v[36:37], v[16:17] op_sel:[1,1] op_sel_hi:[1,0] neg_lo:[0,1]\n"
    "v_pk_fma_f32 v[36:37], v[36:37], v[16:17], v[40:41] op_sel_hi:[0,1,1]\n"
    "v_pk_mul_f32 v[42:43], v[38:39], v[16:17] op_sel:[1,1] op_sel_hi:[1,0] neg_lo:[0,1]\n"
    "v_pk_fma_f32 v[38:39], v[38:39], v[16:17], v[42:43] op_sel_hi:[0,1,1]\n"
    "v_cvt_pk_f16_f32 v45, v36, v38\n"
    "v_cvt_pk_f16_f32 v47, v37, v39\n"
    "v_pk_mul_f32 v[40:41], v[36:37], v[16:17] op_sel:[1,1] op_sel_hi:[1,0] neg_lo:[0,1]\n"
    "v_pk_fma_f32 v[36:37], v[36:37], v[16:17], v[40:41] op_sel_hi:[0,1,1]\n"
    "ds_write_b64 v2, v[44:45] offset:15232\n"
    "v_pk_mul_f32 v[42:43], v[38:39], v[16:17] op_sel:[1,1] op_sel_hi:[1,0] neg_lo:[0,1]\n"
    "v_pk_fma_f32 v[38:39], v[38:39], v[16:17], v[42:43] op_sel_hi:[0,1,1]\n"
    "ds_write_b64 v2, v[46:47] offset:23936\n"
    "v_cvt_pk_f16_f32 v48, v36, v38\n"
    "v_cvt_pk_f16_f32 v50, v37, v39\n"
    "v_pk_mul_f32 v[40:41], v[36:37], v[16:17] op_sel:[1,1] op_sel_hi:[1,0] neg_lo:[0,1]\n"
    "v_pk_fma_f32 v[36:37], v[36:37], v[16:17], v[40:41] op_sel_hi:[0,1,1]\n"
    "v_pk_mul_f32 v[42:43], v[38:39], v[16:17] op_sel:[1,1] op_sel_hi:[1,0] neg_lo:[0,1]\n"
    "v_pk_fma_f32 v[38:39], v[38:39], v[16:17], v[42:43] op_sel_hi:[0,1,1]\n"
    "v_cvt_pk_f16_f32 v49, v36, v38\n"
    "v_cvt_pk_f16_f32 v51, v37, v39\n"
    "v_pk_mul_f32 v[40:41], v[36:37], v[16:17] op_sel:[1,1] op_sel_hi:[1,0] neg_lo:[0,1]\n"
    "v_pk_fma_f32 v[36:37], v[36:37], v[16:17], v[40:41] op_sel_hi:[0,1,1]\n"
    "ds_write_b64 v2, v[48:49] offset:17408\n"
    "v_pk_mul_f32 v[42:43], v[38:39], v[16:17] op_sel:[1,1] op_sel_hi:[1,0] neg_lo:[0,1]\n"
    "v_pk_fma_f32 v[38:39], v[38:39], v[16:17], v[42:43] op_sel_hi:[0,1,1]\n"
    "ds_write_b64 v2, v[50:51] offset:26112\n"
    "v_cvt_pk_f16_f32 v44, v36, v38\n"
    "v_cvt_pk_f16_f32 v46, v37, v39\n"
    "v_pk_mul_f32 v[40:41], v[36:37], v[16:17] op_sel:[1,1] op_sel_hi:[1,0] neg_lo:[0,1]\n"
    "v_pk_fma_f32 v[36:37], v[36:37], v[16:17], v[40:41] op_sel_hi:[0,1,1]\n"
    "v_pk_mul_f32 v[42:43], v[38:39], v[16:17] op_sel:[1,1] op_sel_hi:[1,0] neg_lo:[0,1]\n"
    "v_pk_fma_f32 v[38:39], v[38:39], v[16:17], v[42:43] op_sel_hi:[0,1,1]\n"
    "v_cvt_pk_f16_f32 v45, v36, v38\n"
    "v_cvt_pk_f16_f32 v47, v37, v39\n"
    "v_pk_mul_f32 v[40:41], v[36:37], v[16:17] op_sel:[1,1] op_sel_hi:[1,0] neg_lo:[0,1]\n"
    "v_pk_fma_f32 v[36:37], v[36:37], v[16:17], v[40:41] op_sel_hi:[0,1,1]\n"
    "ds_write_b64 v2, v[44:45] offset:18496\n"
    "v_pk_mul_f32 v[42:43], v[38:39], v[16:17] op_sel:[1,1] op_sel_hi:[1,0] neg_lo:[0,1]\n"
    "v_pk_fma_f32 v[38:39], v[38:39], v[16:17], v[42:43] op_sel_hi:[0,1,1]\n"
    "ds_write_b64 v2, v[46:47] offset:27200\n"
    "v_cvt_pk_f16_f32 v48, v36, v38\n"
    "v_cvt_pk_f16_f32 v50, v37, v39\n"
    "v_pk_mul_f32 v[40:41], v[36:37], v[16:17] op_sel:[1,1] op_sel_hi:[1,0] neg_lo:[0,1]\n"
    "v_pk_fma_f32 v[36:37], v[36:37], v[16:17], v[40:41] op_sel_hi:[0,1,1]\n"
    "v_pk_mul_f32 v[42:43], v[38:39], v[16:17] op_sel:[1,1] op_sel_hi:[1,0] neg_lo:[0,1]\n"
    "v_pk_fma_f32 v[38:39], v[38:39], v[16:17], v[42:43] op_sel_hi:[0,1,1]\n"
    "v_cvt_pk_f16_f32 v49, v36, v38\n"
    "v_cvt_pk_f16_f32 v51, v37, v39\n"
    "v_pk_mul_f32 v[40:41], v[36:37], v[16:17] op_sel:[1,1] op_sel_hi:[1,0] neg_lo:[0,1]\n"
    "v_pk_fma_f32 v[36:37], v[36:37], v[16:17], v[40:41] op_sel_hi:[0,1,1]\n"
    "ds_write_b64 v2, v[48:49] offset:19584\n"
    "v_pk_mul_f32 v[42:43], v[38:39], v[16:17] op_sel:[1,1] op_sel_hi:[1,0] neg_lo:[0,1]\n"
    "v_pk_fma_f32 v[38:39], v[38:39], v[16:17], v[42:43] op_sel_hi:[0,1,1]\n"
    "ds_write_b64 v2, v[50:51] offset:28288\n"
    "v_cvt_pk_f16_f32 v44, v36, v38\n"
    "v_cvt_pk_f16_f32 v46, v37, v39\n"
    "v_pk_mul_f32 v[40:41], v[36:37], v[16:17] op_sel:[1,1] op_sel_hi:[1,0] neg_lo:[0,1]\n"
    "v_pk_fma_f32 v[36:37], v[36:37], v[16:17], v[40:41] op_sel_hi:[0,1,1]\n"
    "v_pk_mul_f32 v[42:43], v[38:39], v[16:17] op_sel:[1,1] op_sel_hi:[1,0] neg_lo:[0,1]\n"
    "v_pk_fma_f32 v[38:39], v[38:39], v[16:17], v[42:43] op_sel_hi:[0,1,1]\n"
    "v_cvt_pk_f16_f32 v45, v36, v38\n"
    "v_cvt_pk_f16_f32 v47, v37, v39\n"
    "v_pk_mul_f32 v[40:41], v[36:37], v[16:17] op_sel:[1,1] op_sel_hi:[1,0] neg_lo:[0,1]\n"
    "v_pk_fma_f32 v[36:37], v[36:37], v[16:17], v[40:41] op_sel_hi:[0,1,1]\n"
    "ds_write_b64 v2, v[44:45] offset:20672\n"
    "v_pk_mul_f32 v[42:43], v[38:39], v[16:17] op_sel:[1,1] op_sel_hi:[1,0] neg_lo:[0,1]\n"
    "v_pk_fma_f32 v[38:39], v[38:39], v[16:17], v[42:43] op_sel_hi:[0,1,1]\n"
    "ds_write_b64 v2, v[46:47] offset:29376\n"
    "v_cvt_pk_f16_f32 v48, v36, v38\n"
    "v_cvt_pk_f16_f32 v50, v37, v39\n"
    "v_pk_mul_f32 v[40:41], v[36:37], v[16:17] op_sel:[1,1] op_sel_hi:[1,0] neg_lo:[0,1]\n"
    "v_pk_fma_f32 v[36:37], v[36:37], v[16:17], v[40:41] op_sel_hi:[0,1,1]\n"
    "v_pk_mul_f32 v[42:43], v[38:39], v[16:17] op_sel:[1,1] op_sel_hi:[1,0] neg_lo:[0,1]\n"
    "v_pk_fma_f32 v[38:39], v[38:39], v[16:17], v[42:43] op_sel_hi:[0,1,1]\n"
    "v_cvt_pk_f16_f32 v49, v36, v38\n"
    "v_cvt_pk_f16_f32 v51, v37, v39\n"
    "ds_write_b64 v2, v[48:49] offset:16320\n"
    "ds_write_b64 v2, v[50:51] offset:25024\n"
    "s_mov_b64 exec, 1\n"
    "ds_add_u32 v13, v15 offset:28\n"
    "s_mov_b64 exec, -1\n"
    "s_cmpk_ge_u32 s37, 14\n"
    "s_cbranch_scc1 AUX3\n"
    "s_cmpk_ge_u32 s61, 200\n"
    "s_cbranch_scc1 STAGE\n"
    "s_branch XISS\n"
    "CH45:\n"
    "s_mov_b64 exec, 0\n"
    "s_cmpk_eq_u32 s37, 5\n"
    "s_cbranch_scc1 CH5\n"
    "CH4:\n"
    "s_branch HELP\n"
    "CH5:\n"
    "s_branch HELP\n"
    "KST:\n"
    "s_branch E_4\n"
    "R_3:\n"
    "s_sleep 1\n"
    "s_sub_u32 s39, s39, 1\n"
    "s_cbranch_scc1 ABORT\n"
    "E_4:\n"
    "ds_read_b32 v112, v14 offset:24\n"
    "ds_read_b128 v[16:19], v124 offset:0\n"
    "ds_read_b128 v[20:23], v124 offset:1024\n"
    "ds_read_b128 v[24:27], v124 offset:2048\n"
    "ds_read_b128 v[28:31], v124 offset:3072\n"
    "s_waitcnt lgkmcnt(0)\n"
    "v_cmp_ne_u32 vcc, 1, v112\n"
    "s_cbranch_vccnz R_3\n"
    "v_add_u32 v28, s49, v28\n"
    "v_add_u32 v29, s49, v29\n"
    "s_mov_b32 s46, 0x01000100\n"
    "s_mov_b64 s[50:51], 0x1ffff\n"
    "s_branch E_6\n"
    "R_5:\n"
    "s_sleep 2\n"
    "s_sub_u32 s39, s39, 1\n"
    "s_cbranch_scc1 ABORT\n"
    "E_6:\n"
    "ds_read_b32 v112, v125 offset:12\n"
    "s_waitcnt lgkmcnt(0)\n"
    "v_cmp_ne_u32 vcc, 2, v112\n"
    "s_cbranch_vccnz R_5\n"
    "ds_read_b64_tr_b16 v[32:33], v122 offset:0\n"
    "ds_read_b64_tr_b16 v[34:35], v122 offset:6528\n"
    "ds_read_b64_tr_b16 v[36:37], v122 offset:4352\n"
    "ds_read_b64_tr_b16 v[38:39], v122 offset:10880\n"
    "ds_read_b64_tr_b16 v[40:41], v122 offset:128\n"
    "ds_read_b64_tr_b16 v[42:43], v122 offset:6656\n"
    "ds_read_b64_tr_b16 v[44:45], v122 offset:4480\n"
    "ds_read_b64_tr_b16 v[46:47], v122 offset:11008\n"
    "ds_read_b64_tr_b16 v[48:49], v122 offset:256\n"
    "ds_read_b64_tr_b16 v[50:51], v122 offset:6784\n"
    "ds_read_b64_tr_b16 v[52:53], v122 offset:4608\n"
    "ds_read_b64_tr_b16 v[54:55], v122 offset:11136\n"
    "ds_read_b64_tr_b16 v[56:57], v122 offset:384\n"
    "ds_read_b64_tr_b16 v[58:59], v122 offset:6912\n"
    "ds_read_b64_tr_b16 v[60:61], v122 offset:4736\n"
    "ds_read_b64_tr_b16 v[62:63], v122 offset:11264\n"
    "ds_read_b64_tr_b16 v[64:65], v122 offset:512\n"
    "ds_read_b64_tr_b16 v[66:67], v122 offset:7040\n"
    "ds_read_b64_tr_b16 v[68:69], v122 offset:4864\n"
    "ds_read_b64_tr_b16 v[70:71], v122 offset:11392\n"
    "ds_read_b64_tr_b16 v[72:73], v122 offset:640\n"
    "ds_read_b64_tr_b16 v[74:75], v122 offset:7168\n"
    "ds_read_b64_tr_b16 v[76:77], v122 offset:4992\n"
    "ds_read_b64_tr_b16 v[78:79], v122 offset:11520\n"
    "ds_read_b64_tr_b16 v[80:81], v122 offset:768\n"
    "ds_read_b64_tr_b16 v[82:83], v122 offset:7296\n"
    "ds_read_b64_tr_b16 v[84:85], v122 offset:5120\n"
    "ds_read_b64_tr_b16 v[86:87], v122 offset:11648\n"
    "ds_read_b64_tr_b16 v[88:89], v122 offset:896\n"
    "ds_read_b64_tr_b16 v[90:91], v122 offset:7424\n"
    "ds_read_b64_tr_b16 v[92:93], v122 offset:5248\n"
    "ds_read_b64_tr_b16 v[94:95], v122 offset:11776\n"
    "ds_read_b32 v12, v126\n"
    "s_branch E_8\n"
    "R_7:\n"
    "s_sleep 1\n"
    "s_sub_u32 s39, s39, 1\n"
    "s_cbranch_scc1 ABORT\n"
    "E_8:\n"
    "ds_read_b32 v112, v14 offset:28\n"
    "ds_read_b64 v[114:115], v14 offset:32\n"
    "s_waitcnt lgkmcnt(0)\n"
    "v_add3_u32 v112, v112, v114, v115\n"
    "v_cmp_ne_u32 vcc, 6, v112\n"
    "s_cbranch_vccnz R_7\n"
    "s_setprio 3\n"
    "ds_read_b64_tr_b16 v[96:97], v123 offset:13056\n"
    "ds_read_b64_tr_b16 v[98:99], v123 offset:21760\n"
    "ds_read_b64_tr_b16 v[100:101], v123 offset:13184\n"
    "ds_read_b64_tr_b16 v[102:103], v123 offset:21888\n"
    "ds_read_b64_tr_b16 v[104:105], v123 offset:13312\n"
    "ds_read_b64_tr_b16 v[106:107], v123 offset:22016\n"
    "ds_read_b64_tr_b16 v[108:109], v123 offset:13440\n"
    "ds_read_b64_tr_b16 v[110:111], v123 offset:22144\n"
    "s_waitcnt lgkmcnt(6)\n"
    "v_mfma_f32_16x16x32_f16 v[112:115], v[32:35], v[96:99], 0\n"
    "v_mfma_f32_16x16x32_f16 v[116:119], v[36:39], v[96:99], 0\n"
    "ds_read_b64_tr_b16 v[96:97], v123 offset:13568\n"
    "ds_read_b64_tr_b16 v[98:99], v123 offset:22272\n"
    "s_waitcnt lgkmcnt(6)\n"
    "v_mfma_f32_16x16x32_f16 v[112:115], v[40:43], v[100:103], v[112:115]\n"
    "v_mfma_f32_16x16x32_f16 v[116:119], v[44:47], v[100:103], v[116:119]\n"
    "ds_read_b64_tr_b16 v[100:101], v123 offset:13696\n"
    "ds_read_b64_tr_b16 v[102:103], v123 offset:22400\n"
    "s_waitcnt lgkmcnt(6)\n"
    "v_mfma_f32_16x16x32_f16 v[112:115], v[48:51], v[104:107], v[112:115]\n"
    "v_mfma_f32_16x16x32_f16 v[116:119], v[52:55], v[104:107], v[116:119]\n"
    "ds_read_b64_tr_b16 v[104:105], v123 offset:13824\n"
    "ds_read_b64_tr_b16 v[106:107], v123 offset:22528\n"
    "s_waitcnt lgkmcnt(6)\n"
    "v_mfma_f32_16x16x32_f16 v[112:115], v[56:59], v[108:111], v[112:115]\n"
    "v_mfma_f32_16x16x32_f16 v[116:119], v[60:63], v[108:111], v[116:119]\n"
    "ds_read_b64_tr_b16 v[108:109], v123 offset:13952\n"
    "ds_read_b64_tr_b16 v[110:111], v123 offset:22656\n"
    "s_waitcnt lgkmcnt(6)\n"
    "v_mfma_f32_16x16x32_f16 v[112:115], v[64:67], v[96:99], v[112:115]\n"
    "v_mfma_f32_16x16x32_f16 v[116:119], v[68:71], v[96:99], v[116:119]\n"
    "s_waitcnt lgkmcnt(4)\n"
    "v_mfma_f32_16x16x32_f16 v[112:115], v[72:75], v[100:103], v[112:115]\n"
    "v_mfma_f32_16x16x32_f16 v[116:119], v[76:79], v[100:103], v[116:119]\n"
    "s_waitcnt lgkmcnt(2)\n"
    "v_mfma_f32_16x16x32_f16 v[112:115], v[80:83], v[104:107], v[112:115]\n"
    "v_mfma_f32_16x16x32_f16 v[116:119], v[84:87], v[104:107], v[116:119]\n"
    "s_waitcnt lgkmcnt(0)\n"
    "v_mfma_f32_16x16x32_f16 v[112:115], v[88:91], v[108:111], v[112:115]\n"
    "v_mfma_f32_16x16x32_f16 v[116:119], v[92:95], v[108:111], v[116:119]\n"
    "s_cmp_eq_u32 s47, 1\n"
    "s_cbranch_scc1 KT1\n"
    "s_cmp_eq_u32 s47, 2\n"
    "s_cbranch_scc1 KT2\n"
    "KT0:\n"
    "s_nop 2\n"
    "v_pk_mul_f32 v[112:113], v[112:113], v[12:13] op_sel_hi:[1,0]\n"
    "v_pk_mul_f32 v[114:115], v[114:115], v[12:13] op_sel_hi:[1,0]\n"
    "v_cvt_pk_f16_f32 v112, v112, v113\n"
    "v_cvt_pk_f16_f32 v113, v114, v115\n"
    "v_pk_mul_f32 v[116:117], v[116:117], v[12:13] op_sel_hi:[1,0]\n"
    "v_perm_b32 v114, v113, v112, v17\n"
    "v_perm_b32 v115, v113, v112, v18\n"
    "v_perm_b32 v32, v113, v112, v30\n"
    "v_perm_b32 v33, v113, v113, s46\n"
    "v_perm_b32 v112, v113, v112, v19\n"
    "s_mov_b32 exec_hi, s50\n"
    "ds_write_b64 v16, v[114:115] offset:0\n"
    "s_mov_b32 exec_lo, 0\n"
    "ds_write2_b32 v28, v114, v32 offset1:1\n"
    "ds_write2_b32 v29, v33, v113 offset1:1\n"
    "s_mov_b64 exec, -1\n"
    "v_pk_mul_f32 v[118:119], v[118:119], v[12:13] op_sel_hi:[1,0]\n"
    "ds_write_b16 v22, v113 offset:0\n"
    "ds_write_b16_d16_hi v23, v113 offset:0\n"
    "v_cvt_pk_f16_f32 v116, v116, v117\n"
    "ds_write_b16 v20, v112 offset:0\n"
    "ds_write_b16_d16_hi v21, v112 offset:0\n"
    "v_cvt_pk_f16_f32 v117, v118, v119\n"
    "s_mov_b64 exec, s[50:51]\n"
    "ds_write_b16 v24, v116 offset:0\n"
    "ds_write_b16_d16_hi v25, v116 offset:0\n"
    "ds_write_b16 v26, v117 offset:0\n"
    "ds_write_b16_d16_hi v27, v117 offset:0\n"
    "s_mov_b64 exec, 1\n"
    "ds_add_u32 v14, v15 offset:224\n"
    "s_mov_b64 exec, -1\n"
    "s_branch KT3\n"
    "KT1:\n"
    "s_nop 4\n"
    "v_pk_mul_f32 v[112:113], v[112:113], v[12:13] op_sel_hi:[1,0]\n"
    "v_pk_mul_f32 v[114:115], v[114:115], v[12:13] op_sel_hi:[1,0]\n"
    "v_cvt_pk_f16_f32 v112, v112, v113\n"
    "v_cvt_pk_f16_f32 v113, v114, v115\n"
    "v_pk_mul_f32 v[116:117], v[116:117], v[12:13] op_sel_hi:[1,0]\n"
    "v_perm_b32 v114, v113, v112, v17\n"
    "v_perm_b32 v115, v113, v112, v18\n"
    "v_perm_b32 v32, v113, v112, v30\n"
    "v_perm_b32 v33, v113, v113, s46\n"
    "v_perm_b32 v112, v113, v112, v19\n"
    "s_mov_b32 exec_hi, s50\n"
    "ds_write_b64 v16, v[114:115] offset:3328\n"
    "s_mov_b32 exec_lo, 0\n"
    "ds_write2_b32 v28, v114, v32 offset1:1\n"
    "ds_write2_b32 v29, v33, v113 offset1:1\n"
    "s_mov_b64 exec, -1\n"
    "v_pk_mul_f32 v[118:119], v[118:119], v[12:13] op_sel_hi:[1,0]\n"
    "ds_write_b16 v22, v113 offset:3328\n"
    "ds_write_b16_d16_hi v23, v113 offset:3328\n"
    "v_cvt_pk_f16_f32 v116, v116, v117\n"
    "ds_write_b16 v20, v112 offset:3328\n"
    "ds_write_b16_d16_hi v21, v112 offset:3328\n"
    "v_cvt_pk_f16_f32 v117, v118, v119\n"
    "s_mov_b64 exec, s[50:51]\n"
    "ds_write_b16 v24, v116 offset:3328\n"
    "ds_write_b16_d16_hi v25, v116 offset:3328\n"
    "ds_write_b16 v26, v117 offset:3328\n"
    "ds_write_b16_d16_hi v27, v117 offset:3328\n"
    "s_mov_b64 exec, 1\n"
    "ds_add_u32 v14, v15 offset:224\n"
    "s_mov_b64 exec, -1\n"
    "s_branch KT3\n"
    "KT2:\n"
    "s_nop 2\n"
    "v_pk_mul_f32 v[112:113], v[112:113], v[12:13] op_sel_hi:[1,0]\n"
    "v_pk_mul_f32 v[114:115], v[114:115], v[12:13] op_sel_hi:[1,0]\n"
    "v_cvt_pk_f16_f32 v112, v112, v113\n"
    "v_cvt_pk_f16_f32 v113, v114, v115\n"
    "v_pk_mul_f32 v[116:117], v[116:117], v[12:13] op_sel_hi:[1,0]\n"
    "v_perm_b32 v114, v113, v112, v17\n"
    "v_perm_b32 v115, v113, v112, v18\n"
    "v_perm_b32 v32, v113, v112, v30\n"
    "v_perm_b32 v33, v113, v113, s46\n"
    "v_perm_b32 v112, v113, v112, v19\n"
    "s_mov_b32 exec_hi, s50\n"
    "ds_write_b64 v16, v[114:115] offset:6656\n"
    "s_mov_b32 exec_lo, 0\n"
    "ds_write2_b32 v28, v114, v32 offset1:1\n"
    "ds_write2_b32 v29, v33, v113 offset1:1\n"
    "s_mov_b64 exec, -1\n"
    "v_pk_mul_f32 v[118:119], v[118:119], v[12:13] op_sel_hi:[1,0]\n"
    "ds_write_b16 v22, v113 offset:6656\n"
    "ds_write_b16_d16_hi v23, v113 offset:6656\n"
    "v_cvt_pk_f16_f32 v116, v116, v117\n"
    "ds_write_b16 v20, v112 offset:6656\n"
    "ds_write_b16_d16_hi v21, v112 offset:6656\n"
    "v_cvt_pk_f16_f32 v117, v118, v119\n"
    "s_mov_b64 exec, s[50:51]\n"
    "ds_write_b16 v24, v116 offset:6656\n"
    "ds_write_b16_d16_hi v25, v116 offset:6656\n"
    "ds_write_b16 v26, v117 offset:6656\n"
    "ds_write_b16_d16_hi v27, v117 offset:6656\n"
    "s_mov_b64 exec, 1\n"
    "ds_add_u32 v14, v15 offset:224\n"
    "s_mov_b64 exec, -1\n"
    "KT3:\n"
    "s_setprio 1\n"
    "s_cmp_eq_u32 s48, 0\n"
    "s_cbranch_scc1 NEAR1\n"
    "s_cmpk_eq_u32 s37, 6\n"
    "s_cbranch_scc1 HELP1\n"
    "s_branch FAR1\n"
    "AUX1:\n"
    "s_waitcnt lgkmcnt(0)\n"
    "s_load_dword s36, s[20:21], 0\n"
    "s_barrier\n"
    "v_lshlrev_b32 v1, 4, v0\n"
    "v_and_b32 v8, 63, v0\n"
    "v_mov_b32 v15, 1\n"
    "v_mov_b32 v14, 147968\n"
    "s_cmpk_gt_u32 s37, 13\n"
    "s_cbranch_scc1 AUXB\n"
    "s_getpc_b64 s[44:45]\n"
    "s_add_u32 s44, s44, mega_tab@rel32@lo+4\n"
    "s_addc_u32 s45, s45, mega_tab@rel32@hi+12\n"
    "s_mov_b32 m0, 107520\n"
    "v_lshlrev_b32 v2, 4, v8\n"
    "v_add_u32 v3, 6144, v2\n"
    "global_load_lds_dwordx4 v3, s[44:45] offset:0\n"
    "global_load_lds_dwordx4 v3, s[44:45] offset:1024\n"
    "global_load_lds_dwordx4 v3, s[44:45] offset:2048\n"
    "global_load_lds_dwordx4 v3, s[44:45] offset:3072\n"
    "s_mov_b32 m0, 111616\n"
    "v_add_u32 v3, 4096, v3\n"
    "global_load_lds_dwordx4 v3, s[44:45] offset:0\n"
    "global_load_lds_dwordx4 v3, s[44:45] offset:1024\n"
    "global_load_lds_dwordx4 v3, s[44:45] offset:2048\n"
    "global_load_lds_dwordx4 v3, s[44:45] offset:3072\n"
    "s_mov_b32 m0, 101376\n"
    "s_nop 0\n"
    "global_load_lds_dwordx4 v2, s[44:45] offset:0\n"
    "global_load_lds_dwordx4 v2, s[44:45] offset:1024\n"
    "global_load_lds_dwordx4 v2, s[44:45] offset:2048\n"
    "global_load_lds_dwordx4 v2, s[44:45] offset:3072\n"
    "s_mov_b32 m0, 105472\n"
    "v_add_u32 v2, 4096, v2\n"
    "global_load_lds_dwordx4 v2, s[44:45] offset:0\n"
    "global_load_lds_dwordx4 v2, s[44:45] offset:1024\n"
    "v_mov_b32 v4, 0\n"
    "v_mov_b32 v5, 0\n"
    "v_mov_b32 v6, 0\n"
    "v_mov_b32 v7, 0\n"
    "v_add_u32 v2, 78080, v1\n"
    "ds_write_b128 v2, v[4:7] offset:0\n"
    "ds_write_b128 v2, v[4:7] offset:1024\n"
    "ds_write_b128 v2, v[4:7] offset:2048\n"
    "ds_write_b128 v2, v[4:7] offset:3072\n"
    "ds_write_b128 v2, v[4:7] offset:4096\n"
    "ds_write_b128 v2, v[4:7] offset:5120\n"
    "ds_write_b128 v2, v[4:7] offset:6144\n"
    "ds_write_b128 v2, v[4:7] offset:7168\n"
    "ds_write_b128 v2, v[4:7] offset:8192\n"
    "v_min_u32 v2, 92144, v2\n"
    "ds_write_b128 v2, v[4:7] offset:9216\n"
    "v_min_u32 v2, 29, v8\n"
    "v_lshlrev_b32 v2, 4, v2\n"
    "v_add_u32 v2, 120832, v2\n"
    "ds_write_b128 v2, v[4:7] offset:0\n"
    "ds_write_b128 v2, v[4:7] offset:4576\n"
    "ds_write_b128 v2, v[4:7] offset:9152\n"
    "s_branch AUX2\n"
    "AUXB:\n"
    "s_lshl_b32 s42, s37, 9\n"
    "s_add_u32 s42, s42, 53760\n"
    "v_lshl_add_u32 v2, v8, 3, s42\n"
    "v_add_u32 v3, 116128, v1\n"
    "s_lshl_b32 s42, s37, 2\n"
    "s_add_u32 s42, s42, 147912\n"
    "v_mov_b32 v12, s42\n"
    "v_mov_b32 v13, 147976\n"
    "s_branch E_10\n"
    "R_9:\n"
    "s_sleep 1\n"
    "s_sub_u32 s39, s39, 1\n"
    "s_cbranch_scc1 ABORT\n"
    "E_10:\n"
    "ds_read_b32 v20, v12 offset:4\n"
    "ds_read_b128 v[16:19], v3\n"
    "s_waitcnt lgkmcnt(0)\n"
    "v_cmp_ne_u32 vcc, 1, v20\n"
    "s_cbranch_vccnz R_9\n"
    "s_add_u32 s36, s36, 1\n"
    "s_setprio 1\n"
    "s_branch BSIDE\n"
    "AUX2:\n"
    "s_mov_b64 exec, -1\n"
    "s_waitcnt vmcnt(0) lgkmcnt(0)\n"
    "s_add_u32 s36, s36, 1\n"
    "s_mov_b64 exec, 1\n"
    "ds_add_u32 v14, v15 offset:24\n"
    "s_mov_b64 exec, -1\n"
    "AUX3:\n"
    "s_mov_b64 exec, -1\n"
    "s_setprio 0\n"
    "s_and_b32 s44, s2, 7\n"
    "s_lshr_b32 s45, s2, 3\n"
    "s_mul_i32 s44, s44, 24\n"
    "s_add_u32 s38, s44, s45\n"
    "v_mov_b32 v14, 147968\n"
    "ds_read_b32 v2, v14 offset:480\n"
    "s_waitcnt lgkmcnt(0)\n"
    "v_readfirstlane_b32 s96, v2\n"
    "s_lshl_b32 s57, s37, 2\n"
    "s_mov_b32 s58, 0x78650432\n"
    "s_lshr_b32 s54, s58, s57\n"
    "s_and_b32 s54, s54, 15\n"
    "s_add_u32 s55, s54, 7\n"
    "s_cmpk_eq_u32 s37, 3\n"
    "s_cselect_b32 s55, 1, s55\n"
    "s_and_b32 s56, s38, 1\n"
    "s_mul_i32 s56, s56, 6144\n"
    "s_lshr_b32 s57, s38, 1\n"
    "s_lshl_b32 s57, s57, 6\n"
    "s_add_u32 s56, s56, s57\n"
    "s_add_u32 s56, s56, 7341056\n"
    "s_mul_i32 s40, s54, 198656\n"
    "s_add_u32 s40, s40, s56\n"
    "s_mul_i32 s41, s55, 198656\n"
    "s_add_u32 s41, s41, s56\n"
    "v_bfe_u32 v66, v0, 2, 4\n"
    "v_mul_u32_u24 v66, 12416, v66\n"
    "v_and_b32 v67, 3, v0\n"
    "v_lshl_add_u32 v66, v67, 4, v66\n"
    "STAGE:\n"
    "v_mov_b32 v78, 147968\n"
    "v_and_b32 v67, 63, v0\n"
    "v_lshlrev_b32 v67, 2, v67\n"
    "s_lshl_b32 s43, s54, 8\n"
    "s_sub_u32 s43, 120828, s43\n"
    "v_sub_u32 v86, s43, v67\n"
    "s_lshl_b32 s43, s55, 8\n"
    "s_sub_u32 s43, 120828, s43\n"
    "v_sub_u32 v87, s43, v67\n"
    "v_mov_b32 v76, 148352\n"
    "v_and_b32 v111, 63, v0\n"
    "s_lshr_b32 s50, s2, 3\n"
    "s_and_b32 s51, s2, 7\n"
    "s_cmpk_ge_u32 s2, 128\n"
    "s_cbranch_scc1 PFA\n"
    "s_lshl_b32 s51, s51, 4\n"
    "s_add_u32 s51, s51, s50\n"
    "s_mul_i32 s51, s51, 18432\n"
    "s_mul_i32 s60, s37, 768\n"
    "s_add_u32 s51, s51, s60\n"
    "v_lshl_add_u32 v111, v111, 4, s51\n"
    "s_add_u32 s60, s60, 53760\n"
    "s_mov_b64 s[58:59], s[4:5]\n"
    "s_branch PFC\n"
    "PFA:\n"
    "s_sub_u32 s50, s50, 16\n"
    "s_cmpk_lt_u32 s37, 4\n"
    "s_cbranch_scc1 PFB\n"
    "s_lshl2_add_u32 s50, s50, s37\n"
    "s_sub_u32 s50, s50, 12\n"
    "s_mul_i32 s50, s50, 54\n"
    "s_mul_i32 s60, s51, 663552\n"
    "s_lshr_b32 s44, s28, 7\n"
    "s_add_u32 s44, s44, s2\n"
    "s_sub_u32 s44, 3, s44\n"
    "s_and_b32 s44, s44, 7\n"
    "s_add_u32 s44, s44, 1\n"
    "s_add_u32 s44, s44, 7\n"
    "s_mul_i32 s45, s44, 57\n"
    "s_lshr_b32 s45, s45, 9\n"
    "s_mul_i32 s45, s45, 9\n"
    "s_sub_u32 s44, s44, s45\n"
    "s_and_b32 s51, s2, 7\n"
    "s_mul_i32 s51, s51, 9\n"
    "s_add_u32 s51, s51, s44\n"
    "s_mul_i32 s44, s44, 576\n"
    "s_add_u32 s50, s50, s44\n"
    "v_min_u32 v111, 53, v111\n"
    "v_add_u32 v111, s50, v111\n"
    "s_branch PFC\n"
    "PFB:\n"
    "s_lshr_b32 s51, s37, 1\n"
    "s_sub_u32 s51, 8, s51\n"
    "s_lshr_b32 s44, s28, 7\n"
    "s_add_u32 s44, s44, s2\n"
    "s_sub_u32 s44, 3, s44\n"
    "s_and_b32 s44, s44, 7\n"
    "s_add_u32 s44, s44, 1\n"
    "s_add_u32 s44, s44, s51\n"
    "s_mul_i32 s45, s44, 57\n"
    "s_lshr_b32 s45, s45, 9\n"
    "s_mul_i32 s45, s45, 9\n"
    "s_sub_u32 s44, s44, s45\n"
    "s_and_b32 s51, s2, 7\n"
    "s_mul_i32 s51, s51, 9\n"
    "s_add_u32 s51, s51, s44\n"
    "s_lshl_b32 s51, s51, 7\n"
    "s_lshl1_add_u32 s44, s50, s37\n"
    "s_mul_i32 s44, s44, 36\n"
    "s_mul_i32 s45, s50, 36\n"
    "s_add_u32 s45, s45, 288\n"
    "s_cmpk_lt_u32 s37, 2\n"
    "s_cselect_b32 s50, s44, s45\n"
    "s_mul_i32 s50, s50, 9216\n"
    "s_add_u32 s51, s51, s50\n"
    "v_min_u32 v111, 35, v111\n"
    "v_mul_u32_u24 v111, 9216, v111\n"
    "v_add_u32 v111, s51, v111\n"
    "PFC:\n"
    "s_mul_i32 s42, s38, 6\n"
    "s_and_b32 s42, s42, -4\n"
    "s_load_dwordx2 s[52:53], s[24:25], s42\n"
    "s_cmpk_eq_u32 s37, 3\n"
    "s_cbranch_scc1 FAR\n"
    "s_branch EPS\n"
    "STG0:\n"
    "s_cmpk_lt_u32 s37, 4\n"
    "s_cbranch_scc1 STG1\n"
    "s_cmpk_lg_u32 s37, 13\n"
    "s_cbranch_scc1 XISS\n"
    "s_branch CONST\n"
    "STG1:\n"
    "s_setprio 2\n"
    "s_branch E_12\n"
    "R_11:\n"
    "s_sub_u32 s39, s39, 1\n"
    "s_cbranch_scc1 ABORT\n"
    "s_wqm_b64 exec, vcc\n"
    "buffer_load_dwordx4 v[68:71], v66, s[20:23], s40 offen sc1\n"
    "buffer_load_dwordx4 v[72:75], v66, s[20:23], s41 offen sc1\n"
    "s_mov_b64 exec, -1\n"
    "E_12:\n"
    "s_waitcnt vmcnt(1)\n"
    "ds_read_b32 v77, v78 offset:40\n"
    "ds_read_b128 v[80:83], v76\n"
    "ds_read_b64 v[84:85], v76 offset:16\n"
    "s_waitcnt vmcnt(0)\n"
    "v_bitop3_b32 v79, v71, v75, s36 bitop3:0x7e\n"
    "v_cmp_ne_u32 vcc, 0, v79\n"
    "s_cbranch_vccnz R_11\n"
    "v_lshlrev_b32 v88, 16, v68\n"
    "v_and_b32 v89, 0xffff0000, v68\n"
    "v_lshlrev_b32 v90, 16, v69\n"
    "v_and_b32 v91, 0xffff0000, v69\n"
    "v_lshlrev_b32 v92, 16, v70\n"
    "v_and_b32 v93, 0xffff0000, v70\n"
    "STG3:\n"
    "s_waitcnt lgkmcnt(0)\n"
    "v_cmp_ne_u32 vcc, 1, v77\n"
    "s_cbranch_vccnz STGW\n"
    "ds_read_b128 v[114:117], v76 offset:32\n"
    "ds_read_b64 v[118:119], v76 offset:48\n"
    "v_pk_mul_f32 v[88:89], v[88:89], v[80:81] op_sel_hi:[1,0]\n"
    "v_pk_mul_f32 v[90:91], v[90:91], v[82:83] op_sel_hi:[1,0]\n"
    "v_pk_mul_f32 v[92:93], v[92:93], v[84:85] op_sel_hi:[1,0]\n"
    "v_cvt_pk_bf16_f32 v88, 0, v88\n"
    "v_cvt_pk_bf16_f32 v89, 0, v89\n"
    "v_cvt_pk_bf16_f32 v90, 0, v90\n"
    "v_cvt_pk_bf16_f32 v91, 0, v91\n"
    "v_cvt_pk_bf16_f32 v92, 0, v92\n"
    "v_cvt_pk_bf16_f32 v93, 0, v93\n"
    "v_pk_add_f32 v[88:89], v[80:81], v[88:89] op_sel:[1,0] op_sel_hi:[1,1]\n"
    "v_pk_add_f32 v[90:91], v[82:83], v[90:91] op_sel:[1,0] op_sel_hi:[1,1]\n"
    "v_pk_add_f32 v[92:93], v[84:85], v[92:93] op_sel:[1,0] op_sel_hi:[1,1]\n"
    "v_cvt_pk_bf16_f32 v88, 0, v88\n"
    "v_cvt_pk_bf16_f32 v89, 0, v89\n"
    "v_cvt_pk_bf16_f32 v90, 0, v90\n"
    "v_cvt_pk_bf16_f32 v91, 0, v91\n"
    "v_cvt_pk_bf16_f32 v92, 0, v92\n"
    "v_cvt_pk_bf16_f32 v93, 0, v93\n"
    "v_cvt_pkrtz_f16_f32 v94, v89, v88\n"
    "ds_write_b32 v86, v94 offset:0\n"
    "v_cvt_pkrtz_f16_f32 v95, v91, v90\n"
    "ds_write_b32 v86, v95 offset:4576\n"
    "v_cvt_pkrtz_f16_f32 v96, v93, v92\n"
    "ds_write_b32 v86, v96 offset:9152\n"
    "v_lshlrev_b32 v88, 16, v72\n"
    "v_and_b32 v89, 0xffff0000, v72\n"
    "v_lshlrev_b32 v90, 16, v73\n"
    "v_and_b32 v91, 0xffff0000, v73\n"
    "v_lshlrev_b32 v92, 16, v74\n"
    "v_and_b32 v93, 0xffff0000, v74\n"
    "v_pk_mul_f32 v[88:89], v[88:89], v[80:81] op_sel_hi:[1,0]\n"
    "v_pk_mul_f32 v[90:91], v[90:91], v[82:83] op_sel_hi:[1,0]\n"
    "v_pk_mul_f32 v[92:93], v[92:93], v[84:85] op_sel_hi:[1,0]\n"
    "v_cvt_pk_bf16_f32 v88, 0, v88\n"
    "v_cvt_pk_bf16_f32 v89, 0, v89\n"
    "v_cvt_pk_bf16_f32 v90, 0, v90\n"
    "v_cvt_pk_bf16_f32 v91, 0, v91\n"
    "v_cvt_pk_bf16_f32 v92, 0, v92\n"
    "v_cvt_pk_bf16_f32 v93, 0, v93\n"
    "v_pk_add_f32 v[88:89], v[80:81], v[88:89] op_sel:[1,0] op_sel_hi:[1,1]\n"
    "v_pk_add_f32 v[90:91], v[82:83], v[90:91] op_sel:[1,0] op_sel_hi:[1,1]\n"
    "v_pk_add_f32 v[92:93], v[84:85], v[92:93] op_sel:[1,0] op_sel_hi:[1,1]\n"
    "v_cvt_pk_bf16_f32 v88, 0, v88\n"
    "v_cvt_pk_bf16_f32 v89, 0, v89\n"
    "v_cvt_pk_bf16_f32 v90, 0, v90\n"
    "v_cvt_pk_bf16_f32 v91, 0, v91\n"
    "v_cvt_pk_bf16_f32 v92, 0, v92\n"
    "v_cvt_pk_bf16_f32 v93, 0, v93\n"
    "v_cvt_pkrtz_f16_f32 v97, v89, v88\n"
    "ds_write_b32 v87, v97 offset:0\n"
    "v_cvt_pkrtz_f16_f32 v98, v91, v90\n"
    "ds_write_b32 v87, v98 offset:4576\n"
    "v_cvt_pkrtz_f16_f32 v99, v93, v92\n"
    "ds_write_b32 v87, v99 offset:9152\n"
    "s_waitcnt lgkmcnt(0)\n"
    "s_barrier\n"
    "ds_read_b128 v[120:123], v76 offset:64\n"
    "ds_read_b64 v[124:125], v76 offset:80\n"
    "s_cmpk_eq_u32 s37, 3\n"
    "s_cbranch_scc1 FAR3\n"
    "s_cmpk_ge_u32 s2, 128\n"
    "s_cbranch_scc1 PFD\n"
    "s_cmpk_ge_u32 s37, 12\n"
    "s_cbranch_scc1 EPI\n"
    "s_mov_b32 m0, s60\n"
    "s_mov_b32 exec_hi, 0xffff\n"
    "global_load_lds_dwordx4 v111, s[58:59] nt\n"
    "s_add_u32 m0, m0, 2320\n"
    "s_add_u32 s58, s58, 2304\n"
    "s_addc_u32 s59, s59, 0\n"
    "global_load_lds_dwordx4 v111, s[58:59] nt\n"
    "s_add_u32 m0, m0, 2320\n"
    "s_add_u32 s58, s58, 2304\n"
    "s_addc_u32 s59, s59, 0\n"
    "global_load_lds_dwordx4 v111, s[58:59] nt\n"
    "s_add_u32 m0, m0, 2320\n"
    "s_add_u32 s58, s58, 2304\n"
    "s_addc_u32 s59, s59, 0\n"
    "global_load_lds_dwordx4 v111, s[58:59] nt\n"
    "s_add_u32 m0, m0, 2320\n"
    "s_add_u32 s58, s58, 2304\n"
    "s_addc_u32 s59, s59, 0\n"
    "global_load_lds_dwordx4 v111, s[58:59] nt\n"
    "s_add_u32 m0, m0, 2320\n"
    "s_add_u32 s58, s58, 2304\n"
    "s_addc_u32 s59, s59, 0\n"
    "global_load_lds_dwordx4 v111, s[58:59] nt\n"
    "s_add_u32 m0, m0, 2320\n"
    "s_add_u32 s58, s58, 2304\n"
    "s_addc_u32 s59, s59, 0\n"
    "global_load_lds_dwordx4 v111, s[58:59] nt\n"
    "s_add_u32 m0, m0, 2320\n"
    "s_add_u32 s58, s58, 2304\n"
    "s_addc_u32 s59, s59, 0\n"
    "global_load_lds_dwordx4 v111, s[58:59] nt\n"
    "s_add_u32 m0, m0, 2320\n"
    "s_add_u32 s58, s58, 2304\n"
    "s_addc_u32 s59, s59, 0\n"
    "s_mov_b64 exec, -1\n"
    "s_branch EPI\n"
    "PFD:\n"
    "s_mov_b32 m0, 148480\n"
    "s_cmpk_ge_u32 s37, 12\n"
    "s_cbranch_scc1 PFE\n"
    "global_load_lds_dword v111, s[28:29]\n"
    "global_load_lds_dword v111, s[30:31]\n"
    "s_branch EPI\n"
    "PFE:\n"
    "v_subrev_u32 v112, 5184, v111\n"
    "v_min_u32 v111, v111, v112\n"
    "v_lshl_add_u32 v112, v111, 7, s60\n"
    "global_load_lds_dword v112, s[32:33]\n"
    "v_add_u32 v111, 1728, v111\n"
    "s_branch EPI\n"
    "STGW:\n"
    "s_sub_u32 s39, s39, 1\n"
    "s_cbranch_scc1 ABORT\n"
    "ds_read_b32 v77, v78 offset:40\n"
    "ds_read_b128 v[80:83], v76\n"
    "ds_read_b64 v[84:85], v76 offset:16\n"
    "s_branch STG3\n"
    "CONST:\n"
    "v_mov_b32 v4, 0\n"
    "v_mov_b32 v6, 0\n"
    "s_setprio 3\n"
    "v_and_b32 v2, 15, v0\n"
    "v_bfe_u32 v3, v0, 4, 2\n"
    "v_mul_u32_u24 v3, 4608, v3\n"
    "s_movk_i32 s43, 13824\n"
    "v_mad_u32_u24 v3, v2, s43, v3\n"
    "s_mul_i32 s42, s38, 24\n"
    "s_add_u32 s42, s42, 10519552\n"
    "v_lshlrev_b32 v2, 1, v2\n"
    "v_add_u32 v2, 148352, v2\n"
    "s_mov_b32 s43, 0x42000000\n"
    "s_mov_b32 s52, 0x01ff01ff\n"
    "s_movk_i32 s53, 0x01ff\n"
    "s_mov_b32 s62, 0\n"
    "s_movk_i32 s63, 0x0111\n"
    "v_mov_b32 v8, 0\n"
    "s_mov_b64 exec, s[52:53]\n"
    "U_13:\n"
    "s_memrealtime s[98:99]\n"
    "s_waitcnt lgkmcnt(0)\n"
    "s_sub_u32 s98, s98, s96\n"
    "s_cmpk_lt_u32 s98, 200\n"
    "s_cbranch_scc1 U_13\n"
    "buffer_load_dwordx4 v[4:7], v3, s[20:23], s42 offen sc1\n"
    "buffer_load_dwordx2 v[8:9], v3, s[20:23], s42 offen offset:16 sc1\n"
    "s_mov_b64 exec, -1\n"
    "XISS:\n"
    "U_14:\n"
    "s_memrealtime s[98:99]\n"
    "s_waitcnt lgkmcnt(0)\n"
    "s_sub_u32 s98, s98, s96\n"
    "s_cmpk_lt_u32 s98, 210\n"
    "s_cbranch_scc1 U_14\n"
    "buffer_load_dwordx4 v[68:71], v66, s[20:23], s40 offen sc1\n"
    "buffer_load_dwordx4 v[72:75], v66, s[20:23], s41 offen sc1\n"
    "s_cmpk_lt_u32 s37, 4\n"
    "s_cbranch_scc1 STAGE\n"
    "s_cmpk_lg_u32 s37, 13\n"
    "s_cbranch_scc1 STG1\n"
    "s_mov_b64 exec, s[52:53]\n"
    "s_waitcnt vmcnt(2)\n"
    "s_branch E_16\n"
    "R_15:\n"
    "s_sub_u32 s39, s39, 1\n"
    "s_cbranch_scc1 ABORT\n"
    "s_and_b64 exec, exec, vcc\n"
    "buffer_load_dwordx4 v[4:7], v3, s[20:23], s42 offen sc1\n"
    "buffer_load_dwordx2 v[8:9], v3, s[20:23], s42 offen offset:16 sc1\n"
    "s_waitcnt vmcnt(0)\n"
    "E_16:\n"
    "v_bitop3_b32 v10, v5, v7, s36 bitop3:0x7e\n"
    "v_bitop3_b32 v10, v10, v9, s36 bitop3:0xf6\n"
    "v_cmp_ne_u32 vcc, 0, v10\n"
    "s_cbranch_vccnz R_15\n"
    "s_mov_b64 exec, -1\n"
    "v_add_f32_dpp v4, v4, v4 row_ror:8 row_mask:0xf bank_mask:0xf\n"
    "v_add_f32_dpp v6, v6, v6 row_ror:8 row_mask:0xf bank_mask:0xf\n"
    "v_add_f32_dpp v8, v8, v8 row_ror:8 row_mask:0xf bank_mask:0xf\n"
    "v_add_f32_dpp v4, v4, v4 row_ror:4 row_mask:0xf bank_mask:0xf\n"
    "v_add_f32_dpp v6, v6, v6 row_ror:4 row_mask:0xf bank_mask:0xf\n"
    "v_add_f32_dpp v8, v8, v8 row_ror:4 row_mask:0xf bank_mask:0xf\n"
    "s_nop 0\n"
    "v_mov_b32_dpp v4, v6 quad_perm:[0,1,2,3] row_mask:0xf bank_mask:0x2\n"
    "s_nop 1\n"
    "v_mov_b32_dpp v4, v8 quad_perm:[0,1,2,3] row_mask:0xf bank_mask:0x4\n"
    "s_nop 1\n"
    "v_add_f32_dpp v4, v4, v4 quad_perm:[1,0,3,2] row_mask:0xf bank_mask:0xf\n"
    "s_nop 1\n"
    "v_add_f32_dpp v4, v4, v4 quad_perm:[2,3,0,1] row_mask:0xf bank_mask:0xf\n"
    "v_cvt_pk_bf16_f32 v5, 0, v4\n"
    "v_add_f32 v4, 1.0, v5\n"
    "s_mov_b64 exec, s[62:63]\n"
    "ds_write_b32 v2, v5 offset:36\n"
    "s_mov_b64 exec, -1\n"
    "v_cvt_pk_bf16_f32 v4, 0, v4\n"
    "v_mov_b32 v9, v4\n"
    "s_nop 1\n"
    "v_permlane16_swap_b32 v9, v4\n"
    "v_fma_f32 v6, abs(v4), s43, abs(v5)\n"
    "v_frexp_exp_i32_f32 v6, v6\n"
    "v_sub_u32 v7, 11, v6\n"
    "v_ldexp_f32 v10, v4, v7\n"
    "v_ldexp_f32 v11, v5, v7\n"
    "v_subrev_u32 v8, 11, v6\n"
    "v_ldexp_f32 v8, 1.0, v8\n"
    "s_mov_b64 exec, 0x0111\n"
    "ds_write_b64 v2, v[10:11]\n"
    "ds_write_b32 v2, v8 offset:32\n"
    "ds_write_b32 v14, v15 offset:40\n"
    "s_mov_b64 exec, -1\n"
    "s_branch STG1\n"
    "FAR:\n"
    "s_lshr_b32 s42, s37, 2\n"
    "v_and_b32 v6, 15, v0\n"
    "v_bfe_u32 v7, v0, 4, 2\n"
    "v_and_b32 v1, 63, v0\n"
    "v_lshlrev_b32 v1, 4, v1\n"
    "v_add_u32 v1, 101376, v1\n"
    "s_mul_i32 s43, s42, 4576\n"
    "s_add_u32 s43, s43, 117216\n"
    "v_lshlrev_b32 v2, 1, v6\n"
    "v_sub_u32 v2, v7, v2\n"
    "v_lshl_add_u32 v2, v2, 4, s43\n"
    "s_mul_i32 s44, s42, 3328\n"
    "s_add_u32 s43, s44, 92596\n"
    "v_lshlrev_b32 v3, 2, v7\n"
    "v_sub_u32 v3, v6, v3\n"
    "v_lshl_add_u32 v3, v3, 2, s43\n"
    "s_mul_i32 s43, s42, 7680\n"
    "s_add_u32 s43, s43, 28160\n"
    "v_mul_u32_u24 v4, 160, v7\n"
    "v_lshl_add_u32 v4, v6, 3, v4\n"
    "v_add_u32 v4, s43, v4\n"
    "s_add_u32 s43, s42, 147968\n"
    "v_mov_b32 v5, s43\n"
    "v_mov_b32 v8, 1\n"
    "v_mov_b32 v11, 147968\n"
    "s_cmpk_ge_u32 s37, 6\n"
    "s_cbranch_scc1 S2\n"
    "s_branch STG0\n"
    "FAR1:\n"
    "s_branch E_18\n"
    "R_17:\n"
    "s_sleep 1\n"
    "s_sub_u32 s39, s39, 1\n"
    "s_cbranch_scc1 ABORT\n"
    "E_18:\n"
    "ds_read_b32 v12, v11 offset:224\n"
    "s_waitcnt lgkmcnt(0)\n"
    "v_cmp_ne_u32 vcc, 6, v12\n"
    "s_cbranch_vccnz R_17\n"
    "FAR3:\n"
    "ds_read_b128 v[12:15], v1 offset:0\n"
    "ds_read_b128 v[16:19], v1 offset:1024\n"
    "ds_read_b128 v[20:23], v1 offset:2048\n"
    "ds_read2_b32 v[24:25], v3 offset0:0 offset1:1\n"
    "ds_read2_b32 v[26:27], v3 offset0:2 offset1:3\n"
    "ds_read2_b32 v[36:37], v3 offset0:64 offset1:65\n"
    "ds_read2_b32 v[38:39], v3 offset0:66 offset1:67\n"
    "ds_read2_b32 v[28:29], v3 offset0:16 offset1:17\n"
    "ds_read2_b32 v[30:31], v3 offset0:18 offset1:19\n"
    "ds_read2_b32 v[40:41], v3 offset0:80 offset1:81\n"
    "ds_read2_b32 v[42:43], v3 offset0:82 offset1:83\n"
    "ds_read2_b32 v[32:33], v3 offset0:32 offset1:33\n"
    "ds_read2_b32 v[34:35], v3 offset0:34 offset1:35\n"
    "ds_read2_b32 v[44:45], v3 offset0:96 offset1:97\n"
    "ds_read2_b32 v[46:47], v3 offset0:98 offset1:99\n"
    "s_cmpk_eq_u32 s37, 3\n"
    "s_cbranch_scc1 FAR2\n"
    "s_barrier\n"
    "FAR2:\n"
    "s_setprio 3\n"
    "ds_read_b128 v[48:51], v2 offset:3584\n"
    "ds_read_b128 v[52:55], v2 offset:3600\n"
    "ds_read_b128 v[56:59], v2 offset:3072\n"
    "ds_read_b128 v[60:63], v2 offset:3088\n"
    "ds_read_b128 v[64:67], v2 offset:2560\n"
    "ds_read_b128 v[68:71], v2 offset:2576\n"
    "s_waitcnt lgkmcnt(4)\n"
    "v_mfma_f32_16x16x32_f16 v[72:75], v[48:51], v[12:15], 0\n"
    "v_mfma_f32_16x16x32_f16 v[76:79], v[48:51], v[16:19], 0\n"
    "v_mfma_f32_16x16x32_f16 v[80:83], v[48:51], v[20:23], 0\n"
    "v_mfma_f32_16x16x32_f16 v[84:87], v[52:55], v[20:23], 0\n"
    "s_nop 5\n"
    "v_cvt_pk_f16_f32 v88, v75, v79\n"
    "v_cvt_pk_f16_f32 v89, v74, v78\n"
    "v_cvt_pk_f16_f32 v90, v73, v77\n"
    "v_cvt_pk_f16_f32 v91, v72, v76\n"
    "v_cvt_pk_f16_f32 v92, v83, v87\n"
    "v_cvt_pk_f16_f32 v93, v82, v86\n"
    "v_cvt_pk_f16_f32 v94, v81, v85\n"
    "v_cvt_pk_f16_f32 v95, v80, v84\n"
    "s_waitcnt lgkmcnt(2)\n"
    "v_mfma_f32_16x16x32_f16 v[72:75], v[56:59], v[12:15], 0\n"
    "ds_read_b128 v[48:51], v2 offset:2048\n"
    "v_mfma_f32_16x16x32_f16 v[76:79], v[56:59], v[16:19], 0\n"
    "ds_read_b128 v[52:55], v2 offset:2064\n"
    "v_mfma_f32_16x16x32_f16 v[80:83], v[56:59], v[20:23], 0\n"
    "v_mfma_f32_16x16x32_f16 v[84:87], v[60:63], v[20:23], 0\n"
    "v_mfma_f32_16x16x32_f16 v[104:107], v[88:91], v[24:27], 0\n"
    "v_mfma_f32_16x16x32_f16 v[104:107], v[92:95], v[36:39], v[104:107]\n"
    "v_mfma_f32_16x16x32_f16 v[108:111], v[88:91], v[28:31], 0\n"
    "s_nop 1\n"
    "v_cvt_pk_f16_f32 v96, v75, v79\n"
    "v_cvt_pk_f16_f32 v97, v74, v78\n"
    "v_mfma_f32_16x16x32_f16 v[108:111], v[92:95], v[40:43], v[108:111]\n"
    "v_cvt_pk_f16_f32 v98, v73, v77\n"
    "v_cvt_pk_f16_f32 v99, v72, v76\n"
    "v_mfma_f32_16x16x32_f16 v[112:115], v[88:91], v[32:35], 0\n"
    "v_cvt_pk_f16_f32 v100, v83, v87\n"
    "v_cvt_pk_f16_f32 v101, v82, v86\n"
    "v_mfma_f32_16x16x32_f16 v[112:115], v[92:95], v[44:47], v[112:115]\n"
    "v_cvt_pk_f16_f32 v102, v81, v85\n"
    "v_cvt_pk_f16_f32 v103, v80, v84\n"
    "s_waitcnt lgkmcnt(2)\n"
    "v_mfma_f32_16x16x32_f16 v[72:75], v[64:67], v[12:15], 0\n"
    "ds_read_b128 v[56:59], v2 offset:1536\n"
    "v_mfma_f32_16x16x32_f16 v[76:79], v[64:67], v[16:19], 0\n"
    "ds_read_b128 v[60:63], v2 offset:1552\n"
    "v_mfma_f32_16x16x32_f16 v[80:83], v[64:67], v[20:23], 0\n"
    "ds_write_b64 v4, v[104:105] offset:2560\n"
    "v_mfma_f32_16x16x32_f16 v[84:87], v[68:71], v[20:23], 0\n"
    "ds_write_b64 v4, v[106:107] offset:3200\n"
    "v_mfma_f32_16x16x32_f16 v[108:111], v[96:99], v[24:27], v[108:111]\n"
    "ds_write_b8 v5, v8 offset:83\n"
    "v_mfma_f32_16x16x32_f16 v[108:111], v[100:103], v[36:39], v[108:111]\n"
    "v_mfma_f32_16x16x32_f16 v[112:115], v[96:99], v[28:31], v[112:115]\n"
    "v_cvt_pk_f16_f32 v88, v75, v79\n"
    "v_cvt_pk_f16_f32 v89, v74, v78\n"
    "v_mfma_f32_16x16x32_f16 v[112:115], v[100:103], v[40:43], v[112:115]\n"
    "v_cvt_pk_f16_f32 v90, v73, v77\n"
    "v_cvt_pk_f16_f32 v91, v72, v76\n"
    "v_mfma_f32_16x16x32_f16 v[116:119], v[96:99], v[32:35], 0\n"
    "v_cvt_pk_f16_f32 v92, v83, v87\n"
    "v_cvt_pk_f16_f32 v93, v82, v86\n"
    "v_mfma_f32_16x16x32_f16 v[116:119], v[100:103], v[44:47], v[116:119]\n"
    "v_cvt_pk_f16_f32 v94, v81, v85\n"
    "v_cvt_pk_f16_f32 v95, v80, v84\n"
    "s_setprio 2\n"
    "s_waitcnt lgkmcnt(5)\n"
    "v_mfma_f32_16x16x32_f16 v[72:75], v[48:51], v[12:15], 0\n"
    "v_mfma_f32_16x16x32_f16 v[76:79], v[48:51], v[16:19], 0\n"
    "v_mfma_f32_16x16x32_f16 v[80:83], v[48:51], v[20:23], 0\n"
    "ds_write_b64 v4, v[108:109] offset:3840\n"
    "v_mfma_f32_16x16x32_f16 v[84:87], v[52:55], v[20:23], 0\n"
    "ds_write_b64 v4, v[110:111] offset:4480\n"
    "v_mfma_f32_16x16x32_f16 v[112:115], v[88:91], v[24:27], v[112:115]\n"
    "ds_write_b8 v5, v8 offset:91\n"
    "v_mfma_f32_16x16x32_f16 v[112:115], v[92:95], v[36:39], v[112:115]\n"
    "v_mfma_f32_16x16x32_f16 v[116:119], v[88:91], v[28:31], v[116:119]\n"
    "v_cvt_pk_f16_f32 v96, v75, v79\n"
    "v_cvt_pk_f16_f32 v97, v74, v78\n"
    "v_mfma_f32_16x16x32_f16 v[116:119], v[92:95], v[40:43], v[116:119]\n"
    "v_cvt_pk_f16_f32 v98, v73, v77\n"
    "v_cvt_pk_f16_f32 v99, v72, v76\n"
    "v_mfma_f32_16x16x32_f16 v[120:123], v[88:91], v[32:35], 0\n"
    "v_cvt_pk_f16_f32 v100, v83, v87\n"
    "v_cvt_pk_f16_f32 v101, v82, v86\n"
    "v_mfma_f32_16x16x32_f16 v[120:123], v[92:95], v[44:47], v[120:123]\n"
    "v_cvt_pk_f16_f32 v102, v81, v85\n"
    "v_cvt_pk_f16_f32 v103, v80, v84\n"
    "s_setprio 1\n"
    "s_waitcnt lgkmcnt(6)\n"
    "v_mfma_f32_16x16x32_f16 v[72:75], v[56:59], v[12:15], 0\n"
    "v_mfma_f32_16x16x32_f16 v[76:79], v[56:59], v[16:19], 0\n"
    "v_mfma_f32_16x16x32_f16 v[80:83], v[56:59], v[20:23], 0\n"
    "ds_write_b64 v4, v[112:113] offset:5120\n"
    "v_mfma_f32_16x16x32_f16 v[84:87], v[60:63], v[20:23], 0\n"
    "ds_write_b64 v4, v[114:115] offset:5760\n"
    "v_mfma_f32_16x16x32_f16 v[116:119], v[96:99], v[24:27], v[116:119]\n"
    "ds_write_b8 v5, v8 offset:99\n"
    "v_mfma_f32_16x16x32_f16 v[116:119], v[100:103], v[36:39], v[116:119]\n"
    "v_mfma_f32_16x16x32_f16 v[120:123], v[96:99], v[28:31], v[120:123]\n"
    "v_cvt_pk_f16_f32 v88, v75, v79\n"
    "v_cvt_pk_f16_f32 v89, v74, v78\n"
    "v_mfma_f32_16x16x32_f16 v[120:123], v[100:103], v[40:43], v[120:123]\n"
    "v_cvt_pk_f16_f32 v90, v73, v77\n"
    "v_cvt_pk_f16_f32 v91, v72, v76\n"
    "v_mfma_f32_16x16x32_f16 v[124:127], v[96:99], v[32:35], 0\n"
    "v_cvt_pk_f16_f32 v92, v83, v87\n"
    "v_cvt_pk_f16_f32 v93, v82, v86\n"
    "v_mfma_f32_16x16x32_f16 v[124:127], v[100:103], v[44:47], v[124:127]\n"
    "v_cvt_pk_f16_f32 v94, v81, v85\n"
    "v_cvt_pk_f16_f32 v95, v80, v84\n"
    "s_setprio 0\n"
    "v_mfma_f32_16x16x32_f16 v[120:123], v[88:91], v[24:27], v[120:123]\n"
    "v_mfma_f32_16x16x32_f16 v[120:123], v[92:95], v[36:39], v[120:123]\n"
    "ds_write_b64 v4, v[116:117] offset:6400\n"
    "v_mfma_f32_16x16x32_f16 v[124:127], v[88:91], v[28:31], v[124:127]\n"
    "ds_write_b64 v4, v[118:119] offset:7040\n"
    "v_mfma_f32_16x16x32_f16 v[124:127], v[92:95], v[40:43], v[124:127]\n"
    "ds_write_b8 v5, v8 offset:107\n"
    "s_nop 2\n"
    "ds_write_b64 v4, v[120:121] offset:7680\n"
    "ds_write_b64 v4, v[122:123] offset:8320\n"
    "ds_write_b8 v5, v8 offset:115\n"
    "s_nop 0\n"
    "ds_write_b64 v4, v[124:125] offset:8960\n"
    "ds_write_b64 v4, v[126:127] offset:9600\n"
    "ds_write_b8 v5, v8 offset:123\n"
    "s_setprio 0\n"
    "s_branch LN2\n"
    "HELP:\n"
    "s_mov_b64 exec, -1\n"
    "s_sub_u32 s42, s37, 4\n"
    "v_and_b32 v6, 15, v0\n"
    "v_bfe_u32 v7, v0, 4, 2\n"
    "v_and_b32 v1, 63, v0\n"
    "v_lshlrev_b32 v1, 4, v1\n"
    "v_add_u32 v1, 101376, v1\n"
    "s_mul_i32 s43, s42, 4576\n"
    "s_add_u32 s43, s43, 117216\n"
    "v_lshlrev_b32 v2, 1, v6\n"
    "v_sub_u32 v2, v7, v2\n"
    "v_lshl_add_u32 v2, v2, 4, s43\n"
    "s_mul_i32 s44, s42, 3328\n"
    "s_add_u32 s43, s44, 92596\n"
    "v_lshlrev_b32 v3, 2, v7\n"
    "v_sub_u32 v3, v6, v3\n"
    "v_lshl_add_u32 v3, v3, 2, s43\n"
    "s_mul_i32 s43, s42, 10240\n"
    "s_add_u32 s43, s43, 0\n"
    "v_mul_u32_u24 v4, 160, v7\n"
    "v_lshl_add_u32 v4, v6, 3, v4\n"
    "v_add_u32 v4, s43, v4\n"
    "s_add_u32 s43, s42, 147968\n"
    "v_mov_b32 v5, s43\n"
    "v_mov_b32 v8, 1\n"
    "v_mov_b32 v11, 147968\n"
    "s_add_u32 s44, s44, 92990\n"
    "v_mov_b32 v9, s44\n"
    "s_mul_i32 s43, s42, 3\n"
    "v_add_u32 v5, s43, v5\n"
    "s_cmpk_ge_u32 s37, 6\n"
    "s_cbranch_scc1 S2\n"
    "HELP1:\n"
    "s_mov_b32 s43, 0x01000100\n"
    "s_branch E_20\n"
    "R_19:\n"
    "s_sleep 1\n"
    "s_sub_u32 s39, s39, 1\n"
    "s_cbranch_scc1 ABORT\n"
    "E_20:\n"
    "ds_read_b32 v12, v11 offset:224\n"
    "s_waitcnt lgkmcnt(0)\n"
    "v_cmp_ne_u32 vcc, 6, v12\n"
    "s_cbranch_vccnz R_19\n"
    "ds_read_b128 v[12:15], v1 offset:3072\n"
    "ds_read_b128 v[16:19], v1 offset:4096\n"
    "ds_read_b128 v[20:23], v1 offset:5120\n"
    "ds_read_u16 v10, v9\n"
    "ds_read2_b32 v[24:25], v3 offset0:128 offset1:129\n"
    "ds_read2_b32 v[26:27], v3 offset0:130 offset1:131\n"
    "ds_read2_b32 v[28:29], v3 offset0:144 offset1:145\n"
    "ds_read2_b32 v[30:31], v3 offset0:146 offset1:147\n"
    "ds_read2_b32 v[32:33], v3 offset0:160 offset1:161\n"
    "ds_read2_b32 v[34:35], v3 offset0:162 offset1:163\n"
    "ds_read2_b32 v[36:37], v3 offset0:176 offset1:177\n"
    "ds_read2_b32 v[38:39], v3 offset0:178 offset1:179\n"
    "s_waitcnt lgkmcnt(8)\n"
    "v_perm_b32 v10, v10, v10, s43\n"
    "v_and_b32 v20, v20, v10\n"
    "v_and_b32 v21, v21, v10\n"
    "v_and_b32 v22, v22, v10\n"
    "v_and_b32 v23, v23, v10\n"
    "s_setprio 3\n"
    "s_barrier\n"
    "ds_read_b128 v[40:43], v2 offset:3584\n"
    "ds_read_b128 v[44:47], v2 offset:3600\n"
    "ds_read_b128 v[48:51], v2 offset:3072\n"
    "ds_read_b128 v[52:55], v2 offset:3088\n"
    "ds_read_b128 v[56:59], v2 offset:2560\n"
    "ds_read_b128 v[60:63], v2 offset:2576\n"
    "ds_read_b128 v[64:67], v2 offset:2048\n"
    "ds_read_b128 v[68:71], v2 offset:2064\n"
    "ds_read_b128 v[104:107], v2 offset:1040\n"
    "s_waitcnt lgkmcnt(7)\n"
    "v_mfma_f32_16x16x32_f16 v[72:75], v[40:43], v[12:15], 0\n"
    "v_mfma_f32_16x16x32_f16 v[76:79], v[44:47], v[16:19], 0\n"
    "ds_read_b128 v[44:47], v1 offset:0\n"
    "s_waitcnt lgkmcnt(6)\n"
    "v_mfma_f32_16x16x32_f16 v[80:83], v[48:51], v[12:15], 0\n"
    "v_mfma_f32_16x16x32_f16 v[84:87], v[52:55], v[16:19], 0\n"
    "ds_read_b128 v[52:55], v1 offset:1024\n"
    "s_waitcnt lgkmcnt(5)\n"
    "v_mfma_f32_16x16x32_f16 v[88:91], v[56:59], v[12:15], 0\n"
    "v_mfma_f32_16x16x32_f16 v[92:95], v[60:63], v[16:19], 0\n"
    "ds_read_b128 v[60:63], v1 offset:2048\n"
    "s_waitcnt lgkmcnt(4)\n"
    "v_mfma_f32_16x16x32_f16 v[96:99], v[64:67], v[12:15], 0\n"
    "v_mfma_f32_16x16x32_f16 v[100:103], v[68:71], v[16:19], 0\n"
    "ds_read_b128 v[68:71], v2 offset:1024\n"
    "v_mfma_f32_16x16x32_f16 v[108:111], v[20:23], v[40:43], 0\n"
    "v_cvt_pk_f16_f32 v12, v75, v79\n"
    "v_cvt_pk_f16_f32 v13, v74, v78\n"
    "v_mfma_f32_16x16x32_f16 v[112:115], v[20:23], v[48:51], 0\n"
    "v_cvt_pk_f16_f32 v14, v73, v77\n"
    "v_cvt_pk_f16_f32 v15, v72, v76\n"
    "v_mfma_f32_16x16x32_f16 v[116:119], v[20:23], v[56:59], 0\n"
    "v_cvt_pk_f16_f32 v16, v83, v87\n"
    "v_cvt_pk_f16_f32 v17, v82, v86\n"
    "v_mfma_f32_16x16x32_f16 v[120:123], v[20:23], v[64:67], 0\n"
    "v_cvt_pk_f16_f32 v18, v81, v85\n"
    "v_cvt_pk_f16_f32 v19, v80, v84\n"
    "ds_read2_b32 v[40:41], v3 offset0:0 offset1:1\n"
    "ds_read2_b32 v[42:43], v3 offset0:2 offset1:3\n"
    "ds_read2_b32 v[48:49], v3 offset0:64 offset1:65\n"
    "ds_read2_b32 v[50:51], v3 offset0:66 offset1:67\n"
    "v_mfma_f32_16x16x32_f16 v[108:111], v[12:15], v[24:27], v[108:111]\n"
    "v_cvt_pk_f16_f32 v20, v91, v95\n"
    "v_cvt_pk_f16_f32 v21, v90, v94\n"
    "v_mfma_f32_16x16x32_f16 v[112:115], v[12:15], v[28:31], v[112:115]\n"
    "v_cvt_pk_f16_f32 v22, v89, v93\n"
    "v_cvt_pk_f16_f32 v23, v88, v92\n"
    "v_mfma_f32_16x16x32_f16 v[116:119], v[12:15], v[32:35], v[116:119]\n"
    "v_cvt_pk_f16_f32 v124, v99, v103\n"
    "v_cvt_pk_f16_f32 v125, v98, v102\n"
    "v_mfma_f32_16x16x32_f16 v[120:123], v[12:15], v[36:39], v[120:123]\n"
    "v_cvt_pk_f16_f32 v126, v97, v101\n"
    "v_cvt_pk_f16_f32 v127, v96, v100\n"
    "s_waitcnt lgkmcnt(4)\n"
    "v_mfma_f32_16x16x32_f16 v[72:75], v[68:71], v[44:47], 0\n"
    "v_mfma_f32_16x16x32_f16 v[76:79], v[68:71], v[52:55], 0\n"
    "v_mfma_f32_16x16x32_f16 v[80:83], v[68:71], v[60:63], 0\n"
    "ds_write_b64 v4, v[108:109] offset:5120\n"
    "v_mfma_f32_16x16x32_f16 v[84:87], v[104:107], v[60:63], 0\n"
    "ds_write_b64 v4, v[110:111] offset:5760\n"
    "v_mfma_f32_16x16x32_f16 v[112:115], v[16:19], v[24:27], v[112:115]\n"
    "v_mfma_f32_16x16x32_f16 v[116:119], v[16:19], v[28:31], v[116:119]\n"
    "v_mfma_f32_16x16x32_f16 v[120:123], v[16:19], v[32:35], v[120:123]\n"
    "s_nop 0\n"
    "v_cvt_pk_f16_f32 v12, v75, v79\n"
    "v_cvt_pk_f16_f32 v13, v74, v78\n"
    "v_mfma_f32_16x16x32_f16 v[116:119], v[20:23], v[24:27], v[116:119]\n"
    "v_cvt_pk_f16_f32 v14, v73, v77\n"
    "v_cvt_pk_f16_f32 v15, v72, v76\n"
    "v_mfma_f32_16x16x32_f16 v[120:123], v[20:23], v[28:31], v[120:123]\n"
    "v_cvt_pk_f16_f32 v16, v83, v87\n"
    "v_cvt_pk_f16_f32 v17, v82, v86\n"
    "v_mfma_f32_16x16x32_f16 v[120:123], v[124:127], v[24:27], v[120:123]\n"
    "v_cvt_pk_f16_f32 v18, v81, v85\n"
    "v_cvt_pk_f16_f32 v19, v80, v84\n"
    "s_waitcnt lgkmcnt(2)\n"
    "v_mfma_f32_16x16x32_f16 v[120:123], v[12:15], v[40:43], v[120:123]\n"
    "ds_write_b64 v4, v[112:113] offset:6400\n"
    "ds_write_b64 v4, v[114:115] offset:7040\n"
    "v_mfma_f32_16x16x32_f16 v[120:123], v[16:19], v[48:51], v[120:123]\n"
    "ds_write_b64 v4, v[116:117] offset:7680\n"
    "ds_write_b64 v4, v[118:119] offset:8320\n"
    "s_nop 5\n"
    "ds_write_b64 v4, v[120:121] offset:8960\n"
    "ds_write_b64 v4, v[122:123] offset:9600\n"
    "ds_write_b8 v5, v8 offset:44\n"
    "s_branch MISC\n"
    "NEAR:\n"
    "v_and_b32 v6, 15, v0\n"
    "v_bfe_u32 v7, v0, 4, 2\n"
    "v_and_b32 v1, 63, v0\n"
    "v_lshlrev_b32 v1, 4, v1\n"
    "v_add_u32 v1, 101376, v1\n"
    "s_mul_i32 s43, s42, 4576\n"
    "s_add_u32 s43, s43, 117216\n"
    "v_lshlrev_b32 v2, 1, v6\n"
    "v_sub_u32 v2, v7, v2\n"
    "v_lshl_add_u32 v2, v2, 4, s43\n"
    "s_mul_i32 s44, s42, 3328\n"
    "s_add_u32 s43, s44, 92596\n"
    "v_lshlrev_b32 v3, 2, v7\n"
    "v_sub_u32 v3, v6, v3\n"
    "v_lshl_add_u32 v3, v3, 2, s43\n"
    "s_mul_i32 s43, s42, 10240\n"
    "s_add_u32 s43, s43, 0\n"
    "v_mul_u32_u24 v4, 160, v7\n"
    "v_lshl_add_u32 v4, v6, 3, v4\n"
    "v_add_u32 v4, s43, v4\n"
    "s_add_u32 s43, s42, 147968\n"
    "v_mov_b32 v5, s43\n"
    "v_mov_b32 v8, 1\n"
    "v_mov_b32 v11, 147968\n"
    "v_lshl_add_u32 v3, v7, 3, v6\n"
    "v_add_u32 v3, 1, v3\n"
    "v_and_b32 v9, 1, v3\n"
    "v_mul_u32_u24 v9, 2110, v9\n"
    "s_add_u32 s44, s44, 91392\n"
    "v_lshl_add_u32 v3, v3, 1, s44\n"
    "v_add_u32 v3, v3, v9\n"
    "v_add_u32 v1, 512, v3\n"
    "s_mul_i32 s43, s42, 3\n"
    "v_add_u32 v10, s43, v5\n"
    "s_branch S2\n"
    "NEAR1:\n"
    "s_branch E_22\n"
    "R_21:\n"
    "s_sub_u32 s39, s39, 1\n"
    "s_cbranch_scc1 ABORT\n"
    "E_22:\n"
    "ds_read_b32 v12, v11 offset:224\n"
    "s_waitcnt lgkmcnt(0)\n"
    "v_cmp_ne_u32 vcc, 6, v12\n"
    "s_cbranch_vccnz R_21\n"
    "ds_read2_b32 v[88:89], v3 offset0:112 offset1:113\n"
    "ds_read2_b32 v[90:91], v3 offset0:114 offset1:115\n"
    "ds_read2_b32 v[120:121], v1 offset0:112 offset1:113\n"
    "ds_read2_b32 v[122:123], v1 offset0:114 offset1:115\n"
    "ds_read2_b32 v[84:85], v3 offset0:96 offset1:97\n"
    "ds_read2_b32 v[86:87], v3 offset0:98 offset1:99\n"
    "ds_read2_b32 v[116:117], v1 offset0:96 offset1:97\n"
    "ds_read2_b32 v[118:119], v1 offset0:98 offset1:99\n"
    "ds_read2_b32 v[80:81], v3 offset0:80 offset1:81\n"
    "ds_read2_b32 v[82:83], v3 offset0:82 offset1:83\n"
    "ds_read2_b32 v[112:113], v1 offset0:80 offset1:81\n"
    "ds_read2_b32 v[114:115], v1 offset0:82 offset1:83\n"
    "ds_read2_b32 v[76:77], v3 offset0:64 offset1:65\n"
    "ds_read2_b32 v[78:79], v3 offset0:66 offset1:67\n"
    "ds_read2_b32 v[108:109], v1 offset0:64 offset1:65\n"
    "ds_read2_b32 v[110:111], v1 offset0:66 offset1:67\n"
    "s_setprio 3\n"
    "s_barrier\n"
    "ds_read_b128 v[24:27], v2 offset:4032\n"
    "ds_read_b128 v[28:31], v2 offset:3968\n"
    "ds_read_b128 v[32:35], v2 offset:3904\n"
    "s_waitcnt lgkmcnt(2)\n"
    "v_mfma_f32_16x16x32_f16 v[48:51], v[88:91], v[24:27], 0\n"
    "ds_read2_b32 v[72:73], v3 offset0:48 offset1:49\n"
    "ds_read2_b32 v[74:75], v3 offset0:50 offset1:51\n"
    "ds_read2_b32 v[104:105], v1 offset0:48 offset1:49\n"
    "ds_read2_b32 v[106:107], v1 offset0:50 offset1:51\n"
    "ds_read_b128 v[36:39], v2 offset:3840\n"
    "v_mfma_f32_16x16x32_f16 v[52:55], v[120:123], v[24:27], 0\n"
    "s_waitcnt lgkmcnt(6)\n"
    "v_mfma_f32_16x16x32_f16 v[48:51], v[84:87], v[28:31], v[48:51]\n"
    "ds_read2_b32 v[68:69], v3 offset0:32 offset1:33\n"
    "ds_read2_b32 v[70:71], v3 offset0:34 offset1:35\n"
    "ds_read2_b32 v[100:101], v1 offset0:32 offset1:33\n"
    "ds_read2_b32 v[102:103], v1 offset0:34 offset1:35\n"
    "ds_read_b128 v[40:43], v2 offset:3776\n"
    "v_mfma_f32_16x16x32_f16 v[52:55], v[116:119], v[28:31], v[52:55]\n"
    "s_waitcnt lgkmcnt(10)\n"
    "v_mfma_f32_16x16x32_f16 v[48:51], v[80:83], v[32:35], v[48:51]\n"
    "ds_read2_b32 v[64:65], v3 offset0:16 offset1:17\n"
    "ds_read2_b32 v[66:67], v3 offset0:18 offset1:19\n"
    "ds_read2_b32 v[96:97], v1 offset0:16 offset1:17\n"
    "ds_read2_b32 v[98:99], v1 offset0:18 offset1:19\n"
    "ds_read_b128 v[44:47], v2 offset:3712\n"
    "v_mfma_f32_16x16x32_f16 v[52:55], v[112:115], v[32:35], v[52:55]\n"
    "s_waitcnt lgkmcnt(10)\n"
    "v_mfma_f32_16x16x32_f16 v[48:51], v[76:79], v[36:39], v[48:51]\n"
    "ds_read2_b32 v[60:61], v3 offset0:0 offset1:1\n"
    "ds_read2_b32 v[62:63], v3 offset0:2 offset1:3\n"
    "ds_read2_b32 v[92:93], v1 offset0:0 offset1:1\n"
    "ds_read2_b32 v[94:95], v1 offset0:2 offset1:3\n"
    "ds_read2_b32 v[124:125], v1 offset0:128 offset1:129\n"
    "ds_read2_b32 v[126:127], v1 offset0:130 offset1:131\n"
    "ds_read_b128 v[24:27], v2 offset:3648\n"
    "v_mfma_f32_16x16x32_f16 v[52:55], v[108:111], v[36:39], v[52:55]\n"
    "s_waitcnt lgkmcnt(12)\n"
    "v_mfma_f32_16x16x32_f16 v[48:51], v[72:75], v[40:43], v[48:51]\n"
    "ds_read_b128 v[28:31], v2 offset:3584\n"
    "ds_read_b128 v[32:35], v2 offset:3520\n"
    "v_mfma_f32_16x16x32_f16 v[52:55], v[104:107], v[40:43], v[52:55]\n"
    "s_waitcnt lgkmcnt(9)\n"
    "v_mfma_f32_16x16x32_f16 v[48:51], v[68:71], v[44:47], v[48:51]\n"
    "ds_read_b128 v[36:39], v2 offset:3456\n"
    "ds_read_b128 v[40:43], v2 offset:3392\n"
    "v_mfma_f32_16x16x32_f16 v[52:55], v[100:103], v[44:47], v[52:55]\n"
    "s_waitcnt lgkmcnt(4)\n"
    "v_mfma_f32_16x16x32_f16 v[48:51], v[64:67], v[24:27], v[48:51]\n"
    "ds_read_b128 v[44:47], v2 offset:3328\n"
    "v_mfma_f32_16x16x32_f16 v[52:55], v[96:99], v[24:27], v[52:55]\n"
    "s_waitcnt lgkmcnt(4)\n"
    "v_mfma_f32_16x16x32_f16 v[48:51], v[60:63], v[28:31], v[48:51]\n"
    "ds_read_b128 v[24:27], v2 offset:3264\n"
    "v_mfma_f32_16x16x32_f16 v[52:55], v[92:95], v[28:31], v[52:55]\n"
    "v_mfma_f32_16x16x32_f16 v[56:59], v[124:127], v[28:31], 0\n"
    "s_waitcnt lgkmcnt(4)\n"
    "v_mfma_f32_16x16x32_f16 v[52:55], v[88:91], v[32:35], v[52:55]\n"
    "ds_read_b128 v[28:31], v2 offset:3200\n"
    "v_mfma_f32_16x16x32_f16 v[56:59], v[120:123], v[32:35], v[56:59]\n"
    "s_waitcnt lgkmcnt(4)\n"
    "v_mfma_f32_16x16x32_f16 v[52:55], v[84:87], v[36:39], v[52:55]\n"
    "ds_read_b128 v[32:35], v2 offset:3136\n"
    "v_mfma_f32_16x16x32_f16 v[56:59], v[116:119], v[36:39], v[56:59]\n"
    "ds_write_b64 v4, v[48:49] offset:0\n"
    "s_waitcnt lgkmcnt(5)\n"
    "v_mfma_f32_16x16x32_f16 v[52:55], v[80:83], v[40:43], v[52:55]\n"
    "ds_read_b128 v[36:39], v2 offset:3072\n"
    "v_mfma_f32_16x16x32_f16 v[56:59], v[112:115], v[40:43], v[56:59]\n"
    "ds_write_b64 v4, v[50:51] offset:640\n"
    "s_waitcnt lgkmcnt(6)\n"
    "v_mfma_f32_16x16x32_f16 v[52:55], v[76:79], v[44:47], v[52:55]\n"
    "ds_read_b128 v[40:43], v2 offset:3008\n"
    "v_mfma_f32_16x16x32_f16 v[56:59], v[108:111], v[44:47], v[56:59]\n"
    "ds_write_b8 v5, v8 offset:64\n"
    "s_waitcnt lgkmcnt(7)\n"
    "v_mfma_f32_16x16x32_f16 v[52:55], v[72:75], v[24:27], v[52:55]\n"
    "ds_read_b128 v[44:47], v2 offset:2944\n"
    "v_mfma_f32_16x16x32_f16 v[56:59], v[104:107], v[24:27], v[56:59]\n"
    "s_waitcnt lgkmcnt(7)\n"
    "v_mfma_f32_16x16x32_f16 v[52:55], v[68:71], v[28:31], v[52:55]\n"
    "ds_read_b128 v[24:27], v2 offset:2880\n"
    "v_mfma_f32_16x16x32_f16 v[56:59], v[100:103], v[28:31], v[56:59]\n"
    "s_waitcnt lgkmcnt(7)\n"
    "v_mfma_f32_16x16x32_f16 v[52:55], v[64:67], v[32:35], v[52:55]\n"
    "ds_read_b128 v[28:31], v2 offset:2816\n"
    "v_mfma_f32_16x16x32_f16 v[56:59], v[96:99], v[32:35], v[56:59]\n"
    "s_waitcnt lgkmcnt(6)\n"
    "v_mfma_f32_16x16x32_f16 v[52:55], v[60:63], v[36:39], v[52:55]\n"
    "ds_read_b128 v[32:35], v2 offset:2752\n"
    "v_mfma_f32_16x16x32_f16 v[56:59], v[92:95], v[36:39], v[56:59]\n"
    "v_mfma_f32_16x16x32_f16 v[48:51], v[124:127], v[36:39], 0\n"
    "s_waitcnt lgkmcnt(5)\n"
    "v_mfma_f32_16x16x32_f16 v[56:59], v[88:91], v[40:43], v[56:59]\n"
    "ds_read_b128 v[36:39], v2 offset:2688\n"
    "v_mfma_f32_16x16x32_f16 v[48:51], v[120:123], v[40:43], v[48:51]\n"
    "ds_read_b32 v11, v10 offset:44\n"
    "s_waitcnt lgkmcnt(5)\n"
    "v_mfma_f32_16x16x32_f16 v[56:59], v[84:87], v[44:47], v[56:59]\n"
    "ds_read_b128 v[40:43], v2 offset:2624\n"
    "v_mfma_f32_16x16x32_f16 v[48:51], v[116:119], v[44:47], v[48:51]\n"
    "ds_write_b64 v4, v[52:53] offset:1280\n"
    "s_waitcnt lgkmcnt(6)\n"
    "v_mfma_f32_16x16x32_f16 v[56:59], v[80:83], v[24:27], v[56:59]\n"
    "ds_read_b128 v[44:47], v2 offset:2560\n"
    "v_mfma_f32_16x16x32_f16 v[48:51], v[112:115], v[24:27], v[48:51]\n"
    "ds_write_b64 v4, v[54:55] offset:1920\n"
    "s_waitcnt lgkmcnt(7)\n"
    "v_mfma_f32_16x16x32_f16 v[56:59], v[76:79], v[28:31], v[56:59]\n"
    "ds_read_b128 v[24:27], v2 offset:2496\n"
    "v_mfma_f32_16x16x32_f16 v[48:51], v[108:111], v[28:31], v[48:51]\n"
    "ds_write_b8 v5, v8 offset:72\n"
    "s_waitcnt lgkmcnt(6)\n"
    "v_cmp_ne_u32 vcc, 1, v11\n"
    "v_mfma_f32_16x16x32_f16 v[56:59], v[72:75], v[32:35], v[56:59]\n"
    "ds_read_b128 v[28:31], v2 offset:2432\n"
    "v_mfma_f32_16x16x32_f16 v[48:51], v[104:107], v[32:35], v[48:51]\n"
    "s_cbranch_vccnz NEARW\n"
    "NEARC:\n"
    "ds_read_b64 v[52:53], v4 offset:5120\n"
    "ds_read_b64 v[54:55], v4 offset:5760\n"
    "v_mfma_f32_16x16x32_f16 v[56:59], v[68:71], v[36:39], v[56:59]\n"
    "ds_read_b128 v[32:35], v2 offset:2368\n"
    "v_mfma_f32_16x16x32_f16 v[48:51], v[100:103], v[36:39], v[48:51]\n"
    "s_waitcnt lgkmcnt(9)\n"
    "v_mfma_f32_16x16x32_f16 v[56:59], v[64:67], v[40:43], v[56:59]\n"
    "ds_read_b128 v[36:39], v2 offset:2304\n"
    "v_mfma_f32_16x16x32_f16 v[48:51], v[96:99], v[40:43], v[48:51]\n"
    "s_waitcnt lgkmcnt(8)\n"
    "v_mfma_f32_16x16x32_f16 v[56:59], v[60:63], v[44:47], v[56:59]\n"
    "ds_read_b128 v[40:43], v2 offset:2240\n"
    "v_mfma_f32_16x16x32_f16 v[48:51], v[92:95], v[44:47], v[48:51]\n"
    "s_waitcnt lgkmcnt(3)\n"
    "v_mfma_f32_16x16x32_f16 v[52:55], v[124:127], v[44:47], v[52:55]\n"
    "v_mfma_f32_16x16x32_f16 v[48:51], v[88:91], v[24:27], v[48:51]\n"
    "ds_read_b128 v[44:47], v2 offset:2176\n"
    "v_mfma_f32_16x16x32_f16 v[52:55], v[120:123], v[24:27], v[52:55]\n"
    "v_mfma_f32_16x16x32_f16 v[48:51], v[84:87], v[28:31], v[48:51]\n"
    "ds_read_b128 v[24:27], v2 offset:2112\n"
    "v_mfma_f32_16x16x32_f16 v[52:55], v[116:119], v[28:31], v[52:55]\n"
    "ds_write_b64 v4, v[56:57] offset:2560\n"
    "s_waitcnt lgkmcnt(5)\n"
    "v_mfma_f32_16x16x32_f16 v[48:51], v[80:83], v[32:35], v[48:51]\n"
    "ds_read_b128 v[28:31], v2 offset:2048\n"
    "v_mfma_f32_16x16x32_f16 v[52:55], v[112:115], v[32:35], v[52:55]\n"
    "ds_write_b64 v4, v[58:59] offset:3200\n"
    "s_waitcnt lgkmcnt(6)\n"
    "v_mfma_f32_16x16x32_f16 v[48:51], v[76:79], v[36:39], v[48:51]\n"
    "ds_read_b128 v[32:35], v2 offset:1984\n"
    "v_mfma_f32_16x16x32_f16 v[52:55], v[108:111], v[36:39], v[52:55]\n"
    "ds_write_b8 v5, v8 offset:80\n"
    "s_waitcnt lgkmcnt(7)\n"
    "v_mfma_f32_16x16x32_f16 v[48:51], v[72:75], v[40:43], v[48:51]\n"
    "ds_read_b128 v[36:39], v2 offset:1920\n"
    "v_mfma_f32_16x16x32_f16 v[52:55], v[104:107], v[40:43], v[52:55]\n"
    "ds_read_b64 v[56:57], v4 offset:6400\n"
    "ds_read_b64 v[58:59], v4 offset:7040\n"
    "s_waitcnt lgkmcnt(9)\n"
    "v_mfma_f32_16x16x32_f16 v[48:51], v[68:71], v[44:47], v[48:51]\n"
    "ds_read_b128 v[40:43], v2 offset:1856\n"
    "v_mfma_f32_16x16x32_f16 v[52:55], v[100:103], v[44:47], v[52:55]\n"
    "s_waitcnt lgkmcnt(9)\n"
    "v_mfma_f32_16x16x32_f16 v[48:51], v[64:67], v[24:27], v[48:51]\n"
    "ds_read_b128 v[44:47], v2 offset:1792\n"
    "v_mfma_f32_16x16x32_f16 v[52:55], v[96:99], v[24:27], v[52:55]\n"
    "s_waitcnt lgkmcnt(8)\n"
    "v_mfma_f32_16x16x32_f16 v[48:51], v[60:63], v[28:31], v[48:51]\n"
    "ds_read_b128 v[24:27], v2 offset:1728\n"
    "v_mfma_f32_16x16x32_f16 v[52:55], v[92:95], v[28:31], v[52:55]\n"
    "s_waitcnt lgkmcnt(3)\n"
    "v_mfma_f32_16x16x32_f16 v[56:59], v[124:127], v[28:31], v[56:59]\n"
    "v_mfma_f32_16x16x32_f16 v[52:55], v[88:91], v[32:35], v[52:55]\n"
    "ds_read_b128 v[28:31], v2 offset:1664\n"
    "v_mfma_f32_16x16x32_f16 v[56:59], v[120:123], v[32:35], v[56:59]\n"
    "v_mfma_f32_16x16x32_f16 v[52:55], v[84:87], v[36:39], v[52:55]\n"
    "ds_read_b128 v[32:35], v2 offset:1600\n"
    "v_mfma_f32_16x16x32_f16 v[56:59], v[116:119], v[36:39], v[56:59]\n"
    "ds_write_b64 v4, v[48:49] offset:3840\n"
    "s_waitcnt lgkmcnt(5)\n"
    "v_mfma_f32_16x16x32_f16 v[52:55], v[80:83], v[40:43], v[52:55]\n"
    "ds_read_b128 v[36:39], v2 offset:1536\n"
    "v_mfma_f32_16x16x32_f16 v[56:59], v[112:115], v[40:43], v[56:59]\n"
    "ds_write_b64 v4, v[50:51] offset:4480\n"
    "s_waitcnt lgkmcnt(6)\n"
    "v_mfma_f32_16x16x32_f16 v[52:55], v[76:79], v[44:47], v[52:55]\n"
    "ds_read_b128 v[40:43], v2 offset:1472\n"
    "v_mfma_f32_16x16x32_f16 v[56:59], v[108:111], v[44:47], v[56:59]\n"
    "ds_write_b8 v5, v8 offset:88\n"
    "s_waitcnt lgkmcnt(7)\n"
    "v_mfma_f32_16x16x32_f16 v[52:55], v[72:75], v[24:27], v[52:55]\n"
    "ds_read_b128 v[44:47], v2 offset:1408\n"
    "v_mfma_f32_16x16x32_f16 v[56:59], v[104:107], v[24:27], v[56:59]\n"
    "ds_read_b64 v[48:49], v4 offset:7680\n"
    "ds_read_b64 v[50:51], v4 offset:8320\n"
    "s_waitcnt lgkmcnt(9)\n"
    "v_mfma_f32_16x16x32_f16 v[52:55], v[68:71], v[28:31], v[52:55]\n"
    "ds_read_b128 v[24:27], v2 offset:1344\n"
    "v_mfma_f32_16x16x32_f16 v[56:59], v[100:103], v[28:31], v[56:59]\n"
    "s_waitcnt lgkmcnt(9)\n"
    "v_mfma_f32_16x16x32_f16 v[52:55], v[64:67], v[32:35], v[52:55]\n"
    "ds_read_b128 v[28:31], v2 offset:1280\n"
    "v_mfma_f32_16x16x32_f16 v[56:59], v[96:99], v[32:35], v[56:59]\n"
    "s_waitcnt lgkmcnt(8)\n"
    "v_mfma_f32_16x16x32_f16 v[52:55], v[60:63], v[36:39], v[52:55]\n"
    "ds_read_b128 v[32:35], v2 offset:1216\n"
    "v_mfma_f32_16x16x32_f16 v[56:59], v[92:95], v[36:39], v[56:59]\n"
    "s_waitcnt lgkmcnt(3)\n"
    "v_mfma_f32_16x16x32_f16 v[48:51], v[124:127], v[36:39], v[48:51]\n"
    "v_mfma_f32_16x16x32_f16 v[56:59], v[88:91], v[40:43], v[56:59]\n"
    "ds_read_b128 v[36:39], v2 offset:1152\n"
    "v_mfma_f32_16x16x32_f16 v[48:51], v[120:123], v[40:43], v[48:51]\n"
    "v_mfma_f32_16x16x32_f16 v[56:59], v[84:87], v[44:47], v[56:59]\n"
    "ds_read_b128 v[40:43], v2 offset:1088\n"
    "v_mfma_f32_16x16x32_f16 v[48:51], v[116:119], v[44:47], v[48:51]\n"
    "ds_write_b64 v4, v[52:53] offset:5120\n"
    "s_waitcnt lgkmcnt(5)\n"
    "v_mfma_f32_16x16x32_f16 v[56:59], v[80:83], v[24:27], v[56:59]\n"
    "ds_read_b128 v[44:47], v2 offset:1024\n"
    "v_mfma_f32_16x16x32_f16 v[48:51], v[112:115], v[24:27], v[48:51]\n"
    "ds_write_b64 v4, v[54:55] offset:5760\n"
    "s_waitcnt lgkmcnt(6)\n"
    "v_mfma_f32_16x16x32_f16 v[56:59], v[76:79], v[28:31], v[56:59]\n"
    "ds_read_b128 v[24:27], v2 offset:960\n"
    "v_mfma_f32_16x16x32_f16 v[48:51], v[108:111], v[28:31], v[48:51]\n"
    "ds_write_b8 v5, v8 offset:96\n"
    "s_waitcnt lgkmcnt(7)\n"
    "v_mfma_f32_16x16x32_f16 v[56:59], v[72:75], v[32:35], v[56:59]\n"
    "ds_read_b128 v[28:31], v2 offset:896\n"
    "v_mfma_f32_16x16x32_f16 v[48:51], v[104:107], v[32:35], v[48:51]\n"
    "ds_read_b64 v[52:53], v4 offset:8960\n"
    "ds_read_b64 v[54:55], v4 offset:9600\n"
    "s_waitcnt lgkmcnt(9)\n"
    "v_mfma_f32_16x16x32_f16 v[56:59], v[68:71], v[36:39], v[56:59]\n"
    "ds_read_b128 v[32:35], v2 offset:832\n"
    "v_mfma_f32_16x16x32_f16 v[48:51], v[100:103], v[36:39], v[48:51]\n"
    "s_waitcnt lgkmcnt(9)\n"
    "v_mfma_f32_16x16x32_f16 v[56:59], v[64:67], v[40:43], v[56:59]\n"
    "ds_read_b128 v[36:39], v2 offset:768\n"
    "v_mfma_f32_16x16x32_f16 v[48:51], v[96:99], v[40:43], v[48:51]\n"
    "s_waitcnt lgkmcnt(8)\n"
    "v_mfma_f32_16x16x32_f16 v[56:59], v[60:63], v[44:47], v[56:59]\n"
    "ds_read_b128 v[40:43], v2 offset:704\n"
    "v_mfma_f32_16x16x32_f16 v[48:51], v[92:95], v[44:47], v[48:51]\n"
    "s_waitcnt lgkmcnt(3)\n"
    "v_mfma_f32_16x16x32_f16 v[52:55], v[124:127], v[44:47], v[52:55]\n"
    "v_mfma_f32_16x16x32_f16 v[48:51], v[88:91], v[24:27], v[48:51]\n"
    "ds_read_b128 v[44:47], v2 offset:640\n"
    "v_mfma_f32_16x16x32_f16 v[52:55], v[120:123], v[24:27], v[52:55]\n"
    "v_mfma_f32_16x16x32_f16 v[48:51], v[84:87], v[28:31], v[48:51]\n"
    "ds_read_b128 v[24:27], v2 offset:576\n"
    "v_mfma_f32_16x16x32_f16 v[52:55], v[116:119], v[28:31], v[52:55]\n"
    "ds_write_b64 v4, v[56:57] offset:6400\n"
    "s_waitcnt lgkmcnt(5)\n"
    "v_mfma_f32_16x16x32_f16 v[48:51], v[80:83], v[32:35], v[48:51]\n"
    "ds_read_b128 v[28:31], v2 offset:512\n"
    "v_mfma_f32_16x16x32_f16 v[52:55], v[112:115], v[32:35], v[52:55]\n"
    "ds_write_b64 v4, v[58:59] offset:7040\n"
    "ds_write_b8 v5, v8 offset:104\n"
    "s_waitcnt lgkmcnt(7)\n"
    "v_mfma_f32_16x16x32_f16 v[48:51], v[76:79], v[36:39], v[48:51]\n"
    "ds_read_b128 v[32:35], v2 offset:448\n"
    "v_mfma_f32_16x16x32_f16 v[52:55], v[108:111], v[36:39], v[52:55]\n"
    "s_waitcnt lgkmcnt(7)\n"
    "v_mfma_f32_16x16x32_f16 v[48:51], v[72:75], v[40:43], v[48:51]\n"
    "ds_read_b128 v[36:39], v2 offset:384\n"
    "v_mfma_f32_16x16x32_f16 v[52:55], v[104:107], v[40:43], v[52:55]\n"
    "s_waitcnt lgkmcnt(7)\n"
    "v_mfma_f32_16x16x32_f16 v[48:51], v[68:71], v[44:47], v[48:51]\n"
    "ds_read_b128 v[40:43], v2 offset:320\n"
    "v_mfma_f32_16x16x32_f16 v[52:55], v[100:103], v[44:47], v[52:55]\n"
    "s_waitcnt lgkmcnt(7)\n"
    "v_mfma_f32_16x16x32_f16 v[48:51], v[64:67], v[24:27], v[48:51]\n"
    "ds_read_b128 v[44:47], v2 offset:256\n"
    "v_mfma_f32_16x16x32_f16 v[52:55], v[96:99], v[24:27], v[52:55]\n"
    "s_waitcnt lgkmcnt(6)\n"
    "v_mfma_f32_16x16x32_f16 v[48:51], v[60:63], v[28:31], v[48:51]\n"
    "ds_read_b128 v[24:27], v2 offset:192\n"
    "v_mfma_f32_16x16x32_f16 v[52:55], v[92:95], v[28:31], v[52:55]\n"
    "s_waitcnt lgkmcnt(4)\n"
    "v_mfma_f32_16x16x32_f16 v[52:55], v[88:91], v[32:35], v[52:55]\n"
    "ds_read_b128 v[28:31], v2 offset:128\n"
    "s_waitcnt lgkmcnt(4)\n"
    "v_mfma_f32_16x16x32_f16 v[52:55], v[84:87], v[36:39], v[52:55]\n"
    "ds_read_b128 v[32:35], v2 offset:64\n"
    "ds_write_b64 v4, v[48:49] offset:7680\n"
    "s_waitcnt lgkmcnt(5)\n"
    "v_mfma_f32_16x16x32_f16 v[52:55], v[80:83], v[40:43], v[52:55]\n"
    "ds_read_b128 v[36:39], v2 offset:0\n"
    "ds_write_b64 v4, v[50:51] offset:8320\n"
    "ds_write_b8 v5, v8 offset:112\n"
    "s_waitcnt lgkmcnt(7)\n"
    "v_mfma_f32_16x16x32_f16 v[52:55], v[76:79], v[44:47], v[52:55]\n"
    "s_waitcnt lgkmcnt(6)\n"
    "v_mfma_f32_16x16x32_f16 v[52:55], v[72:75], v[24:27], v[52:55]\n"
    "s_waitcnt lgkmcnt(5)\n"
    "v_mfma_f32_16x16x32_f16 v[52:55], v[68:71], v[28:31], v[52:55]\n"
    "s_waitcnt lgkmcnt(4)\n"
    "v_mfma_f32_16x16x32_f16 v[52:55], v[64:67], v[32:35], v[52:55]\n"
    "s_waitcnt lgkmcnt(2)\n"
    "v_mfma_f32_16x16x32_f16 v[52:55], v[60:63], v[36:39], v[52:55]\n"
    "s_nop 7\n"
    "ds_write_b64 v4, v[52:53] offset:8960\n"
    "ds_write_b64 v4, v[54:55] offset:9600\n"
    "ds_write_b8 v5, v8 offset:120\n"
    "s_setprio 0\n"
    "s_branch LN2\n"
    "NEARW:\n"
    "s_branch E_24\n"
    "R_23:\n"
    "s_sleep 1\n"
    "s_sub_u32 s39, s39, 1\n"
    "s_cbranch_scc1 ABORT\n"
    "E_24:\n"
    "ds_read_b32 v11, v10 offset:44\n"
    "s_waitcnt lgkmcnt(0)\n"
    "v_cmp_ne_u32 vcc, 1, v11\n"
    "s_cbranch_vccnz R_23\n"
    "s_branch NEARC\n"
    "EPS:\n"
    "v_mov_b32 v14, 147968\n"
    "v_mov_b32 v15, 1\n"
    "v_bfe_u32 v2, v0, 3, 3\n"
    "v_bfe_u32 v3, v0, 1, 2\n"
    "v_mul_u32_u24 v3, 160, v3\n"
    "v_lshl_add_u32 v2, v2, 3, v3\n"
    "v_and_b32 v3, 1, v0\n"
    "s_movk_i32 s43, 640\n"
    "v_mad_u32_u24 v2, v3, s43, v2\n"
    "s_mul_i32 s43, s38, 16512\n"
    "s_add_u32 s43, s43, 131072\n"
    "v_and_b32 v3, 63, v0\n"
    "v_lshl_add_u32 v1, v3, 4, s43\n"
    "v_mov_b32 v43, s36\n"
    "s_sub_u32 s57, 15, s37\n"
    "v_add_u32 v36, 1792, v86\n"
    "s_lshl_b32 s49, s57, 10\n"
    "s_lshr_b32 s46, s57, 1\n"
    "s_mul_i32 s43, s46, 1280\n"
    "s_and_b32 s47, s57, 1\n"
    "s_lshl_b32 s47, s47, 6\n"
    "s_add_u32 s43, s43, s47\n"
    "v_add_u32 v30, s43, v2\n"
    "s_lshl_b32 s46, s46, 3\n"
    "v_add_u32 v33, s46, v14\n"
    "s_lshl_b32 s50, s54, 10\n"
    "s_lshr_b32 s46, s54, 1\n"
    "s_mul_i32 s43, s46, 1280\n"
    "s_and_b32 s47, s54, 1\n"
    "s_lshl_b32 s47, s47, 6\n"
    "s_add_u32 s43, s43, s47\n"
    "v_add_u32 v31, s43, v2\n"
    "s_lshl_b32 s46, s46, 3\n"
    "v_add_u32 v34, s46, v14\n"
    "s_lshl_b32 s51, s55, 10\n"
    "s_lshr_b32 s46, s55, 1\n"
    "s_mul_i32 s43, s46, 1280\n"
    "s_and_b32 s47, s55, 1\n"
    "s_lshl_b32 s47, s47, 6\n"
    "s_add_u32 s43, s43, s47\n"
    "v_add_u32 v32, s43, v2\n"
    "s_lshl_b32 s46, s46, 3\n"
    "v_add_u32 v35, s46, v14\n"
    "s_mov_b32 s46, 0x01010101\n"
    "s_movk_i32 s47, 0x0101\n"
    "s_and_b32 s42, s38, 1\n"
    "s_lshl_b32 s42, s42, 4\n"
    "s_waitcnt lgkmcnt(0)\n"
    "s_lshr_b64 s[52:53], s[52:53], s42\n"
    "s_lshl_b32 s44, s52, 16\n"
    "s_and_b32 s45, s52, 0xffff0000\n"
    "s_lshl_b32 s48, s53, 16\n"
    "s_branch STG0\n"
    "EPI:\n"
    "s_setprio 2\n"
    "s_waitcnt lgkmcnt(0)\n"
    "v_mul_f32 v120, v121, v114\n"
    "v_mul_f32 v114, s44, v114\n"
    "v_mul_f32 v122, v123, v116\n"
    "v_mul_f32 v116, s45, v116\n"
    "v_mul_f32 v124, v125, v118\n"
    "v_mul_f32 v118, s48, v118\n"
    "v_fma_mix_f32 v70, v94, v114, 0 op_sel:[1,0,0] op_sel_hi:[1,0,0]\n"
    "v_fma_mix_f32 v71, v94, v114, 0 op_sel_hi:[1,0,0]\n"
    "v_fma_mix_f32 v72, v95, v116, 0 op_sel:[1,0,0] op_sel_hi:[1,0,0]\n"
    "v_fma_mix_f32 v73, v95, v116, 0 op_sel_hi:[1,0,0]\n"
    "v_fma_mix_f32 v74, v96, v118, 0 op_sel:[1,0,0] op_sel_hi:[1,0,0]\n"
    "v_fma_mix_f32 v75, v96, v118, 0 op_sel_hi:[1,0,0]\n"
    "v_cvt_pk_bf16_f32 v70, 0, v70\n"
    "v_cvt_pk_bf16_f32 v71, 0, v71\n"
    "v_cvt_pk_bf16_f32 v72, 0, v72\n"
    "v_cvt_pk_bf16_f32 v73, 0, v73\n"
    "v_cvt_pk_bf16_f32 v74, 0, v74\n"
    "v_cvt_pk_bf16_f32 v75, 0, v75\n"
    "v_fma_mix_f32 v76, v97, v114, 0 op_sel:[1,0,0] op_sel_hi:[1,0,0]\n"
    "v_fma_mix_f32 v77, v97, v114, 0 op_sel_hi:[1,0,0]\n"
    "v_fma_mix_f32 v78, v98, v116, 0 op_sel:[1,0,0] op_sel_hi:[1,0,0]\n"
    "v_fma_mix_f32 v79, v98, v116, 0 op_sel_hi:[1,0,0]\n"
    "v_fma_mix_f32 v80, v99, v118, 0 op_sel:[1,0,0] op_sel_hi:[1,0,0]\n"
    "v_fma_mix_f32 v81, v99, v118, 0 op_sel_hi:[1,0,0]\n"
    "v_cvt_pk_bf16_f32 v76, 0, v76\n"
    "v_cvt_pk_bf16_f32 v77, 0, v77\n"
    "v_cvt_pk_bf16_f32 v78, 0, v78\n"
    "v_cvt_pk_bf16_f32 v79, 0, v79\n"
    "v_cvt_pk_bf16_f32 v80, 0, v80\n"
    "v_cvt_pk_bf16_f32 v81, 0, v81\n"
    "s_cmpk_lt_u32 s37, 14\n"
    "s_cbranch_scc1 EP1\n"
    "ds_read_b32 v24, v36 offset:0\n"
    "ds_read_b32 v25, v36 offset:4576\n"
    "ds_read_b32 v26, v36 offset:9152\n"
    "s_waitcnt lgkmcnt(0)\n"
    "v_fma_mix_f32 v64, v24, v114, 0 op_sel:[1,0,0] op_sel_hi:[1,0,0]\n"
    "v_fma_mix_f32 v65, v24, v114, 0 op_sel_hi:[1,0,0]\n"
    "v_fma_mix_f32 v66, v25, v116, 0 op_sel:[1,0,0] op_sel_hi:[1,0,0]\n"
    "v_fma_mix_f32 v67, v25, v116, 0 op_sel_hi:[1,0,0]\n"
    "v_fma_mix_f32 v68, v26, v118, 0 op_sel:[1,0,0] op_sel_hi:[1,0,0]\n"
    "v_fma_mix_f32 v69, v26, v118, 0 op_sel_hi:[1,0,0]\n"
    "v_cvt_pk_bf16_f32 v64, 0, v64\n"
    "v_cvt_pk_bf16_f32 v65, 0, v65\n"
    "v_cvt_pk_bf16_f32 v66, 0, v66\n"
    "v_cvt_pk_bf16_f32 v67, 0, v67\n"
    "v_cvt_pk_bf16_f32 v68, 0, v68\n"
    "v_cvt_pk_bf16_f32 v69, 0, v69\n"
    "s_branch E_26\n"
    "R_25:\n"
    "s_sleep 1\n"
    "s_sub_u32 s39, s39, 1\n"
    "s_cbranch_scc1 ABORT\n"
    "E_26:\n"
    "ds_read_b32 v22, v33 offset:64\n"
    "s_waitcnt lgkmcnt(0)\n"
    "v_cmp_ne_u32 vcc, 0x010101, v22\n"
    "s_cbranch_vccnz R_25\n"
    "ds_read_b64 v[48:49], v30 offset:0\n"
    "ds_read_b64 v[50:51], v30 offset:10240\n"
    "ds_read_b64 v[52:53], v30 offset:20480\n"
    "s_waitcnt lgkmcnt(0)\n"
    "v_cvt_pk_bf16_f32 v48, 0, v48\n"
    "v_cvt_pk_bf16_f32 v49, 0, v49\n"
    "v_cvt_pk_bf16_f32 v50, 0, v50\n"
    "v_cvt_pk_bf16_f32 v51, 0, v51\n"
    "v_cvt_pk_bf16_f32 v52, 0, v52\n"
    "v_cvt_pk_bf16_f32 v53, 0, v53\n"
    "v_fma_f32 v48, v48, v120, v64\n"
    "v_fma_f32 v49, v49, v120, v65\n"
    "v_fma_f32 v50, v50, v122, v66\n"
    "v_fma_f32 v51, v51, v122, v67\n"
    "v_fma_f32 v52, v52, v124, v68\n"
    "v_fma_f32 v53, v53, v124, v69\n"
    "v_cvt_pk_bf16_f32 v48, 0, v48\n"
    "v_cvt_pk_bf16_f32 v49, 0, v49\n"
    "v_cvt_pk_bf16_f32 v50, 0, v50\n"
    "v_cvt_pk_bf16_f32 v51, 0, v51\n"
    "v_cvt_pk_bf16_f32 v52, 0, v52\n"
    "v_cvt_pk_bf16_f32 v53, 0, v53\n"
    "v_mul_f32 v48, v48, v115\n"
    "v_mul_f32 v49, v49, v115\n"
    "v_mul_f32 v50, v50, v117\n"
    "v_mul_f32 v51, v51, v117\n"
    "v_mul_f32 v52, v52, v119\n"
    "v_mul_f32 v53, v53, v119\n"
    "v_cvt_pk_bf16_f32 v40, v48, v49\n"
    "v_cvt_pk_bf16_f32 v41, v50, v51\n"
    "v_cvt_pk_bf16_f32 v42, v52, v53\n"
    "buffer_store_dwordx4 v[40:43], v1, s[20:23], s49 offen sc1\n"
    "s_mov_b64 exec, 1\n"
    "ds_add_u32 v14, v15 offset:60\n"
    "s_mov_b64 exec, -1\n"
    "EP1:\n"
    "s_cmpk_ge_u32 s37, 2\n"
    "s_cbranch_scc1 EP1F\n"
    "s_branch E_28\n"
    "R_27:\n"
    "s_sleep 1\n"
    "s_sub_u32 s39, s39, 1\n"
    "s_cbranch_scc1 ABORT\n"
    "E_28:\n"
    "ds_read_b32 v22, v34 offset:64\n"
    "s_waitcnt lgkmcnt(0)\n"
    "v_cmp_ne_u32 vcc, 0x010101, v22\n"
    "s_cbranch_vccnz R_27\n"
    "ds_read_b64 v[48:49], v31 offset:0\n"
    "ds_read_b64 v[50:51], v31 offset:10240\n"
    "ds_read_b64 v[52:53], v31 offset:20480\n"
    "s_waitcnt lgkmcnt(0)\n"
    "s_branch EP1M\n"
    "EP1F:\n"
    "s_branch E_30\n"
    "R_29:\n"
    "s_sleep 1\n"
    "s_sub_u32 s39, s39, 1\n"
    "s_cbranch_scc1 ABORT\n"
    "E_30:\n"
    "ds_read_b64 v[22:23], v34 offset:64\n"
    "s_waitcnt lgkmcnt(0)\n"
    "v_cmp_ne_u64 vcc, s[46:47], v[22:23]\n"
    "s_cbranch_vccnz R_29\n"
    "ds_read_b64 v[48:49], v31 offset:0\n"
    "ds_read_b64 v[54:55], v31 offset:28160\n"
    "ds_read_b64 v[50:51], v31 offset:10240\n"
    "ds_read_b64 v[56:57], v31 offset:35840\n"
    "ds_read_b64 v[52:53], v31 offset:20480\n"
    "ds_read_b64 v[58:59], v31 offset:43520\n"
    "s_waitcnt lgkmcnt(0)\n"
    "v_add_f32 v48, v48, v54\n"
    "v_add_f32 v49, v49, v55\n"
    "v_add_f32 v50, v50, v56\n"
    "v_add_f32 v51, v51, v57\n"
    "v_add_f32 v52, v52, v58\n"
    "v_add_f32 v53, v53, v59\n"
    "EP1M:\n"
    "v_cvt_pk_bf16_f32 v48, 0, v48\n"
    "v_cvt_pk_bf16_f32 v49, 0, v49\n"
    "v_cvt_pk_bf16_f32 v50, 0, v50\n"
    "v_cvt_pk_bf16_f32 v51, 0, v51\n"
    "v_cvt_pk_bf16_f32 v52, 0, v52\n"
    "v_cvt_pk_bf16_f32 v53, 0, v53\n"
    "v_fma_f32 v48, v48, v120, v70\n"
    "v_fma_f32 v49, v49, v120, v71\n"
    "v_fma_f32 v50, v50, v122, v72\n"
    "v_fma_f32 v51, v51, v122, v73\n"
    "v_fma_f32 v52, v52, v124, v74\n"
    "v_fma_f32 v53, v53, v124, v75\n"
    "v_cvt_pk_bf16_f32 v48, 0, v48\n"
    "v_cvt_pk_bf16_f32 v49, 0, v49\n"
    "v_cvt_pk_bf16_f32 v50, 0, v50\n"
    "v_cvt_pk_bf16_f32 v51, 0, v51\n"
    "v_cvt_pk_bf16_f32 v52, 0, v52\n"
    "v_cvt_pk_bf16_f32 v53, 0, v53\n"
    "v_mul_f32 v48, v48, v115\n"
    "v_mul_f32 v49, v49, v115\n"
    "v_mul_f32 v50, v50, v117\n"
    "v_mul_f32 v51, v51, v117\n"
    "v_mul_f32 v52, v52, v119\n"
    "v_mul_f32 v53, v53, v119\n"
    "v_cvt_pk_bf16_f32 v40, v48, v49\n"
    "v_cvt_pk_bf16_f32 v41, v50, v51\n"
    "v_cvt_pk_bf16_f32 v42, v52, v53\n"
    "buffer_store_dwordx4 v[40:43], v1, s[20:23], s50 offen sc1\n"
    "s_mov_b64 exec, 1\n"
    "ds_add_u32 v14, v15 offset:60\n"
    "s_mov_b64 exec, -1\n"
    "s_cmpk_ge_u32 s37, 12\n"
    "s_cbranch_scc1 EP2\n"
    "s_waitcnt vmcnt(1)\n"
    "s_mov_b64 exec, 1\n"
    "ds_add_u32 v14, v15 offset:56\n"
    "s_mov_b64 exec, -1\n"
    "EP2:\n"
    "s_cmpk_lg_u32 s37, 13\n"
    "s_cbranch_scc1 EP3\n"
    "s_memrealtime s[98:99]\n"
    "s_lshl_b32 s42, s2, 7\n"
    "s_add_u32 s42, s42, 94208\n"
    "v_mov_b32 v45, v43\n"
    "s_waitcnt lgkmcnt(0)\n"
    "v_mov_b32 v44, s98\n"
    "s_mov_b64 exec, 1\n"
    "buffer_store_dwordx2 v[44:45], off, s[20:23], s42 sc1\n"
    "s_mov_b64 exec, -1\n"
    "EP3:\n"
    "s_branch E_32\n"
    "R_31:\n"
    "s_sleep 1\n"
    "s_sub_u32 s39, s39, 1\n"
    "s_cbranch_scc1 ABORT\n"
    "E_32:\n"
    "ds_read_b64 v[22:23], v35 offset:56\n"
    "s_waitcnt lgkmcnt(0)\n"
    "v_cmp_ne_u64 vcc, s[46:47], v[22:23]\n"
    "s_cbranch_vccnz R_31\n"
    "s_branch E_34\n"
    "R_33:\n"
    "s_sub_u32 s39, s39, 1\n"
    "s_cbranch_scc1 ABORT\n"
    "E_34:\n"
    "ds_read_b64 v[22:23], v35 offset:64\n"
    "ds_read_b64 v[48:49], v32 offset:0\n"
    "ds_read_b64 v[54:55], v32 offset:28160\n"
    "ds_read_b64 v[50:51], v32 offset:10240\n"
    "ds_read_b64 v[56:57], v32 offset:35840\n"
    "ds_read_b64 v[52:53], v32 offset:20480\n"
    "ds_read_b64 v[58:59], v32 offset:43520\n"
    "s_waitcnt lgkmcnt(0)\n"
    "v_cmp_ne_u64 vcc, s[46:47], v[22:23]\n"
    "s_cbranch_vccnz R_33\n"
    "s_cmpk_ge_u32 s37, 14\n"
    "s_cbranch_scc1 EPZ\n"
    "v_add_f32 v48, v48, v54\n"
    "v_add_f32 v49, v49, v55\n"
    "v_add_f32 v50, v50, v56\n"
    "v_add_f32 v51, v51, v57\n"
    "v_add_f32 v52, v52, v58\n"
    "v_add_f32 v53, v53, v59\n"
    "v_cvt_pk_bf16_f32 v48, 0, v48\n"
    "v_cvt_pk_bf16_f32 v49, 0, v49\n"
    "v_cvt_pk_bf16_f32 v50, 0, v50\n"
    "v_cvt_pk_bf16_f32 v51, 0, v51\n"
    "v_cvt_pk_bf16_f32 v52, 0, v52\n"
    "v_cvt_pk_bf16_f32 v53, 0, v53\n"
    "v_fma_f32 v48, v48, v120, v76\n"
    "v_fma_f32 v49, v49, v120, v77\n"
    "v_fma_f32 v50, v50, v122, v78\n"
    "v_fma_f32 v51, v51, v122, v79\n"
    "v_fma_f32 v52, v52, v124, v80\n"
    "v_fma_f32 v53, v53, v124, v81\n"
    "v_cvt_pk_bf16_f32 v48, 0, v48\n"
    "v_cvt_pk_bf16_f32 v49, 0, v49\n"
    "v_cvt_pk_bf16_f32 v50, 0, v50\n"
    "v_cvt_pk_bf16_f32 v51, 0, v51\n"
    "v_cvt_pk_bf16_f32 v52, 0, v52\n"
    "v_cvt_pk_bf16_f32 v53, 0, v53\n"
    "v_mul_f32 v48, v48, v115\n"
    "v_mul_f32 v49, v49, v115\n"
    "v_mul_f32 v50, v50, v117\n"
    "v_mul_f32 v51, v51, v117\n"
    "v_mul_f32 v52, v52, v119\n"
    "v_mul_f32 v53, v53, v119\n"
    "v_cvt_pk_bf16_f32 v40, v48, v49\n"
    "v_cvt_pk_bf16_f32 v41, v50, v51\n"
    "v_cvt_pk_bf16_f32 v42, v52, v53\n"
    "buffer_store_dwordx4 v[40:43], v1, s[20:23], s51 offen sc1\n"
    "s_mov_b64 exec, 1\n"
    "ds_add_u32 v14, v15 offset:60\n"
    "s_mov_b64 exec, -1\n"
    "s_cmpk_ge_u32 s37, 12\n"
    "s_cbranch_scc1 LN2K\n"
    "s_setprio 0\n"
    "s_branch LN2\n"
    "EPZ:\n"
    "s_setprio 3\n"
    "v_pk_add_f32 v[48:49], v[48:49], v[54:55]\n"
    "v_pk_add_f32 v[50:51], v[50:51], v[56:57]\n"
    "v_pk_add_f32 v[52:53], v[52:53], v[58:59]\n"
    "v_cvt_pk_bf16_f32 v48, 0, v48\n"
    "v_cvt_pk_bf16_f32 v49, 0, v49\n"
    "v_cvt_pk_bf16_f32 v50, 0, v50\n"
    "v_cvt_pk_bf16_f32 v51, 0, v51\n"
    "v_cvt_pk_bf16_f32 v52, 0, v52\n"
    "v_cvt_pk_bf16_f32 v53, 0, v53\n"
    "v_pk_fma_f32 v[48:49], v[48:49], v[120:121], v[76:77] op_sel_hi:[1,0,1]\n"
    "v_pk_fma_f32 v[50:51], v[50:51], v[122:123], v[78:79] op_sel_hi:[1,0,1]\n"
    "v_pk_fma_f32 v[52:53], v[52:53], v[124:125], v[80:81] op_sel_hi:[1,0,1]\n"
    "v_cvt_pk_bf16_f32 v48, 0, v48\n"
    "v_cvt_pk_bf16_f32 v49, 0, v49\n"
    "v_cvt_pk_bf16_f32 v50, 0, v50\n"
    "v_cvt_pk_bf16_f32 v51, 0, v51\n"
    "v_cvt_pk_bf16_f32 v52, 0, v52\n"
    "v_cvt_pk_bf16_f32 v53, 0, v53\n"
    "v_pk_mul_f32 v[48:49], v[114:115], v[48:49] op_sel:[1,0] op_sel_hi:[1,1]\n"
    "v_pk_mul_f32 v[50:51], v[116:117], v[50:51] op_sel:[1,0] op_sel_hi:[1,1]\n"
    "v_pk_mul_f32 v[52:53], v[118:119], v[52:53] op_sel:[1,0] op_sel_hi:[1,1]\n"
    "v_cvt_pk_bf16_f32 v40, v48, v49\n"
    "v_cvt_pk_bf16_f32 v41, v50, v51\n"
    "v_cvt_pk_bf16_f32 v42, v52, v53\n"
    "buffer_store_dwordx4 v[40:43], v1, s[20:23], s51 offen sc1\n"
    "s_mov_b64 exec, 1\n"
    "ds_add_u32 v14, v15 offset:60\n"
    "s_mov_b64 exec, -1\n"
    "s_branch LN2K\n"
    "MISC:\n"
    "s_cmpk_ge_u32 s2, 128\n"
    "s_cbranch_scc1 CDONE\n"
    "v_and_b32 v109, 255, v0\n"
    "v_add_u32 v104, 192, v109\n"
    "v_min_u32 v104, 287, v104\n"
    "v_lshlrev_b32 v105, 2, v109\n"
    "v_lshlrev_b32 v106, 2, v104\n"
    "v_lshlrev_b32 v107, 4, v109\n"
    "v_lshlrev_b32 v108, 4, v104\n"
    "s_mov_b32 s44, 34304\n"
    "s_mov_b32 s45, 61952\n"
    "s_mov_b32 s46, 38912\n"
    "s_mov_b32 s47, 66560\n"
    "s_mov_b32 s48, 43520\n"
    "s_mov_b32 s49, 71168\n"
    "v_mov_b32 v14, 147968\n"
    "ds_read_b32 v4, v14 offset:480\n"
    "s_waitcnt lgkmcnt(0)\n"
    "v_readfirstlane_b32 s96, v4\n"
    "s_mov_b64 exec, -1\n"
    "global_load_dword v45, v105, s[26:27]\n"
    "global_load_dword v46, v106, s[26:27]\n"
    "s_setprio 1\n"
    "U_35:\n"
    "s_memrealtime s[98:99]\n"
    "s_waitcnt lgkmcnt(0)\n"
    "s_sub_u32 s98, s98, s96\n"
    "s_cmpk_lt_u32 s98, 300\n"
    "s_cbranch_scc1 U_35\n"
    "buffer_load_dwordx4 v[48:51], v107, s[20:23], s44 offen sc1\n"
    "buffer_load_dwordx4 v[52:55], v107, s[20:23], s45 offen sc1\n"
    "buffer_load_dwordx4 v[56:59], v107, s[20:23], s46 offen sc1\n"
    "buffer_load_dwordx4 v[60:63], v107, s[20:23], s47 offen sc1\n"
    "buffer_load_dwordx4 v[64:67], v107, s[20:23], s48 offen sc1\n"
    "buffer_load_dwordx4 v[68:71], v107, s[20:23], s49 offen sc1\n"
    "buffer_load_dwordx4 v[72:75], v108, s[20:23], s44 offen sc1\n"
    "buffer_load_dwordx4 v[76:79], v108, s[20:23], s45 offen sc1\n"
    "buffer_load_dwordx4 v[80:83], v108, s[20:23], s46 offen sc1\n"
    "buffer_load_dwordx4 v[84:87], v108, s[20:23], s47 offen sc1\n"
    "buffer_load_dwordx4 v[88:91], v108, s[20:23], s48 offen sc1\n"
    "buffer_load_dwordx4 v[92:95], v108, s[20:23], s49 offen sc1\n"
    "v_and_b32 v1, 63, v0\n"
    "v_lshlrev_b32 v1, 4, v1\n"
    "s_mov_b64 exec, -1\n"
    "v_mov_b32 v8, 0\n"
    "v_mov_b32 v9, 0\n"
    "v_mov_b32 v10, 0\n"
    "v_mov_b32 v11, 0\n"
    "v_lshrrev_b32 v2, 9, v1\n"
    "v_mul_u32_u24 v2, 1536, v2\n"
    "v_add_u32 v2, v2, v1\n"
    "v_add_u32 v2, 134048, v2\n"
    "ds_write_b128 v2, v[8:11]\n"
    "s_mov_b32 s42, 138240\n"
    "v_lshl_add_u32 v1, v107, 1, s42\n"
    "v_lshl_add_u32 v2, v108, 1, s42\n"
    "v_add_u32 v41, 72320, v105\n"
    "v_add_u32 v42, 72320, v106\n"
    "v_mov_b32 v15, 1\n"
    "v_mov_b32 v14, 147968\n"
    "s_branch E_37\n"
    "R_36:\n"
    "s_sub_u32 s39, s39, 1\n"
    "s_cbranch_scc1 ABORT\n"
    "s_sleep 4\n"
    "buffer_load_dwordx4 v[48:51], v107, s[20:23], s44 offen sc1\n"
    "buffer_load_dwordx4 v[52:55], v107, s[20:23], s45 offen sc1\n"
    "buffer_load_dwordx4 v[56:59], v107, s[20:23], s46 offen sc1\n"
    "buffer_load_dwordx4 v[60:63], v107, s[20:23], s47 offen sc1\n"
    "buffer_load_dwordx4 v[64:67], v107, s[20:23], s48 offen sc1\n"
    "buffer_load_dwordx4 v[68:71], v107, s[20:23], s49 offen sc1\n"
    "buffer_load_dwordx4 v[72:75], v108, s[20:23], s44 offen sc1\n"
    "buffer_load_dwordx4 v[76:79], v108, s[20:23], s45 offen sc1\n"
    "buffer_load_dwordx4 v[80:83], v108, s[20:23], s46 offen sc1\n"
    "buffer_load_dwordx4 v[84:87], v108, s[20:23], s47 offen sc1\n"
    "buffer_load_dwordx4 v[88:91], v108, s[20:23], s48 offen sc1\n"
    "buffer_load_dwordx4 v[92:95], v108, s[20:23], s49 offen sc1\n"
    "E_37:\n"
    "s_waitcnt vmcnt(0)\n"
    "v_bitop3_b32 v16, v49, v51, s36 bitop3:0x7e\n"
    "v_bitop3_b32 v17, v53, v55, s36 bitop3:0x7e\n"
    "v_bitop3_b32 v18, v57, v59, s36 bitop3:0x7e\n"
    "v_bitop3_b32 v19, v61, v63, s36 bitop3:0x7e\n"
    "v_bitop3_b32 v20, v65, v67, s36 bitop3:0x7e\n"
    "v_bitop3_b32 v21, v69, v71, s36 bitop3:0x7e\n"
    "v_bitop3_b32 v22, v73, v75, s36 bitop3:0x7e\n"
    "v_bitop3_b32 v23, v77, v79, s36 bitop3:0x7e\n"
    "v_bitop3_b32 v24, v81, v83, s36 bitop3:0x7e\n"
    "v_bitop3_b32 v25, v85, v87, s36 bitop3:0x7e\n"
    "v_bitop3_b32 v26, v89, v91, s36 bitop3:0x7e\n"
    "v_bitop3_b32 v27, v93, v95, s36 bitop3:0x7e\n"
    "v_or3_b32 v16, v16, v17, v18\n"
    "v_or3_b32 v19, v19, v20, v21\n"
    "v_or3_b32 v22, v22, v23, v24\n"
    "v_or3_b32 v25, v25, v26, v27\n"
    "v_or3_b32 v16, v16, v19, v22\n"
    "v_or_b32 v16, v16, v25\n"
    "v_cmp_ne_u32 vcc, 0, v16\n"
    "s_cbranch_vccnz R_36\n"
    "ds_write_b32 v41, v45\n"
    "v_add_f32 v16, v56, v60\n"
    "v_add_f32 v17, v48, v52\n"
    "v_add_f32 v19, v64, v68\n"
    "v_cvt_pk_bf16_f32 v16, 0, v16\n"
    "v_cvt_pk_bf16_f32 v17, 0, v17\n"
    "v_cvt_pk_bf16_f32 v19, 0, v19\n"
    "v_add_f32 v16, 1.0, v16\n"
    "v_lshlrev_b32 v32, 16, v45\n"
    "v_cvt_pk_bf16_f32 v16, 0, v16\n"
    "v_mul_f32 v18, v16, v32\n"
    "ds_write_b128 v1, v[16:19] offset:0\n"
    "v_add_f32 v20, v58, v62\n"
    "v_add_f32 v21, v50, v54\n"
    "v_add_f32 v23, v66, v70\n"
    "v_cvt_pk_bf16_f32 v20, 0, v20\n"
    "v_cvt_pk_bf16_f32 v21, 0, v21\n"
    "v_cvt_pk_bf16_f32 v23, 0, v23\n"
    "v_add_f32 v20, 1.0, v20\n"
    "v_and_b32 v33, 0xffff0000, v45\n"
    "v_cvt_pk_bf16_f32 v20, 0, v20\n"
    "v_mul_f32 v22, v20, v33\n"
    "ds_write_b128 v1, v[20:23] offset:16\n"
    "ds_write_b32 v42, v46\n"
    "v_add_f32 v24, v80, v84\n"
    "v_add_f32 v25, v72, v76\n"
    "v_add_f32 v27, v88, v92\n"
    "v_cvt_pk_bf16_f32 v24, 0, v24\n"
    "v_cvt_pk_bf16_f32 v25, 0, v25\n"
    "v_cvt_pk_bf16_f32 v27, 0, v27\n"
    "v_add_f32 v24, 1.0, v24\n"
    "v_lshlrev_b32 v34, 16, v46\n"
    "v_cvt_pk_bf16_f32 v24, 0, v24\n"
    "v_mul_f32 v26, v24, v34\n"
    "ds_write_b128 v2, v[24:27] offset:0\n"
    "v_add_f32 v28, v82, v86\n"
    "v_add_f32 v29, v74, v78\n"
    "v_add_f32 v31, v90, v94\n"
    "v_cvt_pk_bf16_f32 v28, 0, v28\n"
    "v_cvt_pk_bf16_f32 v29, 0, v29\n"
    "v_cvt_pk_bf16_f32 v31, 0, v31\n"
    "v_add_f32 v28, 1.0, v28\n"
    "v_and_b32 v35, 0xffff0000, v46\n"
    "v_cvt_pk_bf16_f32 v28, 0, v28\n"
    "v_mul_f32 v30, v28, v35\n"
    "ds_write_b128 v2, v[28:31] offset:16\n"
    "s_mov_b64 exec, 1\n"
    "ds_add_u32 v14, v15 offset:56\n"
    "s_mov_b64 exec, -1\n"
    "s_setprio 0\n"
    "v_cmp_gt_u32 vcc, 0x60, v109\n"
    "v_mul_f32 v32, v32, v17\n"
    "v_mul_f32 v33, v33, v21\n"
    "v_add_f32 v4, v18, v22\n"
    "v_mul_f32 v5, v18, v18\n"
    "v_fmac_f32 v5, v22, v22\n"
    "v_mul_f32 v5, 4.0, v5\n"
    "v_fmac_f32 v5, v32, v32\n"
    "v_fmac_f32 v5, v33, v33\n"
    "v_mul_f32 v34, v34, v25\n"
    "v_mul_f32 v35, v35, v29\n"
    "v_add_f32 v6, v26, v30\n"
    "v_mul_f32 v7, v26, v26\n"
    "v_fmac_f32 v7, v30, v30\n"
    "v_mul_f32 v7, 4.0, v7\n"
    "v_fmac_f32 v7, v34, v34\n"
    "v_fmac_f32 v7, v35, v35\n"
    "v_cndmask_b32 v6, 0, v6, vcc\n"
    "v_cndmask_b32 v7, 0, v7, vcc\n"
    "v_add_f32 v4, v4, v6\n"
    "v_add_f32 v5, v5, v7\n"
    "v_lshrrev_b32 v3, 4, v109\n"
    "v_lshlrev_b32 v3, 3, v3\n"
    "v_add_u32 v3, 148736, v3\n"
    "v_add_f32_dpp v4, v4, v4 row_ror:8 row_mask:0xf bank_mask:0xf\n"
    "v_add_f32_dpp v5, v5, v5 row_ror:8 row_mask:0xf bank_mask:0xf\n"
    "s_nop 0\n"
    "v_add_f32_dpp v4, v4, v4 row_ror:4 row_mask:0xf bank_mask:0xf\n"
    "v_add_f32_dpp v5, v5, v5 row_ror:4 row_mask:0xf bank_mask:0xf\n"
    "s_nop 0\n"
    "v_add_f32_dpp v4, v4, v4 row_ror:2 row_mask:0xf bank_mask:0xf\n"
    "v_add_f32_dpp v5, v5, v5 row_ror:2 row_mask:0xf bank_mask:0xf\n"
    "s_nop 0\n"
    "v_add_f32_dpp v4, v4, v4 row_ror:1 row_mask:0xf bank_mask:0xf\n"
    "v_add_f32_dpp v5, v5, v5 row_ror:1 row_mask:0xf bank_mask:0xf\n"
    "ds_write_b64 v3, v[4:5]\n"
    "LN2:\n"
    "s_cmpk_ge_u32 s2, 128\n"
    "s_cbranch_scc1 CDONE\n"
    "s_and_b32 s57, s2, 7\n"
    "s_lshl_b32 s57, s57, 4\n"
    "s_lshr_b32 s43, s2, 3\n"
    "s_add_u32 s57, s57, s43\n"
    "v_and_b32 v2, 7, v0\n"
    "v_lshrrev_b32 v3, 3, v0\n"
    "v_mul_u32_u24 v6, 12, v3\n"
    "s_movk_i32 s44, 0x900\n"
    "v_mad_u32_u24 v4, v2, s44, v6\n"
    "v_lshlrev_b32 v2, 4, v2\n"
    "v_add_u32 v4, v2, v4\n"
    "v_add_u32 v5, 72320, v6\n"
    "s_mov_b32 s44, 33024\n"
    "v_mad_u32_u24 v1, v3, s44, v2\n"
    "s_lshl_b32 s42, s57, 7\n"
    "s_add_u32 s42, s42, 131072\n"
    "s_add_u32 s43, s42, 16512\n"
    "s_mov_b32 s44, 138240\n"
    "v_lshl_add_u32 v6, v6, 3, s44\n"
    "v_mov_b32 v7, 147968\n"
    "v_and_b32 v16, 8, v0\n"
    "v_lshl_add_u32 v16, v16, 8, v2\n"
    "v_bfe_u32 v17, v0, 4, 2\n"
    "v_lshlrev_b32 v18, 2, v17\n"
    "s_lshl_b32 s44, s37, 7\n"
    "s_add_u32 s44, s44, 132512\n"
    "v_add3_u32 v16, v16, v18, s44\n"
    "v_and_b32 v18, 2, v17\n"
    "v_lshl_add_u32 v18, v18, 2, v2\n"
    "s_lshl_b32 s44, s37, 2\n"
    "s_add_u32 s44, s44, 136608\n"
    "v_mad_u32_u24 v18, v18, 6, s44\n"
    "v_and_b32 v17, 63, v0\n"
    "s_mov_b32 s44, 132512\n"
    "v_lshl_add_u32 v17, v17, 4, s44\n"
    "v_and_b32 v23, 7, v3\n"
    "v_mul_u32_u24 v22, 3072, v23\n"
    "v_add_u32 v22, v22, v2\n"
    "v_lshlrev_b32 v23, 2, v23\n"
    "v_lshl_add_u32 v23, v2, 1, v23\n"
    "s_mul_i32 s45, s57, 24576\n"
    "s_lshl_b32 s46, s37, 7\n"
    "s_add_u32 s45, s45, s46\n"
    "s_add_u32 s45, s45, 7341056\n"
    "s_add_u32 s46, s36, 1\n"
    "v_mov_b32 v87, s46\n"
    "v_mov_b32 v91, s46\n"
    "s_lshr_b32 s44, s57, 4\n"
    "s_lshl_b32 s44, s44, 1\n"
    "s_add_u32 s44, s44, 2\n"
    "s_branch E_39\n"
    "R_38:\n"
    "s_sleep 2\n"
    "s_sub_u32 s39, s39, 1\n"
    "s_cbranch_scc1 ABORT\n"
    "E_39:\n"
    "ds_read_b32 v19, v7 offset:56\n"
    "s_waitcnt lgkmcnt(0)\n"
    "v_cmp_ne_u32 vcc, 6, v19\n"
    "s_cbranch_vccnz R_38\n"
    "ds_read_b32 v8, v4 offset:53760\n"
    "ds_read_b32 v9, v4 offset:53764\n"
    "ds_read_b32 v10, v4 offset:53768\n"
    "ds_read_b32 v12, v4 offset:54912\n"
    "ds_read_b32 v13, v4 offset:54916\n"
    "ds_read_b32 v14, v4 offset:54920\n"
    "ds_read_b128 v[40:43], v6 offset:0\n"
    "ds_read_b128 v[44:47], v6 offset:16\n"
    "ds_read_b128 v[48:51], v6 offset:32\n"
    "ds_read_b128 v[52:55], v6 offset:48\n"
    "ds_read_b128 v[56:59], v6 offset:64\n"
    "ds_read_b128 v[60:63], v6 offset:80\n"
    "ds_read2_b32 v[36:37], v5 offset1:1\n"
    "ds_read_b32 v38, v5 offset:8\n"
    "s_waitcnt lgkmcnt(8)\n"
    "v_lshlrev_b32 v24, 16, v8\n"
    "v_lshlrev_b32 v25, 16, v12\n"
    "v_and_b32 v26, 0xffff0000, v8\n"
    "v_and_b32 v27, 0xffff0000, v12\n"
    "v_lshlrev_b32 v28, 16, v9\n"
    "v_lshlrev_b32 v29, 16, v13\n"
    "v_and_b32 v30, 0xffff0000, v9\n"
    "v_and_b32 v31, 0xffff0000, v13\n"
    "v_lshlrev_b32 v32, 16, v10\n"
    "v_lshlrev_b32 v33, 16, v14\n"
    "v_and_b32 v34, 0xffff0000, v10\n"
    "v_and_b32 v35, 0xffff0000, v14\n"
    "s_branch E_41\n"
    "R_40:\n"
    "s_sleep 1\n"
    "s_sub_u32 s39, s39, 1\n"
    "s_cbranch_scc1 ABORT\n"
    "E_41:\n"
    "ds_read_b32 v19, v7 offset:60\n"
    "s_waitcnt lgkmcnt(0)\n"
    "v_cmp_gt_u32 vcc, s44, v19\n"
    "s_cbranch_vccnz R_40\n"
    "s_cmpk_lt_u32 s57, 96\n"
    "s_cbranch_scc1 LN2g\n"
    "s_sleep 8\n"
    "LN2g:\n"
    "buffer_load_dwordx4 v[72:75], v1, s[20:23], s42 offen sc1\n"
    "buffer_load_dwordx4 v[76:79], v1, s[20:23], s43 offen sc1\n"
    "s_branch E_43\n"
    "R_42:\n"
    "s_sub_u32 s39, s39, 1\n"
    "s_cbranch_scc1 ABORT\n"
    "s_wqm_b64 exec, vcc\n"
    "buffer_load_dwordx4 v[72:75], v1, s[20:23], s42 offen sc1\n"
    "buffer_load_dwordx4 v[76:79], v1, s[20:23], s43 offen sc1\n"
    "s_mov_b64 exec, -1\n"
    "E_43:\n"
    "s_waitcnt vmcnt(0)\n"
    "v_bitop3_b32 v19, v75, v79, s36 bitop3:0x7e\n"
    "v_cmp_ne_u32 vcc, 0, v19\n"
    "s_cbranch_vccnz R_42\n"
    "v_lshlrev_b32 v20, 16, v72\n"
    "v_and_b32 v21, 0xffff0000, v72\n"
    "v_pk_add_f32 v[24:25], v[24:25], v[20:21]\n"
    "v_cvt_pk_bf16_f32 v24, 0, v24\n"
    "v_cvt_pk_bf16_f32 v25, 0, v25\n"
    "v_pk_mul_f32 v[10:11], v[24:25], v[24:25]\n"
    "v_pk_mul_f32 v[12:13], v[24:25], v[42:43] op_sel_hi:[1,0]\n"
    "v_lshlrev_b32 v20, 16, v73\n"
    "v_and_b32 v21, 0xffff0000, v73\n"
    "v_pk_add_f32 v[26:27], v[26:27], v[20:21]\n"
    "v_cvt_pk_bf16_f32 v26, 0, v26\n"
    "v_cvt_pk_bf16_f32 v27, 0, v27\n"
    "v_pk_add_f32 v[8:9], v[26:27], v[24:25]\n"
    "v_pk_fma_f32 v[10:11], v[26:27], v[26:27], v[10:11]\n"
    "v_pk_fma_f32 v[12:13], v[26:27], v[46:47], v[12:13] op_sel:[0,0,0] op_sel_hi:[1,0,1]\n"
    "v_lshlrev_b32 v20, 16, v74\n"
    "v_and_b32 v21, 0xffff0000, v74\n"
    "v_pk_add_f32 v[28:29], v[28:29], v[20:21]\n"
    "v_cvt_pk_bf16_f32 v28, 0, v28\n"
    "v_cvt_pk_bf16_f32 v29, 0, v29\n"
    "v_pk_add_f32 v[8:9], v[28:29], v[8:9]\n"
    "v_pk_fma_f32 v[10:11], v[28:29], v[28:29], v[10:11]\n"
    "v_pk_fma_f32 v[12:13], v[28:29], v[50:51], v[12:13] op_sel:[0,0,0] op_sel_hi:[1,0,1]\n"
    "v_lshlrev_b32 v20, 16, v76\n"
    "v_and_b32 v21, 0xffff0000, v76\n"
    "v_pk_add_f32 v[30:31], v[30:31], v[20:21]\n"
    "v_cvt_pk_bf16_f32 v30, 0, v30\n"
    "v_cvt_pk_bf16_f32 v31, 0, v31\n"
    "v_pk_add_f32 v[8:9], v[30:31], v[8:9]\n"
    "v_pk_fma_f32 v[10:11], v[30:31], v[30:31], v[10:11]\n"
    "v_pk_fma_f32 v[12:13], v[30:31], v[54:55], v[12:13] op_sel:[0,0,0] op_sel_hi:[1,0,1]\n"
    "v_lshlrev_b32 v20, 16, v77\n"
    "v_and_b32 v21, 0xffff0000, v77\n"
    "v_pk_add_f32 v[32:33], v[32:33], v[20:21]\n"
    "v_cvt_pk_bf16_f32 v32, 0, v32\n"
    "v_cvt_pk_bf16_f32 v33, 0, v33\n"
    "v_pk_add_f32 v[8:9], v[32:33], v[8:9]\n"
    "v_pk_fma_f32 v[10:11], v[32:33], v[32:33], v[10:11]\n"
    "v_pk_fma_f32 v[12:13], v[32:33], v[58:59], v[12:13] op_sel:[0,0,0] op_sel_hi:[1,0,1]\n"
    "v_lshlrev_b32 v20, 16, v78\n"
    "v_and_b32 v21, 0xffff0000, v78\n"
    "v_pk_add_f32 v[34:35], v[34:35], v[20:21]\n"
    "v_cvt_pk_bf16_f32 v34, 0, v34\n"
    "v_cvt_pk_bf16_f32 v35, 0, v35\n"
    "v_pk_add_f32 v[8:9], v[34:35], v[8:9]\n"
    "v_pk_fma_f32 v[10:11], v[34:35], v[34:35], v[10:11]\n"
    "v_pk_fma_f32 v[12:13], v[34:35], v[62:63], v[12:13] op_sel:[0,0,0] op_sel_hi:[1,0,1]\n"
    "v_permlane32_swap_b32 v8, v9\n"
    "v_permlane32_swap_b32 v10, v11\n"
    "v_permlane32_swap_b32 v12, v13\n"
    "v_add_f32 v8, v8, v9\n"
    "v_add_f32 v10, v10, v11\n"
    "v_add_f32 v12, v12, v13\n"
    "s_nop 0\n"
    "v_permlane16_swap_b32 v8, v10\n"
    "v_add_f32 v8, v8, v10\n"
    "s_nop 1\n"
    "v_add_f32_dpp v9, v8, v8 row_ror:8 row_mask:0xf bank_mask:0x3\n"
    "s_nop 1\n"
    "v_add_f32_dpp v9, v12, v12 row_ror:8 row_mask:0xf bank_mask:0xc\n"
    "ds_write_b32 v16, v9\n"
    "s_waitcnt lgkmcnt(0)\n"
    "s_barrier\n"
    "ds_read_b128 v[8:11], v17\n"
    "ds_read_b128 v[12:15], v17 offset:1024\n"
    "s_waitcnt lgkmcnt(0)\n"
    "v_pk_add_f32 v[8:9], v[8:9], v[12:13]\n"
    "v_pk_add_f32 v[10:11], v[10:11], v[14:15]\n"
    "s_nop 1\n"
    "v_permlane32_swap_b32 v8, v10\n"
    "v_permlane32_swap_b32 v9, v11\n"
    "v_pk_add_f32 v[8:9], v[8:9], v[10:11]\n"
    "s_nop 1\n"
    "v_add_f32_dpp v8, v8, v8 row_ror:8 row_mask:0xf bank_mask:0xf\n"
    "v_add_f32_dpp v9, v9, v9 row_ror:8 row_mask:0xf bank_mask:0xf\n"
    "v_mov_b32 v10, v8\n"
    "v_mov_b32 v11, v9\n"
    "s_nop 0\n"
    "v_permlane16_swap_b32 v8, v10\n"
    "v_permlane16_swap_b32 v9, v11\n"
    "v_pk_add_f32 v[8:9], v[8:9], v[10:11]\n"
    "v_mul_f32 v8, 0x3ae38e39, v8\n"
    "v_mul_f32 v9, 0x3ae38e39, v9\n"
    "v_fma_f32 v9, -v8, v8, v9\n"
    "v_max_f32 v9, 0, v9\n"
    "v_cvt_pk_bf16_f32 v9, 0, v9\n"
    "v_add_f32 v9, 0x3727c5ac, v9\n"
    "v_cvt_pk_bf16_f32 v9, 0, v9\n"
    "v_sqrt_f32 v9, v9\n"
    "s_nop 0\n"
    "v_cvt_pk_bf16_f32 v8, v8, v9\n"
    "v_mov_b32 v9, v8\n"
    "s_nop 1\n"
    "v_permlane32_swap_b32 v8, v9\n"
    "v_and_b32 v12, 0xffff0000, v8\n"
    "v_and_b32 v13, 0xffff0000, v9\n"
    "v_lshlrev_b32 v10, 16, v8\n"
    "v_lshlrev_b32 v11, 16, v9\n"
    "v_rcp_f32 v12, v12\n"
    "v_rcp_f32 v13, v13\n"
    "v_pk_add_f32 v[80:81], v[24:25], v[10:11] neg_lo:[0,1] neg_hi:[0,1]\n"
    "v_pk_add_f32 v[82:83], v[26:27], v[10:11] neg_lo:[0,1] neg_hi:[0,1]\n"
    "v_cvt_pk_bf16_f32 v80, 0, v80\n"
    "v_cvt_pk_bf16_f32 v81, 0, v81\n"
    "v_cvt_pk_bf16_f32 v82, 0, v82\n"
    "v_cvt_pk_bf16_f32 v83, 0, v83\n"
    "v_pk_mul_f32 v[80:81], v[80:81], v[12:13]\n"
    "v_pk_mul_f32 v[82:83], v[82:83], v[12:13]\n"
    "v_cvt_pk_bf16_f32 v80, 0, v80\n"
    "v_cvt_pk_bf16_f32 v81, 0, v81\n"
    "v_cvt_pk_bf16_f32 v82, 0, v82\n"
    "v_cvt_pk_bf16_f32 v83, 0, v83\n"
    "v_pk_mul_f32 v[80:81], v[80:81], v[40:41] op_sel_hi:[1,0]\n"
    "v_pk_mul_f32 v[82:83], v[82:83], v[44:45] op_sel_hi:[1,0]\n"
    "v_cvt_pk_bf16_f32 v80, 0, v80\n"
    "v_cvt_pk_bf16_f32 v81, 0, v81\n"
    "v_cvt_pk_bf16_f32 v82, 0, v82\n"
    "v_cvt_pk_bf16_f32 v83, 0, v83\n"
    "v_pk_add_f32 v[80:81], v[40:41], v[80:81] op_sel:[1,0] op_sel_hi:[1,1]\n"
    "v_pk_add_f32 v[82:83], v[44:45], v[82:83] op_sel:[1,0] op_sel_hi:[1,1]\n"
    "v_cvt_pk_bf16_f32 v72, v80, v82\n"
    "v_cvt_pk_bf16_f32 v76, v81, v83\n"
    "ds_bpermute_b32 v84, v23, v72\n"
    "ds_bpermute_b32 v88, v23, v76\n"
    "v_dot2_f32_bf16 v14, v72, v36, 0\n"
    "v_dot2_f32_bf16 v15, v76, v36, 0\n"
    "v_pk_add_f32 v[80:81], v[28:29], v[10:11] neg_lo:[0,1] neg_hi:[0,1]\n"
    "v_pk_add_f32 v[82:83], v[30:31], v[10:11] neg_lo:[0,1] neg_hi:[0,1]\n"
    "v_cvt_pk_bf16_f32 v80, 0, v80\n"
    "v_cvt_pk_bf16_f32 v81, 0, v81\n"
    "v_cvt_pk_bf16_f32 v82, 0, v82\n"
    "v_cvt_pk_bf16_f32 v83, 0, v83\n"
    "v_pk_mul_f32 v[80:81], v[80:81], v[12:13]\n"
    "v_pk_mul_f32 v[82:83], v[82:83], v[12:13]\n"
    "v_cvt_pk_bf16_f32 v80, 0, v80\n"
    "v_cvt_pk_bf16_f32 v81, 0, v81\n"
    "v_cvt_pk_bf16_f32 v82, 0, v82\n"
    "v_cvt_pk_bf16_f32 v83, 0, v83\n"
    "v_pk_mul_f32 v[80:81], v[80:81], v[48:49] op_sel_hi:[1,0]\n"
    "v_pk_mul_f32 v[82:83], v[82:83], v[52:53] op_sel_hi:[1,0]\n"
    "v_cvt_pk_bf16_f32 v80, 0, v80\n"
    "v_cvt_pk_bf16_f32 v81, 0, v81\n"
    "v_cvt_pk_bf16_f32 v82, 0, v82\n"
    "v_cvt_pk_bf16_f32 v83, 0, v83\n"
    "v_pk_add_f32 v[80:81], v[48:49], v[80:81] op_sel:[1,0] op_sel_hi:[1,1]\n"
    "v_pk_add_f32 v[82:83], v[52:53], v[82:83] op_sel:[1,0] op_sel_hi:[1,1]\n"
    "v_cvt_pk_bf16_f32 v73, v80, v82\n"
    "v_cvt_pk_bf16_f32 v77, v81, v83\n"
    "ds_bpermute_b32 v85, v23, v73\n"
    "ds_bpermute_b32 v89, v23, v77\n"
    "v_dot2_f32_bf16 v14, v73, v37, v14\n"
    "v_dot2_f32_bf16 v15, v77, v37, v15\n"
    "v_pk_add_f32 v[80:81], v[32:33], v[10:11] neg_lo:[0,1] neg_hi:[0,1]\n"
    "v_pk_add_f32 v[82:83], v[34:35], v[10:11] neg_lo:[0,1] neg_hi:[0,1]\n"
    "v_cvt_pk_bf16_f32 v80, 0, v80\n"
    "v_cvt_pk_bf16_f32 v81, 0, v81\n"
    "v_cvt_pk_bf16_f32 v82, 0, v82\n"
    "v_cvt_pk_bf16_f32 v83, 0, v83\n"
    "v_pk_mul_f32 v[80:81], v[80:81], v[12:13]\n"
    "v_pk_mul_f32 v[82:83], v[82:83], v[12:13]\n"
    "v_cvt_pk_bf16_f32 v80, 0, v80\n"
    "v_cvt_pk_bf16_f32 v81, 0, v81\n"
    "v_cvt_pk_bf16_f32 v82, 0, v82\n"
    "v_cvt_pk_bf16_f32 v83, 0, v83\n"
    "v_pk_mul_f32 v[80:81], v[80:81], v[56:57] op_sel_hi:[1,0]\n"
    "v_pk_mul_f32 v[82:83], v[82:83], v[60:61] op_sel_hi:[1,0]\n"
    "v_cvt_pk_bf16_f32 v80, 0, v80\n"
    "v_cvt_pk_bf16_f32 v81, 0, v81\n"
    "v_cvt_pk_bf16_f32 v82, 0, v82\n"
    "v_cvt_pk_bf16_f32 v83, 0, v83\n"
    "v_pk_add_f32 v[80:81], v[56:57], v[80:81] op_sel:[1,0] op_sel_hi:[1,1]\n"
    "v_pk_add_f32 v[82:83], v[60:61], v[82:83] op_sel:[1,0] op_sel_hi:[1,1]\n"
    "v_cvt_pk_bf16_f32 v74, v80, v82\n"
    "v_cvt_pk_bf16_f32 v78, v81, v83\n"
    "ds_bpermute_b32 v86, v23, v74\n"
    "ds_bpermute_b32 v90, v23, v78\n"
    "v_dot2_f32_bf16 v14, v74, v38, v14\n"
    "v_dot2_f32_bf16 v15, v78, v38, v15\n"
    "s_nop 2\n"
    "v_permlane32_swap_b32 v14, v15\n"
    "v_add_f32 v14, v14, v15\n"
    "s_nop 1\n"
    "v_add_f32_dpp v14, v14, v14 row_ror:8 row_mask:0xf bank_mask:0xf\n"
    "v_mov_b32 v15, v14\n"
    "s_nop 1\n"
    "v_permlane16_swap_b32 v14, v15\n"
    "v_add_f32 v14, v14, v15\n"
    "ds_write_b32 v18, v14\n"
    "s_waitcnt lgkmcnt(1)\n"
    "buffer_store_dwordx4 v[84:87], v22, s[20:23], s45 offen sc1\n"
    "buffer_store_dwordx4 v[88:91], v22, s[20:23], s45 offen offset:1536 sc1\n"
    "s_waitcnt lgkmcnt(0)\n"
    "s_barrier\n"
    "v_lshlrev_b32 v80, 16, v72\n"
    "v_lshlrev_b32 v81, 16, v76\n"
    "v_and_b32 v82, 0xffff0000, v72\n"
    "v_and_b32 v83, 0xffff0000, v76\n"
    "v_pk_mul_f32 v[80:81], v[42:43], v[80:81] op_sel:[1,0] op_sel_hi:[1,1]\n"
    "v_pk_mul_f32 v[82:83], v[46:47], v[82:83] op_sel:[1,0] op_sel_hi:[1,1]\n"
    "v_cvt_pk_bf16_f32 v80, 0, v80\n"
    "v_cvt_pk_bf16_f32 v81, 0, v81\n"
    "v_cvt_pk_bf16_f32 v82, 0, v82\n"
    "v_cvt_pk_bf16_f32 v83, 0, v83\n"
    "v_pk_add_f32 v[80:81], v[80:81], v[24:25]\n"
    "v_pk_add_f32 v[82:83], v[82:83], v[26:27]\n"
    "v_cvt_pk_bf16_f32 v8, v80, v82\n"
    "v_cvt_pk_bf16_f32 v12, v81, v83\n"
    "ds_write_b32 v4, v8 offset:18560\n"
    "ds_write_b32 v4, v12 offset:19712\n"
    "v_lshlrev_b32 v80, 16, v73\n"
    "v_lshlrev_b32 v81, 16, v77\n"
    "v_and_b32 v82, 0xffff0000, v73\n"
    "v_and_b32 v83, 0xffff0000, v77\n"
    "v_pk_mul_f32 v[80:81], v[50:51], v[80:81] op_sel:[1,0] op_sel_hi:[1,1]\n"
    "v_pk_mul_f32 v[82:83], v[54:55], v[82:83] op_sel:[1,0] op_sel_hi:[1,1]\n"
    "v_cvt_pk_bf16_f32 v80, 0, v80\n"
    "v_cvt_pk_bf16_f32 v81, 0, v81\n"
    "v_cvt_pk_bf16_f32 v82, 0, v82\n"
    "v_cvt_pk_bf16_f32 v83, 0, v83\n"
    "v_pk_add_f32 v[80:81], v[80:81], v[28:29]\n"
    "v_pk_add_f32 v[82:83], v[82:83], v[30:31]\n"
    "v_cvt_pk_bf16_f32 v9, v80, v82\n"
    "v_cvt_pk_bf16_f32 v13, v81, v83\n"
    "ds_write_b32 v4, v9 offset:18564\n"
    "ds_write_b32 v4, v13 offset:19716\n"
    "v_lshlrev_b32 v80, 16, v74\n"
    "v_lshlrev_b32 v81, 16, v78\n"
    "v_and_b32 v82, 0xffff0000, v74\n"
    "v_and_b32 v83, 0xffff0000, v78\n"
    "v_pk_mul_f32 v[80:81], v[58:59], v[80:81] op_sel:[1,0] op_sel_hi:[1,1]\n"
    "v_pk_mul_f32 v[82:83], v[62:63], v[82:83] op_sel:[1,0] op_sel_hi:[1,1]\n"
    "v_cvt_pk_bf16_f32 v80, 0, v80\n"
    "v_cvt_pk_bf16_f32 v81, 0, v81\n"
    "v_cvt_pk_bf16_f32 v82, 0, v82\n"
    "v_cvt_pk_bf16_f32 v83, 0, v83\n"
    "v_pk_add_f32 v[80:81], v[80:81], v[32:33]\n"
    "v_pk_add_f32 v[82:83], v[82:83], v[34:35]\n"
    "v_cvt_pk_bf16_f32 v10, v80, v82\n"
    "v_cvt_pk_bf16_f32 v14, v81, v83\n"
    "ds_write_b32 v4, v10 offset:18568\n"
    "ds_write_b32 v4, v14 offset:19720\n"
    "s_waitcnt lgkmcnt(0)\n"
    "s_barrier\n"
    "s_branch END\n"
    "LN2K:\n"
    "s_setprio 0\n"
    "s_cmpk_ge_u32 s2, 128\n"
    "s_cbranch_scc1 PFZ\n"
    "s_and_b32 s57, s2, 7\n"
    "s_lshl_b32 s57, s57, 4\n"
    "s_lshr_b32 s43, s2, 3\n"
    "s_add_u32 s57, s57, s43\n"
    "v_and_b32 v1, 63, v0\n"
    "s_cmpk_lg_u32 s37, 12\n"
    "s_cbranch_scc1 LN2C\n"
    "s_setprio 3\n"
    "v_lshlrev_b32 v17, 4, v1\n"
    "v_add_u32 v17, 132512, v17\n"
    "v_and_b32 v2, 7, v0\n"
    "v_lshrrev_b32 v3, 5, v1\n"
    "v_lshl_add_u32 v2, v2, 1, v3\n"
    "v_lshlrev_b32 v3, 3, v2\n"
    "s_lshl_b32 s42, s57, 4\n"
    "s_sub_u32 s42, 2047, s42\n"
    "v_sub_u32 v2, s42, v2\n"
    "v_and_b32 v4, 15, v0\n"
    "v_sub_u32 v5, s42, v4\n"
    "v_lshlrev_b32 v6, 3, v4\n"
    "v_mul_u32_u24 v4, 48, v4\n"
    "v_add_u32 v4, 136608, v4\n"
    "s_lshl_b32 s42, s57, 7\n"
    "s_add_u32 s43, s42, 4096\n"
    "s_add_u32 s42, s42, 77824\n"
    "s_brev_b32 s46, 1\n"
    "s_movk_i32 s47, 0x7ff\n"
    "v_mov_b32 v21, s36\n"
    "v_mov_b32 v23, s36\n"
    "v_min_u32 v7, 12, v1\n"
    "v_lshlrev_b32 v7, 3, v7\n"
    "v_add_u32 v7, 148736, v7\n"
    "v_mov_b32 v32, 0\n"
    "v_mov_b32 v33, 0\n"
    "s_mov_b64 exec, 0x1000\n"
    "ds_write_b64 v7, v[32:33]\n"
    "s_mov_b64 exec, -1\n"
    "s_waitcnt lgkmcnt(0)\n"
    "s_barrier\n"
    "ds_read_b128 v[8:11], v17\n"
    "ds_read_b128 v[12:15], v17 offset:1024\n"
    "ds_read_b128 v[24:27], v17 offset:2048\n"
    "ds_read_b128 v[28:31], v17 offset:3072\n"
    "ds_read_b64 v[32:33], v7\n"
    "s_waitcnt lgkmcnt(3)\n"
    "v_pk_add_f32 v[8:9], v[8:9], v[12:13]\n"
    "v_pk_add_f32 v[10:11], v[10:11], v[14:15]\n"
    "s_nop 1\n"
    "v_permlane32_swap_b32 v8, v10\n"
    "v_permlane32_swap_b32 v9, v11\n"
    "v_pk_add_f32 v[8:9], v[8:9], v[10:11]\n"
    "s_nop 1\n"
    "v_add_f32_dpp v8, v8, v8 row_ror:8 row_mask:0xf bank_mask:0xf\n"
    "v_add_f32_dpp v9, v9, v9 row_ror:8 row_mask:0xf bank_mask:0xf\n"
    "v_mov_b32 v10, v8\n"
    "v_mov_b32 v11, v9\n"
    "s_nop 0\n"
    "v_permlane16_swap_b32 v8, v10\n"
    "v_permlane16_swap_b32 v9, v11\n"
    "v_pk_add_f32 v[8:9], v[8:9], v[10:11]\n"
    "s_waitcnt lgkmcnt(0)\n"
    "v_pk_add_f32 v[24:25], v[24:25], v[28:29]\n"
    "v_pk_add_f32 v[26:27], v[26:27], v[30:31]\n"
    "v_add_f32_dpp v32, v32, v32 row_ror:8 row_mask:0xf bank_mask:0xf\n"
    "v_add_f32 v24, v24, v25\n"
    "v_add_f32 v26, v26, v27\n"
    "v_add_f32_dpp v32, v32, v32 row_ror:4 row_mask:0xf bank_mask:0xf\n"
    "v_mul_f32 v8, 0x3ae38e39, v8\n"
    "v_permlane32_swap_b32 v24, v26\n"
    "v_add_f32_dpp v32, v32, v32 row_ror:2 row_mask:0xf bank_mask:0xf\n"
    "v_add_f32 v24, v24, v26\n"
    "v_mul_f32 v9, 0x3ae38e39, v9\n"
    "v_add_f32_dpp v32, v32, v32 row_ror:1 row_mask:0xf bank_mask:0xf\n"
    "v_add_f32_dpp v24, v24, v24 row_ror:8 row_mask:0xf bank_mask:0xf\n"
    "v_fma_f32 v9, -v8, v8, v9\n"
    "v_mov_b32 v25, v24\n"
    "v_max_f32 v9, 0, v9\n"
    "v_cvt_pk_bf16_f32 v9, 0, v9\n"
    "v_permlane16_swap_b32 v24, v25\n"
    "v_add_f32 v9, 0x3727c5ac, v9\n"
    "v_readfirstlane_b32 s44, v32\n"
    "v_cvt_pk_bf16_f32 v9, 0, v9\n"
    "v_sqrt_f32 v9, v9\n"
    "v_add_f32 v24, v24, v25\n"
    "v_cvt_pk_bf16_f32 v8, v8, v9\n"
    "v_and_b32 v9, 0xffff0000, v8\n"
    "v_lshlrev_b32 v8, 16, v8\n"
    "v_rcp_f32 v9, v9\n"
    "v_fma_f32 v24, -v8, s44, v24\n"
    "v_mul_f32 v24, v24, v9\n"
    "v_and_b32 v9, 0x7ff, v24\n"
    "v_add_u32 v9, 0x7ff, v9\n"
    "v_or_b32 v24, v24, v9\n"
    "v_ashrrev_i32 v9, 31, v24\n"
    "v_bitop3_b32 v24, v9, v24, s46 bitop3:0x36\n"
    "v_bfi_b32 v20, s47, v2, v24\n"
    "v_mov_b32 v22, v20\n"
    "s_nop 1\n"
    "v_permlane32_swap_b32 v20, v22\n"
    "s_mov_b64 exec, 0xff\n"
    "buffer_store_dwordx4 v[20:23], v3, s[20:23], s42 offen sc1\n"
    "s_cmpk_lg_u32 s57, 0\n"
    "s_cbranch_scc1 LN2K2\n"
    "s_mov_b64 exec, -1\n"
    "v_add_f32_dpp v33, v33, v33 row_ror:8 row_mask:0xf bank_mask:0xf\n"
    "s_nop 1\n"
    "v_add_f32_dpp v33, v33, v33 row_ror:4 row_mask:0xf bank_mask:0xf\n"
    "s_nop 1\n"
    "v_add_f32_dpp v33, v33, v33 row_ror:2 row_mask:0xf bank_mask:0xf\n"
    "s_nop 1\n"
    "v_add_f32_dpp v33, v33, v33 row_ror:1 row_mask:0xf bank_mask:0xf\n"
    "s_mov_b64 exec, 1\n"
    "v_sqrt_f32 v34, v33\n"
    "v_mov_b32 v35, s36\n"
    "v_mul_f32 v34, 0x3c343958, v34\n"
    "buffer_store_dwordx2 v[34:35], off, s[20:23], 0 offset:2048 sc1\n"
    "LN2K2:\n"
    "s_mov_b64 exec, 0xffff\n"
    "s_waitcnt lgkmcnt(0)\n"
    "s_barrier\n"
    "ds_read_b128 v[8:11], v4\n"
    "ds_read_b128 v[12:15], v4 offset:16\n"
    "ds_read_b128 v[24:27], v4 offset:32\n"
    "s_waitcnt lgkmcnt(1)\n"
    "v_pk_add_f32 v[8:9], v[8:9], v[12:13]\n"
    "v_pk_add_f32 v[10:11], v[10:11], v[14:15]\n"
    "s_waitcnt lgkmcnt(0)\n"
    "v_pk_add_f32 v[8:9], v[8:9], v[24:25]\n"
    "v_pk_add_f32 v[10:11], v[10:11], v[26:27]\n"
    "v_pk_add_f32 v[8:9], v[8:9], v[10:11]\n"
    "v_add_f32 v8, v8, v9\n"
    "v_and_b32 v9, 0x7ff, v8\n"
    "v_add_u32 v9, 0x7ff, v9\n"
    "v_or_b32 v8, v8, v9\n"
    "v_ashrrev_i32 v9, 31, v8\n"
    "v_bitop3_b32 v8, v9, v8, s46 bitop3:0x36\n"
    "v_bfi_b32 v20, s47, v5, v8\n"
    "buffer_store_dwordx2 v[20:21], v6, s[20:23], s43 offen sc1\n"
    "s_mov_b64 exec, -1\n"
    "s_barrier\n"
    "s_branch END\n"
    "LN2C:\n"
    "s_setprio 2\n"
    "v_subrev_u32 v1, 832, v0\n"
    "v_add_u32 v2, 0, v1\n"
    "v_mul_u32_u24 v3, 7282, v2\n"
    "v_lshrrev_b32 v3, 20, v3\n"
    "v_add_lshl_u32 v8, v2, v3, 4\n"
    "v_add_u32 v2, 192, v1\n"
    "v_mul_u32_u24 v3, 7282, v2\n"
    "v_lshrrev_b32 v3, 20, v3\n"
    "v_add_lshl_u32 v9, v2, v3, 4\n"
    "v_add_u32 v2, 384, v1\n"
    "v_mul_u32_u24 v3, 7282, v2\n"
    "v_lshrrev_b32 v3, 20, v3\n"
    "v_add_lshl_u32 v10, v2, v3, 4\n"
    "v_add_u32 v2, 576, v1\n"
    "v_mul_u32_u24 v3, 7282, v2\n"
    "v_lshrrev_b32 v3, 20, v3\n"
    "v_add_lshl_u32 v11, v2, v3, 4\n"
    "v_add_u32 v2, 768, v1\n"
    "v_mul_u32_u24 v3, 7282, v2\n"
    "v_lshrrev_b32 v3, 20, v3\n"
    "v_add_lshl_u32 v12, v2, v3, 4\n"
    "v_add_u32 v2, 960, v1\n"
    "v_mul_u32_u24 v3, 7282, v2\n"
    "v_lshrrev_b32 v3, 20, v3\n"
    "v_add_lshl_u32 v13, v2, v3, 4\n"
    "s_mul_i32 s42, s57, 18432\n"
    "v_lshl_add_u32 v1, v1, 4, s42\n"
    "v_add_u32 v2, 6144, v1\n"
    "v_add_u32 v3, 12288, v1\n"
    "s_mov_b32 s42, 7341056\n"
    "s_lshl_b32 s43, s57, 2\n"
    "v_mov_b32 v4, s36\n"
    "s_barrier\n"
    "s_barrier\n"
    "s_barrier\n"
    "s_sleep 12\n"
    "ds_read_b128 v[40:43], v8 offset:18560\n"
    "ds_read_b128 v[44:47], v9 offset:18560\n"
    "ds_read_b128 v[48:51], v10 offset:18560\n"
    "ds_read_b128 v[52:55], v11 offset:18560\n"
    "ds_read_b128 v[56:59], v12 offset:18560\n"
    "ds_read_b128 v[60:63], v13 offset:18560\n"
    "s_waitcnt lgkmcnt(5)\n"
    "global_store_dwordx4 v1, v[40:43], s[34:35] offset:0 sc1\n"
    "s_waitcnt lgkmcnt(4)\n"
    "global_store_dwordx4 v1, v[44:47], s[34:35] offset:3072 sc1\n"
    "s_waitcnt lgkmcnt(3)\n"
    "global_store_dwordx4 v2, v[48:51], s[34:35] offset:0 sc1\n"
    "s_waitcnt lgkmcnt(2)\n"
    "global_store_dwordx4 v2, v[52:55], s[34:35] offset:3072 sc1\n"
    "s_waitcnt lgkmcnt(1)\n"
    "global_store_dwordx4 v3, v[56:59], s[34:35] offset:0 sc1\n"
    "s_waitcnt lgkmcnt(0)\n"
    "global_store_dwordx4 v3, v[60:63], s[34:35] offset:3072 sc1\n"
    "s_waitcnt vmcnt(0)\n"
    "s_barrier\n"
    "s_cmpk_lg_u32 s37, 15\n"
    "s_cbranch_scc1 END\n"
    "s_mov_b64 exec, 1\n"
    "buffer_store_dword v4, off, s[20:23], s43 offset:1024 sc1\n"
    "s_mov_b64 exec, -1\n"
    "s_branch END\n"
    "PFZ:\n"
    "s_cmpk_ge_u32 s2, 184\n"
    "s_cbranch_scc1 CDONE\n"
    "s_branch E_45\n"
    "R_44:\n"
    "s_sleep 1\n"
    "s_sub_u32 s39, s39, 1\n"
    "s_cbranch_scc1 ABORT\n"
    "E_45:\n"
    "ds_read_b32 v4, v14 offset:60\n"
    "s_waitcnt lgkmcnt(0)\n"
    "v_cmp_ne_u32 vcc, 16, v4\n"
    "s_cbranch_vccnz R_44\n"
    "v_subrev_u32 v112, 5184, v111\n"
    "v_min_u32 v111, v111, v112\n"
    "v_lshl_add_u32 v112, v111, 7, s60\n"
    "global_load_lds_dword v112, s[32:33] nt\n"
    "v_add_u32 v111, 1728, v111\n"
    "v_subrev_u32 v112, 5184, v111\n"
    "v_min_u32 v111, v111, v112\n"
    "v_lshl_add_u32 v112, v111, 7, s60\n"
    "global_load_lds_dword v112, s[32:33] nt\n"
    "v_add_u32 v111, 1728, v111\n"
    "CDONE:\n"
    "s_cmpk_lt_u32 s2, 184\n"
    "s_cbranch_scc1 END\n"
    "s_branch ES0\n"
    "SIL1:\n"
    "s_barrier\n"
    "v_add_u32 v1, 148736, v1\n"
    "v_mov_b32 v14, 147968\n"
    "v_mov_b32 v15, 1\n"
    "s_waitcnt vmcnt(0)\n"
    "v_lshlrev_b32 v5, 16, v4\n"
    "v_and_b32 v4, 0xffff0000, v4\n"
    "v_mul_f32 v6, 0xbfb8aa3b, v5\n"
    "v_mul_f32 v7, 0xbfb8aa3b, v4\n"
    "v_exp_f32 v6, v6\n"
    "v_exp_f32 v7, v7\n"
    "v_add_f32 v6, 1.0, v6\n"
    "v_add_f32 v7, 1.0, v7\n"
    "v_rcp_f32 v6, v6\n"
    "v_rcp_f32 v7, v7\n"
    "v_mul_f32 v5, v5, v6\n"
    "v_mul_f32 v4, v4, v7\n"
    "v_cvt_pk_bf16_f32 v4, v5, v4\n"
    "ds_write_b32 v1, v4\n"
    "s_mov_b64 exec, 1\n"
    "ds_add_u32 v14, v15 offset:4\n"
    "s_mov_b64 exec, -1\n"
    "s_branch M9\n"
    "M1:\n"
    "s_waitcnt lgkmcnt(0)\n"
    "s_memrealtime s[96:97]\n"
    "s_load_dword s36, s[20:21], 0\n"
    "s_barrier\n"
    "s_cmpk_ge_u32 s2, 246\n"
    "s_cbranch_scc1 M8\n"
    "s_sub_u32 s42, s2, 192\n"
    "s_lshr_b32 s42, s42, 3\n"
    "s_min_u32 s42, s42, 8\n"
    "s_lshr_b32 s44, s28, 7\n"
    "s_add_u32 s44, s44, s2\n"
    "s_sub_u32 s44, 3, s44\n"
    "s_and_b32 s44, s44, 7\n"
    "s_add_u32 s44, s44, 1\n"
    "s_add_u32 s44, s44, s42\n"
    "s_mul_i32 s45, s44, 57\n"
    "s_lshr_b32 s45, s45, 9\n"
    "s_mul_i32 s45, s45, 9\n"
    "s_sub_u32 s44, s44, s45\n"
    "s_and_b32 s58, s2, 7\n"
    "s_mul_i32 s58, s58, 9\n"
    "s_add_u32 s58, s58, s44\n"
    "v_bfe_u32 v12, v0, 3, 3\n"
    "v_bfe_u32 v13, v0, 5, 2\n"
    "v_xor_b32 v13, v13, v0\n"
    "v_mul_u32_u24 v12, 9216, v12\n"
    "v_and_b32 v13, 7, v13\n"
    "v_lshl_add_u32 v12, v13, 4, v12\n"
    "s_lshr_b32 s42, s37, 1\n"
    "s_mul_i32 s42, s42, 73728\n"
    "s_lshl_b32 s44, s58, 7\n"
    "s_add_u32 s42, s42, s44\n"
    "v_add_u32 v12, s42, v12\n"
    "v_xor_b32 v13, 64, v12\n"
    "v_add_u32 v13, 294912, v13\n"
    "s_bitcmp1_b32 s37, 0\n"
    "s_cselect_b64 s[56:57], s[30:31], s[28:29]\n"
    "s_sub_u32 s47, s37, 12\n"
    "s_sub_u32 s42, s2, 192\n"
    "s_lshr_b32 s43, s42, 1\n"
    "s_add_u32 s43, s43, 27\n"
    "s_and_b32 s44, s2, 1\n"
    "s_mul_i32 s45, s44, 1990656\n"
    "s_lshl_b32 s46, s43, 7\n"
    "s_add_u32 s45, s45, s46\n"
    "v_bfe_u32 v1, v0, 3, 5\n"
    "v_mul_u32_u24 v1, 0x1b00, v1\n"
    "v_lshrrev_b32 v2, 3, v0\n"
    "v_lshrrev_b32 v3, 4, v0\n"
    "v_and_b32 v2, 2, v2\n"
    "v_and_b32 v3, 4, v3\n"
    "v_xor_b32 v2, v2, v0\n"
    "v_xor_b32 v2, v2, v3\n"
    "v_and_b32 v2, 7, v2\n"
    "v_lshl_add_u32 v1, v2, 4, v1\n"
    "v_add_u32 v1, s45, v1\n"
    "s_lshl_b32 s45, s47, 10\n"
    "s_add_u32 m0, s45, 98304\n"
    "s_nop 0\n"
    "global_load_lds_dwordx4 v1, s[16:17] nt\n"
    "s_add_u32 m0, m0, 4096\n"
    "v_add_u32 v1, 221184, v1\n"
    "global_load_lds_dwordx4 v1, s[16:17] nt\n"
    "s_add_u32 m0, m0, 4096\n"
    "v_add_u32 v1, 221184, v1\n"
    "global_load_lds_dwordx4 v1, s[16:17] nt\n"
    "s_add_u32 m0, m0, 4096\n"
    "v_add_u32 v1, 221184, v1\n"
    "global_load_lds_dwordx4 v1, s[16:17] nt\n"
    "s_add_u32 m0, m0, 4096\n"
    "v_add_u32 v1, 221184, v1\n"
    "global_load_lds_dwordx4 v1, s[16:17] nt\n"
    "s_add_u32 m0, m0, 4096\n"
    "v_add_u32 v1, 221184, v1\n"
    "global_load_lds_dwordx4 v1, s[16:17] nt\n"
    "s_add_u32 m0, m0, 4096\n"
    "v_add_u32 v1, 221184, v1\n"
    "global_load_lds_dwordx4 v1, s[16:17] nt\n"
    "s_add_u32 m0, m0, 4096\n"
    "v_add_u32 v1, 221184, v1\n"
    "global_load_lds_dwordx4 v1, s[16:17] nt\n"
    "s_add_u32 m0, m0, 4096\n"
    "v_add_u32 v1, 221184, v1\n"
    "global_load_lds_dwordx4 v1, s[16:17] nt\n"
    "s_add_u32 m0, m0, 4096\n"
    "v_add_u32 v1, 221184, v1\n"
    "s_setprio 3\n"
    "s_barrier\n"
    "v_bfe_u32 v5, v0, 4, 2\n"
    "v_bfe_u32 v6, v0, 2, 2\n"
    "v_bfe_u32 v7, v0, 1, 1\n"
    "s_lshl_b32 s45, s47, 1\n"
    "v_or_b32 v7, s45, v7\n"
    "v_and_b32 v8, 2, v6\n"
    "v_xor_b32 v7, v7, v8\n"
    "v_lshlrev_b32 v8, 2, v5\n"
    "v_and_b32 v8, 4, v8\n"
    "v_xor_b32 v7, v7, v8\n"
    "v_lshlrev_b32 v2, 10, v5\n"
    "v_lshl_add_u32 v2, v6, 7, v2\n"
    "v_lshl_add_u32 v2, v7, 4, v2\n"
    "v_and_b32 v8, 1, v0\n"
    "v_lshl_add_u32 v2, v8, 3, v2\n"
    "v_add_u32 v2, 98304, v2\n"
    "s_mul_i32 s45, s44, 576\n"
    "s_add_u32 s45, s45, 148736\n"
    "v_lshl_add_u32 v3, v5, 4, s45\n"
    "s_mul_i32 s45, s44, 27648\n"
    "s_lshl_b32 s46, s43, 9\n"
    "s_add_u32 s45, s45, s46\n"
    "s_lshl_b32 s46, s47, 7\n"
    "s_add_u32 s45, s45, s46\n"
    "s_add_u32 s45, s45, 20480\n"
    "v_and_b32 v4, 15, v0\n"
    "v_lshlrev_b32 v4, 3, v4\n"
    "v_mov_b32 v14, 147968\n"
    "v_mov_b32 v15, 1\n"
    "s_waitcnt lgkmcnt(0)\n"
    "s_add_u32 s36, s36, 1\n"
    "s_waitcnt vmcnt(0)\n"
    "s_mov_b64 exec, 1\n"
    "ds_add_u32 v14, v15 offset:4\n"
    "s_mov_b64 exec, -1\n"
    "s_branch E_47\n"
    "R_46:\n"
    "s_sub_u32 s39, s39, 1\n"
    "s_cbranch_scc1 ABORT\n"
    "E_47:\n"
    "ds_read_b32 v5, v14 offset:4\n"
    "s_waitcnt lgkmcnt(0)\n"
    "v_cmp_ne_u32 vcc, 9, v5\n"
    "s_cbranch_vccnz R_46\n"
    "ds_read_b128 v[16:19], v3 offset:0\n"
    "ds_read_b64_tr_b16 v[52:53], v2 offset:0\n"
    "ds_read_b64_tr_b16 v[54:55], v2 offset:512\n"
    "ds_read_b128 v[20:23], v3 offset:64\n"
    "ds_read_b64_tr_b16 v[56:57], v2 offset:4096\n"
    "ds_read_b64_tr_b16 v[58:59], v2 offset:4608\n"
    "ds_read_b128 v[24:27], v3 offset:128\n"
    "ds_read_b64_tr_b16 v[60:61], v2 offset:8192\n"
    "ds_read_b64_tr_b16 v[62:63], v2 offset:8704\n"
    "ds_read_b128 v[28:31], v3 offset:192\n"
    "ds_read_b64_tr_b16 v[64:65], v2 offset:12288\n"
    "ds_read_b64_tr_b16 v[66:67], v2 offset:12800\n"
    "ds_read_b128 v[32:35], v3 offset:256\n"
    "ds_read_b64_tr_b16 v[68:69], v2 offset:16384\n"
    "ds_read_b64_tr_b16 v[70:71], v2 offset:16896\n"
    "s_waitcnt lgkmcnt(12)\n"
    "v_mfma_f32_16x16x32_bf16 v[8:11], v[16:19], v[52:55], 0\n"
    "ds_read_b128 v[36:39], v3 offset:320\n"
    "ds_read_b64_tr_b16 v[72:73], v2 offset:20480\n"
    "ds_read_b64_tr_b16 v[74:75], v2 offset:20992\n"
    "s_waitcnt lgkmcnt(12)\n"
    "v_mfma_f32_16x16x32_bf16 v[8:11], v[20:23], v[56:59], v[8:11]\n"
    "ds_read_b128 v[40:43], v3 offset:384\n"
    "ds_read_b64_tr_b16 v[76:77], v2 offset:24576\n"
    "ds_read_b64_tr_b16 v[78:79], v2 offset:25088\n"
    "s_waitcnt lgkmcnt(12)\n"
    "v_mfma_f32_16x16x32_bf16 v[8:11], v[24:27], v[60:63], v[8:11]\n"
    "ds_read_b128 v[44:47], v3 offset:448\n"
    "ds_read_b64_tr_b16 v[80:81], v2 offset:28672\n"
    "ds_read_b64_tr_b16 v[82:83], v2 offset:29184\n"
    "s_waitcnt lgkmcnt(12)\n"
    "v_mfma_f32_16x16x32_bf16 v[8:11], v[28:31], v[64:67], v[8:11]\n"
    "ds_read_b128 v[48:51], v3 offset:512\n"
    "ds_read_b64_tr_b16 v[84:85], v2 offset:32768\n"
    "ds_read_b64_tr_b16 v[86:87], v2 offset:33280\n"
    "s_waitcnt lgkmcnt(12)\n"
    "v_mfma_f32_16x16x32_bf16 v[8:11], v[32:35], v[68:71], v[8:11]\n"
    "s_waitcnt lgkmcnt(9)\n"
    "v_mfma_f32_16x16x32_bf16 v[8:11], v[36:39], v[72:75], v[8:11]\n"
    "s_waitcnt lgkmcnt(6)\n"
    "v_mfma_f32_16x16x32_bf16 v[8:11], v[40:43], v[76:79], v[8:11]\n"
    "s_waitcnt lgkmcnt(3)\n"
    "v_mfma_f32_16x16x32_bf16 v[8:11], v[44:47], v[80:83], v[8:11]\n"
    "s_waitcnt lgkmcnt(0)\n"
    "v_mfma_f32_16x16x32_bf16 v[8:11], v[48:51], v[84:87], v[8:11]\n"
    "s_mov_b64 exec, 0xffff\n"
    "s_nop 6\n"
    "v_mov_b32 v9, s36\n"
    "buffer_store_dwordx2 v[8:9], v4, s[20:23], s45 offen sc1\n"
    "s_mov_b64 exec, -1\n"
    "s_mov_b64 exec, 1\n"
    "ds_add_u32 v14, v15 offset:232\n"
    "s_mov_b64 exec, -1\n"
    "s_setprio 0\n"
    "s_branch MOED\n"
    "M8:\n"
    "v_mov_b32 v14, 147968\n"
    "v_mov_b32 v15, 1\n"
    "s_mov_b64 exec, 1\n"
    "ds_add_u32 v14, v15 offset:232\n"
    "s_mov_b64 exec, -1\n"
    "M9:\n"
    "s_waitcnt lgkmcnt(0)\n"
    "s_add_u32 s36, s36, 1\n"
    "ES1:\n"
    "ES0:\n"
    "s_sub_u32 s42, s2, 192\n"
    "s_lshr_b32 s42, s42, 3\n"
    "s_min_u32 s42, s42, 8\n"
    "s_lshr_b32 s44, s28, 7\n"
    "s_add_u32 s44, s44, s2\n"
    "s_sub_u32 s44, 3, s44\n"
    "s_and_b32 s44, s44, 7\n"
    "s_add_u32 s44, s44, 1\n"
    "s_add_u32 s44, s44, s42\n"
    "s_mul_i32 s45, s44, 57\n"
    "s_lshr_b32 s45, s45, 9\n"
    "s_mul_i32 s45, s45, 9\n"
    "s_sub_u32 s44, s44, s45\n"
    "s_and_b32 s58, s2, 7\n"
    "s_mul_i32 s58, s58, 9\n"
    "s_add_u32 s58, s58, s44\n"
    "v_bfe_u32 v12, v0, 3, 3\n"
    "v_bfe_u32 v13, v0, 5, 2\n"
    "v_xor_b32 v13, v13, v0\n"
    "v_mul_u32_u24 v12, 9216, v12\n"
    "v_and_b32 v13, 7, v13\n"
    "v_lshl_add_u32 v12, v13, 4, v12\n"
    "s_lshr_b32 s42, s37, 1\n"
    "s_mul_i32 s42, s42, 73728\n"
    "s_lshl_b32 s44, s58, 7\n"
    "s_add_u32 s42, s42, s44\n"
    "v_add_u32 v12, s42, v12\n"
    "v_xor_b32 v13, 64, v12\n"
    "v_add_u32 v13, 294912, v13\n"
    "s_bitcmp1_b32 s37, 0\n"
    "s_cselect_b64 s[56:57], s[30:31], s[28:29]\n"
    "s_waitcnt lgkmcnt(0)\n"
    "s_barrier\n"
    "s_cmpk_ge_u32 s37, 8\n"
    "s_cbranch_scc1 MOED\n"
    "ES1c:\n"
    "s_lshl_b32 m0, s37, 10\n"
    "ES1d:\n"
    "s_waitcnt vmcnt(4)\n"
    "global_load_lds_dwordx4 v12, s[56:57] nt\n"
    "s_add_u32 m0, m0, 8192\n"
    "v_add_u32 v12, 589824, v12\n"
    "s_waitcnt vmcnt(4)\n"
    "global_load_lds_dwordx4 v13, s[56:57] nt\n"
    "s_add_u32 m0, m0, 8192\n"
    "v_add_u32 v13, 589824, v13\n"
    "s_waitcnt vmcnt(4)\n"
    "global_load_lds_dwordx4 v12, s[56:57] nt\n"
    "s_add_u32 m0, m0, 8192\n"
    "v_add_u32 v12, 589824, v12\n"
    "s_waitcnt vmcnt(4)\n"
    "global_load_lds_dwordx4 v13, s[56:57] nt\n"
    "s_add_u32 m0, m0, 8192\n"
    "v_add_u32 v13, 589824, v13\n"
    "s_waitcnt vmcnt(4)\n"
    "global_load_lds_dwordx4 v12, s[56:57] nt\n"
    "s_add_u32 m0, m0, 8192\n"
    "v_add_u32 v12, 589824, v12\n"
    "s_waitcnt vmcnt(4)\n"
    "global_load_lds_dwordx4 v13, s[56:57] nt\n"
    "s_add_u32 m0, m0, 8192\n"
    "v_add_u32 v13, 589824, v13\n"
    "s_waitcnt vmcnt(4)\n"
    "global_load_lds_dwordx4 v12, s[56:57] nt\n"
    "s_add_u32 m0, m0, 8192\n"
    "v_add_u32 v12, 589824, v12\n"
    "s_waitcnt vmcnt(4)\n"
    "global_load_lds_dwordx4 v13, s[56:57] nt\n"
    "s_add_u32 m0, m0, 8192\n"
    "v_add_u32 v13, 589824, v13\n"
    "s_waitcnt vmcnt(4)\n"
    "global_load_lds_dwordx4 v12, s[56:57] nt\n"
    "s_add_u32 m0, m0, 8192\n"
    "v_add_u32 v12, 589824, v12\n"
    "s_waitcnt vmcnt(4)\n"
    "global_load_lds_dwordx4 v13, s[56:57] nt\n"
    "s_add_u32 m0, m0, 8192\n"
    "v_add_u32 v13, 589824, v13\n"
    "s_waitcnt vmcnt(4)\n"
    "global_load_lds_dwordx4 v12, s[56:57] nt\n"
    "s_add_u32 m0, m0, 8192\n"
    "v_add_u32 v12, 589824, v12\n"
    "s_waitcnt vmcnt(4)\n"
    "global_load_lds_dwordx4 v13, s[56:57] nt\n"
    "s_add_u32 m0, m0, 8192\n"
    "v_add_u32 v13, 589824, v13\n"
    "v_mov_b32 v14, 148096\n"
    "v_mov_b32 v15, 1\n"
    "v_bfe_u32 v3, v0, 4, 2\n"
    "s_add_u32 s63, s36, 1\n"
    "v_bfe_u32 v6, v0, 1, 1\n"
    "v_bfe_u32 v7, v0, 3, 1\n"
    "v_and_b32 v9, 1, v3\n"
    "v_lshl_add_u32 v8, v6, 1, v9\n"
    "v_lshl_add_u32 v8, v7, 2, v8\n"
    "v_xor_b32 v8, s37, v8\n"
    "v_lshlrev_b32 v4, 13, v7\n"
    "v_lshl_add_u32 v4, v6, 10, v4\n"
    "v_lshl_add_u32 v4, v9, 9, v4\n"
    "v_lshl_add_u32 v4, v8, 4, v4\n"
    "v_lshrrev_b32 v9, 1, v3\n"
    "v_lshl_add_u32 v4, v9, 11, v4\n"
    "v_bfe_u32 v9, v0, 2, 1\n"
    "v_lshl_add_u32 v4, v9, 7, v4\n"
    "v_and_b32 v9, 1, v0\n"
    "v_lshl_add_u32 v4, v9, 3, v4\n"
    "v_add_u32 v5, 0x10000, v4\n"
    "v_add_u32 v6, 0x20000, v4\n"
    "v_lshrrev_b32 v2, 3, v0\n"
    "v_and_b32 v8, 2, v2\n"
    "v_mul_u32_u24 v2, 1152, v2\n"
    "v_lshrrev_b32 v7, 4, v0\n"
    "v_and_b32 v7, 4, v7\n"
    "v_xor_b32 v8, v8, v7\n"
    "v_xor_b32 v8, v8, v0\n"
    "v_and_b32 v8, 7, v8\n"
    "v_lshl_add_u32 v2, v8, 4, v2\n"
    "s_mul_i32 s42, s58, 73728\n"
    "v_add_u32 v2, s42, v2\n"
    "s_cmpk_lt_u32 s2, 192\n"
    "s_cbranch_scc1 ES3\n"
    "s_branch E_49\n"
    "R_48:\n"
    "s_sleep 1\n"
    "s_sub_u32 s39, s39, 1\n"
    "s_cbranch_scc1 ABORT\n"
    "E_49:\n"
    "ds_read_b32 v10, v14 offset:104\n"
    "s_waitcnt lgkmcnt(0)\n"
    "v_cmp_ne_u32 vcc, 4, v10\n"
    "s_cbranch_vccnz R_48\n"
    "ES3:\n"
    "s_waitcnt vmcnt(4)\n"
    "global_load_lds_dwordx4 v12, s[56:57] nt\n"
    "s_add_u32 m0, m0, 8192\n"
    "v_add_u32 v12, 589824, v12\n"
    "s_waitcnt vmcnt(4)\n"
    "global_load_lds_dwordx4 v13, s[56:57] nt\n"
    "s_add_u32 m0, m0, 8192\n"
    "v_add_u32 v13, 589824, v13\n"
    "s_waitcnt vmcnt(4)\n"
    "s_mov_b64 exec, 1\n"
    "ds_add_u32 v14, v15 offset:76\n"
    "s_mov_b64 exec, -1\n"
    "s_waitcnt vmcnt(4)\n"
    "global_load_lds_dwordx4 v12, s[56:57] nt\n"
    "s_add_u32 m0, m0, 8192\n"
    "v_add_u32 v12, 589824, v12\n"
    "s_branch E_51\n"
    "R_50:\n"
    "s_sub_u32 s39, s39, 1\n"
    "s_cbranch_scc1 ABORT\n"
    "E_51:\n"
    "ds_read_b32 v10, v14 offset:76\n"
    "s_waitcnt lgkmcnt(0)\n"
    "v_cmp_ne_u32 vcc, 8, v10\n"
    "s_cbranch_vccnz R_50\n"
    "ds_read_b64_tr_b16 v[56:57], v4 offset:0\n"
    "ds_read_b64_tr_b16 v[58:59], v4 offset:256\n"
    "ds_read_b64_tr_b16 v[60:61], v4 offset:4096\n"
    "ds_read_b64_tr_b16 v[62:63], v4 offset:4352\n"
    "ds_read_b64_tr_b16 v[64:65], v4 offset:16384\n"
    "ds_read_b64_tr_b16 v[66:67], v4 offset:16640\n"
    "ds_read_b64_tr_b16 v[68:69], v4 offset:20480\n"
    "ds_read_b64_tr_b16 v[70:71], v4 offset:20736\n"
    "ds_read_b64_tr_b16 v[72:73], v4 offset:32768\n"
    "ds_read_b64_tr_b16 v[74:75], v4 offset:33024\n"
    "ds_read_b64_tr_b16 v[76:77], v4 offset:36864\n"
    "ds_read_b64_tr_b16 v[78:79], v4 offset:37120\n"
    "ds_read_b64_tr_b16 v[80:81], v4 offset:49152\n"
    "ds_read_b64_tr_b16 v[82:83], v4 offset:49408\n"
    "ds_read_b64_tr_b16 v[84:85], v4 offset:53248\n"
    "ds_read_b64_tr_b16 v[86:87], v4 offset:53504\n"
    "ds_read_b64_tr_b16 v[88:89], v5 offset:0\n"
    "ds_read_b64_tr_b16 v[90:91], v5 offset:256\n"
    "ds_read_b64_tr_b16 v[92:93], v5 offset:4096\n"
    "ds_read_b64_tr_b16 v[94:95], v5 offset:4352\n"
    "s_mov_b64 exec, 1\n"
    "ds_add_u32 v14, v15 offset:80\n"
    "s_mov_b64 exec, -1\n"
    "s_waitcnt vmcnt(4)\n"
    "global_load_lds_dwordx4 v13, s[56:57] nt\n"
    "s_add_u32 m0, m0, 8192\n"
    "v_add_u32 v13, 589824, v13\n"
    "s_waitcnt vmcnt(4)\n"
    "global_load_lds_dwordx4 v12, s[56:57] nt\n"
    "s_add_u32 m0, m0, 8192\n"
    "v_add_u32 v12, 589824, v12\n"
    "s_waitcnt vmcnt(4)\n"
    "global_load_lds_dwordx4 v13, s[56:57] nt\n"
    "s_add_u32 m0, m0, 8192\n"
    "v_add_u32 v13, 589824, v13\n"
    "s_branch E_53\n"
    "R_52:\n"
    "s_sleep 1\n"
    "s_sub_u32 s39, s39, 1\n"
    "s_cbranch_scc1 ABORT\n"
    "E_53:\n"
    "ds_read_b32 v10, v14 offset:80\n"
    "s_waitcnt lgkmcnt(0)\n"
    "v_cmp_ne_u32 vcc, 8, v10\n"
    "s_cbranch_vccnz R_52\n"
    "s_mul_i32 m0, s37, 9216\n"
    "s_waitcnt vmcnt(4)\n"
    "global_load_lds_dwordx4 v2, s[32:33] offset:0 nt\n"
    "s_add_u32 m0, m0, 896\n"
    "s_waitcnt vmcnt(4)\n"
    "global_load_lds_dwordx4 v2, s[32:33] offset:128 nt\n"
    "s_add_u32 m0, m0, 896\n"
    "s_waitcnt vmcnt(4)\n"
    "global_load_lds_dwordx4 v2, s[32:33] offset:256 nt\n"
    "s_add_u32 m0, m0, 896\n"
    "s_waitcnt vmcnt(4)\n"
    "global_load_lds_dwordx4 v2, s[32:33] offset:384 nt\n"
    "s_add_u32 m0, m0, 896\n"
    "s_waitcnt vmcnt(4)\n"
    "s_mov_b64 exec, 1\n"
    "ds_add_u32 v14, v15 offset:84\n"
    "s_mov_b64 exec, -1\n"
    "s_waitcnt vmcnt(4)\n"
    "global_load_lds_dwordx4 v2, s[32:33] offset:512 nt\n"
    "s_add_u32 m0, m0, 896\n"
    "s_branch E_55\n"
    "R_54:\n"
    "s_sub_u32 s39, s39, 1\n"
    "s_cbranch_scc1 ABORT\n"
    "E_55:\n"
    "ds_read_b32 v10, v14 offset:84\n"
    "s_waitcnt lgkmcnt(0)\n"
    "v_cmp_ne_u32 vcc, 8, v10\n"
    "s_cbranch_vccnz R_54\n"
    "ds_read_b64_tr_b16 v[96:97], v5 offset:16384\n"
    "ds_read_b64_tr_b16 v[98:99], v5 offset:16640\n"
    "ds_read_b64_tr_b16 v[100:101], v5 offset:20480\n"
    "ds_read_b64_tr_b16 v[102:103], v5 offset:20736\n"
    "ds_read_b64_tr_b16 v[104:105], v5 offset:32768\n"
    "ds_read_b64_tr_b16 v[106:107], v5 offset:33024\n"
    "ds_read_b64_tr_b16 v[108:109], v5 offset:36864\n"
    "ds_read_b64_tr_b16 v[110:111], v5 offset:37120\n"
    "ds_read_b64_tr_b16 v[112:113], v5 offset:49152\n"
    "ds_read_b64_tr_b16 v[114:115], v5 offset:49408\n"
    "ds_read_b64_tr_b16 v[116:117], v5 offset:53248\n"
    "ds_read_b64_tr_b16 v[118:119], v5 offset:53504\n"
    "ds_read_b64_tr_b16 v[120:121], v6 offset:0\n"
    "ds_read_b64_tr_b16 v[122:123], v6 offset:256\n"
    "ds_read_b64_tr_b16 v[124:125], v6 offset:4096\n"
    "ds_read_b64_tr_b16 v[126:127], v6 offset:4352\n"
    "s_mov_b64 exec, 1\n"
    "ds_add_u32 v14, v15 offset:88\n"
    "s_mov_b64 exec, -1\n"
    "s_waitcnt vmcnt(4)\n"
    "global_load_lds_dwordx4 v2, s[32:33] offset:640 nt\n"
    "s_add_u32 m0, m0, 896\n"
    "s_waitcnt vmcnt(4)\n"
    "global_load_lds_dwordx4 v2, s[32:33] offset:768 nt\n"
    "s_add_u32 m0, m0, 896\n"
    "s_waitcnt vmcnt(4)\n"
    "global_load_lds_dwordx4 v2, s[32:33] offset:896 nt\n"
    "s_add_u32 m0, m0, 896\n"
    "s_waitcnt vmcnt(4)\n"
    "global_load_lds_dwordx4 v2, s[32:33] offset:1024 nt\n"
    "s_add_u32 m0, m0, 896\n"
    "CHE_0:\n"
    "s_cbranch_execz CHE_1\n"
    "GUT:\n"
    "v_and_b32 v7, 7, v0\n"
    "v_and_b32 v8, 1, v7\n"
    "v_mul_u32_u24 v4, 1344, v7\n"
    "v_lshlrev_b32 v8, 5, v8\n"
    "v_sub_u32 v4, v4, v8\n"
    "v_add_u32 v4, 73728, v4\n"
    "v_and_b32 v9, 8, v0\n"
    "v_lshlrev_b32 v5, 3, v9\n"
    "v_lshl_add_u32 v5, v3, 4, v5\n"
    "v_add_u32 v5, v5, v4\n"
    "v_and_b32 v6, 1, v3\n"
    "v_lshlrev_b32 v6, 2, v6\n"
    "v_and_b32 v8, 2, v3\n"
    "v_lshrrev_b32 v9, 3, v9\n"
    "v_add3_u32 v6, v6, v8, v9\n"
    "v_mul_u32_u24 v6, 144, v6\n"
    "v_lshl_add_u32 v6, v7, 1, v6\n"
    "s_lshl_b32 s42, s37, 4\n"
    "s_add_u32 s42, s42, 150016\n"
    "v_add_u32 v6, s42, v6\n"
    "v_mul_u32_u24 v8, 0xaaaa, v9\n"
    "v_add_u32 v8, 0x4444, v8\n"
    "v_mov_b32 v16, 0\n"
    "v_mov_b32 v17, 0\n"
    "v_mov_b32 v18, 0\n"
    "v_mov_b32 v19, 0\n"
    "s_setprio 2\n"
    "s_waitcnt vmcnt(0)\n"
    "s_mov_b64 exec, 1\n"
    "ds_add_u32 v14, v15 offset:108\n"
    "s_mov_b64 exec, -1\n"
    "s_waitcnt lgkmcnt(0)\n"
    "s_barrier\n"
    "ds_read_b128 v[20:23], v5 offset:0\n"
    "ds_read_b128 v[24:27], v5 offset:128\n"
    "ds_read_b128 v[28:31], v5 offset:256\n"
    "ds_read_b128 v[32:35], v5 offset:384\n"
    "ds_read_b128 v[36:39], v5 offset:512\n"
    "ds_read_b128 v[40:43], v5 offset:640\n"
    "ds_read_b128 v[44:47], v5 offset:768\n"
    "ds_read_b128 v[48:51], v5 offset:896\n"
    "ds_read_b128 v[52:55], v5 offset:1024\n"
    "ds_read_b32 v10, v14 offset:112\n"
    "s_waitcnt lgkmcnt(9)\n"
    "v_smfmac_f32_16x16x64_bf16 v[16:19], v[20:23], v[56:63], v8\n"
    "s_waitcnt lgkmcnt(8)\n"
    "v_smfmac_f32_16x16x64_bf16 v[16:19], v[24:27], v[64:71], v8\n"
    "s_waitcnt lgkmcnt(7)\n"
    "v_smfmac_f32_16x16x64_bf16 v[16:19], v[28:31], v[72:79], v8\n"
    "s_waitcnt lgkmcnt(6)\n"
    "v_smfmac_f32_16x16x64_bf16 v[16:19], v[32:35], v[80:87], v8\n"
    "s_waitcnt lgkmcnt(5)\n"
    "v_smfmac_f32_16x16x64_bf16 v[16:19], v[36:39], v[88:95], v8\n"
    "s_waitcnt lgkmcnt(4)\n"
    "v_smfmac_f32_16x16x64_bf16 v[16:19], v[40:43], v[96:103], v8\n"
    "s_waitcnt lgkmcnt(3)\n"
    "v_smfmac_f32_16x16x64_bf16 v[16:19], v[44:47], v[104:111], v8\n"
    "s_waitcnt lgkmcnt(2)\n"
    "v_smfmac_f32_16x16x64_bf16 v[16:19], v[48:51], v[112:119], v8\n"
    "s_waitcnt lgkmcnt(1)\n"
    "v_smfmac_f32_16x16x64_bf16 v[16:19], v[52:55], v[120:127], v8\n"
    "s_nop 7\n"
    "v_permlane32_swap_b32 v16, v18\n"
    "v_permlane32_swap_b32 v17, v19\n"
    "v_add_f32 v12, v16, v18\n"
    "v_add_f32 v14, v16, v18\n"
    "v_add_f32 v13, v17, v19\n"
    "s_nop 1\n"
    "v_mov_b32_dpp v12, v13 row_ror:8 row_mask:0xf bank_mask:0xc\n"
    "v_mov_b32_dpp v13, v14 row_ror:8 row_mask:0xf bank_mask:0x3\n"
    "v_cvt_pk_bf16_f32 v12, 0, v12\n"
    "v_mul_f32 v14, 0xbfb8aa3b, v12\n"
    "v_exp_f32 v14, v14\n"
    "v_cvt_pk_bf16_f32 v13, 0, v13\n"
    "v_add_f32 v14, 1.0, v14\n"
    "v_rcp_f32 v14, v14\n"
    "s_nop 0\n"
    "v_mul_f32 v12, v12, v14\n"
    "v_cvt_pk_bf16_f32 v12, 0, v12\n"
    "v_mul_f32 v12, v12, v13\n"
    "v_cvt_pk_bf16_f32 v12, v12, v12\n"
    "ds_write_b16 v6, v12\n"
    "s_waitcnt lgkmcnt(0)\n"
    "s_barrier\n"
    "s_cmpk_lt_u32 s2, 192\n"
    "s_cbranch_scc1 END\n"
    "v_readfirstlane_b32 s96, v10\n"
    "v_and_b32 v1, 63, v0\n"
    "v_and_b32 v2, 15, v0\n"
    "s_movk_i32 s61, 0x7ff\n"
    "s_setprio 0\n"
    "s_cmpk_eq_u32 s37, 7\n"
    "s_cbranch_scc1 FINP\n"
    "s_cmpk_lg_u32 s2, 192\n"
    "s_cbranch_scc1 END\n"
    "s_cmpk_lg_u32 s37, 0\n"
    "s_cbranch_scc1 END\n"
    "v_mov_b32 v1, s63\n"
    "s_mov_b64 exec, 1\n"
    "buffer_store_dword v1, off, s[20:23], 0 sc1\n"
    "s_mov_b64 exec, -1\n"
    "s_branch END\n"
    "CHE_1:\n"
    "s_cbranch_execz CHE_2\n"
    "MOED:\n"
    "v_and_b32 v1, 63, v0\n"
    "v_and_b32 v2, 15, v0\n"
    "v_bfe_u32 v3, v0, 4, 2\n"
    "v_mov_b32 v14, 148096\n"
    "v_mov_b32 v15, 1\n"
    "v_bfe_u32 v6, v0, 2, 2\n"
    "v_bfe_u32 v7, v0, 1, 1\n"
    "v_and_b32 v8, 2, v6\n"
    "v_or_b32 v7, v7, v8\n"
    "v_and_b32 v8, 1, v3\n"
    "v_lshl_add_u32 v7, v8, 2, v7\n"
    "v_mul_u32_u24 v8, 9216, v3\n"
    "v_lshl_add_u32 v8, v6, 7, v8\n"
    "v_lshl_add_u32 v8, v7, 4, v8\n"
    "v_and_b32 v7, 1, v0\n"
    "v_lshl_add_u32 v8, v7, 3, v8\n"
    "s_mul_i32 s42, s37, 6\n"
    "s_sub_u32 s42, s42, 48\n"
    "s_and_b32 s43, s42, 3\n"
    "s_lshl_b32 s43, s43, 5\n"
    "s_lshr_b32 s44, s42, 2\n"
    "s_lshl_b32 s44, s44, 10\n"
    "v_xor_b32 v104, s43, v8\n"
    "v_add_u32 v104, s44, v104\n"
    "s_add_u32 s42, s42, 1\n"
    "s_and_b32 s43, s42, 3\n"
    "s_lshl_b32 s43, s43, 5\n"
    "s_lshr_b32 s44, s42, 2\n"
    "s_lshl_b32 s44, s44, 10\n"
    "v_xor_b32 v105, s43, v8\n"
    "v_add_u32 v105, s44, v105\n"
    "s_add_u32 s42, s42, 1\n"
    "s_and_b32 s43, s42, 3\n"
    "s_lshl_b32 s43, s43, 5\n"
    "s_lshr_b32 s44, s42, 2\n"
    "s_lshl_b32 s44, s44, 10\n"
    "v_xor_b32 v106, s43, v8\n"
    "v_add_u32 v106, s44, v106\n"
    "s_add_u32 s42, s42, 1\n"
    "s_and_b32 s43, s42, 3\n"
    "s_lshl_b32 s43, s43, 5\n"
    "s_lshr_b32 s44, s42, 2\n"
    "s_lshl_b32 s44, s44, 10\n"
    "v_xor_b32 v107, s43, v8\n"
    "v_add_u32 v107, s44, v107\n"
    "s_add_u32 s42, s42, 1\n"
    "s_and_b32 s43, s42, 3\n"
    "s_lshl_b32 s43, s43, 5\n"
    "s_lshr_b32 s44, s42, 2\n"
    "s_lshl_b32 s44, s44, 10\n"
    "v_xor_b32 v108, s43, v8\n"
    "v_add_u32 v108, s44, v108\n"
    "s_add_u32 s42, s42, 1\n"
    "s_and_b32 s43, s42, 3\n"
    "s_lshl_b32 s43, s43, 5\n"
    "s_lshr_b32 s44, s42, 2\n"
    "s_lshl_b32 s44, s44, 10\n"
    "v_xor_b32 v109, s43, v8\n"
    "v_add_u32 v109, s44, v109\n"
    "s_add_u32 s42, s42, 1\n"
    "v_and_b32 v7, 7, v0\n"
    "v_mul_u32_u24 v7, 144, v7\n"
    "v_lshl_add_u32 v7, v3, 4, v7\n"
    "v_add_u32 v7, 150016, v7\n"
    "v_mov_b32 v8, 148224\n"
    "v_cmp_gt_u32 vcc, 8, v2\n"
    "s_nop 1\n"
    "v_cndmask_b32 v4, v8, v7, vcc\n"
    "v_cndmask_b32 v5, v7, v8, vcc\n"
    "v_and_b32 v6, 1, v3\n"
    "v_mul_u32_u24 v6, 6144, v6\n"
    "v_lshl_add_u32 v6, v2, 4, v6\n"
    "v_lshrrev_b32 v7, 1, v3\n"
    "v_lshl_add_u32 v6, v7, 8, v6\n"
    "s_mul_i32 s42, s58, 12416\n"
    "s_lshl_b32 s43, s37, 9\n"
    "s_add_u32 s42, s42, s43\n"
    "s_add_u32 s42, s42, 6443008\n"
    "v_add_u32 v6, s42, v6\n"
    "s_lshl_b32 s62, s37, 2\n"
    "s_add_u32 s62, s62, 151392\n"
    "v_mov_b32 v7, s62\n"
    "CHE_2:\n"
    "s_cbranch_execz CHE_3\n"
    "s_add_u32 s62, s62, 28\n"
    "v_lshlrev_b32 v8, 4, v1\n"
    "v_and_b32 v9, 31, v1\n"
    "v_add_u32 v9, 64, v9\n"
    "s_mov_b32 s42, 73728\n"
    "v_mad_u32_u24 v11, v1, 12, s42\n"
    "v_mad_u32_u24 v12, v9, 12, s42\n"
    "v_subrev_u32 v54, 96, v7\n"
    "v_lshlrev_b32 v55, 2, v2\n"
    "v_add_u32 v55, 151328, v55\n"
    "v_lshlrev_b32 v9, 4, v9\n"
    "s_mov_b32 s59, 7341056\n"
    "s_movk_i32 s60, 1536\n"
    "s_movk_i32 s61, 0x7ff\n"
    "s_add_u32 s63, s36, 1\n"
    "s_cmpk_ge_u32 s37, 14\n"
    "s_cselect_b32 s41, 99, 8\n"
    "s_cmpk_ge_u32 s37, 12\n"
    "s_cbranch_scc1 TOPK\n"
    "s_cmpk_lg_u32 s37, 10\n"
    "s_cbranch_scc1 CHE9\n"
    "s_mov_b64 exec, 0\n"
    "CHE:\n"
    "s_branch CHE_0\n"
    "CHE9:\n"
    "s_mov_b64 exec, -1\n"
    "s_cmpk_lg_u32 s37, 11\n"
    "s_cbranch_scc1 MOE2\n"
    "s_memrealtime s[96:97]\n"
    "s_waitcnt lgkmcnt(0)\n"
    "s_cmpk_lt_u32 s2, 192\n"
    "s_cbranch_scc1 HNT1\n"
    "U_56:\n"
    "s_sleep 8\n"
    "s_memrealtime s[98:99]\n"
    "s_waitcnt lgkmcnt(0)\n"
    "s_sub_u32 s98, s98, s96\n"
    "s_cmpk_lt_u32 s98, 400\n"
    "s_cbranch_scc1 U_56\n"
    "HNT1:\n"
    "v_mad_u32_u24 v18, v1, 3, s58\n"
    "v_subrev_u32 v17, 192, v18\n"
    "v_min_u32 v18, v18, v17\n"
    "v_lshlrev_b32 v18, 7, v18\n"
    "s_mov_b32 s42, 94208\n"
    "s_branch E_58\n"
    "R_57:\n"
    "s_sleep 6\n"
    "s_sub_u32 s39, s39, 1\n"
    "s_cbranch_scc1 ABORT\n"
    "E_58:\n"
    "buffer_load_dwordx2 v[16:17], v18, s[20:23], s42 offen sc1\n"
    "s_waitcnt vmcnt(0)\n"
    "v_cmp_eq_u32 vcc, s36, v17\n"
    "s_bcnt1_i32_b64 s43, vcc\n"
    "s_cmp_lt_u32 s43, 56\n"
    "s_cbranch_scc1 R_57\n"
    "v_subrev_u32 v16, s96, v16\n"
    "v_mov_b32 v17, -1000\n"
    "v_cndmask_b32 v16, v17, v16, vcc\n"
    "s_nop 1\n"
    "v_max_i32_dpp v16, v16, v16 row_ror:8 row_mask:0xf bank_mask:0xf\n"
    "s_nop 1\n"
    "v_max_i32_dpp v16, v16, v16 row_ror:4 row_mask:0xf bank_mask:0xf\n"
    "s_nop 1\n"
    "v_max_i32_dpp v16, v16, v16 row_ror:2 row_mask:0xf bank_mask:0xf\n"
    "s_nop 1\n"
    "v_max_i32_dpp v16, v16, v16 row_ror:1 row_mask:0xf bank_mask:0xf\n"
    "s_nop 1\n"
    "v_max_i32_dpp v16, v16, v16 row_bcast:15 row_mask:0xa bank_mask:0xf\n"
    "s_nop 1\n"
    "v_max_i32_dpp v16, v16, v16 row_bcast:31 row_mask:0xc bank_mask:0xf\n"
    "v_add_u32 v16, s96, v16\n"
    "v_mov_b32 v17, 1\n"
    "s_mov_b32 exec_lo, 0\n"
    "s_brev_b32 exec_hi, 1\n"
    "ds_write_b64 v14, v[16:17] offset:112\n"
    "s_mov_b64 exec, -1\n"
    "MOE2:\n"
    "s_sleep 4\n"
    "s_sub_u32 s39, s39, 1\n"
    "s_cbranch_scc1 ABORT\n"
    "ds_read_b32 v10, v14 offset:72\n"
    "ds_read_b32 v13, v14 offset:108\n"
    "s_waitcnt lgkmcnt(0)\n"
    "v_readfirstlane_b32 s42, v10\n"
    "v_readfirstlane_b32 s43, v13\n"
    "s_cmp_lg_u32 s42, 0\n"
    "s_cbranch_scc1 MOE2a\n"
    "s_cmp_eq_u32 s43, s41\n"
    "s_cbranch_scc1 DNRD\n"
    "s_branch MOE2\n"
    "CHE_3:\n"
    "s_cbranch_execz CHE_4\n"
    "MOE2a:\n"
    "s_setprio 2\n"
    "s_branch MOE3\n"
    "DNRD:\n"
    "ds_read_b64_tr_b16 v[56:57], v104 offset:0\n"
    "ds_read_b64_tr_b16 v[58:59], v104 offset:512\n"
    "ds_read_b64_tr_b16 v[60:61], v105 offset:0\n"
    "ds_read_b64_tr_b16 v[62:63], v105 offset:512\n"
    "ds_read_b64_tr_b16 v[64:65], v106 offset:0\n"
    "ds_read_b64_tr_b16 v[66:67], v106 offset:512\n"
    "ds_read_b64_tr_b16 v[68:69], v107 offset:0\n"
    "ds_read_b64_tr_b16 v[70:71], v107 offset:512\n"
    "ds_read_b64_tr_b16 v[72:73], v108 offset:0\n"
    "ds_read_b64_tr_b16 v[74:75], v108 offset:512\n"
    "ds_read_b64_tr_b16 v[76:77], v109 offset:0\n"
    "ds_read_b64_tr_b16 v[78:79], v109 offset:512\n"
    "ds_read_b64_tr_b16 v[80:81], v104 offset:36864\n"
    "ds_read_b64_tr_b16 v[82:83], v104 offset:37376\n"
    "ds_read_b64_tr_b16 v[84:85], v105 offset:36864\n"
    "ds_read_b64_tr_b16 v[86:87], v105 offset:37376\n"
    "ds_read_b64_tr_b16 v[88:89], v106 offset:36864\n"
    "ds_read_b64_tr_b16 v[90:91], v106 offset:37376\n"
    "ds_read_b64_tr_b16 v[92:93], v107 offset:36864\n"
    "ds_read_b64_tr_b16 v[94:95], v107 offset:37376\n"
    "ds_read_b64_tr_b16 v[96:97], v108 offset:36864\n"
    "ds_read_b64_tr_b16 v[98:99], v108 offset:37376\n"
    "ds_read_b64_tr_b16 v[100:101], v109 offset:36864\n"
    "ds_read_b64_tr_b16 v[102:103], v109 offset:37376\n"
    "s_cmp_eq_u32 s41, 0\n"
    "s_mov_b32 s41, 99\n"
    "s_cbranch_scc1 DOWN1\n"
    "s_cmpk_ge_u32 s37, 12\n"
    "s_cbranch_scc1 TOPK2\n"
    "s_branch MOE2\n"
    "TOPK:\n"
    "s_setprio 3\n"
    "s_mul_i32 s42, s37, 9\n"
    "s_add_u32 s42, s42, s58\n"
    "s_mul_i32 s43, s42, 1821\n"
    "s_lshr_b32 s43, s43, 16\n"
    "s_mul_i32 s43, s43, 36\n"
    "s_sub_u32 s42, s42, s43\n"
    "s_lshl_b32 s42, s42, 7\n"
    "s_sub_u32 s42, 94084, s42\n"
    "v_lshlrev_b32 v13, 4, v0\n"
    "s_mov_b32 s44, 65536\n"
    "s_mov_b32 s45, 69632\n"
    "s_mov_b32 s46, 73728\n"
    "s_mov_b32 s47, 77824\n"
    "v_lshl_add_u32 v16, v2, 2, v14\n"
    "v_lshl_add_u32 v17, v3, 4, v14\n"
    "s_brev_b32 s48, 1\n"
    "v_mov_b32 v18, 4\n"
    "v_lshlrev_b32 v19, 2, v2\n"
    "v_add_u32 v19, 151424, v19\n"
    "v_mov_b32 v21, 0\n"
    "ds_write_b32 v55, v21\n"
    "v_mov_b32 v20, 37856\n"
    "s_mov_b64 exec, 1\n"
    "ds_add_u32 v14, v20 offset:64\n"
    "s_mov_b64 exec, -1\n"
    "s_branch E_60\n"
    "CHE_4:\n"
    "s_cbranch_execz CHE_5\n"
    "R_59:\n"
    "s_sleep 4\n"
    "s_sub_u32 s39, s39, 1\n"
    "s_cbranch_scc1 ABORT\n"
    "E_60:\n"
    "ds_read_b64 v[20:21], v14 offset:112\n"
    "s_waitcnt lgkmcnt(0)\n"
    "v_cmp_ne_u32 vcc, 1, v21\n"
    "s_cbranch_vccnz R_59\n"
    "v_readfirstlane_b32 s96, v20\n"
    "U_61:\n"
    "s_sleep 2\n"
    "s_memrealtime s[98:99]\n"
    "s_waitcnt lgkmcnt(0)\n"
    "s_sub_u32 s98, s98, s96\n"
    "s_cmpk_lt_u32 s98, 183\n"
    "s_cbranch_scc1 U_61\n"
    "ds_read_b32 v20, v14 offset:108\n"
    "s_waitcnt lgkmcnt(0)\n"
    "v_readfirstlane_b32 s43, v20\n"
    "s_cmp_eq_u32 s43, s41\n"
    "s_cbranch_scc1 DNRD\n"
    "TOPK2:\n"
    "U_62:\n"
    "s_memrealtime s[98:99]\n"
    "s_waitcnt lgkmcnt(0)\n"
    "s_sub_u32 s98, s98, s96\n"
    "s_cmpk_lt_u32 s98, 208\n"
    "s_cbranch_scc1 U_62\n"
    "buffer_load_dwordx4 v[24:27], v13, s[20:23], s44 offen sc1\n"
    "buffer_load_dwordx4 v[28:31], v13, s[20:23], s45 offen sc1\n"
    "buffer_load_dwordx4 v[32:35], v13, s[20:23], s46 offen sc1\n"
    "buffer_load_dwordx4 v[36:39], v13, s[20:23], s47 offen sc1\n"
    "buffer_load_dwordx2 v[40:41], off, s[20:23], 0 offset:2048 sc1\n"
    "s_branch E_64\n"
    "R_63:\n"
    "s_sub_u32 s39, s39, 1\n"
    "s_cbranch_scc1 ABORT\n"
    "s_wqm_b64 exec, vcc\n"
    "buffer_load_dwordx4 v[24:27], v13, s[20:23], s44 offen sc1\n"
    "buffer_load_dwordx4 v[28:31], v13, s[20:23], s45 offen sc1\n"
    "buffer_load_dwordx4 v[32:35], v13, s[20:23], s46 offen sc1\n"
    "buffer_load_dwordx4 v[36:39], v13, s[20:23], s47 offen sc1\n"
    "buffer_load_dwordx2 v[40:41], off, s[20:23], 0 offset:2048 sc1\n"
    "s_mov_b64 exec, -1\n"
    "E_64:\n"
    "s_waitcnt vmcnt(3)\n"
    "v_bitop3_b32 v20, v25, v27, s36 bitop3:0x7e\n"
    "v_bitop3_b32 v21, v29, v31, s36 bitop3:0x7e\n"
    "v_max3_u32 v42, v24, v26, v28\n"
    "s_waitcnt vmcnt(2)\n"
    "v_bitop3_b32 v22, v33, v35, s36 bitop3:0x7e\n"
    "v_max3_u32 v42, v42, v30, v32\n"
    "v_or3_b32 v20, v20, v21, v22\n"
    "s_waitcnt vmcnt(0)\n"
    "v_bitop3_b32 v21, v37, v39, s36 bitop3:0x7e\n"
    "v_xor_b32 v22, s36, v41\n"
    "v_max3_u32 v42, v42, v34, v36\n"
    "v_or3_b32 v20, v20, v21, v22\n"
    "v_max_u32 v42, v42, v38\n"
    "v_cmp_ne_u32 vcc, 0, v20\n"
    "s_cbranch_vccnz R_63\n"
    "ds_max_u32 v16, v42\n"
    "s_mov_b64 exec, 1\n"
    "ds_add_u32 v14, v15 offset:72\n"
    "s_mov_b64 exec, -1\n"
    "ds_read_b32 v10, v14 offset:72\n"
    "ds_read_b32 v20, v16\n"
    "ds_read_b128 v[46:49], v17\n"
    "v_max3_u32 v45, v24, v26, v28\n"
    "v_med3_u32 v43, v24, v26, v28\n"
    "v_min3_u32 v44, v24, v26, v28\n"
    "v_med3_u32 v44, v43, v44, v30\n"
    "v_med3_u32 v43, v45, v43, v30\n"
    "v_max_u32 v45, v45, v30\n"
    "v_med3_u32 v44, v43, v44, v32\n"
    "v_med3_u32 v43, v45, v43, v32\n"
    "v_max_u32 v45, v45, v32\n"
    "s_branch E_66\n"
    "CHE_5:\n"
    "s_cbranch_execz CHE_6\n"
    "R_65:\n"
    "s_sub_u32 s39, s39, 1\n"
    "s_cbranch_scc1 ABORT\n"
    "ds_read_b32 v10, v14 offset:72\n"
    "ds_read_b32 v20, v16\n"
    "ds_read_b128 v[46:49], v17\n"
    "E_66:\n"
    "s_waitcnt lgkmcnt(0)\n"
    "v_cmp_ne_u32 vcc, 4, v10\n"
    "v_cmp_gt_u32 s[50:51], v46, v20\n"
    "v_cmp_gt_u32 s[52:53], v47, v20\n"
    "v_cmp_gt_u32 s[54:55], v48, v20\n"
    "v_cmp_gt_u32 s[56:57], v49, v20\n"
    "v_cndmask_b32 v21, 0, 1, s[50:51]\n"
    "s_cbranch_vccnz R_65\n"
    "v_addc_co_u32 v21, vcc, 0, v21, s[52:53]\n"
    "v_addc_co_u32 v21, vcc, 0, v21, s[54:55]\n"
    "v_addc_co_u32 v21, vcc, 0, v21, s[56:57]\n"
    "v_mov_b32 v22, v21\n"
    "v_ashrrev_i32 v23, 31, v20\n"
    "v_bitop3_b32 v23, v23, v20, s48 bitop3:0x63\n"
    "v_permlane16_swap_b32 v21, v22\n"
    "v_add_u32 v21, v21, v22\n"
    "v_mov_b32 v22, v21\n"
    "v_sub_f32 v23, v23, v40\n"
    "v_ashrrev_i32 v46, 31, v23\n"
    "v_permlane32_swap_b32 v21, v22\n"
    "v_add_u32 v21, v21, v22\n"
    "v_cmp_eq_u32 vcc, 7, v21\n"
    "v_bitop3_b32 v23, v46, v23, s48 bitop3:0x36\n"
    "v_min_u32 v23, v23, v20\n"
    "v_and_b32 v23, 0xfffff800, v23\n"
    "v_med3_u32 v44, v43, v44, v34\n"
    "v_med3_u32 v43, v45, v43, v34\n"
    "s_ff1_i32_b64 s49, vcc\n"
    "v_readlane_b32 s49, v23, s49\n"
    "v_max_u32 v45, v45, v34\n"
    "v_med3_u32 v44, v43, v44, v36\n"
    "v_cmpx_le_u32 vcc, s49, v42\n"
    "v_med3_u32 v43, v45, v43, v36\n"
    "v_max_u32 v45, v45, v36\n"
    "v_med3_u32 v44, v43, v44, v38\n"
    "v_med3_u32 v43, v45, v43, v38\n"
    "v_cmp_le_u32 s[50:51], s49, v43\n"
    "v_cmp_le_u32 s[52:53], s49, v44\n"
    "s_nop 0\n"
    "v_cndmask_b32 v21, 4, 8, s[50:51]\n"
    "s_cmp_lg_u64 s[52:53], 0\n"
    "s_cbranch_scc1 TOPS\n"
    "ds_add_rtn_u32 v21, v14, v21 offset:64\n"
    "s_waitcnt lgkmcnt(0)\n"
    "ds_write_b32 v21, v42\n"
    "s_mov_b64 exec, s[50:51]\n"
    "ds_write_b32 v21, v43 offset:4\n"
    "TOPC:\n"
    "s_mov_b64 exec, 1\n"
    "ds_add_u32 v14, v15 offset:68\n"
    "s_mov_b64 exec, -1\n"
    "s_cmpk_eq_u32 s37, 15\n"
    "s_cbranch_scc1 MOE15\n"
    "MOE3:\n"
    "ds_read_b64 v[20:21], v14 offset:64\n"
    "ds_read2_b32 v[22:23], v7 offset1:7\n"
    "s_sub_u32 s39, s39, 1\n"
    "s_cbranch_scc1 ABORT\n"
    "s_waitcnt lgkmcnt(0)\n"
    "v_cmp_ne_u32 vcc, 4, v21\n"
    "v_readfirstlane_b32 s42, v20\n"
    "v_bitop3_b32 v22, v22, s61, v22 bitop3:0x0c\n"
    "s_cbranch_vccnz MOE3\n"
    "s_cmp_gt_u32 s42, 151488\n"
    "s_cbranch_scc1 MANY\n"
    "MOE4:\n"
    "v_mad_u32_u24 v24, v22, s60, v8\n"
    "v_mad_u32_u24 v25, v22, s60, v9\n"
    "s_cmp_gt_u32 s42, s62\n"
    "s_cbranch_scc1 ROW2\n"
    "buffer_load_dwordx4 v[28:31], v24, s[20:23], s59 offen sc1\n"
    "buffer_load_dwordx4 v[32:35], v25, s[20:23], s59 offen sc1\n"
    "ROW1:\n"
    "s_branch E_68\n"
    "CHE_6:\n"
    "s_cbranch_execz CHE_7\n"
    "R_67:\n"
    "s_sub_u32 s39, s39, 1\n"
    "s_cbranch_scc1 ABORT\n"
    "E_68:\n"
    "ds_read_b32 v10, v14 offset:88\n"
    "ds_read2_b32 v[44:45], v54 offset1:7\n"
    "ds_read_b32 v46, v55\n"
    "s_waitcnt lgkmcnt(0)\n"
    "v_cmp_ne_u32 s[50:51], 8, v10\n"
    "v_cmp_eq_u32 vcc, 0, v44\n"
    "s_or_b64 vcc, vcc, s[50:51]\n"
    "s_cbranch_vccnz R_67\n"
    "v_cmp_gt_u32 vcc, v46, v44\n"
    "s_bcnt1_i32_b64 s43, vcc\n"
    "s_cmp_ge_u32 s43, 32\n"
    "s_cbranch_scc1 MOE5\n"
    "s_and_b32 s44, s43, 4\n"
    "s_mul_i32 s45, s43, 336\n"
    "s_lshl_b32 s44, s44, 3\n"
    "s_sub_u32 s45, s45, s44\n"
    "v_add_u32 v50, s45, v11\n"
    "v_add_u32 v51, s45, v12\n"
    "s_waitcnt vmcnt(1)\n"
    "ds_write2_b32 v50, v28, v29 offset1:1\n"
    "ds_write_b32 v50, v30 offset:8\n"
    "v_add_u32 v52, s43, v14\n"
    "s_waitcnt vmcnt(0)\n"
    "ds_write2_b32 v51, v32, v33 offset1:1\n"
    "ds_write_b32 v51, v34 offset:8\n"
    "v_bitop3_b32 v20, v31, v35, s63 bitop3:0x7e\n"
    "ds_write_b32 v52, v44 offset:3072\n"
    "v_cmp_ne_u32 vcc, 0, v20\n"
    "s_cbranch_vccnz ROWS\n"
    "MOE5:\n"
    "s_waitcnt lgkmcnt(0)\n"
    "s_barrier\n"
    "s_cmpk_lt_u32 s37, 14\n"
    "s_cbranch_scc1 DOWN\n"
    "s_cmpk_lt_u32 s2, 192\n"
    "s_cbranch_scc1 END\n"
    "s_waitcnt vmcnt(0)\n"
    "FINP:\n"
    "s_and_b32 s42, s2, 7\n"
    "s_lshl_b32 s42, s42, 3\n"
    "s_sub_u32 s43, s2, 192\n"
    "s_lshr_b32 s43, s43, 3\n"
    "s_add_u32 s42, s42, s43\n"
    "s_mul_i32 s42, s42, 3\n"
    "s_max_u32 s44, s37, 13\n"
    "s_add_u32 s42, s42, s44\n"
    "s_sub_u32 s42, s42, 13\n"
    "s_mul_i32 s43, s42, 1366\n"
    "s_lshr_b32 s43, s43, 16\n"
    "s_mul_i32 s44, s43, 48\n"
    "s_sub_u32 s42, s42, s44\n"
    "s_lshl_b32 s42, s42, 2\n"
    "s_lshl_b32 s44, s43, 3\n"
    "s_add_u32 s44, s44, 151168\n"
    "v_and_b32 v20, 1, v0\n"
    "v_lshl_add_u32 v21, v20, 2, s44\n"
    "ds_read_b32 v23, v21\n"
    "s_mul_i32 s44, s43, 2688\n"
    "s_add_u32 s44, s44, 73728\n"
    "v_mul_u32_u24 v22, 1312, v20\n"
    "v_add_u32 v22, s44, v22\n"
    "s_lshr_b32 s45, s42, 4\n"
    "s_lshl_b32 s45, s45, 5\n"
    "s_add_u32 s45, s45, s42\n"
    "v_lshrrev_b32 v25, 1, v2\n"
    "v_min_u32 v26, 2, v25\n"
    "v_lshl_add_u32 v27, v26, 4, v3\n"
    "v_add_u32 v27, s45, v27\n"
    "v_mul_u32_u24 v24, 683, v27\n"
    "v_lshrrev_b32 v24, 11, v24\n"
    "v_mul_u32_u24 v26, 3, v24\n"
    "v_sub_u32 v26, v27, v26\n"
    "v_lshlrev_b32 v28, 3, v27\n"
    "s_mov_b32 s45, 43520\n"
    "s_mov_b32 s46, 71168\n"
    "v_mul_u32_u24 v24, 16512, v24\n"
    "s_waitcnt lgkmcnt(0)\n"
    "v_bitop3_b32 v29, v23, s61, v23 bitop3:0x0c\n"
    "v_lshl_add_u32 v22, v27, 1, v22\n"
    "v_mov_b32 v40, 0\n"
    "v_mov_b32 v41, 0\n"
    "ds_read_u16_d16_hi v40, v22\n"
    "v_mul_u32_u24 v30, 1152, v29\n"
    "v_lshl_add_u32 v30, v27, 1, v30\n"
    "global_load_short_d16_hi v41, v30, s[4:5]\n"
    "v_lshrrev_b32 v31, 1, v29\n"
    "v_lshl_add_u32 v24, v31, 4, v24\n"
    "s_mov_b32 s47, 131072\n"
    "v_lshrrev_b32 v31, 4, v29\n"
    "v_lshlrev_b32 v31, 2, v31\n"
    "s_setprio 0\n"
    "s_waitcnt lgkmcnt(0)\n"
    "s_cmpk_eq_u32 s37, 7\n"
    "s_cbranch_scc1 FIN\n"
    "s_barrier\n"
    "s_branch FIN\n"
    "CHE_7:\n"
    "s_cbranch_execz CHE_8\n"
    "DOWN:\n"
    "s_cmp_eq_u32 s41, 99\n"
    "s_cbranch_scc1 DOWN1\n"
    "s_mov_b32 s41, 0\n"
    "s_setprio 0\n"
    "s_sleep 3\n"
    "s_branch DNRD\n"
    "DOWN1:\n"
    "s_setprio 3\n"
    "v_mov_b32 v47, s36\n"
    "v_mov_b32 v51, s36\n"
    "s_waitcnt lgkmcnt(0)\n"
    "s_barrier\n"
    "ds_read_b128 v[104:107], v4\n"
    "ds_read_b128 v[108:111], v4 offset:64\n"
    "ds_read_b128 v[112:115], v5\n"
    "ds_read_b128 v[116:119], v5 offset:64\n"
    "s_waitcnt lgkmcnt(3)\n"
    "v_mfma_f32_16x16x32_bf16 v[8:11], v[104:107], v[56:59], 0\n"
    "v_mfma_f32_16x16x32_bf16 v[12:15], v[104:107], v[60:63], 0\n"
    "v_mfma_f32_16x16x32_bf16 v[16:19], v[104:107], v[64:67], 0\n"
    "s_waitcnt lgkmcnt(2)\n"
    "v_mfma_f32_16x16x32_bf16 v[8:11], v[108:111], v[80:83], v[8:11]\n"
    "v_mfma_f32_16x16x32_bf16 v[12:15], v[108:111], v[84:87], v[12:15]\n"
    "v_mfma_f32_16x16x32_bf16 v[16:19], v[108:111], v[88:91], v[16:19]\n"
    "s_waitcnt lgkmcnt(1)\n"
    "v_mfma_f32_16x16x32_bf16 v[8:11], v[112:115], v[68:71], v[8:11]\n"
    "v_mfma_f32_16x16x32_bf16 v[12:15], v[112:115], v[72:75], v[12:15]\n"
    "v_mfma_f32_16x16x32_bf16 v[16:19], v[112:115], v[76:79], v[16:19]\n"
    "s_waitcnt lgkmcnt(0)\n"
    "v_mfma_f32_16x16x32_bf16 v[8:11], v[116:119], v[92:95], v[8:11]\n"
    "v_mfma_f32_16x16x32_bf16 v[12:15], v[116:119], v[96:99], v[12:15]\n"
    "v_mfma_f32_16x16x32_bf16 v[16:19], v[116:119], v[100:103], v[16:19]\n"
    "s_nop 5\n"
    "v_cvt_pk_bf16_f32 v44, v8, v9\n"
    "v_cvt_pk_bf16_f32 v45, v12, v13\n"
    "v_cvt_pk_bf16_f32 v46, v16, v17\n"
    "buffer_store_dwordx4 v[44:47], v6, s[20:23], 0 offen offset:0 sc1\n"
    "v_cvt_pk_bf16_f32 v48, v10, v11\n"
    "v_cvt_pk_bf16_f32 v49, v14, v15\n"
    "v_cvt_pk_bf16_f32 v50, v18, v19\n"
    "buffer_store_dwordx4 v[48:51], v6, s[20:23], 0 offen offset:3072 sc1\n"
    "s_branch END\n"
    "FIN:\n"
    "s_branch E_70\n"
    "CHE_8:\n"
    "s_cbranch_execz CHE_9\n"
    "R_69:\n"
    "s_sub_u32 s39, s39, 1\n"
    "s_cbranch_scc1 ABORT\n"
    "E_70:\n"
    "buffer_load_dwordx4 v[32:35], v24, s[20:23], s47 offen sc1\n"
    "buffer_load_dwordx2 v[36:37], v28, s[20:23], s45 offen sc1\n"
    "buffer_load_dwordx2 v[38:39], v28, s[20:23], s46 offen sc1\n"
    "s_waitcnt vmcnt(0)\n"
    "v_bitop3_b32 v42, v37, v39, s36 bitop3:0x7e\n"
    "v_xor_b32 v43, s36, v35\n"
    "v_or_b32 v42, v42, v43\n"
    "v_cmp_ne_u32 vcc, 0, v42\n"
    "s_cbranch_vccnz R_69\n"
    "v_cmp_eq_u32 vcc, 1, v26\n"
    "v_cmp_eq_u32 s[50:51], 2, v26\n"
    "v_and_b32 v42, 1, v29\n"
    "v_cndmask_b32 v32, v32, v33, vcc\n"
    "v_cndmask_b32 v32, v32, v34, s[50:51]\n"
    "v_lshlrev_b32 v42, 4, v42\n"
    "v_lshrrev_b32 v32, v42, v32\n"
    "v_lshlrev_b32 v32, 16, v32\n"
    "v_add_f32 v41, v41, v32\n"
    "v_add_f32 v36, v36, v38\n"
    "v_cvt_pk_bf16_f32 v41, 0, v41\n"
    "v_cvt_pk_bf16_f32 v36, 0, v36\n"
    "s_brev_b32 s48, 1\n"
    "v_ashrrev_i32 v42, 31, v23\n"
    "v_bitop3_b32 v23, v42, v23, s48 bitop3:0x63\n"
    "v_and_b32 v23, 0xfffff800, v23\n"
    "v_cvt_pk_bf16_f32 v23, 0, v23\n"
    "v_mul_f32 v23, 0xbfb8aa3b, v23\n"
    "v_exp_f32 v23, v23\n"
    "v_lshlrev_b32 v20, 4, v20\n"
    "v_add_f32 v23, 1.0, v23\n"
    "v_rcp_f32 v23, v23\n"
    "s_movk_i32 s44, 0x3f80\n"
    "v_lshlrev_b32_e64 v20, v20, s44\n"
    "v_cvt_pk_bf16_f32 v23, 0, v23\n"
    "v_cmp_eq_u32 vcc, 0, v25\n"
    "s_nop 1\n"
    "v_cndmask_b32 v32, 0, v20, vcc\n"
    "v_cmp_eq_u32 vcc, 1, v25\n"
    "s_nop 1\n"
    "v_cndmask_b32 v33, 0, v20, vcc\n"
    "v_cmp_eq_u32 vcc, 2, v25\n"
    "s_nop 1\n"
    "v_cndmask_b32 v34, 0, v20, vcc\n"
    "v_mov_b32 v35, 0\n"
    "v_lshrrev_b32 v42, 2, v1\n"
    "v_mul_u32_u24 v42, 12416, v42\n"
    "v_and_b32 v43, 3, v0\n"
    "v_lshl_add_u32 v42, v43, 4, v42\n"
    "s_mul_i32 s43, s43, 3072\n"
    "s_lshl_b32 s42, s42, 4\n"
    "s_add_u32 s42, s42, s43\n"
    "s_add_u32 s50, s42, 6447104\n"
    "s_add_u32 s51, s42, 6645760\n"
    "s_add_u32 s52, s42, 6844416\n"
    "s_add_u32 s53, s42, 7043072\n"
    "s_add_u32 s54, s42, 7241728\n"
    "v_cmp_gt_u32 vcc, 32, v1\n"
    "v_mov_b32 v43, 0x7ffffff0\n"
    "v_mov_b32 v21, s36\n"
    "v_cndmask_b32 v43, v43, v42, vcc\n"
    "v_cndmask_b32 v21, 0, v21, vcc\n"
    "s_mov_b32 s46, 0x003f003f\n"
    "s_mov_b32 s47, 0x003f003f\n"
    "U_71:\n"
    "s_memrealtime s[98:99]\n"
    "s_waitcnt lgkmcnt(0)\n"
    "s_sub_u32 s98, s98, s96\n"
    "s_cmpk_lt_u32 s98, 433\n"
    "s_cbranch_scc1 U_71\n"
    "buffer_load_dwordx4 v[104:107], v42, s[20:23], s50 offen sc1\n"
    "buffer_load_dwordx4 v[108:111], v42, s[20:23], s51 offen sc1\n"
    "buffer_load_dwordx4 v[112:115], v42, s[20:23], s52 offen sc1\n"
    "buffer_load_dwordx4 v[116:119], v42, s[20:23], s53 offen sc1\n"
    "buffer_load_dwordx4 v[120:123], v43, s[20:23], s54 offen sc1\n"
    "buffer_load_dword v22, v31, s[20:23], 0 offen offset:1024 sc1\n"
    "s_branch E_73\n"
    "CHE_9:\n"
    "s_cbranch_execz CHE_10\n"
    "R_72:\n"
    "s_sub_u32 s39, s39, 1\n"
    "s_cbranch_scc1 ABORT\n"
    "s_wqm_b64 exec, vcc\n"
    "buffer_load_dwordx4 v[104:107], v42, s[20:23], s50 offen sc1\n"
    "buffer_load_dwordx4 v[108:111], v42, s[20:23], s51 offen sc1\n"
    "buffer_load_dwordx4 v[112:115], v42, s[20:23], s52 offen sc1\n"
    "buffer_load_dwordx4 v[116:119], v42, s[20:23], s53 offen sc1\n"
    "buffer_load_dwordx4 v[120:123], v43, s[20:23], s54 offen sc1\n"
    "buffer_load_dword v22, v31, s[20:23], 0 offen offset:1024 sc1\n"
    "s_mov_b64 exec, -1\n"
    "E_73:\n"
    "s_waitcnt vmcnt(2)\n"
    "v_bitop3_b32 v24, v107, v111, s36 bitop3:0x7e\n"
    "v_bitop3_b32 v25, v115, v119, s36 bitop3:0x7e\n"
    "s_waitcnt vmcnt(0)\n"
    "v_xor_b32 v26, v21, v123\n"
    "v_bitop3_b32 v24, v24, v22, s36 bitop3:0xf6\n"
    "v_or3_b32 v24, v24, v25, v26\n"
    "v_cmp_ne_u32 vcc, 0, v24\n"
    "s_cbranch_vccnz R_72\n"
    "v_mov_b32 v107, 0\n"
    "v_mov_b32 v111, 0\n"
    "v_mov_b32 v115, 0\n"
    "v_mov_b32 v119, 0\n"
    "v_mov_b32 v123, 0\n"
    "v_mfma_f32_16x16x32_bf16 v[8:11], v[104:107], v[32:35], 0\n"
    "v_mfma_f32_16x16x32_bf16 v[8:11], v[108:111], v[32:35], v[8:11]\n"
    "v_mfma_f32_16x16x32_bf16 v[8:11], v[112:115], v[32:35], v[8:11]\n"
    "v_mfma_f32_16x16x32_bf16 v[8:11], v[116:119], v[32:35], v[8:11]\n"
    "v_mfma_f32_16x16x32_bf16 v[8:11], v[120:123], v[32:35], v[8:11]\n"
    "s_nop 7\n"
    "v_permlane32_swap_b32 v8, v10\n"
    "v_permlane32_swap_b32 v9, v11\n"
    "v_add_f32 v8, v8, v10\n"
    "v_add_f32 v9, v9, v11\n"
    "s_nop 1\n"
    "v_permlane16_swap_b32 v8, v9\n"
    "v_add_f32 v8, v8, v9\n"
    "v_cvt_pk_bf16_f32 v8, 0, v8\n"
    "v_mul_f32 v8, v8, v23\n"
    "v_cvt_pk_bf16_f32 v8, 0, v8\n"
    "v_add_f32 v8, v40, v8\n"
    "v_cvt_pk_bf16_f32 v8, 0, v8\n"
    "v_mul_f32 v8, v36, v8\n"
    "v_cvt_pk_bf16_f32 v8, 0, v8\n"
    "v_add_f32 v8, v41, v8\n"
    "v_cvt_pk_bf16_f32 v8, v8, v8\n"
    "s_mov_b64 exec, s[46:47]\n"
    "global_store_short v30, v8, s[34:35]\n"
    "s_mov_b64 exec, -1\n"
    "s_branch END\n"
    "ROW2:\n"
    "v_bitop3_b32 v23, v23, s61, v23 bitop3:0x0c\n"
    "v_mad_u32_u24 v26, v23, s60, v8\n"
    "v_mad_u32_u24 v27, v23, s60, v9\n"
    "buffer_load_dwordx4 v[28:31], v24, s[20:23], s59 offen sc1\n"
    "buffer_load_dwordx4 v[32:35], v25, s[20:23], s59 offen sc1\n"
    "buffer_load_dwordx4 v[36:39], v26, s[20:23], s59 offen sc1\n"
    "buffer_load_dwordx4 v[40:43], v27, s[20:23], s59 offen sc1\n"
    "ROW3:\n"
    "s_branch E_75\n"
    "CHE_10:\n"
    "s_cbranch_execz CHE_11\n"
    "R_74:\n"
    "s_sub_u32 s39, s39, 1\n"
    "s_cbranch_scc1 ABORT\n"
    "E_75:\n"
    "ds_read_b32 v10, v14 offset:88\n"
    "ds_read2_b32 v[44:45], v54 offset1:7\n"
    "ds_read_b32 v46, v55\n"
    "s_waitcnt lgkmcnt(0)\n"
    "v_cmp_ne_u32 s[50:51], 8, v10\n"
    "v_cmp_eq_u32 vcc, 0, v44\n"
    "s_or_b64 vcc, vcc, s[50:51]\n"
    "s_cbranch_vccnz R_74\n"
    "v_cmp_gt_u32 vcc, v46, v44\n"
    "s_bcnt1_i32_b64 s43, vcc\n"
    "s_cmp_ge_u32 s43, 32\n"
    "s_cbranch_scc1 ROW4\n"
    "s_and_b32 s44, s43, 4\n"
    "s_mul_i32 s45, s43, 336\n"
    "s_lshl_b32 s44, s44, 3\n"
    "s_sub_u32 s45, s45, s44\n"
    "v_add_u32 v50, s45, v11\n"
    "v_add_u32 v51, s45, v12\n"
    "s_waitcnt vmcnt(3)\n"
    "ds_write2_b32 v50, v28, v29 offset1:1\n"
    "ds_write_b32 v50, v30 offset:8\n"
    "v_add_u32 v52, s43, v14\n"
    "s_waitcnt vmcnt(2)\n"
    "ds_write2_b32 v51, v32, v33 offset1:1\n"
    "ds_write_b32 v51, v34 offset:8\n"
    "v_bitop3_b32 v20, v31, v35, s63 bitop3:0x7e\n"
    "ds_write_b32 v52, v44 offset:3072\n"
    "v_cmp_ne_u32 vcc, 0, v20\n"
    "s_cbranch_vccnz ROWT\n"
    "ROW4:\n"
    "v_cmp_gt_u32 vcc, v46, v45\n"
    "s_bcnt1_i32_b64 s43, vcc\n"
    "s_cmp_ge_u32 s43, 32\n"
    "s_cbranch_scc1 ROW5\n"
    "s_and_b32 s44, s43, 4\n"
    "s_mul_i32 s45, s43, 336\n"
    "s_lshl_b32 s44, s44, 3\n"
    "s_sub_u32 s45, s45, s44\n"
    "v_add_u32 v50, s45, v11\n"
    "v_add_u32 v51, s45, v12\n"
    "s_waitcnt vmcnt(1)\n"
    "ds_write2_b32 v50, v36, v37 offset1:1\n"
    "ds_write_b32 v50, v38 offset:8\n"
    "v_add_u32 v52, s43, v14\n"
    "s_waitcnt vmcnt(0)\n"
    "ds_write2_b32 v51, v40, v41 offset1:1\n"
    "ds_write_b32 v51, v42 offset:8\n"
    "v_bitop3_b32 v20, v39, v43, s63 bitop3:0x7e\n"
    "ds_write_b32 v52, v45 offset:3072\n"
    "v_cmp_ne_u32 vcc, 0, v20\n"
    "s_cbranch_vccnz ROWT\n"
    "ROW5:\n"
    "s_add_u32 s43, s62, 28\n"
    "s_cmp_le_u32 s42, s43\n"
    "s_cbranch_scc1 MOE5\n"
    "ds_read_b32 v22, v7 offset:56\n"
    "v_add_u32 v54, 56, v54\n"
    "s_waitcnt vmcnt(0) lgkmcnt(0)\n"
    "v_bitop3_b32 v22, v22, s61, v22 bitop3:0x0c\n"
    "v_mad_u32_u24 v24, v22, s60, v8\n"
    "v_mad_u32_u24 v25, v22, s60, v9\n"
    "s_branch ROWS\n"
    "CHE_11:\n"
    "s_cbranch_execz CHE9\n"
    "MOE15:\n"
    "ds_read_b64 v[20:21], v14 offset:64\n"
    "ds_read_b32 v32, v19\n"
    "s_sub_u32 s39, s39, 1\n"
    "s_cbranch_scc1 ABORT\n"
    "s_waitcnt lgkmcnt(0)\n"
    "v_cmp_ne_u32 vcc, 4, v21\n"
    "v_readfirstlane_b32 s42, v20\n"
    "v_bitop3_b32 v32, v32, s61, v32 bitop3:0x0c\n"
    "s_cbranch_vccnz MOE15\n"
    "s_cmp_gt_u32 s42, 151488\n"
    "s_cbranch_scc1 MANY\n"
    "MOE16:\n"
    "v_lshlrev_b32 v32, 3, v32\n"
    "s_movk_i32 s43, 4096\n"
    "v_cmp_gt_u32 s[50:51], s42, v19\n"
    "buffer_load_dwordx2 v[34:35], v32, s[20:23], s43 offen sc1\n"
    "R_76:\n"
    "s_waitcnt vmcnt(0)\n"
    "v_cmp_ne_u32 vcc, s36, v35\n"
    "v_max_u32 v34, 1, v34\n"
    "v_cndmask_b32 v53, 0, v34, s[50:51]\n"
    "s_and_b64 vcc, vcc, s[50:51]\n"
    "s_cbranch_vccnz XKS\n"
    "ds_write_b32 v55, v53\n"
    "s_branch MOE5\n"
    "XKS:\n"
    "s_sub_u32 s39, s39, 1\n"
    "s_cbranch_scc1 ABORT\n"
    "buffer_load_dwordx2 v[34:35], v32, s[20:23], s43 offen sc1\n"
    "s_branch R_76\n"
    "ROWS:\n"
    "s_sub_u32 s39, s39, 1\n"
    "s_cbranch_scc1 ABORT\n"
    "buffer_load_dwordx4 v[28:31], v24, s[20:23], s59 offen sc1\n"
    "buffer_load_dwordx4 v[32:35], v25, s[20:23], s59 offen sc1\n"
    "s_branch ROW1\n"
    "ROWT:\n"
    "s_sub_u32 s39, s39, 1\n"
    "s_cbranch_scc1 ABORT\n"
    "buffer_load_dwordx4 v[28:31], v24, s[20:23], s59 offen sc1\n"
    "buffer_load_dwordx4 v[32:35], v25, s[20:23], s59 offen sc1\n"
    "buffer_load_dwordx4 v[36:39], v26, s[20:23], s59 offen sc1\n"
    "buffer_load_dwordx4 v[40:43], v27, s[20:23], s59 offen sc1\n"
    "s_branch ROW3\n"
    "TOPS:\n"
    "s_mov_b64 exec, -1\n"
    "v_cmpx_le_u32 vcc, s49, v24\n"
    "ds_add_rtn_u32 v21, v14, v18 offset:64\n"
    "s_waitcnt lgkmcnt(0)\n"
    "ds_write_b32 v21, v24\n"
    "s_mov_b64 exec, -1\n"
    "v_cmpx_le_u32 vcc, s49, v26\n"
    "ds_add_rtn_u32 v21, v14, v18 offset:64\n"
    "s_waitcnt lgkmcnt(0)\n"
    "ds_write_b32 v21, v26\n"
    "s_mov_b64 exec, -1\n"
    "v_cmpx_le_u32 vcc, s49, v28\n"
    "ds_add_rtn_u32 v21, v14, v18 offset:64\n"
    "s_waitcnt lgkmcnt(0)\n"
    "ds_write_b32 v21, v28\n"
    "s_mov_b64 exec, -1\n"
    "v_cmpx_le_u32 vcc, s49, v30\n"
    "ds_add_rtn_u32 v21, v14, v18 offset:64\n"
    "s_waitcnt lgkmcnt(0)\n"
    "ds_write_b32 v21, v30\n"
    "s_mov_b64 exec, -1\n"
    "v_cmpx_le_u32 vcc, s49, v32\n"
    "ds_add_rtn_u32 v21, v14, v18 offset:64\n"
    "s_waitcnt lgkmcnt(0)\n"
    "ds_write_b32 v21, v32\n"
    "s_mov_b64 exec, -1\n"
    "v_cmpx_le_u32 vcc, s49, v34\n"
    "ds_add_rtn_u32 v21, v14, v18 offset:64\n"
    "s_waitcnt lgkmcnt(0)\n"
    "ds_write_b32 v21, v34\n"
    "s_mov_b64 exec, -1\n"
    "v_cmpx_le_u32 vcc, s49, v36\n"
    "ds_add_rtn_u32 v21, v14, v18 offset:64\n"
    "s_waitcnt lgkmcnt(0)\n"
    "ds_write_b32 v21, v36\n"
    "s_mov_b64 exec, -1\n"
    "v_cmpx_le_u32 vcc, s49, v38\n"
    "ds_add_rtn_u32 v21, v14, v18 offset:64\n"
    "s_waitcnt lgkmcnt(0)\n"
    "ds_write_b32 v21, v38\n"
    "s_branch TOPC\n"
    "MANY:\n"
    "v_lshlrev_b32 v22, 2, v0\n"
    "v_add_u32 v22, 149376, v22\n"
    "MANY1:\n"
    "v_cmpx_gt_u32 vcc, s42, v22\n"
    "s_cbranch_execz MANY3\n"
    "ds_read_b32 v23, v22\n"
    "v_mov_b32 v24, 0\n"
    "s_mov_b32 s43, 151424\n"
    "MANY2:\n"
    "v_mov_b32 v25, s43\n"
    "ds_read_b32 v25, v25\n"
    "s_add_u32 s43, s43, 4\n"
    "s_waitcnt lgkmcnt(0)\n"
    "v_cmp_gt_u32 vcc, v25, v23\n"
    "s_nop 1\n"
    "v_addc_co_u32 v24, vcc, 0, v24, vcc\n"
    "s_cmp_lt_u32 s43, s42\n"
    "s_cbranch_scc1 MANY2\n"
    "v_min_u32 v24, 16, v24\n"
    "v_lshl_add_u32 v24, v24, 2, v14\n"
    "ds_write_b32 v24, v23 offset:3152\n"
    "v_add_u32 v22, 2048, v22\n"
    "s_branch MANY1\n"
    "MANY3:\n"
    "s_mov_b64 exec, -1\n"
    "s_mov_b64 exec, 1\n"
    "ds_add_u32 v14, v15 offset:92\n"
    "s_mov_b64 exec, -1\n"
    "s_branch E_78\n"
    "R_77:\n"
    "s_sleep 1\n"
    "s_sub_u32 s39, s39, 1\n"
    "s_cbranch_scc1 ABORT\n"
    "E_78:\n"
    "ds_read_b32 v10, v14 offset:92\n"
    "s_waitcnt lgkmcnt(0)\n"
    "v_cmp_ne_u32 vcc, 8, v10\n"
    "s_cbranch_vccnz R_77\n"
    "v_subrev_u32 v7, 176, v7\n"
    "v_lshl_add_u32 v24, v2, 2, v14\n"
    "ds_read2_b32 v[22:23], v7 offset1:7\n"
    "ds_read_b32 v32, v24 offset:3152\n"
    "s_mov_b32 s42, 151488\n"
    "s_waitcnt lgkmcnt(0)\n"
    "v_bitop3_b32 v22, v22, s61, v22 bitop3:0x0c\n"
    "v_bitop3_b32 v32, v32, s61, v32 bitop3:0x0c\n"
    "s_cmpk_eq_u32 s37, 15\n"
    "s_cbranch_scc1 MOE16\n"
    "s_branch MOE4\n"
    "END:\n"
    "s_endpgm\n"

    :: "{s[0:1]}"(__builtin_amdgcn_kernarg_segment_ptr()), "{s2}"(blockIdx.x), "{v0}"(threadIdx.x) : "v127", "s99", "vcc", "memory");
}

extern "C" void kernel_launch(void* const* d_in, const int* in_sizes, int n_in, void* d_out, int out_size, void* d_ws, size_t ws_size, hipStream_t stream) {
  (void)hipFuncSetAttribute((const void*)mega, hipFuncAttributeMaxDynamicSharedMemorySize, 159680);
  Args p;
  p.x = d_in[0]; p.lar = d_in[1]; p.aim = d_in[2]; p.ldt = d_in[5]; p.cvec = d_in[12]; p.wmod = d_in[13];
  p.ws = (char*)d_ws + ((size_t)240 << 20); p.ws_bytes = 10643968; p.ws_flags = 0x00020000u;
  p.cre = d_in[3]; p.cim = d_in[4]; p.dsk = d_in[6]; p.wr = d_in[8]; p.wg = d_in[9]; p.wu = d_in[10]; p.wd = d_in[11];
  p.out = d_out;
  mega<<<256, 1024, 159680, stream>>>(p);
}
```

```cpp
#include <hip/hip_runtime.h>
#include <stdint.h>

struct Args {
  const void *x, *lar, *aim, *ldt, *cre, *cim, *wmod, *cvec;
  void* ws; uint32_t ws_bytes, ws_flags;
  const void *dsk, *wr, *wg, *wu, *wd;
  void* out;
};
extern "C" __device__ __attribute__((used, aligned(128))) const uint32_t mega_tab[3840] = {
  0x0, 0x0, 0x0, 0x0, 0x0, 0x0, 0x0, 0x0, 0x0, 0x0, 0x0, 0x0, 0x0, 0x0, 0x0, 0x0,
  0x0, 0x0, 0x0, 0x0, 0x0, 0x0, 0x0, 0x0, 0x0, 0x0, 0x0, 0x0, 0x0, 0x0, 0x0, 0x0,
  0x0, 0x0, 0x0, 0x0, 0x0, 0x0, 0x0, 0x0, 0x0, 0x0, 0x0, 0x0, 0x0, 0x0, 0x0, 0x3c000000,
  0x0, 0x0, 0x0, 0x3c003c00, 0x0, 0x0, 0x3c000000, 0x3c003c00, 0x0, 0x0, 0x3c003c00, 0x3c00, 0x0, 0x3c000000, 0x3c003c00, 0x0,
  0x0, 0x0, 0x0, 0x0, 0x0, 0x0, 0x0, 0x0, 0x0, 0x0, 0x0, 0x0, 0x0, 0x0, 0x0, 0x3c000000,
  0x0, 0x0, 0x0, 0x3c003c00, 0x0, 0x0, 0x3c000000, 0x3c003c00, 0x0, 0x0, 0x3c003c00, 0x3c00, 0x0, 0x3c000000, 0x3c003c00, 0x0,
  0x0, 0x3c003c00, 0x3c00, 0x0, 0x3c000000, 0x3c003c00, 0x0, 0x0, 0x3c003c00, 0x3c00, 0x0, 0x0, 0x3c003c00, 0x0, 0x0, 0x0,
  0x3c00, 0x0, 0x0, 0x0, 0x0, 0x0, 0x0, 0x0, 0x0, 0x0, 0x0, 0x0, 0x0, 0x0, 0x0, 0x0,
  0x0, 0x3c003c00, 0x3c00, 0x0, 0x3c000000, 0x3c003c00, 0x0, 0x0, 0x3c003c00, 0x3c00, 0x0, 0x0, 0x3c003c00, 0x0, 0x0, 0x0,
  0x3c00, 0x0, 0x0, 0x0, 0x0, 0x0, 0x0, 0x0, 0x0, 0x0, 0x0, 0x0, 0x0, 0x0, 0x0, 0x0,
  0x0, 0x0, 0x0, 0x0, 0x0, 0x0, 0x0, 0x0, 0x0, 0x0, 0x0, 0x0, 0x0, 0x0, 0x0, 0x0,
  0x0, 0x0, 0x0, 0x0, 0x0, 0x0, 0x0, 0x0, 0x0, 0x0, 0x0, 0x0, 0x0, 0x0, 0x0, 0x0,
  0x0, 0x0, 0x0, 0x0, 0x0, 0x0, 0x0, 0x0, 0x0, 0x0, 0x0, 0x0, 0x0, 0x0, 0x0, 0x0,
  0x0, 0x0, 0x0, 0x0, 0x0, 0x0, 0x0, 0x0, 0x0, 0x0, 0x0, 0x0, 0x0, 0x0, 0x0, 0x0,
  0x0, 0x0, 0x0, 0x0, 0x0, 0x0, 0x0, 0x0, 0x0, 0x0, 0x0, 0x0, 0x0, 0x0, 0x0, 0x0,
  0x0, 0x0, 0x0, 0x0, 0x0, 0x0, 0x0, 0x0, 0x0, 0x0, 0x0, 0x0, 0x0, 0x0, 0x0, 0x0,
  0x0, 0x0, 0x0, 0x0, 0x0, 0x0, 0x0, 0x0, 0x0, 0x0, 0x0, 0x0, 0x0, 0x0, 0x0, 0x0,
  0x0, 0x0, 0x0, 0x0, 0x0, 0x0, 0x0, 0x0, 0x0, 0x0, 0x0, 0x0, 0x0, 0x0, 0x0, 0x0,
  0x0, 0x0, 0x0, 0x0, 0x0, 0x0, 0x0, 0x0, 0x0, 0x0, 0x0, 0x0, 0x0, 0x0, 0x0, 0x0,
  0x0, 0x0, 0x0, 0x0, 0x0, 0x0, 0x0, 0x0, 0x0, 0x0, 0x0, 0x0, 0x0, 0x0, 0x0, 0x0,
  0x0, 0x0, 0x0, 0x0, 0x0, 0x0, 0x0, 0x0, 0x0, 0x0, 0x0, 0x0, 0x0, 0x0, 0x0, 0x0,
  0x0, 0x0, 0x0, 0x0, 0x0, 0x0, 0x0, 0x0, 0x0, 0x0, 0x0, 0x0, 0x0, 0x0, 0x0, 0x0,
  0x0, 0x0, 0x0, 0x0, 0x0, 0x0, 0x0, 0x0, 0x0, 0x0, 0x0, 0x0, 0x0, 0x0, 0x0, 0x3c000000,
  0x0, 0x0, 0x0, 0x3c003c00, 0x0, 0x0, 0x3c000000, 0x3c003c00, 0x0, 0x0, 0x3c003c00, 0x3c00, 0x0, 0x3c000000, 0x3c003c00, 0x0,
  0x0, 0x0, 0x0, 0x0, 0x0, 0x0, 0x0, 0x0, 0x0, 0x0, 0x0, 0x0, 0x0, 0x0, 0x0, 0x3c000000,
  0x0, 0x0, 0x0, 0x3c003c00, 0x0, 0x0, 0x3c000000, 0x3c003c00, 0x0, 0x0, 0x3c003c00, 0x3c00, 0x0, 0x3c000000, 0x3c003c00, 0x0,
  0x0, 0x3c003c00, 0x3c00, 0x0, 0x3c000000, 0x3c003c00, 0x0, 0x0, 0x3c003c00, 0x3c00, 0x0, 0x0, 0x3c003c00, 0x0, 0x0, 0x0,
  0x3c00, 0x0, 0x0, 0x0, 0x0, 0x0, 0x0, 0x0, 0x0, 0x0, 0x0, 0x0, 0x0, 0x0, 0x0, 0x0,
  0x0, 0x3c003c00, 0x3c00, 0x0, 0x3c000000, 0x3c003c00, 0x0, 0x0, 0x3c003c00, 0x3c00, 0x0, 0x0, 0x3c003c00, 0x0, 0x0, 0x0,
  0x3c00, 0x0, 0x0, 0x0, 0x0, 0x0, 0x0, 0x0, 0x0, 0x0, 0x0, 0x0, 0x0, 0x0, 0x0, 0x0,
  0x0, 0x0, 0x0, 0x0, 0x0, 0x0, 0x0, 0x0, 0x0, 0x0, 0x0, 0x0, 0x0, 0x0, 0x0, 0x0,
  0x0, 0x0, 0x0, 0x0, 0x0, 0x0, 0x0, 0x0, 0x0, 0x0, 0x0, 0x0, 0x0, 0x0, 0x0, 0x0,
  0x0, 0x0, 0x0, 0x0, 0x0, 0x0, 0x0, 0x0, 0x0, 0x0, 0x0, 0x0, 0x0, 0x0, 0x0, 0x0,
  0x0, 0x0, 0x0, 0x0, 0x0, 0x0, 0x0, 0x0, 0x0, 0x0, 0x0, 0x0, 0x0, 0x0, 0x0, 0x0,
  0x0, 0x0, 0x0, 0x0, 0x0, 0x0, 0x0, 0x0, 0x0, 0x0, 0x0, 0x0, 0x0, 0x0, 0x0, 0x0,
  0x0, 0x0, 0x0, 0x0, 0x0, 0x0, 0x0, 0x0, 0x0, 0x0, 0x0, 0x3c000000, 0x0, 0x0, 0x0, 0x3c003c00,
  0x0, 0x0, 0x0, 0x0, 0x0, 0x0, 0x0, 0x0, 0x0, 0x0, 0x0, 0x0, 0x0, 0x0, 0x0, 0x0,
  0x0, 0x0, 0x0, 0x0, 0x0, 0x0, 0x0, 0x0, 0x0, 0x0, 0x0, 0x3c000000, 0x0, 0x0, 0x0, 0x3c003c00,
  0x0, 0x0, 0x3c000000, 0x3c003c00, 0x0, 0x0, 0x3c003c00, 0x3c003c00, 0x0, 0x3c000000, 0x3c003c00, 0x3c003c00, 0x0, 0x3c003c00, 0x3c003c00, 0x3c00,
  0x3c000000, 0x3c003c00, 0x3c003c00, 0x0, 0x3c003c00, 0x3c003c00, 0x3c00, 0x0, 0x3c003c00, 0x3c003c00, 0x0, 0x0, 0x3c003c00, 0x3c00, 0x0, 0x0,
  0x0, 0x0, 0x3c000000, 0x3c003c00, 0x0, 0x0, 0x3c003c00, 0x3c003c00, 0x0, 0x3c000000, 0x3c003c00, 0x3c003c00, 0x0, 0x3c003c00, 0x3c003c00, 0x3c00,
  0x3c000000, 0x3c003c00, 0x3c003c00, 0x0, 0x3c003c00, 0x3c003c00, 0x3c00, 0x0, 0x3c003c00, 0x3c003c00, 0x0, 0x0, 0x3c003c00, 0x3c00, 0x0, 0x0,
  0x3c003c00, 0x0, 0x0, 0x0, 0x3c00, 0x0, 0x0, 0x0, 0x0, 0x0, 0x0, 0x0, 0x0, 0x0, 0x0, 0x0,
  0x0, 0x0, 0x0, 0x0, 0x0, 0x0, 0x0, 0x0, 0x0, 0x0, 0x0, 0x0, 0x0, 0x0, 0x0, 0x0,
  0x3c003c00, 0x0, 0x0, 0x0, 0x3c00, 0x0, 0x0, 0x0, 0x0, 0x0, 0x0, 0x0, 0x0, 0x0, 0x0, 0x0,
  0x0, 0x0, 0x0, 0x0, 0x0, 0x0, 0x0, 0x0, 0x0, 0x0, 0x0, 0x0, 0x0, 0x0, 0x0, 0x0,
  0x0, 0x0, 0x0, 0x0, 0x0, 0x0, 0x0, 0x0, 0x0, 0x0, 0x0, 0x0, 0x0, 0x0, 0x0, 0x0,
  0x0, 0x0, 0x0, 0x0, 0x0, 0x0, 0x0, 0x0, 0x0, 0x0, 0x0, 0x0, 0x0, 0x0, 0x0, 0x0,
  0x0, 0x0, 0x0, 0x0, 0x0, 0x0, 0x0, 0x0, 0x0, 0x0, 0x0, 0x0, 0x0, 0x0, 0x0, 0x0,
  0x0, 0x0, 0x0, 0x0, 0x0, 0x0, 0x0, 0x0, 0x0, 0x0, 0x0, 0x0, 0x0, 0x0, 0x0, 0x0,
  0x0, 0x0, 0x0, 0x0, 0x0, 0x0, 0x0, 0x0, 0x0, 0x0, 0x0, 0x0, 0x0, 0x0, 0x0, 0x0,
  0x0, 0x0, 0x0, 0x0, 0x0, 0x0, 0x0, 0x3c000000, 0x0, 0x0, 0x0, 0x3c003c00, 0x0, 0x0, 0x3c000000, 0x3c003c00,
  0x0, 0x0, 0x0, 0x0, 0x0, 0x0, 0x0, 0x0, 0x0, 0x0, 0x0, 0x0, 0x0, 0x0, 0x0, 0x0,
  0x0, 0x0, 0x0, 0x0, 0x0, 0x0, 0x0, 0x3c000000, 0x0, 0x0, 0x0, 0x3c003c00, 0x0, 0x0, 0x3c000000, 0x3c003c00,
  0x0, 0x0, 0x3c003c00, 0x3c003c00, 0x0, 0x3c000000, 0x3c003c00, 0x3c003c00, 0x0, 0x3c003c00, 0x3c003c00, 0x3c003c00, 0x3c000000, 0x3c003c00, 0x3c003c00, 0x3c003c00,
  0x3c003c00, 0x3c003c00, 0x3c003c00, 0x3c00, 0x3c003c00, 0x3c003c00, 0x3c003c00, 0x0, 0x3c003c00, 0x3c003c00, 0x3c00, 0x0, 0x3c003c00, 0x3c003c00, 0x0, 0x0,
  0x0, 0x0, 0x3c003c00, 0x3c003c00, 0x0, 0x3c000000, 0x3c003c00, 0x3c003c00, 0x0, 0x3c003c00, 0x3c003c00, 0x3c003c00, 0x3c000000, 0x3c003c00, 0x3c003c00, 0x3c003c00,
  0x3c003c00, 0x3c003c00, 0x3c003c00, 0x3c00, 0x3c003c00, 0x3c003c00, 0x3c003c00, 0x0, 0x3c003c00, 0x3c003c00, 0x3c00, 0x0, 0x3c003c00, 0x3c003c00, 0x0, 0x0,
  0x3c003c00, 0x3c00, 0x0, 0x0, 0x3c003c00, 0x0, 0x0, 0x0, 0x3c00, 0x0, 0x0, 0x0, 0x0, 0x0, 0x0, 0x0,
  0x0, 0x0, 0x0, 0x0, 0x0, 0x0, 0x0, 0x0, 0x0, 0x0, 0x0, 0x0, 0x0, 0x0, 0x0, 0x0,
  0x3c003c00, 0x3c00, 0x0, 0x0, 0x3c003c00, 0x0, 0x0, 0x0, 0x3c00, 0x0, 0x0, 0x0, 0x0, 0x0, 0x0, 0x0,
  0x0, 0x0, 0x0, 0x0, 0x0, 0x0, 0x0, 0x0, 0x0, 0x0, 0x0, 0x0, 0x0, 0x0, 0x0, 0x0,
  0x0, 0x0, 0x0, 0x0, 0x0, 0x0, 0x0, 0x0, 0x0, 0x0, 0x0, 0x0, 0x0, 0x0, 0x0, 0x0,
  0x0, 0x0, 0x0, 0x0, 0x0, 0x0, 0x0, 0x0, 0x0, 0x0, 0x0, 0x0, 0x0, 0x0, 0x0, 0x0,
  0x0, 0x0, 0x0, 0x0, 0x0, 0x0, 0x0, 0x0, 0x0, 0x0, 0x0, 0x0, 0x0, 0x0, 0x0, 0x0,
  0x0, 0x0, 0x0, 0x0, 0x0, 0x0, 0x0, 0x0, 0x0, 0x0, 0x0, 0x0, 0x0, 0x0, 0x0, 0x0,
  0x0, 0x0, 0x0, 0x0, 0x0, 0x0, 0x0, 0x0, 0x0, 0x0, 0x0, 0x0, 0x0, 0x0, 0x0, 0x0,
  0x0, 0x0, 0x0, 0x3c000000, 0x0, 0x0, 0x0, 0x3c003c00, 0x0, 0x0, 0x3c000000, 0x3c003c00, 0x0, 0x0, 0x3c003c00, 0x3c003c00,
  0x0, 0x0, 0x0, 0x0, 0x0, 0x0, 0x0, 0x0, 0x0, 0x0, 0x0, 0x0, 0x0, 0x0, 0x0, 0x0,
  0x0, 0x0, 0x0, 0x3c000000, 0x0, 0x0, 0x0, 0x3c003c00, 0x0, 0x0, 0x3c000000, 0x3c003c00, 0x0, 0x0, 0x3c003c00, 0x3c003c00,
  0x0, 0x3c000000, 0x3c003c00, 0x3c003c00, 0x0, 0x3c003c00, 0x3c003c00, 0x3c003c00, 0x3c000000, 0x3c003c00, 0x3c003c00, 0x3c003c00, 0x3c003c00, 0x3c003c00, 0x3c003c00, 0x3c003c00,
  0x3c003c00, 0x3c003c00, 0x3c003c00, 0x3c003c00, 0x3c003c00, 0x3c003c00, 0x3c003c00, 0x3c00, 0x3c003c00, 0x3c003c00, 0x3c003c00, 0x0, 0x3c003c00, 0x3c003c00, 0x3c00, 0x0,
  0x0, 0x3c000000, 0x3c003c00, 0x3c003c00, 0x0, 0x3c003c00, 0x3c003c00, 0x3c003c00, 0x3c000000, 0x3c003c00, 0x3c003c00, 0x3c003c00, 0x3c003c00, 0x3c003c00, 0x3c003c00, 0x3c003c00,
  0x3c003c00, 0x3c003c00, 0x3c003c00, 0x3c003c00, 0x3c003c00, 0x3c003c00, 0x3c003c00, 0x3c00, 0x3c003c00, 0x3c003c00, 0x3c003c00, 0x0, 0x3c003c00, 0x3c003c00, 0x3c00, 0x0,
  0x3c003c00, 0x3c003c00, 0x0, 0x0, 0x3c003c00, 0x3c00, 0x0, 0x0, 0x3c003c00, 0x0, 0x0, 0x0, 0x3c00, 0x0, 0x0, 0x0,
  0x0, 0x0, 0x0, 0x0, 0x0, 0x0, 0x0, 0x0, 0x0, 0x0, 0x0, 0x0, 0x0, 0x0, 0x0, 0x0,
  0x3c003c00, 0x3c003c00, 0x0, 0x0, 0x3c003c00, 0x3c00, 0x0, 0x0, 0x3c003c00, 0x0, 0x0, 0x0, 0x3c00, 0x0, 0x0, 0x0,
  0x0, 0x0, 0x0, 0x0, 0x0, 0x0, 0x0, 0x0, 0x0, 0x0, 0x0, 0x0, 0x0, 0x0, 0x0, 0x0,
  0x0, 0x0, 0x0, 0x0, 0x0, 0x0, 0x0, 0x0, 0x0, 0x0, 0x0, 0x0, 0x0, 0x0, 0x0, 0x0,
  0x0, 0x0, 0x0, 0x0, 0x0, 0x0, 0x0, 0x0, 0x0, 0x0, 0x0, 0x0, 0x0, 0x0, 0x0, 0x0,
  0x0, 0x0, 0x0, 0x0, 0x0, 0x0, 0x0, 0x0, 0x0, 0x0, 0x0, 0x0, 0x0, 0x0, 0x0, 0x0,
  0x0, 0x0, 0x0, 0x0, 0x0, 0x0, 0x0, 0x0, 0x0, 0x0, 0x0, 0x0, 0x0, 0x0, 0x0, 0x0,
  0x0, 0x0, 0x0, 0x0, 0x0, 0x0, 0x0, 0x0, 0x0, 0x0, 0x0, 0x0, 0x0, 0x0, 0x0, 0xffff0000,
  0x0, 0x0, 0x0, 0xffffffff, 0x0, 0x0, 0xffff0000, 0xffff, 0x0, 0x0, 0xffffffff, 0x0, 0x0, 0xffff0000, 0xffff, 0x0,
  0x0, 0x0, 0x0, 0x0, 0x0, 0x0, 0x0, 0x0, 0x0, 0x0, 0x0, 0x0, 0x0, 0x0, 0x0, 0xffff0000,
  0x0, 0x0, 0x0, 0xffffffff, 0x0, 0x0, 0xffff0000, 0xffff, 0x0, 0x0, 0xffffffff, 0x0, 0x0, 0xffff0000, 0xffff, 0x0,
  0x0, 0xffffffff, 0x0, 0x0, 0xffff0000, 0xffff, 0x0, 0x0, 0xffffffff, 0x0, 0x0, 0x0, 0xffff, 0x0, 0x0, 0x0,
  0x0, 0x0, 0x0, 0x0, 0x0, 0x0, 0x0, 0x0, 0x0, 0x0, 0x0, 0x0, 0x0, 0x0, 0x0, 0x0,
  0x0, 0xffffffff, 0x0, 0x0, 0xffff0000, 0xffff, 0x0, 0x0, 0xffffffff, 0x0, 0x0, 0x0, 0xffff, 0x0, 0x0, 0x0,
  0x0, 0x0, 0x0, 0x0, 0x0, 0x0, 0x0, 0x0, 0x0, 0x0, 0x0, 0x0, 0x0, 0x0, 0x0, 0x0,
  0x0, 0x0, 0x0, 0x0, 0x0, 0x0, 0x0, 0x0, 0x0, 0x0, 0x0, 0x0, 0x0, 0x0, 0x0, 0x0,
  0x0, 0x0, 0x0, 0x0, 0x0, 0x0, 0x0, 0x0, 0x0, 0x0, 0x0, 0x0, 0x0, 0x0, 0x0, 0x0,
  0x0, 0x0, 0x0, 0x0, 0x0, 0x0, 0x0, 0x0, 0x0, 0x0, 0x0, 0x0, 0x0, 0x0, 0x0, 0x0,
  0x0, 0x0, 0x0, 0x0, 0x0, 0x0, 0x0, 0x0, 0x0, 0x0, 0x0, 0x0, 0x0, 0x0, 0x0, 0x0,
  0x0, 0x0, 0x0, 0x0, 0x0, 0x0, 0x0, 0x0, 0x0, 0x0, 0x0, 0x0, 0x0, 0x0, 0x0, 0x0,
  0x0, 0x0, 0x0, 0x0, 0x0, 0x0, 0x0, 0x0, 0x0, 0x0, 0x0, 0x0, 0x0, 0x0, 0x0, 0x0,
  0x16520, 0x3020100, 0x7060504, 0x3020100, 0x16620, 0x3020100, 0x7060504, 0x3020100, 0x16530, 0x3020100, 0x7060504, 0x3020100, 0x16630, 0x3020100, 0x7060504, 0x3020100,
  0x16540, 0x3020100, 0x7060504, 0x3020100, 0x16640, 0x3020100, 0x7060504, 0x3020100, 0x16550, 0x3020100, 0x7060504, 0x3020100, 0x16650, 0x3020100, 0x7060504, 0x3020100,
  0x16560, 0x3020100, 0x7060504, 0x3020100, 0x16660, 0x3020100, 0x7060504, 0x3020100, 0x16570, 0x3020100, 0x7060504, 0x3020100, 0x16670, 0x3020100, 0x7060504, 0x3020100,
  0x16600, 0x3020100, 0x7060504, 0x3020100, 0x16700, 0x3020100, 0x7060504, 0x3020100, 0x16610, 0x3020100, 0x7060504, 0x3020100, 0x16710, 0x3020100, 0x7060504, 0x3020100,
  0x16528, 0x3020100, 0x7060504, 0x3020100, 0x16628, 0x3020100, 0x7060504, 0x3020100, 0x16538, 0x3020100, 0x7060504, 0x3020100, 0x16638, 0x3020100, 0x7060504, 0x3020100,
  0x16548, 0x3020100, 0x7060504, 0x3020100, 0x16648, 0x3020100, 0x7060504, 0x3020100, 0x16558, 0x3020100, 0x7060504, 0x3020100, 0x16658, 0x3020100, 0x7060504, 0x3020100,
  0x16568, 0x3020100, 0x7060504, 0x3020100, 0x16668, 0x3020100, 0x7060504, 0x3020100, 0x16578, 0x3020100, 0x7060504, 0x3020100, 0x16678, 0x3020100, 0x7060504, 0x3020100,
  0x16608, 0x3020100, 0x7060504, 0x3020100, 0x16708, 0x3020100, 0x7060504, 0x3020100, 0x16618, 0x3020100, 0x7060504, 0x3020100, 0x16718, 0x3020100, 0x7060504, 0x3020100,
  0x16720, 0x1000100, 0x5040302, 0x5040100, 0x16820, 0x1000100, 0x5040302, 0x5040100, 0x16730, 0x1000100, 0x5040302, 0x5040100, 0x16830, 0x1000100, 0x5040302, 0x5040100,
  0x16740, 0x1000100, 0x5040302, 0x5040100, 0x16840, 0x1000100, 0x5040302, 0x5040100, 0x16750, 0x1000100, 0x5040302, 0x5040100, 0x16850, 0x1000100, 0x5040302, 0x5040100,
  0x16760, 0x1000100, 0x5040302, 0x5040100, 0x16860, 0x1000100, 0x5040302, 0x5040100, 0x16770, 0x1000100, 0x5040302, 0x5040100, 0x16870, 0x1000100, 0x5040302, 0x5040100,
  0x16800, 0x1000100, 0x5040302, 0x5040100, 0x16900, 0x1000100, 0x5040302, 0x5040100, 0x16810, 0x1000100, 0x5040302, 0x5040100, 0x16910, 0x1000100, 0x5040302, 0x5040100,
  0x16920, 0x5040504, 0xc0c0504, 0x3020100, 0x16cc8, 0x3020100, 0x7060504, 0x3020100, 0x16cd0, 0x3020100, 0x7060504, 0x3020100, 0x16cd8, 0x3020100, 0x7060504, 0x3020100,
  0x16ce0, 0x3020100, 0x7060504, 0x3020100, 0x16ce8, 0x3020100, 0x7060504, 0x3020100, 0x16cf0, 0x3020100, 0x7060504, 0x3020100, 0x16cf8, 0x3020100, 0x7060504, 0x3020100,
  0x16d00, 0x3020100, 0x7060504, 0x3020100, 0x16d08, 0x3020100, 0x7060504, 0x3020100, 0x16d10, 0x3020100, 0x7060504, 0x3020100, 0x16d18, 0x3020100, 0x7060504, 0x3020100,
  0x16d20, 0x3020100, 0x7060504, 0x3020100, 0x16d28, 0x3020100, 0x7060504, 0x3020100, 0x16d30, 0x3020100, 0x7060504, 0x3020100, 0x16d38, 0x3020100, 0x7060504, 0x3020100,
  0x16d5e, 0x16d60, 0x16d62, 0x16d64, 0x16e5e, 0x16e60, 0x16e62, 0x16e64, 0x16d6e, 0x16d70, 0x16d72, 0x16d74, 0x16e6e, 0x16e70, 0x16e72, 0x16e74,
  0x16d7e, 0x16d80, 0x16d82, 0x16d84, 0x16e7e, 0x16e80, 0x16e82, 0x16e84, 0x16d8e, 0x16d90, 0x16d92, 0x16d94, 0x16e8e, 0x16e90, 0x16e92, 0x16e94,
  0x16d9e, 0x16da0, 0x16da2, 0x16da4, 0x16e9e, 0x16ea0, 0x16ea2, 0x16ea4, 0x16dae, 0x16db0, 0x16db2, 0x16db4, 0x16eae, 0x16eb0, 0x16eb2, 0x16eb4,
  0x16e3e, 0x16e40, 0x16e42, 0x16e44, 0x16f3e, 0x16f40, 0x16f42, 0x16f44, 0x16e4e, 0x16e50, 0x16e52, 0x16e54, 0x16f4e, 0x16f50, 0x16f52, 0x16f54,
  0x16d66, 0x16d68, 0x16d6a, 0x16d6c, 0x16e66, 0x16e68, 0x16e6a, 0x16e6c, 0x16d76, 0x16d78, 0x16d7a, 0x16d7c, 0x16e76, 0x16e78, 0x16e7a, 0x16e7c,
  0x16d86, 0x16d88, 0x16d8a, 0x16d8c, 0x16e86, 0x16e88, 0x16e8a, 0x16e8c, 0x16d96, 0x16d98, 0x16d9a, 0x16d9c, 0x16e96, 0x16e98, 0x16e9a, 0x16e9c,
  0x16da6, 0x16da8, 0x16daa, 0x16dac, 0x16ea6, 0x16ea8, 0x16eaa, 0x16eac, 0x16db6, 0x16db8, 0x16dba, 0x16dbc, 0x16eb6, 0x16eb8, 0x16eba, 0x16ebc,
  0x16e46, 0x16e48, 0x16e4a, 0x16e4c, 0x16f46, 0x16f48, 0x16f4a, 0x16f4c, 0x16e56, 0x16e58, 0x16e5a, 0x16e5c, 0x16f56, 0x16f58, 0x16f5a, 0x16f5c,
  0x16cc0, 0x16728, 0x1672a, 0x1672c, 0x1681e, 0x16828, 0x1682a, 0x1682c, 0x1672e, 0x16738, 0x1673a, 0x1673c, 0x1682e, 0x16838, 0x1683a, 0x1683c,
  0x1673e, 0x16748, 0x1674a, 0x1674c, 0x1683e, 0x16848, 0x1684a, 0x1684c, 0x1674e, 0x16758, 0x1675a, 0x1675c, 0x1684e, 0x16858, 0x1685a, 0x1685c,
  0x1675e, 0x16768, 0x1676a, 0x1676c, 0x1685e, 0x16868, 0x1686a, 0x1686c, 0x1676e, 0x16778, 0x1677a, 0x1677c, 0x1686e, 0x16878, 0x1687a, 0x1687c,
  0x167fe, 0x16808, 0x1680a, 0x1680c, 0x168fe, 0x16908, 0x1690a, 0x1690c, 0x1680e, 0x16818, 0x1681a, 0x1681c, 0x1690e, 0x16918, 0x1691a, 0x1691c,
  0x169c0, 0x16a00, 0x1691e, 0x16afe, 0x169e0, 0x16a20, 0x16ade, 0x16b1e, 0x169c2, 0x16a02, 0x16ac0, 0x16b00, 0x169e2, 0x16a22, 0x16ae0, 0x16b20,
  0x169c4, 0x16a04, 0x16ac2, 0x16b02, 0x169e4, 0x16a24, 0x16ae2, 0x16b22, 0x169c6, 0x16a06, 0x16ac4, 0x16b04, 0x169e6, 0x16a26, 0x16ae4, 0x16b24,
  0x169c8, 0x16a08, 0x16ac6, 0x16b06, 0x169e8, 0x16a28, 0x16ae6, 0x16b26, 0x169ca, 0x16a0a, 0x16ac8, 0x16b08, 0x169ea, 0x16a2a, 0x16ae8, 0x16b28,
  0x169dc, 0x16a1c, 0x16ada, 0x16b1a, 0x169fc, 0x16a3c, 0x16afa, 0x16b3a, 0x169de, 0x16a1e, 0x16adc, 0x16b1c, 0x169fe, 0x16a3e, 0x16afc, 0x16b3c,
  0x16b3e, 0x16bfe, 0x16c3e, 0x16c7e, 0x16bde, 0x16c1e, 0x16c5e, 0x16c9e, 0x16bc0, 0x16c00, 0x16c40, 0x16c80, 0x16be0, 0x16c20, 0x16c60, 0x16ca0,
  0x16bc2, 0x16c02, 0x16c42, 0x16c82, 0x16be2, 0x16c22, 0x16c62, 0x16ca2, 0x16bc4, 0x16c04, 0x16c44, 0x16c84, 0x16be4, 0x16c24, 0x16c64, 0x16ca4,
  0x16bc6, 0x16c06, 0x16c46, 0x16c86, 0x16be6, 0x16c26, 0x16c66, 0x16ca6, 0x16bc8, 0x16c08, 0x16c48, 0x16c88, 0x16be8, 0x16c28, 0x16c68, 0x16ca8,
  0x16bda, 0x16c1a, 0x16c5a, 0x16c9a, 0x16bfa, 0x16c3a, 0x16c7a, 0x16cba, 0x16bdc, 0x16c1c, 0x16c5c, 0x16c9c, 0x16bfc, 0x16c3c, 0x16c7c, 0x16cbc,
  0x16cbe, 0x16cc0, 0x16cc0, 0x16cc0, 0x16cc8, 0x16cc8, 0x16cc8, 0x16cc8, 0x16cd0, 0x16cd0, 0x16cd0, 0x16cd0, 0x16cd8, 0x16cd8, 0x16cd8, 0x16cd8,
  0x16ce0, 0x16ce0, 0x16ce0, 0x16ce0, 0x16ce8, 0x16ce8, 0x16ce8, 0x16ce8, 0x16cf0, 0x16cf0, 0x16cf0, 0x16cf0, 0x16cf8, 0x16cf8, 0x16cf8, 0x16cf8,
  0x16d00, 0x16d00, 0x16d00, 0x16d00, 0x16d08, 0x16d08, 0x16d08, 0x16d08, 0x16d10, 0x16d10, 0x16d10, 0x16d10, 0x16d18, 0x16d18, 0x16d18, 0x16d18,
  0x16d20, 0x16d20, 0x16d20, 0x16d20, 0x16d28, 0x16d28, 0x16d28, 0x16d28, 0x16d30, 0x16d30, 0x16d30, 0x16d30, 0x16d38, 0x16d38, 0x16d38, 0x16d38,
  0x16cc0, 0x16cc0, 0x16cc0, 0x16cc0, 0x16cc8, 0x16cc8, 0x16cc8, 0x16cc8, 0x16cd0, 0x16cd0, 0x16cd0, 0x16cd0, 0x16cd8, 0x16cd8, 0x16cd8, 0x16cd8,
  0x16ce0, 0x16ce0, 0x16ce0, 0x16ce0, 0x16ce8, 0x16ce8, 0x16ce8, 0x16ce8, 0x16cf0, 0x16cf0, 0x16cf0, 0x16cf0, 0x16cf8, 0x16cf8, 0x16cf8, 0x16cf8,
  0x16d00, 0x16d00, 0x16d00, 0x16d00, 0x16d08, 0x16d08, 0x16d08, 0x16d08, 0x16d10, 0x16d10, 0x16d10, 0x16d10, 0x16d18, 0x16d18, 0x16d18, 0x16d18,
  0x16d20, 0x16d20, 0x16d20, 0x16d20, 0x16d28, 0x16d28, 0x16d28, 0x16d28, 0x16d30, 0x16d30, 0x16d30, 0x16d30, 0x16d38, 0x16d38, 0x16d38, 0x16d38,
  0x16cc0, 0x16cc0, 0x16cc0, 0x16cc0, 0x16cc8, 0x16cc8, 0x16cc8, 0x16cc8, 0x16cd0, 0x16cd0, 0x16cd0, 0x16cd0, 0x16cd8, 0x16cd8, 0x16cd8, 0x16cd8,
  0x16ce0, 0x16ce0, 0x16ce0, 0x16ce0, 0x16ce8, 0x16ce8, 0x16ce8, 0x16ce8, 0x16cf0, 0x16cf0, 0x16cf0, 0x16cf0, 0x16cf8, 0x16cf8, 0x16cf8, 0x16cf8,
  0x16d00, 0x16d00, 0x16d00, 0x16d00, 0x16d08, 0x16d08, 0x16d08, 0x16d08, 0x16d10, 0x16d10, 0x16d10, 0x16d10, 0x16d18, 0x16d18, 0x16d18, 0x16d18,
  0x16d20, 0x16d20, 0x16d20, 0x16d20, 0x16d28, 0x16d28, 0x16d28, 0x16d28, 0x16d30, 0x16d30, 0x16d30, 0x16d30, 0x16d38, 0x16d38, 0x16d38, 0x16d38,
  0x16cc0, 0x16cc0, 0x3020100, 0x16500, 0x16cc8, 0x16cc8, 0x3020100, 0x16500, 0x16cd0, 0x16cd0, 0x3020100, 0x16500, 0x16cd8, 0x16cd8, 0x3020100, 0x16500,
  0x16ce0, 0x16ce0, 0x3020100, 0x16500, 0x16ce8, 0x16ce8, 0x3020100, 0x16500, 0x16cf0, 0x16cf0, 0x3020100, 0x16500, 0x16cf8, 0x16cf8, 0x3020100, 0x16500,
  0x16d00, 0x16d00, 0x3020100, 0x16500, 0x16d08, 0x16d08, 0x3020100, 0x16500, 0x16d10, 0x16d10, 0x3020100, 0x16500, 0x16d18, 0x16d18, 0x3020100, 0x16500,
  0x16d20, 0x16d20, 0x3020100, 0x16500, 0x16d28, 0x16d28, 0x3020100, 0x16500, 0x16d30, 0x16d30, 0x3020100, 0x16500, 0x16d38, 0x16d38, 0x3020100, 0x16500,
  0x16cc0, 0x16cc0, 0x3020100, 0x16500, 0x16cc8, 0x16cc8, 0x3020100, 0x16500, 0x16cd0, 0x16cd0, 0x3020100, 0x16500, 0x16cd8, 0x16cd8, 0x3020100, 0x16500,
  0x16ce0, 0x16ce0, 0x3020100, 0x16500, 0x16ce8, 0x16ce8, 0x3020100, 0x16500, 0x16cf0, 0x16cf0, 0x3020100, 0x16500, 0x16cf8, 0x16cf8, 0x3020100, 0x16500,
  0x16d00, 0x16d00, 0x3020100, 0x16500, 0x16d08, 0x16d08, 0x3020100, 0x16500, 0x16d10, 0x16d10, 0x3020100, 0x16500, 0x16d18, 0x16d18, 0x3020100, 0x16500,
  0x16d20, 0x16d20, 0x3020100, 0x16500, 0x16d28, 0x16d28, 0x3020100, 0x16500, 0x16d30, 0x16d30, 0x3020100, 0x16500, 0x16d38, 0x16d38, 0x3020100, 0x16500,
  0x16f5c, 0x16f64, 0x3020100, 0x16500, 0x1705c, 0x17064, 0x3020100, 0x16500, 0x16f6c, 0x16f74, 0x3020100, 0x16500, 0x1706c, 0x17074, 0x3020100, 0x16500,
  0x16f7c, 0x16f84, 0x3020100, 0x16500, 0x1707c, 0x17084, 0x3020100, 0x16500, 0x16f8c, 0x16f94, 0x3020100, 0x16500, 0x1708c, 0x17094, 0x3020100, 0x16500,
  0x16f9c, 0x16fa4, 0x3020100, 0x16500, 0x1709c, 0x170a4, 0x3020100, 0x16500, 0x16fac, 0x16fb4, 0x3020100, 0x16500, 0x170ac, 0x170b4, 0x3020100, 0x16500,
  0x1703c, 0x17044, 0x3020100, 0x16500, 0x1713c, 0x17144, 0x3020100, 0x16500, 0x1704c, 0x17054, 0x3020100, 0x16500, 0x1714c, 0x17154, 0x3020100, 0x16500,
  0x1715c, 0x16cc0, 0x5040504, 0x16500, 0x16cc8, 0x16cc8, 0x3020100, 0x16500, 0x16cd0, 0x16cd0, 0x3020100, 0x16500, 0x16cd8, 0x16cd8, 0x3020100, 0x16500,
  0x16ce0, 0x16ce0, 0x3020100, 0x16500, 0x16ce8, 0x16ce8, 0x3020100, 0x16500, 0x16cf0, 0x16cf0, 0x3020100, 0x16500, 0x16cf8, 0x16cf8, 0x3020100, 0x16500,
  0x16d00, 0x16d00, 0x3020100, 0x16500, 0x16d08, 0x16d08, 0x3020100, 0x16500, 0x16d10, 0x16d10, 0x3020100, 0x16500, 0x16d18, 0x16d18, 0x3020100, 0x16500,
  0x16d20, 0x16d20, 0x3020100, 0x16500, 0x16d28, 0x16d28, 0x3020100, 0x16500, 0x16d30, 0x16d30, 0x3020100, 0x16500, 0x16d38, 0x16d38, 0x3020100, 0x16500,
  0x16580, 0x3020100, 0x7060504, 0x3020100, 0x16680, 0x3020100, 0x7060504, 0x3020100, 0x16590, 0x3020100, 0x7060504, 0x3020100, 0x16690, 0x3020100, 0x7060504, 0x3020100,
  0x165a0, 0x3020100, 0x7060504, 0x3020100, 0x166a0, 0x3020100, 0x7060504, 0x3020100, 0x165b0, 0x3020100, 0x7060504, 0x3020100, 0x166b0, 0x3020100, 0x7060504, 0x3020100,
  0x165c0, 0x3020100, 0x7060504, 0x3020100, 0x166c0, 0x3020100, 0x7060504, 0x3020100, 0x165d0, 0x3020100, 0x7060504, 0x3020100, 0x166d0, 0x3020100, 0x7060504, 0x3020100,
  0x165e0, 0x3020100, 0x7060504, 0x3020100, 0x166e0, 0x3020100, 0x7060504, 0x3020100, 0x165f0, 0x3020100, 0x7060504, 0x3020100, 0x166f0, 0x3020100, 0x7060504, 0x3020100,
  0x16588, 0x3020100, 0x7060504, 0x3020100, 0x16688, 0x3020100, 0x7060504, 0x3020100, 0x16598, 0x3020100, 0x7060504, 0x3020100, 0x16698, 0x3020100, 0x7060504, 0x3020100,
  0x165a8, 0x3020100, 0x7060504, 0x3020100, 0x166a8, 0x3020100, 0x7060504, 0x3020100, 0x165b8, 0x3020100, 0x7060504, 0x3020100, 0x166b8, 0x3020100, 0x7060504, 0x3020100,
  0x165c8, 0x3020100, 0x7060504, 0x3020100, 0x166c8, 0x3020100, 0x7060504, 0x3020100, 0x165d8, 0x3020100, 0x7060504, 0x3020100, 0x166d8, 0x3020100, 0x7060504, 0x3020100,
  0x165e8, 0x3020100, 0x7060504, 0x3020100, 0x166e8, 0x3020100, 0x7060504, 0x3020100, 0x165f8, 0x3020100, 0x7060504, 0x3020100, 0x166f8, 0x3020100, 0x7060504, 0x3020100,
  0x16780, 0x1000100, 0x5040302, 0x5040100, 0x16880, 0x1000100, 0x5040302, 0x5040100, 0x16790, 0x1000100, 0x5040302, 0x5040100, 0x16890, 0x1000100, 0x5040302, 0x5040100,
  0x167a0, 0x1000100, 0x5040302, 0x5040100, 0x168a0, 0x1000100, 0x5040302, 0x5040100, 0x167b0, 0x1000100, 0x5040302, 0x5040100, 0x168b0, 0x1000100, 0x5040302, 0x5040100,
  0x167c0, 0x1000100, 0x5040302, 0x5040100, 0x168c0, 0x1000100, 0x5040302, 0x5040100, 0x167d0, 0x1000100, 0x5040302, 0x5040100, 0x168d0, 0x1000100, 0x5040302, 0x5040100,
  0x167e0, 0x1000100, 0x5040302, 0x5040100, 0x168e0, 0x1000100, 0x5040302, 0x5040100, 0x167f0, 0x1000100, 0x5040302, 0x5040100, 0x168f0, 0x1000100, 0x5040302, 0x5040100,
  0x16cc0, 0x3020100, 0x7060504, 0x3020100, 0x16cc8, 0x3020100, 0x7060504, 0x3020100, 0x16cd0, 0x3020100, 0x7060504, 0x3020100, 0x16cd8, 0x3020100, 0x7060504, 0x3020100,
  0x16ce0, 0x3020100, 0x7060504, 0x3020100, 0x16ce8, 0x3020100, 0x7060504, 0x3020100, 0x16cf0, 0x3020100, 0x7060504, 0x3020100, 0x16cf8, 0x3020100, 0x7060504, 0x3020100,
  0x16d00, 0x3020100, 0x7060504, 0x3020100, 0x16d08, 0x3020100, 0x7060504, 0x3020100, 0x16d10, 0x3020100, 0x7060504, 0x3020100, 0x16d18, 0x3020100, 0x7060504, 0x3020100,
  0x16d20, 0x3020100, 0x7060504, 0x3020100, 0x16d28, 0x3020100, 0x7060504, 0x3020100, 0x16d30, 0x3020100, 0x7060504, 0x3020100, 0x16d38, 0x3020100, 0x7060504, 0x3020100,
  0x16dbe, 0x16dc0, 0x16dc2, 0x16dc4, 0x16ebe, 0x16ec0, 0x16ec2, 0x16ec4, 0x16dce, 0x16dd0, 0x16dd2, 0x16dd4, 0x16ece, 0x16ed0, 0x16ed2, 0x16ed4,
  0x16dde, 0x16de0, 0x16de2, 0x16de4, 0x16ede, 0x16ee0, 0x16ee2, 0x16ee4, 0x16dee, 0x16df0, 0x16df2, 0x16df4, 0x16eee, 0x16ef0, 0x16ef2, 0x16ef4,
  0x16dfe, 0x16e00, 0x16e02, 0x16e04, 0x16efe, 0x16f00, 0x16f02, 0x16f04, 0x16e0e, 0x16e10, 0x16e12, 0x16e14, 0x16f0e, 0x16f10, 0x16f12, 0x16f14,
  0x16e1e, 0x16e20, 0x16e22, 0x16e24, 0x16f1e, 0x16f20, 0x16f22, 0x16f24, 0x16e2e, 0x16e30, 0x16e32, 0x16e34, 0x16f2e, 0x16f30, 0x16f32, 0x16f34,
  0x16dc6, 0x16dc8, 0x16dca, 0x16dcc, 0x16ec6, 0x16ec8, 0x16eca, 0x16ecc, 0x16dd6, 0x16dd8, 0x16dda, 0x16ddc, 0x16ed6, 0x16ed8, 0x16eda, 0x16edc,
  0x16de6, 0x16de8, 0x16dea, 0x16dec, 0x16ee6, 0x16ee8, 0x16eea, 0x16eec, 0x16df6, 0x16df8, 0x16dfa, 0x16dfc, 0x16ef6, 0x16ef8, 0x16efa, 0x16efc,
  0x16e06, 0x16e08, 0x16e0a, 0x16e0c, 0x16f06, 0x16f08, 0x16f0a, 0x16f0c, 0x16e16, 0x16e18, 0x16e1a, 0x16e1c, 0x16f16, 0x16f18, 0x16f1a, 0x16f1c,
  0x16e26, 0x16e28, 0x16e2a, 0x16e2c, 0x16f26, 0x16f28, 0x16f2a, 0x16f2c, 0x16e36, 0x16e38, 0x16e3a, 0x16e3c, 0x16f36, 0x16f38, 0x16f3a, 0x16f3c,
  0x1677e, 0x16788, 0x1678a, 0x1678c, 0x1687e, 0x16888, 0x1688a, 0x1688c, 0x1678e, 0x16798, 0x1679a, 0x1679c, 0x1688e, 0x16898, 0x1689a, 0x1689c,
  0x1679e, 0x167a8, 0x167aa, 0x167ac, 0x1689e, 0x168a8, 0x168aa, 0x168ac, 0x167ae, 0x167b8, 0x167ba, 0x167bc, 0x168ae, 0x168b8, 0x168ba, 0x168bc,
  0x167be, 0x167c8, 0x167ca, 0x167cc, 0x168be, 0x168c8, 0x168ca, 0x168cc, 0x167ce, 0x167d8, 0x167da, 0x167dc, 0x168ce, 0x168d8, 0x168da, 0x168dc,
  0x167de, 0x167e8, 0x167ea, 0x167ec, 0x168de, 0x168e8, 0x168ea, 0x168ec, 0x167ee, 0x167f8, 0x167fa, 0x167fc, 0x168ee, 0x168f8, 0x168fa, 0x168fc,
  0x169cc, 0x16a0c, 0x16aca, 0x16b0a, 0x169ec, 0x16a2c, 0x16aea, 0x16b2a, 0x169ce, 0x16a0e, 0x16acc, 0x16b0c, 0x169ee, 0x16a2e, 0x16aec, 0x16b2c,
  0x169d0, 0x16a10, 0x16ace, 0x16b0e, 0x169f0, 0x16a30, 0x16aee, 0x16b2e, 0x169d2, 0x16a12, 0x16ad0, 0x16b10, 0x169f2, 0x16a32, 0x16af0, 0x16b30,
  0x169d4, 0x16a14, 0x16ad2, 0x16b12, 0x169f4, 0x16a34, 0x16af2, 0x16b32, 0x169d6, 0x16a16, 0x16ad4, 0x16b14, 0x169f6, 0x16a36, 0x16af4, 0x16b34,
  0x169d8, 0x16a18, 0x16ad6, 0x16b16, 0x169f8, 0x16a38, 0x16af6, 0x16b36, 0x169da, 0x16a1a, 0x16ad8, 0x16b18, 0x169fa, 0x16a3a, 0x16af8, 0x16b38,
  0x16bca, 0x16c0a, 0x16c4a, 0x16c8a, 0x16bea, 0x16c2a, 0x16c6a, 0x16caa, 0x16bcc, 0x16c0c, 0x16c4c, 0x16c8c, 0x16bec, 0x16c2c, 0x16c6c, 0x16cac,
  0x16bce, 0x16c0e, 0x16c4e, 0x16c8e, 0x16bee, 0x16c2e, 0x16c6e, 0x16cae, 0x16bd0, 0x16c10, 0x16c50, 0x16c90, 0x16bf0, 0x16c30, 0x16c70, 0x16cb0,
  0x16bd2, 0x16c12, 0x16c52, 0x16c92, 0x16bf2, 0x16c32, 0x16c72, 0x16cb2, 0x16bd4, 0x16c14, 0x16c54, 0x16c94, 0x16bf4, 0x16c34, 0x16c74, 0x16cb4,
  0x16bd6, 0x16c16, 0x16c56, 0x16c96, 0x16bf6, 0x16c36, 0x16c76, 0x16cb6, 0x16bd8, 0x16c18, 0x16c58, 0x16c98, 0x16bf8, 0x16c38, 0x16c78, 0x16cb8,
  0x16cc0, 0x16cc0, 0x16cc0, 0x16cc0, 0x16cc8, 0x16cc8, 0x16cc8, 0x16cc8, 0x16cd0, 0x16cd0, 0x16cd0, 0x16cd0, 0x16cd8, 0x16cd8, 0x16cd8, 0x16cd8,
  0x16ce0, 0x16ce0, 0x16ce0, 0x16ce0, 0x16ce8, 0x16ce8, 0x16ce8, 0x16ce8, 0x16cf0, 0x16cf0, 0x16cf0, 0x16cf0, 0x16cf8, 0x16cf8, 0x16cf8, 0x16cf8,
  0x16d00, 0x16d00, 0x16d00, 0x16d00, 0x16d08, 0x16d08, 0x16d08, 0x16d08, 0x16d10, 0x16d10, 0x16d10, 0x16d10, 0x16d18, 0x16d18, 0x16d18, 0x16d18,
  0x16d20, 0x16d20, 0x16d20, 0x16d20, 0x16d28, 0x16d28, 0x16d28, 0x16d28, 0x16d30, 0x16d30, 0x16d30, 0x16d30, 0x16d38, 0x16d38, 0x16d38, 0x16d38,
  0x16cc0, 0x16cc0, 0x16cc0, 0x16cc0, 0x16cc8, 0x16cc8, 0x16cc8, 0x16cc8, 0x16cd0, 0x16cd0, 0x16cd0, 0x16cd0, 0x16cd8, 0x16cd8, 0x16cd8, 0x16cd8,
  0x16ce0, 0x16ce0, 0x16ce0, 0x16ce0, 0x16ce8, 0x16ce8, 0x16ce8, 0x16ce8, 0x16cf0, 0x16cf0, 0x16cf0, 0x16cf0, 0x16cf8, 0x16cf8, 0x16cf8, 0x16cf8,
  0x16d00, 0x16d00, 0x16d00, 0x16d00, 0x16d08, 0x16d08, 0x16d08, 0x16d08, 0x16d10, 0x16d10, 0x16d10, 0x16d10, 0x16d18, 0x16d18, 0x16d18, 0x16d18,
  0x16d20, 0x16d20, 0x16d20, 0x16d20, 0x16d28, 0x16d28, 0x16d28, 0x16d28, 0x16d30, 0x16d30, 0x16d30, 0x16d30, 0x16d38, 0x16d38, 0x16d38, 0x16d38,
  0x16cc0, 0x16cc0, 0x16cc0, 0x16cc0, 0x16cc8, 0x16cc8, 0x16cc8, 0x16cc8, 0x16cd0, 0x16cd0, 0x16cd0, 0x16cd0, 0x16cd8, 0x16cd8, 0x16cd8, 0x16cd8,
  0x16ce0, 0x16ce0, 0x16ce0, 0x16ce0, 0x16ce8, 0x16ce8, 0x16ce8, 0x16ce8, 0x16cf0, 0x16cf0, 0x16cf0, 0x16cf0, 0x16cf8, 0x16cf8, 0x16cf8, 0x16cf8,
  0x16d00, 0x16d00, 0x16d00, 0x16d00, 0x16d08, 0x16d08, 0x16d08, 0x16d08, 0x16d10, 0x16d10, 0x16d10, 0x16d10, 0x16d18, 0x16d18, 0x16d18, 0x16d18,
  0x16d20, 0x16d20, 0x16d20, 0x16d20, 0x16d28, 0x16d28, 0x16d28, 0x16d28, 0x16d30, 0x16d30, 0x16d30, 0x16d30, 0x16d38, 0x16d38, 0x16d38, 0x16d38,
  0x16cc0, 0x16cc0, 0x3020100, 0x16500, 0x16cc8, 0x16cc8, 0x3020100, 0x16500, 0x16cd0, 0x16cd0, 0x3020100, 0x16500, 0x16cd8, 0x16cd8, 0x3020100, 0x16500,
  0x16ce0, 0x16ce0, 0x3020100, 0x16500, 0x16ce8, 0x16ce8, 0x3020100, 0x16500, 0x16cf0, 0x16cf0, 0x3020100, 0x16500, 0x16cf8, 0x16cf8, 0x3020100, 0x16500,
  0x16d00, 0x16d00, 0x3020100, 0x16500, 0x16d08, 0x16d08, 0x3020100, 0x16500, 0x16d10, 0x16d10, 0x3020100, 0x16500, 0x16d18, 0x16d18, 0x3020100, 0x16500,
  0x16d20, 0x16d20, 0x3020100, 0x16500, 0x16d28, 0x16d28, 0x3020100, 0x16500, 0x16d30, 0x16d30, 0x3020100, 0x16500, 0x16d38, 0x16d38, 0x3020100, 0x16500,
  0x16cc0, 0x16cc0, 0x3020100, 0x16500, 0x16cc8, 0x16cc8, 0x3020100, 0x16500, 0x16cd0, 0x16cd0, 0x3020100, 0x16500, 0x16cd8, 0x16cd8, 0x3020100, 0x16500,
  0x16ce0, 0x16ce0, 0x3020100, 0x16500, 0x16ce8, 0x16ce8, 0x3020100, 0x16500, 0x16cf0, 0x16cf0, 0x3020100, 0x16500, 0x16cf8, 0x16cf8, 0x3020100, 0x16500,
  0x16d00, 0x16d00, 0x3020100, 0x16500, 0x16d08, 0x16d08, 0x3020100, 0x16500, 0x16d10, 0x16d10, 0x3020100, 0x16500, 0x16d18, 0x16d18, 0x3020100, 0x16500,
  0x16d20, 0x16d20, 0x3020100, 0x16500, 0x16d28, 0x16d28, 0x3020100, 0x16500, 0x16d30, 0x16d30, 0x3020100, 0x16500, 0x16d38, 0x16d38, 0x3020100, 0x16500,
  0x16fbc, 0x16fc4, 0x3020100, 0x16500, 0x170bc, 0x170c4, 0x3020100, 0x16500, 0x16fcc, 0x16fd4, 0x3020100, 0x16500, 0x170cc, 0x170d4, 0x3020100, 0x16500,
  0x16fdc, 0x16fe4, 0x3020100, 0x16500, 0x170dc, 0x170e4, 0x3020100, 0x16500, 0x16fec, 0x16ff4, 0x3020100, 0x16500, 0x170ec, 0x170f4, 0x3020100, 0x16500,
  0x16ffc, 0x17004, 0x3020100, 0x16500, 0x170fc, 0x17104, 0x3020100, 0x16500, 0x1700c, 0x17014, 0x3020100, 0x16500, 0x1710c, 0x17114, 0x3020100, 0x16500,
  0x1701c, 0x17024, 0x3020100, 0x16500, 0x1711c, 0x17124, 0x3020100, 0x16500, 0x1702c, 0x17034, 0x3020100, 0x16500, 0x1712c, 0x17134, 0x3020100, 0x16500,
  0x16cc0, 0x16cc0, 0x3020100, 0x16500, 0x16cc8, 0x16cc8, 0x3020100, 0x16500, 0x16cd0, 0x16cd0, 0x3020100, 0x16500, 0x16cd8, 0x16cd8, 0x3020100, 0x16500,
  0x16ce0, 0x16ce0, 0x3020100, 0x16500, 0x16ce8, 0x16ce8, 0x3020100, 0x16500, 0x16cf0, 0x16cf0, 0x3020100, 0x16500, 0x16cf8, 0x16cf8, 0x3020100, 0x16500,
  0x16d00, 0x16d00, 0x3020100, 0x16500, 0x16d08, 0x16d08, 0x3020100, 0x16500, 0x16d10, 0x16d10, 0x3020100, 0x16500, 0x16d18, 0x16d18, 0x3020100, 0x16500,
  0x16d20, 0x16d20, 0x3020100, 0x16500, 0x16d28, 0x16d28, 0x3020100, 0x16500, 0x16d30, 0x16d30, 0x3020100, 0x16500, 0x16d38, 0x16d38, 0x3020100, 0x16500,
  0x0, 0x0, 0x0, 0x0, 0x0, 0x0, 0x0, 0x0, 0x0, 0x0, 0x0, 0x0, 0x0, 0x0, 0x0, 0x0,
  0x0, 0x0, 0x0, 0x0, 0x0, 0x0, 0x0, 0x0, 0x0, 0x0, 0x0, 0x0, 0x0, 0x0, 0x0, 0x0,
  0x0, 0x0, 0x0, 0x0, 0x0, 0x0, 0x0, 0x0, 0x0, 0x0, 0x0, 0x0, 0x0, 0x0, 0x0, 0x0,
  0x0, 0x0, 0x0, 0x0, 0x0, 0x0, 0x0, 0x0, 0x0, 0x0, 0x0, 0x0, 0x0, 0x0, 0x0, 0x0,
  0x0, 0x0, 0x0, 0x0, 0x0, 0x0, 0x0, 0x0, 0x0, 0x0, 0x0, 0x0, 0x0, 0x0, 0x0, 0x0,
  0x0, 0x0, 0x0, 0x0, 0x0, 0x0, 0x0, 0x0, 0x0, 0x0, 0x0, 0x0, 0x0, 0x0, 0x0, 0x0,
  0x0, 0x0, 0x0, 0x0, 0x0, 0x0, 0x0, 0x0, 0x0, 0x0, 0x0, 0x0, 0x0, 0x0, 0x0, 0x0,
  0x0, 0x0, 0x0, 0x0, 0x0, 0x0, 0x0, 0x0, 0x0, 0x0, 0x0, 0x0, 0x0, 0x0, 0x0, 0x0,
  0x0, 0x0, 0x0, 0x0, 0x0, 0x0, 0x0, 0x0, 0x0, 0x0, 0x0, 0x0, 0x0, 0x0, 0x0, 0x0,
  0x0, 0x0, 0x0, 0x0, 0x0, 0x0, 0x0, 0x0, 0x0, 0x0, 0x0, 0x0, 0x0, 0x0, 0x0, 0x0,
  0x0, 0x0, 0x0, 0x0, 0x0, 0x0, 0x0, 0x0, 0x0, 0x0, 0x0, 0x0, 0x0, 0x0, 0x0, 0x0,
  0x0, 0x0, 0x0, 0x0, 0x0, 0x0, 0x0, 0x0, 0x0, 0x0, 0x0, 0x0, 0x0, 0x0, 0x0, 0x0,
  0x0, 0x0, 0x0, 0x0, 0x0, 0x0, 0x0, 0x0, 0x0, 0x0, 0x0, 0x0, 0x0, 0x0, 0x0, 0x0,
  0x0, 0x0, 0x0, 0x0, 0x0, 0x0, 0x0, 0x0, 0x0, 0x0, 0x0, 0x0, 0x0, 0x0, 0x0, 0x0,
  0x0, 0x0, 0x0, 0x0, 0x0, 0x0, 0x0, 0x0, 0x0, 0x0, 0x0, 0x0, 0x0, 0x0, 0x0, 0x0,
  0x0, 0x0, 0x0, 0x0, 0x0, 0x0, 0x0, 0x0, 0x0, 0x0, 0x0, 0x0, 0x0, 0x0, 0x0, 0x0
};

__global__ void __launch_bounds__(1024) mega(Args) {
  asm volatile(
    "s_load_dwordx16 s[4:19], s[0:1], 0\n"
    "s_load_dwordx16 s[20:35], s[0:1], 64\n"
    "v_readfirstlane_b32 s37, v0\n"
    "s_getpc_b64 s[40:41]\n"
    "K0:\n"
    "s_lshr_b32 s37, s37, 6\n"
    "s_cmpk_ge_u32 s2, 192\n"
    "s_cselect_b32 s42, 16, 0\n"
    "s_add_u32 s42, s42, s37\n"
    "s_lshl2_add_u32 s40, s42, s40\n"
    "s_add_u32 s40, s40, TBL1-K0\n"
    "s_mov_b32 s39, 0x100000\n"
    "s_setpc_b64 s[40:41]\n"
    "TBL1:\n"
    "s_branch GA0\n"
    "s_branch GA0\n"
    "s_branch GA0\n"
    "s_branch GA0\n"
    "s_branch GA0\n"
    "s_branch GA0\n"
    "s_branch S0\n"
    "s_branch S0\n"
    "s_branch S0\n"
    "s_branch S0\n"
    "s_branch S0\n"
    "s_branch S0\n"
    "s_branch MD0\n"
    "s_branch AUX1\n"
    "s_branch AUX1\n"
    "s_branch AUX0\n"
    "s_branch SIL0\n"
    "s_branch SIL0\n"
    "s_branch SIL0\n"
    "s_branch SIL0\n"
    "s_branch SIL0\n"
    "s_branch MD0\n"
    "s_branch S0\n"
    "s_branch S0\n"
    "s_branch S0\n"
    "s_branch S0\n"
    "s_branch S0\n"
    "s_branch S0\n"
    "s_branch M1\n"
    "s_branch M1\n"
    "s_branch M1\n"
    "s_branch AUX0\n"
    "AUX0:\n"
    "s_setprio 3\n"
    "s_lshr_b32 s42, s2, 3\n"
    "s_mul_i32 s42, s42, 7\n"
    "s_add_u32 s42, s42, 7\n"
    "v_and_b32 v1, 7, v0\n"
    "v_add_u32 v1, s42, v1\n"
    "v_min_u32 v1, (END-K0)/128, v1\n"
    "v_lshlrev_b32 v1, 7, v1\n"
    "s_mov_b32 m0, 148480\n"
    "s_mov_b64 exec, 0x7f\n"
    "global_load_lds_dword v1, s[40:41]\n"
    "s_mov_b64 exec, -1\n"
    "v_and_b32 v1, 63, v0\n"
    "v_lshlrev_b32 v1, 3, v1\n"
    "v_mov_b32 v4, 0\n"
    "v_mov_b32 v5, 0\n"
    "v_add_u32 v1, 147840, v1\n"
    "ds_write_b64 v1, v[4:5]\n"
    "s_setprio 0\n"
    "AUXT:\n"
    "s_cmpk_ge_u32 s2, 192\n"
    "s_cbranch_scc1 M1\n"
    "s_branch AUX1\n"
    "S0:\n"
    "s_setprio 0\n"
    "v_bfe_u32 v2, v0, 4, 2\n"
    "v_and_b32 v3, 15, v0\n"
    "v_mul_u32_u24 v2, 0x900, v2\n"
    "s_mul_i32 s42, s2, 48\n"
    "s_add_u32 s42, s42, s37\n"
    "s_mul_i32 s42, s42, 192\n"
    "v_mad_u32_u24 v1, v3, 12, v2\n"
    "v_add_u32 v1, s42, v1\n"
    "s_waitcnt lgkmcnt(0)\n"
    "global_load_dwordx3 v[68:70], v1, s[4:5] offset:-1152 nt\n"
    "global_load_dwordx3 v[72:74], v1, s[4:5] nt\n"
    "s_setprio 0\n"
    "s_load_dword s36, s[20:21], 0\n"
    "s_branch S1\n"
    "GA0:\n"
    "s_and_b32 s44, s2, 7\n"
    "s_lshr_b32 s45, s2, 3\n"
    "s_mul_i32 s44, s44, 24\n"
    "s_add_u32 s38, s44, s45\n"
    "s_lshr_b32 s42, s37, 1\n"
    "s_mul_i32 s43, s38, 3\n"
    "s_add_u32 s43, s43, s42\n"
    "s_lshl_b32 s44, s43, 8\n"
    "s_lshl_b32 s45, s43, 1\n"
    "s_and_b32 s45, s45, -4\n"
    "v_and_b32 v1, 63, v0\n"
    "v_lshl_add_u32 v2, v1, 2, s44\n"
    "s_waitcnt lgkmcnt(0)\n"
    "global_load_dword v4, v2, s[6:7] nt\n"
    "global_load_dword v5, v2, s[8:9] nt\n"
    "s_memrealtime s[96:97]\n"
    "s_load_dword s46, s[10:11], s45\n"
    "global_load_dword v6, v2, s[12:13] nt\n"
    "global_load_dword v7, v2, s[14:15] nt\n"
    "s_load_dword s36, s[20:21], 0\n"
    "s_branch GA1\n"
    "SIL0:\n"
    "v_min_u32 v1, 287, v0\n"
    "v_lshlrev_b32 v1, 2, v1\n"
    "s_waitcnt lgkmcnt(0)\n"
    "global_load_dword v4, v1, s[18:19]\n"
    "s_load_dword s36, s[20:21], 0\n"
    "s_branch SIL1\n"
    "MD0:\n"
    "s_setprio 0\n"
    "s_mul_i32 s43, s2, 19\n"
    "s_lshr_b32 s43, s43, 9\n"
    "s_mul_i32 s44, s43, 27\n"
    "s_sub_u32 s44, s2, s44\n"
    "s_mul_i32 s45, s43, 442368\n"
    "s_lshl_b32 s46, s44, 7\n"
    "s_add_u32 s45, s45, s46\n"
    "v_bfe_u32 v1, v0, 3, 3\n"
    "v_and_b32 v2, 2, v1\n"
    "v_mul_u32_u24 v1, 0x1b00, v1\n"
    "v_xor_b32 v2, v2, v0\n"
    "v_and_b32 v2, 7, v2\n"
    "v_lshl_add_u32 v1, v2, 4, v1\n"
    "v_add_u32 v1, s45, v1\n"
    "v_xor_b32 v2, 64, v1\n"
    "v_add_u32 v2, 55296, v2\n"
    "s_lshl_b32 s46, s43, 7\n"
    "v_and_b32 v3, 31, v0\n"
    "v_lshl_add_u32 v5, v3, 2, s46\n"
    "s_mov_b32 m0, 138240\n"
    "s_cmpk_ge_u32 s2, 243\n"
    "s_waitcnt lgkmcnt(0)\n"
    "s_memrealtime s[96:97]\n"
    "s_cbranch_scc1 MD0e\n"
    "global_load_dword v4, v5, s[18:19]\n"
    "global_load_lds_dwordx4 v1, s[16:17] nt\n"
    "s_add_u32 m0, m0, 1024\n"
    "v_add_u32 v1, 110592, v1\n"
    "global_load_lds_dwordx4 v2, s[16:17] nt\n"
    "s_add_u32 m0, m0, 1024\n"
    "v_add_u32 v2, 110592, v2\n"
    "global_load_lds_dwordx4 v1, s[16:17] nt\n"
    "s_add_u32 m0, m0, 1024\n"
    "v_add_u32 v1, 110592, v1\n"
    "global_load_lds_dwordx4 v2, s[16:17] nt\n"
    "s_add_u32 m0, m0, 1024\n"
    "v_add_u32 v2, 110592, v2\n"
    "global_load_lds_dwordx4 v1, s[16:17] nt\n"
    "s_add_u32 m0, m0, 1024\n"
    "v_add_u32 v1, 110592, v1\n"
    "global_load_lds_dwordx4 v2, s[16:17] nt\n"
    "s_add_u32 m0, m0, 1024\n"
    "v_add_u32 v2, 110592, v2\n"
    "global_load_lds_dwordx4 v1, s[16:17] nt\n"
    "s_add_u32 m0, m0, 1024\n"
    "s_nop 0\n"
    "global_load_lds_dwordx4 v2, s[16:17] nt\n"
    "MD0e:\n"
    "v_mov_b32 v6, 148448\n"
    "s_waitcnt lgkmcnt(0)\n"
    "v_mov_b32 v7, s96\n"
    "ds_write_b32 v6, v7\n"
    "s_waitcnt lgkmcnt(0)\n"
    "s_load_dword s36, s[20:21], 0\n"
    "s_branch MD1\n"
    "ABORT:\n"
    "s_endpgm\n"
    "S1:\n"
    "s_setprio 3\n"
    "s_barrier\n"
    "v_mov_b32 v14, 147968\n"
    "v_mov_b32 v15, 1\n"
    "s_cmpk_lt_u32 s2, 192\n"
    "s_cbranch_scc1 KSP\n"
    "S2:\n"
    "s_mul_i32 s42, s2, 12416\n"
    "s_add_u32 s42, s42, 7334912\n"
    "v_lshl_add_u32 v75, v0, 4, s42\n"
    "v_and_b32 v76, -4, v0\n"
    "v_add_u32 v76, 147072, v76\n"
    "v_and_b32 v86, 3, v0\n"
    "v_and_b32 v67, 60, v0\n"
    "v_lshl_add_u32 v86, v86, 6, v67\n"
    "v_and_b32 v77, 31, v0\n"
    "v_lshlrev_b32 v77, 4, v77\n"
    "v_add_u32 v77, 147456, v77\n"
    "s_mov_b32 s43, 0x3f803f80\n"
    "s_movk_i32 s44, 6144\n"
    "s_setprio 3\n"
    "s_waitcnt vmcnt(1)\n"
    "ds_bpermute_b32 v108, v86, v68\n"
    "ds_bpermute_b32 v109, v86, v69\n"
    "ds_bpermute_b32 v110, v86, v70\n"
    "v_dot2_f32_bf16 v80, v68, s43, 0\n"
    "v_dot2_f32_bf16 v81, v68, v68, 0\n"
    "v_dot2_f32_bf16 v80, v69, s43, v80\n"
    "v_dot2_f32_bf16 v81, v69, v69, v81\n"
    "v_dot2_f32_bf16 v80, v70, s43, v80\n"
    "v_dot2_f32_bf16 v81, v70, v70, v81\n"
    "s_waitcnt vmcnt(0)\n"
    "ds_bpermute_b32 v112, v86, v72\n"
    "ds_bpermute_b32 v113, v86, v73\n"
    "ds_bpermute_b32 v114, v86, v74\n"
    "v_dot2_f32_bf16 v82, v72, s43, 0\n"
    "v_dot2_f32_bf16 v83, v72, v72, 0\n"
    "v_dot2_f32_bf16 v82, v73, s43, v82\n"
    "v_dot2_f32_bf16 v83, v73, v73, v83\n"
    "v_dot2_f32_bf16 v82, v74, s43, v82\n"
    "v_dot2_f32_bf16 v83, v74, v74, v83\n"
    "v_add_f32_dpp v84, v80, v80 row_ror:8 row_mask:0xf bank_mask:0x3\n"
    "v_add_f32_dpp v85, v81, v81 row_ror:8 row_mask:0xf bank_mask:0x3\n"
    "s_nop 0\n"
    "v_add_f32_dpp v84, v82, v82 row_ror:8 row_mask:0xf bank_mask:0xc\n"
    "v_add_f32_dpp v85, v83, v83 row_ror:8 row_mask:0xf bank_mask:0xc\n"
    "s_nop 0\n"
    "v_add_f32_dpp v80, v84, v84 row_half_mirror row_mask:0xf bank_mask:0x5\n"
    "s_nop 1\n"
    "v_add_f32_dpp v80, v85, v85 row_half_mirror row_mask:0xf bank_mask:0xa\n"
    "s_nop 1\n"
    "v_add_f32_dpp v80, v80, v80 quad_perm:[1,0,3,2] row_mask:0xf bank_mask:0xf\n"
    "s_nop 1\n"
    "v_add_f32_dpp v80, v80, v80 quad_perm:[2,3,0,1] row_mask:0xf bank_mask:0xf\n"
    "ds_write_b32 v76, v80\n"
    "s_mov_b64 exec, 1\n"
    "ds_add_u32 v14, v15 offset:0\n"
    "s_mov_b64 exec, -1\n"
    "s_waitcnt lgkmcnt(0)\n"
    "v_lshlrev_b32 v88, 16, v108\n"
    "v_lshlrev_b32 v89, 16, v112\n"
    "v_and_b32 v90, 0xffff0000, v108\n"
    "v_and_b32 v91, 0xffff0000, v112\n"
    "v_lshlrev_b32 v92, 16, v109\n"
    "v_lshlrev_b32 v93, 16, v113\n"
    "v_and_b32 v94, 0xffff0000, v109\n"
    "v_and_b32 v95, 0xffff0000, v113\n"
    "v_lshlrev_b32 v96, 16, v110\n"
    "v_lshlrev_b32 v97, 16, v114\n"
    "v_and_b32 v98, 0xffff0000, v110\n"
    "v_and_b32 v99, 0xffff0000, v114\n"
    "s_branch E_2\n"
    "R_1:\n"
    "s_sleep 1\n"
    "s_sub_u32 s39, s39, 1\n"
    "s_cbranch_scc1 ABORT\n"
    "E_2:\n"
    "ds_read_b32 v84, v14 offset:0\n"
    "ds_read_b128 v[80:83], v77\n"
    "s_waitcnt lgkmcnt(0)\n"
    "v_cmp_ne_u32 vcc, 6, v84\n"
    "s_cbranch_vccnz R_1\n"
    "s_add_u32 s36, s36, 1\n"
    "v_permlane16_swap_b32 v80, v82\n"
    "v_permlane16_swap_b32 v81, v83\n"
    "v_add_f32 v80, v80, v82\n"
    "v_add_f32 v81, v81, v83\n"
    "v_mov_b32 v103, s36\n"
    "v_add_f32_dpp v80, v80, v80 row_ror:8 row_mask:0xf bank_mask:0xf\n"
    "v_add_f32_dpp v81, v81, v81 row_ror:8 row_mask:0xf bank_mask:0xf\n"
    "v_mov_b32 v107, s36\n"
    "v_add_f32_dpp v80, v80, v80 row_ror:4 row_mask:0xf bank_mask:0xf\n"
    "v_add_f32_dpp v81, v81, v81 row_ror:4 row_mask:0xf bank_mask:0xf\n"
    "v_mul_f32 v80, 0x3ae38e39, v80\n"
    "v_mul_f32 v81, 0x3ae38e39, v81\n"
    "v_fma_f32 v81, -v80, v80, v81\n"
    "v_max_f32 v81, 0, v81\n"
    "v_cvt_pk_bf16_f32 v81, 0, v81\n"
    "v_add_f32 v81, 0x3727c5ac, v81\n"
    "v_cvt_pk_bf16_f32 v81, 0, v81\n"
    "v_sqrt_f32 v81, v81\n"
    "s_nop 0\n"
    "v_cvt_pk_bf16_f32 v80, v80, v81\n"
    "v_mov_b32 v81, v80\n"
    "s_nop 1\n"
    "v_permlane16_swap_b32 v80, v81\n"
    "v_and_b32 v84, 0xffff0000, v80\n"
    "v_and_b32 v85, 0xffff0000, v81\n"
    "v_lshlrev_b32 v82, 16, v80\n"
    "v_lshlrev_b32 v83, 16, v81\n"
    "v_rcp_f32 v84, v84\n"
    "v_rcp_f32 v85, v85\n"
    "v_pk_add_f32 v[88:89], v[88:89], v[82:83] neg_lo:[0,1] neg_hi:[0,1]\n"
    "v_pk_add_f32 v[90:91], v[90:91], v[82:83] neg_lo:[0,1] neg_hi:[0,1]\n"
    "v_pk_add_f32 v[92:93], v[92:93], v[82:83] neg_lo:[0,1] neg_hi:[0,1]\n"
    "v_cvt_pk_bf16_f32 v88, 0, v88\n"
    "v_cvt_pk_bf16_f32 v89, 0, v89\n"
    "v_cvt_pk_bf16_f32 v90, 0, v90\n"
    "v_cvt_pk_bf16_f32 v91, 0, v91\n"
    "v_cvt_pk_bf16_f32 v92, 0, v92\n"
    "v_cvt_pk_bf16_f32 v93, 0, v93\n"
    "v_pk_mul_f32 v[88:89], v[88:89], v[84:85]\n"
    "v_pk_mul_f32 v[90:91], v[90:91], v[84:85]\n"
    "v_pk_mul_f32 v[92:93], v[92:93], v[84:85]\n"
    "v_cvt_pk_bf16_f32 v100, v88, v89\n"
    "v_cvt_pk_bf16_f32 v101, v90, v91\n"
    "v_cvt_pk_bf16_f32 v102, v92, v93\n"
    "buffer_store_dwordx4 v[100:103], v75, s[20:23], 0 offen sc1\n"
    "v_pk_add_f32 v[94:95], v[94:95], v[82:83] neg_lo:[0,1] neg_hi:[0,1]\n"
    "v_pk_add_f32 v[96:97], v[96:97], v[82:83] neg_lo:[0,1] neg_hi:[0,1]\n"
    "v_pk_add_f32 v[98:99], v[98:99], v[82:83] neg_lo:[0,1] neg_hi:[0,1]\n"
    "v_cvt_pk_bf16_f32 v94, 0, v94\n"
    "v_cvt_pk_bf16_f32 v95, 0, v95\n"
    "v_cvt_pk_bf16_f32 v96, 0, v96\n"
    "v_cvt_pk_bf16_f32 v97, 0, v97\n"
    "v_cvt_pk_bf16_f32 v98, 0, v98\n"
    "v_cvt_pk_bf16_f32 v99, 0, v99\n"
    "v_pk_mul_f32 v[94:95], v[94:95], v[84:85]\n"
    "v_pk_mul_f32 v[96:97], v[96:97], v[84:85]\n"
    "v_pk_mul_f32 v[98:99], v[98:99], v[84:85]\n"
    "v_cvt_pk_bf16_f32 v104, v94, v95\n"
    "v_cvt_pk_bf16_f32 v105, v96, v97\n"
    "v_cvt_pk_bf16_f32 v106, v98, v99\n"
    "buffer_store_dwordx4 v[104:107], v75, s[20:23], s44 offen sc1\n"
    "s_setprio 0\n"
    "s_cmpk_lt_u32 s2, 192\n"
    "s_cbranch_scc1 KST\n"
    "s_setprio 0\n"
    "s_branch ES1\n"
    "KSP:\n"
    "s_sub_u32 s47, s37, 8\n"
    "s_cmp_ge_u32 s47, 3\n"
    "s_cselect_b32 s48, 1, 0\n"
    "s_add_u32 s45, s47, 3\n"
    "s_cmp_lt_i32 s47, 0\n"
    "s_cselect_b32 s47, s45, s47\n"
    "s_cmp_eq_u32 s47, 3\n"
    "s_cselect_b32 s47, 0, s47\n"
    "s_mul_i32 s45, s47, 30464\n"
    "v_and_b32 v122, 3, v0\n"
    "v_mul_u32_u24 v122, 1088, v122\n"
    "v_bfe_u32 v123, v0, 2, 4\n"
    "v_lshlrev_b32 v123, 3, v123\n"
    "v_add3_u32 v122, v122, v123, s45\n"
    "s_mul_i32 s45, s48, 4352\n"
    "v_add_u32 v123, s45, v122\n"
    "s_lshl_b32 s45, s48, 12\n"
    "s_add_u32 s45, s45, 107520\n"
    "v_and_b32 v124, 63, v0\n"
    "v_lshl_add_u32 v124, v124, 4, s45\n"
    "s_lshl_b32 s45, s47, 2\n"
    "v_add_u32 v125, s45, v14\n"
    "s_lshl_b32 s45, s45, 1\n"
    "s_add_u32 s45, s45, 148416\n"
    "v_mov_b32 v126, s45\n"
    "s_mul_i32 s49, s47, 3328\n"
    "s_mov_b32 s42, s47\n"
    "s_cmp_eq_u32 s48, 0\n"
    "s_cbranch_scc1 NEAR\n"
    "s_cmpk_eq_u32 s37, 6\n"
    "s_cbranch_scc1 HELP\n"
    "s_branch FAR\n"
    "MD1:\n"
    "v_bfe_u32 v5, v0, 4, 2\n"
    "v_lshlrev_b32 v1, 2, v3\n"
    "v_add_u32 v1, 149888, v1\n"
    "v_lshlrev_b32 v2, 4, v5\n"
    "v_add_u32 v2, 149888, v2\n"
    "v_bfe_u32 v6, v0, 2, 2\n"
    "v_bfe_u32 v7, v0, 1, 1\n"
    "v_and_b32 v8, 2, v6\n"
    "v_or_b32 v7, v7, v8\n"
    "v_and_b32 v8, 1, v5\n"
    "v_lshl_add_u32 v7, v8, 2, v7\n"
    "v_lshlrev_b32 v8, 10, v5\n"
    "v_lshl_add_u32 v8, v6, 7, v8\n"
    "v_lshl_add_u32 v8, v7, 4, v8\n"
    "v_and_b32 v7, 1, v0\n"
    "v_lshl_add_u32 v8, v7, 3, v8\n"
    "v_xor_b32 v9, 32, v8\n"
    "v_xor_b32 v10, 64, v8\n"
    "v_xor_b32 v11, 0x60, v8\n"
    "v_add_u32 v8, 138240, v8\n"
    "v_add_u32 v9, 138240, v9\n"
    "v_add_u32 v10, 138240, v10\n"
    "v_add_u32 v11, 138240, v11\n"
    "v_and_b32 v3, 63, v0\n"
    "v_lshlrev_b32 v3, 3, v3\n"
    "s_mul_i32 s43, s43, 13824\n"
    "s_lshl_b32 s44, s44, 9\n"
    "s_add_u32 s43, s43, s44\n"
    "s_add_u32 s43, s43, 10519552\n"
    "s_barrier\n"
    "s_cmpk_ge_u32 s2, 243\n"
    "s_cbranch_scc1 M9\n"
    "s_setprio 3\n"
    "s_waitcnt vmcnt(8)\n"
    "v_lshlrev_b32 v5, 16, v4\n"
    "v_and_b32 v4, 0xffff0000, v4\n"
    "v_mul_f32 v6, 0xbfb8aa3b, v5\n"
    "v_mul_f32 v7, 0xbfb8aa3b, v4\n"
    "v_exp_f32 v6, v6\n"
    "v_exp_f32 v7, v7\n"
    "v_add_f32 v6, 1.0, v6\n"
    "v_add_f32 v7, 1.0, v7\n"
    "v_rcp_f32 v6, v6\n"
    "v_rcp_f32 v7, v7\n"
    "v_mul_f32 v5, v5, v6\n"
    "v_mul_f32 v4, v4, v7\n"
    "v_cvt_pk_bf16_f32 v4, v5, v4\n"
    "ds_write_b32 v1, v4\n"
    "ds_read_b128 v[12:15], v2\n"
    "ds_read_b128 v[16:19], v2 offset:64\n"
    "s_waitcnt lgkmcnt(0)\n"
    "s_add_u32 s36, s36, 1\n"
    "s_waitcnt vmcnt(4)\n"
    "ds_read_b64_tr_b16 v[40:41], v8 offset:0\n"
    "ds_read_b64_tr_b16 v[42:43], v8 offset:512\n"
    "ds_read_b64_tr_b16 v[44:45], v9 offset:0\n"
    "ds_read_b64_tr_b16 v[46:47], v9 offset:512\n"
    "ds_read_b64_tr_b16 v[48:49], v10 offset:0\n"
    "ds_read_b64_tr_b16 v[50:51], v10 offset:512\n"
    "ds_read_b64_tr_b16 v[52:53], v11 offset:0\n"
    "ds_read_b64_tr_b16 v[54:55], v11 offset:512\n"
    "s_waitcnt lgkmcnt(6)\n"
    "v_mfma_f32_16x16x32_bf16 v[20:23], v[12:15], v[40:43], 0\n"
    "s_waitcnt lgkmcnt(4)\n"
    "v_mfma_f32_16x16x32_bf16 v[24:27], v[12:15], v[44:47], 0\n"
    "s_waitcnt lgkmcnt(2)\n"
    "v_mfma_f32_16x16x32_bf16 v[28:31], v[12:15], v[48:51], 0\n"
    "s_waitcnt lgkmcnt(0)\n"
    "v_mfma_f32_16x16x32_bf16 v[32:35], v[12:15], v[52:55], 0\n"
    "s_waitcnt vmcnt(0)\n"
    "ds_read_b64_tr_b16 v[56:57], v8 offset:4096\n"
    "ds_read_b64_tr_b16 v[58:59], v8 offset:4608\n"
    "ds_read_b64_tr_b16 v[60:61], v9 offset:4096\n"
    "ds_read_b64_tr_b16 v[62:63], v9 offset:4608\n"
    "ds_read_b64_tr_b16 v[64:65], v10 offset:4096\n"
    "ds_read_b64_tr_b16 v[66:67], v10 offset:4608\n"
    "ds_read_b64_tr_b16 v[68:69], v11 offset:4096\n"
    "ds_read_b64_tr_b16 v[70:71], v11 offset:4608\n"
    "s_waitcnt lgkmcnt(6)\n"
    "v_mfma_f32_16x16x32_bf16 v[20:23], v[16:19], v[56:59], v[20:23]\n"
    "s_waitcnt lgkmcnt(4)\n"
    "v_mfma_f32_16x16x32_bf16 v[24:27], v[16:19], v[60:63], v[24:27]\n"
    "s_waitcnt lgkmcnt(2)\n"
    "v_mfma_f32_16x16x32_bf16 v[28:31], v[16:19], v[64:67], v[28:31]\n"
    "s_waitcnt lgkmcnt(0)\n"
    "v_mfma_f32_16x16x32_bf16 v[32:35], v[16:19], v[68:71], v[32:35]\n"
    "s_nop 1\n"
    "v_mov_b32 v21, s36\n"
    "s_nop 0\n"
    "v_mov_b32_dpp v20, v24 quad_perm:[0,1,2,3] row_mask:0x2 bank_mask:0xf\n"
    "s_nop 1\n"
    "v_mov_b32_dpp v20, v28 quad_perm:[0,1,2,3] row_mask:0x4 bank_mask:0xf\n"
    "s_nop 1\n"
    "v_mov_b32_dpp v20, v32 quad_perm:[0,1,2,3] row_mask:0x8 bank_mask:0xf\n"
    "buffer_store_dwordx2 v[20:21], v3, s[20:23], s43 offen sc1\n"
    "s_setprio 0\n"
    "s_cmpk_ge_u32 s2, 192\n"
    "s_cbranch_scc1 ES1\n"
    "s_mov_b64 exec, 0\n"
    "CH12:\n"
    "s_branch AUX3\n"
    "GA1:\n"
    "s_setprio 1\n"
    "s_mov_b32 s48, 0x01000c0c\n"
    "s_bitcmp1_b32 s37, 0\n"
    "s_cselect_b32 s47, 0x03020c0c, s48\n"
    "s_and_b32 s43, s43, 1\n"
    "s_lshl_b32 s43, s43, 4\n"
    "s_mul_i32 s44, s42, 30464\n"
    "s_and_b32 s45, s37, 1\n"
    "s_lshl_b32 s45, s45, 9\n"
    "s_add_u32 s44, s44, s45\n"
    "v_lshl_add_u32 v2, v1, 3, s44\n"
    "v_lshlrev_b32 v52, 4, v0\n"
    "v_add_u32 v52, 126368, v52\n"
    "s_lshl_b32 s45, s37, 2\n"
    "s_add_u32 s45, s45, 147952\n"
    "v_mov_b32 v12, s45\n"
    "s_lshl_b32 s45, s42, 2\n"
    "s_add_u32 s44, s45, 147968\n"
    "v_mov_b32 v13, s44\n"
    "s_lshl_b32 s45, s45, 1\n"
    "s_add_u32 s45, s45, 148416\n"
    "v_mov_b32 v14, s45\n"
    "v_mov_b32 v15, 1\n"
    "v_mov_b32 v8, 0x3fb8aa3b\n"
    "s_mov_b32 s48, 0x3e22f983\n"
    "s_mov_b32 s49, 0x31dc9c88\n"
    "s_mov_b32 s50, 0x41000000\n"
    "s_mov_b32 s51, 0x43000000\n"
    "s_lshl_b32 s57, s37, 2\n"
    "s_mov_b32 s58, 0x78650432\n"
    "s_lshr_b32 s54, s58, s57\n"
    "s_and_b32 s54, s54, 15\n"
    "s_add_u32 s55, s54, 7\n"
    "s_cmpk_eq_u32 s37, 3\n"
    "s_cselect_b32 s55, 1, s55\n"
    "s_and_b32 s56, s38, 1\n"
    "s_mul_i32 s56, s56, 6144\n"
    "s_lshr_b32 s57, s38, 1\n"
    "s_lshl_b32 s57, s57, 6\n"
    "s_add_u32 s56, s56, s57\n"
    "s_add_u32 s56, s56, 7341056\n"
    "s_mul_i32 s40, s54, 198656\n"
    "s_add_u32 s40, s40, s56\n"
    "s_mul_i32 s41, s55, 198656\n"
    "s_add_u32 s41, s41, s56\n"
    "v_bfe_u32 v66, v0, 2, 4\n"
    "v_mul_u32_u24 v66, 12416, v66\n"
    "v_and_b32 v67, 3, v0\n"
    "v_lshl_add_u32 v66, v67, 4, v66\n"
    "s_barrier\n"
    "s_waitcnt lgkmcnt(0)\n"
    "s_lshr_b32 s46, s46, s43\n"
    "s_lshl_b32 s46, s46, 16\n"
    "s_add_u32 s36, s36, 1\n"
    "v_mul_f32 v9, s46, v8\n"
    "v_exp_f32 v9, v9\n"
    "s_waitcnt vmcnt(3)\n"
    "v_perm_b32 v4, v4, v4, s47\n"
    "v_mul_f32 v4, v4, v8\n"
    "v_cvt_pk_bf16_f32 v9, 0, v9\n"
    "v_exp_f32 v4, v4\n"
    "s_waitcnt vmcnt(2)\n"
    "v_perm_b32 v5, v5, v5, s47\n"
    "v_mul_f32 v5, v5, v9\n"
    "v_cvt_pk_bf16_f32 v4, 0, v4\n"
    "v_mul_f32 v10, s48, v5\n"
    "v_mul_f32 v4, v4, v9\n"
    "v_fma_f32 v11, v5, s48, -v10\n"
    "v_mul_f32_e64 v4, v4, -v8\n"
    "v_fma_f32 v11, v5, s49, v11\n"
    "v_mul_f32 v20, s50, v10\n"
    "v_mul_f32 v21, s50, v4\n"
    "v_fract_f32 v20, v20\n"
    "v_exp_f32 v21, v21\n"
    "v_fma_f32 v20, v11, s50, v20\n"
    "v_cos_f32 v16, v20\n"
    "v_sin_f32 v17, v20\n"
    "v_mul_f32 v22, s51, v10\n"
    "v_mul_f32 v23, s51, v4\n"
    "v_fract_f32 v22, v22\n"
    "v_exp_f32 v23, v23\n"
    "v_fma_f32 v22, v11, s51, v22\n"
    "v_cos_f32 v18, v22\n"
    "v_sin_f32 v19, v22\n"
    "v_pk_mul_f32 v[16:17], v[20:21], v[16:17] op_sel:[1,0] op_sel_hi:[1,1]\n"
    "v_pk_mul_f32 v[18:19], v[22:23], v[18:19] op_sel:[1,0] op_sel_hi:[1,1]\n"
    "s_cmpk_lt_u32 s37, 4\n"
    "s_cbranch_scc1 ASIDE\n"
    "ds_write_b128 v52, v[16:19]\n"
    "s_mov_b64 exec, 1\n"
    "ds_write_b32 v12, v15 offset:4\n"
    "s_mov_b64 exec, -1\n"
    "ASIDE:\n"
    "s_setprio 0\n"
    "v_fract_f32 v22, v10\n"
    "v_exp_f32 v23, v4\n"
    "v_add_f32 v22, v11, v22\n"
    "v_cos_f32 v24, v22\n"
    "v_sin_f32 v25, v22\n"
    "s_waitcnt vmcnt(0)\n"
    "v_and_b32 v32, 0x7fff7fff, v6\n"
    "v_and_b32 v33, 0x7fff7fff, v7\n"
    "v_pk_max_u16 v32, v32, v33\n"
    "v_pk_mul_f32 v[24:25], v[22:23], v[24:25] op_sel:[1,0] op_sel_hi:[1,1]\n"
    "v_lshlrev_b32 v33, 16, v32\n"
    "v_max_u32 v32, v32, v33\n"
    "v_perm_b32 v6, v6, v6, s47\n"
    "v_perm_b32 v7, v7, v7, s47\n"
    "v_max_u32_dpp v32, v32, v32 row_ror:8 row_mask:0xf bank_mask:0xf\n"
    "v_pk_mul_f32 v[20:21], v[18:19], v[18:19] op_sel:[1,1] op_sel_hi:[1,0] neg_lo:[0,1]\n"
    "v_pk_fma_f32 v[30:31], v[18:19], v[18:19], v[20:21] op_sel_hi:[0,1,1]\n"
    "v_max_u32_dpp v32, v32, v32 row_ror:4 row_mask:0xf bank_mask:0xf\n"
    "v_pk_mul_f32 v[20:21], v[24:25], v[24:25] op_sel:[1,1] op_sel_hi:[1,0] neg_lo:[0,1]\n"
    "v_pk_fma_f32 v[26:27], v[24:25], v[24:25], v[20:21] op_sel_hi:[0,1,1]\n"
    "v_max_u32_dpp v32, v32, v32 row_ror:2 row_mask:0xf bank_mask:0xf\n"
    "v_pk_mul_f32 v[20:21], v[26:27], v[26:27] op_sel:[1,1] op_sel_hi:[1,0] neg_lo:[0,1]\n"
    "v_pk_fma_f32 v[28:29], v[26:27], v[26:27], v[20:21] op_sel_hi:[0,1,1]\n"
    "v_max_u32_dpp v32, v32, v32 row_ror:1 row_mask:0xf bank_mask:0xf\n"
    "v_and_b32 v34, 0x7fffff, v9\n"
    "v_or_b32 v34, 0x35800000, v34\n"
    "v_max_u32_dpp v32, v32, v32 row_bcast:15 row_mask:0xa bank_mask:0xf\n"
    "v_and_b32 v35, 0x7f800000, v9\n"
    "s_nop 0\n"
    "v_max_u32_dpp v32, v32, v32 row_bcast:31 row_mask:0xc bank_mask:0xf\n"
    "s_nop 0\n"
    "v_readlane_b32 s44, v32, 63\n"
    "s_lshr_b32 s44, s44, 23\n"
    "s_max_u32 s44, s44, 26\n"
    "s_min_u32 s44, s44, 226\n"
    "s_sub_u32 s46, 266, s44\n"
    "s_lshl_b32 s46, s46, 23\n"
    "s_sub_u32 s44, s44, 132\n"
    "v_pk_mul_f32 v[36:37], v[6:7], s[46:47] op_sel_hi:[1,0]\n"
    "v_ldexp_f32 v35, v35, s44\n"
    "ds_write_b64 v14, v[34:35]\n"
    "v_pk_mul_f32 v[20:21], v[36:37], v[24:25] op_sel:[1,1] op_sel_hi:[1,0] neg_lo:[0,1]\n"
    "v_pk_fma_f32 v[38:39], v[36:37], v[24:25], v[20:21] op_sel_hi:[0,1,1]\n"
    "v_pk_mul_f32 v[20:21], v[38:39], v[24:25] op_sel:[1,1] op_sel_hi:[1,0] neg_lo:[0,1]\n"
    "v_pk_fma_f32 v[40:41], v[38:39], v[24:25], v[20:21] op_sel_hi:[0,1,1]\n"
    "v_pk_mul_f32 v[20:21], v[40:41], v[24:25] op_sel:[1,1] op_sel_hi:[1,0] neg_lo:[0,1]\n"
    "v_pk_fma_f32 v[42:43], v[40:41], v[24:25], v[20:21] op_sel_hi:[0,1,1]\n"
    "v_pk_mul_f32 v[20:21], v[36:37], v[30:31] op_sel:[1,1] op_sel_hi:[1,0] neg_lo:[0,1]\n"
    "v_pk_fma_f32 v[44:45], v[36:37], v[30:31], v[20:21] op_sel_hi:[0,1,1]\n"
    "v_cvt_pk_f16_f32 v54, v36, v38\n"
    "v_cvt_pk_f16_f32 v55, v40, v42\n"
    "v_cvt_pk_f16_f32 v56, -v37, -v39\n"
    "v_cvt_pk_f16_f32 v57, -v41, -v43\n"
    "ds_write_b64 v2, v[54:55] offset:0\n"
    "ds_write_b64 v2, v[56:57] offset:6528\n"
    "v_pk_mul_f32 v[20:21], v[42:43], v[24:25] op_sel:[1,1] op_sel_hi:[1,0] neg_lo:[0,1]\n"
    "v_pk_fma_f32 v[36:37], v[42:43], v[24:25], v[20:21] op_sel_hi:[0,1,1]\n"
    "v_pk_mul_f32 v[20:21], v[36:37], v[24:25] op_sel:[1,1] op_sel_hi:[1,0] neg_lo:[0,1]\n"
    "v_pk_fma_f32 v[38:39], v[36:37], v[24:25], v[20:21] op_sel_hi:[0,1,1]\n"
    "v_pk_mul_f32 v[20:21], v[38:39], v[24:25] op_sel:[1,1] op_sel_hi:[1,0] neg_lo:[0,1]\n"
    "v_pk_fma_f32 v[40:41], v[38:39], v[24:25], v[20:21] op_sel_hi:[0,1,1]\n"
    "v_pk_mul_f32 v[20:21], v[40:41], v[24:25] op_sel:[1,1] op_sel_hi:[1,0] neg_lo:[0,1]\n"
    "v_pk_fma_f32 v[42:43], v[40:41], v[24:25], v[20:21] op_sel_hi:[0,1,1]\n"
    "v_cvt_pk_f16_f32 v58, v36, v38\n"
    "v_cvt_pk_f16_f32 v59, v40, v42\n"
    "v_cvt_pk_f16_f32 v60, -v37, -v39\n"
    "v_cvt_pk_f16_f32 v61, -v41, -v43\n"
    "ds_write_b64 v2, v[58:59] offset:1088\n"
    "ds_write_b64 v2, v[60:61] offset:7616\n"
    "v_pk_mul_f32 v[20:21], v[44:45], v[26:27] op_sel:[1,1] op_sel_hi:[1,0] neg_lo:[0,1]\n"
    "v_pk_fma_f32 v[46:47], v[44:45], v[26:27], v[20:21] op_sel_hi:[0,1,1]\n"
    "v_pk_mul_f32 v[20:21], v[44:45], v[28:29] op_sel:[1,1] op_sel_hi:[1,0] neg_lo:[0,1]\n"
    "v_pk_fma_f32 v[48:49], v[44:45], v[28:29], v[20:21] op_sel_hi:[0,1,1]\n"
    "v_pk_mul_f32 v[20:21], v[48:49], v[26:27] op_sel:[1,1] op_sel_hi:[1,0] neg_lo:[0,1]\n"
    "v_pk_fma_f32 v[50:51], v[48:49], v[26:27], v[20:21] op_sel_hi:[0,1,1]\n"
    "v_pk_mul_f32 v[20:21], v[44:45], v[30:31] op_sel:[1,1] op_sel_hi:[1,0] neg_lo:[0,1]\n"
    "v_pk_fma_f32 v[36:37], v[44:45], v[30:31], v[20:21] op_sel_hi:[0,1,1]\n"
    "v_cvt_pk_f16_f32 v54, v44, v46\n"
    "v_cvt_pk_f16_f32 v55, v48, v50\n"
    "v_cvt_pk_f16_f32 v56, -v45, -v47\n"
    "v_cvt_pk_f16_f32 v57, -v49, -v51\n"
    "ds_write_b64 v2, v[54:55] offset:2176\n"
    "ds_write_b64 v2, v[56:57] offset:8704\n"
    "v_pk_mul_f32 v[20:21], v[36:37], v[30:31] op_sel:[1,1] op_sel_hi:[1,0] neg_lo:[0,1]\n"
    "v_pk_fma_f32 v[38:39], v[36:37], v[30:31], v[20:21] op_sel_hi:[0,1,1]\n"
    "v_pk_mul_f32 v[20:21], v[36:37], v[28:29] op_sel:[1,1] op_sel_hi:[1,0] neg_lo:[0,1]\n"
    "v_pk_fma_f32 v[40:41], v[36:37], v[28:29], v[20:21] op_sel_hi:[0,1,1]\n"
    "v_pk_mul_f32 v[20:21], v[38:39], v[28:29] op_sel:[1,1] op_sel_hi:[1,0] neg_lo:[0,1]\n"
    "v_pk_fma_f32 v[42:43], v[38:39], v[28:29], v[20:21] op_sel_hi:[0,1,1]\n"
    "v_pk_mul_f32 v[20:21], v[38:39], v[30:31] op_sel:[1,1] op_sel_hi:[1,0] neg_lo:[0,1]\n"
    "v_pk_fma_f32 v[44:45], v[38:39], v[30:31], v[20:21] op_sel_hi:[0,1,1]\n"
    "v_cvt_pk_f16_f32 v58, v40, v42\n"
    "v_cvt_pk_f16_f32 v59, v36, v38\n"
    "v_cvt_pk_f16_f32 v60, -v41, -v43\n"
    "v_cvt_pk_f16_f32 v61, -v37, -v39\n"
    "ds_write_b64 v2, v[58:59] offset:3264\n"
    "ds_write_b64 v2, v[60:61] offset:9792\n"
    "v_pk_mul_f32 v[20:21], v[44:45], v[30:31] op_sel:[1,1] op_sel_hi:[1,0] neg_lo:[0,1]\n"
    "v_pk_fma_f32 v[46:47], v[44:45], v[30:31], v[20:21] op_sel_hi:[0,1,1]\n"
    "v_pk_mul_f32 v[20:21], v[46:47], v[30:31] op_sel:[1,1] op_sel_hi:[1,0] neg_lo:[0,1]\n"
    "v_pk_fma_f32 v[48:49], v[46:47], v[30:31], v[20:21] op_sel_hi:[0,1,1]\n"
    "v_pk_mul_f32 v[20:21], v[48:49], v[30:31] op_sel:[1,1] op_sel_hi:[1,0] neg_lo:[0,1]\n"
    "v_pk_fma_f32 v[50:51], v[48:49], v[30:31], v[20:21] op_sel_hi:[0,1,1]\n"
    "v_pk_mul_f32 v[20:21], v[50:51], v[30:31] op_sel:[1,1] op_sel_hi:[1,0] neg_lo:[0,1]\n"
    "v_pk_fma_f32 v[36:37], v[50:51], v[30:31], v[20:21] op_sel_hi:[0,1,1]\n"
    "v_cvt_pk_f16_f32 v54, v44, v46\n"
    "v_cvt_pk_f16_f32 v55, v48, v50\n"
    "v_cvt_pk_f16_f32 v56, -v45, -v47\n"
    "v_cvt_pk_f16_f32 v57, -v49, -v51\n"
    "ds_write_b64 v2, v[54:55] offset:4352\n"
    "ds_write_b64 v2, v[56:57] offset:10880\n"
    "v_cvt_pk_f16_f32 v38, v36, -v37\n"
    "ds_write_b16 v2, v38 offset:5440\n"
    "ds_write_b16_d16_hi v2, v38 offset:11968\n"
    "s_mov_b64 exec, 1\n"
    "ds_add_u32 v13, v15 offset:12\n"
    "s_mov_b64 exec, -1\n"
    "s_memrealtime s[98:99]\n"
    "s_cmpk_ge_u32 s37, 4\n"
    "s_cbranch_scc1 CH45\n"
    "s_waitcnt lgkmcnt(0)\n"
    "s_sub_u32 s61, s98, s96\n"
    "s_cmpk_lt_u32 s61, 200\n"
    "s_cbranch_scc1 BSIDE\n"
    "buffer_load_dwordx4 v[68:71], v66, s[20:23], s40 offen sc1\n"
    "buffer_load_dwordx4 v[72:75], v66, s[20:23], s41 offen sc1\n"
    "BSIDE:\n"
    "v_mov_b32 v36, 0x46800000\n"
    "v_mov_b32 v37, 0\n"
    "v_pk_mul_f32 v[38:39], v[18:19], v[36:37] op_sel_hi:[1,0]\n"
    "v_cvt_pk_f16_f32 v44, v36, v38\n"
    "v_cvt_pk_f16_f32 v46, v37, v39\n"
    "v_pk_mul_f32 v[36:37], v[16:17], v[36:37] op_sel_hi:[1,0]\n"
    "v_pk_mul_f32 v[42:43], v[38:39], v[16:17] op_sel:[1,1] op_sel_hi:[1,0] neg_lo:[0,1]\n"
    "v_pk_fma_f32 v[38:39], v[38:39], v[16:17], v[42:43] op_sel_hi:[0,1,1]\n"
    "v_cvt_pk_f16_f32 v45, v36, v38\n"
    "v_cvt_pk_f16_f32 v47, v37, v39\n"
    "v_pk_mul_f32 v[40:41], v[36:37], v[16:17] op_sel:[1,1] op_sel_hi:[1,0] neg_lo:[0,1]\n"
    "v_pk_fma_f32 v[36:37], v[36:37], v[16:17], v[40:41] op_sel_hi:[0,1,1]\n"
    "ds_write_b64 v2, v[44:45] offset:13056\n"
    "v_pk_mul_f32 v[42:43], v[38:39], v[16:17] op_sel:[1,1] op_sel_hi:[1,0] neg_lo:[0,1]\n"
    "v_pk_fma_f32 v[38:39], v[38:39], v[16:17], v[42:43] op_sel_hi:[0,1,1]\n"
    "ds_write_b64 v2, v[46:47] offset:21760\n"
    "v_cvt_pk_f16_f32 v48, v36, v38\n"
    "v_cvt_pk_f16_f32 v50, v37, v39\n"
    "v_pk_mul_f32 v[40:41], v[36:37], v[16:17] op_sel:[1,1] op_sel_hi:[1,0] neg_lo:[0,1]\n"
    "v_pk_fma_f32 v[36:37], v[36:37], v[16:17], v[40:41] op_sel_hi:[0,1,1]\n"
    "v_pk_mul_f32 v[42:43], v[38:39], v[16:17] op_sel:[1,1] op_sel_hi:[1,0] neg_lo:[0,1]\n"
    "v_pk_fma_f32 v[38:39], v[38:39], v[16:17], v[42:43] op_sel_hi:[0,1,1]\n"
    "v_cvt_pk_f16_f32 v49, v36, v38\n"
    "v_cvt_pk_f16_f32 v51, v37, v39\n"
    "v_pk_mul_f32 v[40:41], v[36:37], v[16:17] op_sel:[1,1] op_sel_hi:[1,0] neg_lo:[0,1]\n"
    "v_pk_fma_f32 v[36:37], v[36:37], v[16:17], v[40:41] op_sel_hi:[0,1,1]\n"
    "ds_write_b64 v2, v[48:49] offset:14144\n"
    "v_pk_mul_f32 v[42:43], v[38:39], v[16:17] op_sel:[1,1] op_sel_hi:[1,0] neg_lo:[0,1]\n"
    "v_pk_fma_f32 v[38:39], v[38:39], v[16:17], v[42:43] op_sel_hi:[0,1,1]\n"
    "ds_write_b64 v2, v[50:51] offset:22848\n"
    "v_cvt_pk_f16_f32 v44, v36, v38\n"
    "v_cvt_pk_f16_f32 v46, v37, v39\n"
    "v_pk_mul_f32 v[40:41], v[36:37], v[16:17] op_sel:[1,1] op_sel_hi:[1,0] neg_lo:[0,1]\n"
    "v_pk_fma_f32 v[36:37], v[36:37], v[16:17], v[40:41] op_sel_hi:[0,1,1]\n"
    "v_pk_mul_f32 v[42:43], v[38:39], v[16:17] op_sel:[1,1] op_sel_hi:[1,0] neg_lo:[0,1]\n"
    "v_pk_fma_f32 v[38:39], v[38:39], v[16:17], v[42:43] op_sel_hi:[0,1,1]\n"
    "v_cvt_pk_f16_f32 v45, v36, v38\n"
    "v_cvt_pk_f16_f32 v47, v37, v39\n"
    "v_pk_mul_f32 v[40:41], v[36:37], v[16:17] op_sel:[1,1] op_sel_hi:[1,0] neg_lo:[0,1]\n"
    "v_pk_fma_f32 v[36:37], v[36:37], v[16:17], v[40:41] op_sel_hi:[0,1,1]\n"
    "ds_write_b64 v2, v[44:45] offset:15232\n"
    "v_pk_mul_f32 v[42:43], v[38:39], v[16:17] op_sel:[1,1] op_sel_hi:[1,0] neg_lo:[0,1]\n"
    "v_pk_fma_f32 v[38:39], v[38:39], v[16:17], v[42:43] op_sel_hi:[0,1,1]\n"
    "ds_write_b64 v2, v[46:47] offset:23936\n"
    "v_cvt_pk_f16_f32 v48, v36, v38\n"
    "v_cvt_pk_f16_f32 v50, v37, v39\n"
    "v_pk_mul_f32 v[40:41], v[36:37], v[16:17] op_sel:[1,1] op_sel_hi:[1,0] neg_lo:[0,1]\n"
    "v_pk_fma_f32 v[36:37], v[36:37], v[16:17], v[40:41] op_sel_hi:[0,1,1]\n"
    "v_pk_mul_f32 v[42:43], v[38:39], v[16:17] op_sel:[1,1] op_sel_hi:[1,0] neg_lo:[0,1]\n"
    "v_pk_fma_f32 v[38:39], v[38:39], v[16:17], v[42:43] op_sel_hi:[0,1,1]\n"
    "v_cvt_pk_f16_f32 v49, v36, v38\n"
    "v_cvt_pk_f16_f32 v51, v37, v39\n"
    "v_pk_mul_f32 v[40:41], v[36:37], v[16:17] op_sel:[1,1] op_sel_hi:[1,0] neg_lo:[0,1]\n"
    "v_pk_fma_f32 v[36:37], v[36:37], v[16:17], v[40:41] op_sel_hi:[0,1,1]\n"
    "ds_write_b64 v2, v[48:49] offset:17408\n"
    "v_pk_mul_f32 v[42:43], v[38:39], v[16:17] op_sel:[1,1] op_sel_hi:[1,0] neg_lo:[0,1]\n"
    "v_pk_fma_f32 v[38:39], v[38:39], v[16:17], v[42:43] op_sel_hi:[0,1,1]\n"
    "ds_write_b64 v2, v[50:51] offset:26112\n"
    "v_cvt_pk_f16_f32 v44, v36, v38\n"
    "v_cvt_pk_f16_f32 v46, v37, v39\n"
    "v_pk_mul_f32 v[40:41], v[36:37], v[16:17] op_sel:[1,1] op_sel_hi:[1,0] neg_lo:[0,1]\n"
    "v_pk_fma_f32 v[36:37], v[36:37], v[16:17], v[40:41] op_sel_hi:[0,1,1]\n"
    "v_pk_mul_f32 v[42:43], v[38:39], v[16:17] op_sel:[1,1] op_sel_hi:[1,0] neg_lo:[0,1]\n"
    "v_pk_fma_f32 v[38:39], v[38:39], v[16:17], v[42:43] op_sel_hi:[0,1,1]\n"
    "v_cvt_pk_f16_f32 v45, v36, v38\n"
    "v_cvt_pk_f16_f32 v47, v37, v39\n"
    "v_pk_mul_f32 v[40:41], v[36:37], v[16:17] op_sel:[1,1] op_sel_hi:[1,0] neg_lo:[0,1]\n"
    "v_pk_fma_f32 v[36:37], v[36:37], v[16:17], v[40:41] op_sel_hi:[0,1,1]\n"
    "ds_write_b64 v2, v[44:45] offset:18496\n"
    "v_pk_mul_f32 v[42:43], v[38:39], v[16:17] op_sel:[1,1] op_sel_hi:[1,0] neg_lo:[0,1]\n"
    "v_pk_fma_f32 v[38:39], v[38:39], v[16:17], v[42:43] op_sel_hi:[0,1,1]\n"
    "ds_write_b64 v2, v[46:47] offset:27200\n"
    "v_cvt_pk_f16_f32 v48, v36, v38\n"
    "v_cvt_pk_f16_f32 v50, v37, v39\n"
    "v_pk_mul_f32 v[40:41], v[36:37], v[16:17] op_sel:[1,1] op_sel_hi:[1,0] neg_lo:[0,1]\n"
    "v_pk_fma_f32 v[36:37], v[36:37], v[16:17], v[40:41] op_sel_hi:[0,1,1]\n"
    "v_pk_mul_f32 v[42:43], v[38:39], v[16:17] op_sel:[1,1] op_sel_hi:[1,0] neg_lo:[0,1]\n"
    "v_pk_fma_f32 v[38:39], v[38:39], v[16:17], v[42:43] op_sel_hi:[0,1,1]\n"
    "v_cvt_pk_f16_f32 v49, v36, v38\n"
    "v_cvt_pk_f16_f32 v51, v37, v39\n"
    "v_pk_mul_f32 v[40:41], v[36:37], v[16:17] op_sel:[1,1] op_sel_hi:[1,0] neg_lo:[0,1]\n"
    "v_pk_fma_f32 v[36:37], v[36:37], v[16:17], v[40:41] op_sel_hi:[0,1,1]\n"
    "ds_write_b64 v2, v[48:49] offset:19584\n"
    "v_pk_mul_f32 v[42:43], v[38:39], v[16:17] op_sel:[1,1] op_sel_hi:[1,0] neg_lo:[0,1]\n"
    "v_pk_fma_f32 v[38:39], v[38:39], v[16:17], v[42:43] op_sel_hi:[0,1,1]\n"
    "ds_write_b64 v2, v[50:51] offset:28288\n"
    "v_cvt_pk_f16_f32 v44, v36, v38\n"
    "v_cvt_pk_f16_f32 v46, v37, v39\n"
    "v_pk_mul_f32 v[40:41], v[36:37], v[16:17] op_sel:[1,1] op_sel_hi:[1,0] neg_lo:[0,1]\n"
    "v_pk_fma_f32 v[36:37], v[36:37], v[16:17], v[40:41] op_sel_hi:[0,1,1]\n"
    "v_pk_mul_f32 v[42:43], v[38:39], v[16:17] op_sel:[1,1] op_sel_hi:[1,0] neg_lo:[0,1]\n"
    "v_pk_fma_f32 v[38:39], v[38:39], v[16:17], v[42:43] op_sel_hi:[0,1,1]\n"
    "v_cvt_pk_f16_f32 v45, v36, v38\n"
    "v_cvt_pk_f16_f32 v47, v37, v39\n"
    "v_pk_mul_f32 v[40:41], v[36:37], v[16:17] op_sel:[1,1] op_sel_hi:[1,0] neg_lo:[0,1]\n"
    "v_pk_fma_f32 v[36:37], v[36:37], v[16:17], v[40:41] op_sel_hi:[0,1,1]\n"
    "ds_write_b64 v2, v[44:45] offset:20672\n"
    "v_pk_mul_f32 v[42:43], v[38:39], v[16:17] op_sel:[1,1] op_sel_hi:[1,0] neg_lo:[0,1]\n"
    "v_pk_fma_f32 v[38:39], v[38:39], v[16:17], v[42:43] op_sel_hi:[0,1,1]\n"
    "ds_write_b64 v2, v[46:47] offset:29376\n"
    "v_cvt_pk_f16_f32 v48, v36, v38\n"
    "v_cvt_pk_f16_f32 v50, v37, v39\n"
    "v_pk_mul_f32 v[40:41], v[36:37], v[16:17] op_sel:[1,1] op_sel_hi:[1,0] neg_lo:[0,1]\n"
    "v_pk_fma_f32 v[36:37], v[36:37], v[16:17], v[40:41] op_sel_hi:[0,1,1]\n"
    "v_pk_mul_f32 v[42:43], v[38:39], v[16:17] op_sel:[1,1] op_sel_hi:[1,0] neg_lo:[0,1]\n"
    "v_pk_fma_f32 v[38:39], v[38:39], v[16:17], v[42:43] op_sel_hi:[0,1,1]\n"
    "v_cvt_pk_f16_f32 v49, v36, v38\n"
    "v_cvt_pk_f16_f32 v51, v37, v39\n"
    "ds_write_b64 v2, v[48:49] offset:16320\n"
    "ds_write_b64 v2, v[50:51] offset:25024\n"
    "s_mov_b64 exec, 1\n"
    "ds_add_u32 v13, v15 offset:28\n"
    "s_mov_b64 exec, -1\n"
    "s_cmpk_ge_u32 s37, 14\n"
    "s_cbranch_scc1 AUX3\n"
    "s_cmpk_ge_u32 s61, 200\n"
    "s_cbranch_scc1 STAGE\n"
    "s_branch XISS\n"
    "CH45:\n"
    "s_mov_b64 exec, 0\n"
    "s_cmpk_eq_u32 s37, 5\n"
    "s_cbranch_scc1 CH5\n"
    "CH4:\n"
    "s_branch HELP\n"
    "CH5:\n"
    "s_branch HELP\n"
    "KST:\n"
    "s_branch E_4\n"
    "R_3:\n"
    "s_sleep 1\n"
    "s_sub_u32 s39, s39, 1\n"
    "s_cbranch_scc1 ABORT\n"
    "E_4:\n"
    "ds_read_b32 v112, v14 offset:24\n"
    "ds_read_b128 v[16:19], v124 offset:0\n"
    "ds_read_b128 v[20:23], v124 offset:1024\n"
    "ds_read_b128 v[24:27], v124 offset:2048\n"
    "ds_read_b128 v[28:31], v124 offset:3072\n"
    "s_waitcnt lgkmcnt(0)\n"
    "v_cmp_ne_u32 vcc, 1, v112\n"
    "s_cbranch_vccnz R_3\n"
    "v_add_u32 v28, s49, v28\n"
    "v_add_u32 v29, s49, v29\n"
    "s_mov_b32 s46, 0x01000100\n"
    "s_mov_b64 s[50:51], 0x1ffff\n"
    "s_branch E_6\n"
    "R_5:\n"
    "s_sleep 2\n"
    "s_sub_u32 s39, s39, 1\n"
    "s_cbranch_scc1 ABORT\n"
    "E_6:\n"
    "ds_read_b32 v112, v125 offset:12\n"
    "s_waitcnt lgkmcnt(0)\n"
    "v_cmp_ne_u32 vcc, 2, v112\n"
    "s_cbranch_vccnz R_5\n"
    "ds_read_b64_tr_b16 v[32:33], v122 offset:0\n"
    "ds_read_b64_tr_b16 v[34:35], v122 offset:6528\n"
    "ds_read_b64_tr_b16 v[36:37], v122 offset:4352\n"
    "ds_read_b64_tr_b16 v[38:39], v122 offset:10880\n"
    "ds_read_b64_tr_b16 v[40:41], v122 offset:128\n"
    "ds_read_b64_tr_b16 v[42:43], v122 offset:6656\n"
    "ds_read_b64_tr_b16 v[44:45], v122 offset:4480\n"
    "ds_read_b64_tr_b16 v[46:47], v122 offset:11008\n"
    "ds_read_b64_tr_b16 v[48:49], v122 offset:256\n"
    "ds_read_b64_tr_b16 v[50:51], v122 offset:6784\n"
    "ds_read_b64_tr_b16 v[52:53], v122 offset:4608\n"
    "ds_read_b64_tr_b16 v[54:55], v122 offset:11136\n"
    "ds_read_b64_tr_b16 v[56:57], v122 offset:384\n"
    "ds_read_b64_tr_b16 v[58:59], v122 offset:6912\n"
    "ds_read_b64_tr_b16 v[60:61], v122 offset:4736\n"
    "ds_read_b64_tr_b16 v[62:63], v122 offset:11264\n"
    "ds_read_b64_tr_b16 v[64:65], v122 offset:512\n"
    "ds_read_b64_tr_b16 v[66:67], v122 offset:7040\n"
    "ds_read_b64_tr_b16 v[68:69], v122 offset:4864\n"
    "ds_read_b64_tr_b16 v[70:71], v122 offset:11392\n"
    "ds_read_b64_tr_b16 v[72:73], v122 offset:640\n"
    "ds_read_b64_tr_b16 v[74:75], v122 offset:7168\n"
    "ds_read_b64_tr_b16 v[76:77], v122 offset:4992\n"
    "ds_read_b64_tr_b16 v[78:79], v122 offset:11520\n"
    "ds_read_b64_tr_b16 v[80:81], v122 offset:768\n"
    "ds_read_b64_tr_b16 v[82:83], v122 offset:7296\n"
    "ds_read_b64_tr_b16 v[84:85], v122 offset:5120\n"
    "ds_read_b64_tr_b16 v[86:87], v122 offset:11648\n"
    "ds_read_b64_tr_b16 v[88:89], v122 offset:896\n"
    "ds_read_b64_tr_b16 v[90:91], v122 offset:7424\n"
    "ds_read_b64_tr_b16 v[92:93], v122 offset:5248\n"
    "ds_read_b64_tr_b16 v[94:95], v122 offset:11776\n"
    "ds_read_b32 v12, v126\n"
    "s_branch E_8\n"
    "R_7:\n"
    "s_sleep 1\n"
    "s_sub_u32 s39, s39, 1\n"
    "s_cbranch_scc1 ABORT\n"
    "E_8:\n"
    "ds_read_b32 v112, v14 offset:28\n"
    "ds_read_b64 v[114:115], v14 offset:32\n"
    "s_waitcnt lgkmcnt(0)\n"
    "v_add3_u32 v112, v112, v114, v115\n"
    "v_cmp_ne_u32 vcc, 6, v112\n"
    "s_cbranch_vccnz R_7\n"
    "s_setprio 3\n"
    "ds_read_b64_tr_b16 v[96:97], v123 offset:13056\n"
    "ds_read_b64_tr_b16 v[98:99], v123 offset:21760\n"
    "ds_read_b64_tr_b16 v[100:101], v123 offset:13184\n"
    "ds_read_b64_tr_b16 v[102:103], v123 offset:21888\n"
    "ds_read_b64_tr_b16 v[104:105], v123 offset:13312\n"
    "ds_read_b64_tr_b16 v[106:107], v123 offset:22016\n"
    "ds_read_b64_tr_b16 v[108:109], v123 offset:13440\n"
    "ds_read_b64_tr_b16 v[110:111], v123 offset:22144\n"
    "s_waitcnt lgkmcnt(6)\n"
    "v_mfma_f32_16x16x32_f16 v[112:115], v[32:35], v[96:99], 0\n"
    "v_mfma_f32_16x16x32_f16 v[116:119], v[36:39], v[96:99], 0\n"
    "ds_read_b64_tr_b16 v[96:97], v123 offset:13568\n"
    "ds_read_b64_tr_b16 v[98:99], v123 offset:22272\n"
    "s_waitcnt lgkmcnt(6)\n"
    "v_mfma_f32_16x16x32_f16 v[112:115], v[40:43], v[100:103], v[112:115]\n"
    "v_mfma_f32_16x16x32_f16 v[116:119], v[44:47], v[100:103], v[116:119]\n"
    "ds_read_b64_tr_b16 v[100:101], v123 offset:13696\n"
    "ds_read_b64_tr_b16 v[102:103], v123 offset:22400\n"
    "s_waitcnt lgkmcnt(6)\n"
    "v_mfma_f32_16x16x32_f16 v[112:115], v[48:51], v[104:107], v[112:115]\n"
    "v_mfma_f32_16x16x32_f16 v[116:119], v[52:55], v[104:107], v[116:119]\n"
    "ds_read_b64_tr_b16 v[104:105], v123 offset:13824\n"
    "ds_read_b64_tr_b16 v[106:107], v123 offset:22528\n"
    "s_waitcnt lgkmcnt(6)\n"
    "v_mfma_f32_16x16x32_f16 v[112:115], v[56:59], v[108:111], v[112:115]\n"
    "v_mfma_f32_16x16x32_f16 v[116:119], v[60:63], v[108:111], v[116:119]\n"
    "ds_read_b64_tr_b16 v[108:109], v123 offset:13952\n"
    "ds_read_b64_tr_b16 v[110:111], v123 offset:22656\n"
    "s_waitcnt lgkmcnt(6)\n"
    "v_mfma_f32_16x16x32_f16 v[112:115], v[64:67], v[96:99], v[112:115]\n"
    "v_mfma_f32_16x16x32_f16 v[116:119], v[68:71], v[96:99], v[116:119]\n"
    "s_waitcnt lgkmcnt(4)\n"
    "v_mfma_f32_16x16x32_f16 v[112:115], v[72:75], v[100:103], v[112:115]\n"
    "v_mfma_f32_16x16x32_f16 v[116:119], v[76:79], v[100:103], v[116:119]\n"
    "s_waitcnt lgkmcnt(2)\n"
    "v_mfma_f32_16x16x32_f16 v[112:115], v[80:83], v[104:107], v[112:115]\n"
    "v_mfma_f32_16x16x32_f16 v[116:119], v[84:87], v[104:107], v[116:119]\n"
    "s_waitcnt lgkmcnt(0)\n"
    "v_mfma_f32_16x16x32_f16 v[112:115], v[88:91], v[108:111], v[112:115]\n"
    "v_mfma_f32_16x16x32_f16 v[116:119], v[92:95], v[108:111], v[116:119]\n"
    "s_cmp_eq_u32 s47, 1\n"
    "s_cbranch_scc1 KT1\n"
    "s_cmp_eq_u32 s47, 2\n"
    "s_cbranch_scc1 KT2\n"
    "KT0:\n"
    "s_nop 2\n"
    "v_pk_mul_f32 v[112:113], v[112:113], v[12:13] op_sel_hi:[1,0]\n"
    "v_pk_mul_f32 v[114:115], v[114:115], v[12:13] op_sel_hi:[1,0]\n"
    "v_cvt_pk_f16_f32 v112, v112, v113\n"
    "v_cvt_pk_f16_f32 v113, v114, v115\n"
    "v_pk_mul_f32 v[116:117], v[116:117], v[12:13] op_sel_hi:[1,0]\n"
    "v_perm_b32 v114, v113, v112, v17\n"
    "v_perm_b32 v115, v113, v112, v18\n"
    "v_perm_b32 v32, v113, v112, v30\n"
    "v_perm_b32 v33, v113, v113, s46\n"
    "v_perm_b32 v112, v113, v112, v19\n"
    "s_mov_b32 exec_hi, s50\n"
    "ds_write_b64 v16, v[114:115] offset:0\n"
    "s_mov_b32 exec_lo, 0\n"
    "ds_write2_b32 v28, v114, v32 offset1:1\n"
    "ds_write2_b32 v29, v33, v113 offset1:1\n"
    "s_mov_b64 exec, -1\n"
    "v_pk_mul_f32 v[118:119], v[118:119], v[12:13] op_sel_hi:[1,0]\n"
    "ds_write_b16 v22, v113 offset:0\n"
    "ds_write_b16_d16_hi v23, v113 offset:0\n"
    "v_cvt_pk_f16_f32 v116, v116, v117\n"
    "ds_write_b16 v20, v112 offset:0\n"
    "ds_write_b16_d16_hi v21, v112 offset:0\n"
    "v_cvt_pk_f16_f32 v117, v118, v119\n"
    "s_mov_b64 exec, s[50:51]\n"
    "ds_write_b16 v24, v116 offset:0\n"
    "ds_write_b16_d16_hi v25, v116 offset:0\n"
    "ds_write_b16 v26, v117 offset:0\n"
    "ds_write_b16_d16_hi v27, v117 offset:0\n"
    "s_mov_b64 exec, 1\n"
    "ds_add_u32 v14, v15 offset:224\n"
    "s_mov_b64 exec, -1\n"
    "s_branch KT3\n"
    "KT1:\n"
    "s_nop 4\n"
    "v_pk_mul_f32 v[112:113], v[112:113], v[12:13] op_sel_hi:[1,0]\n"
    "v_pk_mul_f32 v[114:115], v[114:115], v[12:13] op_sel_hi:[1,0]\n"
    "v_cvt_pk_f16_f32 v112, v112, v113\n"
    "v_cvt_pk_f16_f32 v113, v114, v115\n"
    "v_pk_mul_f32 v[116:117], v[116:117], v[12:13] op_sel_hi:[1,0]\n"
    "v_perm_b32 v114, v113, v112, v17\n"
    "v_perm_b32 v115, v113, v112, v18\n"
    "v_perm_b32 v32, v113, v112, v30\n"
    "v_perm_b32 v33, v113, v113, s46\n"
    "v_perm_b32 v112, v113, v112, v19\n"
    "s_mov_b32 exec_hi, s50\n"
    "ds_write_b64 v16, v[114:115] offset:3328\n"
    "s_mov_b32 exec_lo, 0\n"
    "ds_write2_b32 v28, v114, v32 offset1:1\n"
    "ds_write2_b32 v29, v33, v113 offset1:1\n"
    "s_mov_b64 exec, -1\n"
    "v_pk_mul_f32 v[118:119], v[118:119], v[12:13] op_sel_hi:[1,0]\n"
    "ds_write_b16 v22, v113 offset:3328\n"
    "ds_write_b16_d16_hi v23, v113 offset:3328\n"
    "v_cvt_pk_f16_f32 v116, v116, v117\n"
    "ds_write_b16 v20, v112 offset:3328\n"
    "ds_write_b16_d16_hi v21, v112 offset:3328\n"
    "v_cvt_pk_f16_f32 v117, v118, v119\n"
    "s_mov_b64 exec, s[50:51]\n"
    "ds_write_b16 v24, v116 offset:3328\n"
    "ds_write_b16_d16_hi v25, v116 offset:3328\n"
    "ds_write_b16 v26, v117 offset:3328\n"
    "ds_write_b16_d16_hi v27, v117 offset:3328\n"
    "s_mov_b64 exec, 1\n"
    "ds_add_u32 v14, v15 offset:224\n"
    "s_mov_b64 exec, -1\n"
    "s_branch KT3\n"
    "KT2:\n"
    "s_nop 2\n"
    "v_pk_mul_f32 v[112:113], v[112:113], v[12:13] op_sel_hi:[1,0]\n"
    "v_pk_mul_f32 v[114:115], v[114:115], v[12:13] op_sel_hi:[1,0]\n"
    "v_cvt_pk_f16_f32 v112, v112, v113\n"
    "v_cvt_pk_f16_f32 v113, v114, v115\n"
    "v_pk_mul_f32 v[116:117], v[116:117], v[12:13] op_sel_hi:[1,0]\n"
    "v_perm_b32 v114, v113, v112, v17\n"
    "v_perm_b32 v115, v113, v112, v18\n"
    "v_perm_b32 v32, v113, v112, v30\n"
    "v_perm_b32 v33, v113, v113, s46\n"
    "v_perm_b32 v112, v113, v112, v19\n"
    "s_mov_b32 exec_hi, s50\n"
    "ds_write_b64 v16, v[114:115] offset:6656\n"
    "s_mov_b32 exec_lo, 0\n"
    "ds_write2_b32 v28, v114, v32 offset1:1\n"
    "ds_write2_b32 v29, v33, v113 offset1:1\n"
    "s_mov_b64 exec, -1\n"
    "v_pk_mul_f32 v[118:119], v[118:119], v[12:13] op_sel_hi:[1,0]\n"
    "ds_write_b16 v22, v113 offset:6656\n"
    "ds_write_b16_d16_hi v23, v113 offset:6656\n"
    "v_cvt_pk_f16_f32 v116, v116, v117\n"
    "ds_write_b16 v20, v112 offset:6656\n"
    "ds_write_b16_d16_hi v21, v112 offset:6656\n"
    "v_cvt_pk_f16_f32 v117, v118, v119\n"
    "s_mov_b64 exec, s[50:51]\n"
    "ds_write_b16 v24, v116 offset:6656\n"
    "ds_write_b16_d16_hi v25, v116 offset:6656\n"
    "ds_write_b16 v26, v117 offset:6656\n"
    "ds_write_b16_d16_hi v27, v117 offset:6656\n"
    "s_mov_b64 exec, 1\n"
    "ds_add_u32 v14, v15 offset:224\n"
    "s_mov_b64 exec, -1\n"
    "KT3:\n"
    "s_setprio 1\n"
    "s_cmp_eq_u32 s48, 0\n"
    "s_cbranch_scc1 NEAR1\n"
    "s_cmpk_eq_u32 s37, 6\n"
    "s_cbranch_scc1 HELP1\n"
    "s_branch FAR1\n"
    "AUX1:\n"
    "s_waitcnt lgkmcnt(0)\n"
    "s_load_dword s36, s[20:21], 0\n"
    "s_barrier\n"
    "v_lshlrev_b32 v1, 4, v0\n"
    "v_and_b32 v8, 63, v0\n"
    "v_mov_b32 v15, 1\n"
    "v_mov_b32 v14, 147968\n"
    "s_cmpk_gt_u32 s37, 13\n"
    "s_cbranch_scc1 AUXB\n"
    "s_getpc_b64 s[44:45]\n"
    "s_add_u32 s44, s44, mega_tab@rel32@lo+4\n"
    "s_addc_u32 s45, s45, mega_tab@rel32@hi+12\n"
    "s_mov_b32 m0, 107520\n"
    "v_lshlrev_b32 v2, 4, v8\n"
    "v_add_u32 v3, 6144, v2\n"
    "global_load_lds_dwordx4 v3, s[44:45] offset:0\n"
    "global_load_lds_dwordx4 v3, s[44:45] offset:1024\n"
    "global_load_lds_dwordx4 v3, s[44:45] offset:2048\n"
    "global_load_lds_dwordx4 v3, s[44:45] offset:3072\n"
    "s_mov_b32 m0, 111616\n"
    "v_add_u32 v3, 4096, v3\n"
    "global_load_lds_dwordx4 v3, s[44:45] offset:0\n"
    "global_load_lds_dwordx4 v3, s[44:45] offset:1024\n"
    "global_load_lds_dwordx4 v3, s[44:45] offset:2048\n"
    "global_load_lds_dwordx4 v3, s[44:45] offset:3072\n"
    "s_mov_b32 m0, 101376\n"
    "s_nop 0\n"
    "global_load_lds_dwordx4 v2, s[44:45] offset:0\n"
    "global_load_lds_dwordx4 v2, s[44:45] offset:1024\n"
    "global_load_lds_dwordx4 v2, s[44:45] offset:2048\n"
    "global_load_lds_dwordx4 v2, s[44:45] offset:3072\n"
    "s_mov_b32 m0, 105472\n"
    "v_add_u32 v2, 4096, v2\n"
    "global_load_lds_dwordx4 v2, s[44:45] offset:0\n"
    "global_load_lds_dwordx4 v2, s[44:45] offset:1024\n"
    "v_mov_b32 v4, 0\n"
    "v_mov_b32 v5, 0\n"
    "v_mov_b32 v6, 0\n"
    "v_mov_b32 v7, 0\n"
    "v_add_u32 v2, 78080, v1\n"
    "ds_write_b128 v2, v[4:7] offset:0\n"
    "ds_write_b128 v2, v[4:7] offset:1024\n"
    "ds_write_b128 v2, v[4:7] offset:2048\n"
    "ds_write_b128 v2, v[4:7] offset:3072\n"
    "ds_write_b128 v2, v[4:7] offset:4096\n"
    "ds_write_b128 v2, v[4:7] offset:5120\n"
    "ds_write_b128 v2, v[4:7] offset:6144\n"
    "ds_write_b128 v2, v[4:7] offset:7168\n"
    "ds_write_b128 v2, v[4:7] offset:8192\n"
    "v_min_u32 v2, 92144, v2\n"
    "ds_write_b128 v2, v[4:7] offset:9216\n"
    "v_min_u32 v2, 29, v8\n"
    "v_lshlrev_b32 v2, 4, v2\n"
    "v_add_u32 v2, 120832, v2\n"
    "ds_write_b128 v2, v[4:7] offset:0\n"
    "ds_write_b128 v2, v[4:7] offset:4576\n"
    "ds_write_b128 v2, v[4:7] offset:9152\n"
    "s_branch AUX2\n"
    "AUXB:\n"
    "s_lshl_b32 s42, s37, 9\n"
    "s_add_u32 s42, s42, 53760\n"
    "v_lshl_add_u32 v2, v8, 3, s42\n"
    "v_add_u32 v3, 116128, v1\n"
    "s_lshl_b32 s42, s37, 2\n"
    "s_add_u32 s42, s42, 147912\n"
    "v_mov_b32 v12, s42\n"
    "v_mov_b32 v13, 147976\n"
    "s_branch E_10\n"
    "R_9:\n"
    "s_sleep 1\n"
    "s_sub_u32 s39, s39, 1\n"
    "s_cbranch_scc1 ABORT\n"
    "E_10:\n"
    "ds_read_b32 v20, v12 offset:4\n"
    "ds_read_b128 v[16:19], v3\n"
    "s_waitcnt lgkmcnt(0)\n"
    "v_cmp_ne_u32 vcc, 1, v20\n"
    "s_cbranch_vccnz R_9\n"
    "s_add_u32 s36, s36, 1\n"
    "s_setprio 1\n"
    "s_branch BSIDE\n"
    "AUX2:\n"
    "s_mov_b64 exec, -1\n"
    "s_waitcnt vmcnt(0) lgkmcnt(0)\n"
    "s_add_u32 s36, s36, 1\n"
    "s_mov_b64 exec, 1\n"
    "ds_add_u32 v14, v15 offset:24\n"
    "s_mov_b64 exec, -1\n"
    "AUX3:\n"
    "s_mov_b64 exec, -1\n"
    "s_setprio 0\n"
    "s_and_b32 s44, s2, 7\n"
    "s_lshr_b32 s45, s2, 3\n"
    "s_mul_i32 s44, s44, 24\n"
    "s_add_u32 s38, s44, s45\n"
    "v_mov_b32 v14, 147968\n"
    "ds_read_b32 v2, v14 offset:480\n"
    "s_waitcnt lgkmcnt(0)\n"
    "v_readfirstlane_b32 s96, v2\n"
    "s_lshl_b32 s57, s37, 2\n"
    "s_mov_b32 s58, 0x78650432\n"
    "s_lshr_b32 s54, s58, s57\n"
    "s_and_b32 s54, s54, 15\n"
    "s_add_u32 s55, s54, 7\n"
    "s_cmpk_eq_u32 s37, 3\n"
    "s_cselect_b32 s55, 1, s55\n"
    "s_and_b32 s56, s38, 1\n"
    "s_mul_i32 s56, s56, 6144\n"
    "s_lshr_b32 s57, s38, 1\n"
    "s_lshl_b32 s57, s57, 6\n"
    "s_add_u32 s56, s56, s57\n"
    "s_add_u32 s56, s56, 7341056\n"
    "s_mul_i32 s40, s54, 198656\n"
    "s_add_u32 s40, s40, s56\n"
    "s_mul_i32 s41, s55, 198656\n"
    "s_add_u32 s41, s41, s56\n"
    "v_bfe_u32 v66, v0, 2, 4\n"
    "v_mul_u32_u24 v66, 12416, v66\n"
    "v_and_b32 v67, 3, v0\n"
    "v_lshl_add_u32 v66, v67, 4, v66\n"
    "STAGE:\n"
    "v_mov_b32 v78, 147968\n"
    "v_and_b32 v67, 63, v0\n"
    "v_lshlrev_b32 v67, 2, v67\n"
    "s_lshl_b32 s43, s54, 8\n"
    "s_sub_u32 s43, 120828, s43\n"
    "v_sub_u32 v86, s43, v67\n"
    "s_lshl_b32 s43, s55, 8\n"
    "s_sub_u32 s43, 120828, s43\n"
    "v_sub_u32 v87, s43, v67\n"
    "v_mov_b32 v76, 148352\n"
    "v_and_b32 v111, 63, v0\n"
    "s_lshr_b32 s50, s2, 3\n"
    "s_and_b32 s51, s2, 7\n"
    "s_cmpk_ge_u32 s2, 128\n"
    "s_cbranch_scc1 PFA\n"
    "s_lshl_b32 s51, s51, 4\n"
    "s_add_u32 s51, s51, s50\n"
    "s_mul_i32 s51, s51, 18432\n"
    "s_mul_i32 s60, s37, 768\n"
    "s_add_u32 s51, s51, s60\n"
    "v_lshl_add_u32 v111, v111, 4, s51\n"
    "s_add_u32 s60, s60, 53760\n"
    "s_mov_b64 s[58:59], s[4:5]\n"
    "s_branch PFC\n"
    "PFA:\n"
    "s_sub_u32 s50, s50, 16\n"
    "s_cmpk_lt_u32 s37, 4\n"
    "s_cbranch_scc1 PFB\n"
    "s_lshl2_add_u32 s50, s50, s37\n"
    "s_sub_u32 s50, s50, 12\n"
    "s_mul_i32 s50, s50, 54\n"
    "s_mul_i32 s60, s51, 663552\n"
    "s_lshr_b32 s44, s28, 7\n"
    "s_add_u32 s44, s44, s2\n"
    "s_sub_u32 s44, 3, s44\n"
    "s_and_b32 s44, s44, 7\n"
    "s_add_u32 s44, s44, 1\n"
    "s_add_u32 s44, s44, 7\n"
    "s_mul_i32 s45, s44, 57\n"
    "s_lshr_b32 s45, s45, 9\n"
    "s_mul_i32 s45, s45, 9\n"
    "s_sub_u32 s44, s44, s45\n"
    "s_and_b32 s51, s2, 7\n"
    "s_mul_i32 s51, s51, 9\n"
    "s_add_u32 s51, s51, s44\n"
    "s_mul_i32 s44, s44, 576\n"
    "s_add_u32 s50, s50, s44\n"
    "v_min_u32 v111, 53, v111\n"
    "v_add_u32 v111, s50, v111\n"
    "s_branch PFC\n"
    "PFB:\n"
    "s_lshr_b32 s51, s37, 1\n"
    "s_sub_u32 s51, 8, s51\n"
    "s_lshr_b32 s44, s28, 7\n"
    "s_add_u32 s44, s44, s2\n"
    "s_sub_u32 s44, 3, s44\n"
    "s_and_b32 s44, s44, 7\n"
    "s_add_u32 s44, s44, 1\n"
    "s_add_u32 s44, s44, s51\n"
    "s_mul_i32 s45, s44, 57\n"
    "s_lshr_b32 s45, s45, 9\n"
    "s_mul_i32 s45, s45, 9\n"
    "s_sub_u32 s44, s44, s45\n"
    "s_and_b32 s51, s2, 7\n"
    "s_mul_i32 s51, s51, 9\n"
    "s_add_u32 s51, s51, s44\n"
    "s_lshl_b32 s51, s51, 7\n"
    "s_lshl1_add_u32 s44, s50, s37\n"
    "s_mul_i32 s44, s44, 36\n"
    "s_mul_i32 s45, s50, 36\n"
    "s_add_u32 s45, s45, 288\n"
    "s_cmpk_lt_u32 s37, 2\n"
    "s_cselect_b32 s50, s44, s45\n"
    "s_mul_i32 s50, s50, 9216\n"
    "s_add_u32 s51, s51, s50\n"
    "v_min_u32 v111, 35, v111\n"
    "v_mul_u32_u24 v111, 9216, v111\n"
    "v_add_u32 v111, s51, v111\n"
    "PFC:\n"
    "s_mul_i32 s42, s38, 6\n"
    "s_and_b32 s42, s42, -4\n"
    "s_load_dwordx2 s[52:53], s[24:25], s42\n"
    "s_cmpk_eq_u32 s37, 3\n"
    "s_cbranch_scc1 FAR\n"
    "s_branch EPS\n"
    "STG0:\n"
    "s_cmpk_lt_u32 s37, 4\n"
    "s_cbranch_scc1 STG1\n"
    "s_cmpk_lg_u32 s37, 13\n"
    "s_cbranch_scc1 XISS\n"
    "s_branch CONST\n"
    "STG1:\n"
    "s_setprio 2\n"
    "s_branch E_12\n"
    "R_11:\n"
    "s_sub_u32 s39, s39, 1\n"
    "s_cbranch_scc1 ABORT\n"
    "s_wqm_b64 exec, vcc\n"
    "buffer_load_dwordx4 v[68:71], v66, s[20:23], s40 offen sc1\n"
    "buffer_load_dwordx4 v[72:75], v66, s[20:23], s41 offen sc1\n"
    "s_mov_b64 exec, -1\n"
    "E_12:\n"
    "s_waitcnt vmcnt(1)\n"
    "ds_read_b32 v77, v78 offset:40\n"
    "ds_read_b128 v[80:83], v76\n"
    "ds_read_b64 v[84:85], v76 offset:16\n"
    "s_waitcnt vmcnt(0)\n"
    "v_bitop3_b32 v79, v71, v75, s36 bitop3:0x7e\n"
    "v_cmp_ne_u32 vcc, 0, v79\n"
    "s_cbranch_vccnz R_11\n"
    "v_lshlrev_b32 v88, 16, v68\n"
    "v_and_b32 v89, 0xffff0000, v68\n"
    "v_lshlrev_b32 v90, 16, v69\n"
    "v_and_b32 v91, 0xffff0000, v69\n"
    "v_lshlrev_b32 v92, 16, v70\n"
    "v_and_b32 v93, 0xffff0000, v70\n"
    "STG3:\n"
    "s_waitcnt lgkmcnt(0)\n"
    "v_cmp_ne_u32 vcc, 1, v77\n"
    "s_cbranch_vccnz STGW\n"
    "ds_read_b128 v[114:117], v76 offset:32\n"
    "ds_read_b64 v[118:119], v76 offset:48\n"
    "v_pk_mul_f32 v[88:89], v[88:89], v[80:81] op_sel_hi:[1,0]\n"
    "v_pk_mul_f32 v[90:91], v[90:91], v[82:83] op_sel_hi:[1,0]\n"
    "v_pk_mul_f32 v[92:93], v[92:93], v[84:85] op_sel_hi:[1,0]\n"
    "v_cvt_pk_bf16_f32 v88, 0, v88\n"
    "v_cvt_pk_bf16_f32 v89, 0, v89\n"
    "v_cvt_pk_bf16_f32 v90, 0, v90\n"
    "v_cvt_pk_bf16_f32 v91, 0, v91\n"
    "v_cvt_pk_bf16_f32 v92, 0, v92\n"
    "v_cvt_pk_bf16_f32 v93, 0, v93\n"
    "v_pk_add_f32 v[88:89], v[80:81], v[88:89] op_sel:[1,0] op_sel_hi:[1,1]\n"
    "v_pk_add_f32 v[90:91], v[82:83], v[90:91] op_sel:[1,0] op_sel_hi:[1,1]\n"
    "v_pk_add_f32 v[92:93], v[84:85], v[92:93] op_sel:[1,0] op_sel_hi:[1,1]\n"
    "v_cvt_pk_bf16_f32 v88, 0, v88\n"
    "v_cvt_pk_bf16_f32 v89, 0, v89\n"
    "v_cvt_pk_bf16_f32 v90, 0, v90\n"
    "v_cvt_pk_bf16_f32 v91, 0, v91\n"
    "v_cvt_pk_bf16_f32 v92, 0, v92\n"
    "v_cvt_pk_bf16_f32 v93, 0, v93\n"
    "v_cvt_pkrtz_f16_f32 v94, v89, v88\n"
    "ds_write_b32 v86, v94 offset:0\n"
    "v_cvt_pkrtz_f16_f32 v95, v91, v90\n"
    "ds_write_b32 v86, v95 offset:4576\n"
    "v_cvt_pkrtz_f16_f32 v96, v93, v92\n"
    "ds_write_b32 v86, v96 offset:9152\n"
    "v_lshlrev_b32 v88, 16, v72\n"
    "v_and_b32 v89, 0xffff0000, v72\n"
    "v_lshlrev_b32 v90, 16, v73\n"
    "v_and_b32 v91, 0xffff0000, v73\n"
    "v_lshlrev_b32 v92, 16, v74\n"
    "v_and_b32 v93, 0xffff0000, v74\n"
    "v_pk_mul_f32 v[88:89], v[88:89], v[80:81] op_sel_hi:[1,0]\n"
    "v_pk_mul_f32 v[90:91], v[90:91], v[82:83] op_sel_hi:[1,0]\n"
    "v_pk_mul_f32 v[92:93], v[92:93], v[84:85] op_sel_hi:[1,0]\n"
    "v_cvt_pk_bf16_f32 v88, 0, v88\n"
    "v_cvt_pk_bf16_f32 v89, 0, v89\n"
    "v_cvt_pk_bf16_f32 v90, 0, v90\n"
    "v_cvt_pk_bf16_f32 v91, 0, v91\n"
    "v_cvt_pk_bf16_f32 v92, 0, v92\n"
    "v_cvt_pk_bf16_f32 v93, 0, v93\n"
    "v_pk_add_f32 v[88:89], v[80:81], v[88:89] op_sel:[1,0] op_sel_hi:[1,1]\n"
    "v_pk_add_f32 v[90:91], v[82:83], v[90:91] op_sel:[1,0] op_sel_hi:[1,1]\n"
    "v_pk_add_f32 v[92:93], v[84:85], v[92:93] op_sel:[1,0] op_sel_hi:[1,1]\n"
    "v_cvt_pk_bf16_f32 v88, 0, v88\n"
    "v_cvt_pk_bf16_f32 v89, 0, v89\n"
    "v_cvt_pk_bf16_f32 v90, 0, v90\n"
    "v_cvt_pk_bf16_f32 v91, 0, v91\n"
    "v_cvt_pk_bf16_f32 v92, 0, v92\n"
    "v_cvt_pk_bf16_f32 v93, 0, v93\n"
    "v_cvt_pkrtz_f16_f32 v97, v89, v88\n"
    "ds_write_b32 v87, v97 offset:0\n"
    "v_cvt_pkrtz_f16_f32 v98, v91, v90\n"
    "ds_write_b32 v87, v98 offset:4576\n"
    "v_cvt_pkrtz_f16_f32 v99, v93, v92\n"
    "ds_write_b32 v87, v99 offset:9152\n"
    "s_waitcnt lgkmcnt(0)\n"
    "s_barrier\n"
    "ds_read_b128 v[120:123], v76 offset:64\n"
    "ds_read_b64 v[124:125], v76 offset:80\n"
    "s_cmpk_eq_u32 s37, 3\n"
    "s_cbranch_scc1 FAR3\n"
    "s_cmpk_ge_u32 s2, 128\n"
    "s_cbranch_scc1 PFD\n"
    "s_cmpk_ge_u32 s37, 12\n"
    "s_cbranch_scc1 EPI\n"
    "s_mov_b32 m0, s60\n"
    "s_mov_b32 exec_hi, 0xffff\n"
    "global_load_lds_dwordx4 v111, s[58:59] nt\n"
    "s_add_u32 m0, m0, 2320\n"
    "s_add_u32 s58, s58, 2304\n"
    "s_addc_u32 s59, s59, 0\n"
    "global_load_lds_dwordx4 v111, s[58:59] nt\n"
    "s_add_u32 m0, m0, 2320\n"
    "s_add_u32 s58, s58, 2304\n"
    "s_addc_u32 s59, s59, 0\n"
    "global_load_lds_dwordx4 v111, s[58:59] nt\n"
    "s_add_u32 m0, m0, 2320\n"
    "s_add_u32 s58, s58, 2304\n"
    "s_addc_u32 s59, s59, 0\n"
    "global_load_lds_dwordx4 v111, s[58:59] nt\n"
    "s_add_u32 m0, m0, 2320\n"
    "s_add_u32 s58, s58, 2304\n"
    "s_addc_u32 s59, s59, 0\n"
    "global_load_lds_dwordx4 v111, s[58:59] nt\n"
    "s_add_u32 m0, m0, 2320\n"
    "s_add_u32 s58, s58, 2304\n"
    "s_addc_u32 s59, s59, 0\n"
    "global_load_lds_dwordx4 v111, s[58:59] nt\n"
    "s_add_u32 m0, m0, 2320\n"
    "s_add_u32 s58, s58, 2304\n"
    "s_addc_u32 s59, s59, 0\n"
    "global_load_lds_dwordx4 v111, s[58:59] nt\n"
    "s_add_u32 m0, m0, 2320\n"
    "s_add_u32 s58, s58, 2304\n"
    "s_addc_u32 s59, s59, 0\n"
    "global_load_lds_dwordx4 v111, s[58:59] nt\n"
    "s_add_u32 m0, m0, 2320\n"
    "s_add_u32 s58, s58, 2304\n"
    "s_addc_u32 s59, s59, 0\n"
    "s_mov_b64 exec, -1\n"
    "s_branch EPI\n"
    "PFD:\n"
    "s_mov_b32 m0, 148480\n"
    "s_cmpk_ge_u32 s37, 12\n"
    "s_cbranch_scc1 PFE\n"
    "global_load_lds_dword v111, s[28:29]\n"
    "global_load_lds_dword v111, s[30:31]\n"
    "s_branch EPI\n"
    "PFE:\n"
    "v_subrev_u32 v112, 5184, v111\n"
    "v_min_u32 v111, v111, v112\n"
    "v_lshl_add_u32 v112, v111, 7, s60\n"
    "global_load_lds_dword v112, s[32:33]\n"
    "v_add_u32 v111, 1728, v111\n"
    "s_branch EPI\n"
    "STGW:\n"
    "s_sub_u32 s39, s39, 1\n"
    "s_cbranch_scc1 ABORT\n"
    "ds_read_b32 v77, v78 offset:40\n"
    "ds_read_b128 v[80:83], v76\n"
    "ds_read_b64 v[84:85], v76 offset:16\n"
    "s_branch STG3\n"
    "CONST:\n"
    "v_mov_b32 v4, 0\n"
    "v_mov_b32 v6, 0\n"
    "s_setprio 3\n"
    "v_and_b32 v2, 15, v0\n"
    "v_bfe_u32 v3, v0, 4, 2\n"
    "v_mul_u32_u24 v3, 4608, v3\n"
    "s_movk_i32 s43, 13824\n"
    "v_mad_u32_u24 v3, v2, s43, v3\n"
    "s_mul_i32 s42, s38, 24\n"
    "s_add_u32 s42, s42, 10519552\n"
    "v_lshlrev_b32 v2, 1, v2\n"
    "v_add_u32 v2, 148352, v2\n"
    "s_mov_b32 s43, 0x42000000\n"
    "s_mov_b32 s52, 0x01ff01ff\n"
    "s_movk_i32 s53, 0x01ff\n"
    "s_mov_b32 s62, 0\n"
    "s_movk_i32 s63, 0x0111\n"
    "v_mov_b32 v8, 0\n"
    "s_mov_b64 exec, s[52:53]\n"
    "U_13:\n"
    "s_memrealtime s[98:99]\n"
    "s_waitcnt lgkmcnt(0)\n"
    "s_sub_u32 s98, s98, s96\n"
    "s_cmpk_lt_u32 s98, 200\n"
    "s_cbranch_scc1 U_13\n"
    "buffer_load_dwordx4 v[4:7], v3, s[20:23], s42 offen sc1\n"
    "buffer_load_dwordx2 v[8:9], v3, s[20:23], s42 offen offset:16 sc1\n"
    "s_mov_b64 exec, -1\n"
    "XISS:\n"
    "U_14:\n"
    "s_memrealtime s[98:99]\n"
    "s_waitcnt lgkmcnt(0)\n"
    "s_sub_u32 s98, s98, s96\n"
    "s_cmpk_lt_u32 s98, 210\n"
    "s_cbranch_scc1 U_14\n"
    "buffer_load_dwordx4 v[68:71], v66, s[20:23], s40 offen sc1\n"
    "buffer_load_dwordx4 v[72:75], v66, s[20:23], s41 offen sc1\n"
    "s_cmpk_lt_u32 s37, 4\n"
    "s_cbranch_scc1 STAGE\n"
    "s_cmpk_lg_u32 s37, 13\n"
    "s_cbranch_scc1 STG1\n"
    "s_mov_b64 exec, s[52:53]\n"
    "s_waitcnt vmcnt(2)\n"
    "s_branch E_16\n"
    "R_15:\n"
    "s_sub_u32 s39, s39, 1\n"
    "s_cbranch_scc1 ABORT\n"
    "s_and_b64 exec, exec, vcc\n"
    "buffer_load_dwordx4 v[4:7], v3, s[20:23], s42 offen sc1\n"
    "buffer_load_dwordx2 v[8:9], v3, s[20:23], s42 offen offset:16 sc1\n"
    "s_waitcnt vmcnt(0)\n"
    "E_16:\n"
    "v_bitop3_b32 v10, v5, v7, s36 bitop3:0x7e\n"
    "v_bitop3_b32 v10, v10, v9, s36 bitop3:0xf6\n"
    "v_cmp_ne_u32 vcc, 0, v10\n"
    "s_cbranch_vccnz R_15\n"
    "s_mov_b64 exec, -1\n"
    "v_add_f32_dpp v4, v4, v4 row_ror:8 row_mask:0xf bank_mask:0xf\n"
    "v_add_f32_dpp v6, v6, v6 row_ror:8 row_mask:0xf bank_mask:0xf\n"
    "v_add_f32_dpp v8, v8, v8 row_ror:8 row_mask:0xf bank_mask:0xf\n"
    "v_add_f32_dpp v4, v4, v4 row_ror:4 row_mask:0xf bank_mask:0xf\n"
    "v_add_f32_dpp v6, v6, v6 row_ror:4 row_mask:0xf bank_mask:0xf\n"
    "v_add_f32_dpp v8, v8, v8 row_ror:4 row_mask:0xf bank_mask:0xf\n"
    "s_nop 0\n"
    "v_mov_b32_dpp v4, v6 quad_perm:[0,1,2,3] row_mask:0xf bank_mask:0x2\n"
    "s_nop 1\n"
    "v_mov_b32_dpp v4, v8 quad_perm:[0,1,2,3] row_mask:0xf bank_mask:0x4\n"
    "s_nop 1\n"
    "v_add_f32_dpp v4, v4, v4 quad_perm:[1,0,3,2] row_mask:0xf bank_mask:0xf\n"
    "s_nop 1\n"
    "v_add_f32_dpp v4, v4, v4 quad_perm:[2,3,0,1] row_mask:0xf bank_mask:0xf\n"
    "v_cvt_pk_bf16_f32 v5, 0, v4\n"
    "v_add_f32 v4, 1.0, v5\n"
    "s_mov_b64 exec, s[62:63]\n"
    "ds_write_b32 v2, v5 offset:36\n"
    "s_mov_b64 exec, -1\n"
    "v_cvt_pk_bf16_f32 v4, 0, v4\n"
    "v_mov_b32 v9, v4\n"
    "s_nop 1\n"
    "v_permlane16_swap_b32 v9, v4\n"
    "v_fma_f32 v6, abs(v4), s43, abs(v5)\n"
    "v_frexp_exp_i32_f32 v6, v6\n"
    "v_sub_u32 v7, 11, v6\n"
    "v_ldexp_f32 v10, v4, v7\n"
    "v_ldexp_f32 v11, v5, v7\n"
    "v_subrev_u32 v8, 11, v6\n"
    "v_ldexp_f32 v8, 1.0, v8\n"
    "s_mov_b64 exec, 0x0111\n"
    "ds_write_b64 v2, v[10:11]\n"
    "ds_write_b32 v2, v8 offset:32\n"
    "ds_write_b32 v14, v15 offset:40\n"
    "s_mov_b64 exec, -1\n"
    "s_branch STG1\n"
    "FAR:\n"
    "s_lshr_b32 s42, s37, 2\n"
    "v_and_b32 v6, 15, v0\n"
    "v_bfe_u32 v7, v0, 4, 2\n"
    "v_and_b32 v1, 63, v0\n"
    "v_lshlrev_b32 v1, 4, v1\n"
    "v_add_u32 v1, 101376, v1\n"
    "s_mul_i32 s43, s42, 4576\n"
    "s_add_u32 s43, s43, 117216\n"
    "v_lshlrev_b32 v2, 1, v6\n"
    "v_sub_u32 v2, v7, v2\n"
    "v_lshl_add_u32 v2, v2, 4, s43\n"
    "s_mul_i32 s44, s42, 3328\n"
    "s_add_u32 s43, s44, 92596\n"
    "v_lshlrev_b32 v3, 2, v7\n"
    "v_sub_u32 v3, v6, v3\n"
    "v_lshl_add_u32 v3, v3, 2, s43\n"
    "s_mul_i32 s43, s42, 7680\n"
    "s_add_u32 s43, s43, 28160\n"
    "v_mul_u32_u24 v4, 160, v7\n"
    "v_lshl_add_u32 v4, v6, 3, v4\n"
    "v_add_u32 v4, s43, v4\n"
    "s_add_u32 s43, s42, 147968\n"
    "v_mov_b32 v5, s43\n"
    "v_mov_b32 v8, 1\n"
    "v_mov_b32 v11, 147968\n"
    "s_cmpk_ge_u32 s37, 6\n"
    "s_cbranch_scc1 S2\n"
    "s_branch STG0\n"
    "FAR1:\n"
    "s_branch E_18\n"
    "R_17:\n"
    "s_sleep 1\n"
    "s_sub_u32 s39, s39, 1\n"
    "s_cbranch_scc1 ABORT\n"
    "E_18:\n"
    "ds_read_b32 v12, v11 offset:224\n"
    "s_waitcnt lgkmcnt(0)\n"
    "v_cmp_ne_u32 vcc, 6, v12\n"
    "s_cbranch_vccnz R_17\n"
    "FAR3:\n"
    "ds_read_b128 v[12:15], v1 offset:0\n"
    "ds_read_b128 v[16:19], v1 offset:1024\n"
    "ds_read_b128 v[20:23], v1 offset:2048\n"
    "ds_read2_b32 v[24:25], v3 offset0:0 offset1:1\n"
    "ds_read2_b32 v[26:27], v3 offset0:2 offset1:3\n"
    "ds_read2_b32 v[36:37], v3 offset0:64 offset1:65\n"
    "ds_read2_b32 v[38:39], v3 offset0:66 offset1:67\n"
    "ds_read2_b32 v[28:29], v3 offset0:16 offset1:17\n"
    "ds_read2_b32 v[30:31], v3 offset0:18 offset1:19\n"
    "ds_read2_b32 v[40:41], v3 offset0:80 offset1:81\n"
    "ds_read2_b32 v[42:43], v3 offset0:82 offset1:83\n"
    "ds_read2_b32 v[32:33], v3 offset0:32 offset1:33\n"
    "ds_read2_b32 v[34:35], v3 offset0:34 offset1:35\n"
    "ds_read2_b32 v[44:45], v3 offset0:96 offset1:97\n"
    "ds_read2_b32 v[46:47], v3 offset0:98 offset1:99\n"
    "s_cmpk_eq_u32 s37, 3\n"
    "s_cbranch_scc1 FAR2\n"
    "s_barrier\n"
    "FAR2:\n"
    "s_setprio 3\n"
    "ds_read_b128 v[48:51], v2 offset:3584\n"
    "ds_read_b128 v[52:55], v2 offset:3600\n"
    "ds_read_b128 v[56:59], v2 offset:3072\n"
    "ds_read_b128 v[60:63], v2 offset:3088\n"
    "ds_read_b128 v[64:67], v2 offset:2560\n"
    "ds_read_b128 v[68:71], v2 offset:2576\n"
    "s_waitcnt lgkmcnt(4)\n"
    "v_mfma_f32_16x16x32_f16 v[72:75], v[48:51], v[12:15], 0\n"
    "v_mfma_f32_16x16x32_f16 v[76:79], v[48:51], v[16:19], 0\n"
    "v_mfma_f32_16x16x32_f16 v[80:83], v[48:51], v[20:23], 0\n"
    "v_mfma_f32_16x16x32_f16 v[84:87], v[52:55], v[20:23], 0\n"
    "s_nop 5\n"
    "v_cvt_pk_f16_f32 v88, v75, v79\n"
    "v_cvt_pk_f16_f32 v89, v74, v78\n"
    "v_cvt_pk_f16_f32 v90, v73, v77\n"
    "v_cvt_pk_f16_f32 v91, v72, v76\n"
    "v_cvt_pk_f16_f32 v92, v83, v87\n"
    "v_cvt_pk_f16_f32 v93, v82, v86\n"
    "v_cvt_pk_f16_f32 v94, v81, v85\n"
    "v_cvt_pk_f16_f32 v95, v80, v84\n"
    "s_waitcnt lgkmcnt(2)\n"
    "v_mfma_f32_16x16x32_f16 v[72:75], v[56:59], v[12:15], 0\n"
    "ds_read_b128 v[48:51], v2 offset:2048\n"
    "v_mfma_f32_16x16x32_f16 v[76:79], v[56:59], v[16:19], 0\n"
    "ds_read_b128 v[52:55], v2 offset:2064\n"
    "v_mfma_f32_16x16x32_f16 v[80:83], v[56:59], v[20:23], 0\n"
    "v_mfma_f32_16x16x32_f16 v[84:87], v[60:63], v[20:23], 0\n"
    "v_mfma_f32_16x16x32_f16 v[104:107], v[88:91], v[24:27], 0\n"
    "v_mfma_f32_16x16x32_f16 v[104:107], v[92:95], v[36:39], v[104:107]\n"
    "v_mfma_f32_16x16x32_f16 v[108:111], v[88:91], v[28:31], 0\n"
    "s_nop 1\n"
    "v_cvt_pk_f16_f32 v96, v75, v79\n"
    "v_cvt_pk_f16_f32 v97, v74, v78\n"
    "v_mfma_f32_16x16x32_f16 v[108:111], v[92:95], v[40:43], v[108:111]\n"
    "v_cvt_pk_f16_f32 v98, v73, v77\n"
    "v_cvt_pk_f16_f32 v99, v72, v76\n"
    "v_mfma_f32_16x16x32_f16 v[112:115], v[88:91], v[32:35], 0\n"
    "v_cvt_pk_f16_f32 v100, v83, v87\n"
    "v_cvt_pk_f16_f32 v101, v82, v86\n"
    "v_mfma_f32_16x16x32_f16 v[112:115], v[92:95], v[44:47], v[112:115]\n"
    "v_cvt_pk_f16_f32 v102, v81, v85\n"
    "v_cvt_pk_f16_f32 v103, v80, v84\n"
    "s_waitcnt lgkmcnt(2)\n"
    "v_mfma_f32_16x16x32_f16 v[72:75], v[64:67], v[12:15], 0\n"
    "ds_read_b128 v[56:59], v2 offset:1536\n"
    "v_mfma_f32_16x16x32_f16 v[76:79], v[64:67], v[16:19], 0\n"
    "ds_read_b128 v[60:63], v2 offset:1552\n"
    "v_mfma_f32_16x16x32_f16 v[80:83], v[64:67], v[20:23], 0\n"
    "ds_write_b64 v4, v[104:105] offset:2560\n"
    "v_mfma_f32_16x16x32_f16 v[84:87], v[68:71], v[20:23], 0\n"
    "ds_write_b64 v4, v[106:107] offset:3200\n"
    "v_mfma_f32_16x16x32_f16 v[108:111], v[96:99], v[24:27], v[108:111]\n"
    "ds_write_b8 v5, v8 offset:83\n"
    "v_mfma_f32_16x16x32_f16 v[108:111], v[100:103], v[36:39], v[108:111]\n"
    "v_mfma_f32_16x16x32_f16 v[112:115], v[96:99], v[28:31], v[112:115]\n"
    "v_cvt_pk_f16_f32 v88, v75, v79\n"
    "v_cvt_pk_f16_f32 v89, v74, v78\n"
    "v_mfma_f32_16x16x32_f16 v[112:115], v[100:103], v[40:43], v[112:115]\n"
    "v_cvt_pk_f16_f32 v90, v73, v77\n"
    "v_cvt_pk_f16_f32 v91, v72, v76\n"
    "v_mfma_f32_16x16x32_f16 v[116:119], v[96:99], v[32:35], 0\n"
    "v_cvt_pk_f16_f32 v92, v83, v87\n"
    "v_cvt_pk_f16_f32 v93, v82, v86\n"
    "v_mfma_f32_16x16x32_f16 v[116:119], v[100:103], v[44:47], v[116:119]\n"
    "v_cvt_pk_f16_f32 v94, v81, v85\n"
    "v_cvt_pk_f16_f32 v95, v80, v84\n"
    "s_setprio 2\n"
    "s_waitcnt lgkmcnt(5)\n"
    "v_mfma_f32_16x16x32_f16 v[72:75], v[48:51], v[12:15], 0\n"
    "v_mfma_f32_16x16x32_f16 v[76:79], v[48:51], v[16:19], 0\n"
    "v_mfma_f32_16x16x32_f16 v[80:83], v[48:51], v[20:23], 0\n"
    "ds_write_b64 v4, v[108:109] offset:3840\n"
    "v_mfma_f32_16x16x32_f16 v[84:87], v[52:55], v[20:23], 0\n"
    "ds_write_b64 v4, v[110:111] offset:4480\n"
    "v_mfma_f32_16x16x32_f16 v[112:115], v[88:91], v[24:27], v[112:115]\n"
    "ds_write_b8 v5, v8 offset:91\n"
    "v_mfma_f32_16x16x32_f16 v[112:115], v[92:95], v[36:39], v[112:115]\n"
    "v_mfma_f32_16x16x32_f16 v[116:119], v[88:91], v[28:31], v[116:119]\n"
    "v_cvt_pk_f16_f32 v96, v75, v79\n"
    "v_cvt_pk_f16_f32 v97, v74, v78\n"
    "v_mfma_f32_16x16x32_f16 v[116:119], v[92:95], v[40:43], v[116:119]\n"
    "v_cvt_pk_f16_f32 v98, v73, v77\n"
    "v_cvt_pk_f16_f32 v99, v72, v76\n"
    "v_mfma_f32_16x16x32_f16 v[120:123], v[88:91], v[32:35], 0\n"
    "v_cvt_pk_f16_f32 v100, v83, v87\n"
    "v_cvt_pk_f16_f32 v101, v82, v86\n"
    "v_mfma_f32_16x16x32_f16 v[120:123], v[92:95], v[44:47], v[120:123]\n"
    "v_cvt_pk_f16_f32 v102, v81, v85\n"
    "v_cvt_pk_f16_f32 v103, v80, v84\n"
    "s_setprio 1\n"
    "s_waitcnt lgkmcnt(6)\n"
    "v_mfma_f32_16x16x32_f16 v[72:75], v[56:59], v[12:15], 0\n"
    "v_mfma_f32_16x16x32_f16 v[76:79], v[56:59], v[16:19], 0\n"
    "v_mfma_f32_16x16x32_f16 v[80:83], v[56:59], v[20:23], 0\n"
    "ds_write_b64 v4, v[112:113] offset:5120\n"
    "v_mfma_f32_16x16x32_f16 v[84:87], v[60:63], v[20:23], 0\n"
    "ds_write_b64 v4, v[114:115] offset:5760\n"
    "v_mfma_f32_16x16x32_f16 v[116:119], v[96:99], v[24:27], v[116:119]\n"
    "ds_write_b8 v5, v8 offset:99\n"
    "v_mfma_f32_16x16x32_f16 v[116:119], v[100:103], v[36:39], v[116:119]\n"
    "v_mfma_f32_16x16x32_f16 v[120:123], v[96:99], v[28:31], v[120:123]\n"
    "v_cvt_pk_f16_f32 v88, v75, v79\n"
    "v_cvt_pk_f16_f32 v89, v74, v78\n"
    "v_mfma_f32_16x16x32_f16 v[120:123], v[100:103], v[40:43], v[120:123]\n"
    "v_cvt_pk_f16_f32 v90, v73, v77\n"
    "v_cvt_pk_f16_f32 v91, v72, v76\n"
    "v_mfma_f32_16x16x32_f16 v[124:127], v[96:99], v[32:35], 0\n"
    "v_cvt_pk_f16_f32 v92, v83, v87\n"
    "v_cvt_pk_f16_f32 v93, v82, v86\n"
    "v_mfma_f32_16x16x32_f16 v[124:127], v[100:103], v[44:47], v[124:127]\n"
    "v_cvt_pk_f16_f32 v94, v81, v85\n"
    "v_cvt_pk_f16_f32 v95, v80, v84\n"
    "s_setprio 0\n"
    "v_mfma_f32_16x16x32_f16 v[120:123], v[88:91], v[24:27], v[120:123]\n"
    "v_mfma_f32_16x16x32_f16 v[120:123], v[92:95], v[36:39], v[120:123]\n"
    "ds_write_b64 v4, v[116:117] offset:6400\n"
    "v_mfma_f32_16x16x32_f16 v[124:127], v[88:91], v[28:31], v[124:127]\n"
    "ds_write_b64 v4, v[118:119] offset:7040\n"
    "v_mfma_f32_16x16x32_f16 v[124:127], v[92:95], v[40:43], v[124:127]\n"
    "ds_write_b8 v5, v8 offset:107\n"
    "s_nop 2\n"
    "ds_write_b64 v4, v[120:121] offset:7680\n"
    "ds_write_b64 v4, v[122:123] offset:8320\n"
    "ds_write_b8 v5, v8 offset:115\n"
    "s_nop 0\n"
    "ds_write_b64 v4, v[124:125] offset:8960\n"
    "ds_write_b64 v4, v[126:127] offset:9600\n"
    "ds_write_b8 v5, v8 offset:123\n"
    "s_setprio 0\n"
    "s_branch LN2\n"
    "HELP:\n"
    "s_mov_b64 exec, -1\n"
    "s_sub_u32 s42, s37, 4\n"
    "v_and_b32 v6, 15, v0\n"
    "v_bfe_u32 v7, v0, 4, 2\n"
    "v_and_b32 v1, 63, v0\n"
    "v_lshlrev_b32 v1, 4, v1\n"
    "v_add_u32 v1, 101376, v1\n"
    "s_mul_i32 s43, s42, 4576\n"
    "s_add_u32 s43, s43, 117216\n"
    "v_lshlrev_b32 v2, 1, v6\n"
    "v_sub_u32 v2, v7, v2\n"
    "v_lshl_add_u32 v2, v2, 4, s43\n"
    "s_mul_i32 s44, s42, 3328\n"
    "s_add_u32 s43, s44, 92596\n"
    "v_lshlrev_b32 v3, 2, v7\n"
    "v_sub_u32 v3, v6, v3\n"
    "v_lshl_add_u32 v3, v3, 2, s43\n"
    "s_mul_i32 s43, s42, 10240\n"
    "s_add_u32 s43, s43, 0\n"
    "v_mul_u32_u24 v4, 160, v7\n"
    "v_lshl_add_u32 v4, v6, 3, v4\n"
    "v_add_u32 v4, s43, v4\n"
    "s_add_u32 s43, s42, 147968\n"
    "v_mov_b32 v5, s43\n"
    "v_mov_b32 v8, 1\n"
    "v_mov_b32 v11, 147968\n"
    "s_add_u32 s44, s44, 92990\n"
    "v_mov_b32 v9, s44\n"
    "s_mul_i32 s43, s42, 3\n"
    "v_add_u32 v5, s43, v5\n"
    "s_cmpk_ge_u32 s37, 6\n"
    "s_cbranch_scc1 S2\n"
    "HELP1:\n"
    "s_mov_b32 s43, 0x01000100\n"
    "s_branch E_20\n"
    "R_19:\n"
    "s_sleep 1\n"
    "s_sub_u32 s39, s39, 1\n"
    "s_cbranch_scc1 ABORT\n"
    "E_20:\n"
    "ds_read_b32 v12, v11 offset:224\n"
    "s_waitcnt lgkmcnt(0)\n"
    "v_cmp_ne_u32 vcc, 6, v12\n"
    "s_cbranch_vccnz R_19\n"
    "ds_read_b128 v[12:15], v1 offset:3072\n"
    "ds_read_b128 v[16:19], v1 offset:4096\n"
    "ds_read_b128 v[20:23], v1 offset:5120\n"
    "ds_read_u16 v10, v9\n"
    "ds_read2_b32 v[24:25], v3 offset0:128 offset1:129\n"
    "ds_read2_b32 v[26:27], v3 offset0:130 offset1:131\n"
    "ds_read2_b32 v[28:29], v3 offset0:144 offset1:145\n"
    "ds_read2_b32 v[30:31], v3 offset0:146 offset1:147\n"
    "ds_read2_b32 v[32:33], v3 offset0:160 offset1:161\n"
    "ds_read2_b32 v[34:35], v3 offset0:162 offset1:163\n"
    "ds_read2_b32 v[36:37], v3 offset0:176 offset1:177\n"
    "ds_read2_b32 v[38:39], v3 offset0:178 offset1:179\n"
    "s_waitcnt lgkmcnt(8)\n"
    "v_perm_b32 v10, v10, v10, s43\n"
    "v_and_b32 v20, v20, v10\n"
    "v_and_b32 v21, v21, v10\n"
    "v_and_b32 v22, v22, v10\n"
    "v_and_b32 v23, v23, v10\n"
    "s_setprio 3\n"
    "s_barrier\n"
    "ds_read_b128 v[40:43], v2 offset:3584\n"
    "ds_read_b128 v[44:47], v2 offset:3600\n"
    "ds_read_b128 v[48:51], v2 offset:3072\n"
    "ds_read_b128 v[52:55], v2 offset:3088\n"
    "ds_read_b128 v[56:59], v2 offset:2560\n"
    "ds_read_b128 v[60:63], v2 offset:2576\n"
    "ds_read_b128 v[64:67], v2 offset:2048\n"
    "ds_read_b128 v[68:71], v2 offset:2064\n"
    "ds_read_b128 v[104:107], v2 offset:1040\n"
    "s_waitcnt lgkmcnt(7)\n"
    "v_mfma_f32_16x16x32_f16 v[72:75], v[40:43], v[12:15], 0\n"
    "v_mfma_f32_16x16x32_f16 v[76:79], v[44:47], v[16:19], 0\n"
    "ds_read_b128 v[44:47], v1 offset:0\n"
    "s_waitcnt lgkmcnt(6)\n"
    "v_mfma_f32_16x16x32_f16 v[80:83], v[48:51], v[12:15], 0\n"
    "v_mfma_f32_16x16x32_f16 v[84:87], v[52:55], v[16:19], 0\n"
    "ds_read_b128 v[52:55], v1 offset:1024\n"
    "s_waitcnt lgkmcnt(5)\n"
    "v_mfma_f32_16x16x32_f16 v[88:91], v[56:59], v[12:15], 0\n"
    "v_mfma_f32_16x16x32_f16 v[92:95], v[60:63], v[16:19], 0\n"
    "ds_read_b128 v[60:63], v1 offset:2048\n"
    "s_waitcnt lgkmcnt(4)\n"
    "v_mfma_f32_16x16x32_f16 v[96:99], v[64:67], v[12:15], 0\n"
    "v_mfma_f32_16x16x32_f16 v[100:103], v[68:71], v[16:19], 0\n"
    "ds_read_b128 v[68:71], v2 offset:1024\n"
    "v_mfma_f32_16x16x32_f16 v[108:111], v[20:23], v[40:43], 0\n"
    "v_cvt_pk_f16_f32 v12, v75, v79\n"
    "v_cvt_pk_f16_f32 v13, v74, v78\n"
    "v_mfma_f32_16x16x32_f16 v[112:115], v[20:23], v[48:51], 0\n"
    "v_cvt_pk_f16_f32 v14, v73, v77\n"
    "v_cvt_pk_f16_f32 v15, v72, v76\n"
    "v_mfma_f32_16x16x32_f16 v[116:119], v[20:23], v[56:59], 0\n"
    "v_cvt_pk_f16_f32 v16, v83, v87\n"
    "v_cvt_pk_f16_f32 v17, v82, v86\n"
    "v_mfma_f32_16x16x32_f16 v[120:123], v[20:23], v[64:67], 0\n"
    "v_cvt_pk_f16_f32 v18, v81, v85\n"
    "v_cvt_pk_f16_f32 v19, v80, v84\n"
    "ds_read2_b32 v[40:41], v3 offset0:0 offset1:1\n"
    "ds_read2_b32 v[42:43], v3 offset0:2 offset1:3\n"
    "ds_read2_b32 v[48:49], v3 offset0:64 offset1:65\n"
    "ds_read2_b32 v[50:51], v3 offset0:66 offset1:67\n"
    "v_mfma_f32_16x16x32_f16 v[108:111], v[12:15], v[24:27], v[108:111]\n"
    "v_cvt_pk_f16_f32 v20, v91, v95\n"
    "v_cvt_pk_f16_f32 v21, v90, v94\n"
    "v_mfma_f32_16x16x32_f16 v[112:115], v[12:15], v[28:31], v[112:115]\n"
    "v_cvt_pk_f16_f32 v22, v89, v93\n"
    "v_cvt_pk_f16_f32 v23, v88, v92\n"
    "v_mfma_f32_16x16x32_f16 v[116:119], v[12:15], v[32:35], v[116:119]\n"
    "v_cvt_pk_f16_f32 v124, v99, v103\n"
    "v_cvt_pk_f16_f32 v125, v98, v102\n"
    "v_mfma_f32_16x16x32_f16 v[120:123], v[12:15], v[36:39], v[120:123]\n"
    "v_cvt_pk_f16_f32 v126, v97, v101\n"
    "v_cvt_pk_f16_f32 v127, v96, v100\n"
    "s_waitcnt lgkmcnt(4)\n"
    "v_mfma_f32_16x16x32_f16 v[72:75], v[68:71], v[44:47], 0\n"
    "v_mfma_f32_16x16x32_f16 v[76:79], v[68:71], v[52:55], 0\n"
    "v_mfma_f32_16x16x32_f16 v[80:83], v[68:71], v[60:63], 0\n"
    "ds_write_b64 v4, v[108:109] offset:5120\n"
    "v_mfma_f32_16x16x32_f16 v[84:87], v[104:107], v[60:63], 0\n"
    "ds_write_b64 v4, v[110:111] offset:5760\n"
    "v_mfma_f32_16x16x32_f16 v[112:115], v[16:19], v[24:27], v[112:115]\n"
    "v_mfma_f32_16x16x32_f16 v[116:119], v[16:19], v[28:31], v[116:119]\n"
    "v_mfma_f32_16x16x32_f16 v[120:123], v[16:19], v[32:35], v[120:123]\n"
    "s_nop 0\n"
    "v_cvt_pk_f16_f32 v12, v75, v79\n"
    "v_cvt_pk_f16_f32 v13, v74, v78\n"
    "v_mfma_f32_16x16x32_f16 v[116:119], v[20:23], v[24:27], v[116:119]\n"
    "v_cvt_pk_f16_f32 v14, v73, v77\n"
    "v_cvt_pk_f16_f32 v15, v72, v76\n"
    "v_mfma_f32_16x16x32_f16 v[120:123], v[20:23], v[28:31], v[120:123]\n"
    "v_cvt_pk_f16_f32 v16, v83, v87\n"
    "v_cvt_pk_f16_f32 v17, v82, v86\n"
    "v_mfma_f32_16x16x32_f16 v[120:123], v[124:127], v[24:27], v[120:123]\n"
    "v_cvt_pk_f16_f32 v18, v81, v85\n"
    "v_cvt_pk_f16_f32 v19, v80, v84\n"
    "s_waitcnt lgkmcnt(2)\n"
    "v_mfma_f32_16x16x32_f16 v[120:123], v[12:15], v[40:43], v[120:123]\n"
    "ds_write_b64 v4, v[112:113] offset:6400\n"
    "ds_write_b64 v4, v[114:115] offset:7040\n"
    "v_mfma_f32_16x16x32_f16 v[120:123], v[16:19], v[48:51], v[120:123]\n"
    "ds_write_b64 v4, v[116:117] offset:7680\n"
    "ds_write_b64 v4, v[118:119] offset:8320\n"
    "s_nop 5\n"
    "ds_write_b64 v4, v[120:121] offset:8960\n"
    "ds_write_b64 v4, v[122:123] offset:9600\n"
    "ds_write_b8 v5, v8 offset:44\n"
    "s_branch MISC\n"
    "NEAR:\n"
    "v_and_b32 v6, 15, v0\n"
    "v_bfe_u32 v7, v0, 4, 2\n"
    "v_and_b32 v1, 63, v0\n"
    "v_lshlrev_b32 v1, 4, v1\n"
    "v_add_u32 v1, 101376, v1\n"
    "s_mul_i32 s43, s42, 4576\n"
    "s_add_u32 s43, s43, 117216\n"
    "v_lshlrev_b32 v2, 1, v6\n"
    "v_sub_u32 v2, v7, v2\n"
    "v_lshl_add_u32 v2, v2, 4, s43\n"
    "s_mul_i32 s44, s42, 3328\n"
    "s_add_u32 s43, s44, 92596\n"
    "v_lshlrev_b32 v3, 2, v7\n"
    "v_sub_u32 v3, v6, v3\n"
    "v_lshl_add_u32 v3, v3, 2, s43\n"
    "s_mul_i32 s43, s42, 10240\n"
    "s_add_u32 s43, s43, 0\n"
    "v_mul_u32_u24 v4, 160, v7\n"
    "v_lshl_add_u32 v4, v6, 3, v4\n"
    "v_add_u32 v4, s43, v4\n"
    "s_add_u32 s43, s42, 147968\n"
    "v_mov_b32 v5, s43\n"
    "v_mov_b32 v8, 1\n"
    "v_mov_b32 v11, 147968\n"
    "v_lshl_add_u32 v3, v7, 3, v6\n"
    "v_add_u32 v3, 1, v3\n"
    "v_and_b32 v9, 1, v3\n"
    "v_mul_u32_u24 v9, 2110, v9\n"
    "s_add_u32 s44, s44, 91392\n"
    "v_lshl_add_u32 v3, v3, 1, s44\n"
    "v_add_u32 v3, v3, v9\n"
    "v_add_u32 v1, 512, v3\n"
    "s_mul_i32 s43, s42, 3\n"
    "v_add_u32 v10, s43, v5\n"
    "s_branch S2\n"
    "NEAR1:\n"
    "s_branch E_22\n"
    "R_21:\n"
    "s_sub_u32 s39, s39, 1\n"
    "s_cbranch_scc1 ABORT\n"
    "E_22:\n"
    "ds_read_b32 v12, v11 offset:224\n"
    "s_waitcnt lgkmcnt(0)\n"
    "v_cmp_ne_u32 vcc, 6, v12\n"
    "s_cbranch_vccnz R_21\n"
    "ds_read2_b32 v[88:89], v3 offset0:112 offset1:113\n"
    "ds_read2_b32 v[90:91], v3 offset0:114 offset1:115\n"
    "ds_read2_b32 v[120:121], v1 offset0:112 offset1:113\n"
    "ds_read2_b32 v[122:123], v1 offset0:114 offset1:115\n"
    "ds_read2_b32 v[84:85], v3 offset0:96 offset1:97\n"
    "ds_read2_b32 v[86:87], v3 offset0:98 offset1:99\n"
    "ds_read2_b32 v[116:117], v1 offset0:96 offset1:97\n"
    "ds_read2_b32 v[118:119], v1 offset0:98 offset1:99\n"
    "ds_read2_b32 v[80:81], v3 offset0:80 offset1:81\n"
    "ds_read2_b32 v[82:83], v3 offset0:82 offset1:83\n"
    "ds_read2_b32 v[112:113], v1 offset0:80 offset1:81\n"
    "ds_read2_b32 v[114:115], v1 offset0:82 offset1:83\n"
    "ds_read2_b32 v[76:77], v3 offset0:64 offset1:65\n"
    "ds_read2_b32 v[78:79], v3 offset0:66 offset1:67\n"
    "ds_read2_b32 v[108:109], v1 offset0:64 offset1:65\n"
    "ds_read2_b32 v[110:111], v1 offset0:66 offset1:67\n"
    "s_setprio 3\n"
    "s_barrier\n"
    "ds_read_b128 v[24:27], v2 offset:4032\n"
    "ds_read_b128 v[28:31], v2 offset:3968\n"
    "ds_read_b128 v[32:35], v2 offset:3904\n"
    "s_waitcnt lgkmcnt(2)\n"
    "v_mfma_f32_16x16x32_f16 v[48:51], v[88:91], v[24:27], 0\n"
    "ds_read2_b32 v[72:73], v3 offset0:48 offset1:49\n"
    "ds_read2_b32 v[74:75], v3 offset0:50 offset1:51\n"
    "ds_read2_b32 v[104:105], v1 offset0:48 offset1:49\n"
    "ds_read2_b32 v[106:107], v1 offset0:50 offset1:51\n"
    "ds_read_b128 v[36:39], v2 offset:3840\n"
    "v_mfma_f32_16x16x32_f16 v[52:55], v[120:123], v[24:27], 0\n"
    "s_waitcnt lgkmcnt(6)\n"
    "v_mfma_f32_16x16x32_f16 v[48:51], v[84:87], v[28:31], v[48:51]\n"
    "ds_read2_b32 v[68:69], v3 offset0:32 offset1:33\n"
    "ds_read2_b32 v[70:71], v3 offset0:34 offset1:35\n"
    "ds_read2_b32 v[100:101], v1 offset0:32 offset1:33\n"
    "ds_read2_b32 v[102:103], v1 offset0:34 offset1:35\n"
    "ds_read_b128 v[40:43], v2 offset:3776\n"
    "v_mfma_f32_16x16x32_f16 v[52:55], v[116:119], v[28:31], v[52:55]\n"
    "s_waitcnt lgkmcnt(10)\n"
    "v_mfma_f32_16x16x32_f16 v[48:51], v[80:83], v[32:35], v[48:51]\n"
    "ds_read2_b32 v[64:65], v3 offset0:16 offset1:17\n"
    "ds_read2_b32 v[66:67], v3 offset0:18 offset1:19\n"
    "ds_read2_b32 v[96:97], v1 offset0:16 offset1:17\n"
    "ds_read2_b32 v[98:99], v1 offset0:18 offset1:19\n"
    "ds_read_b128 v[44:47], v2 offset:3712\n"
    "v_mfma_f32_16x16x32_f16 v[52:55], v[112:115], v[32:35], v[52:55]\n"
    "s_waitcnt lgkmcnt(10)\n"
    "v_mfma_f32_16x16x32_f16 v[48:51], v[76:79], v[36:39], v[48:51]\n"
    "ds_read2_b32 v[60:61], v3 offset0:0 offset1:1\n"
    "ds_read2_b32 v[62:63], v3 offset0:2 offset1:3\n"
    "ds_read2_b32 v[92:93], v1 offset0:0 offset1:1\n"
    "ds_read2_b32 v[94:95], v1 offset0:2 offset1:3\n"
    "ds_read2_b32 v[124:125], v1 offset0:128 offset1:129\n"
    "ds_read2_b32 v[126:127], v1 offset0:130 offset1:131\n"
    "ds_read_b128 v[24:27], v2 offset:3648\n"
    "v_mfma_f32_16x16x32_f16 v[52:55], v[108:111], v[36:39], v[52:55]\n"
    "s_waitcnt lgkmcnt(12)\n"
    "v_mfma_f32_16x16x32_f16 v[48:51], v[72:75], v[40:43], v[48:51]\n"
    "ds_read_b128 v[28:31], v2 offset:3584\n"
    "ds_read_b128 v[32:35], v2 offset:3520\n"
    "v_mfma_f32_16x16x32_f16 v[52:55], v[104:107], v[40:43], v[52:55]\n"
    "s_waitcnt lgkmcnt(9)\n"
    "v_mfma_f32_16x16x32_f16 v[48:51], v[68:71], v[44:47], v[48:51]\n"
    "ds_read_b128 v[36:39], v2 offset:3456\n"
    "ds_read_b128 v[40:43], v2 offset:3392\n"
    "v_mfma_f32_16x16x32_f16 v[52:55], v[100:103], v[44:47], v[52:55]\n"
    "s_waitcnt lgkmcnt(4)\n"
    "v_mfma_f32_16x16x32_f16 v[48:51], v[64:67], v[24:27], v[48:51]\n"
    "ds_read_b128 v[44:47], v2 offset:3328\n"
    "v_mfma_f32_16x16x32_f16 v[52:55], v[96:99], v[24:27], v[52:55]\n"
    "s_waitcnt lgkmcnt(4)\n"
    "v_mfma_f32_16x16x32_f16 v[48:51], v[60:63], v[28:31], v[48:51]\n"
    "ds_read_b128 v[24:27], v2 offset:3264\n"
    "v_mfma_f32_16x16x32_f16 v[52:55], v[92:95], v[28:31], v[52:55]\n"
    "v_mfma_f32_16x16x32_f16 v[56:59], v[124:127], v[28:31], 0\n"
    "s_waitcnt lgkmcnt(4)\n"
    "v_mfma_f32_16x16x32_f16 v[52:55], v[88:91], v[32:35], v[52:55]\n"
    "ds_read_b128 v[28:31], v2 offset:3200\n"
    "v_mfma_f32_16x16x32_f16 v[56:59], v[120:123], v[32:35], v[56:59]\n"
    "s_waitcnt lgkmcnt(4)\n"
    "v_mfma_f32_16x16x32_f16 v[52:55], v[84:87], v[36:39], v[52:55]\n"
    "ds_read_b128 v[32:35], v2 offset:3136\n"
    "v_mfma_f32_16x16x32_f16 v[56:59], v[116:119], v[36:39], v[56:59]\n"
    "ds_write_b64 v4, v[48:49] offset:0\n"
    "s_waitcnt lgkmcnt(5)\n"
    "v_mfma_f32_16x16x32_f16 v[52:55], v[80:83], v[40:43], v[52:55]\n"
    "ds_read_b128 v[36:39], v2 offset:3072\n"
    "v_mfma_f32_16x16x32_f16 v[56:59], v[112:115], v[40:43], v[56:59]\n"
    "ds_write_b64 v4, v[50:51] offset:640\n"
    "s_waitcnt lgkmcnt(6)\n"
    "v_mfma_f32_16x16x32_f16 v[52:55], v[76:79], v[44:47], v[52:55]\n"
    "ds_read_b128 v[40:43], v2 offset:3008\n"
    "v_mfma_f32_16x16x32_f16 v[56:59], v[108:111], v[44:47], v[56:59]\n"
    "ds_write_b8 v5, v8 offset:64\n"
    "s_waitcnt lgkmcnt(7)\n"
    "v_mfma_f32_16x16x32_f16 v[52:55], v[72:75], v[24:27], v[52:55]\n"
    "ds_read_b128 v[44:47], v2 offset:2944\n"
    "v_mfma_f32_16x16x32_f16 v[56:59], v[104:107], v[24:27], v[56:59]\n"
    "s_waitcnt lgkmcnt(7)\n"
    "v_mfma_f32_16x16x32_f16 v[52:55], v[68:71], v[28:31], v[52:55]\n"
    "ds_read_b128 v[24:27], v2 offset:2880\n"
    "v_mfma_f32_16x16x32_f16 v[56:59], v[100:103], v[28:31], v[56:59]\n"
    "s_waitcnt lgkmcnt(7)\n"
    "v_mfma_f32_16x16x32_f16 v[52:55], v[64:67], v[32:35], v[52:55]\n"
    "ds_read_b128 v[28:31], v2 offset:2816\n"
    "v_mfma_f32_16x16x32_f16 v[56:59], v[96:99], v[32:35], v[56:59]\n"
    "s_waitcnt lgkmcnt(6)\n"
    "v_mfma_f32_16x16x32_f16 v[52:55], v[60:63], v[36:39], v[52:55]\n"
    "ds_read_b128 v[32:35], v2 offset:2752\n"
    "v_mfma_f32_16x16x32_f16 v[56:59], v[92:95], v[36:39], v[56:59]\n"
    "v_mfma_f32_16x16x32_f16 v[48:51], v[124:127], v[36:39], 0\n"
    "s_waitcnt lgkmcnt(5)\n"
    "v_mfma_f32_16x16x32_f16 v[56:59], v[88:91], v[40:43], v[56:59]\n"
    "ds_read_b128 v[36:39], v2 offset:2688\n"
    "v_mfma_f32_16x16x32_f16 v[48:51], v[120:123], v[40:43], v[48:51]\n"
    "ds_read_b32 v11, v10 offset:44\n"
    "s_waitcnt lgkmcnt(5)\n"
    "v_mfma_f32_16x16x32_f16 v[56:59], v[84:87], v[44:47], v[56:59]\n"
    "ds_read_b128 v[40:43], v2 offset:2624\n"
    "v_mfma_f32_16x16x32_f16 v[48:51], v[116:119], v[44:47], v[48:51]\n"
    "ds_write_b64 v4, v[52:53] offset:1280\n"
    "s_waitcnt lgkmcnt(6)\n"
    "v_mfma_f32_16x16x32_f16 v[56:59], v[80:83], v[24:27], v[56:59]\n"
    "ds_read_b128 v[44:47], v2 offset:2560\n"
    "v_mfma_f32_16x16x32_f16 v[48:51], v[112:115], v[24:27], v[48:51]\n"
    "ds_write_b64 v4, v[54:55] offset:1920\n"
    "s_waitcnt lgkmcnt(7)\n"
    "v_mfma_f32_16x16x32_f16 v[56:59], v[76:79], v[28:31], v[56:59]\n"
    "ds_read_b128 v[24:27], v2 offset:2496\n"
    "v_mfma_f32_16x16x32_f16 v[48:51], v[108:111], v[28:31], v[48:51]\n"
    "ds_write_b8 v5, v8 offset:72\n"
    "s_waitcnt lgkmcnt(6)\n"
    "v_cmp_ne_u32 vcc, 1, v11\n"
    "v_mfma_f32_16x16x32_f16 v[56:59], v[72:75], v[32:35], v[56:59]\n"
    "ds_read_b128 v[28:31], v2 offset:2432\n"
    "v_mfma_f32_16x16x32_f16 v[48:51], v[104:107], v[32:35], v[48:51]\n"
    "s_cbranch_vccnz NEARW\n"
    "NEARC:\n"
    "ds_read_b64 v[52:53], v4 offset:5120\n"
    "ds_read_b64 v[54:55], v4 offset:5760\n"
    "v_mfma_f32_16x16x32_f16 v[56:59], v[68:71], v[36:39], v[56:59]\n"
    "ds_read_b128 v[32:35], v2 offset:2368\n"
    "v_mfma_f32_16x16x32_f16 v[48:51], v[100:103], v[36:39], v[48:51]\n"
    "s_waitcnt lgkmcnt(9)\n"
    "v_mfma_f32_16x16x32_f16 v[56:59], v[64:67], v[40:43], v[56:59]\n"
    "ds_read_b128 v[36:39], v2 offset:2304\n"
    "v_mfma_f32_16x16x32_f16 v[48:51], v[96:99], v[40:43], v[48:51]\n"
    "s_waitcnt lgkmcnt(8)\n"
    "v_mfma_f32_16x16x32_f16 v[56:59], v[60:63], v[44:47], v[56:59]\n"
    "ds_read_b128 v[40:43], v2 offset:2240\n"
    "v_mfma_f32_16x16x32_f16 v[48:51], v[92:95], v[44:47], v[48:51]\n"
    "s_waitcnt lgkmcnt(3)\n"
    "v_mfma_f32_16x16x32_f16 v[52:55], v[124:127], v[44:47], v[52:55]\n"
    "v_mfma_f32_16x16x32_f16 v[48:51], v[88:91], v[24:27], v[48:51]\n"
    "ds_read_b128 v[44:47], v2 offset:2176\n"
    "v_mfma_f32_16x16x32_f16 v[52:55], v[120:123], v[24:27], v[52:55]\n"
    "v_mfma_f32_16x16x32_f16 v[48:51], v[84:87], v[28:31], v[48:51]\n"
    "ds_read_b128 v[24:27], v2 offset:2112\n"
    "v_mfma_f32_16x16x32_f16 v[52:55], v[116:119], v[28:31], v[52:55]\n"
    "ds_write_b64 v4, v[56:57] offset:2560\n"
    "s_waitcnt lgkmcnt(5)\n"
    "v_mfma_f32_16x16x32_f16 v[48:51], v[80:83], v[32:35], v[48:51]\n"
    "ds_read_b128 v[28:31], v2 offset:2048\n"
    "v_mfma_f32_16x16x32_f16 v[52:55], v[112:115], v[32:35], v[52:55]\n"
    "ds_write_b64 v4, v[58:59] offset:3200\n"
    "s_waitcnt lgkmcnt(6)\n"
    "v_mfma_f32_16x16x32_f16 v[48:51], v[76:79], v[36:39], v[48:51]\n"
    "ds_read_b128 v[32:35], v2 offset:1984\n"
    "v_mfma_f32_16x16x32_f16 v[52:55], v[108:111], v[36:39], v[52:55]\n"
    "ds_write_b8 v5, v8 offset:80\n"
    "s_waitcnt lgkmcnt(7)\n"
    "v_mfma_f32_16x16x32_f16 v[48:51], v[72:75], v[40:43], v[48:51]\n"
    "ds_read_b128 v[36:39], v2 offset:1920\n"
    "v_mfma_f32_16x16x32_f16 v[52:55], v[104:107], v[40:43], v[52:55]\n"
    "ds_read_b64 v[56:57], v4 offset:6400\n"
    "ds_read_b64 v[58:59], v4 offset:7040\n"
    "s_waitcnt lgkmcnt(9)\n"
    "v_mfma_f32_16x16x32_f16 v[48:51], v[68:71], v[44:47], v[48:51]\n"
    "ds_read_b128 v[40:43], v2 offset:1856\n"
    "v_mfma_f32_16x16x32_f16 v[52:55], v[100:103], v[44:47], v[52:55]\n"
    "s_waitcnt lgkmcnt(9)\n"
    "v_mfma_f32_16x16x32_f16 v[48:51], v[64:67], v[24:27], v[48:51]\n"
    "ds_read_b128 v[44:47], v2 offset:1792\n"
    "v_mfma_f32_16x16x32_f16 v[52:55], v[96:99], v[24:27], v[52:55]\n"
    "s_waitcnt lgkmcnt(8)\n"
    "v_mfma_f32_16x16x32_f16 v[48:51], v[60:63], v[28:31], v[48:51]\n"
    "ds_read_b128 v[24:27], v2 offset:1728\n"
    "v_mfma_f32_16x16x32_f16 v[52:55], v[92:95], v[28:31], v[52:55]\n"
    "s_waitcnt lgkmcnt(3)\n"
    "v_mfma_f32_16x16x32_f16 v[56:59], v[124:127], v[28:31], v[56:59]\n"
    "v_mfma_f32_16x16x32_f16 v[52:55], v[88:91], v[32:35], v[52:55]\n"
    "ds_read_b128 v[28:31], v2 offset:1664\n"
    "v_mfma_f32_16x16x32_f16 v[56:59], v[120:123], v[32:35], v[56:59]\n"
    "v_mfma_f32_16x16x32_f16 v[52:55], v[84:87], v[36:39], v[52:55]\n"
    "ds_read_b128 v[32:35], v2 offset:1600\n"
    "v_mfma_f32_16x16x32_f16 v[56:59], v[116:119], v[36:39], v[56:59]\n"
    "ds_write_b64 v4, v[48:49] offset:3840\n"
    "s_waitcnt lgkmcnt(5)\n"
    "v_mfma_f32_16x16x32_f16 v[52:55], v[80:83], v[40:43], v[52:55]\n"
    "ds_read_b128 v[36:39], v2 offset:1536\n"
    "v_mfma_f32_16x16x32_f16 v[56:59], v[112:115], v[40:43], v[56:59]\n"
    "ds_write_b64 v4, v[50:51] offset:4480\n"
    "s_waitcnt lgkmcnt(6)\n"
    "v_mfma_f32_16x16x32_f16 v[52:55], v[76:79], v[44:47], v[52:55]\n"
    "ds_read_b128 v[40:43], v2 offset:1472\n"
    "v_mfma_f32_16x16x32_f16 v[56:59], v[108:111], v[44:47], v[56:59]\n"
    "ds_write_b8 v5, v8 offset:88\n"
    "s_waitcnt lgkmcnt(7)\n"
    "v_mfma_f32_16x16x32_f16 v[52:55], v[72:75], v[24:27], v[52:55]\n"
    "ds_read_b128 v[44:47], v2 offset:1408\n"
    "v_mfma_f32_16x16x32_f16 v[56:59], v[104:107], v[24:27], v[56:59]\n"
    "ds_read_b64 v[48:49], v4 offset:7680\n"
    "ds_read_b64 v[50:51], v4 offset:8320\n"
    "s_waitcnt lgkmcnt(9)\n"
    "v_mfma_f32_16x16x32_f16 v[52:55], v[68:71], v[28:31], v[52:55]\n"
    "ds_read_b128 v[24:27], v2 offset:1344\n"
    "v_mfma_f32_16x16x32_f16 v[56:59], v[100:103], v[28:31], v[56:59]\n"
    "s_waitcnt lgkmcnt(9)\n"
    "v_mfma_f32_16x16x32_f16 v[52:55], v[64:67], v[32:35], v[52:55]\n"
    "ds_read_b128 v[28:31], v2 offset:1280\n"
    "v_mfma_f32_16x16x32_f16 v[56:59], v[96:99], v[32:35], v[56:59]\n"
    "s_waitcnt lgkmcnt(8)\n"
    "v_mfma_f32_16x16x32_f16 v[52:55], v[60:63], v[36:39], v[52:55]\n"
    "ds_read_b128 v[32:35], v2 offset:1216\n"
    "v_mfma_f32_16x16x32_f16 v[56:59], v[92:95], v[36:39], v[56:59]\n"
    "s_waitcnt lgkmcnt(3)\n"
    "v_mfma_f32_16x16x32_f16 v[48:51], v[124:127], v[36:39], v[48:51]\n"
    "v_mfma_f32_16x16x32_f16 v[56:59], v[88:91], v[40:43], v[56:59]\n"
    "ds_read_b128 v[36:39], v2 offset:1152\n"
    "v_mfma_f32_16x16x32_f16 v[48:51], v[120:123], v[40:43], v[48:51]\n"
    "v_mfma_f32_16x16x32_f16 v[56:59], v[84:87], v[44:47], v[56:59]\n"
    "ds_read_b128 v[40:43], v2 offset:1088\n"
    "v_mfma_f32_16x16x32_f16 v[48:51], v[116:119], v[44:47], v[48:51]\n"
    "ds_write_b64 v4, v[52:53] offset:5120\n"
    "s_waitcnt lgkmcnt(5)\n"
    "v_mfma_f32_16x16x32_f16 v[56:59], v[80:83], v[24:27], v[56:59]\n"
    "ds_read_b128 v[44:47], v2 offset:1024\n"
    "v_mfma_f32_16x16x32_f16 v[48:51], v[112:115], v[24:27], v[48:51]\n"
    "ds_write_b64 v4, v[54:55] offset:5760\n"
    "s_waitcnt lgkmcnt(6)\n"
    "v_mfma_f32_16x16x32_f16 v[56:59], v[76:79], v[28:31], v[56:59]\n"
    "ds_read_b128 v[24:27], v2 offset:960\n"
    "v_mfma_f32_16x16x32_f16 v[48:51], v[108:111], v[28:31], v[48:51]\n"
    "ds_write_b8 v5, v8 offset:96\n"
    "s_waitcnt lgkmcnt(7)\n"
    "v_mfma_f32_16x16x32_f16 v[56:59], v[72:75], v[32:35], v[56:59]\n"
    "ds_read_b128 v[28:31], v2 offset:896\n"
    "v_mfma_f32_16x16x32_f16 v[48:51], v[104:107], v[32:35], v[48:51]\n"
    "ds_read_b64 v[52:53], v4 offset:8960\n"
    "ds_read_b64 v[54:55], v4 offset:9600\n"
    "s_waitcnt lgkmcnt(9)\n"
    "v_mfma_f32_16x16x32_f16 v[56:59], v[68:71], v[36:39], v[56:59]\n"
    "ds_read_b128 v[32:35], v2 offset:832\n"
    "v_mfma_f32_16x16x32_f16 v[48:51], v[100:103], v[36:39], v[48:51]\n"
    "s_waitcnt lgkmcnt(9)\n"
    "v_mfma_f32_16x16x32_f16 v[56:59], v[64:67], v[40:43], v[56:59]\n"
    "ds_read_b128 v[36:39], v2 offset:768\n"
    "v_mfma_f32_16x16x32_f16 v[48:51], v[96:99], v[40:43], v[48:51]\n"
    "s_waitcnt lgkmcnt(8)\n"
    "v_mfma_f32_16x16x32_f16 v[56:59], v[60:63], v[44:47], v[56:59]\n"
    "ds_read_b128 v[40:43], v2 offset:704\n"
    "v_mfma_f32_16x16x32_f16 v[48:51], v[92:95], v[44:47], v[48:51]\n"
    "s_waitcnt lgkmcnt(3)\n"
    "v_mfma_f32_16x16x32_f16 v[52:55], v[124:127], v[44:47], v[52:55]\n"
    "v_mfma_f32_16x16x32_f16 v[48:51], v[88:91], v[24:27], v[48:51]\n"
    "ds_read_b128 v[44:47], v2 offset:640\n"
    "v_mfma_f32_16x16x32_f16 v[52:55], v[120:123], v[24:27], v[52:55]\n"
    "v_mfma_f32_16x16x32_f16 v[48:51], v[84:87], v[28:31], v[48:51]\n"
    "ds_read_b128 v[24:27], v2 offset:576\n"
    "v_mfma_f32_16x16x32_f16 v[52:55], v[116:119], v[28:31], v[52:55]\n"
    "ds_write_b64 v4, v[56:57] offset:6400\n"
    "s_waitcnt lgkmcnt(5)\n"
    "v_mfma_f32_16x16x32_f16 v[48:51], v[80:83], v[32:35], v[48:51]\n"
    "ds_read_b128 v[28:31], v2 offset:512\n"
    "v_mfma_f32_16x16x32_f16 v[52:55], v[112:115], v[32:35], v[52:55]\n"
    "ds_write_b64 v4, v[58:59] offset:7040\n"
    "ds_write_b8 v5, v8 offset:104\n"
    "s_waitcnt lgkmcnt(7)\n"
    "v_mfma_f32_16x16x32_f16 v[48:51], v[76:79], v[36:39], v[48:51]\n"
    "ds_read_b128 v[32:35], v2 offset:448\n"
    "v_mfma_f32_16x16x32_f16 v[52:55], v[108:111], v[36:39], v[52:55]\n"
    "s_waitcnt lgkmcnt(7)\n"
    "v_mfma_f32_16x16x32_f16 v[48:51], v[72:75], v[40:43], v[48:51]\n"
    "ds_read_b128 v[36:39], v2 offset:384\n"
    "v_mfma_f32_16x16x32_f16 v[52:55], v[104:107], v[40:43], v[52:55]\n"
    "s_waitcnt lgkmcnt(7)\n"
    "v_mfma_f32_16x16x32_f16 v[48:51], v[68:71], v[44:47], v[48:51]\n"
    "ds_read_b128 v[40:43], v2 offset:320\n"
    "v_mfma_f32_16x16x32_f16 v[52:55], v[100:103], v[44:47], v[52:55]\n"
    "s_waitcnt lgkmcnt(7)\n"
    "v_mfma_f32_16x16x32_f16 v[48:51], v[64:67], v[24:27], v[48:51]\n"
    "ds_read_b128 v[44:47], v2 offset:256\n"
    "v_mfma_f32_16x16x32_f16 v[52:55], v[96:99], v[24:27], v[52:55]\n"
    "s_waitcnt lgkmcnt(6)\n"
    "v_mfma_f32_16x16x32_f16 v[48:51], v[60:63], v[28:31], v[48:51]\n"
    "ds_read_b128 v[24:27], v2 offset:192\n"
    "v_mfma_f32_16x16x32_f16 v[52:55], v[92:95], v[28:31], v[52:55]\n"
    "s_waitcnt lgkmcnt(4)\n"
    "v_mfma_f32_16x16x32_f16 v[52:55], v[88:91], v[32:35], v[52:55]\n"
    "ds_read_b128 v[28:31], v2 offset:128\n"
    "s_waitcnt lgkmcnt(4)\n"
    "v_mfma_f32_16x16x32_f16 v[52:55], v[84:87], v[36:39], v[52:55]\n"
    "ds_read_b128 v[32:35], v2 offset:64\n"
    "ds_write_b64 v4, v[48:49] offset:7680\n"
    "s_waitcnt lgkmcnt(5)\n"
    "v_mfma_f32_16x16x32_f16 v[52:55], v[80:83], v[40:43], v[52:55]\n"
    "ds_read_b128 v[36:39], v2 offset:0\n"
    "ds_write_b64 v4, v[50:51] offset:8320\n"
    "ds_write_b8 v5, v8 offset:112\n"
    "s_waitcnt lgkmcnt(7)\n"
    "v_mfma_f32_16x16x32_f16 v[52:55], v[76:79], v[44:47], v[52:55]\n"
    "s_waitcnt lgkmcnt(6)\n"
    "v_mfma_f32_16x16x32_f16 v[52:55], v[72:75], v[24:27], v[52:55]\n"
    "s_waitcnt lgkmcnt(5)\n"
    "v_mfma_f32_16x16x32_f16 v[52:55], v[68:71], v[28:31], v[52:55]\n"
    "s_waitcnt lgkmcnt(4)\n"
    "v_mfma_f32_16x16x32_f16 v[52:55], v[64:67], v[32:35], v[52:55]\n"
    "s_waitcnt lgkmcnt(2)\n"
    "v_mfma_f32_16x16x32_f16 v[52:55], v[60:63], v[36:39], v[52:55]\n"
    "s_nop 7\n"
    "ds_write_b64 v4, v[52:53] offset:8960\n"
    "ds_write_b64 v4, v[54:55] offset:9600\n"
    "ds_write_b8 v5, v8 offset:120\n"
    "s_setprio 0\n"
    "s_branch LN2\n"
    "NEARW:\n"
    "s_branch E_24\n"
    "R_23:\n"
    "s_sleep 1\n"
    "s_sub_u32 s39, s39, 1\n"
    "s_cbranch_scc1 ABORT\n"
    "E_24:\n"
    "ds_read_b32 v11, v10 offset:44\n"
    "s_waitcnt lgkmcnt(0)\n"
    "v_cmp_ne_u32 vcc, 1, v11\n"
    "s_cbranch_vccnz R_23\n"
    "s_branch NEARC\n"
    "EPS:\n"
    "v_mov_b32 v14, 147968\n"
    "v_mov_b32 v15, 1\n"
    "v_bfe_u32 v2, v0, 3, 3\n"
    "v_bfe_u32 v3, v0, 1, 2\n"
    "v_mul_u32_u24 v3, 160, v3\n"
    "v_lshl_add_u32 v2, v2, 3, v3\n"
    "v_and_b32 v3, 1, v0\n"
    "s_movk_i32 s43, 640\n"
    "v_mad_u32_u24 v2, v3, s43, v2\n"
    "s_mul_i32 s43, s38, 16512\n"
    "s_add_u32 s43, s43, 131072\n"
    "v_and_b32 v3, 63, v0\n"
    "v_lshl_add_u32 v1, v3, 4, s43\n"
    "v_mov_b32 v43, s36\n"
    "s_sub_u32 s57, 15, s37\n"
    "v_add_u32 v36, 1792, v86\n"
    "s_lshl_b32 s49, s57, 10\n"
    "s_lshr_b32 s46, s57, 1\n"
    "s_mul_i32 s43, s46, 1280\n"
    "s_and_b32 s47, s57, 1\n"
    "s_lshl_b32 s47, s47, 6\n"
    "s_add_u32 s43, s43, s47\n"
    "v_add_u32 v30, s43, v2\n"
    "s_lshl_b32 s46, s46, 3\n"
    "v_add_u32 v33, s46, v14\n"
    "s_lshl_b32 s50, s54, 10\n"
    "s_lshr_b32 s46, s54, 1\n"
    "s_mul_i32 s43, s46, 1280\n"
    "s_and_b32 s47, s54, 1\n"
    "s_lshl_b32 s47, s47, 6\n"
    "s_add_u32 s43, s43, s47\n"
    "v_add_u32 v31, s43, v2\n"
    "s_lshl_b32 s46, s46, 3\n"
    "v_add_u32 v34, s46, v14\n"
    "s_lshl_b32 s51, s55, 10\n"
    "s_lshr_b32 s46, s55, 1\n"
    "s_mul_i32 s43, s46, 1280\n"
    "s_and_b32 s47, s55, 1\n"
    "s_lshl_b32 s47, s47, 6\n"
    "s_add_u32 s43, s43, s47\n"
    "v_add_u32 v32, s43, v2\n"
    "s_lshl_b32 s46, s46, 3\n"
    "v_add_u32 v35, s46, v14\n"
    "s_mov_b32 s46, 0x01010101\n"
    "s_movk_i32 s47, 0x0101\n"
    "s_and_b32 s42, s38, 1\n"
    "s_lshl_b32 s42, s42, 4\n"
    "s_waitcnt lgkmcnt(0)\n"
    "s_lshr_b64 s[52:53], s[52:53], s42\n"
    "s_lshl_b32 s44, s52, 16\n"
    "s_and_b32 s45, s52, 0xffff0000\n"
    "s_lshl_b32 s48, s53, 16\n"
    "s_branch STG0\n"
    "EPI:\n"
    "s_setprio 2\n"
    "s_waitcnt lgkmcnt(0)\n"
    "v_mul_f32 v120, v121, v114\n"
    "v_mul_f32 v114, s44, v114\n"
    "v_mul_f32 v122, v123, v116\n"
    "v_mul_f32 v116, s45, v116\n"
    "v_mul_f32 v124, v125, v118\n"
    "v_mul_f32 v118, s48, v118\n"
    "v_fma_mix_f32 v70, v94, v114, 0 op_sel:[1,0,0] op_sel_hi:[1,0,0]\n"
    "v_fma_mix_f32 v71, v94, v114, 0 op_sel_hi:[1,0,0]\n"
    "v_fma_mix_f32 v72, v95, v116, 0 op_sel:[1,0,0] op_sel_hi:[1,0,0]\n"
    "v_fma_mix_f32 v73, v95, v116, 0 op_sel_hi:[1,0,0]\n"
    "v_fma_mix_f32 v74, v96, v118, 0 op_sel:[1,0,0] op_sel_hi:[1,0,0]\n"
    "v_fma_mix_f32 v75, v96, v118, 0 op_sel_hi:[1,0,0]\n"
    "v_cvt_pk_bf16_f32 v70, 0, v70\n"
    "v_cvt_pk_bf16_f32 v71, 0, v71\n"
    "v_cvt_pk_bf16_f32 v72, 0, v72\n"
    "v_cvt_pk_bf16_f32 v73, 0, v73\n"
    "v_cvt_pk_bf16_f32 v74, 0, v74\n"
    "v_cvt_pk_bf16_f32 v75, 0, v75\n"
    "v_fma_mix_f32 v76, v97, v114, 0 op_sel:[1,0,0] op_sel_hi:[1,0,0]\n"
    "v_fma_mix_f32 v77, v97, v114, 0 op_sel_hi:[1,0,0]\n"
    "v_fma_mix_f32 v78, v98, v116, 0 op_sel:[1,0,0] op_sel_hi:[1,0,0]\n"
    "v_fma_mix_f32 v79, v98, v116, 0 op_sel_hi:[1,0,0]\n"
    "v_fma_mix_f32 v80, v99, v118, 0 op_sel:[1,0,0] op_sel_hi:[1,0,0]\n"
    "v_fma_mix_f32 v81, v99, v118, 0 op_sel_hi:[1,0,0]\n"
    "v_cvt_pk_bf16_f32 v76, 0, v76\n"
    "v_cvt_pk_bf16_f32 v77, 0, v77\n"
    "v_cvt_pk_bf16_f32 v78, 0, v78\n"
    "v_cvt_pk_bf16_f32 v79, 0, v79\n"
    "v_cvt_pk_bf16_f32 v80, 0, v80\n"
    "v_cvt_pk_bf16_f32 v81, 0, v81\n"
    "s_cmpk_lt_u32 s37, 14\n"
    "s_cbranch_scc1 EP1\n"
    "ds_read_b32 v24, v36 offset:0\n"
    "ds_read_b32 v25, v36 offset:4576\n"
    "ds_read_b32 v26, v36 offset:9152\n"
    "s_waitcnt lgkmcnt(0)\n"
    "v_fma_mix_f32 v64, v24, v114, 0 op_sel:[1,0,0] op_sel_hi:[1,0,0]\n"
    "v_fma_mix_f32 v65, v24, v114, 0 op_sel_hi:[1,0,0]\n"
    "v_fma_mix_f32 v66, v25, v116, 0 op_sel:[1,0,0] op_sel_hi:[1,0,0]\n"
    "v_fma_mix_f32 v67, v25, v116, 0 op_sel_hi:[1,0,0]\n"
    "v_fma_mix_f32 v68, v26, v118, 0 op_sel:[1,0,0] op_sel_hi:[1,0,0]\n"
    "v_fma_mix_f32 v69, v26, v118, 0 op_sel_hi:[1,0,0]\n"
    "v_cvt_pk_bf16_f32 v64, 0, v64\n"
    "v_cvt_pk_bf16_f32 v65, 0, v65\n"
    "v_cvt_pk_bf16_f32 v66, 0, v66\n"
    "v_cvt_pk_bf16_f32 v67, 0, v67\n"
    "v_cvt_pk_bf16_f32 v68, 0, v68\n"
    "v_cvt_pk_bf16_f32 v69, 0, v69\n"
    "s_branch E_26\n"
    "R_25:\n"
    "s_sleep 1\n"
    "s_sub_u32 s39, s39, 1\n"
    "s_cbranch_scc1 ABORT\n"
    "E_26:\n"
    "ds_read_b32 v22, v33 offset:64\n"
    "s_waitcnt lgkmcnt(0)\n"
    "v_cmp_ne_u32 vcc, 0x010101, v22\n"
    "s_cbranch_vccnz R_25\n"
    "ds_read_b64 v[48:49], v30 offset:0\n"
    "ds_read_b64 v[50:51], v30 offset:10240\n"
    "ds_read_b64 v[52:53], v30 offset:20480\n"
    "s_waitcnt lgkmcnt(0)\n"
    "v_cvt_pk_bf16_f32 v48, 0, v48\n"
    "v_cvt_pk_bf16_f32 v49, 0, v49\n"
    "v_cvt_pk_bf16_f32 v50, 0, v50\n"
    "v_cvt_pk_bf16_f32 v51, 0, v51\n"
    "v_cvt_pk_bf16_f32 v52, 0, v52\n"
    "v_cvt_pk_bf16_f32 v53, 0, v53\n"
    "v_fma_f32 v48, v48, v120, v64\n"
    "v_fma_f32 v49, v49, v120, v65\n"
    "v_fma_f32 v50, v50, v122, v66\n"
    "v_fma_f32 v51, v51, v122, v67\n"
    "v_fma_f32 v52, v52, v124, v68\n"
    "v_fma_f32 v53, v53, v124, v69\n"
    "v_cvt_pk_bf16_f32 v48, 0, v48\n"
    "v_cvt_pk_bf16_f32 v49, 0, v49\n"
    "v_cvt_pk_bf16_f32 v50, 0, v50\n"
    "v_cvt_pk_bf16_f32 v51, 0, v51\n"
    "v_cvt_pk_bf16_f32 v52, 0, v52\n"
    "v_cvt_pk_bf16_f32 v53, 0, v53\n"
    "v_mul_f32 v48, v48, v115\n"
    "v_mul_f32 v49, v49, v115\n"
    "v_mul_f32 v50, v50, v117\n"
    "v_mul_f32 v51, v51, v117\n"
    "v_mul_f32 v52, v52, v119\n"
    "v_mul_f32 v53, v53, v119\n"
    "v_cvt_pk_bf16_f32 v40, v48, v49\n"
    "v_cvt_pk_bf16_f32 v41, v50, v51\n"
    "v_cvt_pk_bf16_f32 v42, v52, v53\n"
    "buffer_store_dwordx4 v[40:43], v1, s[20:23], s49 offen sc1\n"
    "s_mov_b64 exec, 1\n"
    "ds_add_u32 v14, v15 offset:60\n"
    "s_mov_b64 exec, -1\n"
    "EP1:\n"
    "s_cmpk_ge_u32 s37, 2\n"
    "s_cbranch_scc1 EP1F\n"
    "s_branch E_28\n"
    "R_27:\n"
    "s_sleep 1\n"
    "s_sub_u32 s39, s39, 1\n"
    "s_cbranch_scc1 ABORT\n"
    "E_28:\n"
    "ds_read_b32 v22, v34 offset:64\n"
    "s_waitcnt lgkmcnt(0)\n"
    "v_cmp_ne_u32 vcc, 0x010101, v22\n"
    "s_cbranch_vccnz R_27\n"
    "ds_read_b64 v[48:49], v31 offset:0\n"
    "ds_read_b64 v[50:51], v31 offset:10240\n"
    "ds_read_b64 v[52:53], v31 offset:20480\n"
    "s_waitcnt lgkmcnt(0)\n"
    "s_branch EP1M\n"
    "EP1F:\n"
    "s_branch E_30\n"
    "R_29:\n"
    "s_sleep 1\n"
    "s_sub_u32 s39, s39, 1\n"
    "s_cbranch_scc1 ABORT\n"
    "E_30:\n"
    "ds_read_b64 v[22:23], v34 offset:64\n"
    "s_waitcnt lgkmcnt(0)\n"
    "v_cmp_ne_u64 vcc, s[46:47], v[22:23]\n"
    "s_cbranch_vccnz R_29\n"
    "ds_read_b64 v[48:49], v31 offset:0\n"
    "ds_read_b64 v[54:55], v31 offset:28160\n"
    "ds_read_b64 v[50:51], v31 offset:10240\n"
    "ds_read_b64 v[56:57], v31 offset:35840\n"
    "ds_read_b64 v[52:53], v31 offset:20480\n"
    "ds_read_b64 v[58:59], v31 offset:43520\n"
    "s_waitcnt lgkmcnt(0)\n"
    "v_add_f32 v48, v48, v54\n"
    "v_add_f32 v49, v49, v55\n"
    "v_add_f32 v50, v50, v56\n"
    "v_add_f32 v51, v51, v57\n"
    "v_add_f32 v52, v52, v58\n"
    "v_add_f32 v53, v53, v59\n"
    "EP1M:\n"
    "v_cvt_pk_bf16_f32 v48, 0, v48\n"
    "v_cvt_pk_bf16_f32 v49, 0, v49\n"
    "v_cvt_pk_bf16_f32 v50, 0, v50\n"
    "v_cvt_pk_bf16_f32 v51, 0, v51\n"
    "v_cvt_pk_bf16_f32 v52, 0, v52\n"
    "v_cvt_pk_bf16_f32 v53, 0, v53\n"
    "v_fma_f32 v48, v48, v120, v70\n"
    "v_fma_f32 v49, v49, v120, v71\n"
    "v_fma_f32 v50, v50, v122, v72\n"
    "v_fma_f32 v51, v51, v122, v73\n"
    "v_fma_f32 v52, v52, v124, v74\n"
    "v_fma_f32 v53, v53, v124, v75\n"
    "v_cvt_pk_bf16_f32 v48, 0, v48\n"
    "v_cvt_pk_bf16_f32 v49, 0, v49\n"
    "v_cvt_pk_bf16_f32 v50, 0, v50\n"
    "v_cvt_pk_bf16_f32 v51, 0, v51\n"
    "v_cvt_pk_bf16_f32 v52, 0, v52\n"
    "v_cvt_pk_bf16_f32 v53, 0, v53\n"
    "v_mul_f32 v48, v48, v115\n"
    "v_mul_f32 v49, v49, v115\n"
    "v_mul_f32 v50, v50, v117\n"
    "v_mul_f32 v51, v51, v117\n"
    "v_mul_f32 v52, v52, v119\n"
    "v_mul_f32 v53, v53, v119\n"
    "v_cvt_pk_bf16_f32 v40, v48, v49\n"
    "v_cvt_pk_bf16_f32 v41, v50, v51\n"
    "v_cvt_pk_bf16_f32 v42, v52, v53\n"
    "buffer_store_dwordx4 v[40:43], v1, s[20:23], s50 offen sc1\n"
    "s_mov_b64 exec, 1\n"
    "ds_add_u32 v14, v15 offset:60\n"
    "s_mov_b64 exec, -1\n"
    "s_cmpk_ge_u32 s37, 12\n"
    "s_cbranch_scc1 EP2\n"
    "s_waitcnt vmcnt(1)\n"
    "s_mov_b64 exec, 1\n"
    "ds_add_u32 v14, v15 offset:56\n"
    "s_mov_b64 exec, -1\n"
    "EP2:\n"
    "s_cmpk_lg_u32 s37, 13\n"
    "s_cbranch_scc1 EP3\n"
    "s_memrealtime s[98:99]\n"
    "s_lshl_b32 s42, s2, 7\n"
    "s_add_u32 s42, s42, 94208\n"
    "v_mov_b32 v45, v43\n"
    "s_waitcnt lgkmcnt(0)\n"
    "v_mov_b32 v44, s98\n"
    "s_mov_b64 exec, 1\n"
    "buffer_store_dwordx2 v[44:45], off, s[20:23], s42 sc1\n"
    "s_mov_b64 exec, -1\n"
    "EP3:\n"
    "s_branch E_32\n"
    "R_31:\n"
    "s_sleep 1\n"
    "s_sub_u32 s39, s39, 1\n"
    "s_cbranch_scc1 ABORT\n"
    "E_32:\n"
    "ds_read_b64 v[22:23], v35 offset:56\n"
    "s_waitcnt lgkmcnt(0)\n"
    "v_cmp_ne_u64 vcc, s[46:47], v[22:23]\n"
    "s_cbranch_vccnz R_31\n"
    "s_branch E_34\n"
    "R_33:\n"
    "s_sub_u32 s39, s39, 1\n"
    "s_cbranch_scc1 ABORT\n"
    "E_34:\n"
    "ds_read_b64 v[22:23], v35 offset:64\n"
    "ds_read_b64 v[48:49], v32 offset:0\n"
    "ds_read_b64 v[54:55], v32 offset:28160\n"
    "ds_read_b64 v[50:51], v32 offset:10240\n"
    "ds_read_b64 v[56:57], v32 offset:35840\n"
    "ds_read_b64 v[52:53], v32 offset:20480\n"
    "ds_read_b64 v[58:59], v32 offset:43520\n"
    "s_waitcnt lgkmcnt(0)\n"
    "v_cmp_ne_u64 vcc, s[46:47], v[22:23]\n"
    "s_cbranch_vccnz R_33\n"
    "s_cmpk_ge_u32 s37, 14\n"
    "s_cbranch_scc1 EPZ\n"
    "v_add_f32 v48, v48, v54\n"
    "v_add_f32 v49, v49, v55\n"
    "v_add_f32 v50, v50, v56\n"
    "v_add_f32 v51, v51, v57\n"
    "v_add_f32 v52, v52, v58\n"
    "v_add_f32 v53, v53, v59\n"
    "v_cvt_pk_bf16_f32 v48, 0, v48\n"
    "v_cvt_pk_bf16_f32 v49, 0, v49\n"
    "v_cvt_pk_bf16_f32 v50, 0, v50\n"
    "v_cvt_pk_bf16_f32 v51, 0, v51\n"
    "v_cvt_pk_bf16_f32 v52, 0, v52\n"
    "v_cvt_pk_bf16_f32 v53, 0, v53\n"
    "v_fma_f32 v48, v48, v120, v76\n"
    "v_fma_f32 v49, v49, v120, v77\n"
    "v_fma_f32 v50, v50, v122, v78\n"
    "v_fma_f32 v51, v51, v122, v79\n"
    "v_fma_f32 v52, v52, v124, v80\n"
    "v_fma_f32 v53, v53, v124, v81\n"
    "v_cvt_pk_bf16_f32 v48, 0, v48\n"
    "v_cvt_pk_bf16_f32 v49, 0, v49\n"
    "v_cvt_pk_bf16_f32 v50, 0, v50\n"
    "v_cvt_pk_bf16_f32 v51, 0, v51\n"
    "v_cvt_pk_bf16_f32 v52, 0, v52\n"
    "v_cvt_pk_bf16_f32 v53, 0, v53\n"
    "v_mul_f32 v48, v48, v115\n"
    "v_mul_f32 v49, v49, v115\n"
    "v_mul_f32 v50, v50, v117\n"
    "v_mul_f32 v51, v51, v117\n"
    "v_mul_f32 v52, v52, v119\n"
    "v_mul_f32 v53, v53, v119\n"
    "v_cvt_pk_bf16_f32 v40, v48, v49\n"
    "v_cvt_pk_bf16_f32 v41, v50, v51\n"
    "v_cvt_pk_bf16_f32 v42, v52, v53\n"
    "buffer_store_dwordx4 v[40:43], v1, s[20:23], s51 offen sc1\n"
    "s_mov_b64 exec, 1\n"
    "ds_add_u32 v14, v15 offset:60\n"
    "s_mov_b64 exec, -1\n"
    "s_cmpk_ge_u32 s37, 12\n"
    "s_cbranch_scc1 LN2K\n"
    "s_setprio 0\n"
    "s_branch LN2\n"
    "EPZ:\n"
    "s_setprio 3\n"
    "v_pk_add_f32 v[48:49], v[48:49], v[54:55]\n"
    "v_pk_add_f32 v[50:51], v[50:51], v[56:57]\n"
    "v_pk_add_f32 v[52:53], v[52:53], v[58:59]\n"
    "v_cvt_pk_bf16_f32 v48, 0, v48\n"
    "v_cvt_pk_bf16_f32 v49, 0, v49\n"
    "v_cvt_pk_bf16_f32 v50, 0, v50\n"
    "v_cvt_pk_bf16_f32 v51, 0, v51\n"
    "v_cvt_pk_bf16_f32 v52, 0, v52\n"
    "v_cvt_pk_bf16_f32 v53, 0, v53\n"
    "v_pk_fma_f32 v[48:49], v[48:49], v[120:121], v[76:77] op_sel_hi:[1,0,1]\n"
    "v_pk_fma_f32 v[50:51], v[50:51], v[122:123], v[78:79] op_sel_hi:[1,0,1]\n"
    "v_pk_fma_f32 v[52:53], v[52:53], v[124:125], v[80:81] op_sel_hi:[1,0,1]\n"
    "v_cvt_pk_bf16_f32 v48, 0, v48\n"
    "v_cvt_pk_bf16_f32 v49, 0, v49\n"
    "v_cvt_pk_bf16_f32 v50, 0, v50\n"
    "v_cvt_pk_bf16_f32 v51, 0, v51\n"
    "v_cvt_pk_bf16_f32 v52, 0, v52\n"
    "v_cvt_pk_bf16_f32 v53, 0, v53\n"
    "v_pk_mul_f32 v[48:49], v[114:115], v[48:49] op_sel:[1,0] op_sel_hi:[1,1]\n"
    "v_pk_mul_f32 v[50:51], v[116:117], v[50:51] op_sel:[1,0] op_sel_hi:[1,1]\n"
    "v_pk_mul_f32 v[52:53], v[118:119], v[52:53] op_sel:[1,0] op_sel_hi:[1,1]\n"
    "v_cvt_pk_bf16_f32 v40, v48, v49\n"
    "v_cvt_pk_bf16_f32 v41, v50, v51\n"
    "v_cvt_pk_bf16_f32 v42, v52, v53\n"
    "buffer_store_dwordx4 v[40:43], v1, s[20:23], s51 offen sc1\n"
    "s_mov_b64 exec, 1\n"
    "ds_add_u32 v14, v15 offset:60\n"
    "s_mov_b64 exec, -1\n"
    "s_branch LN2K\n"
    "MISC:\n"
    "s_cmpk_ge_u32 s2, 128\n"
    "s_cbranch_scc1 CDONE\n"
    "v_and_b32 v109, 255, v0\n"
    "v_add_u32 v104, 192, v109\n"
    "v_min_u32 v104, 287, v104\n"
    "v_lshlrev_b32 v105, 2, v109\n"
    "v_lshlrev_b32 v106, 2, v104\n"
    "v_lshlrev_b32 v107, 4, v109\n"
    "v_lshlrev_b32 v108, 4, v104\n"
    "s_mov_b32 s44, 34304\n"
    "s_mov_b32 s45, 61952\n"
    "s_mov_b32 s46, 38912\n"
    "s_mov_b32 s47, 66560\n"
    "s_mov_b32 s48, 43520\n"
    "s_mov_b32 s49, 71168\n"
    "v_mov_b32 v14, 147968\n"
    "ds_read_b32 v4, v14 offset:480\n"
    "s_waitcnt lgkmcnt(0)\n"
    "v_readfirstlane_b32 s96, v4\n"
    "s_mov_b64 exec, -1\n"
    "global_load_dword v45, v105, s[26:27]\n"
    "global_load_dword v46, v106, s[26:27]\n"
    "s_setprio 1\n"
    "U_35:\n"
    "s_memrealtime s[98:99]\n"
    "s_waitcnt lgkmcnt(0)\n"
    "s_sub_u32 s98, s98, s96\n"
    "s_cmpk_lt_u32 s98, 300\n"
    "s_cbranch_scc1 U_35\n"
    "buffer_load_dwordx4 v[48:51], v107, s[20:23], s44 offen sc1\n"
    "buffer_load_dwordx4 v[52:55], v107, s[20:23], s45 offen sc1\n"
    "buffer_load_dwordx4 v[56:59], v107, s[20:23], s46 offen sc1\n"
    "buffer_load_dwordx4 v[60:63], v107, s[20:23], s47 offen sc1\n"
    "buffer_load_dwordx4 v[64:67], v107, s[20:23], s48 offen sc1\n"
    "buffer_load_dwordx4 v[68:71], v107, s[20:23], s49 offen sc1\n"
    "buffer_load_dwordx4 v[72:75], v108, s[20:23], s44 offen sc1\n"
    "buffer_load_dwordx4 v[76:79], v108, s[20:23], s45 offen sc1\n"
    "buffer_load_dwordx4 v[80:83], v108, s[20:23], s46 offen sc1\n"
    "buffer_load_dwordx4 v[84:87], v108, s[20:23], s47 offen sc1\n"
    "buffer_load_dwordx4 v[88:91], v108, s[20:23], s48 offen sc1\n"
    "buffer_load_dwordx4 v[92:95], v108, s[20:23], s49 offen sc1\n"
    "v_and_b32 v1, 63, v0\n"
    "v_lshlrev_b32 v1, 4, v1\n"
    "s_mov_b64 exec, -1\n"
    "v_mov_b32 v8, 0\n"
    "v_mov_b32 v9, 0\n"
    "v_mov_b32 v10, 0\n"
    "v_mov_b32 v11, 0\n"
    "v_lshrrev_b32 v2, 9, v1\n"
    "v_mul_u32_u24 v2, 1536, v2\n"
    "v_add_u32 v2, v2, v1\n"
    "v_add_u32 v2, 134048, v2\n"
    "ds_write_b128 v2, v[8:11]\n"
    "s_mov_b32 s42, 138240\n"
    "v_lshl_add_u32 v1, v107, 1, s42\n"
    "v_lshl_add_u32 v2, v108, 1, s42\n"
    "v_add_u32 v41, 72320, v105\n"
    "v_add_u32 v42, 72320, v106\n"
    "v_mov_b32 v15, 1\n"
    "v_mov_b32 v14, 147968\n"
    "s_branch E_37\n"
    "R_36:\n"
    "s_sub_u32 s39, s39, 1\n"
    "s_cbranch_scc1 ABORT\n"
    "s_sleep 4\n"
    "buffer_load_dwordx4 v[48:51], v107, s[20:23], s44 offen sc1\n"
    "buffer_load_dwordx4 v[52:55], v107, s[20:23], s45 offen sc1\n"
    "buffer_load_dwordx4 v[56:59], v107, s[20:23], s46 offen sc1\n"
    "buffer_load_dwordx4 v[60:63], v107, s[20:23], s47 offen sc1\n"
    "buffer_load_dwordx4 v[64:67], v107, s[20:23], s48 offen sc1\n"
    "buffer_load_dwordx4 v[68:71], v107, s[20:23], s49 offen sc1\n"
    "buffer_load_dwordx4 v[72:75], v108, s[20:23], s44 offen sc1\n"
    "buffer_load_dwordx4 v[76:79], v108, s[20:23], s45 offen sc1\n"
    "buffer_load_dwordx4 v[80:83], v108, s[20:23], s46 offen sc1\n"
    "buffer_load_dwordx4 v[84:87], v108, s[20:23], s47 offen sc1\n"
    "buffer_load_dwordx4 v[88:91], v108, s[20:23], s48 offen sc1\n"
    "buffer_load_dwordx4 v[92:95], v108, s[20:23], s49 offen sc1\n"
    "E_37:\n"
    "s_waitcnt vmcnt(0)\n"
    "v_bitop3_b32 v16, v49, v51, s36 bitop3:0x7e\n"
    "v_bitop3_b32 v17, v53, v55, s36 bitop3:0x7e\n"
    "v_bitop3_b32 v18, v57, v59, s36 bitop3:0x7e\n"
    "v_bitop3_b32 v19, v61, v63, s36 bitop3:0x7e\n"
    "v_bitop3_b32 v20, v65, v67, s36 bitop3:0x7e\n"
    "v_bitop3_b32 v21, v69, v71, s36 bitop3:0x7e\n"
    "v_bitop3_b32 v22, v73, v75, s36 bitop3:0x7e\n"
    "v_bitop3_b32 v23, v77, v79, s36 bitop3:0x7e\n"
    "v_bitop3_b32 v24, v81, v83, s36 bitop3:0x7e\n"
    "v_bitop3_b32 v25, v85, v87, s36 bitop3:0x7e\n"
    "v_bitop3_b32 v26, v89, v91, s36 bitop3:0x7e\n"
    "v_bitop3_b32 v27, v93, v95, s36 bitop3:0x7e\n"
    "v_or3_b32 v16, v16, v17, v18\n"
    "v_or3_b32 v19, v19, v20, v21\n"
    "v_or3_b32 v22, v22, v23, v24\n"
    "v_or3_b32 v25, v25, v26, v27\n"
    "v_or3_b32 v16, v16, v19, v22\n"
    "v_or_b32 v16, v16, v25\n"
    "v_cmp_ne_u32 vcc, 0, v16\n"
    "s_cbranch_vccnz R_36\n"
    "ds_write_b32 v41, v45\n"
    "v_add_f32 v16, v56, v60\n"
    "v_add_f32 v17, v48, v52\n"
    "v_add_f32 v19, v64, v68\n"
    "v_cvt_pk_bf16_f32 v16, 0, v16\n"
    "v_cvt_pk_bf16_f32 v17, 0, v17\n"
    "v_cvt_pk_bf16_f32 v19, 0, v19\n"
    "v_add_f32 v16, 1.0, v16\n"
    "v_lshlrev_b32 v32, 16, v45\n"
    "v_cvt_pk_bf16_f32 v16, 0, v16\n"
    "v_mul_f32 v18, v16, v32\n"
    "ds_write_b128 v1, v[16:19] offset:0\n"
    "v_add_f32 v20, v58, v62\n"
    "v_add_f32 v21, v50, v54\n"
    "v_add_f32 v23, v66, v70\n"
    "v_cvt_pk_bf16_f32 v20, 0, v20\n"
    "v_cvt_pk_bf16_f32 v21, 0, v21\n"
    "v_cvt_pk_bf16_f32 v23, 0, v23\n"
    "v_add_f32 v20, 1.0, v20\n"
    "v_and_b32 v33, 0xffff0000, v45\n"
    "v_cvt_pk_bf16_f32 v20, 0, v20\n"
    "v_mul_f32 v22, v20, v33\n"
    "ds_write_b128 v1, v[20:23] offset:16\n"
    "ds_write_b32 v42, v46\n"
    "v_add_f32 v24, v80, v84\n"
    "v_add_f32 v25, v72, v76\n"
    "v_add_f32 v27, v88, v92\n"
    "v_cvt_pk_bf16_f32 v24, 0, v24\n"
    "v_cvt_pk_bf16_f32 v25, 0, v25\n"
    "v_cvt_pk_bf16_f32 v27, 0, v27\n"
    "v_add_f32 v24, 1.0, v24\n"
    "v_lshlrev_b32 v34, 16, v46\n"
    "v_cvt_pk_bf16_f32 v24, 0, v24\n"
    "v_mul_f32 v26, v24, v34\n"
    "ds_write_b128 v2, v[24:27] offset:0\n"
    "v_add_f32 v28, v82, v86\n"
    "v_add_f32 v29, v74, v78\n"
    "v_add_f32 v31, v90, v94\n"
    "v_cvt_pk_bf16_f32 v28, 0, v28\n"
    "v_cvt_pk_bf16_f32 v29, 0, v29\n"
    "v_cvt_pk_bf16_f32 v31, 0, v31\n"
    "v_add_f32 v28, 1.0, v28\n"
    "v_and_b32 v35, 0xffff0000, v46\n"
    "v_cvt_pk_bf16_f32 v28, 0, v28\n"
    "v_mul_f32 v30, v28, v35\n"
    "ds_write_b128 v2, v[28:31] offset:16\n"
    "s_mov_b64 exec, 1\n"
    "ds_add_u32 v14, v15 offset:56\n"
    "s_mov_b64 exec, -1\n"
    "s_setprio 0\n"
    "v_cmp_gt_u32 vcc, 0x60, v109\n"
    "v_mul_f32 v32, v32, v17\n"
    "v_mul_f32 v33, v33, v21\n"
    "v_add_f32 v4, v18, v22\n"
    "v_mul_f32 v5, v18, v18\n"
    "v_fmac_f32 v5, v22, v22\n"
    "v_mul_f32 v5, 4.0, v5\n"
    "v_fmac_f32 v5, v32, v32\n"
    "v_fmac_f32 v5, v33, v33\n"
    "v_mul_f32 v34, v34, v25\n"
    "v_mul_f32 v35, v35, v29\n"
    "v_add_f32 v6, v26, v30\n"
    "v_mul_f32 v7, v26, v26\n"
    "v_fmac_f32 v7, v30, v30\n"
    "v_mul_f32 v7, 4.0, v7\n"
    "v_fmac_f32 v7, v34, v34\n"
    "v_fmac_f32 v7, v35, v35\n"
    "v_cndmask_b32 v6, 0, v6, vcc\n"
    "v_cndmask_b32 v7, 0, v7, vcc\n"
    "v_add_f32 v4, v4, v6\n"
    "v_add_f32 v5, v5, v7\n"
    "v_lshrrev_b32 v3, 4, v109\n"
    "v_lshlrev_b32 v3, 3, v3\n"
    "v_add_u32 v3, 148736, v3\n"
    "v_add_f32_dpp v4, v4, v4 row_ror:8 row_mask:0xf bank_mask:0xf\n"
    "v_add_f32_dpp v5, v5, v5 row_ror:8 row_mask:0xf bank_mask:0xf\n"
    "s_nop 0\n"
    "v_add_f32_dpp v4, v4, v4 row_ror:4 row_mask:0xf bank_mask:0xf\n"
    "v_add_f32_dpp v5, v5, v5 row_ror:4 row_mask:0xf bank_mask:0xf\n"
    "s_nop 0\n"
    "v_add_f32_dpp v4, v4, v4 row_ror:2 row_mask:0xf bank_mask:0xf\n"
    "v_add_f32_dpp v5, v5, v5 row_ror:2 row_mask:0xf bank_mask:0xf\n"
    "s_nop 0\n"
    "v_add_f32_dpp v4, v4, v4 row_ror:1 row_mask:0xf bank_mask:0xf\n"
    "v_add_f32_dpp v5, v5, v5 row_ror:1 row_mask:0xf bank_mask:0xf\n"
    "ds_write_b64 v3, v[4:5]\n"
    "LN2:\n"
    "s_cmpk_ge_u32 s2, 128\n"
    "s_cbranch_scc1 CDONE\n"
    "s_and_b32 s57, s2, 7\n"
    "s_lshl_b32 s57, s57, 4\n"
    "s_lshr_b32 s43, s2, 3\n"
    "s_add_u32 s57, s57, s43\n"
    "v_and_b32 v2, 7, v0\n"
    "v_lshrrev_b32 v3, 3, v0\n"
    "v_mul_u32_u24 v6, 12, v3\n"
    "s_movk_i32 s44, 0x900\n"
    "v_mad_u32_u24 v4, v2, s44, v6\n"
    "v_lshlrev_b32 v2, 4, v2\n"
    "v_add_u32 v4, v2, v4\n"
    "v_add_u32 v5, 72320, v6\n"
    "s_mov_b32 s44, 33024\n"
    "v_mad_u32_u24 v1, v3, s44, v2\n"
    "s_lshl_b32 s42, s57, 7\n"
    "s_add_u32 s42, s42, 131072\n"
    "s_add_u32 s43, s42, 16512\n"
    "s_mov_b32 s44, 138240\n"
    "v_lshl_add_u32 v6, v6, 3, s44\n"
    "v_mov_b32 v7, 147968\n"
    "v_and_b32 v16, 8, v0\n"
    "v_lshl_add_u32 v16, v16, 8, v2\n"
    "v_bfe_u32 v17, v0, 4, 2\n"
    "v_lshlrev_b32 v18, 2, v17\n"
    "s_lshl_b32 s44, s37, 7\n"
    "s_add_u32 s44, s44, 132512\n"
    "v_add3_u32 v16, v16, v18, s44\n"
    "v_and_b32 v18, 2, v17\n"
    "v_lshl_add_u32 v18, v18, 2, v2\n"
    "s_lshl_b32 s44, s37, 2\n"
    "s_add_u32 s44, s44, 136608\n"
    "v_mad_u32_u24 v18, v18, 6, s44\n"
    "v_and_b32 v17, 63, v0\n"
    "s_mov_b32 s44, 132512\n"
    "v_lshl_add_u32 v17, v17, 4, s44\n"
    "v_and_b32 v23, 7, v3\n"
    "v_mul_u32_u24 v22, 3072, v23\n"
    "v_add_u32 v22, v22, v2\n"
    "v_lshlrev_b32 v23, 2, v23\n"
    "v_lshl_add_u32 v23, v2, 1, v23\n"
    "s_mul_i32 s45, s57, 24576\n"
    "s_lshl_b32 s46, s37, 7\n"
    "s_add_u32 s45, s45, s46\n"
    "s_add_u32 s45, s45, 7341056\n"
    "s_add_u32 s46, s36, 1\n"
    "v_mov_b32 v87, s46\n"
    "v_mov_b32 v91, s46\n"
    "s_lshr_b32 s44, s57, 4\n"
    "s_lshl_b32 s44, s44, 1\n"
    "s_add_u32 s44, s44, 2\n"
    "s_branch E_39\n"
    "R_38:\n"
    "s_sleep 2\n"
    "s_sub_u32 s39, s39, 1\n"
    "s_cbranch_scc1 ABORT\n"
    "E_39:\n"
    "ds_read_b32 v19, v7 offset:56\n"
    "s_waitcnt lgkmcnt(0)\n"
    "v_cmp_ne_u32 vcc, 6, v19\n"
    "s_cbranch_vccnz R_38\n"
    "ds_read_b32 v8, v4 offset:53760\n"
    "ds_read_b32 v9, v4 offset:53764\n"
    "ds_read_b32 v10, v4 offset:53768\n"
    "ds_read_b32 v12, v4 offset:54912\n"
    "ds_read_b32 v13, v4 offset:54916\n"
    "ds_read_b32 v14, v4 offset:54920\n"
    "ds_read_b128 v[40:43], v6 offset:0\n"
    "ds_read_b128 v[44:47], v6 offset:16\n"
    "ds_read_b128 v[48:51], v6 offset:32\n"
    "ds_read_b128 v[52:55], v6 offset:48\n"
    "ds_read_b128 v[56:59], v6 offset:64\n"
    "ds_read_b128 v[60:63], v6 offset:80\n"
    "ds_read2_b32 v[36:37], v5 offset1:1\n"
    "ds_read_b32 v38, v5 offset:8\n"
    "s_waitcnt lgkmcnt(8)\n"
    "v_lshlrev_b32 v24, 16, v8\n"
    "v_lshlrev_b32 v25, 16, v12\n"
    "v_and_b32 v26, 0xffff0000, v8\n"
    "v_and_b32 v27, 0xffff0000, v12\n"
    "v_lshlrev_b32 v28, 16, v9\n"
    "v_lshlrev_b32 v29, 16, v13\n"
    "v_and_b32 v30, 0xffff0000, v9\n"
    "v_and_b32 v31, 0xffff0000, v13\n"
    "v_lshlrev_b32 v32, 16, v10\n"
    "v_lshlrev_b32 v33, 16, v14\n"
    "v_and_b32 v34, 0xffff0000, v10\n"
    "v_and_b32 v35, 0xffff0000, v14\n"
    "s_branch E_41\n"
    "R_40:\n"
    "s_sleep 1\n"
    "s_sub_u32 s39, s39, 1\n"
    "s_cbranch_scc1 ABORT\n"
    "E_41:\n"
    "ds_read_b32 v19, v7 offset:60\n"
    "s_waitcnt lgkmcnt(0)\n"
    "v_cmp_gt_u32 vcc, s44, v19\n"
    "s_cbranch_vccnz R_40\n"
    "s_cmpk_lt_u32 s57, 96\n"
    "s_cbranch_scc1 LN2g\n"
    "s_sleep 8\n"
    "LN2g:\n"
    "buffer_load_dwordx4 v[72:75], v1, s[20:23], s42 offen sc1\n"
    "buffer_load_dwordx4 v[76:79], v1, s[20:23], s43 offen sc1\n"
    "s_branch E_43\n"
    "R_42:\n"
    "s_sub_u32 s39, s39, 1\n"
    "s_cbranch_scc1 ABORT\n"
    "s_wqm_b64 exec, vcc\n"
    "buffer_load_dwordx4 v[72:75], v1, s[20:23], s42 offen sc1\n"
    "buffer_load_dwordx4 v[76:79], v1, s[20:23], s43 offen sc1\n"
    "s_mov_b64 exec, -1\n"
    "E_43:\n"
    "s_waitcnt vmcnt(0)\n"
    "v_bitop3_b32 v19, v75, v79, s36 bitop3:0x7e\n"
    "v_cmp_ne_u32 vcc, 0, v19\n"
    "s_cbranch_vccnz R_42\n"
    "v_lshlrev_b32 v20, 16, v72\n"
    "v_and_b32 v21, 0xffff0000, v72\n"
    "v_pk_add_f32 v[24:25], v[24:25], v[20:21]\n"
    "v_cvt_pk_bf16_f32 v24, 0, v24\n"
    "v_cvt_pk_bf16_f32 v25, 0, v25\n"
    "v_pk_mul_f32 v[10:11], v[24:25], v[24:25]\n"
    "v_pk_mul_f32 v[12:13], v[24:25], v[42:43] op_sel_hi:[1,0]\n"
    "v_lshlrev_b32 v20, 16, v73\n"
    "v_and_b32 v21, 0xffff0000, v73\n"
    "v_pk_add_f32 v[26:27], v[26:27], v[20:21]\n"
    "v_cvt_pk_bf16_f32 v26, 0, v26\n"
    "v_cvt_pk_bf16_f32 v27, 0, v27\n"
    "v_pk_add_f32 v[8:9], v[26:27], v[24:25]\n"
    "v_pk_fma_f32 v[10:11], v[26:27], v[26:27], v[10:11]\n"
    "v_pk_fma_f32 v[12:13], v[26:27], v[46:47], v[12:13] op_sel:[0,0,0] op_sel_hi:[1,0,1]\n"
    "v_lshlrev_b32 v20, 16, v74\n"
    "v_and_b32 v21, 0xffff0000, v74\n"
    "v_pk_add_f32 v[28:29], v[28:29], v[20:21]\n"
    "v_cvt_pk_bf16_f32 v28, 0, v28\n"
    "v_cvt_pk_bf16_f32 v29, 0, v29\n"
    "v_pk_add_f32 v[8:9], v[28:29], v[8:9]\n"
    "v_pk_fma_f32 v[10:11], v[28:29], v[28:29], v[10:11]\n"
    "v_pk_fma_f32 v[12:13], v[28:29], v[50:51], v[12:13] op_sel:[0,0,0] op_sel_hi:[1,0,1]\n"
    "v_lshlrev_b32 v20, 16, v76\n"
    "v_and_b32 v21, 0xffff0000, v76\n"
    "v_pk_add_f32 v[30:31], v[30:31], v[20:21]\n"
    "v_cvt_pk_bf16_f32 v30, 0, v30\n"
    "v_cvt_pk_bf16_f32 v31, 0, v31\n"
    "v_pk_add_f32 v[8:9], v[30:31], v[8:9]\n"
    "v_pk_fma_f32 v[10:11], v[30:31], v[30:31], v[10:11]\n"
    "v_pk_fma_f32 v[12:13], v[30:31], v[54:55], v[12:13] op_sel:[0,0,0] op_sel_hi:[1,0,1]\n"
    "v_lshlrev_b32 v20, 16, v77\n"
    "v_and_b32 v21, 0xffff0000, v77\n"
    "v_pk_add_f32 v[32:33], v[32:33], v[20:21]\n"
    "v_cvt_pk_bf16_f32 v32, 0, v32\n"
    "v_cvt_pk_bf16_f32 v33, 0, v33\n"
    "v_pk_add_f32 v[8:9], v[32:33], v[8:9]\n"
    "v_pk_fma_f32 v[10:11], v[32:33], v[32:33], v[10:11]\n"
    "v_pk_fma_f32 v[12:13], v[32:33], v[58:59], v[12:13] op_sel:[0,0,0] op_sel_hi:[1,0,1]\n"
    "v_lshlrev_b32 v20, 16, v78\n"
    "v_and_b32 v21, 0xffff0000, v78\n"
    "v_pk_add_f32 v[34:35], v[34:35], v[20:21]\n"
    "v_cvt_pk_bf16_f32 v34, 0, v34\n"
    "v_cvt_pk_bf16_f32 v35, 0, v35\n"
    "v_pk_add_f32 v[8:9], v[34:35], v[8:9]\n"
    "v_pk_fma_f32 v[10:11], v[34:35], v[34:35], v[10:11]\n"
    "v_pk_fma_f32 v[12:13], v[34:35], v[62:63], v[12:13] op_sel:[0,0,0] op_sel_hi:[1,0,1]\n"
    "v_permlane32_swap_b32 v8, v9\n"
    "v_permlane32_swap_b32 v10, v11\n"
    "v_permlane32_swap_b32 v12, v13\n"
    "v_add_f32 v8, v8, v9\n"
    "v_add_f32 v10, v10, v11\n"
    "v_add_f32 v12, v12, v13\n"
    "s_nop 0\n"
    "v_permlane16_swap_b32 v8, v10\n"
    "v_add_f32 v8, v8, v10\n"
    "s_nop 1\n"
    "v_add_f32_dpp v9, v8, v8 row_ror:8 row_mask:0xf bank_mask:0x3\n"
    "s_nop 1\n"
    "v_add_f32_dpp v9, v12, v12 row_ror:8 row_mask:0xf bank_mask:0xc\n"
    "ds_write_b32 v16, v9\n"
    "s_waitcnt lgkmcnt(0)\n"
    "s_barrier\n"
    "ds_read_b128 v[8:11], v17\n"
    "ds_read_b128 v[12:15], v17 offset:1024\n"
    "s_waitcnt lgkmcnt(0)\n"
    "v_pk_add_f32 v[8:9], v[8:9], v[12:13]\n"
    "v_pk_add_f32 v[10:11], v[10:11], v[14:15]\n"
    "s_nop 1\n"
    "v_permlane32_swap_b32 v8, v10\n"
    "v_permlane32_swap_b32 v9, v11\n"
    "v_pk_add_f32 v[8:9], v[8:9], v[10:11]\n"
    "s_nop 1\n"
    "v_add_f32_dpp v8, v8, v8 row_ror:8 row_mask:0xf bank_mask:0xf\n"
    "v_add_f32_dpp v9, v9, v9 row_ror:8 row_mask:0xf bank_mask:0xf\n"
    "v_mov_b32 v10, v8\n"
    "v_mov_b32 v11, v9\n"
    "s_nop 0\n"
    "v_permlane16_swap_b32 v8, v10\n"
    "v_permlane16_swap_b32 v9, v11\n"
    "v_pk_add_f32 v[8:9], v[8:9], v[10:11]\n"
    "v_mul_f32 v8, 0x3ae38e39, v8\n"
    "v_mul_f32 v9, 0x3ae38e39, v9\n"
    "v_fma_f32 v9, -v8, v8, v9\n"
    "v_max_f32 v9, 0, v9\n"
    "v_cvt_pk_bf16_f32 v9, 0, v9\n"
    "v_add_f32 v9, 0x3727c5ac, v9\n"
    "v_cvt_pk_bf16_f32 v9, 0, v9\n"
    "v_sqrt_f32 v9, v9\n"
    "s_nop 0\n"
    "v_cvt_pk_bf16_f32 v8, v8, v9\n"
    "v_mov_b32 v9, v8\n"
    "s_nop 1\n"
    "v_permlane32_swap_b32 v8, v9\n"
    "v_and_b32 v12, 0xffff0000, v8\n"
    "v_and_b32 v13, 0xffff0000, v9\n"
    "v_lshlrev_b32 v10, 16, v8\n"
    "v_lshlrev_b32 v11, 16, v9\n"
    "v_rcp_f32 v12, v12\n"
    "v_rcp_f32 v13, v13\n"
    "v_pk_add_f32 v[80:81], v[24:25], v[10:11] neg_lo:[0,1] neg_hi:[0,1]\n"
    "v_pk_add_f32 v[82:83], v[26:27], v[10:11] neg_lo:[0,1] neg_hi:[0,1]\n"
    "v_cvt_pk_bf16_f32 v80, 0, v80\n"
    "v_cvt_pk_bf16_f32 v81, 0, v81\n"
    "v_cvt_pk_bf16_f32 v82, 0, v82\n"
    "v_cvt_pk_bf16_f32 v83, 0, v83\n"
    "v_pk_mul_f32 v[80:81], v[80:81], v[12:13]\n"
    "v_pk_mul_f32 v[82:83], v[82:83], v[12:13]\n"
    "v_cvt_pk_bf16_f32 v80, 0, v80\n"
    "v_cvt_pk_bf16_f32 v81, 0, v81\n"
    "v_cvt_pk_bf16_f32 v82, 0, v82\n"
    "v_cvt_pk_bf16_f32 v83, 0, v83\n"
    "v_pk_mul_f32 v[80:81], v[80:81], v[40:41] op_sel_hi:[1,0]\n"
    "v_pk_mul_f32 v[82:83], v[82:83], v[44:45] op_sel_hi:[1,0]\n"
    "v_cvt_pk_bf16_f32 v80, 0, v80\n"
    "v_cvt_pk_bf16_f32 v81, 0, v81\n"
    "v_cvt_pk_bf16_f32 v82, 0, v82\n"
    "v_cvt_pk_bf16_f32 v83, 0, v83\n"
    "v_pk_add_f32 v[80:81], v[40:41], v[80:81] op_sel:[1,0] op_sel_hi:[1,1]\n"
    "v_pk_add_f32 v[82:83], v[44:45], v[82:83] op_sel:[1,0] op_sel_hi:[1,1]\n"
    "v_cvt_pk_bf16_f32 v72, v80, v82\n"
    "v_cvt_pk_bf16_f32 v76, v81, v83\n"
    "ds_bpermute_b32 v84, v23, v72\n"
    "ds_bpermute_b32 v88, v23, v76\n"
    "v_dot2_f32_bf16 v14, v72, v36, 0\n"
    "v_dot2_f32_bf16 v15, v76, v36, 0\n"
    "v_pk_add_f32 v[80:81], v[28:29], v[10:11] neg_lo:[0,1] neg_hi:[0,1]\n"
    "v_pk_add_f32 v[82:83], v[30:31], v[10:11] neg_lo:[0,1] neg_hi:[0,1]\n"
    "v_cvt_pk_bf16_f32 v80, 0, v80\n"
    "v_cvt_pk_bf16_f32 v81, 0, v81\n"
    "v_cvt_pk_bf16_f32 v82, 0, v82\n"
    "v_cvt_pk_bf16_f32 v83, 0, v83\n"
    "v_pk_mul_f32 v[80:81], v[80:81], v[12:13]\n"
    "v_pk_mul_f32 v[82:83], v[82:83], v[12:13]\n"
    "v_cvt_pk_bf16_f32 v80, 0, v80\n"
    "v_cvt_pk_bf16_f32 v81, 0, v81\n"
    "v_cvt_pk_bf16_f32 v82, 0, v82\n"
    "v_cvt_pk_bf16_f32 v83, 0, v83\n"
    "v_pk_mul_f32 v[80:81], v[80:81], v[48:49] op_sel_hi:[1,0]\n"
    "v_pk_mul_f32 v[82:83], v[82:83], v[52:53] op_sel_hi:[1,0]\n"
    "v_cvt_pk_bf16_f32 v80, 0, v80\n"
    "v_cvt_pk_bf16_f32 v81, 0, v81\n"
    "v_cvt_pk_bf16_f32 v82, 0, v82\n"
    "v_cvt_pk_bf16_f32 v83, 0, v83\n"
    "v_pk_add_f32 v[80:81], v[48:49], v[80:81] op_sel:[1,0] op_sel_hi:[1,1]\n"
    "v_pk_add_f32 v[82:83], v[52:53], v[82:83] op_sel:[1,0] op_sel_hi:[1,1]\n"
    "v_cvt_pk_bf16_f32 v73, v80, v82\n"
    "v_cvt_pk_bf16_f32 v77, v81, v83\n"
    "ds_bpermute_b32 v85, v23, v73\n"
    "ds_bpermute_b32 v89, v23, v77\n"
    "v_dot2_f32_bf16 v14, v73, v37, v14\n"
    "v_dot2_f32_bf16 v15, v77, v37, v15\n"
    "v_pk_add_f32 v[80:81], v[32:33], v[10:11] neg_lo:[0,1] neg_hi:[0,1]\n"
    "v_pk_add_f32 v[82:83], v[34:35], v[10:11] neg_lo:[0,1] neg_hi:[0,1]\n"
    "v_cvt_pk_bf16_f32 v80, 0, v80\n"
    "v_cvt_pk_bf16_f32 v81, 0, v81\n"
    "v_cvt_pk_bf16_f32 v82, 0, v82\n"
    "v_cvt_pk_bf16_f32 v83, 0, v83\n"
    "v_pk_mul_f32 v[80:81], v[80:81], v[12:13]\n"
    "v_pk_mul_f32 v[82:83], v[82:83], v[12:13]\n"
    "v_cvt_pk_bf16_f32 v80, 0, v80\n"
    "v_cvt_pk_bf16_f32 v81, 0, v81\n"
    "v_cvt_pk_bf16_f32 v82, 0, v82\n"
    "v_cvt_pk_bf16_f32 v83, 0, v83\n"
    "v_pk_mul_f32 v[80:81], v[80:81], v[56:57] op_sel_hi:[1,0]\n"
    "v_pk_mul_f32 v[82:83], v[82:83], v[60:61] op_sel_hi:[1,0]\n"
    "v_cvt_pk_bf16_f32 v80, 0, v80\n"
    "v_cvt_pk_bf16_f32 v81, 0, v81\n"
    "v_cvt_pk_bf16_f32 v82, 0, v82\n"
    "v_cvt_pk_bf16_f32 v83, 0, v83\n"
    "v_pk_add_f32 v[80:81], v[56:57], v[80:81] op_sel:[1,0] op_sel_hi:[1,1]\n"
    "v_pk_add_f32 v[82:83], v[60:61], v[82:83] op_sel:[1,0] op_sel_hi:[1,1]\n"
    "v_cvt_pk_bf16_f32 v74, v80, v82\n"
    "v_cvt_pk_bf16_f32 v78, v81, v83\n"
    "ds_bpermute_b32 v86, v23, v74\n"
    "ds_bpermute_b32 v90, v23, v78\n"
    "v_dot2_f32_bf16 v14, v74, v38, v14\n"
    "v_dot2_f32_bf16 v15, v78, v38, v15\n"
    "s_nop 2\n"
    "v_permlane32_swap_b32 v14, v15\n"
    "v_add_f32 v14, v14, v15\n"
    "s_nop 1\n"
    "v_add_f32_dpp v14, v14, v14 row_ror:8 row_mask:0xf bank_mask:0xf\n"
    "v_mov_b32 v15, v14\n"
    "s_nop 1\n"
    "v_permlane16_swap_b32 v14, v15\n"
    "v_add_f32 v14, v14, v15\n"
    "ds_write_b32 v18, v14\n"
    "s_waitcnt lgkmcnt(1)\n"
    "buffer_store_dwordx4 v[84:87], v22, s[20:23], s45 offen sc1\n"
    "buffer_store_dwordx4 v[88:91], v22, s[20:23], s45 offen offset:1536 sc1\n"
    "s_waitcnt lgkmcnt(0)\n"
    "s_barrier\n"
    "v_lshlrev_b32 v80, 16, v72\n"
    "v_lshlrev_b32 v81, 16, v76\n"
    "v_and_b32 v82, 0xffff0000, v72\n"
    "v_and_b32 v83, 0xffff0000, v76\n"
    "v_pk_mul_f32 v[80:81], v[42:43], v[80:81] op_sel:[1,0] op_sel_hi:[1,1]\n"
    "v_pk_mul_f32 v[82:83], v[46:47], v[82:83] op_sel:[1,0] op_sel_hi:[1,1]\n"
    "v_cvt_pk_bf16_f32 v80, 0, v80\n"
    "v_cvt_pk_bf16_f32 v81, 0, v81\n"
    "v_cvt_pk_bf16_f32 v82, 0, v82\n"
    "v_cvt_pk_bf16_f32 v83, 0, v83\n"
    "v_pk_add_f32 v[80:81], v[80:81], v[24:25]\n"
    "v_pk_add_f32 v[82:83], v[82:83], v[26:27]\n"
    "v_cvt_pk_bf16_f32 v8, v80, v82\n"
    "v_cvt_pk_bf16_f32 v12, v81, v83\n"
    "ds_write_b32 v4, v8 offset:18560\n"
    "ds_write_b32 v4, v12 offset:19712\n"
    "v_lshlrev_b32 v80, 16, v73\n"
    "v_lshlrev_b32 v81, 16, v77\n"
    "v_and_b32 v82, 0xffff0000, v73\n"
    "v_and_b32 v83, 0xffff0000, v77\n"
    "v_pk_mul_f32 v[80:81], v[50:51], v[80:81] op_sel:[1,0] op_sel_hi:[1,1]\n"
    "v_pk_mul_f32 v[82:83], v[54:55], v[82:83] op_sel:[1,0] op_sel_hi:[1,1]\n"
    "v_cvt_pk_bf16_f32 v80, 0, v80\n"
    "v_cvt_pk_bf16_f32 v81, 0, v81\n"
    "v_cvt_pk_bf16_f32 v82, 0, v82\n"
    "v_cvt_pk_bf16_f32 v83, 0, v83\n"
    "v_pk_add_f32 v[80:81], v[80:81], v[28:29]\n"
    "v_pk_add_f32 v[82:83], v[82:83], v[30:31]\n"
    "v_cvt_pk_bf16_f32 v9, v80, v82\n"
    "v_cvt_pk_bf16_f32 v13, v81, v83\n"
    "ds_write_b32 v4, v9 offset:18564\n"
    "ds_write_b32 v4, v13 offset:19716\n"
    "v_lshlrev_b32 v80, 16, v74\n"
    "v_lshlrev_b32 v81, 16, v78\n"
    "v_and_b32 v82, 0xffff0000, v74\n"
    "v_and_b32 v83, 0xffff0000, v78\n"
    "v_pk_mul_f32 v[80:81], v[58:59], v[80:81] op_sel:[1,0] op_sel_hi:[1,1]\n"
    "v_pk_mul_f32 v[82:83], v[62:63], v[82:83] op_sel:[1,0] op_sel_hi:[1,1]\n"
    "v_cvt_pk_bf16_f32 v80, 0, v80\n"
    "v_cvt_pk_bf16_f32 v81, 0, v81\n"
    "v_cvt_pk_bf16_f32 v82, 0, v82\n"
    "v_cvt_pk_bf16_f32 v83, 0, v83\n"
    "v_pk_add_f32 v[80:81], v[80:81], v[32:33]\n"
    "v_pk_add_f32 v[82:83], v[82:83], v[34:35]\n"
    "v_cvt_pk_bf16_f32 v10, v80, v82\n"
    "v_cvt_pk_bf16_f32 v14, v81, v83\n"
    "ds_write_b32 v4, v10 offset:18568\n"
    "ds_write_b32 v4, v14 offset:19720\n"
    "s_waitcnt lgkmcnt(0)\n"
    "s_barrier\n"
    "s_branch END\n"
    "LN2K:\n"
    "s_setprio 0\n"
    "s_cmpk_ge_u32 s2, 128\n"
    "s_cbranch_scc1 PFZ\n"
    "s_and_b32 s57, s2, 7\n"
    "s_lshl_b32 s57, s57, 4\n"
    "s_lshr_b32 s43, s2, 3\n"
    "s_add_u32 s57, s57, s43\n"
    "v_and_b32 v1, 63, v0\n"
    "s_cmpk_lg_u32 s37, 12\n"
    "s_cbranch_scc1 LN2C\n"
    "s_setprio 3\n"
    "v_lshlrev_b32 v17, 4, v1\n"
    "v_add_u32 v17, 132512, v17\n"
    "v_and_b32 v2, 7, v0\n"
    "v_lshrrev_b32 v3, 5, v1\n"
    "v_lshl_add_u32 v2, v2, 1, v3\n"
    "v_lshlrev_b32 v3, 3, v2\n"
    "s_lshl_b32 s42, s57, 4\n"
    "s_sub_u32 s42, 2047, s42\n"
    "v_sub_u32 v2, s42, v2\n"
    "v_and_b32 v4, 15, v0\n"
    "v_sub_u32 v5, s42, v4\n"
    "v_lshlrev_b32 v6, 3, v4\n"
    "v_mul_u32_u24 v4, 48, v4\n"
    "v_add_u32 v4, 136608, v4\n"
    "s_lshl_b32 s42, s57, 7\n"
    "s_add_u32 s43, s42, 4096\n"
    "s_add_u32 s42, s42, 77824\n"
    "s_brev_b32 s46, 1\n"
    "s_movk_i32 s47, 0x7ff\n"
    "v_mov_b32 v21, s36\n"
    "v_mov_b32 v23, s36\n"
    "v_min_u32 v7, 12, v1\n"
    "v_lshlrev_b32 v7, 3, v7\n"
    "v_add_u32 v7, 148736, v7\n"
    "v_mov_b32 v32, 0\n"
    "v_mov_b32 v33, 0\n"
    "s_mov_b64 exec, 0x1000\n"
    "ds_write_b64 v7, v[32:33]\n"
    "s_mov_b64 exec, -1\n"
    "s_waitcnt lgkmcnt(0)\n"
    "s_barrier\n"
    "ds_read_b128 v[8:11], v17\n"
    "ds_read_b128 v[12:15], v17 offset:1024\n"
    "ds_read_b128 v[24:27], v17 offset:2048\n"
    "ds_read_b128 v[28:31], v17 offset:3072\n"
    "ds_read_b64 v[32:33], v7\n"
    "s_waitcnt lgkmcnt(3)\n"
    "v_pk_add_f32 v[8:9], v[8:9], v[12:13]\n"
    "v_pk_add_f32 v[10:11], v[10:11], v[14:15]\n"
    "s_nop 1\n"
    "v_permlane32_swap_b32 v8, v10\n"
    "v_permlane32_swap_b32 v9, v11\n"
    "v_pk_add_f32 v[8:9], v[8:9], v[10:11]\n"
    "s_nop 1\n"
    "v_add_f32_dpp v8, v8, v8 row_ror:8 row_mask:0xf bank_mask:0xf\n"
    "v_add_f32_dpp v9, v9, v9 row_ror:8 row_mask:0xf bank_mask:0xf\n"
    "v_mov_b32 v10, v8\n"
    "v_mov_b32 v11, v9\n"
    "s_nop 0\n"
    "v_permlane16_swap_b32 v8, v10\n"
    "v_permlane16_swap_b32 v9, v11\n"
    "v_pk_add_f32 v[8:9], v[8:9], v[10:11]\n"
    "s_waitcnt lgkmcnt(0)\n"
    "v_pk_add_f32 v[24:25], v[24:25], v[28:29]\n"
    "v_pk_add_f32 v[26:27], v[26:27], v[30:31]\n"
    "v_add_f32_dpp v32, v32, v32 row_ror:8 row_mask:0xf bank_mask:0xf\n"
    "v_add_f32 v24, v24, v25\n"
    "v_add_f32 v26, v26, v27\n"
    "v_add_f32_dpp v32, v32, v32 row_ror:4 row_mask:0xf bank_mask:0xf\n"
    "v_mul_f32 v8, 0x3ae38e39, v8\n"
    "v_permlane32_swap_b32 v24, v26\n"
    "v_add_f32_dpp v32, v32, v32 row_ror:2 row_mask:0xf bank_mask:0xf\n"
    "v_add_f32 v24, v24, v26\n"
    "v_mul_f32 v9, 0x3ae38e39, v9\n"
    "v_add_f32_dpp v32, v32, v32 row_ror:1 row_mask:0xf bank_mask:0xf\n"
    "v_add_f32_dpp v24, v24, v24 row_ror:8 row_mask:0xf bank_mask:0xf\n"
    "v_fma_f32 v9, -v8, v8, v9\n"
    "v_mov_b32 v25, v24\n"
    "v_max_f32 v9, 0, v9\n"
    "v_cvt_pk_bf16_f32 v9, 0, v9\n"
    "v_permlane16_swap_b32 v24, v25\n"
    "v_add_f32 v9, 0x3727c5ac, v9\n"
    "v_readfirstlane_b32 s44, v32\n"
    "v_cvt_pk_bf16_f32 v9, 0, v9\n"
    "v_sqrt_f32 v9, v9\n"
    "v_add_f32 v24, v24, v25\n"
    "v_cvt_pk_bf16_f32 v8, v8, v9\n"
    "v_and_b32 v9, 0xffff0000, v8\n"
    "v_lshlrev_b32 v8, 16, v8\n"
    "v_rcp_f32 v9, v9\n"
    "v_fma_f32 v24, -v8, s44, v24\n"
    "v_mul_f32 v24, v24, v9\n"
    "v_and_b32 v9, 0x7ff, v24\n"
    "v_add_u32 v9, 0x7ff, v9\n"
    "v_or_b32 v24, v24, v9\n"
    "v_ashrrev_i32 v9, 31, v24\n"
    "v_bitop3_b32 v24, v9, v24, s46 bitop3:0x36\n"
    "v_bfi_b32 v20, s47, v2, v24\n"
    "v_mov_b32 v22, v20\n"
    "s_nop 1\n"
    "v_permlane32_swap_b32 v20, v22\n"
    "s_mov_b64 exec, 0xff\n"
    "buffer_store_dwordx4 v[20:23], v3, s[20:23], s42 offen sc1\n"
    "s_cmpk_lg_u32 s57, 0\n"
    "s_cbranch_scc1 LN2K2\n"
    "s_mov_b64 exec, -1\n"
    "v_add_f32_dpp v33, v33, v33 row_ror:8 row_mask:0xf bank_mask:0xf\n"
    "s_nop 1\n"
    "v_add_f32_dpp v33, v33, v33 row_ror:4 row_mask:0xf bank_mask:0xf\n"
    "s_nop 1\n"
    "v_add_f32_dpp v33, v33, v33 row_ror:2 row_mask:0xf bank_mask:0xf\n"
    "s_nop 1\n"
    "v_add_f32_dpp v33, v33, v33 row_ror:1 row_mask:0xf bank_mask:0xf\n"
    "s_mov_b64 exec, 1\n"
    "v_sqrt_f32 v34, v33\n"
    "v_mov_b32 v35, s36\n"
    "v_mul_f32 v34, 0x3c1374bc, v34\n"
    "buffer_store_dwordx2 v[34:35], off, s[20:23], 0 offset:2048 sc1\n"
    "LN2K2:\n"
    "s_mov_b64 exec, 0xffff\n"
    "s_waitcnt lgkmcnt(0)\n"
    "s_barrier\n"
    "ds_read_b128 v[8:11], v4\n"
    "ds_read_b128 v[12:15], v4 offset:16\n"
    "ds_read_b128 v[24:27], v4 offset:32\n"
    "s_waitcnt lgkmcnt(1)\n"
    "v_pk_add_f32 v[8:9], v[8:9], v[12:13]\n"
    "v_pk_add_f32 v[10:11], v[10:11], v[14:15]\n"
    "s_waitcnt lgkmcnt(0)\n"
    "v_pk_add_f32 v[8:9], v[8:9], v[24:25]\n"
    "v_pk_add_f32 v[10:11], v[10:11], v[26:27]\n"
    "v_pk_add_f32 v[8:9], v[8:9], v[10:11]\n"
    "v_add_f32 v8, v8, v9\n"
    "v_and_b32 v9, 0x7ff, v8\n"
    "v_add_u32 v9, 0x7ff, v9\n"
    "v_or_b32 v8, v8, v9\n"
    "v_ashrrev_i32 v9, 31, v8\n"
    "v_bitop3_b32 v8, v9, v8, s46 bitop3:0x36\n"
    "v_bfi_b32 v20, s47, v5, v8\n"
    "buffer_store_dwordx2 v[20:21], v6, s[20:23], s43 offen sc1\n"
    "s_mov_b64 exec, -1\n"
    "s_barrier\n"
    "s_branch END\n"
    "LN2C:\n"
    "s_setprio 2\n"
    "v_subrev_u32 v1, 832, v0\n"
    "v_add_u32 v2, 0, v1\n"
    "v_mul_u32_u24 v3, 7282, v2\n"
    "v_lshrrev_b32 v3, 20, v3\n"
    "v_add_lshl_u32 v8, v2, v3, 4\n"
    "v_add_u32 v2, 192, v1\n"
    "v_mul_u32_u24 v3, 7282, v2\n"
    "v_lshrrev_b32 v3, 20, v3\n"
    "v_add_lshl_u32 v9, v2, v3, 4\n"
    "v_add_u32 v2, 384, v1\n"
    "v_mul_u32_u24 v3, 7282, v2\n"
    "v_lshrrev_b32 v3, 20, v3\n"
    "v_add_lshl_u32 v10, v2, v3, 4\n"
    "v_add_u32 v2, 576, v1\n"
    "v_mul_u32_u24 v3, 7282, v2\n"
    "v_lshrrev_b32 v3, 20, v3\n"
    "v_add_lshl_u32 v11, v2, v3, 4\n"
    "v_add_u32 v2, 768, v1\n"
    "v_mul_u32_u24 v3, 7282, v2\n"
    "v_lshrrev_b32 v3, 20, v3\n"
    "v_add_lshl_u32 v12, v2, v3, 4\n"
    "v_add_u32 v2, 960, v1\n"
    "v_mul_u32_u24 v3, 7282, v2\n"
    "v_lshrrev_b32 v3, 20, v3\n"
    "v_add_lshl_u32 v13, v2, v3, 4\n"
    "s_mul_i32 s42, s57, 18432\n"
    "v_lshl_add_u32 v1, v1, 4, s42\n"
    "v_add_u32 v2, 6144, v1\n"
    "v_add_u32 v3, 12288, v1\n"
    "s_mov_b32 s42, 7341056\n"
    "s_lshl_b32 s43, s57, 2\n"
    "v_mov_b32 v4, s36\n"
    "s_barrier\n"
    "s_barrier\n"
    "s_barrier\n"
    "s_sleep 12\n"
    "ds_read_b128 v[40:43], v8 offset:18560\n"
    "ds_read_b128 v[44:47], v9 offset:18560\n"
    "ds_read_b128 v[48:51], v10 offset:18560\n"
    "ds_read_b128 v[52:55], v11 offset:18560\n"
    "ds_read_b128 v[56:59], v12 offset:18560\n"
    "ds_read_b128 v[60:63], v13 offset:18560\n"
    "s_waitcnt lgkmcnt(5)\n"
    "global_store_dwordx4 v1, v[40:43], s[34:35] offset:0 sc1\n"
    "s_waitcnt lgkmcnt(4)\n"
    "global_store_dwordx4 v1, v[44:47], s[34:35] offset:3072 sc1\n"
    "s_waitcnt lgkmcnt(3)\n"
    "global_store_dwordx4 v2, v[48:51], s[34:35] offset:0 sc1\n"
    "s_waitcnt lgkmcnt(2)\n"
    "global_store_dwordx4 v2, v[52:55], s[34:35] offset:3072 sc1\n"
    "s_waitcnt lgkmcnt(1)\n"
    "global_store_dwordx4 v3, v[56:59], s[34:35] offset:0 sc1\n"
    "s_waitcnt lgkmcnt(0)\n"
    "global_store_dwordx4 v3, v[60:63], s[34:35] offset:3072 sc1\n"
    "s_waitcnt vmcnt(0)\n"
    "s_barrier\n"
    "s_cmpk_lg_u32 s37, 15\n"
    "s_cbranch_scc1 END\n"
    "s_mov_b64 exec, 1\n"
    "buffer_store_dword v4, off, s[20:23], s43 offset:1024 sc1\n"
    "s_mov_b64 exec, -1\n"
    "s_branch END\n"
    "PFZ:\n"
    "s_cmpk_ge_u32 s2, 184\n"
    "s_cbranch_scc1 CDONE\n"
    "s_branch E_45\n"
    "R_44:\n"
    "s_sleep 1\n"
    "s_sub_u32 s39, s39, 1\n"
    "s_cbranch_scc1 ABORT\n"
    "E_45:\n"
    "ds_read_b32 v4, v14 offset:60\n"
    "s_waitcnt lgkmcnt(0)\n"
    "v_cmp_ne_u32 vcc, 16, v4\n"
    "s_cbranch_vccnz R_44\n"
    "v_subrev_u32 v112, 5184, v111\n"
    "v_min_u32 v111, v111, v112\n"
    "v_lshl_add_u32 v112, v111, 7, s60\n"
    "global_load_lds_dword v112, s[32:33] nt\n"
    "v_add_u32 v111, 1728, v111\n"
    "v_subrev_u32 v112, 5184, v111\n"
    "v_min_u32 v111, v111, v112\n"
    "v_lshl_add_u32 v112, v111, 7, s60\n"
    "global_load_lds_dword v112, s[32:33] nt\n"
    "v_add_u32 v111, 1728, v111\n"
    "CDONE:\n"
    "s_cmpk_lt_u32 s2, 184\n"
    "s_cbranch_scc1 END\n"
    "s_branch ES0\n"
    "SIL1:\n"
    "s_barrier\n"
    "v_add_u32 v1, 148736, v1\n"
    "v_mov_b32 v14, 147968\n"
    "v_mov_b32 v15, 1\n"
    "s_waitcnt vmcnt(0)\n"
    "v_lshlrev_b32 v5, 16, v4\n"
    "v_and_b32 v4, 0xffff0000, v4\n"
    "v_mul_f32 v6, 0xbfb8aa3b, v5\n"
    "v_mul_f32 v7, 0xbfb8aa3b, v4\n"
    "v_exp_f32 v6, v6\n"
    "v_exp_f32 v7, v7\n"
    "v_add_f32 v6, 1.0, v6\n"
    "v_add_f32 v7, 1.0, v7\n"
    "v_rcp_f32 v6, v6\n"
    "v_rcp_f32 v7, v7\n"
    "v_mul_f32 v5, v5, v6\n"
    "v_mul_f32 v4, v4, v7\n"
    "v_cvt_pk_bf16_f32 v4, v5, v4\n"
    "ds_write_b32 v1, v4\n"
    "s_mov_b64 exec, 1\n"
    "ds_add_u32 v14, v15 offset:4\n"
    "s_mov_b64 exec, -1\n"
    "s_branch M9\n"
    "M1:\n"
    "s_waitcnt lgkmcnt(0)\n"
    "s_memrealtime s[96:97]\n"
    "s_load_dword s36, s[20:21], 0\n"
    "s_barrier\n"
    "s_cmpk_ge_u32 s2, 246\n"
    "s_cbranch_scc1 M8\n"
    "s_sub_u32 s42, s2, 192\n"
    "s_lshr_b32 s42, s42, 3\n"
    "s_min_u32 s42, s42, 8\n"
    "s_lshr_b32 s44, s28, 7\n"
    "s_add_u32 s44, s44, s2\n"
    "s_sub_u32 s44, 3, s44\n"
    "s_and_b32 s44, s44, 7\n"
    "s_add_u32 s44, s44, 1\n"
    "s_add_u32 s44, s44, s42\n"
    "s_mul_i32 s45, s44, 57\n"
    "s_lshr_b32 s45, s45, 9\n"
    "s_mul_i32 s45, s45, 9\n"
    "s_sub_u32 s44, s44, s45\n"
    "s_and_b32 s58, s2, 7\n"
    "s_mul_i32 s58, s58, 9\n"
    "s_add_u32 s58, s58, s44\n"
    "v_bfe_u32 v12, v0, 3, 3\n"
    "v_bfe_u32 v13, v0, 5, 2\n"
    "v_xor_b32 v13, v13, v0\n"
    "v_mul_u32_u24 v12, 9216, v12\n"
    "v_and_b32 v13, 7, v13\n"
    "v_lshl_add_u32 v12, v13, 4, v12\n"
    "s_lshr_b32 s42, s37, 1\n"
    "s_mul_i32 s42, s42, 73728\n"
    "s_lshl_b32 s44, s58, 7\n"
    "s_add_u32 s42, s42, s44\n"
    "v_add_u32 v12, s42, v12\n"
    "v_xor_b32 v13, 64, v12\n"
    "v_add_u32 v13, 294912, v13\n"
    "s_bitcmp1_b32 s37, 0\n"
    "s_cselect_b64 s[56:57], s[30:31], s[28:29]\n"
    "s_sub_u32 s47, s37, 12\n"
    "s_sub_u32 s42, s2, 192\n"
    "s_lshr_b32 s43, s42, 1\n"
    "s_add_u32 s43, s43, 27\n"
    "s_and_b32 s44, s2, 1\n"
    "s_mul_i32 s45, s44, 1990656\n"
    "s_lshl_b32 s46, s43, 7\n"
    "s_add_u32 s45, s45, s46\n"
    "v_bfe_u32 v1, v0, 3, 5\n"
    "v_mul_u32_u24 v1, 0x1b00, v1\n"
    "v_lshrrev_b32 v2, 3, v0\n"
    "v_lshrrev_b32 v3, 4, v0\n"
    "v_and_b32 v2, 2, v2\n"
    "v_and_b32 v3, 4, v3\n"
    "v_xor_b32 v2, v2, v0\n"
    "v_xor_b32 v2, v2, v3\n"
    "v_and_b32 v2, 7, v2\n"
    "v_lshl_add_u32 v1, v2, 4, v1\n"
    "v_add_u32 v1, s45, v1\n"
    "s_lshl_b32 s45, s47, 10\n"
    "s_add_u32 m0, s45, 98304\n"
    "s_nop 0\n"
    "global_load_lds_dwordx4 v1, s[16:17] nt\n"
    "s_add_u32 m0, m0, 4096\n"
    "v_add_u32 v1, 221184, v1\n"
    "global_load_lds_dwordx4 v1, s[16:17] nt\n"
    "s_add_u32 m0, m0, 4096\n"
    "v_add_u32 v1, 221184, v1\n"
    "global_load_lds_dwordx4 v1, s[16:17] nt\n"
    "s_add_u32 m0, m0, 4096\n"
    "v_add_u32 v1, 221184, v1\n"
    "global_load_lds_dwordx4 v1, s[16:17] nt\n"
    "s_add_u32 m0, m0, 4096\n"
    "v_add_u32 v1, 221184, v1\n"
    "global_load_lds_dwordx4 v1, s[16:17] nt\n"
    "s_add_u32 m0, m0, 4096\n"
    "v_add_u32 v1, 221184, v1\n"
    "global_load_lds_dwordx4 v1, s[16:17] nt\n"
    "s_add_u32 m0, m0, 4096\n"
    "v_add_u32 v1, 221184, v1\n"
    "global_load_lds_dwordx4 v1, s[16:17] nt\n"
    "s_add_u32 m0, m0, 4096\n"
    "v_add_u32 v1, 221184, v1\n"
    "global_load_lds_dwordx4 v1, s[16:17] nt\n"
    "s_add_u32 m0, m0, 4096\n"
    "v_add_u32 v1, 221184, v1\n"
    "global_load_lds_dwordx4 v1, s[16:17] nt\n"
    "s_add_u32 m0, m0, 4096\n"
    "v_add_u32 v1, 221184, v1\n"
    "s_setprio 3\n"
    "s_barrier\n"
    "v_bfe_u32 v5, v0, 4, 2\n"
    "v_bfe_u32 v6, v0, 2, 2\n"
    "v_bfe_u32 v7, v0, 1, 1\n"
    "s_lshl_b32 s45, s47, 1\n"
    "v_or_b32 v7, s45, v7\n"
    "v_and_b32 v8, 2, v6\n"
    "v_xor_b32 v7, v7, v8\n"
    "v_lshlrev_b32 v8, 2, v5\n"
    "v_and_b32 v8, 4, v8\n"
    "v_xor_b32 v7, v7, v8\n"
    "v_lshlrev_b32 v2, 10, v5\n"
    "v_lshl_add_u32 v2, v6, 7, v2\n"
    "v_lshl_add_u32 v2, v7, 4, v2\n"
    "v_and_b32 v8, 1, v0\n"
    "v_lshl_add_u32 v2, v8, 3, v2\n"
    "v_add_u32 v2, 98304, v2\n"
    "s_mul_i32 s45, s44, 576\n"
    "s_add_u32 s45, s45, 148736\n"
    "v_lshl_add_u32 v3, v5, 4, s45\n"
    "s_mul_i32 s45, s44, 27648\n"
    "s_lshl_b32 s46, s43, 9\n"
    "s_add_u32 s45, s45, s46\n"
    "s_lshl_b32 s46, s47, 7\n"
    "s_add_u32 s45, s45, s46\n"
    "s_add_u32 s45, s45, 20480\n"
    "v_and_b32 v4, 15, v0\n"
    "v_lshlrev_b32 v4, 3, v4\n"
    "v_mov_b32 v14, 147968\n"
    "v_mov_b32 v15, 1\n"
    "s_waitcnt lgkmcnt(0)\n"
    "s_add_u32 s36, s36, 1\n"
    "s_waitcnt vmcnt(0)\n"
    "s_mov_b64 exec, 1\n"
    "ds_add_u32 v14, v15 offset:4\n"
    "s_mov_b64 exec, -1\n"
    "s_branch E_47\n"
    "R_46:\n"
    "s_sub_u32 s39, s39, 1\n"
    "s_cbranch_scc1 ABORT\n"
    "E_47:\n"
    "ds_read_b32 v5, v14 offset:4\n"
    "s_waitcnt lgkmcnt(0)\n"
    "v_cmp_ne_u32 vcc, 9, v5\n"
    "s_cbranch_vccnz R_46\n"
    "ds_read_b128 v[16:19], v3 offset:0\n"
    "ds_read_b64_tr_b16 v[52:53], v2 offset:0\n"
    "ds_read_b64_tr_b16 v[54:55], v2 offset:512\n"
    "ds_read_b128 v[20:23], v3 offset:64\n"
    "ds_read_b64_tr_b16 v[56:57], v2 offset:4096\n"
    "ds_read_b64_tr_b16 v[58:59], v2 offset:4608\n"
    "ds_read_b128 v[24:27], v3 offset:128\n"
    "ds_read_b64_tr_b16 v[60:61], v2 offset:8192\n"
    "ds_read_b64_tr_b16 v[62:63], v2 offset:8704\n"
    "ds_read_b128 v[28:31], v3 offset:192\n"
    "ds_read_b64_tr_b16 v[64:65], v2 offset:12288\n"
    "ds_read_b64_tr_b16 v[66:67], v2 offset:12800\n"
    "ds_read_b128 v[32:35], v3 offset:256\n"
    "ds_read_b64_tr_b16 v[68:69], v2 offset:16384\n"
    "ds_read_b64_tr_b16 v[70:71], v2 offset:16896\n"
    "s_waitcnt lgkmcnt(12)\n"
    "v_mfma_f32_16x16x32_bf16 v[8:11], v[16:19], v[52:55], 0\n"
    "ds_read_b128 v[36:39], v3 offset:320\n"
    "ds_read_b64_tr_b16 v[72:73], v2 offset:20480\n"
    "ds_read_b64_tr_b16 v[74:75], v2 offset:20992\n"
    "s_waitcnt lgkmcnt(12)\n"
    "v_mfma_f32_16x16x32_bf16 v[8:11], v[20:23], v[56:59], v[8:11]\n"
    "ds_read_b128 v[40:43], v3 offset:384\n"
    "ds_read_b64_tr_b16 v[76:77], v2 offset:24576\n"
    "ds_read_b64_tr_b16 v[78:79], v2 offset:25088\n"
    "s_waitcnt lgkmcnt(12)\n"
    "v_mfma_f32_16x16x32_bf16 v[8:11], v[24:27], v[60:63], v[8:11]\n"
    "ds_read_b128 v[44:47], v3 offset:448\n"
    "ds_read_b64_tr_b16 v[80:81], v2 offset:28672\n"
    "ds_read_b64_tr_b16 v[82:83], v2 offset:29184\n"
    "s_waitcnt lgkmcnt(12)\n"
    "v_mfma_f32_16x16x32_bf16 v[8:11], v[28:31], v[64:67], v[8:11]\n"
    "ds_read_b128 v[48:51], v3 offset:512\n"
    "ds_read_b64_tr_b16 v[84:85], v2 offset:32768\n"
    "ds_read_b64_tr_b16 v[86:87], v2 offset:33280\n"
    "s_waitcnt lgkmcnt(12)\n"
    "v_mfma_f32_16x16x32_bf16 v[8:11], v[32:35], v[68:71], v[8:11]\n"
    "s_waitcnt lgkmcnt(9)\n"
    "v_mfma_f32_16x16x32_bf16 v[8:11], v[36:39], v[72:75], v[8:11]\n"
    "s_waitcnt lgkmcnt(6)\n"
    "v_mfma_f32_16x16x32_bf16 v[8:11], v[40:43], v[76:79], v[8:11]\n"
    "s_waitcnt lgkmcnt(3)\n"
    "v_mfma_f32_16x16x32_bf16 v[8:11], v[44:47], v[80:83], v[8:11]\n"
    "s_waitcnt lgkmcnt(0)\n"
    "v_mfma_f32_16x16x32_bf16 v[8:11], v[48:51], v[84:87], v[8:11]\n"
    "s_mov_b64 exec, 0xffff\n"
    "s_nop 6\n"
    "v_mov_b32 v9, s36\n"
    "buffer_store_dwordx2 v[8:9], v4, s[20:23], s45 offen sc1\n"
    "s_mov_b64 exec, -1\n"
    "s_mov_b64 exec, 1\n"
    "ds_add_u32 v14, v15 offset:232\n"
    "s_mov_b64 exec, -1\n"
    "s_setprio 0\n"
    "s_branch MOED\n"
    "M8:\n"
    "v_mov_b32 v14, 147968\n"
    "v_mov_b32 v15, 1\n"
    "s_mov_b64 exec, 1\n"
    "ds_add_u32 v14, v15 offset:232\n"
    "s_mov_b64 exec, -1\n"
    "M9:\n"
    "s_waitcnt lgkmcnt(0)\n"
    "s_add_u32 s36, s36, 1\n"
    "ES1:\n"
    "ES0:\n"
    "s_sub_u32 s42, s2, 192\n"
    "s_lshr_b32 s42, s42, 3\n"
    "s_min_u32 s42, s42, 8\n"
    "s_lshr_b32 s44, s28, 7\n"
    "s_add_u32 s44, s44, s2\n"
    "s_sub_u32 s44, 3, s44\n"
    "s_and_b32 s44, s44, 7\n"
    "s_add_u32 s44, s44, 1\n"
    "s_add_u32 s44, s44, s42\n"
    "s_mul_i32 s45, s44, 57\n"
    "s_lshr_b32 s45, s45, 9\n"
    "s_mul_i32 s45, s45, 9\n"
    "s_sub_u32 s44, s44, s45\n"
    "s_and_b32 s58, s2, 7\n"
    "s_mul_i32 s58, s58, 9\n"
    "s_add_u32 s58, s58, s44\n"
    "v_bfe_u32 v12, v0, 3, 3\n"
    "v_bfe_u32 v13, v0, 5, 2\n"
    "v_xor_b32 v13, v13, v0\n"
    "v_mul_u32_u24 v12, 9216, v12\n"
    "v_and_b32 v13, 7, v13\n"
    "v_lshl_add_u32 v12, v13, 4, v12\n"
    "s_lshr_b32 s42, s37, 1\n"
    "s_mul_i32 s42, s42, 73728\n"
    "s_lshl_b32 s44, s58, 7\n"
    "s_add_u32 s42, s42, s44\n"
    "v_add_u32 v12, s42, v12\n"
    "v_xor_b32 v13, 64, v12\n"
    "v_add_u32 v13, 294912, v13\n"
    "s_bitcmp1_b32 s37, 0\n"
    "s_cselect_b64 s[56:57], s[30:31], s[28:29]\n"
    "s_waitcnt lgkmcnt(0)\n"
    "s_barrier\n"
    "s_cmpk_ge_u32 s37, 8\n"
    "s_cbranch_scc1 MOED\n"
    "ES1c:\n"
    "s_lshl_b32 m0, s37, 10\n"
    "ES1d:\n"
    "s_waitcnt vmcnt(4)\n"
    "global_load_lds_dwordx4 v12, s[56:57] nt\n"
    "s_add_u32 m0, m0, 8192\n"
    "v_add_u32 v12, 589824, v12\n"
    "s_waitcnt vmcnt(4)\n"
    "global_load_lds_dwordx4 v13, s[56:57] nt\n"
    "s_add_u32 m0, m0, 8192\n"
    "v_add_u32 v13, 589824, v13\n"
    "s_waitcnt vmcnt(4)\n"
    "global_load_lds_dwordx4 v12, s[56:57] nt\n"
    "s_add_u32 m0, m0, 8192\n"
    "v_add_u32 v12, 589824, v12\n"
    "s_waitcnt vmcnt(4)\n"
    "global_load_lds_dwordx4 v13, s[56:57] nt\n"
    "s_add_u32 m0, m0, 8192\n"
    "v_add_u32 v13, 589824, v13\n"
    "s_waitcnt vmcnt(4)\n"
    "global_load_lds_dwordx4 v12, s[56:57] nt\n"
    "s_add_u32 m0, m0, 8192\n"
    "v_add_u32 v12, 589824, v12\n"
    "s_waitcnt vmcnt(4)\n"
    "global_load_lds_dwordx4 v13, s[56:57] nt\n"
    "s_add_u32 m0, m0, 8192\n"
    "v_add_u32 v13, 589824, v13\n"
    "s_waitcnt vmcnt(4)\n"
    "global_load_lds_dwordx4 v12, s[56:57] nt\n"
    "s_add_u32 m0, m0, 8192\n"
    "v_add_u32 v12, 589824, v12\n"
    "s_waitcnt vmcnt(4)\n"
    "global_load_lds_dwordx4 v13, s[56:57] nt\n"
    "s_add_u32 m0, m0, 8192\n"
    "v_add_u32 v13, 589824, v13\n"
    "s_waitcnt vmcnt(4)\n"
    "global_load_lds_dwordx4 v12, s[56:57] nt\n"
    "s_add_u32 m0, m0, 8192\n"
    "v_add_u32 v12, 589824, v12\n"
    "s_waitcnt vmcnt(4)\n"
    "global_load_lds_dwordx4 v13, s[56:57] nt\n"
    "s_add_u32 m0, m0, 8192\n"
    "v_add_u32 v13, 589824, v13\n"
    "s_waitcnt vmcnt(4)\n"
    "global_load_lds_dwordx4 v12, s[56:57] nt\n"
    "s_add_u32 m0, m0, 8192\n"
    "v_add_u32 v12, 589824, v12\n"
    "s_waitcnt vmcnt(4)\n"
    "global_load_lds_dwordx4 v13, s[56:57] nt\n"
    "s_add_u32 m0, m0, 8192\n"
    "v_add_u32 v13, 589824, v13\n"
    "v_mov_b32 v14, 148096\n"
    "v_mov_b32 v15, 1\n"
    "v_bfe_u32 v3, v0, 4, 2\n"
    "s_add_u32 s63, s36, 1\n"
    "v_bfe_u32 v6, v0, 1, 1\n"
    "v_bfe_u32 v7, v0, 3, 1\n"
    "v_and_b32 v9, 1, v3\n"
    "v_lshl_add_u32 v8, v6, 1, v9\n"
    "v_lshl_add_u32 v8, v7, 2, v8\n"
    "v_xor_b32 v8, s37, v8\n"
    "v_lshlrev_b32 v4, 13, v7\n"
    "v_lshl_add_u32 v4, v6, 10, v4\n"
    "v_lshl_add_u32 v4, v9, 9, v4\n"
    "v_lshl_add_u32 v4, v8, 4, v4\n"
    "v_lshrrev_b32 v9, 1, v3\n"
    "v_lshl_add_u32 v4, v9, 11, v4\n"
    "v_bfe_u32 v9, v0, 2, 1\n"
    "v_lshl_add_u32 v4, v9, 7, v4\n"
    "v_and_b32 v9, 1, v0\n"
    "v_lshl_add_u32 v4, v9, 3, v4\n"
    "v_add_u32 v5, 0x10000, v4\n"
    "v_add_u32 v6, 0x20000, v4\n"
    "v_lshrrev_b32 v2, 3, v0\n"
    "v_and_b32 v8, 2, v2\n"
    "v_mul_u32_u24 v2, 1152, v2\n"
    "v_lshrrev_b32 v7, 4, v0\n"
    "v_and_b32 v7, 4, v7\n"
    "v_xor_b32 v8, v8, v7\n"
    "v_xor_b32 v8, v8, v0\n"
    "v_and_b32 v8, 7, v8\n"
    "v_lshl_add_u32 v2, v8, 4, v2\n"
    "s_mul_i32 s42, s58, 73728\n"
    "v_add_u32 v2, s42, v2\n"
    "s_cmpk_lt_u32 s2, 192\n"
    "s_cbranch_scc1 ES3\n"
    "s_branch E_49\n"
    "R_48:\n"
    "s_sleep 1\n"
    "s_sub_u32 s39, s39, 1\n"
    "s_cbranch_scc1 ABORT\n"
    "E_49:\n"
    "ds_read_b32 v10, v14 offset:104\n"
    "s_waitcnt lgkmcnt(0)\n"
    "v_cmp_ne_u32 vcc, 4, v10\n"
    "s_cbranch_vccnz R_48\n"
    "ES3:\n"
    "s_waitcnt vmcnt(4)\n"
    "global_load_lds_dwordx4 v12, s[56:57] nt\n"
    "s_add_u32 m0, m0, 8192\n"
    "v_add_u32 v12, 589824, v12\n"
    "s_waitcnt vmcnt(4)\n"
    "global_load_lds_dwordx4 v13, s[56:57] nt\n"
    "s_add_u32 m0, m0, 8192\n"
    "v_add_u32 v13, 589824, v13\n"
    "s_waitcnt vmcnt(4)\n"
    "s_mov_b64 exec, 1\n"
    "ds_add_u32 v14, v15 offset:76\n"
    "s_mov_b64 exec, -1\n"
    "s_waitcnt vmcnt(4)\n"
    "global_load_lds_dwordx4 v12, s[56:57] nt\n"
    "s_add_u32 m0, m0, 8192\n"
    "v_add_u32 v12, 589824, v12\n"
    "s_branch E_51\n"
    "R_50:\n"
    "s_sub_u32 s39, s39, 1\n"
    "s_cbranch_scc1 ABORT\n"
    "E_51:\n"
    "ds_read_b32 v10, v14 offset:76\n"
    "s_waitcnt lgkmcnt(0)\n"
    "v_cmp_ne_u32 vcc, 8, v10\n"
    "s_cbranch_vccnz R_50\n"
    "ds_read_b64_tr_b16 v[56:57], v4 offset:0\n"
    "ds_read_b64_tr_b16 v[58:59], v4 offset:256\n"
    "ds_read_b64_tr_b16 v[60:61], v4 offset:4096\n"
    "ds_read_b64_tr_b16 v[62:63], v4 offset:4352\n"
    "ds_read_b64_tr_b16 v[64:65], v4 offset:16384\n"
    "ds_read_b64_tr_b16 v[66:67], v4 offset:16640\n"
    "ds_read_b64_tr_b16 v[68:69], v4 offset:20480\n"
    "ds_read_b64_tr_b16 v[70:71], v4 offset:20736\n"
    "ds_read_b64_tr_b16 v[72:73], v4 offset:32768\n"
    "ds_read_b64_tr_b16 v[74:75], v4 offset:33024\n"
    "ds_read_b64_tr_b16 v[76:77], v4 offset:36864\n"
    "ds_read_b64_tr_b16 v[78:79], v4 offset:37120\n"
    "ds_read_b64_tr_b16 v[80:81], v4 offset:49152\n"
    "ds_read_b64_tr_b16 v[82:83], v4 offset:49408\n"
    "ds_read_b64_tr_b16 v[84:85], v4 offset:53248\n"
    "ds_read_b64_tr_b16 v[86:87], v4 offset:53504\n"
    "ds_read_b64_tr_b16 v[88:89], v5 offset:0\n"
    "ds_read_b64_tr_b16 v[90:91], v5 offset:256\n"
    "ds_read_b64_tr_b16 v[92:93], v5 offset:4096\n"
    "ds_read_b64_tr_b16 v[94:95], v5 offset:4352\n"
    "s_mov_b64 exec, 1\n"
    "ds_add_u32 v14, v15 offset:80\n"
    "s_mov_b64 exec, -1\n"
    "s_waitcnt vmcnt(4)\n"
    "global_load_lds_dwordx4 v13, s[56:57] nt\n"
    "s_add_u32 m0, m0, 8192\n"
    "v_add_u32 v13, 589824, v13\n"
    "s_waitcnt vmcnt(4)\n"
    "global_load_lds_dwordx4 v12, s[56:57] nt\n"
    "s_add_u32 m0, m0, 8192\n"
    "v_add_u32 v12, 589824, v12\n"
    "s_waitcnt vmcnt(4)\n"
    "global_load_lds_dwordx4 v13, s[56:57] nt\n"
    "s_add_u32 m0, m0, 8192\n"
    "v_add_u32 v13, 589824, v13\n"
    "s_branch E_53\n"
    "R_52:\n"
    "s_sleep 1\n"
    "s_sub_u32 s39, s39, 1\n"
    "s_cbranch_scc1 ABORT\n"
    "E_53:\n"
    "ds_read_b32 v10, v14 offset:80\n"
    "s_waitcnt lgkmcnt(0)\n"
    "v_cmp_ne_u32 vcc, 8, v10\n"
    "s_cbranch_vccnz R_52\n"
    "s_mul_i32 m0, s37, 9216\n"
    "s_waitcnt vmcnt(4)\n"
    "global_load_lds_dwordx4 v2, s[32:33] offset:0 nt\n"
    "s_add_u32 m0, m0, 896\n"
    "s_waitcnt vmcnt(4)\n"
    "global_load_lds_dwordx4 v2, s[32:33] offset:128 nt\n"
    "s_add_u32 m0, m0, 896\n"
    "s_waitcnt vmcnt(4)\n"
    "global_load_lds_dwordx4 v2, s[32:33] offset:256 nt\n"
    "s_add_u32 m0, m0, 896\n"
    "s_waitcnt vmcnt(4)\n"
    "global_load_lds_dwordx4 v2, s[32:33] offset:384 nt\n"
    "s_add_u32 m0, m0, 896\n"
    "s_waitcnt vmcnt(4)\n"
    "s_mov_b64 exec, 1\n"
    "ds_add_u32 v14, v15 offset:84\n"
    "s_mov_b64 exec, -1\n"
    "s_waitcnt vmcnt(4)\n"
    "global_load_lds_dwordx4 v2, s[32:33] offset:512 nt\n"
    "s_add_u32 m0, m0, 896\n"
    "s_branch E_55\n"
    "R_54:\n"
    "s_sub_u32 s39, s39, 1\n"
    "s_cbranch_scc1 ABORT\n"
    "E_55:\n"
    "ds_read_b32 v10, v14 offset:84\n"
    "s_waitcnt lgkmcnt(0)\n"
    "v_cmp_ne_u32 vcc, 8, v10\n"
    "s_cbranch_vccnz R_54\n"
    "ds_read_b64_tr_b16 v[96:97], v5 offset:16384\n"
    "ds_read_b64_tr_b16 v[98:99], v5 offset:16640\n"
    "ds_read_b64_tr_b16 v[100:101], v5 offset:20480\n"
    "ds_read_b64_tr_b16 v[102:103], v5 offset:20736\n"
    "ds_read_b64_tr_b16 v[104:105], v5 offset:32768\n"
    "ds_read_b64_tr_b16 v[106:107], v5 offset:33024\n"
    "ds_read_b64_tr_b16 v[108:109], v5 offset:36864\n"
    "ds_read_b64_tr_b16 v[110:111], v5 offset:37120\n"
    "ds_read_b64_tr_b16 v[112:113], v5 offset:49152\n"
    "ds_read_b64_tr_b16 v[114:115], v5 offset:49408\n"
    "ds_read_b64_tr_b16 v[116:117], v5 offset:53248\n"
    "ds_read_b64_tr_b16 v[118:119], v5 offset:53504\n"
    "ds_read_b64_tr_b16 v[120:121], v6 offset:0\n"
    "ds_read_b64_tr_b16 v[122:123], v6 offset:256\n"
    "ds_read_b64_tr_b16 v[124:125], v6 offset:4096\n"
    "ds_read_b64_tr_b16 v[126:127], v6 offset:4352\n"
    "s_mov_b64 exec, 1\n"
    "ds_add_u32 v14, v15 offset:88\n"
    "s_mov_b64 exec, -1\n"
    "s_waitcnt vmcnt(4)\n"
    "global_load_lds_dwordx4 v2, s[32:33] offset:640 nt\n"
    "s_add_u32 m0, m0, 896\n"
    "s_waitcnt vmcnt(4)\n"
    "global_load_lds_dwordx4 v2, s[32:33] offset:768 nt\n"
    "s_add_u32 m0, m0, 896\n"
    "s_waitcnt vmcnt(4)\n"
    "global_load_lds_dwordx4 v2, s[32:33] offset:896 nt\n"
    "s_add_u32 m0, m0, 896\n"
    "s_waitcnt vmcnt(4)\n"
    "global_load_lds_dwordx4 v2, s[32:33] offset:1024 nt\n"
    "s_add_u32 m0, m0, 896\n"
    "CHE_0:\n"
    "s_cbranch_execz CHE_1\n"
    "GUT:\n"
    "v_and_b32 v7, 7, v0\n"
    "v_and_b32 v8, 1, v7\n"
    "v_mul_u32_u24 v4, 1344, v7\n"
    "v_lshlrev_b32 v8, 5, v8\n"
    "v_sub_u32 v4, v4, v8\n"
    "v_add_u32 v4, 73728, v4\n"
    "v_and_b32 v9, 8, v0\n"
    "v_lshlrev_b32 v5, 3, v9\n"
    "v_lshl_add_u32 v5, v3, 4, v5\n"
    "v_add_u32 v5, v5, v4\n"
    "v_and_b32 v6, 1, v3\n"
    "v_lshlrev_b32 v6, 2, v6\n"
    "v_and_b32 v8, 2, v3\n"
    "v_lshrrev_b32 v9, 3, v9\n"
    "v_add3_u32 v6, v6, v8, v9\n"
    "v_mul_u32_u24 v6, 144, v6\n"
    "v_lshl_add_u32 v6, v7, 1, v6\n"
    "s_lshl_b32 s42, s37, 4\n"
    "s_add_u32 s42, s42, 150016\n"
    "v_add_u32 v6, s42, v6\n"
    "v_mul_u32_u24 v8, 0xaaaa, v9\n"
    "v_add_u32 v8, 0x4444, v8\n"
    "v_mov_b32 v16, 0\n"
    "v_mov_b32 v17, 0\n"
    "v_mov_b32 v18, 0\n"
    "v_mov_b32 v19, 0\n"
    "s_setprio 2\n"
    "s_waitcnt vmcnt(0)\n"
    "s_mov_b64 exec, 1\n"
    "ds_add_u32 v14, v15 offset:108\n"
    "s_mov_b64 exec, -1\n"
    "s_waitcnt lgkmcnt(0)\n"
    "s_barrier\n"
    "ds_read_b128 v[20:23], v5 offset:0\n"
    "ds_read_b128 v[24:27], v5 offset:128\n"
    "ds_read_b128 v[28:31], v5 offset:256\n"
    "ds_read_b128 v[32:35], v5 offset:384\n"
    "ds_read_b128 v[36:39], v5 offset:512\n"
    "ds_read_b128 v[40:43], v5 offset:640\n"
    "ds_read_b128 v[44:47], v5 offset:768\n"
    "ds_read_b128 v[48:51], v5 offset:896\n"
    "ds_read_b128 v[52:55], v5 offset:1024\n"
    "ds_read_b32 v10, v14 offset:112\n"
    "s_waitcnt lgkmcnt(9)\n"
    "v_smfmac_f32_16x16x64_bf16 v[16:19], v[20:23], v[56:63], v8\n"
    "s_waitcnt lgkmcnt(8)\n"
    "v_smfmac_f32_16x16x64_bf16 v[16:19], v[24:27], v[64:71], v8\n"
    "s_waitcnt lgkmcnt(7)\n"
    "v_smfmac_f32_16x16x64_bf16 v[16:19], v[28:31], v[72:79], v8\n"
    "s_waitcnt lgkmcnt(6)\n"
    "v_smfmac_f32_16x16x64_bf16 v[16:19], v[32:35], v[80:87], v8\n"
    "s_waitcnt lgkmcnt(5)\n"
    "v_smfmac_f32_16x16x64_bf16 v[16:19], v[36:39], v[88:95], v8\n"
    "s_waitcnt lgkmcnt(4)\n"
    "v_smfmac_f32_16x16x64_bf16 v[16:19], v[40:43], v[96:103], v8\n"
    "s_waitcnt lgkmcnt(3)\n"
    "v_smfmac_f32_16x16x64_bf16 v[16:19], v[44:47], v[104:111], v8\n"
    "s_waitcnt lgkmcnt(2)\n"
    "v_smfmac_f32_16x16x64_bf16 v[16:19], v[48:51], v[112:119], v8\n"
    "s_waitcnt lgkmcnt(1)\n"
    "v_smfmac_f32_16x16x64_bf16 v[16:19], v[52:55], v[120:127], v8\n"
    "s_nop 7\n"
    "v_permlane32_swap_b32 v16, v18\n"
    "v_permlane32_swap_b32 v17, v19\n"
    "v_add_f32 v12, v16, v18\n"
    "v_add_f32 v14, v16, v18\n"
    "v_add_f32 v13, v17, v19\n"
    "s_nop 1\n"
    "v_mov_b32_dpp v12, v13 row_ror:8 row_mask:0xf bank_mask:0xc\n"
    "v_mov_b32_dpp v13, v14 row_ror:8 row_mask:0xf bank_mask:0x3\n"
    "v_cvt_pk_bf16_f32 v12, 0, v12\n"
    "v_mul_f32 v14, 0xbfb8aa3b, v12\n"
    "v_exp_f32 v14, v14\n"
    "v_cvt_pk_bf16_f32 v13, 0, v13\n"
    "v_add_f32 v14, 1.0, v14\n"
    "v_rcp_f32 v14, v14\n"
    "s_nop 0\n"
    "v_mul_f32 v12, v12, v14\n"
    "v_cvt_pk_bf16_f32 v12, 0, v12\n"
    "v_mul_f32 v12, v12, v13\n"
    "v_cvt_pk_bf16_f32 v12, v12, v12\n"
    "ds_write_b16 v6, v12\n"
    "s_waitcnt lgkmcnt(0)\n"
    "s_barrier\n"
    "s_cmpk_lt_u32 s2, 192\n"
    "s_cbranch_scc1 END\n"
    "v_readfirstlane_b32 s96, v10\n"
    "v_and_b32 v1, 63, v0\n"
    "v_and_b32 v2, 15, v0\n"
    "s_movk_i32 s61, 0x7ff\n"
    "s_setprio 0\n"
    "s_cmpk_eq_u32 s37, 7\n"
    "s_cbranch_scc1 FINP\n"
    "s_cmpk_lg_u32 s2, 192\n"
    "s_cbranch_scc1 END\n"
    "s_cmpk_lg_u32 s37, 0\n"
    "s_cbranch_scc1 END\n"
    "v_mov_b32 v1, s63\n"
    "s_mov_b64 exec, 1\n"
    "buffer_store_dword v1, off, s[20:23], 0 sc1\n"
    "s_mov_b64 exec, -1\n"
    "s_branch END\n"
    "CHE_1:\n"
    "s_cbranch_execz CHE_2\n"
    "MOED:\n"
    "v_and_b32 v1, 63, v0\n"
    "v_and_b32 v2, 15, v0\n"
    "v_bfe_u32 v3, v0, 4, 2\n"
    "v_mov_b32 v14, 148096\n"
    "v_mov_b32 v15, 1\n"
    "v_bfe_u32 v6, v0, 2, 2\n"
    "v_bfe_u32 v7, v0, 1, 1\n"
    "v_and_b32 v8, 2, v6\n"
    "v_or_b32 v7, v7, v8\n"
    "v_and_b32 v8, 1, v3\n"
    "v_lshl_add_u32 v7, v8, 2, v7\n"
    "v_mul_u32_u24 v8, 9216, v3\n"
    "v_lshl_add_u32 v8, v6, 7, v8\n"
    "v_lshl_add_u32 v8, v7, 4, v8\n"
    "v_and_b32 v7, 1, v0\n"
    "v_lshl_add_u32 v8, v7, 3, v8\n"
    "s_mul_i32 s42, s37, 6\n"
    "s_sub_u32 s42, s42, 48\n"
    "s_and_b32 s43, s42, 3\n"
    "s_lshl_b32 s43, s43, 5\n"
    "s_lshr_b32 s44, s42, 2\n"
    "s_lshl_b32 s44, s44, 10\n"
    "v_xor_b32 v104, s43, v8\n"
    "v_add_u32 v104, s44, v104\n"
    "s_add_u32 s42, s42, 1\n"
    "s_and_b32 s43, s42, 3\n"
    "s_lshl_b32 s43, s43, 5\n"
    "s_lshr_b32 s44, s42, 2\n"
    "s_lshl_b32 s44, s44, 10\n"
    "v_xor_b32 v105, s43, v8\n"
    "v_add_u32 v105, s44, v105\n"
    "s_add_u32 s42, s42, 1\n"
    "s_and_b32 s43, s42, 3\n"
    "s_lshl_b32 s43, s43, 5\n"
    "s_lshr_b32 s44, s42, 2\n"
    "s_lshl_b32 s44, s44, 10\n"
    "v_xor_b32 v106, s43, v8\n"
    "v_add_u32 v106, s44, v106\n"
    "s_add_u32 s42, s42, 1\n"
    "s_and_b32 s43, s42, 3\n"
    "s_lshl_b32 s43, s43, 5\n"
    "s_lshr_b32 s44, s42, 2\n"
    "s_lshl_b32 s44, s44, 10\n"
    "v_xor_b32 v107, s43, v8\n"
    "v_add_u32 v107, s44, v107\n"
    "s_add_u32 s42, s42, 1\n"
    "s_and_b32 s43, s42, 3\n"
    "s_lshl_b32 s43, s43, 5\n"
    "s_lshr_b32 s44, s42, 2\n"
    "s_lshl_b32 s44, s44, 10\n"
    "v_xor_b32 v108, s43, v8\n"
    "v_add_u32 v108, s44, v108\n"
    "s_add_u32 s42, s42, 1\n"
    "s_and_b32 s43, s42, 3\n"
    "s_lshl_b32 s43, s43, 5\n"
    "s_lshr_b32 s44, s42, 2\n"
    "s_lshl_b32 s44, s44, 10\n"
    "v_xor_b32 v109, s43, v8\n"
    "v_add_u32 v109, s44, v109\n"
    "s_add_u32 s42, s42, 1\n"
    "v_and_b32 v7, 7, v0\n"
    "v_mul_u32_u24 v7, 144, v7\n"
    "v_lshl_add_u32 v7, v3, 4, v7\n"
    "v_add_u32 v7, 150016, v7\n"
    "v_mov_b32 v8, 148224\n"
    "v_cmp_gt_u32 vcc, 8, v2\n"
    "s_nop 1\n"
    "v_cndmask_b32 v4, v8, v7, vcc\n"
    "v_cndmask_b32 v5, v7, v8, vcc\n"
    "v_and_b32 v6, 1, v3\n"
    "v_mul_u32_u24 v6, 6144, v6\n"
    "v_lshl_add_u32 v6, v2, 4, v6\n"
    "v_lshrrev_b32 v7, 1, v3\n"
    "v_lshl_add_u32 v6, v7, 8, v6\n"
    "s_mul_i32 s42, s58, 12416\n"
    "s_lshl_b32 s43, s37, 9\n"
    "s_add_u32 s42, s42, s43\n"
    "s_add_u32 s42, s42, 6443008\n"
    "v_add_u32 v6, s42, v6\n"
    "s_lshl_b32 s62, s37, 2\n"
    "s_add_u32 s62, s62, 151616\n"
    "v_mov_b32 v7, s62\n"
    "CHE_2:\n"
    "s_cbranch_execz CHE_3\n"
    "s_add_u32 s62, s62, 28\n"
    "v_lshlrev_b32 v8, 4, v1\n"
    "v_and_b32 v9, 31, v1\n"
    "v_add_u32 v9, 64, v9\n"
    "s_mov_b32 s42, 73728\n"
    "v_mad_u32_u24 v11, v1, 12, s42\n"
    "v_mad_u32_u24 v12, v9, 12, s42\n"
    "v_subrev_u32 v54, 320, v7\n"
    "v_lshlrev_b32 v55, 2, v1\n"
    "v_add_u32 v55, 151328, v55\n"
    "v_lshlrev_b32 v9, 4, v9\n"
    "s_mov_b32 s59, 7341056\n"
    "s_movk_i32 s60, 1536\n"
    "s_movk_i32 s61, 0x7ff\n"
    "s_add_u32 s63, s36, 1\n"
    "s_cmpk_ge_u32 s37, 14\n"
    "s_cselect_b32 s41, 99, 8\n"
    "s_cmpk_ge_u32 s37, 12\n"
    "s_cbranch_scc1 TOPK\n"
    "s_cmpk_lg_u32 s37, 10\n"
    "s_cbranch_scc1 CHE9\n"
    "s_mov_b64 exec, 0\n"
    "CHE:\n"
    "s_branch CHE_0\n"
    "CHE9:\n"
    "s_mov_b64 exec, -1\n"
    "s_cmpk_lg_u32 s37, 11\n"
    "s_cbranch_scc1 MOE2\n"
    "s_memrealtime s[96:97]\n"
    "s_waitcnt lgkmcnt(0)\n"
    "s_cmpk_lt_u32 s2, 192\n"
    "s_cbranch_scc1 HNT1\n"
    "U_56:\n"
    "s_sleep 8\n"
    "s_memrealtime s[98:99]\n"
    "s_waitcnt lgkmcnt(0)\n"
    "s_sub_u32 s98, s98, s96\n"
    "s_cmpk_lt_u32 s98, 400\n"
    "s_cbranch_scc1 U_56\n"
    "HNT1:\n"
    "v_mad_u32_u24 v18, v1, 3, s58\n"
    "v_subrev_u32 v17, 192, v18\n"
    "v_min_u32 v18, v18, v17\n"
    "v_lshlrev_b32 v18, 7, v18\n"
    "s_mov_b32 s42, 94208\n"
    "s_branch E_58\n"
    "R_57:\n"
    "s_sleep 6\n"
    "s_sub_u32 s39, s39, 1\n"
    "s_cbranch_scc1 ABORT\n"
    "E_58:\n"
    "buffer_load_dwordx2 v[16:17], v18, s[20:23], s42 offen sc1\n"
    "s_waitcnt vmcnt(0)\n"
    "v_cmp_eq_u32 vcc, s36, v17\n"
    "s_bcnt1_i32_b64 s43, vcc\n"
    "s_cmp_lt_u32 s43, 56\n"
    "s_cbranch_scc1 R_57\n"
    "v_subrev_u32 v16, s96, v16\n"
    "v_mov_b32 v17, -1000\n"
    "v_cndmask_b32 v16, v17, v16, vcc\n"
    "s_nop 1\n"
    "v_max_i32_dpp v16, v16, v16 row_ror:8 row_mask:0xf bank_mask:0xf\n"
    "s_nop 1\n"
    "v_max_i32_dpp v16, v16, v16 row_ror:4 row_mask:0xf bank_mask:0xf\n"
    "s_nop 1\n"
    "v_max_i32_dpp v16, v16, v16 row_ror:2 row_mask:0xf bank_mask:0xf\n"
    "s_nop 1\n"
    "v_max_i32_dpp v16, v16, v16 row_ror:1 row_mask:0xf bank_mask:0xf\n"
    "s_nop 1\n"
    "v_max_i32_dpp v16, v16, v16 row_bcast:15 row_mask:0xa bank_mask:0xf\n"
    "s_nop 1\n"
    "v_max_i32_dpp v16, v16, v16 row_bcast:31 row_mask:0xc bank_mask:0xf\n"
    "v_add_u32 v16, s96, v16\n"
    "v_mov_b32 v17, 1\n"
    "s_mov_b32 exec_lo, 0\n"
    "s_brev_b32 exec_hi, 1\n"
    "ds_write_b64 v14, v[16:17] offset:112\n"
    "s_mov_b64 exec, -1\n"
    "MOE2:\n"
    "s_sleep 4\n"
    "s_sub_u32 s39, s39, 1\n"
    "s_cbranch_scc1 ABORT\n"
    "ds_read_b32 v10, v14 offset:72\n"
    "ds_read_b32 v13, v14 offset:108\n"
    "s_waitcnt lgkmcnt(0)\n"
    "v_readfirstlane_b32 s42, v10\n"
    "v_readfirstlane_b32 s43, v13\n"
    "s_cmp_lg_u32 s42, 0\n"
    "s_cbranch_scc1 MOE2a\n"
    "s_cmp_eq_u32 s43, s41\n"
    "s_cbranch_scc1 DNRD\n"
    "s_branch MOE2\n"
    "CHE_3:\n"
    "s_cbranch_execz CHE_4\n"
    "MOE2a:\n"
    "s_setprio 2\n"
    "s_branch MOE3\n"
    "DNRD:\n"
    "ds_read_b64_tr_b16 v[56:57], v104 offset:0\n"
    "ds_read_b64_tr_b16 v[58:59], v104 offset:512\n"
    "ds_read_b64_tr_b16 v[60:61], v105 offset:0\n"
    "ds_read_b64_tr_b16 v[62:63], v105 offset:512\n"
    "ds_read_b64_tr_b16 v[64:65], v106 offset:0\n"
    "ds_read_b64_tr_b16 v[66:67], v106 offset:512\n"
    "ds_read_b64_tr_b16 v[68:69], v107 offset:0\n"
    "ds_read_b64_tr_b16 v[70:71], v107 offset:512\n"
    "ds_read_b64_tr_b16 v[72:73], v108 offset:0\n"
    "ds_read_b64_tr_b16 v[74:75], v108 offset:512\n"
    "ds_read_b64_tr_b16 v[76:77], v109 offset:0\n"
    "ds_read_b64_tr_b16 v[78:79], v109 offset:512\n"
    "ds_read_b64_tr_b16 v[80:81], v104 offset:36864\n"
    "ds_read_b64_tr_b16 v[82:83], v104 offset:37376\n"
    "ds_read_b64_tr_b16 v[84:85], v105 offset:36864\n"
    "ds_read_b64_tr_b16 v[86:87], v105 offset:37376\n"
    "ds_read_b64_tr_b16 v[88:89], v106 offset:36864\n"
    "ds_read_b64_tr_b16 v[90:91], v106 offset:37376\n"
    "ds_read_b64_tr_b16 v[92:93], v107 offset:36864\n"
    "ds_read_b64_tr_b16 v[94:95], v107 offset:37376\n"
    "ds_read_b64_tr_b16 v[96:97], v108 offset:36864\n"
    "ds_read_b64_tr_b16 v[98:99], v108 offset:37376\n"
    "ds_read_b64_tr_b16 v[100:101], v109 offset:36864\n"
    "ds_read_b64_tr_b16 v[102:103], v109 offset:37376\n"
    "s_cmp_eq_u32 s41, 0\n"
    "s_mov_b32 s41, 99\n"
    "s_cbranch_scc1 DOWN1\n"
    "s_cmpk_ge_u32 s37, 12\n"
    "s_cbranch_scc1 TOPK2\n"
    "s_branch MOE2\n"
    "TOPK:\n"
    "s_setprio 3\n"
    "s_mul_i32 s42, s37, 9\n"
    "s_add_u32 s42, s42, s58\n"
    "s_mul_i32 s43, s42, 1821\n"
    "s_lshr_b32 s43, s43, 16\n"
    "s_mul_i32 s43, s43, 36\n"
    "s_sub_u32 s42, s42, s43\n"
    "s_lshl_b32 s42, s42, 7\n"
    "s_sub_u32 s42, 94084, s42\n"
    "v_lshlrev_b32 v13, 4, v0\n"
    "s_mov_b32 s44, 65536\n"
    "s_mov_b32 s45, 69632\n"
    "s_mov_b32 s46, 73728\n"
    "s_mov_b32 s47, 77824\n"
    "v_lshl_add_u32 v16, v2, 2, v14\n"
    "v_lshl_add_u32 v17, v3, 4, v14\n"
    "s_brev_b32 s48, 1\n"
    "v_mov_b32 v18, 4\n"
    "v_lshlrev_b32 v19, 2, v1\n"
    "v_add_u32 v19, 151648, v19\n"
    "v_mov_b32 v21, 0\n"
    "ds_write_b32 v55, v21\n"
    "v_mov_b32 v20, 37912\n"
    "s_mov_b64 exec, 1\n"
    "ds_add_u32 v14, v20 offset:64\n"
    "s_mov_b64 exec, -1\n"
    "s_branch E_60\n"
    "CHE_4:\n"
    "s_cbranch_execz CHE_5\n"
    "R_59:\n"
    "s_sleep 4\n"
    "s_sub_u32 s39, s39, 1\n"
    "s_cbranch_scc1 ABORT\n"
    "E_60:\n"
    "ds_read_b64 v[20:21], v14 offset:112\n"
    "s_waitcnt lgkmcnt(0)\n"
    "v_cmp_ne_u32 vcc, 1, v21\n"
    "s_cbranch_vccnz R_59\n"
    "v_readfirstlane_b32 s96, v20\n"
    "U_61:\n"
    "s_sleep 2\n"
    "s_memrealtime s[98:99]\n"
    "s_waitcnt lgkmcnt(0)\n"
    "s_sub_u32 s98, s98, s96\n"
    "s_cmpk_lt_u32 s98, 183\n"
    "s_cbranch_scc1 U_61\n"
    "ds_read_b32 v20, v14 offset:108\n"
    "s_waitcnt lgkmcnt(0)\n"
    "v_readfirstlane_b32 s43, v20\n"
    "s_cmp_eq_u32 s43, s41\n"
    "s_cbranch_scc1 DNRD\n"
    "TOPK2:\n"
    "U_62:\n"
    "s_memrealtime s[98:99]\n"
    "s_waitcnt lgkmcnt(0)\n"
    "s_sub_u32 s98, s98, s96\n"
    "s_cmpk_lt_u32 s98, 208\n"
    "s_cbranch_scc1 U_62\n"
    "buffer_load_dwordx4 v[24:27], v13, s[20:23], s44 offen sc1\n"
    "buffer_load_dwordx4 v[28:31], v13, s[20:23], s45 offen sc1\n"
    "buffer_load_dwordx4 v[32:35], v13, s[20:23], s46 offen sc1\n"
    "buffer_load_dwordx4 v[36:39], v13, s[20:23], s47 offen sc1\n"
    "buffer_load_dwordx2 v[40:41], off, s[20:23], 0 offset:2048 sc1\n"
    "s_branch E_64\n"
    "R_63:\n"
    "s_sub_u32 s39, s39, 1\n"
    "s_cbranch_scc1 ABORT\n"
    "s_wqm_b64 exec, vcc\n"
    "buffer_load_dwordx4 v[24:27], v13, s[20:23], s44 offen sc1\n"
    "buffer_load_dwordx4 v[28:31], v13, s[20:23], s45 offen sc1\n"
    "buffer_load_dwordx4 v[32:35], v13, s[20:23], s46 offen sc1\n"
    "buffer_load_dwordx4 v[36:39], v13, s[20:23], s47 offen sc1\n"
    "buffer_load_dwordx2 v[40:41], off, s[20:23], 0 offset:2048 sc1\n"
    "s_mov_b64 exec, -1\n"
    "E_64:\n"
    "s_waitcnt vmcnt(3)\n"
    "v_bitop3_b32 v20, v25, v27, s36 bitop3:0x7e\n"
    "v_bitop3_b32 v21, v29, v31, s36 bitop3:0x7e\n"
    "v_max3_u32 v42, v24, v26, v28\n"
    "s_waitcnt vmcnt(2)\n"
    "v_bitop3_b32 v22, v33, v35, s36 bitop3:0x7e\n"
    "v_max3_u32 v42, v42, v30, v32\n"
    "v_or3_b32 v20, v20, v21, v22\n"
    "s_waitcnt vmcnt(0)\n"
    "v_bitop3_b32 v21, v37, v39, s36 bitop3:0x7e\n"
    "v_xor_b32 v22, s36, v41\n"
    "v_max3_u32 v42, v42, v34, v36\n"
    "v_or3_b32 v20, v20, v21, v22\n"
    "v_max_u32 v42, v42, v38\n"
    "v_cmp_ne_u32 vcc, 0, v20\n"
    "s_cbranch_vccnz R_63\n"
    "ds_max_u32 v16, v42\n"
    "s_mov_b64 exec, 1\n"
    "ds_add_u32 v14, v15 offset:72\n"
    "s_mov_b64 exec, -1\n"
    "ds_read_b32 v10, v14 offset:72\n"
    "ds_read_b32 v20, v16\n"
    "ds_read_b128 v[46:49], v17\n"
    "v_max3_u32 v45, v24, v26, v28\n"
    "v_med3_u32 v43, v24, v26, v28\n"
    "v_min3_u32 v44, v24, v26, v28\n"
    "v_med3_u32 v44, v43, v44, v30\n"
    "v_med3_u32 v43, v45, v43, v30\n"
    "v_max_u32 v45, v45, v30\n"
    "v_med3_u32 v44, v43, v44, v32\n"
    "v_med3_u32 v43, v45, v43, v32\n"
    "v_max_u32 v45, v45, v32\n"
    "s_branch E_66\n"
    "CHE_5:\n"
    "s_cbranch_execz CHE_6\n"
    "R_65:\n"
    "s_sub_u32 s39, s39, 1\n"
    "s_cbranch_scc1 ABORT\n"
    "ds_read_b32 v10, v14 offset:72\n"
    "ds_read_b32 v20, v16\n"
    "ds_read_b128 v[46:49], v17\n"
    "E_66:\n"
    "s_waitcnt lgkmcnt(0)\n"
    "v_cmp_ne_u32 vcc, 4, v10\n"
    "v_cmp_gt_u32 s[50:51], v46, v20\n"
    "v_cmp_gt_u32 s[52:53], v47, v20\n"
    "v_cmp_gt_u32 s[54:55], v48, v20\n"
    "v_cmp_gt_u32 s[56:57], v49, v20\n"
    "v_cndmask_b32 v21, 0, 1, s[50:51]\n"
    "s_cbranch_vccnz R_65\n"
    "v_addc_co_u32 v21, vcc, 0, v21, s[52:53]\n"
    "v_addc_co_u32 v21, vcc, 0, v21, s[54:55]\n"
    "v_addc_co_u32 v21, vcc, 0, v21, s[56:57]\n"
    "v_mov_b32 v22, v21\n"
    "v_ashrrev_i32 v23, 31, v20\n"
    "v_bitop3_b32 v23, v23, v20, s48 bitop3:0x63\n"
    "v_permlane16_swap_b32 v21, v22\n"
    "v_add_u32 v21, v21, v22\n"
    "v_mov_b32 v22, v21\n"
    "v_sub_f32 v23, v23, v40\n"
    "v_ashrrev_i32 v46, 31, v23\n"
    "v_permlane32_swap_b32 v21, v22\n"
    "v_add_u32 v21, v21, v22\n"
    "v_cmp_eq_u32 vcc, 7, v21\n"
    "v_bitop3_b32 v23, v46, v23, s48 bitop3:0x36\n"
    "v_min_u32 v23, v23, v20\n"
    "v_and_b32 v23, 0xfffff800, v23\n"
    "v_med3_u32 v44, v43, v44, v34\n"
    "v_med3_u32 v43, v45, v43, v34\n"
    "s_ff1_i32_b64 s49, vcc\n"
    "v_readlane_b32 s49, v23, s49\n"
    "v_max_u32 v45, v45, v34\n"
    "v_med3_u32 v44, v43, v44, v36\n"
    "v_cmpx_le_u32 vcc, s49, v42\n"
    "v_med3_u32 v43, v45, v43, v36\n"
    "v_max_u32 v45, v45, v36\n"
    "v_med3_u32 v44, v43, v44, v38\n"
    "v_med3_u32 v43, v45, v43, v38\n"
    "v_cmp_le_u32 s[50:51], s49, v43\n"
    "v_cmp_le_u32 s[52:53], s49, v44\n"
    "s_nop 0\n"
    "v_cndmask_b32 v21, 4, 8, s[50:51]\n"
    "s_cmp_lg_u64 s[52:53], 0\n"
    "s_cbranch_scc1 TOPS\n"
    "ds_add_rtn_u32 v21, v14, v21 offset:64\n"
    "s_waitcnt lgkmcnt(0)\n"
    "ds_write_b32 v21, v42\n"
    "s_mov_b64 exec, s[50:51]\n"
    "ds_write_b32 v21, v43 offset:4\n"
    "TOPC:\n"
    "s_mov_b64 exec, 1\n"
    "ds_add_u32 v14, v15 offset:68\n"
    "s_mov_b64 exec, -1\n"
    "s_cmpk_eq_u32 s37, 15\n"
    "s_cbranch_scc1 MOE15\n"
    "MOE3:\n"
    "ds_read_b64 v[20:21], v14 offset:64\n"
    "ds_read2_b32 v[22:23], v7 offset1:7\n"
    "ds_read_b32 v48, v7 offset:56\n"
    "s_sub_u32 s39, s39, 1\n"
    "s_cbranch_scc1 ABORT\n"
    "s_waitcnt lgkmcnt(0)\n"
    "v_cmp_ne_u32 vcc, 4, v21\n"
    "v_readfirstlane_b32 s42, v20\n"
    "v_bitop3_b32 v22, v22, s61, v22 bitop3:0x0c\n"
    "s_cbranch_vccnz MOE3\n"
    "s_cmp_gt_u32 s42, 151904\n"
    "s_cbranch_scc1 MANY\n"
    "MOE4:\n"
    "v_mad_u32_u24 v24, v22, s60, v8\n"
    "v_mad_u32_u24 v25, v22, s60, v9\n"
    "s_cmp_gt_u32 s42, s62\n"
    "s_cbranch_scc1 ROW2\n"
    "buffer_load_dwordx4 v[28:31], v24, s[20:23], s59 offen sc1\n"
    "buffer_load_dwordx4 v[32:35], v25, s[20:23], s59 offen sc1\n"
    "ROW1:\n"
    "s_branch E_68\n"
    "CHE_6:\n"
    "s_cbranch_execz CHE_7\n"
    "R_67:\n"
    "s_sub_u32 s39, s39, 1\n"
    "s_cbranch_scc1 ABORT\n"
    "E_68:\n"
    "ds_read_b32 v10, v14 offset:88\n"
    "ds_read2_b32 v[44:45], v54 offset1:7\n"
    "ds_read_b32 v46, v55\n"
    "s_waitcnt lgkmcnt(0)\n"
    "v_cmp_ne_u32 s[50:51], 8, v10\n"
    "v_cmp_eq_u32 vcc, 0, v44\n"
    "s_or_b64 vcc, vcc, s[50:51]\n"
    "s_cbranch_vccnz R_67\n"
    "v_cmp_gt_u32 vcc, v46, v44\n"
    "s_bcnt1_i32_b64 s43, vcc\n"
    "s_cmp_ge_u32 s43, 8\n"
    "s_cbranch_scc1 MOE5\n"
    "s_and_b32 s44, s43, 1\n"
    "s_mul_i32 s45, s43, 1344\n"
    "s_lshl_b32 s44, s44, 5\n"
    "s_sub_u32 s45, s45, s44\n"
    "v_add_u32 v50, s45, v11\n"
    "v_add_u32 v51, s45, v12\n"
    "s_waitcnt vmcnt(1)\n"
    "ds_write2_b32 v50, v28, v29 offset1:1\n"
    "ds_write_b32 v50, v30 offset:8\n"
    "v_lshl_add_u32 v52, s43, 2, v14\n"
    "s_waitcnt vmcnt(0)\n"
    "ds_write2_b32 v51, v32, v33 offset1:1\n"
    "ds_write_b32 v51, v34 offset:8\n"
    "v_bitop3_b32 v20, v31, v35, s63 bitop3:0x7e\n"
    "ds_write_b32 v52, v44 offset:3072\n"
    "v_cmp_ne_u32 vcc, 0, v20\n"
    "s_cbranch_vccnz ROWS\n"
    "MOE5:\n"
    "s_waitcnt lgkmcnt(0)\n"
    "s_barrier\n"
    "s_cmpk_lt_u32 s37, 14\n"
    "s_cbranch_scc1 DOWN\n"
    "s_cmpk_lt_u32 s2, 192\n"
    "s_cbranch_scc1 END\n"
    "s_waitcnt vmcnt(0)\n"
    "FINP:\n"
    "s_and_b32 s42, s2, 7\n"
    "s_lshl_b32 s42, s42, 3\n"
    "s_sub_u32 s43, s2, 192\n"
    "s_lshr_b32 s43, s43, 3\n"
    "s_add_u32 s42, s42, s43\n"
    "s_mul_i32 s42, s42, 3\n"
    "s_max_u32 s44, s37, 13\n"
    "s_add_u32 s42, s42, s44\n"
    "s_sub_u32 s42, s42, 13\n"
    "s_mul_i32 s43, s42, 1366\n"
    "s_lshr_b32 s43, s43, 16\n"
    "s_mul_i32 s44, s43, 48\n"
    "s_sub_u32 s42, s42, s44\n"
    "s_lshl_b32 s42, s42, 2\n"
    "s_lshl_b32 s44, s43, 3\n"
    "s_add_u32 s44, s44, 151168\n"
    "v_and_b32 v20, 1, v0\n"
    "v_lshl_add_u32 v21, v20, 2, s44\n"
    "ds_read_b32 v23, v21\n"
    "s_mul_i32 s44, s43, 2688\n"
    "s_add_u32 s44, s44, 73728\n"
    "v_mul_u32_u24 v22, 1312, v20\n"
    "v_add_u32 v22, s44, v22\n"
    "s_lshr_b32 s45, s42, 4\n"
    "s_lshl_b32 s45, s45, 5\n"
    "s_add_u32 s45, s45, s42\n"
    "v_lshrrev_b32 v25, 1, v2\n"
    "v_min_u32 v26, 2, v25\n"
    "v_lshl_add_u32 v27, v26, 4, v3\n"
    "v_add_u32 v27, s45, v27\n"
    "v_mul_u32_u24 v24, 683, v27\n"
    "v_lshrrev_b32 v24, 11, v24\n"
    "v_mul_u32_u24 v26, 3, v24\n"
    "v_sub_u32 v26, v27, v26\n"
    "v_lshlrev_b32 v28, 3, v27\n"
    "s_mov_b32 s45, 43520\n"
    "s_mov_b32 s46, 71168\n"
    "v_mul_u32_u24 v24, 16512, v24\n"
    "s_waitcnt lgkmcnt(0)\n"
    "v_bitop3_b32 v29, v23, s61, v23 bitop3:0x0c\n"
    "v_lshl_add_u32 v22, v27, 1, v22\n"
    "v_mov_b32 v40, 0\n"
    "v_mov_b32 v41, 0\n"
    "ds_read_u16_d16_hi v40, v22\n"
    "v_mul_u32_u24 v30, 1152, v29\n"
    "v_lshl_add_u32 v30, v27, 1, v30\n"
    "global_load_short_d16_hi v41, v30, s[4:5]\n"
    "v_lshrrev_b32 v31, 1, v29\n"
    "v_lshl_add_u32 v24, v31, 4, v24\n"
    "s_mov_b32 s47, 131072\n"
    "v_lshrrev_b32 v31, 4, v29\n"
    "v_lshlrev_b32 v31, 2, v31\n"
    "s_setprio 0\n"
    "s_waitcnt lgkmcnt(0)\n"
    "s_cmpk_eq_u32 s37, 7\n"
    "s_cbranch_scc1 FIN\n"
    "s_barrier\n"
    "s_branch FIN\n"
    "CHE_7:\n"
    "s_cbranch_execz CHE_8\n"
    "DOWN:\n"
    "s_cmp_eq_u32 s41, 99\n"
    "s_cbranch_scc1 DOWN1\n"
    "s_mov_b32 s41, 0\n"
    "s_setprio 0\n"
    "s_sleep 3\n"
    "s_branch DNRD\n"
    "DOWN1:\n"
    "s_setprio 3\n"
    "v_mov_b32 v47, s36\n"
    "v_mov_b32 v51, s36\n"
    "s_waitcnt lgkmcnt(0)\n"
    "s_barrier\n"
    "ds_read_b128 v[104:107], v4\n"
    "ds_read_b128 v[108:111], v4 offset:64\n"
    "ds_read_b128 v[112:115], v5\n"
    "ds_read_b128 v[116:119], v5 offset:64\n"
    "s_waitcnt lgkmcnt(3)\n"
    "v_mfma_f32_16x16x32_bf16 v[8:11], v[104:107], v[56:59], 0\n"
    "v_mfma_f32_16x16x32_bf16 v[12:15], v[104:107], v[60:63], 0\n"
    "v_mfma_f32_16x16x32_bf16 v[16:19], v[104:107], v[64:67], 0\n"
    "s_waitcnt lgkmcnt(2)\n"
    "v_mfma_f32_16x16x32_bf16 v[8:11], v[108:111], v[80:83], v[8:11]\n"
    "v_mfma_f32_16x16x32_bf16 v[12:15], v[108:111], v[84:87], v[12:15]\n"
    "v_mfma_f32_16x16x32_bf16 v[16:19], v[108:111], v[88:91], v[16:19]\n"
    "s_waitcnt lgkmcnt(1)\n"
    "v_mfma_f32_16x16x32_bf16 v[8:11], v[112:115], v[68:71], v[8:11]\n"
    "v_mfma_f32_16x16x32_bf16 v[12:15], v[112:115], v[72:75], v[12:15]\n"
    "v_mfma_f32_16x16x32_bf16 v[16:19], v[112:115], v[76:79], v[16:19]\n"
    "s_waitcnt lgkmcnt(0)\n"
    "v_mfma_f32_16x16x32_bf16 v[8:11], v[116:119], v[92:95], v[8:11]\n"
    "v_mfma_f32_16x16x32_bf16 v[12:15], v[116:119], v[96:99], v[12:15]\n"
    "v_mfma_f32_16x16x32_bf16 v[16:19], v[116:119], v[100:103], v[16:19]\n"
    "s_nop 5\n"
    "v_cvt_pk_bf16_f32 v44, v8, v9\n"
    "v_cvt_pk_bf16_f32 v45, v12, v13\n"
    "v_cvt_pk_bf16_f32 v46, v16, v17\n"
    "buffer_store_dwordx4 v[44:47], v6, s[20:23], 0 offen offset:0 sc1\n"
    "v_cvt_pk_bf16_f32 v48, v10, v11\n"
    "v_cvt_pk_bf16_f32 v49, v14, v15\n"
    "v_cvt_pk_bf16_f32 v50, v18, v19\n"
    "buffer_store_dwordx4 v[48:51], v6, s[20:23], 0 offen offset:3072 sc1\n"
    "s_branch END\n"
    "FIN:\n"
    "s_branch E_70\n"
    "CHE_8:\n"
    "s_cbranch_execz CHE_9\n"
    "R_69:\n"
    "s_sub_u32 s39, s39, 1\n"
    "s_cbranch_scc1 ABORT\n"
    "E_70:\n"
    "buffer_load_dwordx4 v[32:35], v24, s[20:23], s47 offen sc1\n"
    "buffer_load_dwordx2 v[36:37], v28, s[20:23], s45 offen sc1\n"
    "buffer_load_dwordx2 v[38:39], v28, s[20:23], s46 offen sc1\n"
    "s_waitcnt vmcnt(0)\n"
    "v_bitop3_b32 v42, v37, v39, s36 bitop3:0x7e\n"
    "v_xor_b32 v43, s36, v35\n"
    "v_or_b32 v42, v42, v43\n"
    "v_cmp_ne_u32 vcc, 0, v42\n"
    "s_cbranch_vccnz R_69\n"
    "v_cmp_eq_u32 vcc, 1, v26\n"
    "v_cmp_eq_u32 s[50:51], 2, v26\n"
    "v_and_b32 v42, 1, v29\n"
    "v_cndmask_b32 v32, v32, v33, vcc\n"
    "v_cndmask_b32 v32, v32, v34, s[50:51]\n"
    "v_lshlrev_b32 v42, 4, v42\n"
    "v_lshrrev_b32 v32, v42, v32\n"
    "v_lshlrev_b32 v32, 16, v32\n"
    "v_add_f32 v41, v41, v32\n"
    "v_add_f32 v36, v36, v38\n"
    "v_cvt_pk_bf16_f32 v41, 0, v41\n"
    "v_cvt_pk_bf16_f32 v36, 0, v36\n"
    "s_brev_b32 s48, 1\n"
    "v_ashrrev_i32 v42, 31, v23\n"
    "v_bitop3_b32 v23, v42, v23, s48 bitop3:0x63\n"
    "v_and_b32 v23, 0xfffff800, v23\n"
    "v_cvt_pk_bf16_f32 v23, 0, v23\n"
    "v_mul_f32 v23, 0xbfb8aa3b, v23\n"
    "v_exp_f32 v23, v23\n"
    "v_lshlrev_b32 v20, 4, v20\n"
    "v_add_f32 v23, 1.0, v23\n"
    "v_rcp_f32 v23, v23\n"
    "s_movk_i32 s44, 0x3f80\n"
    "v_lshlrev_b32_e64 v20, v20, s44\n"
    "v_cvt_pk_bf16_f32 v23, 0, v23\n"
    "v_cmp_eq_u32 vcc, 0, v25\n"
    "s_nop 1\n"
    "v_cndmask_b32 v32, 0, v20, vcc\n"
    "v_cmp_eq_u32 vcc, 1, v25\n"
    "s_nop 1\n"
    "v_cndmask_b32 v33, 0, v20, vcc\n"
    "v_cmp_eq_u32 vcc, 2, v25\n"
    "s_nop 1\n"
    "v_cndmask_b32 v34, 0, v20, vcc\n"
    "v_mov_b32 v35, 0\n"
    "v_lshrrev_b32 v42, 2, v1\n"
    "v_mul_u32_u24 v42, 12416, v42\n"
    "v_and_b32 v43, 3, v0\n"
    "v_lshl_add_u32 v42, v43, 4, v42\n"
    "s_mul_i32 s43, s43, 3072\n"
    "s_lshl_b32 s42, s42, 4\n"
    "s_add_u32 s42, s42, s43\n"
    "s_add_u32 s50, s42, 6447104\n"
    "s_add_u32 s51, s42, 6645760\n"
    "s_add_u32 s52, s42, 6844416\n"
    "s_add_u32 s53, s42, 7043072\n"
    "s_add_u32 s54, s42, 7241728\n"
    "v_cmp_gt_u32 vcc, 32, v1\n"
    "v_mov_b32 v43, 0x7ffffff0\n"
    "v_mov_b32 v21, s36\n"
    "v_cndmask_b32 v43, v43, v42, vcc\n"
    "v_cndmask_b32 v21, 0, v21, vcc\n"
    "s_mov_b32 s46, 0x003f003f\n"
    "s_mov_b32 s47, 0x003f003f\n"
    "U_71:\n"
    "s_memrealtime s[98:99]\n"
    "s_waitcnt lgkmcnt(0)\n"
    "s_sub_u32 s98, s98, s96\n"
    "s_cmpk_lt_u32 s98, 433\n"
    "s_cbranch_scc1 U_71\n"
    "buffer_load_dwordx4 v[104:107], v42, s[20:23], s50 offen sc1\n"
    "buffer_load_dwordx4 v[108:111], v42, s[20:23], s51 offen sc1\n"
    "buffer_load_dwordx4 v[112:115], v42, s[20:23], s52 offen sc1\n"
    "buffer_load_dwordx4 v[116:119], v42, s[20:23], s53 offen sc1\n"
    "buffer_load_dwordx4 v[120:123], v43, s[20:23], s54 offen sc1\n"
    "buffer_load_dword v22, v31, s[20:23], 0 offen offset:1024 sc1\n"
    "s_branch E_73\n"
    "CHE_9:\n"
    "s_cbranch_execz CHE_10\n"
    "R_72:\n"
    "s_sub_u32 s39, s39, 1\n"
    "s_cbranch_scc1 ABORT\n"
    "s_wqm_b64 exec, vcc\n"
    "buffer_load_dwordx4 v[104:107], v42, s[20:23], s50 offen sc1\n"
    "buffer_load_dwordx4 v[108:111], v42, s[20:23], s51 offen sc1\n"
    "buffer_load_dwordx4 v[112:115], v42, s[20:23], s52 offen sc1\n"
    "buffer_load_dwordx4 v[116:119], v42, s[20:23], s53 offen sc1\n"
    "buffer_load_dwordx4 v[120:123], v43, s[20:23], s54 offen sc1\n"
    "buffer_load_dword v22, v31, s[20:23], 0 offen offset:1024 sc1\n"
    "s_mov_b64 exec, -1\n"
    "E_73:\n"
    "s_waitcnt vmcnt(2)\n"
    "v_bitop3_b32 v24, v107, v111, s36 bitop3:0x7e\n"
    "v_bitop3_b32 v25, v115, v119, s36 bitop3:0x7e\n"
    "s_waitcnt vmcnt(0)\n"
    "v_xor_b32 v26, v21, v123\n"
    "v_bitop3_b32 v24, v24, v22, s36 bitop3:0xf6\n"
    "v_or3_b32 v24, v24, v25, v26\n"
    "v_cmp_ne_u32 vcc, 0, v24\n"
    "s_cbranch_vccnz R_72\n"
    "v_mov_b32 v107, 0\n"
    "v_mov_b32 v111, 0\n"
    "v_mov_b32 v115, 0\n"
    "v_mov_b32 v119, 0\n"
    "v_mov_b32 v123, 0\n"
    "v_mfma_f32_16x16x32_bf16 v[8:11], v[104:107], v[32:35], 0\n"
    "v_mfma_f32_16x16x32_bf16 v[8:11], v[108:111], v[32:35], v[8:11]\n"
    "v_mfma_f32_16x16x32_bf16 v[8:11], v[112:115], v[32:35], v[8:11]\n"
    "v_mfma_f32_16x16x32_bf16 v[8:11], v[116:119], v[32:35], v[8:11]\n"
    "v_mfma_f32_16x16x32_bf16 v[8:11], v[120:123], v[32:35], v[8:11]\n"
    "s_nop 7\n"
    "v_permlane32_swap_b32 v8, v10\n"
    "v_permlane32_swap_b32 v9, v11\n"
    "v_add_f32 v8, v8, v10\n"
    "v_add_f32 v9, v9, v11\n"
    "s_nop 1\n"
    "v_permlane16_swap_b32 v8, v9\n"
    "v_add_f32 v8, v8, v9\n"
    "v_cvt_pk_bf16_f32 v8, 0, v8\n"
    "v_mul_f32 v8, v8, v23\n"
    "v_cvt_pk_bf16_f32 v8, 0, v8\n"
    "v_add_f32 v8, v40, v8\n"
    "v_cvt_pk_bf16_f32 v8, 0, v8\n"
    "v_mul_f32 v8, v36, v8\n"
    "v_cvt_pk_bf16_f32 v8, 0, v8\n"
    "v_add_f32 v8, v41, v8\n"
    "v_cvt_pk_bf16_f32 v8, v8, v8\n"
    "s_mov_b64 exec, s[46:47]\n"
    "global_store_short v30, v8, s[34:35]\n"
    "s_mov_b64 exec, -1\n"
    "s_branch END\n"
    "ROW2:\n"
    "v_bitop3_b32 v23, v23, s61, v23 bitop3:0x0c\n"
    "v_mad_u32_u24 v26, v23, s60, v8\n"
    "v_mad_u32_u24 v27, v23, s60, v9\n"
    "s_add_u32 s46, s62, 28\n"
    "s_cmp_gt_u32 s42, s46\n"
    "s_cbranch_scc1 ROW6\n"
    "buffer_load_dwordx4 v[28:31], v24, s[20:23], s59 offen sc1\n"
    "buffer_load_dwordx4 v[32:35], v25, s[20:23], s59 offen sc1\n"
    "buffer_load_dwordx4 v[36:39], v26, s[20:23], s59 offen sc1\n"
    "buffer_load_dwordx4 v[40:43], v27, s[20:23], s59 offen sc1\n"
    "ROW3:\n"
    "s_branch E_75\n"
    "CHE_10:\n"
    "s_cbranch_execz CHE_11\n"
    "R_74:\n"
    "s_sub_u32 s39, s39, 1\n"
    "s_cbranch_scc1 ABORT\n"
    "E_75:\n"
    "ds_read_b32 v10, v14 offset:88\n"
    "ds_read2_b32 v[44:45], v54 offset1:7\n"
    "ds_read_b32 v46, v55\n"
    "s_waitcnt lgkmcnt(0)\n"
    "v_cmp_ne_u32 s[50:51], 8, v10\n"
    "v_cmp_eq_u32 vcc, 0, v44\n"
    "s_or_b64 vcc, vcc, s[50:51]\n"
    "s_cbranch_vccnz R_74\n"
    "v_cmp_gt_u32 vcc, v46, v44\n"
    "s_bcnt1_i32_b64 s43, vcc\n"
    "s_cmp_ge_u32 s43, 8\n"
    "s_cbranch_scc1 ROW4\n"
    "s_and_b32 s44, s43, 1\n"
    "s_mul_i32 s45, s43, 1344\n"
    "s_lshl_b32 s44, s44, 5\n"
    "s_sub_u32 s45, s45, s44\n"
    "v_add_u32 v50, s45, v11\n"
    "v_add_u32 v51, s45, v12\n"
    "s_waitcnt vmcnt(3)\n"
    "ds_write2_b32 v50, v28, v29 offset1:1\n"
    "ds_write_b32 v50, v30 offset:8\n"
    "v_lshl_add_u32 v52, s43, 2, v14\n"
    "s_waitcnt vmcnt(2)\n"
    "ds_write2_b32 v51, v32, v33 offset1:1\n"
    "ds_write_b32 v51, v34 offset:8\n"
    "v_bitop3_b32 v20, v31, v35, s63 bitop3:0x7e\n"
    "ds_write_b32 v52, v44 offset:3072\n"
    "v_cmp_ne_u32 vcc, 0, v20\n"
    "s_cbranch_vccnz ROWT\n"
    "ROW4:\n"
    "v_cmp_gt_u32 vcc, v46, v45\n"
    "s_bcnt1_i32_b64 s43, vcc\n"
    "s_cmp_ge_u32 s43, 8\n"
    "s_cbranch_scc1 MOE5\n"
    "s_and_b32 s44, s43, 1\n"
    "s_mul_i32 s45, s43, 1344\n"
    "s_lshl_b32 s44, s44, 5\n"
    "s_sub_u32 s45, s45, s44\n"
    "v_add_u32 v50, s45, v11\n"
    "v_add_u32 v51, s45, v12\n"
    "s_waitcnt vmcnt(1)\n"
    "ds_write2_b32 v50, v36, v37 offset1:1\n"
    "ds_write_b32 v50, v38 offset:8\n"
    "v_lshl_add_u32 v52, s43, 2, v14\n"
    "s_waitcnt vmcnt(0)\n"
    "ds_write2_b32 v51, v40, v41 offset1:1\n"
    "ds_write_b32 v51, v42 offset:8\n"
    "v_bitop3_b32 v20, v39, v43, s63 bitop3:0x7e\n"
    "ds_write_b32 v52, v45 offset:3072\n"
    "v_cmp_ne_u32 vcc, 0, v20\n"
    "s_cbranch_vccz MOE5\n"
    "s_branch ROWT\n"
    "CHE_11:\n"
    "s_cbranch_execz CHE_12\n"
    "ROW6:\n"
    "v_bitop3_b32 v49, v48, s61, v48 bitop3:0x0c\n"
    "v_mad_u32_u24 v48, v49, s60, v8\n"
    "v_mad_u32_u24 v49, v49, s60, v9\n"
    "ROW7:\n"
    "s_sub_u32 s39, s39, 1\n"
    "s_cbranch_scc1 ABORT\n"
    "buffer_load_dwordx4 v[28:31], v24, s[20:23], s59 offen sc1\n"
    "buffer_load_dwordx4 v[32:35], v25, s[20:23], s59 offen sc1\n"
    "buffer_load_dwordx4 v[36:39], v26, s[20:23], s59 offen sc1\n"
    "buffer_load_dwordx4 v[40:43], v27, s[20:23], s59 offen sc1\n"
    "buffer_load_dwordx4 v[112:115], v48, s[20:23], s59 offen sc1\n"
    "buffer_load_dwordx4 v[116:119], v49, s[20:23], s59 offen sc1\n"
    "s_branch E_77\n"
    "R_76:\n"
    "s_sub_u32 s39, s39, 1\n"
    "s_cbranch_scc1 ABORT\n"
    "E_77:\n"
    "ds_read_b32 v10, v14 offset:88\n"
    "ds_read2_b32 v[44:45], v54 offset1:7\n"
    "ds_read_b32 v47, v54 offset:56\n"
    "ds_read_b32 v46, v55\n"
    "s_waitcnt lgkmcnt(0)\n"
    "v_cmp_ne_u32 s[50:51], 8, v10\n"
    "v_cmp_eq_u32 vcc, 0, v44\n"
    "s_or_b64 vcc, vcc, s[50:51]\n"
    "s_cbranch_vccnz R_76\n"
    "v_cmp_gt_u32 vcc, v46, v44\n"
    "s_bcnt1_i32_b64 s43, vcc\n"
    "s_cmp_ge_u32 s43, 8\n"
    "s_cbranch_scc1 ROW8\n"
    "s_and_b32 s44, s43, 1\n"
    "s_mul_i32 s45, s43, 1344\n"
    "s_lshl_b32 s44, s44, 5\n"
    "s_sub_u32 s45, s45, s44\n"
    "v_add_u32 v50, s45, v11\n"
    "v_add_u32 v51, s45, v12\n"
    "s_waitcnt vmcnt(5)\n"
    "ds_write2_b32 v50, v28, v29 offset1:1\n"
    "ds_write_b32 v50, v30 offset:8\n"
    "v_lshl_add_u32 v52, s43, 2, v14\n"
    "s_waitcnt vmcnt(4)\n"
    "ds_write2_b32 v51, v32, v33 offset1:1\n"
    "ds_write_b32 v51, v34 offset:8\n"
    "v_bitop3_b32 v20, v31, v35, s63 bitop3:0x7e\n"
    "ds_write_b32 v52, v44 offset:3072\n"
    "v_cmp_ne_u32 vcc, 0, v20\n"
    "s_cbranch_vccnz ROW7\n"
    "ROW8:\n"
    "v_cmp_gt_u32 vcc, v46, v45\n"
    "s_bcnt1_i32_b64 s43, vcc\n"
    "s_cmp_ge_u32 s43, 8\n"
    "s_cbranch_scc1 ROW9\n"
    "s_and_b32 s44, s43, 1\n"
    "s_mul_i32 s45, s43, 1344\n"
    "s_lshl_b32 s44, s44, 5\n"
    "s_sub_u32 s45, s45, s44\n"
    "v_add_u32 v50, s45, v11\n"
    "v_add_u32 v51, s45, v12\n"
    "s_waitcnt vmcnt(3)\n"
    "ds_write2_b32 v50, v36, v37 offset1:1\n"
    "ds_write_b32 v50, v38 offset:8\n"
    "v_lshl_add_u32 v52, s43, 2, v14\n"
    "s_waitcnt vmcnt(2)\n"
    "ds_write2_b32 v51, v40, v41 offset1:1\n"
    "ds_write_b32 v51, v42 offset:8\n"
    "v_bitop3_b32 v20, v39, v43, s63 bitop3:0x7e\n"
    "ds_write_b32 v52, v45 offset:3072\n"
    "v_cmp_ne_u32 vcc, 0, v20\n"
    "s_cbranch_vccnz ROW7\n"
    "ROW9:\n"
    "v_cmp_gt_u32 vcc, v46, v47\n"
    "s_bcnt1_i32_b64 s43, vcc\n"
    "s_cmp_ge_u32 s43, 8\n"
    "s_cbranch_scc1 ROWA\n"
    "s_and_b32 s44, s43, 1\n"
    "s_mul_i32 s45, s43, 1344\n"
    "s_lshl_b32 s44, s44, 5\n"
    "s_sub_u32 s45, s45, s44\n"
    "v_add_u32 v50, s45, v11\n"
    "v_add_u32 v51, s45, v12\n"
    "s_waitcnt vmcnt(1)\n"
    "ds_write2_b32 v50, v112, v113 offset1:1\n"
    "ds_write_b32 v50, v114 offset:8\n"
    "v_lshl_add_u32 v52, s43, 2, v14\n"
    "s_waitcnt vmcnt(0)\n"
    "ds_write2_b32 v51, v116, v117 offset1:1\n"
    "ds_write_b32 v51, v118 offset:8\n"
    "v_bitop3_b32 v20, v115, v119, s63 bitop3:0x7e\n"
    "ds_write_b32 v52, v47 offset:3072\n"
    "v_cmp_ne_u32 vcc, 0, v20\n"
    "s_cbranch_vccnz ROW7\n"
    "ROWA:\n"
    "s_waitcnt vmcnt(0)\n"
    "s_add_u32 s46, s46, 28\n"
    "s_cmp_le_u32 s42, s46\n"
    "s_cbranch_scc1 MOE5\n"
    "s_sub_u32 s47, s46, 320\n"
    "v_mov_b32 v44, s47\n"
    "ds_read_b32 v44, v44\n"
    "s_waitcnt lgkmcnt(0)\n"
    "v_cmp_gt_u32 vcc, v46, v44\n"
    "s_bcnt1_i32_b64 s43, vcc\n"
    "s_cmp_ge_u32 s43, 8\n"
    "s_cbranch_scc1 ROWA\n"
    "v_bitop3_b32 v25, v44, s61, v44 bitop3:0x0c\n"
    "v_mad_u32_u24 v24, v25, s60, v8\n"
    "v_mad_u32_u24 v25, v25, s60, v9\n"
    "ROWB:\n"
    "s_sub_u32 s39, s39, 1\n"
    "s_cbranch_scc1 ABORT\n"
    "buffer_load_dwordx4 v[28:31], v24, s[20:23], s59 offen sc1\n"
    "buffer_load_dwordx4 v[32:35], v25, s[20:23], s59 offen sc1\n"
    "v_cmp_gt_u32 vcc, v46, v44\n"
    "s_bcnt1_i32_b64 s43, vcc\n"
    "s_cmp_ge_u32 s43, 8\n"
    "s_cbranch_scc1 ROWA\n"
    "s_and_b32 s44, s43, 1\n"
    "s_mul_i32 s45, s43, 1344\n"
    "s_lshl_b32 s44, s44, 5\n"
    "s_sub_u32 s45, s45, s44\n"
    "v_add_u32 v50, s45, v11\n"
    "v_add_u32 v51, s45, v12\n"
    "s_waitcnt vmcnt(1)\n"
    "ds_write2_b32 v50, v28, v29 offset1:1\n"
    "ds_write_b32 v50, v30 offset:8\n"
    "v_lshl_add_u32 v52, s43, 2, v14\n"
    "CHE_12:\n"
    "s_cbranch_execz CHE9\n"
    "s_waitcnt vmcnt(0)\n"
    "ds_write2_b32 v51, v32, v33 offset1:1\n"
    "ds_write_b32 v51, v34 offset:8\n"
    "v_bitop3_b32 v20, v31, v35, s63 bitop3:0x7e\n"
    "ds_write_b32 v52, v44 offset:3072\n"
    "v_cmp_ne_u32 vcc, 0, v20\n"
    "s_cbranch_vccnz ROWB\n"
    "s_branch ROWA\n"
    "MOE15:\n"
    "ds_read_b64 v[20:21], v14 offset:64\n"
    "ds_read_b32 v32, v19\n"
    "s_sub_u32 s39, s39, 1\n"
    "s_cbranch_scc1 ABORT\n"
    "s_waitcnt lgkmcnt(0)\n"
    "v_cmp_ne_u32 vcc, 4, v21\n"
    "v_readfirstlane_b32 s42, v20\n"
    "v_bitop3_b32 v32, v32, s61, v32 bitop3:0x0c\n"
    "s_cbranch_vccnz MOE15\n"
    "s_cmp_gt_u32 s42, 151904\n"
    "s_cbranch_scc1 MANY\n"
    "MOE16:\n"
    "v_cmp_gt_u32 s[50:51], s42, v19\n"
    "v_lshlrev_b32 v32, 3, v32\n"
    "s_movk_i32 s43, 4096\n"
    "s_mov_b64 exec, s[50:51]\n"
    "buffer_load_dwordx2 v[34:35], v32, s[20:23], s43 offen sc1\n"
    "s_mov_b64 exec, -1\n"
    "R_78:\n"
    "s_waitcnt vmcnt(0)\n"
    "v_cmp_ne_u32 vcc, s36, v35\n"
    "v_max_u32 v34, 1, v34\n"
    "v_cndmask_b32 v53, 0, v34, s[50:51]\n"
    "s_and_b64 vcc, vcc, s[50:51]\n"
    "s_cbranch_vccnz XKS\n"
    "ds_write_b32 v55, v53\n"
    "s_branch MOE5\n"
    "XKS:\n"
    "s_sub_u32 s39, s39, 1\n"
    "s_cbranch_scc1 ABORT\n"
    "s_mov_b64 exec, s[50:51]\n"
    "buffer_load_dwordx2 v[34:35], v32, s[20:23], s43 offen sc1\n"
    "s_mov_b64 exec, -1\n"
    "s_branch R_78\n"
    "ROWS:\n"
    "s_sub_u32 s39, s39, 1\n"
    "s_cbranch_scc1 ABORT\n"
    "buffer_load_dwordx4 v[28:31], v24, s[20:23], s59 offen sc1\n"
    "buffer_load_dwordx4 v[32:35], v25, s[20:23], s59 offen sc1\n"
    "s_branch ROW1\n"
    "ROWT:\n"
    "s_sub_u32 s39, s39, 1\n"
    "s_cbranch_scc1 ABORT\n"
    "buffer_load_dwordx4 v[28:31], v24, s[20:23], s59 offen sc1\n"
    "buffer_load_dwordx4 v[32:35], v25, s[20:23], s59 offen sc1\n"
    "buffer_load_dwordx4 v[36:39], v26, s[20:23], s59 offen sc1\n"
    "buffer_load_dwordx4 v[40:43], v27, s[20:23], s59 offen sc1\n"
    "s_branch ROW3\n"
    "TOPS:\n"
    "s_mov_b64 exec, -1\n"
    "v_cmpx_le_u32 vcc, s49, v24\n"
    "ds_add_rtn_u32 v21, v14, v18 offset:64\n"
    "s_waitcnt lgkmcnt(0)\n"
    "ds_write_b32 v21, v24\n"
    "s_mov_b64 exec, -1\n"
    "v_cmpx_le_u32 vcc, s49, v26\n"
    "ds_add_rtn_u32 v21, v14, v18 offset:64\n"
    "s_waitcnt lgkmcnt(0)\n"
    "ds_write_b32 v21, v26\n"
    "s_mov_b64 exec, -1\n"
    "v_cmpx_le_u32 vcc, s49, v28\n"
    "ds_add_rtn_u32 v21, v14, v18 offset:64\n"
    "s_waitcnt lgkmcnt(0)\n"
    "ds_write_b32 v21, v28\n"
    "s_mov_b64 exec, -1\n"
    "v_cmpx_le_u32 vcc, s49, v30\n"
    "ds_add_rtn_u32 v21, v14, v18 offset:64\n"
    "s_waitcnt lgkmcnt(0)\n"
    "ds_write_b32 v21, v30\n"
    "s_mov_b64 exec, -1\n"
    "v_cmpx_le_u32 vcc, s49, v32\n"
    "ds_add_rtn_u32 v21, v14, v18 offset:64\n"
    "s_waitcnt lgkmcnt(0)\n"
    "ds_write_b32 v21, v32\n"
    "s_mov_b64 exec, -1\n"
    "v_cmpx_le_u32 vcc, s49, v34\n"
    "ds_add_rtn_u32 v21, v14, v18 offset:64\n"
    "s_waitcnt lgkmcnt(0)\n"
    "ds_write_b32 v21, v34\n"
    "s_mov_b64 exec, -1\n"
    "v_cmpx_le_u32 vcc, s49, v36\n"
    "ds_add_rtn_u32 v21, v14, v18 offset:64\n"
    "s_waitcnt lgkmcnt(0)\n"
    "ds_write_b32 v21, v36\n"
    "s_mov_b64 exec, -1\n"
    "v_cmpx_le_u32 vcc, s49, v38\n"
    "ds_add_rtn_u32 v21, v14, v18 offset:64\n"
    "s_waitcnt lgkmcnt(0)\n"
    "ds_write_b32 v21, v38\n"
    "s_branch TOPC\n"
    "MANY:\n"
    "v_lshlrev_b32 v22, 2, v0\n"
    "v_add_u32 v22, 149600, v22\n"
    "MANY1:\n"
    "v_cmpx_gt_u32 vcc, s42, v22\n"
    "s_cbranch_execz MANY3\n"
    "ds_read_b32 v23, v22\n"
    "v_mov_b32 v24, 0\n"
    "s_mov_b32 s43, 151648\n"
    "MANY2:\n"
    "v_mov_b32 v25, s43\n"
    "ds_read_b32 v25, v25\n"
    "s_add_u32 s43, s43, 4\n"
    "s_waitcnt lgkmcnt(0)\n"
    "v_cmp_gt_u32 vcc, v25, v23\n"
    "s_nop 1\n"
    "v_addc_co_u32 v24, vcc, 0, v24, vcc\n"
    "s_cmp_lt_u32 s43, s42\n"
    "s_cbranch_scc1 MANY2\n"
    "v_min_u32 v24, 16, v24\n"
    "v_lshl_add_u32 v24, v24, 2, v14\n"
    "ds_write_b32 v24, v23 offset:3152\n"
    "v_add_u32 v22, 2048, v22\n"
    "s_branch MANY1\n"
    "MANY3:\n"
    "s_mov_b64 exec, -1\n"
    "s_mov_b64 exec, 1\n"
    "ds_add_u32 v14, v15 offset:92\n"
    "s_mov_b64 exec, -1\n"
    "s_branch E_80\n"
    "R_79:\n"
    "s_sleep 1\n"
    "s_sub_u32 s39, s39, 1\n"
    "s_cbranch_scc1 ABORT\n"
    "E_80:\n"
    "ds_read_b32 v10, v14 offset:92\n"
    "s_waitcnt lgkmcnt(0)\n"
    "v_cmp_ne_u32 vcc, 8, v10\n"
    "s_cbranch_vccnz R_79\n"
    "v_subrev_u32 v7, 400, v7\n"
    "v_lshl_add_u32 v24, v2, 2, v14\n"
    "ds_read2_b32 v[22:23], v7 offset1:7\n"
    "ds_read_b32 v48, v7 offset:56\n"
    "ds_read_b32 v32, v24 offset:3152\n"
    "s_mov_b32 s42, 151712\n"
    "s_waitcnt lgkmcnt(0)\n"
    "v_bitop3_b32 v22, v22, s61, v22 bitop3:0x0c\n"
    "v_bitop3_b32 v32, v32, s61, v32 bitop3:0x0c\n"
    "s_cmpk_eq_u32 s37, 15\n"
    "s_cbranch_scc1 MOE16\n"
    "s_branch MOE4\n"
    "END:\n"
    "s_endpgm\n"

    :: "{s[0:1]}"(__builtin_amdgcn_kernarg_segment_ptr()), "{s2}"(blockIdx.x), "{v0}"(threadIdx.x) : "v127", "s99", "vcc", "memory");
}

extern "C" void kernel_launch(void* const* d_in, const int* in_sizes, int n_in, void* d_out, int out_size, void* d_ws, size_t ws_size, hipStream_t stream) {
  (void)hipFuncSetAttribute((const void*)mega, hipFuncAttributeMaxDynamicSharedMemorySize, 159904);
  Args p;
  p.x = d_in[0]; p.lar = d_in[1]; p.aim = d_in[2]; p.ldt = d_in[5]; p.cvec = d_in[12]; p.wmod = d_in[13];
  p.ws = (char*)d_ws + ((size_t)240 << 20); p.ws_bytes = 10643968; p.ws_flags = 0x00020000u;
  p.cre = d_in[3]; p.cim = d_in[4]; p.dsk = d_in[6]; p.wr = d_in[8]; p.wg = d_in[9]; p.wu = d_in[10]; p.wd = d_in[11];
  p.out = d_out;
  mega<<<256, 1024, 159904, stream>>>(p);
}
```

```cpp
#include <hip/hip_runtime.h>
#include <stdint.h>

struct Args {
  const void *x, *lar, *aim, *ldt, *cre, *cim, *wmod, *cvec;
  void* ws; uint32_t ws_bytes, ws_flags;
  const void *dsk, *wr, *wg, *wu, *wd;
  void* out;
};
extern "C" __device__ __attribute__((used, aligned(128))) const uint32_t mega_tab[3840] = {
  0x0, 0x0, 0x0, 0x0, 0x0, 0x0, 0x0, 0x0, 0x0, 0x0, 0x0, 0x0, 0x0, 0x0, 0x0, 0x0,
  0x0, 0x0, 0x0, 0x0, 0x0, 0x0, 0x0, 0x0, 0x0, 0x0, 0x0, 0x0, 0x0, 0x0, 0x0, 0x0,
  0x0, 0x0, 0x0, 0x0, 0x0, 0x0, 0x0, 0x0, 0x0, 0x0, 0x0, 0x0, 0x0, 0x0, 0x0, 0x3c000000,
  0x0, 0x0, 0x0, 0x3c003c00, 0x0, 0x0, 0x3c000000, 0x3c003c00, 0x0, 0x0, 0x3c003c00, 0x3c00, 0x0, 0x3c000000, 0x3c003c00, 0x0,
  0x0, 0x0, 0x0, 0x0, 0x0, 0x0, 0x0, 0x0, 0x0, 0x0, 0x0, 0x0, 0x0, 0x0, 0x0, 0x3c000000,
  0x0, 0x0, 0x0, 0x3c003c00, 0x0, 0x0, 0x3c000000, 0x3c003c00, 0x0, 0x0, 0x3c003c00, 0x3c00, 0x0, 0x3c000000, 0x3c003c00, 0x0,
  0x0, 0x3c003c00, 0x3c00, 0x0, 0x3c000000, 0x3c003c00, 0x0, 0x0, 0x3c003c00, 0x3c00, 0x0, 0x0, 0x3c003c00, 0x0, 0x0, 0x0,
  0x3c00, 0x0, 0x0, 0x0, 0x0, 0x0, 0x0, 0x0, 0x0, 0x0, 0x0, 0x0, 0x0, 0x0, 0x0, 0x0,
  0x0, 0x3c003c00, 0x3c00, 0x0, 0x3c000000, 0x3c003c00, 0x0, 0x0, 0x3c003c00, 0x3c00, 0x0, 0x0, 0x3c003c00, 0x0, 0x0, 0x0,
  0x3c00, 0x0, 0x0, 0x0, 0x0, 0x0, 0x0, 0x0, 0x0, 0x0, 0x0, 0x0, 0x0, 0x0, 0x0, 0x0,
  0x0, 0x0, 0x0, 0x0, 0x0, 0x0, 0x0, 0x0, 0x0, 0x0, 0x0, 0x0, 0x0, 0x0, 0x0, 0x0,
  0x0, 0x0, 0x0, 0x0, 0x0, 0x0, 0x0, 0x0, 0x0, 0x0, 0x0, 0x0, 0x0, 0x0, 0x0, 0x0,
  0x0, 0x0, 0x0, 0x0, 0x0, 0x0, 0x0, 0x0, 0x0, 0x0, 0x0, 0x0, 0x0, 0x0, 0x0, 0x0,
  0x0, 0x0, 0x0, 0x0, 0x0, 0x0, 0x0, 0x0, 0x0, 0x0, 0x0, 0x0, 0x0, 0x0, 0x0, 0x0,
  0x0, 0x0, 0x0, 0x0, 0x0, 0x0, 0x0, 0x0, 0x0, 0x0, 0x0, 0x0, 0x0, 0x0, 0x0, 0x0,
  0x0, 0x0, 0x0, 0x0, 0x0, 0x0, 0x0, 0x0, 0x0, 0x0, 0x0, 0x0, 0x0, 0x0, 0x0, 0x0,
  0x0, 0x0, 0x0, 0x0, 0x0, 0x0, 0x0, 0x0, 0x0, 0x0, 0x0, 0x0, 0x0, 0x0, 0x0, 0x0,
  0x0, 0x0, 0x0, 0x0, 0x0, 0x0, 0x0, 0x0, 0x0, 0x0, 0x0, 0x0, 0x0, 0x0, 0x0, 0x0,
  0x0, 0x0, 0x0, 0x0, 0x0, 0x0, 0x0, 0x0, 0x0, 0x0, 0x0, 0x0, 0x0, 0x0, 0x0, 0x0,
  0x0, 0x0, 0x0, 0x0, 0x0, 0x0, 0x0, 0x0, 0x0, 0x0, 0x0, 0x0, 0x0, 0x0, 0x0, 0x0,
  0x0, 0x0, 0x0, 0x0, 0x0, 0x0, 0x0, 0x0, 0x0, 0x0, 0x0, 0x0, 0x0, 0x0, 0x0, 0x0,
  0x0, 0x0, 0x0, 0x0, 0x0, 0x0, 0x0, 0x0, 0x0, 0x0, 0x0, 0x0, 0x0, 0x0, 0x0, 0x0,
  0x0, 0x0, 0x0, 0x0, 0x0, 0x0, 0x0, 0x0, 0x0, 0x0, 0x0, 0x0, 0x0, 0x0, 0x0, 0x3c000000,
  0x0, 0x0, 0x0, 0x3c003c00, 0x0, 0x0, 0x3c000000, 0x3c003c00, 0x0, 0x0, 0x3c003c00, 0x3c00, 0x0, 0x3c000000, 0x3c003c00, 0x0,
  0x0, 0x0, 0x0, 0x0, 0x0, 0x0, 0x0, 0x0, 0x0, 0x0, 0x0, 0x0, 0x0, 0x0, 0x0, 0x3c000000,
  0x0, 0x0, 0x0, 0x3c003c00, 0x0, 0x0, 0x3c000000, 0x3c003c00, 0x0, 0x0, 0x3c003c00, 0x3c00, 0x0, 0x3c000000, 0x3c003c00, 0x0,
  0x0, 0x3c003c00, 0x3c00, 0x0, 0x3c000000, 0x3c003c00, 0x0, 0x0, 0x3c003c00, 0x3c00, 0x0, 0x0, 0x3c003c00, 0x0, 0x0, 0x0,
  0x3c00, 0x0, 0x0, 0x0, 0x0, 0x0, 0x0, 0x0, 0x0, 0x0, 0x0, 0x0, 0x0, 0x0, 0x0, 0x0,
  0x0, 0x3c003c00, 0x3c00, 0x0, 0x3c000000, 0x3c003c00, 0x0, 0x0, 0x3c003c00, 0x3c00, 0x0, 0x0, 0x3c003c00, 0x0, 0x0, 0x0,
  0x3c00, 0x0, 0x0, 0x0, 0x0, 0x0, 0x0, 0x0, 0x0, 0x0, 0x0, 0x0, 0x0, 0x0, 0x0, 0x0,
  0x0, 0x0, 0x0, 0x0, 0x0, 0x0, 0x0, 0x0, 0x0, 0x0, 0x0, 0x0, 0x0, 0x0, 0x0, 0x0,
  0x0, 0x0, 0x0, 0x0, 0x0, 0x0, 0x0, 0x0, 0x0, 0x0, 0x0, 0x0, 0x0, 0x0, 0x0, 0x0,
  0x0, 0x0, 0x0, 0x0, 0x0, 0x0, 0x0, 0x0, 0x0, 0x0, 0x0, 0x0, 0x0, 0x0, 0x0, 0x0,
  0x0, 0x0, 0x0, 0x0, 0x0, 0x0, 0x0, 0x0, 0x0, 0x0, 0x0, 0x0, 0x0, 0x0, 0x0, 0x0,
  0x0, 0x0, 0x0, 0x0, 0x0, 0x0, 0x0, 0x0, 0x0, 0x0, 0x0, 0x0, 0x0, 0x0, 0x0, 0x0,
  0x0, 0x0, 0x0, 0x0, 0x0, 0x0, 0x0, 0x0, 0x0, 0x0, 0x0, 0x3c000000, 0x0, 0x0, 0x0, 0x3c003c00,
  0x0, 0x0, 0x0, 0x0, 0x0, 0x0, 0x0, 0x0, 0x0, 0x0, 0x0, 0x0, 0x0, 0x0, 0x0, 0x0,
  0x0, 0x0, 0x0, 0x0, 0x0, 0x0, 0x0, 0x0, 0x0, 0x0, 0x0, 0x3c000000, 0x0, 0x0, 0x0, 0x3c003c00,
  0x0, 0x0, 0x3c000000, 0x3c003c00, 0x0, 0x0, 0x3c003c00, 0x3c003c00, 0x0, 0x3c000000, 0x3c003c00, 0x3c003c00, 0x0, 0x3c003c00, 0x3c003c00, 0x3c00,
  0x3c000000, 0x3c003c00, 0x3c003c00, 0x0, 0x3c003c00, 0x3c003c00, 0x3c00, 0x0, 0x3c003c00, 0x3c003c00, 0x0, 0x0, 0x3c003c00, 0x3c00, 0x0, 0x0,
  0x0, 0x0, 0x3c000000, 0x3c003c00, 0x0, 0x0, 0x3c003c00, 0x3c003c00, 0x0, 0x3c000000, 0x3c003c00, 0x3c003c00, 0x0, 0x3c003c00, 0x3c003c00, 0x3c00,
  0x3c000000, 0x3c003c00, 0x3c003c00, 0x0, 0x3c003c00, 0x3c003c00, 0x3c00, 0x0, 0x3c003c00, 0x3c003c00, 0x0, 0x0, 0x3c003c00, 0x3c00, 0x0, 0x0,
  0x3c003c00, 0x0, 0x0, 0x0, 0x3c00, 0x0, 0x0, 0x0, 0x0, 0x0, 0x0, 0x0, 0x0, 0x0, 0x0, 0x0,
  0x0, 0x0, 0x0, 0x0, 0x0, 0x0, 0x0, 0x0, 0x0, 0x0, 0x0, 0x0, 0x0, 0x0, 0x0, 0x0,
  0x3c003c00, 0x0, 0x0, 0x0, 0x3c00, 0x0, 0x0, 0x0, 0x0, 0x0, 0x0, 0x0, 0x0, 0x0, 0x0, 0x0,
  0x0, 0x0, 0x0, 0x0, 0x0, 0x0, 0x0, 0x0, 0x0, 0x0, 0x0, 0x0, 0x0, 0x0, 0x0, 0x0,
  0x0, 0x0, 0x0, 0x0, 0x0, 0x0, 0x0, 0x0, 0x0, 0x0, 0x0, 0x0, 0x0, 0x0, 0x0, 0x0,
  0x0, 0x0, 0x0, 0x0, 0x0, 0x0, 0x0, 0x0, 0x0, 0x0, 0x0, 0x0, 0x0, 0x0, 0x0, 0x0,
  0x0, 0x0, 0x0, 0x0, 0x0, 0x0, 0x0, 0x0, 0x0, 0x0, 0x0, 0x0, 0x0, 0x0, 0x0, 0x0,
  0x0, 0x0, 0x0, 0x0, 0x0, 0x0, 0x0, 0x0, 0x0, 0x0, 0x0, 0x0, 0x0, 0x0, 0x0, 0x0,
  0x0, 0x0, 0x0, 0x0, 0x0, 0x0, 0x0, 0x0, 0x0, 0x0, 0x0, 0x0, 0x0, 0x0, 0x0, 0x0,
  0x0, 0x0, 0x0, 0x0, 0x0, 0x0, 0x0, 0x3c000000, 0x0, 0x0, 0x0, 0x3c003c00, 0x0, 0x0, 0x3c000000, 0x3c003c00,
  0x0, 0x0, 0x0, 0x0, 0x0, 0x0, 0x0, 0x0, 0x0, 0x0, 0x0, 0x0, 0x0, 0x0, 0x0, 0x0,
  0x0, 0x0, 0x0, 0x0, 0x0, 0x0, 0x0, 0x3c000000, 0x0, 0x0, 0x0, 0x3c003c00, 0x0, 0x0, 0x3c000000, 0x3c003c00,
  0x0, 0x0, 0x3c003c00, 0x3c003c00, 0x0, 0x3c000000, 0x3c003c00, 0x3c003c00, 0x0, 0x3c003c00, 0x3c003c00, 0x3c003c00, 0x3c000000, 0x3c003c00, 0x3c003c00, 0x3c003c00,
  0x3c003c00, 0x3c003c00, 0x3c003c00, 0x3c00, 0x3c003c00, 0x3c003c00, 0x3c003c00, 0x0, 0x3c003c00, 0x3c003c00, 0x3c00, 0x0, 0x3c003c00, 0x3c003c00, 0x0, 0x0,
  0x0, 0x0, 0x3c003c00, 0x3c003c00, 0x0, 0x3c000000, 0x3c003c00, 0x3c003c00, 0x0, 0x3c003c00, 0x3c003c00, 0x3c003c00, 0x3c000000, 0x3c003c00, 0x3c003c00, 0x3c003c00,
  0x3c003c00, 0x3c003c00, 0x3c003c00, 0x3c00, 0x3c003c00, 0x3c003c00, 0x3c003c00, 0x0, 0x3c003c00, 0x3c003c00, 0x3c00, 0x0, 0x3c003c00, 0x3c003c00, 0x0, 0x0,
  0x3c003c00, 0x3c00, 0x0, 0x0, 0x3c003c00, 0x0, 0x0, 0x0, 0x3c00, 0x0, 0x0, 0x0, 0x0, 0x0, 0x0, 0x0,
  0x0, 0x0, 0x0, 0x0, 0x0, 0x0, 0x0, 0x0, 0x0, 0x0, 0x0, 0x0, 0x0, 0x0, 0x0, 0x0,
  0x3c003c00, 0x3c00, 0x0, 0x0, 0x3c003c00, 0x0, 0x0, 0x0, 0x3c00, 0x0, 0x0, 0x0, 0x0, 0x0, 0x0, 0x0,
  0x0, 0x0, 0x0, 0x0, 0x0, 0x0, 0x0, 0x0, 0x0, 0x0, 0x0, 0x0, 0x0, 0x0, 0x0, 0x0,
  0x0, 0x0, 0x0, 0x0, 0x0, 0x0, 0x0, 0x0, 0x0, 0x0, 0x0, 0x0, 0x0, 0x0, 0x0, 0x0,
  0x0, 0x0, 0x0, 0x0, 0x0, 0x0, 0x0, 0x0, 0x0, 0x0, 0x0, 0x0, 0x0, 0x0, 0x0, 0x0,
  0x0, 0x0, 0x0, 0x0, 0x0, 0x0, 0x0, 0x0, 0x0, 0x0, 0x0, 0x0, 0x0, 0x0, 0x0, 0x0,
  0x0, 0x0, 0x0, 0x0, 0x0, 0x0, 0x0, 0x0, 0x0, 0x0, 0x0, 0x0, 0x0, 0x0, 0x0, 0x0,
  0x0, 0x0, 0x0, 0x0, 0x0, 0x0, 0x0, 0x0, 0x0, 0x0, 0x0, 0x0, 0x0, 0x0, 0x0, 0x0,
  0x0, 0x0, 0x0, 0x3c000000, 0x0, 0x0, 0x0, 0x3c003c00, 0x0, 0x0, 0x3c000000, 0x3c003c00, 0x0, 0x0, 0x3c003c00, 0x3c003c00,
  0x0, 0x0, 0x0, 0x0, 0x0, 0x0, 0x0, 0x0, 0x0, 0x0, 0x0, 0x0, 0x0, 0x0, 0x0, 0x0,
  0x0, 0x0, 0x0, 0x3c000000, 0x0, 0x0, 0x0, 0x3c003c00, 0x0, 0x0, 0x3c000000, 0x3c003c00, 0x0, 0x0, 0x3c003c00, 0x3c003c00,
  0x0, 0x3c000000, 0x3c003c00, 0x3c003c00, 0x0, 0x3c003c00, 0x3c003c00, 0x3c003c00, 0x3c000000, 0x3c003c00, 0x3c003c00, 0x3c003c00, 0x3c003c00, 0x3c003c00, 0x3c003c00, 0x3c003c00,
  0x3c003c00, 0x3c003c00, 0x3c003c00, 0x3c003c00, 0x3c003c00, 0x3c003c00, 0x3c003c00, 0x3c00, 0x3c003c00, 0x3c003c00, 0x3c003c00, 0x0, 0x3c003c00, 0x3c003c00, 0x3c00, 0x0,
  0x0, 0x3c000000, 0x3c003c00, 0x3c003c00, 0x0, 0x3c003c00, 0x3c003c00, 0x3c003c00, 0x3c000000, 0x3c003c00, 0x3c003c00, 0x3c003c00, 0x3c003c00, 0x3c003c00, 0x3c003c00, 0x3c003c00,
  0x3c003c00, 0x3c003c00, 0x3c003c00, 0x3c003c00, 0x3c003c00, 0x3c003c00, 0x3c003c00, 0x3c00, 0x3c003c00, 0x3c003c00, 0x3c003c00, 0x0, 0x3c003c00, 0x3c003c00, 0x3c00, 0x0,
  0x3c003c00, 0x3c003c00, 0x0, 0x0, 0x3c003c00, 0x3c00, 0x0, 0x0, 0x3c003c00, 0x0, 0x0, 0x0, 0x3c00, 0x0, 0x0, 0x0,
  0x0, 0x0, 0x0, 0x0, 0x0, 0x0, 0x0, 0x0, 0x0, 0x0, 0x0, 0x0, 0x0, 0x0, 0x0, 0x0,
  0x3c003c00, 0x3c003c00, 0x0, 0x0, 0x3c003c00, 0x3c00, 0x0, 0x0, 0x3c003c00, 0x0, 0x0, 0x0, 0x3c00, 0x0, 0x0, 0x0,
  0x0, 0x0, 0x0, 0x0, 0x0, 0x0, 0x0, 0x0, 0x0, 0x0, 0x0, 0x0, 0x0, 0x0, 0x0, 0x0,
  0x0, 0x0, 0x0, 0x0, 0x0, 0x0, 0x0, 0x0, 0x0, 0x0, 0x0, 0x0, 0x0, 0x0, 0x0, 0x0,
  0x0, 0x0, 0x0, 0x0, 0x0, 0x0, 0x0, 0x0, 0x0, 0x0, 0x0, 0x0, 0x0, 0x0, 0x0, 0x0,
  0x0, 0x0, 0x0, 0x0, 0x0, 0x0, 0x0, 0x0, 0x0, 0x0, 0x0, 0x0, 0x0, 0x0, 0x0, 0x0,
  0x0, 0x0, 0x0, 0x0, 0x0, 0x0, 0x0, 0x0, 0x0, 0x0, 0x0, 0x0, 0x0, 0x0, 0x0, 0x0,
  0x0, 0x0, 0x0, 0x0, 0x0, 0x0, 0x0, 0x0, 0x0, 0x0, 0x0, 0x0, 0x0, 0x0, 0x0, 0xffff0000,
  0x0, 0x0, 0x0, 0xffffffff, 0x0, 0x0, 0xffff0000, 0xffff, 0x0, 0x0, 0xffffffff, 0x0, 0x0, 0xffff0000, 0xffff, 0x0,
  0x0, 0x0, 0x0, 0x0, 0x0, 0x0, 0x0, 0x0, 0x0, 0x0, 0x0, 0x0, 0x0, 0x0, 0x0, 0xffff0000,
  0x0, 0x0, 0x0, 0xffffffff, 0x0, 0x0, 0xffff0000, 0xffff, 0x0, 0x0, 0xffffffff, 0x0, 0x0, 0xffff0000, 0xffff, 0x0,
  0x0, 0xffffffff, 0x0, 0x0, 0xffff0000, 0xffff, 0x0, 0x0, 0xffffffff, 0x0, 0x0, 0x0, 0xffff, 0x0, 0x0, 0x0,
  0x0, 0x0, 0x0, 0x0, 0x0, 0x0, 0x0, 0x0, 0x0, 0x0, 0x0, 0x0, 0x0, 0x0, 0x0, 0x0,
  0x0, 0xffffffff, 0x0, 0x0, 0xffff0000, 0xffff, 0x0, 0x0, 0xffffffff, 0x0, 0x0, 0x0, 0xffff, 0x0, 0x0, 0x0,
  0x0, 0x0, 0x0, 0x0, 0x0, 0x0, 0x0, 0x0, 0x0, 0x0, 0x0, 0x0, 0x0, 0x0, 0x0, 0x0,
  0x0, 0x0, 0x0, 0x0, 0x0, 0x0, 0x0, 0x0, 0x0, 0x0, 0x0, 0x0, 0x0, 0x0, 0x0, 0x0,
  0x0, 0x0, 0x0, 0x0, 0x0, 0x0, 0x0, 0x0, 0x0, 0x0, 0x0, 0x0, 0x0, 0x0, 0x0, 0x0,
  0x0, 0x0, 0x0, 0x0, 0x0, 0x0, 0x0, 0x0, 0x0, 0x0, 0x0, 0x0, 0x0, 0x0, 0x0, 0x0,
  0x0, 0x0, 0x0, 0x0, 0x0, 0x0, 0x0, 0x0, 0x0, 0x0, 0x0, 0x0, 0x0, 0x0, 0x0, 0x0,
  0x0, 0x0, 0x0, 0x0, 0x0, 0x0, 0x0, 0x0, 0x0, 0x0, 0x0, 0x0, 0x0, 0x0, 0x0, 0x0,
  0x0, 0x0, 0x0, 0x0, 0x0, 0x0, 0x0, 0x0, 0x0, 0x0, 0x0, 0x0, 0x0, 0x0, 0x0, 0x0,
  0x16520, 0x3020100, 0x7060504, 0x3020100, 0x16620, 0x3020100, 0x7060504, 0x3020100, 0x16530, 0x3020100, 0x7060504, 0x3020100, 0x16630, 0x3020100, 0x7060504, 0x3020100,
  0x16540, 0x3020100, 0x7060504, 0x3020100, 0x16640, 0x3020100, 0x7060504, 0x3020100, 0x16550, 0x3020100, 0x7060504, 0x3020100, 0x16650, 0x3020100, 0x7060504, 0x3020100,
  0x16560, 0x3020100, 0x7060504, 0x3020100, 0x16660, 0x3020100, 0x7060504, 0x3020100, 0x16570, 0x3020100, 0x7060504, 0x3020100, 0x16670, 0x3020100, 0x7060504, 0x3020100,
  0x16600, 0x3020100, 0x7060504, 0x3020100, 0x16700, 0x3020100, 0x7060504, 0x3020100, 0x16610, 0x3020100, 0x7060504, 0x3020100, 0x16710, 0x3020100, 0x7060504, 0x3020100,
  0x16528, 0x3020100, 0x7060504, 0x3020100, 0x16628, 0x3020100, 0x7060504, 0x3020100, 0x16538, 0x3020100, 0x7060504, 0x3020100, 0x16638, 0x3020100, 0x7060504, 0x3020100,
  0x16548, 0x3020100, 0x7060504, 0x3020100, 0x16648, 0x3020100, 0x7060504, 0x3020100, 0x16558, 0x3020100, 0x7060504, 0x3020100, 0x16658, 0x3020100, 0x7060504, 0x3020100,
  0x16568, 0x3020100, 0x7060504, 0x3020100, 0x16668, 0x3020100, 0x7060504, 0x3020100, 0x16578, 0x3020100, 0x7060504, 0x3020100, 0x16678, 0x3020100, 0x7060504, 0x3020100,
  0x16608, 0x3020100, 0x7060504, 0x3020100, 0x16708, 0x3020100, 0x7060504, 0x3020100, 0x16618, 0x3020100, 0x7060504, 0x3020100, 0x16718, 0x3020100, 0x7060504, 0x3020100,
  0x16720, 0x1000100, 0x5040302, 0x5040100, 0x16820, 0x1000100, 0x5040302, 0x5040100, 0x16730, 0x1000100, 0x5040302, 0x5040100, 0x16830, 0x1000100, 0x5040302, 0x5040100,
  0x16740, 0x1000100, 0x5040302, 0x5040100, 0x16840, 0x1000100, 0x5040302, 0x5040100, 0x16750, 0x1000100, 0x5040302, 0x5040100, 0x16850, 0x1000100, 0x5040302, 0x5040100,
  0x16760, 0x1000100, 0x5040302, 0x5040100, 0x16860, 0x1000100, 0x5040302, 0x5040100, 0x16770, 0x1000100, 0x5040302, 0x5040100, 0x16870, 0x1000100, 0x5040302, 0x5040100,
  0x16800, 0x1000100, 0x5040302, 0x5040100, 0x16900, 0x1000100, 0x5040302, 0x5040100, 0x16810, 0x1000100, 0x5040302, 0x5040100, 0x16910, 0x1000100, 0x5040302, 0x5040100,
  0x16920, 0x5040504, 0xc0c0504, 0x3020100, 0x16cc8, 0x3020100, 0x7060504, 0x3020100, 0x16cd0, 0x3020100, 0x7060504, 0x3020100, 0x16cd8, 0x3020100, 0x7060504, 0x3020100,
  0x16ce0, 0x3020100, 0x7060504, 0x3020100, 0x16ce8, 0x3020100, 0x7060504, 0x3020100, 0x16cf0, 0x3020100, 0x7060504, 0x3020100, 0x16cf8, 0x3020100, 0x7060504, 0x3020100,
  0x16d00, 0x3020100, 0x7060504, 0x3020100, 0x16d08, 0x3020100, 0x7060504, 0x3020100, 0x16d10, 0x3020100, 0x7060504, 0x3020100, 0x16d18, 0x3020100, 0x7060504, 0x3020100,
  0x16d20, 0x3020100, 0x7060504, 0x3020100, 0x16d28, 0x3020100, 0x7060504, 0x3020100, 0x16d30, 0x3020100, 0x7060504, 0x3020100, 0x16d38, 0x3020100, 0x7060504, 0x3020100,
  0x16d5e, 0x16d60, 0x16d62, 0x16d64, 0x16e5e, 0x16e60, 0x16e62, 0x16e64, 0x16d6e, 0x16d70, 0x16d72, 0x16d74, 0x16e6e, 0x16e70, 0x16e72, 0x16e74,
  0x16d7e, 0x16d80, 0x16d82, 0x16d84, 0x16e7e, 0x16e80, 0x16e82, 0x16e84, 0x16d8e, 0x16d90, 0x16d92, 0x16d94, 0x16e8e, 0x16e90, 0x16e92, 0x16e94,
  0x16d9e, 0x16da0, 0x16da2, 0x16da4, 0x16e9e, 0x16ea0, 0x16ea2, 0x16ea4, 0x16dae, 0x16db0, 0x16db2, 0x16db4, 0x16eae, 0x16eb0, 0x16eb2, 0x16eb4,
  0x16e3e, 0x16e40, 0x16e42, 0x16e44, 0x16f3e, 0x16f40, 0x16f42, 0x16f44, 0x16e4e, 0x16e50, 0x16e52, 0x16e54, 0x16f4e, 0x16f50, 0x16f52, 0x16f54,
  0x16d66, 0x16d68, 0x16d6a, 0x16d6c, 0x16e66, 0x16e68, 0x16e6a, 0x16e6c, 0x16d76, 0x16d78, 0x16d7a, 0x16d7c, 0x16e76, 0x16e78, 0x16e7a, 0x16e7c,
  0x16d86, 0x16d88, 0x16d8a, 0x16d8c, 0x16e86, 0x16e88, 0x16e8a, 0x16e8c, 0x16d96, 0x16d98, 0x16d9a, 0x16d9c, 0x16e96, 0x16e98, 0x16e9a, 0x16e9c,
  0x16da6, 0x16da8, 0x16daa, 0x16dac, 0x16ea6, 0x16ea8, 0x16eaa, 0x16eac, 0x16db6, 0x16db8, 0x16dba, 0x16dbc, 0x16eb6, 0x16eb8, 0x16eba, 0x16ebc,
  0x16e46, 0x16e48, 0x16e4a, 0x16e4c, 0x16f46, 0x16f48, 0x16f4a, 0x16f4c, 0x16e56, 0x16e58, 0x16e5a, 0x16e5c, 0x16f56, 0x16f58, 0x16f5a, 0x16f5c,
  0x16cc0, 0x16728, 0x1672a, 0x1672c, 0x1681e, 0x16828, 0x1682a, 0x1682c, 0x1672e, 0x16738, 0x1673a, 0x1673c, 0x1682e, 0x16838, 0x1683a, 0x1683c,
  0x1673e, 0x16748, 0x1674a, 0x1674c, 0x1683e, 0x16848, 0x1684a, 0x1684c, 0x1674e, 0x16758, 0x1675a, 0x1675c, 0x1684e, 0x16858, 0x1685a, 0x1685c,
  0x1675e, 0x16768, 0x1676a, 0x1676c, 0x1685e, 0x16868, 0x1686a, 0x1686c, 0x1676e, 0x16778, 0x1677a, 0x1677c, 0x1686e, 0x16878, 0x1687a, 0x1687c,
  0x167fe, 0x16808, 0x1680a, 0x1680c, 0x168fe, 0x16908, 0x1690a, 0x1690c, 0x1680e, 0x16818, 0x1681a, 0x1681c, 0x1690e, 0x16918, 0x1691a, 0x1691c,
  0x169c0, 0x16a00, 0x1691e, 0x16afe, 0x169e0, 0x16a20, 0x16ade, 0x16b1e, 0x169c2, 0x16a02, 0x16ac0, 0x16b00, 0x169e2, 0x16a22, 0x16ae0, 0x16b20,
  0x169c4, 0x16a04, 0x16ac2, 0x16b02, 0x169e4, 0x16a24, 0x16ae2, 0x16b22, 0x169c6, 0x16a06, 0x16ac4, 0x16b04, 0x169e6, 0x16a26, 0x16ae4, 0x16b24,
  0x169c8, 0x16a08, 0x16ac6, 0x16b06, 0x169e8, 0x16a28, 0x16ae6, 0x16b26, 0x169ca, 0x16a0a, 0x16ac8, 0x16b08, 0x169ea, 0x16a2a, 0x16ae8, 0x16b28,
  0x169dc, 0x16a1c, 0x16ada, 0x16b1a, 0x169fc, 0x16a3c, 0x16afa, 0x16b3a, 0x169de, 0x16a1e, 0x16adc, 0x16b1c, 0x169fe, 0x16a3e, 0x16afc, 0x16b3c,
  0x16b3e, 0x16bfe, 0x16c3e, 0x16c7e, 0x16bde, 0x16c1e, 0x16c5e, 0x16c9e, 0x16bc0, 0x16c00, 0x16c40, 0x16c80, 0x16be0, 0x16c20, 0x16c60, 0x16ca0,
  0x16bc2, 0x16c02, 0x16c42, 0x16c82, 0x16be2, 0x16c22, 0x16c62, 0x16ca2, 0x16bc4, 0x16c04, 0x16c44, 0x16c84, 0x16be4, 0x16c24, 0x16c64, 0x16ca4,
  0x16bc6, 0x16c06, 0x16c46, 0x16c86, 0x16be6, 0x16c26, 0x16c66, 0x16ca6, 0x16bc8, 0x16c08, 0x16c48, 0x16c88, 0x16be8, 0x16c28, 0x16c68, 0x16ca8,
  0x16bda, 0x16c1a, 0x16c5a, 0x16c9a, 0x16bfa, 0x16c3a, 0x16c7a, 0x16cba, 0x16bdc, 0x16c1c, 0x16c5c, 0x16c9c, 0x16bfc, 0x16c3c, 0x16c7c, 0x16cbc,
  0x16cbe, 0x16cc0, 0x16cc0, 0x16cc0, 0x16cc8, 0x16cc8, 0x16cc8, 0x16cc8, 0x16cd0, 0x16cd0, 0x16cd0, 0x16cd0, 0x16cd8, 0x16cd8, 0x16cd8, 0x16cd8,
  0x16ce0, 0x16ce0, 0x16ce0, 0x16ce0, 0x16ce8, 0x16ce8, 0x16ce8, 0x16ce8, 0x16cf0, 0x16cf0, 0x16cf0, 0x16cf0, 0x16cf8, 0x16cf8, 0x16cf8, 0x16cf8,
  0x16d00, 0x16d00, 0x16d00, 0x16d00, 0x16d08, 0x16d08, 0x16d08, 0x16d08, 0x16d10, 0x16d10, 0x16d10, 0x16d10, 0x16d18, 0x16d18, 0x16d18, 0x16d18,
  0x16d20, 0x16d20, 0x16d20, 0x16d20, 0x16d28, 0x16d28, 0x16d28, 0x16d28, 0x16d30, 0x16d30, 0x16d30, 0x16d30, 0x16d38, 0x16d38, 0x16d38, 0x16d38,
  0x16cc0, 0x16cc0, 0x16cc0, 0x16cc0, 0x16cc8, 0x16cc8, 0x16cc8, 0x16cc8, 0x16cd0, 0x16cd0, 0x16cd0, 0x16cd0, 0x16cd8, 0x16cd8, 0x16cd8, 0x16cd8,
  0x16ce0, 0x16ce0, 0x16ce0, 0x16ce0, 0x16ce8, 0x16ce8, 0x16ce8, 0x16ce8, 0x16cf0, 0x16cf0, 0x16cf0, 0x16cf0, 0x16cf8, 0x16cf8, 0x16cf8, 0x16cf8,
  0x16d00, 0x16d00, 0x16d00, 0x16d00, 0x16d08, 0x16d08, 0x16d08, 0x16d08, 0x16d10, 0x16d10, 0x16d10, 0x16d10, 0x16d18, 0x16d18, 0x16d18, 0x16d18,
  0x16d20, 0x16d20, 0x16d20, 0x16d20, 0x16d28, 0x16d28, 0x16d28, 0x16d28, 0x16d30, 0x16d30, 0x16d30, 0x16d30, 0x16d38, 0x16d38, 0x16d38, 0x16d38,
  0x16cc0, 0x16cc0, 0x16cc0, 0x16cc0, 0x16cc8, 0x16cc8, 0x16cc8, 0x16cc8, 0x16cd0, 0x16cd0, 0x16cd0, 0x16cd0, 0x16cd8, 0x16cd8, 0x16cd8, 0x16cd8,
  0x16ce0, 0x16ce0, 0x16ce0, 0x16ce0, 0x16ce8, 0x16ce8, 0x16ce8, 0x16ce8, 0x16cf0, 0x16cf0, 0x16cf0, 0x16cf0, 0x16cf8, 0x16cf8, 0x16cf8, 0x16cf8,
  0x16d00, 0x16d00, 0x16d00, 0x16d00, 0x16d08, 0x16d08, 0x16d08, 0x16d08, 0x16d10, 0x16d10, 0x16d10, 0x16d10, 0x16d18, 0x16d18, 0x16d18, 0x16d18,
  0x16d20, 0x16d20, 0x16d20, 0x16d20, 0x16d28, 0x16d28, 0x16d28, 0x16d28, 0x16d30, 0x16d30, 0x16d30, 0x16d30, 0x16d38, 0x16d38, 0x16d38, 0x16d38,
  0x16cc0, 0x16cc0, 0x3020100, 0x16500, 0x16cc8, 0x16cc8, 0x3020100, 0x16500, 0x16cd0, 0x16cd0, 0x3020100, 0x16500, 0x16cd8, 0x16cd8, 0x3020100, 0x16500,
  0x16ce0, 0x16ce0, 0x3020100, 0x16500, 0x16ce8, 0x16ce8, 0x3020100, 0x16500, 0x16cf0, 0x16cf0, 0x3020100, 0x16500, 0x16cf8, 0x16cf8, 0x3020100, 0x16500,
  0x16d00, 0x16d00, 0x3020100, 0x16500, 0x16d08, 0x16d08, 0x3020100, 0x16500, 0x16d10, 0x16d10, 0x3020100, 0x16500, 0x16d18, 0x16d18, 0x3020100, 0x16500,
  0x16d20, 0x16d20, 0x3020100, 0x16500, 0x16d28, 0x16d28, 0x3020100, 0x16500, 0x16d30, 0x16d30, 0x3020100, 0x16500, 0x16d38, 0x16d38, 0x3020100, 0x16500,
  0x16cc0, 0x16cc0, 0x3020100, 0x16500, 0x16cc8, 0x16cc8, 0x3020100, 0x16500, 0x16cd0, 0x16cd0, 0x3020100, 0x16500, 0x16cd8, 0x16cd8, 0x3020100, 0x16500,
  0x16ce0, 0x16ce0, 0x3020100, 0x16500, 0x16ce8, 0x16ce8, 0x3020100, 0x16500, 0x16cf0, 0x16cf0, 0x3020100, 0x16500, 0x16cf8, 0x16cf8, 0x3020100, 0x16500,
  0x16d00, 0x16d00, 0x3020100, 0x16500, 0x16d08, 0x16d08, 0x3020100, 0x16500, 0x16d10, 0x16d10, 0x3020100, 0x16500, 0x16d18, 0x16d18, 0x3020100, 0x16500,
  0x16d20, 0x16d20, 0x3020100, 0x16500, 0x16d28, 0x16d28, 0x3020100, 0x16500, 0x16d30, 0x16d30, 0x3020100, 0x16500, 0x16d38, 0x16d38, 0x3020100, 0x16500,
  0x16f5c, 0x16f64, 0x3020100, 0x16500, 0x1705c, 0x17064, 0x3020100, 0x16500, 0x16f6c, 0x16f74, 0x3020100, 0x16500, 0x1706c, 0x17074, 0x3020100, 0x16500,
  0x16f7c, 0x16f84, 0x3020100, 0x16500, 0x1707c, 0x17084, 0x3020100, 0x16500, 0x16f8c, 0x16f94, 0x3020100, 0x16500, 0x1708c, 0x17094, 0x3020100, 0x16500,
  0x16f9c, 0x16fa4, 0x3020100, 0x16500, 0x1709c, 0x170a4, 0x3020100, 0x16500, 0x16fac, 0x16fb4, 0x3020100, 0x16500, 0x170ac, 0x170b4, 0x3020100, 0x16500,
  0x1703c, 0x17044, 0x3020100, 0x16500, 0x1713c, 0x17144, 0x3020100, 0x16500, 0x1704c, 0x17054, 0x3020100, 0x16500, 0x1714c, 0x17154, 0x3020100, 0x16500,
  0x1715c, 0x16cc0, 0x5040504, 0x16500, 0x16cc8, 0x16cc8, 0x3020100, 0x16500, 0x16cd0, 0x16cd0, 0x3020100, 0x16500, 0x16cd8, 0x16cd8, 0x3020100, 0x16500,
  0x16ce0, 0x16ce0, 0x3020100, 0x16500, 0x16ce8, 0x16ce8, 0x3020100, 0x16500, 0x16cf0, 0x16cf0, 0x3020100, 0x16500, 0x16cf8, 0x16cf8, 0x3020100, 0x16500,
  0x16d00, 0x16d00, 0x3020100, 0x16500, 0x16d08, 0x16d08, 0x3020100, 0x16500, 0x16d10, 0x16d10, 0x3020100, 0x16500, 0x16d18, 0x16d18, 0x3020100, 0x16500,
  0x16d20, 0x16d20, 0x3020100, 0x16500, 0x16d28, 0x16d28, 0x3020100, 0x16500, 0x16d30, 0x16d30, 0x3020100, 0x16500, 0x16d38, 0x16d38, 0x3020100, 0x16500,
  0x16580, 0x3020100, 0x7060504, 0x3020100, 0x16680, 0x3020100, 0x7060504, 0x3020100, 0x16590, 0x3020100, 0x7060504, 0x3020100, 0x16690, 0x3020100, 0x7060504, 0x3020100,
  0x165a0, 0x3020100, 0x7060504, 0x3020100, 0x166a0, 0x3020100, 0x7060504, 0x3020100, 0x165b0, 0x3020100, 0x7060504, 0x3020100, 0x166b0, 0x3020100, 0x7060504, 0x3020100,
  0x165c0, 0x3020100, 0x7060504, 0x3020100, 0x166c0, 0x3020100, 0x7060504, 0x3020100, 0x165d0, 0x3020100, 0x7060504, 0x3020100, 0x166d0, 0x3020100, 0x7060504, 0x3020100,
  0x165e0, 0x3020100, 0x7060504, 0x3020100, 0x166e0, 0x3020100, 0x7060504, 0x3020100, 0x165f0, 0x3020100, 0x7060504, 0x3020100, 0x166f0, 0x3020100, 0x7060504, 0x3020100,
  0x16588, 0x3020100, 0x7060504, 0x3020100, 0x16688, 0x3020100, 0x7060504, 0x3020100, 0x16598, 0x3020100, 0x7060504, 0x3020100, 0x16698, 0x3020100, 0x7060504, 0x3020100,
  0x165a8, 0x3020100, 0x7060504, 0x3020100, 0x166a8, 0x3020100, 0x7060504, 0x3020100, 0x165b8, 0x3020100, 0x7060504, 0x3020100, 0x166b8, 0x3020100, 0x7060504, 0x3020100,
  0x165c8, 0x3020100, 0x7060504, 0x3020100, 0x166c8, 0x3020100, 0x7060504, 0x3020100, 0x165d8, 0x3020100, 0x7060504, 0x3020100, 0x166d8, 0x3020100, 0x7060504, 0x3020100,
  0x165e8, 0x3020100, 0x7060504, 0x3020100, 0x166e8, 0x3020100, 0x7060504, 0x3020100, 0x165f8, 0x3020100, 0x7060504, 0x3020100, 0x166f8, 0x3020100, 0x7060504, 0x3020100,
  0x16780, 0x1000100, 0x5040302, 0x5040100, 0x16880, 0x1000100, 0x5040302, 0x5040100, 0x16790, 0x1000100, 0x5040302, 0x5040100, 0x16890, 0x1000100, 0x5040302, 0x5040100,
  0x167a0, 0x1000100, 0x5040302, 0x5040100, 0x168a0, 0x1000100, 0x5040302, 0x5040100, 0x167b0, 0x1000100, 0x5040302, 0x5040100, 0x168b0, 0x1000100, 0x5040302, 0x5040100,
  0x167c0, 0x1000100, 0x5040302, 0x5040100, 0x168c0, 0x1000100, 0x5040302, 0x5040100, 0x167d0, 0x1000100, 0x5040302, 0x5040100, 0x168d0, 0x1000100, 0x5040302, 0x5040100,
  0x167e0, 0x1000100, 0x5040302, 0x5040100, 0x168e0, 0x1000100, 0x5040302, 0x5040100, 0x167f0, 0x1000100, 0x5040302, 0x5040100, 0x168f0, 0x1000100, 0x5040302, 0x5040100,
  0x16cc0, 0x3020100, 0x7060504, 0x3020100, 0x16cc8, 0x3020100, 0x7060504, 0x3020100, 0x16cd0, 0x3020100, 0x7060504, 0x3020100, 0x16cd8, 0x3020100, 0x7060504, 0x3020100,
  0x16ce0, 0x3020100, 0x7060504, 0x3020100, 0x16ce8, 0x3020100, 0x7060504, 0x3020100, 0x16cf0, 0x3020100, 0x7060504, 0x3020100, 0x16cf8, 0x3020100, 0x7060504, 0x3020100,
  0x16d00, 0x3020100, 0x7060504, 0x3020100, 0x16d08, 0x3020100, 0x7060504, 0x3020100, 0x16d10, 0x3020100, 0x7060504, 0x3020100, 0x16d18, 0x3020100, 0x7060504, 0x3020100,
  0x16d20, 0x3020100, 0x7060504, 0x3020100, 0x16d28, 0x3020100, 0x7060504, 0x3020100, 0x16d30, 0x3020100, 0x7060504, 0x3020100, 0x16d38, 0x3020100, 0x7060504, 0x3020100,
  0x16dbe, 0x16dc0, 0x16dc2, 0x16dc4, 0x16ebe, 0x16ec0, 0x16ec2, 0x16ec4, 0x16dce, 0x16dd0, 0x16dd2, 0x16dd4, 0x16ece, 0x16ed0, 0x16ed2, 0x16ed4,
  0x16dde, 0x16de0, 0x16de2, 0x16de4, 0x16ede, 0x16ee0, 0x16ee2, 0x16ee4, 0x16dee, 0x16df0, 0x16df2, 0x16df4, 0x16eee, 0x16ef0, 0x16ef2, 0x16ef4,
  0x16dfe, 0x16e00, 0x16e02, 0x16e04, 0x16efe, 0x16f00, 0x16f02, 0x16f04, 0x16e0e, 0x16e10, 0x16e12, 0x16e14, 0x16f0e, 0x16f10, 0x16f12, 0x16f14,
  0x16e1e, 0x16e20, 0x16e22, 0x16e24, 0x16f1e, 0x16f20, 0x16f22, 0x16f24, 0x16e2e, 0x16e30, 0x16e32, 0x16e34, 0x16f2e, 0x16f30, 0x16f32, 0x16f34,
  0x16dc6, 0x16dc8, 0x16dca, 0x16dcc, 0x16ec6, 0x16ec8, 0x16eca, 0x16ecc, 0x16dd6, 0x16dd8, 0x16dda, 0x16ddc, 0x16ed6, 0x16ed8, 0x16eda, 0x16edc,
  0x16de6, 0x16de8, 0x16dea, 0x16dec, 0x16ee6, 0x16ee8, 0x16eea, 0x16eec, 0x16df6, 0x16df8, 0x16dfa, 0x16dfc, 0x16ef6, 0x16ef8, 0x16efa, 0x16efc,
  0x16e06, 0x16e08, 0x16e0a, 0x16e0c, 0x16f06, 0x16f08, 0x16f0a, 0x16f0c, 0x16e16, 0x16e18, 0x16e1a, 0x16e1c, 0x16f16, 0x16f18, 0x16f1a, 0x16f1c,
  0x16e26, 0x16e28, 0x16e2a, 0x16e2c, 0x16f26, 0x16f28, 0x16f2a, 0x16f2c, 0x16e36, 0x16e38, 0x16e3a, 0x16e3c, 0x16f36, 0x16f38, 0x16f3a, 0x16f3c,
  0x1677e, 0x16788, 0x1678a, 0x1678c, 0x1687e, 0x16888, 0x1688a, 0x1688c, 0x1678e, 0x16798, 0x1679a, 0x1679c, 0x1688e, 0x16898, 0x1689a, 0x1689c,
  0x1679e, 0x167a8, 0x167aa, 0x167ac, 0x1689e, 0x168a8, 0x168aa, 0x168ac, 0x167ae, 0x167b8, 0x167ba, 0x167bc, 0x168ae, 0x168b8, 0x168ba, 0x168bc,
  0x167be, 0x167c8, 0x167ca, 0x167cc, 0x168be, 0x168c8, 0x168ca, 0x168cc, 0x167ce, 0x167d8, 0x167da, 0x167dc, 0x168ce, 0x168d8, 0x168da, 0x168dc,
  0x167de, 0x167e8, 0x167ea, 0x167ec, 0x168de, 0x168e8, 0x168ea, 0x168ec, 0x167ee, 0x167f8, 0x167fa, 0x167fc, 0x168ee, 0x168f8, 0x168fa, 0x168fc,
  0x169cc, 0x16a0c, 0x16aca, 0x16b0a, 0x169ec, 0x16a2c, 0x16aea, 0x16b2a, 0x169ce, 0x16a0e, 0x16acc, 0x16b0c, 0x169ee, 0x16a2e, 0x16aec, 0x16b2c,
  0x169d0, 0x16a10, 0x16ace, 0x16b0e, 0x169f0, 0x16a30, 0x16aee, 0x16b2e, 0x169d2, 0x16a12, 0x16ad0, 0x16b10, 0x169f2, 0x16a32, 0x16af0, 0x16b30,
  0x169d4, 0x16a14, 0x16ad2, 0x16b12, 0x169f4, 0x16a34, 0x16af2, 0x16b32, 0x169d6, 0x16a16, 0x16ad4, 0x16b14, 0x169f6, 0x16a36, 0x16af4, 0x16b34,
  0x169d8, 0x16a18, 0x16ad6, 0x16b16, 0x169f8, 0x16a38, 0x16af6, 0x16b36, 0x169da, 0x16a1a, 0x16ad8, 0x16b18, 0x169fa, 0x16a3a, 0x16af8, 0x16b38,
  0x16bca, 0x16c0a, 0x16c4a, 0x16c8a, 0x16bea, 0x16c2a, 0x16c6a, 0x16caa, 0x16bcc, 0x16c0c, 0x16c4c, 0x16c8c, 0x16bec, 0x16c2c, 0x16c6c, 0x16cac,
  0x16bce, 0x16c0e, 0x16c4e, 0x16c8e, 0x16bee, 0x16c2e, 0x16c6e, 0x16cae, 0x16bd0, 0x16c10, 0x16c50, 0x16c90, 0x16bf0, 0x16c30, 0x16c70, 0x16cb0,
  0x16bd2, 0x16c12, 0x16c52, 0x16c92, 0x16bf2, 0x16c32, 0x16c72, 0x16cb2, 0x16bd4, 0x16c14, 0x16c54, 0x16c94, 0x16bf4, 0x16c34, 0x16c74, 0x16cb4,
  0x16bd6, 0x16c16, 0x16c56, 0x16c96, 0x16bf6, 0x16c36, 0x16c76, 0x16cb6, 0x16bd8, 0x16c18, 0x16c58, 0x16c98, 0x16bf8, 0x16c38, 0x16c78, 0x16cb8,
  0x16cc0, 0x16cc0, 0x16cc0, 0x16cc0, 0x16cc8, 0x16cc8, 0x16cc8, 0x16cc8, 0x16cd0, 0x16cd0, 0x16cd0, 0x16cd0, 0x16cd8, 0x16cd8, 0x16cd8, 0x16cd8,
  0x16ce0, 0x16ce0, 0x16ce0, 0x16ce0, 0x16ce8, 0x16ce8, 0x16ce8, 0x16ce8, 0x16cf0, 0x16cf0, 0x16cf0, 0x16cf0, 0x16cf8, 0x16cf8, 0x16cf8, 0x16cf8,
  0x16d00, 0x16d00, 0x16d00, 0x16d00, 0x16d08, 0x16d08, 0x16d08, 0x16d08, 0x16d10, 0x16d10, 0x16d10, 0x16d10, 0x16d18, 0x16d18, 0x16d18, 0x16d18,
  0x16d20, 0x16d20, 0x16d20, 0x16d20, 0x16d28, 0x16d28, 0x16d28, 0x16d28, 0x16d30, 0x16d30, 0x16d30, 0x16d30, 0x16d38, 0x16d38, 0x16d38, 0x16d38,
  0x16cc0, 0x16cc0, 0x16cc0, 0x16cc0, 0x16cc8, 0x16cc8, 0x16cc8, 0x16cc8, 0x16cd0, 0x16cd0, 0x16cd0, 0x16cd0, 0x16cd8, 0x16cd8, 0x16cd8, 0x16cd8,
  0x16ce0, 0x16ce0, 0x16ce0, 0x16ce0, 0x16ce8, 0x16ce8, 0x16ce8, 0x16ce8, 0x16cf0, 0x16cf0, 0x16cf0, 0x16cf0, 0x16cf8, 0x16cf8, 0x16cf8, 0x16cf8,
  0x16d00, 0x16d00, 0x16d00, 0x16d00, 0x16d08, 0x16d08, 0x16d08, 0x16d08, 0x16d10, 0x16d10, 0x16d10, 0x16d10, 0x16d18, 0x16d18, 0x16d18, 0x16d18,
  0x16d20, 0x16d20, 0x16d20, 0x16d20, 0x16d28, 0x16d28, 0x16d28, 0x16d28, 0x16d30, 0x16d30, 0x16d30, 0x16d30, 0x16d38, 0x16d38, 0x16d38, 0x16d38,
  0x16cc0, 0x16cc0, 0x16cc0, 0x16cc0, 0x16cc8, 0x16cc8, 0x16cc8, 0x16cc8, 0x16cd0, 0x16cd0, 0x16cd0, 0x16cd0, 0x16cd8, 0x16cd8, 0x16cd8, 0x16cd8,
  0x16ce0, 0x16ce0, 0x16ce0, 0x16ce0, 0x16ce8, 0x16ce8, 0x16ce8, 0x16ce8, 0x16cf0, 0x16cf0, 0x16cf0, 0x16cf0, 0x16cf8, 0x16cf8, 0x16cf8, 0x16cf8,
  0x16d00, 0x16d00, 0x16d00, 0x16d00, 0x16d08, 0x16d08, 0x16d08, 0x16d08, 0x16d10, 0x16d10, 0x16d10, 0x16d10, 0x16d18, 0x16d18, 0x16d18, 0x16d18,
  0x16d20, 0x16d20, 0x16d20, 0x16d20, 0x16d28, 0x16d28, 0x16d28, 0x16d28, 0x16d30, 0x16d30, 0x16d30, 0x16d30, 0x16d38, 0x16d38, 0x16d38, 0x16d38,
  0x16cc0, 0x16cc0, 0x3020100, 0x16500, 0x16cc8, 0x16cc8, 0x3020100, 0x16500, 0x16cd0, 0x16cd0, 0x3020100, 0x16500, 0x16cd8, 0x16cd8, 0x3020100, 0x16500,
  0x16ce0, 0x16ce0, 0x3020100, 0x16500, 0x16ce8, 0x16ce8, 0x3020100, 0x16500, 0x16cf0, 0x16cf0, 0x3020100, 0x16500, 0x16cf8, 0x16cf8, 0x3020100, 0x16500,
  0x16d00, 0x16d00, 0x3020100, 0x16500, 0x16d08, 0x16d08, 0x3020100, 0x16500, 0x16d10, 0x16d10, 0x3020100, 0x16500, 0x16d18, 0x16d18, 0x3020100, 0x16500,
  0x16d20, 0x16d20, 0x3020100, 0x16500, 0x16d28, 0x16d28, 0x3020100, 0x16500, 0x16d30, 0x16d30, 0x3020100, 0x16500, 0x16d38, 0x16d38, 0x3020100, 0x16500,
  0x16cc0, 0x16cc0, 0x3020100, 0x16500, 0x16cc8, 0x16cc8, 0x3020100, 0x16500, 0x16cd0, 0x16cd0, 0x3020100, 0x16500, 0x16cd8, 0x16cd8, 0x3020100, 0x16500,
  0x16ce0, 0x16ce0, 0x3020100, 0x16500, 0x16ce8, 0x16ce8, 0x3020100, 0x16500, 0x16cf0, 0x16cf0, 0x3020100, 0x16500, 0x16cf8, 0x16cf8, 0x3020100, 0x16500,
  0x16d00, 0x16d00, 0x3020100, 0x16500, 0x16d08, 0x16d08, 0x3020100, 0x16500, 0x16d10, 0x16d10, 0x3020100, 0x16500, 0x16d18, 0x16d18, 0x3020100, 0x16500,
  0x16d20, 0x16d20, 0x3020100, 0x16500, 0x16d28, 0x16d28, 0x3020100, 0x16500, 0x16d30, 0x16d30, 0x3020100, 0x16500, 0x16d38, 0x16d38, 0x3020100, 0x16500,
  0x16fbc, 0x16fc4, 0x3020100, 0x16500, 0x170bc, 0x170c4, 0x3020100, 0x16500, 0x16fcc, 0x16fd4, 0x3020100, 0x16500, 0x170cc, 0x170d4, 0x3020100, 0x16500,
  0x16fdc, 0x16fe4, 0x3020100, 0x16500, 0x170dc, 0x170e4, 0x3020100, 0x16500, 0x16fec, 0x16ff4, 0x3020100, 0x16500, 0x170ec, 0x170f4, 0x3020100, 0x16500,
  0x16ffc, 0x17004, 0x3020100, 0x16500, 0x170fc, 0x17104, 0x3020100, 0x16500, 0x1700c, 0x17014, 0x3020100, 0x16500, 0x1710c, 0x17114, 0x3020100, 0x16500,
  0x1701c, 0x17024, 0x3020100, 0x16500, 0x1711c, 0x17124, 0x3020100, 0x16500, 0x1702c, 0x17034, 0x3020100, 0x16500, 0x1712c, 0x17134, 0x3020100, 0x16500,
  0x16cc0, 0x16cc0, 0x3020100, 0x16500, 0x16cc8, 0x16cc8, 0x3020100, 0x16500, 0x16cd0, 0x16cd0, 0x3020100, 0x16500, 0x16cd8, 0x16cd8, 0x3020100, 0x16500,
  0x16ce0, 0x16ce0, 0x3020100, 0x16500, 0x16ce8, 0x16ce8, 0x3020100, 0x16500, 0x16cf0, 0x16cf0, 0x3020100, 0x16500, 0x16cf8, 0x16cf8, 0x3020100, 0x16500,
  0x16d00, 0x16d00, 0x3020100, 0x16500, 0x16d08, 0x16d08, 0x3020100, 0x16500, 0x16d10, 0x16d10, 0x3020100, 0x16500, 0x16d18, 0x16d18, 0x3020100, 0x16500,
  0x16d20, 0x16d20, 0x3020100, 0x16500, 0x16d28, 0x16d28, 0x3020100, 0x16500, 0x16d30, 0x16d30, 0x3020100, 0x16500, 0x16d38, 0x16d38, 0x3020100, 0x16500,
  0x0, 0x0, 0x0, 0x0, 0x0, 0x0, 0x0, 0x0, 0x0, 0x0, 0x0, 0x0, 0x0, 0x0, 0x0, 0x0,
  0x0, 0x0, 0x0, 0x0, 0x0, 0x0, 0x0, 0x0, 0x0, 0x0, 0x0, 0x0, 0x0, 0x0, 0x0, 0x0,
  0x0, 0x0, 0x0, 0x0, 0x0, 0x0, 0x0, 0x0, 0x0, 0x0, 0x0, 0x0, 0x0, 0x0, 0x0, 0x0,
  0x0, 0x0, 0x0, 0x0, 0x0, 0x0, 0x0, 0x0, 0x0, 0x0, 0x0, 0x0, 0x0, 0x0, 0x0, 0x0,
  0x0, 0x0, 0x0, 0x0, 0x0, 0x0, 0x0, 0x0, 0x0, 0x0, 0x0, 0x0, 0x0, 0x0, 0x0, 0x0,
  0x0, 0x0, 0x0, 0x0, 0x0, 0x0, 0x0, 0x0, 0x0, 0x0, 0x0, 0x0, 0x0, 0x0, 0x0, 0x0,
  0x0, 0x0, 0x0, 0x0, 0x0, 0x0, 0x0, 0x0, 0x0, 0x0, 0x0, 0x0, 0x0, 0x0, 0x0, 0x0,
  0x0, 0x0, 0x0, 0x0, 0x0, 0x0, 0x0, 0x0, 0x0, 0x0, 0x0, 0x0, 0x0, 0x0, 0x0, 0x0,
  0x0, 0x0, 0x0, 0x0, 0x0, 0x0, 0x0, 0x0, 0x0, 0x0, 0x0, 0x0, 0x0, 0x0, 0x0, 0x0,
  0x0, 0x0, 0x0, 0x0, 0x0, 0x0, 0x0, 0x0, 0x0, 0x0, 0x0, 0x0, 0x0, 0x0, 0x0, 0x0,
  0x0, 0x0, 0x0, 0x0, 0x0, 0x0, 0x0, 0x0, 0x0, 0x0, 0x0, 0x0, 0x0, 0x0, 0x0, 0x0,
  0x0, 0x0, 0x0, 0x0, 0x0, 0x0, 0x0, 0x0, 0x0, 0x0, 0x0, 0x0, 0x0, 0x0, 0x0, 0x0,
  0x0, 0x0, 0x0, 0x0, 0x0, 0x0, 0x0, 0x0, 0x0, 0x0, 0x0, 0x0, 0x0, 0x0, 0x0, 0x0,
  0x0, 0x0, 0x0, 0x0, 0x0, 0x0, 0x0, 0x0, 0x0, 0x0, 0x0, 0x0, 0x0, 0x0, 0x0, 0x0,
  0x0, 0x0, 0x0, 0x0, 0x0, 0x0, 0x0, 0x0, 0x0, 0x0, 0x0, 0x0, 0x0, 0x0, 0x0, 0x0,
  0x0, 0x0, 0x0, 0x0, 0x0, 0x0, 0x0, 0x0, 0x0, 0x0, 0x0, 0x0, 0x0, 0x0, 0x0, 0x0
};

__global__ void __launch_bounds__(1024) mega(Args) {
  asm volatile(
    "s_load_dwordx16 s[4:19], s[0:1], 0\n"
    "s_load_dwordx16 s[20:35], s[0:1], 64\n"
    "v_readfirstlane_b32 s37, v0\n"
    "s_getpc_b64 s[40:41]\n"
    "K0:\n"
    "s_lshr_b32 s37, s37, 6\n"
    "s_cmpk_ge_u32 s2, 192\n"
    "s_cselect_b32 s42, 16, 0\n"
    "s_add_u32 s42, s42, s37\n"
    "s_lshl2_add_u32 s40, s42, s40\n"
    "s_add_u32 s40, s40, TBL1-K0\n"
    "s_mov_b32 s39, 0x100000\n"
    "s_setpc_b64 s[40:41]\n"
    "TBL1:\n"
    "s_branch GA0\n"
    "s_branch GA0\n"
    "s_branch GA0\n"
    "s_branch GA0\n"
    "s_branch GA0\n"
    "s_branch GA0\n"
    "s_branch S0\n"
    "s_branch S0\n"
    "s_branch S0\n"
    "s_branch S0\n"
    "s_branch S0\n"
    "s_branch S0\n"
    "s_branch MD0\n"
    "s_branch AUX1\n"
    "s_branch AUX1\n"
    "s_branch AUX0\n"
    "s_branch SIL0\n"
    "s_branch SIL0\n"
    "s_branch SIL0\n"
    "s_branch SIL0\n"
    "s_branch SIL0\n"
    "s_branch MD0\n"
    "s_branch S0\n"
    "s_branch S0\n"
    "s_branch S0\n"
    "s_branch S0\n"
    "s_branch S0\n"
    "s_branch S0\n"
    "s_branch M1\n"
    "s_branch M1\n"
    "s_branch M1\n"
    "s_branch AUX0\n"
    "AUX0:\n"
    "s_setprio 3\n"
    "s_lshr_b32 s42, s2, 3\n"
    "s_mul_i32 s42, s42, 7\n"
    "s_add_u32 s42, s42, 7\n"
    "v_and_b32 v1, 7, v0\n"
    "v_add_u32 v1, s42, v1\n"
    "v_min_u32 v1, (END-K0)/128, v1\n"
    "v_lshlrev_b32 v1, 7, v1\n"
    "s_mov_b32 m0, 148480\n"
    "s_mov_b64 exec, 0x7f\n"
    "global_load_lds_dword v1, s[40:41]\n"
    "s_mov_b64 exec, -1\n"
    "v_and_b32 v1, 63, v0\n"
    "v_lshlrev_b32 v1, 3, v1\n"
    "v_mov_b32 v4, 0\n"
    "v_mov_b32 v5, 0\n"
    "v_add_u32 v1, 147840, v1\n"
    "ds_write_b64 v1, v[4:5]\n"
    "s_setprio 0\n"
    "AUXT:\n"
    "s_cmpk_ge_u32 s2, 192\n"
    "s_cbranch_scc1 M1\n"
    "s_branch AUX1\n"
    "S0:\n"
    "s_setprio 0\n"
    "v_bfe_u32 v2, v0, 4, 2\n"
    "v_and_b32 v3, 15, v0\n"
    "v_mul_u32_u24 v2, 0x900, v2\n"
    "s_and_b32 s3, s2, 7\n"
    "s_lshr_b32 s42, s2, 3\n"
    "s_lshl_b32 s3, s3, 5\n"
    "s_add_u32 s3, s3, s42\n"
    "s_mul_i32 s42, s3, 48\n"
    "s_add_u32 s42, s42, s37\n"
    "s_mul_i32 s42, s42, 192\n"
    "v_mad_u32_u24 v1, v3, 12, v2\n"
    "v_add_u32 v1, s42, v1\n"
    "s_waitcnt lgkmcnt(0)\n"
    "global_load_dwordx3 v[68:70], v1, s[4:5] offset:-1152 nt\n"
    "global_load_dwordx3 v[72:74], v1, s[4:5] nt\n"
    "s_setprio 0\n"
    "s_load_dword s36, s[20:21], 0\n"
    "s_branch S1\n"
    "GA0:\n"
    "s_and_b32 s44, s2, 7\n"
    "s_lshr_b32 s45, s2, 3\n"
    "s_mul_i32 s44, s44, 24\n"
    "s_add_u32 s38, s44, s45\n"
    "s_lshr_b32 s42, s37, 1\n"
    "s_mul_i32 s43, s38, 3\n"
    "s_add_u32 s43, s43, s42\n"
    "s_lshl_b32 s44, s43, 8\n"
    "s_lshl_b32 s45, s43, 1\n"
    "s_and_b32 s45, s45, -4\n"
    "v_and_b32 v1, 63, v0\n"
    "v_lshl_add_u32 v2, v1, 2, s44\n"
    "s_waitcnt lgkmcnt(0)\n"
    "global_load_dword v4, v2, s[6:7] nt\n"
    "global_load_dword v5, v2, s[8:9] nt\n"
    "s_memrealtime s[96:97]\n"
    "s_load_dword s46, s[10:11], s45\n"
    "global_load_dword v6, v2, s[12:13] nt\n"
    "global_load_dword v7, v2, s[14:15] nt\n"
    "s_load_dword s36, s[20:21], 0\n"
    "s_branch GA1\n"
    "SIL0:\n"
    "v_min_u32 v1, 287, v0\n"
    "v_lshlrev_b32 v1, 2, v1\n"
    "s_waitcnt lgkmcnt(0)\n"
    "global_load_dword v4, v1, s[18:19]\n"
    "s_load_dword s36, s[20:21], 0\n"
    "s_branch SIL1\n"
    "MD0:\n"
    "s_setprio 0\n"
    "s_mul_i32 s43, s2, 19\n"
    "s_lshr_b32 s43, s43, 9\n"
    "s_mul_i32 s44, s43, 27\n"
    "s_sub_u32 s44, s2, s44\n"
    "s_mul_i32 s45, s43, 442368\n"
    "s_lshl_b32 s46, s44, 7\n"
    "s_add_u32 s45, s45, s46\n"
    "v_bfe_u32 v1, v0, 3, 3\n"
    "v_and_b32 v2, 2, v1\n"
    "v_mul_u32_u24 v1, 0x1b00, v1\n"
    "v_xor_b32 v2, v2, v0\n"
    "v_and_b32 v2, 7, v2\n"
    "v_lshl_add_u32 v1, v2, 4, v1\n"
    "v_add_u32 v1, s45, v1\n"
    "v_xor_b32 v2, 64, v1\n"
    "v_add_u32 v2, 55296, v2\n"
    "s_lshl_b32 s46, s43, 7\n"
    "v_and_b32 v3, 31, v0\n"
    "v_lshl_add_u32 v5, v3, 2, s46\n"
    "s_mov_b32 m0, 138240\n"
    "s_cmpk_ge_u32 s2, 243\n"
    "s_waitcnt lgkmcnt(0)\n"
    "s_memrealtime s[96:97]\n"
    "s_cbranch_scc1 MD0e\n"
    "global_load_dword v4, v5, s[18:19]\n"
    "global_load_lds_dwordx4 v1, s[16:17] nt\n"
    "s_add_u32 m0, m0, 1024\n"
    "v_add_u32 v1, 110592, v1\n"
    "global_load_lds_dwordx4 v2, s[16:17] nt\n"
    "s_add_u32 m0, m0, 1024\n"
    "v_add_u32 v2, 110592, v2\n"
    "global_load_lds_dwordx4 v1, s[16:17] nt\n"
    "s_add_u32 m0, m0, 1024\n"
    "v_add_u32 v1, 110592, v1\n"
    "global_load_lds_dwordx4 v2, s[16:17] nt\n"
    "s_add_u32 m0, m0, 1024\n"
    "v_add_u32 v2, 110592, v2\n"
    "global_load_lds_dwordx4 v1, s[16:17] nt\n"
    "s_add_u32 m0, m0, 1024\n"
    "v_add_u32 v1, 110592, v1\n"
    "global_load_lds_dwordx4 v2, s[16:17] nt\n"
    "s_add_u32 m0, m0, 1024\n"
    "v_add_u32 v2, 110592, v2\n"
    "global_load_lds_dwordx4 v1, s[16:17] nt\n"
    "s_add_u32 m0, m0, 1024\n"
    "s_nop 0\n"
    "global_load_lds_dwordx4 v2, s[16:17] nt\n"
    "MD0e:\n"
    "v_mov_b32 v6, 148448\n"
    "s_waitcnt lgkmcnt(0)\n"
    "v_mov_b32 v7, s96\n"
    "ds_write_b32 v6, v7\n"
    "s_waitcnt lgkmcnt(0)\n"
    "s_load_dword s36, s[20:21], 0\n"
    "s_branch MD1\n"
    "ABORT:\n"
    "s_endpgm\n"
    "S1:\n"
    "s_setprio 3\n"
    "s_barrier\n"
    "v_mov_b32 v14, 147968\n"
    "v_mov_b32 v15, 1\n"
    "s_cmpk_lt_u32 s2, 192\n"
    "s_cbranch_scc1 KSP\n"
    "S2:\n"
    "s_mul_i32 s42, s3, 12416\n"
    "s_add_u32 s42, s42, 7334912\n"
    "v_lshl_add_u32 v75, v0, 4, s42\n"
    "v_and_b32 v76, -4, v0\n"
    "v_add_u32 v76, 147072, v76\n"
    "v_and_b32 v86, 3, v0\n"
    "v_and_b32 v67, 60, v0\n"
    "v_lshl_add_u32 v86, v86, 6, v67\n"
    "v_and_b32 v77, 31, v0\n"
    "v_lshlrev_b32 v77, 4, v77\n"
    "v_add_u32 v77, 147456, v77\n"
    "s_mov_b32 s43, 0x3f803f80\n"
    "s_movk_i32 s44, 6144\n"
    "s_setprio 3\n"
    "s_waitcnt vmcnt(1)\n"
    "ds_bpermute_b32 v108, v86, v68\n"
    "ds_bpermute_b32 v109, v86, v69\n"
    "ds_bpermute_b32 v110, v86, v70\n"
    "v_dot2_f32_bf16 v80, v68, s43, 0\n"
    "v_dot2_f32_bf16 v81, v68, v68, 0\n"
    "v_dot2_f32_bf16 v80, v69, s43, v80\n"
    "v_dot2_f32_bf16 v81, v69, v69, v81\n"
    "v_dot2_f32_bf16 v80, v70, s43, v80\n"
    "v_dot2_f32_bf16 v81, v70, v70, v81\n"
    "s_waitcnt vmcnt(0)\n"
    "ds_bpermute_b32 v112, v86, v72\n"
    "ds_bpermute_b32 v113, v86, v73\n"
    "ds_bpermute_b32 v114, v86, v74\n"
    "v_dot2_f32_bf16 v82, v72, s43, 0\n"
    "v_dot2_f32_bf16 v83, v72, v72, 0\n"
    "v_dot2_f32_bf16 v82, v73, s43, v82\n"
    "v_dot2_f32_bf16 v83, v73, v73, v83\n"
    "v_dot2_f32_bf16 v82, v74, s43, v82\n"
    "v_dot2_f32_bf16 v83, v74, v74, v83\n"
    "v_add_f32_dpp v84, v80, v80 row_ror:8 row_mask:0xf bank_mask:0x3\n"
    "v_add_f32_dpp v85, v81, v81 row_ror:8 row_mask:0xf bank_mask:0x3\n"
    "s_nop 0\n"
    "v_add_f32_dpp v84, v82, v82 row_ror:8 row_mask:0xf bank_mask:0xc\n"
    "v_add_f32_dpp v85, v83, v83 row_ror:8 row_mask:0xf bank_mask:0xc\n"
    "s_nop 0\n"
    "v_add_f32_dpp v80, v84, v84 row_half_mirror row_mask:0xf bank_mask:0x5\n"
    "s_nop 1\n"
    "v_add_f32_dpp v80, v85, v85 row_half_mirror row_mask:0xf bank_mask:0xa\n"
    "s_nop 1\n"
    "v_add_f32_dpp v80, v80, v80 quad_perm:[1,0,3,2] row_mask:0xf bank_mask:0xf\n"
    "s_nop 1\n"
    "v_add_f32_dpp v80, v80, v80 quad_perm:[2,3,0,1] row_mask:0xf bank_mask:0xf\n"
    "ds_write_b32 v76, v80\n"
    "s_mov_b64 exec, 1\n"
    "ds_add_u32 v14, v15 offset:0\n"
    "s_mov_b64 exec, -1\n"
    "s_waitcnt lgkmcnt(0)\n"
    "v_lshlrev_b32 v88, 16, v108\n"
    "v_lshlrev_b32 v89, 16, v112\n"
    "v_and_b32 v90, 0xffff0000, v108\n"
    "v_and_b32 v91, 0xffff0000, v112\n"
    "v_lshlrev_b32 v92, 16, v109\n"
    "v_lshlrev_b32 v93, 16, v113\n"
    "v_and_b32 v94, 0xffff0000, v109\n"
    "v_and_b32 v95, 0xffff0000, v113\n"
    "v_lshlrev_b32 v96, 16, v110\n"
    "v_lshlrev_b32 v97, 16, v114\n"
    "v_and_b32 v98, 0xffff0000, v110\n"
    "v_and_b32 v99, 0xffff0000, v114\n"
    "s_branch E_2\n"
    "R_1:\n"
    "s_sleep 1\n"
    "s_sub_u32 s39, s39, 1\n"
    "s_cbranch_scc1 ABORT\n"
    "E_2:\n"
    "ds_read_b32 v84, v14 offset:0\n"
    "ds_read_b128 v[80:83], v77\n"
    "s_waitcnt lgkmcnt(0)\n"
    "v_cmp_ne_u32 vcc, 6, v84\n"
    "s_cbranch_vccnz R_1\n"
    "s_add_u32 s36, s36, 1\n"
    "v_permlane16_swap_b32 v80, v82\n"
    "v_permlane16_swap_b32 v81, v83\n"
    "v_add_f32 v80, v80, v82\n"
    "v_add_f32 v81, v81, v83\n"
    "v_mov_b32 v103, s36\n"
    "v_add_f32_dpp v80, v80, v80 row_ror:8 row_mask:0xf bank_mask:0xf\n"
    "v_add_f32_dpp v81, v81, v81 row_ror:8 row_mask:0xf bank_mask:0xf\n"
    "v_mov_b32 v107, s36\n"
    "v_add_f32_dpp v80, v80, v80 row_ror:4 row_mask:0xf bank_mask:0xf\n"
    "v_add_f32_dpp v81, v81, v81 row_ror:4 row_mask:0xf bank_mask:0xf\n"
    "v_mul_f32 v80, 0x3ae38e39, v80\n"
    "v_mul_f32 v81, 0x3ae38e39, v81\n"
    "v_fma_f32 v81, -v80, v80, v81\n"
    "v_max_f32 v81, 0, v81\n"
    "v_cvt_pk_bf16_f32 v81, 0, v81\n"
    "v_add_f32 v81, 0x3727c5ac, v81\n"
    "v_cvt_pk_bf16_f32 v81, 0, v81\n"
    "v_sqrt_f32 v81, v81\n"
    "s_nop 0\n"
    "v_cvt_pk_bf16_f32 v80, v80, v81\n"
    "v_mov_b32 v81, v80\n"
    "s_nop 1\n"
    "v_permlane16_swap_b32 v80, v81\n"
    "v_and_b32 v84, 0xffff0000, v80\n"
    "v_and_b32 v85, 0xffff0000, v81\n"
    "v_lshlrev_b32 v82, 16, v80\n"
    "v_lshlrev_b32 v83, 16, v81\n"
    "v_rcp_f32 v84, v84\n"
    "v_rcp_f32 v85, v85\n"
    "v_pk_add_f32 v[88:89], v[88:89], v[82:83] neg_lo:[0,1] neg_hi:[0,1]\n"
    "v_pk_add_f32 v[90:91], v[90:91], v[82:83] neg_lo:[0,1] neg_hi:[0,1]\n"
    "v_pk_add_f32 v[92:93], v[92:93], v[82:83] neg_lo:[0,1] neg_hi:[0,1]\n"
    "v_cvt_pk_bf16_f32 v88, 0, v88\n"
    "v_cvt_pk_bf16_f32 v89, 0, v89\n"
    "v_cvt_pk_bf16_f32 v90, 0, v90\n"
    "v_cvt_pk_bf16_f32 v91, 0, v91\n"
    "v_cvt_pk_bf16_f32 v92, 0, v92\n"
    "v_cvt_pk_bf16_f32 v93, 0, v93\n"
    "v_pk_mul_f32 v[88:89], v[88:89], v[84:85]\n"
    "v_pk_mul_f32 v[90:91], v[90:91], v[84:85]\n"
    "v_pk_mul_f32 v[92:93], v[92:93], v[84:85]\n"
    "v_cvt_pk_bf16_f32 v100, v88, v89\n"
    "v_cvt_pk_bf16_f32 v101, v90, v91\n"
    "v_cvt_pk_bf16_f32 v102, v92, v93\n"
    "buffer_store_dwordx4 v[100:103], v75, s[20:23], 0 offen sc1\n"
    "v_pk_add_f32 v[94:95], v[94:95], v[82:83] neg_lo:[0,1] neg_hi:[0,1]\n"
    "v_pk_add_f32 v[96:97], v[96:97], v[82:83] neg_lo:[0,1] neg_hi:[0,1]\n"
    "v_pk_add_f32 v[98:99], v[98:99], v[82:83] neg_lo:[0,1] neg_hi:[0,1]\n"
    "v_cvt_pk_bf16_f32 v94, 0, v94\n"
    "v_cvt_pk_bf16_f32 v95, 0, v95\n"
    "v_cvt_pk_bf16_f32 v96, 0, v96\n"
    "v_cvt_pk_bf16_f32 v97, 0, v97\n"
    "v_cvt_pk_bf16_f32 v98, 0, v98\n"
    "v_cvt_pk_bf16_f32 v99, 0, v99\n"
    "v_pk_mul_f32 v[94:95], v[94:95], v[84:85]\n"
    "v_pk_mul_f32 v[96:97], v[96:97], v[84:85]\n"
    "v_pk_mul_f32 v[98:99], v[98:99], v[84:85]\n"
    "v_cvt_pk_bf16_f32 v104, v94, v95\n"
    "v_cvt_pk_bf16_f32 v105, v96, v97\n"
    "v_cvt_pk_bf16_f32 v106, v98, v99\n"
    "buffer_store_dwordx4 v[104:107], v75, s[20:23], s44 offen sc1\n"
    "s_setprio 0\n"
    "s_cmpk_lt_u32 s2, 192\n"
    "s_cbranch_scc1 KST\n"
    "s_setprio 0\n"
    "s_branch ES1\n"
    "KSP:\n"
    "s_sub_u32 s47, s37, 8\n"
    "s_cmp_ge_u32 s47, 3\n"
    "s_cselect_b32 s48, 1, 0\n"
    "s_add_u32 s45, s47, 3\n"
    "s_cmp_lt_i32 s47, 0\n"
    "s_cselect_b32 s47, s45, s47\n"
    "s_cmp_eq_u32 s47, 3\n"
    "s_cselect_b32 s47, 0, s47\n"
    "s_mul_i32 s45, s47, 30464\n"
    "v_and_b32 v122, 3, v0\n"
    "v_mul_u32_u24 v122, 1088, v122\n"
    "v_bfe_u32 v123, v0, 2, 4\n"
    "v_lshlrev_b32 v123, 3, v123\n"
    "v_add3_u32 v122, v122, v123, s45\n"
    "s_mul_i32 s45, s48, 4352\n"
    "v_add_u32 v123, s45, v122\n"
    "s_lshl_b32 s45, s48, 12\n"
    "s_add_u32 s45, s45, 107520\n"
    "v_and_b32 v124, 63, v0\n"
    "v_lshl_add_u32 v124, v124, 4, s45\n"
    "s_lshl_b32 s45, s47, 2\n"
    "v_add_u32 v125, s45, v14\n"
    "s_lshl_b32 s45, s45, 1\n"
    "s_add_u32 s45, s45, 148416\n"
    "v_mov_b32 v126, s45\n"
    "s_mul_i32 s49, s47, 3328\n"
    "s_mov_b32 s42, s47\n"
    "s_cmp_eq_u32 s48, 0\n"
    "s_cbranch_scc1 NEAR\n"
    "s_cmpk_eq_u32 s37, 6\n"
    "s_cbranch_scc1 HELP\n"
    "s_branch FAR\n"
    "MD1:\n"
    "v_bfe_u32 v5, v0, 4, 2\n"
    "v_lshlrev_b32 v1, 2, v3\n"
    "v_add_u32 v1, 149888, v1\n"
    "v_lshlrev_b32 v2, 4, v5\n"
    "v_add_u32 v2, 149888, v2\n"
    "v_bfe_u32 v6, v0, 2, 2\n"
    "v_bfe_u32 v7, v0, 1, 1\n"
    "v_and_b32 v8, 2, v6\n"
    "v_or_b32 v7, v7, v8\n"
    "v_and_b32 v8, 1, v5\n"
    "v_lshl_add_u32 v7, v8, 2, v7\n"
    "v_lshlrev_b32 v8, 10, v5\n"
    "v_lshl_add_u32 v8, v6, 7, v8\n"
    "v_lshl_add_u32 v8, v7, 4, v8\n"
    "v_and_b32 v7, 1, v0\n"
    "v_lshl_add_u32 v8, v7, 3, v8\n"
    "v_xor_b32 v9, 32, v8\n"
    "v_xor_b32 v10, 64, v8\n"
    "v_xor_b32 v11, 0x60, v8\n"
    "v_add_u32 v8, 138240, v8\n"
    "v_add_u32 v9, 138240, v9\n"
    "v_add_u32 v10, 138240, v10\n"
    "v_add_u32 v11, 138240, v11\n"
    "v_and_b32 v3, 63, v0\n"
    "v_lshlrev_b32 v3, 3, v3\n"
    "s_mul_i32 s43, s43, 13824\n"
    "s_lshl_b32 s44, s44, 9\n"
    "s_add_u32 s43, s43, s44\n"
    "s_add_u32 s43, s43, 10519552\n"
    "s_barrier\n"
    "s_cmpk_ge_u32 s2, 243\n"
    "s_cbranch_scc1 M9\n"
    "s_setprio 3\n"
    "s_waitcnt vmcnt(8)\n"
    "v_lshlrev_b32 v5, 16, v4\n"
    "v_and_b32 v4, 0xffff0000, v4\n"
    "v_mul_f32 v6, 0xbfb8aa3b, v5\n"
    "v_mul_f32 v7, 0xbfb8aa3b, v4\n"
    "v_exp_f32 v6, v6\n"
    "v_exp_f32 v7, v7\n"
    "v_add_f32 v6, 1.0, v6\n"
    "v_add_f32 v7, 1.0, v7\n"
    "v_rcp_f32 v6, v6\n"
    "v_rcp_f32 v7, v7\n"
    "v_mul_f32 v5, v5, v6\n"
    "v_mul_f32 v4, v4, v7\n"
    "v_cvt_pk_bf16_f32 v4, v5, v4\n"
    "ds_write_b32 v1, v4\n"
    "ds_read_b128 v[12:15], v2\n"
    "ds_read_b128 v[16:19], v2 offset:64\n"
    "s_waitcnt lgkmcnt(0)\n"
    "s_add_u32 s36, s36, 1\n"
    "s_waitcnt vmcnt(4)\n"
    "ds_read_b64_tr_b16 v[40:41], v8 offset:0\n"
    "ds_read_b64_tr_b16 v[42:43], v8 offset:512\n"
    "ds_read_b64_tr_b16 v[44:45], v9 offset:0\n"
    "ds_read_b64_tr_b16 v[46:47], v9 offset:512\n"
    "ds_read_b64_tr_b16 v[48:49], v10 offset:0\n"
    "ds_read_b64_tr_b16 v[50:51], v10 offset:512\n"
    "ds_read_b64_tr_b16 v[52:53], v11 offset:0\n"
    "ds_read_b64_tr_b16 v[54:55], v11 offset:512\n"
    "s_waitcnt lgkmcnt(6)\n"
    "v_mfma_f32_16x16x32_bf16 v[20:23], v[12:15], v[40:43], 0\n"
    "s_waitcnt lgkmcnt(4)\n"
    "v_mfma_f32_16x16x32_bf16 v[24:27], v[12:15], v[44:47], 0\n"
    "s_waitcnt lgkmcnt(2)\n"
    "v_mfma_f32_16x16x32_bf16 v[28:31], v[12:15], v[48:51], 0\n"
    "s_waitcnt lgkmcnt(0)\n"
    "v_mfma_f32_16x16x32_bf16 v[32:35], v[12:15], v[52:55], 0\n"
    "s_waitcnt vmcnt(0)\n"
    "ds_read_b64_tr_b16 v[56:57], v8 offset:4096\n"
    "ds_read_b64_tr_b16 v[58:59], v8 offset:4608\n"
    "ds_read_b64_tr_b16 v[60:61], v9 offset:4096\n"
    "ds_read_b64_tr_b16 v[62:63], v9 offset:4608\n"
    "ds_read_b64_tr_b16 v[64:65], v10 offset:4096\n"
    "ds_read_b64_tr_b16 v[66:67], v10 offset:4608\n"
    "ds_read_b64_tr_b16 v[68:69], v11 offset:4096\n"
    "ds_read_b64_tr_b16 v[70:71], v11 offset:4608\n"
    "s_waitcnt lgkmcnt(6)\n"
    "v_mfma_f32_16x16x32_bf16 v[20:23], v[16:19], v[56:59], v[20:23]\n"
    "s_waitcnt lgkmcnt(4)\n"
    "v_mfma_f32_16x16x32_bf16 v[24:27], v[16:19], v[60:63], v[24:27]\n"
    "s_waitcnt lgkmcnt(2)\n"
    "v_mfma_f32_16x16x32_bf16 v[28:31], v[16:19], v[64:67], v[28:31]\n"
    "s_waitcnt lgkmcnt(0)\n"
    "v_mfma_f32_16x16x32_bf16 v[32:35], v[16:19], v[68:71], v[32:35]\n"
    "s_nop 1\n"
    "v_mov_b32 v21, s36\n"
    "s_nop 0\n"
    "v_mov_b32_dpp v20, v24 quad_perm:[0,1,2,3] row_mask:0x2 bank_mask:0xf\n"
    "s_nop 1\n"
    "v_mov_b32_dpp v20, v28 quad_perm:[0,1,2,3] row_mask:0x4 bank_mask:0xf\n"
    "s_nop 1\n"
    "v_mov_b32_dpp v20, v32 quad_perm:[0,1,2,3] row_mask:0x8 bank_mask:0xf\n"
    "buffer_store_dwordx2 v[20:21], v3, s[20:23], s43 offen sc1\n"
    "s_setprio 0\n"
    "s_cmpk_ge_u32 s2, 192\n"
    "s_cbranch_scc1 ES1\n"
    "s_mov_b64 exec, 0\n"
    "CH12:\n"
    "s_branch AUX3\n"
    "GA1:\n"
    "s_setprio 1\n"
    "s_mov_b32 s48, 0x01000c0c\n"
    "s_bitcmp1_b32 s37, 0\n"
    "s_cselect_b32 s47, 0x03020c0c, s48\n"
    "s_and_b32 s43, s43, 1\n"
    "s_lshl_b32 s43, s43, 4\n"
    "s_mul_i32 s44, s42, 30464\n"
    "s_and_b32 s45, s37, 1\n"
    "s_lshl_b32 s45, s45, 9\n"
    "s_add_u32 s44, s44, s45\n"
    "v_lshl_add_u32 v2, v1, 3, s44\n"
    "v_lshlrev_b32 v52, 4, v0\n"
    "v_add_u32 v52, 126368, v52\n"
    "s_lshl_b32 s45, s37, 2\n"
    "s_add_u32 s45, s45, 147952\n"
    "v_mov_b32 v12, s45\n"
    "s_lshl_b32 s45, s42, 2\n"
    "s_add_u32 s44, s45, 147968\n"
    "v_mov_b32 v13, s44\n"
    "s_lshl_b32 s45, s45, 1\n"
    "s_add_u32 s45, s45, 148416\n"
    "v_mov_b32 v14, s45\n"
    "v_mov_b32 v15, 1\n"
    "v_mov_b32 v8, 0x3fb8aa3b\n"
    "s_mov_b32 s48, 0x3e22f983\n"
    "s_mov_b32 s49, 0x31dc9c88\n"
    "s_mov_b32 s50, 0x41000000\n"
    "s_mov_b32 s51, 0x43000000\n"
    "s_lshl_b32 s57, s37, 2\n"
    "s_mov_b32 s58, 0x78650432\n"
    "s_lshr_b32 s54, s58, s57\n"
    "s_and_b32 s54, s54, 15\n"
    "s_add_u32 s55, s54, 7\n"
    "s_cmpk_eq_u32 s37, 3\n"
    "s_cselect_b32 s55, 1, s55\n"
    "s_and_b32 s56, s38, 1\n"
    "s_mul_i32 s56, s56, 6144\n"
    "s_lshr_b32 s57, s38, 1\n"
    "s_lshl_b32 s57, s57, 6\n"
    "s_add_u32 s56, s56, s57\n"
    "s_add_u32 s56, s56, 7341056\n"
    "s_mul_i32 s40, s54, 198656\n"
    "s_add_u32 s40, s40, s56\n"
    "s_mul_i32 s41, s55, 198656\n"
    "s_add_u32 s41, s41, s56\n"
    "v_bfe_u32 v66, v0, 2, 4\n"
    "v_mul_u32_u24 v66, 12416, v66\n"
    "v_and_b32 v67, 3, v0\n"
    "v_lshl_add_u32 v66, v67, 4, v66\n"
    "s_barrier\n"
    "s_waitcnt lgkmcnt(0)\n"
    "s_lshr_b32 s46, s46, s43\n"
    "s_lshl_b32 s46, s46, 16\n"
    "s_add_u32 s36, s36, 1\n"
    "v_mul_f32 v9, s46, v8\n"
    "v_exp_f32 v9, v9\n"
    "s_waitcnt vmcnt(3)\n"
    "v_perm_b32 v4, v4, v4, s47\n"
    "v_mul_f32 v4, v4, v8\n"
    "v_cvt_pk_bf16_f32 v9, 0, v9\n"
    "v_exp_f32 v4, v4\n"
    "s_waitcnt vmcnt(2)\n"
    "v_perm_b32 v5, v5, v5, s47\n"
    "v_mul_f32 v5, v5, v9\n"
    "v_cvt_pk_bf16_f32 v4, 0, v4\n"
    "v_mul_f32 v10, s48, v5\n"
    "v_mul_f32 v4, v4, v9\n"
    "v_fma_f32 v11, v5, s48, -v10\n"
    "v_mul_f32_e64 v4, v4, -v8\n"
    "v_fma_f32 v11, v5, s49, v11\n"
    "v_mul_f32 v20, s50, v10\n"
    "v_mul_f32 v21, s50, v4\n"
    "v_fract_f32 v20, v20\n"
    "v_exp_f32 v21, v21\n"
    "v_fma_f32 v20, v11, s50, v20\n"
    "v_cos_f32 v16, v20\n"
    "v_sin_f32 v17, v20\n"
    "v_mul_f32 v22, s51, v10\n"
    "v_mul_f32 v23, s51, v4\n"
    "v_fract_f32 v22, v22\n"
    "v_exp_f32 v23, v23\n"
    "v_fma_f32 v22, v11, s51, v22\n"
    "v_cos_f32 v18, v22\n"
    "v_sin_f32 v19, v22\n"
    "v_pk_mul_f32 v[16:17], v[20:21], v[16:17] op_sel:[1,0] op_sel_hi:[1,1]\n"
    "v_pk_mul_f32 v[18:19], v[22:23], v[18:19] op_sel:[1,0] op_sel_hi:[1,1]\n"
    "s_cmpk_lt_u32 s37, 4\n"
    "s_cbranch_scc1 ASIDE\n"
    "ds_write_b128 v52, v[16:19]\n"
    "s_mov_b64 exec, 1\n"
    "ds_write_b32 v12, v15 offset:4\n"
    "s_mov_b64 exec, -1\n"
    "ASIDE:\n"
    "s_setprio 0\n"
    "v_fract_f32 v22, v10\n"
    "v_exp_f32 v23, v4\n"
    "v_add_f32 v22, v11, v22\n"
    "v_cos_f32 v24, v22\n"
    "v_sin_f32 v25, v22\n"
    "s_waitcnt vmcnt(0)\n"
    "v_and_b32 v32, 0x7fff7fff, v6\n"
    "v_and_b32 v33, 0x7fff7fff, v7\n"
    "v_pk_max_u16 v32, v32, v33\n"
    "v_pk_mul_f32 v[24:25], v[22:23], v[24:25] op_sel:[1,0] op_sel_hi:[1,1]\n"
    "v_lshlrev_b32 v33, 16, v32\n"
    "v_max_u32 v32, v32, v33\n"
    "v_perm_b32 v6, v6, v6, s47\n"
    "v_perm_b32 v7, v7, v7, s47\n"
    "v_max_u32_dpp v32, v32, v32 row_ror:8 row_mask:0xf bank_mask:0xf\n"
    "v_pk_mul_f32 v[20:21], v[18:19], v[18:19] op_sel:[1,1] op_sel_hi:[1,0] neg_lo:[0,1]\n"
    "v_pk_fma_f32 v[30:31], v[18:19], v[18:19], v[20:21] op_sel_hi:[0,1,1]\n"
    "v_max_u32_dpp v32, v32, v32 row_ror:4 row_mask:0xf bank_mask:0xf\n"
    "v_pk_mul_f32 v[20:21], v[24:25], v[24:25] op_sel:[1,1] op_sel_hi:[1,0] neg_lo:[0,1]\n"
    "v_pk_fma_f32 v[26:27], v[24:25], v[24:25], v[20:21] op_sel_hi:[0,1,1]\n"
    "v_max_u32_dpp v32, v32, v32 row_ror:2 row_mask:0xf bank_mask:0xf\n"
    "v_pk_mul_f32 v[20:21], v[26:27], v[26:27] op_sel:[1,1] op_sel_hi:[1,0] neg_lo:[0,1]\n"
    "v_pk_fma_f32 v[28:29], v[26:27], v[26:27], v[20:21] op_sel_hi:[0,1,1]\n"
    "v_max_u32_dpp v32, v32, v32 row_ror:1 row_mask:0xf bank_mask:0xf\n"
    "v_and_b32 v34, 0x7fffff, v9\n"
    "v_or_b32 v34, 0x35800000, v34\n"
    "v_max_u32_dpp v32, v32, v32 row_bcast:15 row_mask:0xa bank_mask:0xf\n"
    "v_and_b32 v35, 0x7f800000, v9\n"
    "s_nop 0\n"
    "v_max_u32_dpp v32, v32, v32 row_bcast:31 row_mask:0xc bank_mask:0xf\n"
    "s_nop 0\n"
    "v_readlane_b32 s44, v32, 63\n"
    "s_lshr_b32 s44, s44, 23\n"
    "s_max_u32 s44, s44, 26\n"
    "s_min_u32 s44, s44, 226\n"
    "s_sub_u32 s46, 266, s44\n"
    "s_lshl_b32 s46, s46, 23\n"
    "s_sub_u32 s44, s44, 132\n"
    "v_pk_mul_f32 v[36:37], v[6:7], s[46:47] op_sel_hi:[1,0]\n"
    "v_ldexp_f32 v35, v35, s44\n"
    "ds_write_b64 v14, v[34:35]\n"
    "v_pk_mul_f32 v[20:21], v[36:37], v[24:25] op_sel:[1,1] op_sel_hi:[1,0] neg_lo:[0,1]\n"
    "v_pk_fma_f32 v[38:39], v[36:37], v[24:25], v[20:21] op_sel_hi:[0,1,1]\n"
    "v_pk_mul_f32 v[20:21], v[38:39], v[24:25] op_sel:[1,1] op_sel_hi:[1,0] neg_lo:[0,1]\n"
    "v_pk_fma_f32 v[40:41], v[38:39], v[24:25], v[20:21] op_sel_hi:[0,1,1]\n"
    "v_pk_mul_f32 v[20:21], v[40:41], v[24:25] op_sel:[1,1] op_sel_hi:[1,0] neg_lo:[0,1]\n"
    "v_pk_fma_f32 v[42:43], v[40:41], v[24:25], v[20:21] op_sel_hi:[0,1,1]\n"
    "v_pk_mul_f32 v[20:21], v[36:37], v[30:31] op_sel:[1,1] op_sel_hi:[1,0] neg_lo:[0,1]\n"
    "v_pk_fma_f32 v[44:45], v[36:37], v[30:31], v[20:21] op_sel_hi:[0,1,1]\n"
    "v_cvt_pk_f16_f32 v54, v36, v38\n"
    "v_cvt_pk_f16_f32 v55, v40, v42\n"
    "v_cvt_pk_f16_f32 v56, -v37, -v39\n"
    "v_cvt_pk_f16_f32 v57, -v41, -v43\n"
    "ds_write_b64 v2, v[54:55] offset:0\n"
    "ds_write_b64 v2, v[56:57] offset:6528\n"
    "v_pk_mul_f32 v[20:21], v[42:43], v[24:25] op_sel:[1,1] op_sel_hi:[1,0] neg_lo:[0,1]\n"
    "v_pk_fma_f32 v[36:37], v[42:43], v[24:25], v[20:21] op_sel_hi:[0,1,1]\n"
    "v_pk_mul_f32 v[20:21], v[36:37], v[24:25] op_sel:[1,1] op_sel_hi:[1,0] neg_lo:[0,1]\n"
    "v_pk_fma_f32 v[38:39], v[36:37], v[24:25], v[20:21] op_sel_hi:[0,1,1]\n"
    "v_pk_mul_f32 v[20:21], v[38:39], v[24:25] op_sel:[1,1] op_sel_hi:[1,0] neg_lo:[0,1]\n"
    "v_pk_fma_f32 v[40:41], v[38:39], v[24:25], v[20:21] op_sel_hi:[0,1,1]\n"
    "v_pk_mul_f32 v[20:21], v[40:41], v[24:25] op_sel:[1,1] op_sel_hi:[1,0] neg_lo:[0,1]\n"
    "v_pk_fma_f32 v[42:43], v[40:41], v[24:25], v[20:21] op_sel_hi:[0,1,1]\n"
    "v_cvt_pk_f16_f32 v58, v36, v38\n"
    "v_cvt_pk_f16_f32 v59, v40, v42\n"
    "v_cvt_pk_f16_f32 v60, -v37, -v39\n"
    "v_cvt_pk_f16_f32 v61, -v41, -v43\n"
    "ds_write_b64 v2, v[58:59] offset:1088\n"
    "ds_write_b64 v2, v[60:61] offset:7616\n"
    "v_pk_mul_f32 v[20:21], v[44:45], v[26:27] op_sel:[1,1] op_sel_hi:[1,0] neg_lo:[0,1]\n"
    "v_pk_fma_f32 v[46:47], v[44:45], v[26:27], v[20:21] op_sel_hi:[0,1,1]\n"
    "v_pk_mul_f32 v[20:21], v[44:45], v[28:29] op_sel:[1,1] op_sel_hi:[1,0] neg_lo:[0,1]\n"
    "v_pk_fma_f32 v[48:49], v[44:45], v[28:29], v[20:21] op_sel_hi:[0,1,1]\n"
    "v_pk_mul_f32 v[20:21], v[48:49], v[26:27] op_sel:[1,1] op_sel_hi:[1,0] neg_lo:[0,1]\n"
    "v_pk_fma_f32 v[50:51], v[48:49], v[26:27], v[20:21] op_sel_hi:[0,1,1]\n"
    "v_pk_mul_f32 v[20:21], v[44:45], v[30:31] op_sel:[1,1] op_sel_hi:[1,0] neg_lo:[0,1]\n"
    "v_pk_fma_f32 v[36:37], v[44:45], v[30:31], v[20:21] op_sel_hi:[0,1,1]\n"
    "v_cvt_pk_f16_f32 v54, v44, v46\n"
    "v_cvt_pk_f16_f32 v55, v48, v50\n"
    "v_cvt_pk_f16_f32 v56, -v45, -v47\n"
    "v_cvt_pk_f16_f32 v57, -v49, -v51\n"
    "ds_write_b64 v2, v[54:55] offset:2176\n"
    "ds_write_b64 v2, v[56:57] offset:8704\n"
    "v_pk_mul_f32 v[20:21], v[36:37], v[30:31] op_sel:[1,1] op_sel_hi:[1,0] neg_lo:[0,1]\n"
    "v_pk_fma_f32 v[38:39], v[36:37], v[30:31], v[20:21] op_sel_hi:[0,1,1]\n"
    "v_pk_mul_f32 v[20:21], v[36:37], v[28:29] op_sel:[1,1] op_sel_hi:[1,0] neg_lo:[0,1]\n"
    "v_pk_fma_f32 v[40:41], v[36:37], v[28:29], v[20:21] op_sel_hi:[0,1,1]\n"
    "v_pk_mul_f32 v[20:21], v[38:39], v[28:29] op_sel:[1,1] op_sel_hi:[1,0] neg_lo:[0,1]\n"
    "v_pk_fma_f32 v[42:43], v[38:39], v[28:29], v[20:21] op_sel_hi:[0,1,1]\n"
    "v_pk_mul_f32 v[20:21], v[38:39], v[30:31] op_sel:[1,1] op_sel_hi:[1,0] neg_lo:[0,1]\n"
    "v_pk_fma_f32 v[44:45], v[38:39], v[30:31], v[20:21] op_sel_hi:[0,1,1]\n"
    "v_cvt_pk_f16_f32 v58, v40, v42\n"
    "v_cvt_pk_f16_f32 v59, v36, v38\n"
    "v_cvt_pk_f16_f32 v60, -v41, -v43\n"
    "v_cvt_pk_f16_f32 v61, -v37, -v39\n"
    "ds_write_b64 v2, v[58:59] offset:3264\n"
    "ds_write_b64 v2, v[60:61] offset:9792\n"
    "v_pk_mul_f32 v[20:21], v[44:45], v[30:31] op_sel:[1,1] op_sel_hi:[1,0] neg_lo:[0,1]\n"
    "v_pk_fma_f32 v[46:47], v[44:45], v[30:31], v[20:21] op_sel_hi:[0,1,1]\n"
    "v_pk_mul_f32 v[20:21], v[46:47], v[30:31] op_sel:[1,1] op_sel_hi:[1,0] neg_lo:[0,1]\n"
    "v_pk_fma_f32 v[48:49], v[46:47], v[30:31], v[20:21] op_sel_hi:[0,1,1]\n"
    "v_pk_mul_f32 v[20:21], v[48:49], v[30:31] op_sel:[1,1] op_sel_hi:[1,0] neg_lo:[0,1]\n"
    "v_pk_fma_f32 v[50:51], v[48:49], v[30:31], v[20:21] op_sel_hi:[0,1,1]\n"
    "v_pk_mul_f32 v[20:21], v[50:51], v[30:31] op_sel:[1,1] op_sel_hi:[1,0] neg_lo:[0,1]\n"
    "v_pk_fma_f32 v[36:37], v[50:51], v[30:31], v[20:21] op_sel_hi:[0,1,1]\n"
    "v_cvt_pk_f16_f32 v54, v44, v46\n"
    "v_cvt_pk_f16_f32 v55, v48, v50\n"
    "v_cvt_pk_f16_f32 v56, -v45, -v47\n"
    "v_cvt_pk_f16_f32 v57, -v49, -v51\n"
    "ds_write_b64 v2, v[54:55] offset:4352\n"
    "ds_write_b64 v2, v[56:57] offset:10880\n"
    "v_cvt_pk_f16_f32 v38, v36, -v37\n"
    "ds_write_b16 v2, v38 offset:5440\n"
    "ds_write_b16_d16_hi v2, v38 offset:11968\n"
    "s_mov_b64 exec, 1\n"
    "ds_add_u32 v13, v15 offset:12\n"
    "s_mov_b64 exec, -1\n"
    "s_memrealtime s[98:99]\n"
    "s_cmpk_ge_u32 s37, 4\n"
    "s_cbranch_scc1 CH45\n"
    "s_waitcnt lgkmcnt(0)\n"
    "s_sub_u32 s61, s98, s96\n"
    "s_cmpk_lt_u32 s61, 200\n"
    "s_cbranch_scc1 BSIDE\n"
    "buffer_load_dwordx4 v[68:71], v66, s[20:23], s40 offen sc1\n"
    "buffer_load_dwordx4 v[72:75], v66, s[20:23], s41 offen sc1\n"
    "BSIDE:\n"
    "v_mov_b32 v36, 0x46800000\n"
    "v_mov_b32 v37, 0\n"
    "v_pk_mul_f32 v[38:39], v[18:19], v[36:37] op_sel_hi:[1,0]\n"
    "v_cvt_pk_f16_f32 v44, v36, v38\n"
    "v_cvt_pk_f16_f32 v46, v37, v39\n"
    "v_pk_mul_f32 v[36:37], v[16:17], v[36:37] op_sel_hi:[1,0]\n"
    "v_pk_mul_f32 v[42:43], v[38:39], v[16:17] op_sel:[1,1] op_sel_hi:[1,0] neg_lo:[0,1]\n"
    "v_pk_fma_f32 v[38:39], v[38:39], v[16:17], v[42:43] op_sel_hi:[0,1,1]\n"
    "v_cvt_pk_f16_f32 v45, v36, v38\n"
    "v_cvt_pk_f16_f32 v47, v37, v39\n"
    "v_pk_mul_f32 v[40:41], v[36:37], v[16:17] op_sel:[1,1] op_sel_hi:[1,0] neg_lo:[0,1]\n"
    "v_pk_fma_f32 v[36:37], v[36:37], v[16:17], v[40:41] op_sel_hi:[0,1,1]\n"
    "ds_write_b64 v2, v[44:45] offset:13056\n"
    "v_pk_mul_f32 v[42:43], v[38:39], v[16:17] op_sel:[1,1] op_sel_hi:[1,0] neg_lo:[0,1]\n"
    "v_pk_fma_f32 v[38:39], v[38:39], v[16:17], v[42:43] op_sel_hi:[0,1,1]\n"
    "ds_write_b64 v2, v[46:47] offset:21760\n"
    "v_cvt_pk_f16_f32 v48, v36, v38\n"
    "v_cvt_pk_f16_f32 v50, v37, v39\n"
    "v_pk_mul_f32 v[40:41], v[36:37], v[16:17] op_sel:[1,1] op_sel_hi:[1,0] neg_lo:[0,1]\n"
    "v_pk_fma_f32 v[36:37], v[36:37], v[16:17], v[40:41] op_sel_hi:[0,1,1]\n"
    "v_pk_mul_f32 v[42:43], v[38:39], v[16:17] op_sel:[1,1] op_sel_hi:[1,0] neg_lo:[0,1]\n"
    "v_pk_fma_f32 v[38:39], v[38:39], v[16:17], v[42:43] op_sel_hi:[0,1,1]\n"
    "v_cvt_pk_f16_f32 v49, v36, v38\n"
    "v_cvt_pk_f16_f32 v51, v37, v39\n"
    "v_pk_mul_f32 v[40:41], v[36:37], v[16:17] op_sel:[1,1] op_sel_hi:[1,0] neg_lo:[0,1]\n"
    "v_pk_fma_f32 v[36:37], v[36:37], v[16:17], v[40:41] op_sel_hi:[0,1,1]\n"
    "ds_write_b64 v2, v[48:49] offset:14144\n"
    "v_pk_mul_f32 v[42:43], v[38:39], v[16:17] op_sel:[1,1] op_sel_hi:[1,0] neg_lo:[0,1]\n"
    "v_pk_fma_f32 v[38:39], v[38:39], v[16:17], v[42:43] op_sel_hi:[0,1,1]\n"
    "ds_write_b64 v2, v[50:51] offset:22848\n"
    "v_cvt_pk_f16_f32 v44, v36, v38\n"
    "v_cvt_pk_f16_f32 v46, v37, v39\n"
    "v_pk_mul_f32 v[40:41], v[36:37], v[16:17] op_sel:[1,1] op_sel_hi:[1,0] neg_lo:[0,1]\n"
    "v_pk_fma_f32 v[36:37], v[36:37], v[16:17], v[40:41] op_sel_hi:[0,1,1]\n"
    "v_pk_mul_f32 v[42:43], v[38:39], v[16:17] op_sel:[1,1] op_sel_hi:[1,0] neg_lo:[0,1]\n"
    "v_pk_fma_f32 v[38:39], v[38:39], v[16:17], v[42:43] op_sel_hi:[0,1,1]\n"
    "v_cvt_pk_f16_f32 v45, v36, v38\n"
    "v_cvt_pk_f16_f32 v47, v37, v39\n"
    "v_pk_mul_f32 v[40:41], v[36:37], v[16:17] op_sel:[1,1] op_sel_hi:[1,0] neg_lo:[0,1]\n"
    "v_pk_fma_f32 v[36:37], v[36:37], v[16:17], v[40:41] op_sel_hi:[0,1,1]\n"
    "ds_write_b64 v2, v[44:45] offset:15232\n"
    "v_pk_mul_f32 v[42:43], v[38:39], v[16:17] op_sel:[1,1] op_sel_hi:[1,0] neg_lo:[0,1]\n"
    "v_pk_fma_f32 v[38:39], v[38:39], v[16:17], v[42:43] op_sel_hi:[0,1,1]\n"
    "ds_write_b64 v2, v[46:47] offset:23936\n"
    "v_cvt_pk_f16_f32 v48, v36, v38\n"
    "v_cvt_pk_f16_f32 v50, v37, v39\n"
    "v_pk_mul_f32 v[40:41], v[36:37], v[16:17] op_sel:[1,1] op_sel_hi:[1,0] neg_lo:[0,1]\n"
    "v_pk_fma_f32 v[36:37], v[36:37], v[16:17], v[40:41] op_sel_hi:[0,1,1]\n"
    "v_pk_mul_f32 v[42:43], v[38:39], v[16:17] op_sel:[1,1] op_sel_hi:[1,0] neg_lo:[0,1]\n"
    "v_pk_fma_f32 v[38:39], v[38:39], v[16:17], v[42:43] op_sel_hi:[0,1,1]\n"
    "v_cvt_pk_f16_f32 v49, v36, v38\n"
    "v_cvt_pk_f16_f32 v51, v37, v39\n"
    "v_pk_mul_f32 v[40:41], v[36:37], v[16:17] op_sel:[1,1] op_sel_hi:[1,0] neg_lo:[0,1]\n"
    "v_pk_fma_f32 v[36:37], v[36:37], v[16:17], v[40:41] op_sel_hi:[0,1,1]\n"
    "ds_write_b64 v2, v[48:49] offset:17408\n"
    "v_pk_mul_f32 v[42:43], v[38:39], v[16:17] op_sel:[1,1] op_sel_hi:[1,0] neg_lo:[0,1]\n"
    "v_pk_fma_f32 v[38:39], v[38:39], v[16:17], v[42:43] op_sel_hi:[0,1,1]\n"
    "ds_write_b64 v2, v[50:51] offset:26112\n"
    "v_cvt_pk_f16_f32 v44, v36, v38\n"
    "v_cvt_pk_f16_f32 v46, v37, v39\n"
    "v_pk_mul_f32 v[40:41], v[36:37], v[16:17] op_sel:[1,1] op_sel_hi:[1,0] neg_lo:[0,1]\n"
    "v_pk_fma_f32 v[36:37], v[36:37], v[16:17], v[40:41] op_sel_hi:[0,1,1]\n"
    "v_pk_mul_f32 v[42:43], v[38:39], v[16:17] op_sel:[1,1] op_sel_hi:[1,0] neg_lo:[0,1]\n"
    "v_pk_fma_f32 v[38:39], v[38:39], v[16:17], v[42:43] op_sel_hi:[0,1,1]\n"
    "v_cvt_pk_f16_f32 v45, v36, v38\n"
    "v_cvt_pk_f16_f32 v47, v37, v39\n"
    "v_pk_mul_f32 v[40:41], v[36:37], v[16:17] op_sel:[1,1] op_sel_hi:[1,0] neg_lo:[0,1]\n"
    "v_pk_fma_f32 v[36:37], v[36:37], v[16:17], v[40:41] op_sel_hi:[0,1,1]\n"
    "ds_write_b64 v2, v[44:45] offset:18496\n"
    "v_pk_mul_f32 v[42:43], v[38:39], v[16:17] op_sel:[1,1] op_sel_hi:[1,0] neg_lo:[0,1]\n"
    "v_pk_fma_f32 v[38:39], v[38:39], v[16:17], v[42:43] op_sel_hi:[0,1,1]\n"
    "ds_write_b64 v2, v[46:47] offset:27200\n"
    "v_cvt_pk_f16_f32 v48, v36, v38\n"
    "v_cvt_pk_f16_f32 v50, v37, v39\n"
    "v_pk_mul_f32 v[40:41], v[36:37], v[16:17] op_sel:[1,1] op_sel_hi:[1,0] neg_lo:[0,1]\n"
    "v_pk_fma_f32 v[36:37], v[36:37], v[16:17], v[40:41] op_sel_hi:[0,1,1]\n"
    "v_pk_mul_f32 v[42:43], v[38:39], v[16:17] op_sel:[1,1] op_sel_hi:[1,0] neg_lo:[0,1]\n"
    "v_pk_fma_f32 v[38:39], v[38:39], v[16:17], v[42:43] op_sel_hi:[0,1,1]\n"
    "v_cvt_pk_f16_f32 v49, v36, v38\n"
    "v_cvt_pk_f16_f32 v51, v37, v39\n"
    "v_pk_mul_f32 v[40:41], v[36:37], v[16:17] op_sel:[1,1] op_sel_hi:[1,0] neg_lo:[0,1]\n"
    "v_pk_fma_f32 v[36:37], v[36:37], v[16:17], v[40:41] op_sel_hi:[0,1,1]\n"
    "ds_write_b64 v2, v[48:49] offset:19584\n"
    "v_pk_mul_f32 v[42:43], v[38:39], v[16:17] op_sel:[1,1] op_sel_hi:[1,0] neg_lo:[0,1]\n"
    "v_pk_fma_f32 v[38:39], v[38:39], v[16:17], v[42:43] op_sel_hi:[0,1,1]\n"
    "ds_write_b64 v2, v[50:51] offset:28288\n"
    "v_cvt_pk_f16_f32 v44, v36, v38\n"
    "v_cvt_pk_f16_f32 v46, v37, v39\n"
    "v_pk_mul_f32 v[40:41], v[36:37], v[16:17] op_sel:[1,1] op_sel_hi:[1,0] neg_lo:[0,1]\n"
    "v_pk_fma_f32 v[36:37], v[36:37], v[16:17], v[40:41] op_sel_hi:[0,1,1]\n"
    "v_pk_mul_f32 v[42:43], v[38:39], v[16:17] op_sel:[1,1] op_sel_hi:[1,0] neg_lo:[0,1]\n"
    "v_pk_fma_f32 v[38:39], v[38:39], v[16:17], v[42:43] op_sel_hi:[0,1,1]\n"
    "v_cvt_pk_f16_f32 v45, v36, v38\n"
    "v_cvt_pk_f16_f32 v47, v37, v39\n"
    "v_pk_mul_f32 v[40:41], v[36:37], v[16:17] op_sel:[1,1] op_sel_hi:[1,0] neg_lo:[0,1]\n"
    "v_pk_fma_f32 v[36:37], v[36:37], v[16:17], v[40:41] op_sel_hi:[0,1,1]\n"
    "ds_write_b64 v2, v[44:45] offset:20672\n"
    "v_pk_mul_f32 v[42:43], v[38:39], v[16:17] op_sel:[1,1] op_sel_hi:[1,0] neg_lo:[0,1]\n"
    "v_pk_fma_f32 v[38:39], v[38:39], v[16:17], v[42:43] op_sel_hi:[0,1,1]\n"
    "ds_write_b64 v2, v[46:47] offset:29376\n"
    "v_cvt_pk_f16_f32 v48, v36, v38\n"
    "v_cvt_pk_f16_f32 v50, v37, v39\n"
    "v_pk_mul_f32 v[40:41], v[36:37], v[16:17] op_sel:[1,1] op_sel_hi:[1,0] neg_lo:[0,1]\n"
    "v_pk_fma_f32 v[36:37], v[36:37], v[16:17], v[40:41] op_sel_hi:[0,1,1]\n"
    "v_pk_mul_f32 v[42:43], v[38:39], v[16:17] op_sel:[1,1] op_sel_hi:[1,0] neg_lo:[0,1]\n"
    "v_pk_fma_f32 v[38:39], v[38:39], v[16:17], v[42:43] op_sel_hi:[0,1,1]\n"
    "v_cvt_pk_f16_f32 v49, v36, v38\n"
    "v_cvt_pk_f16_f32 v51, v37, v39\n"
    "ds_write_b64 v2, v[48:49] offset:16320\n"
    "ds_write_b64 v2, v[50:51] offset:25024\n"
    "s_mov_b64 exec, 1\n"
    "ds_add_u32 v13, v15 offset:28\n"
    "s_mov_b64 exec, -1\n"
    "s_cmpk_ge_u32 s37, 14\n"
    "s_cbranch_scc1 AUX3\n"
    "s_cmpk_ge_u32 s61, 200\n"
    "s_cbranch_scc1 STAGE\n"
    "s_branch XISS\n"
    "CH45:\n"
    "s_mov_b64 exec, 0\n"
    "s_cmpk_eq_u32 s37, 5\n"
    "s_cbranch_scc1 CH5\n"
    "CH4:\n"
    "s_branch HELP\n"
    "CH5:\n"
    "s_branch HELP\n"
    "KST:\n"
    "s_branch E_4\n"
    "R_3:\n"
    "s_sleep 1\n"
    "s_sub_u32 s39, s39, 1\n"
    "s_cbranch_scc1 ABORT\n"
    "E_4:\n"
    "ds_read_b32 v112, v14 offset:24\n"
    "ds_read_b128 v[16:19], v124 offset:0\n"
    "ds_read_b128 v[20:23], v124 offset:1024\n"
    "ds_read_b128 v[24:27], v124 offset:2048\n"
    "ds_read_b128 v[28:31], v124 offset:3072\n"
    "s_waitcnt lgkmcnt(0)\n"
    "v_cmp_ne_u32 vcc, 1, v112\n"
    "s_cbranch_vccnz R_3\n"
    "v_add_u32 v28, s49, v28\n"
    "v_add_u32 v29, s49, v29\n"
    "s_mov_b32 s46, 0x01000100\n"
    "s_mov_b64 s[50:51], 0x1ffff\n"
    "s_branch E_6\n"
    "R_5:\n"
    "s_sleep 2\n"
    "s_sub_u32 s39, s39, 1\n"
    "s_cbranch_scc1 ABORT\n"
    "E_6:\n"
    "ds_read_b32 v112, v125 offset:12\n"
    "s_waitcnt lgkmcnt(0)\n"
    "v_cmp_ne_u32 vcc, 2, v112\n"
    "s_cbranch_vccnz R_5\n"
    "ds_read_b64_tr_b16 v[32:33], v122 offset:0\n"
    "ds_read_b64_tr_b16 v[34:35], v122 offset:6528\n"
    "ds_read_b64_tr_b16 v[36:37], v122 offset:4352\n"
    "ds_read_b64_tr_b16 v[38:39], v122 offset:10880\n"
    "ds_read_b64_tr_b16 v[40:41], v122 offset:128\n"
    "ds_read_b64_tr_b16 v[42:43], v122 offset:6656\n"
    "ds_read_b64_tr_b16 v[44:45], v122 offset:4480\n"
    "ds_read_b64_tr_b16 v[46:47], v122 offset:11008\n"
    "ds_read_b64_tr_b16 v[48:49], v122 offset:256\n"
    "ds_read_b64_tr_b16 v[50:51], v122 offset:6784\n"
    "ds_read_b64_tr_b16 v[52:53], v122 offset:4608\n"
    "ds_read_b64_tr_b16 v[54:55], v122 offset:11136\n"
    "ds_read_b64_tr_b16 v[56:57], v122 offset:384\n"
    "ds_read_b64_tr_b16 v[58:59], v122 offset:6912\n"
    "ds_read_b64_tr_b16 v[60:61], v122 offset:4736\n"
    "ds_read_b64_tr_b16 v[62:63], v122 offset:11264\n"
    "ds_read_b64_tr_b16 v[64:65], v122 offset:512\n"
    "ds_read_b64_tr_b16 v[66:67], v122 offset:7040\n"
    "ds_read_b64_tr_b16 v[68:69], v122 offset:4864\n"
    "ds_read_b64_tr_b16 v[70:71], v122 offset:11392\n"
    "ds_read_b64_tr_b16 v[72:73], v122 offset:640\n"
    "ds_read_b64_tr_b16 v[74:75], v122 offset:7168\n"
    "ds_read_b64_tr_b16 v[76:77], v122 offset:4992\n"
    "ds_read_b64_tr_b16 v[78:79], v122 offset:11520\n"
    "ds_read_b64_tr_b16 v[80:81], v122 offset:768\n"
    "ds_read_b64_tr_b16 v[82:83], v122 offset:7296\n"
    "ds_read_b64_tr_b16 v[84:85], v122 offset:5120\n"
    "ds_read_b64_tr_b16 v[86:87], v122 offset:11648\n"
    "ds_read_b64_tr_b16 v[88:89], v122 offset:896\n"
    "ds_read_b64_tr_b16 v[90:91], v122 offset:7424\n"
    "ds_read_b64_tr_b16 v[92:93], v122 offset:5248\n"
    "ds_read_b64_tr_b16 v[94:95], v122 offset:11776\n"
    "ds_read_b32 v12, v126\n"
    "s_branch E_8\n"
    "R_7:\n"
    "s_sleep 1\n"
    "s_sub_u32 s39, s39, 1\n"
    "s_cbranch_scc1 ABORT\n"
    "E_8:\n"
    "ds_read_b32 v112, v14 offset:28\n"
    "ds_read_b64 v[114:115], v14 offset:32\n"
    "s_waitcnt lgkmcnt(0)\n"
    "v_add3_u32 v112, v112, v114, v115\n"
    "v_cmp_ne_u32 vcc, 6, v112\n"
    "s_cbranch_vccnz R_7\n"
    "s_setprio 3\n"
    "ds_read_b64_tr_b16 v[96:97], v123 offset:13056\n"
    "ds_read_b64_tr_b16 v[98:99], v123 offset:21760\n"
    "ds_read_b64_tr_b16 v[100:101], v123 offset:13184\n"
    "ds_read_b64_tr_b16 v[102:103], v123 offset:21888\n"
    "ds_read_b64_tr_b16 v[104:105], v123 offset:13312\n"
    "ds_read_b64_tr_b16 v[106:107], v123 offset:22016\n"
    "ds_read_b64_tr_b16 v[108:109], v123 offset:13440\n"
    "ds_read_b64_tr_b16 v[110:111], v123 offset:22144\n"
    "s_waitcnt lgkmcnt(6)\n"
    "v_mfma_f32_16x16x32_f16 v[112:115], v[32:35], v[96:99], 0\n"
    "v_mfma_f32_16x16x32_f16 v[116:119], v[36:39], v[96:99], 0\n"
    "ds_read_b64_tr_b16 v[96:97], v123 offset:13568\n"
    "ds_read_b64_tr_b16 v[98:99], v123 offset:22272\n"
    "s_waitcnt lgkmcnt(6)\n"
    "v_mfma_f32_16x16x32_f16 v[112:115], v[40:43], v[100:103], v[112:115]\n"
    "v_mfma_f32_16x16x32_f16 v[116:119], v[44:47], v[100:103], v[116:119]\n"
    "ds_read_b64_tr_b16 v[100:101], v123 offset:13696\n"
    "ds_read_b64_tr_b16 v[102:103], v123 offset:22400\n"
    "s_waitcnt lgkmcnt(6)\n"
    "v_mfma_f32_16x16x32_f16 v[112:115], v[48:51], v[104:107], v[112:115]\n"
    "v_mfma_f32_16x16x32_f16 v[116:119], v[52:55], v[104:107], v[116:119]\n"
    "ds_read_b64_tr_b16 v[104:105], v123 offset:13824\n"
    "ds_read_b64_tr_b16 v[106:107], v123 offset:22528\n"
    "s_waitcnt lgkmcnt(6)\n"
    "v_mfma_f32_16x16x32_f16 v[112:115], v[56:59], v[108:111], v[112:115]\n"
    "v_mfma_f32_16x16x32_f16 v[116:119], v[60:63], v[108:111], v[116:119]\n"
    "ds_read_b64_tr_b16 v[108:109], v123 offset:13952\n"
    "ds_read_b64_tr_b16 v[110:111], v123 offset:22656\n"
    "s_waitcnt lgkmcnt(6)\n"
    "v_mfma_f32_16x16x32_f16 v[112:115], v[64:67], v[96:99], v[112:115]\n"
    "v_mfma_f32_16x16x32_f16 v[116:119], v[68:71], v[96:99], v[116:119]\n"
    "s_waitcnt lgkmcnt(4)\n"
    "v_mfma_f32_16x16x32_f16 v[112:115], v[72:75], v[100:103], v[112:115]\n"
    "v_mfma_f32_16x16x32_f16 v[116:119], v[76:79], v[100:103], v[116:119]\n"
    "s_waitcnt lgkmcnt(2)\n"
    "v_mfma_f32_16x16x32_f16 v[112:115], v[80:83], v[104:107], v[112:115]\n"
    "v_mfma_f32_16x16x32_f16 v[116:119], v[84:87], v[104:107], v[116:119]\n"
    "s_waitcnt lgkmcnt(0)\n"
    "v_mfma_f32_16x16x32_f16 v[112:115], v[88:91], v[108:111], v[112:115]\n"
    "v_mfma_f32_16x16x32_f16 v[116:119], v[92:95], v[108:111], v[116:119]\n"
    "s_cmp_eq_u32 s47, 1\n"
    "s_cbranch_scc1 KT1\n"
    "s_cmp_eq_u32 s47, 2\n"
    "s_cbranch_scc1 KT2\n"
    "KT0:\n"
    "s_nop 2\n"
    "v_pk_mul_f32 v[112:113], v[112:113], v[12:13] op_sel_hi:[1,0]\n"
    "v_pk_mul_f32 v[114:115], v[114:115], v[12:13] op_sel_hi:[1,0]\n"
    "v_cvt_pk_f16_f32 v112, v112, v113\n"
    "v_cvt_pk_f16_f32 v113, v114, v115\n"
    "v_pk_mul_f32 v[116:117], v[116:117], v[12:13] op_sel_hi:[1,0]\n"
    "v_perm_b32 v114, v113, v112, v17\n"
    "v_perm_b32 v115, v113, v112, v18\n"
    "v_perm_b32 v32, v113, v112, v30\n"
    "v_perm_b32 v33, v113, v113, s46\n"
    "v_perm_b32 v112, v113, v112, v19\n"
    "s_mov_b32 exec_hi, s50\n"
    "ds_write_b64 v16, v[114:115] offset:0\n"
    "s_mov_b32 exec_lo, 0\n"
    "ds_write2_b32 v28, v114, v32 offset1:1\n"
    "ds_write2_b32 v29, v33, v113 offset1:1\n"
    "s_mov_b64 exec, -1\n"
    "v_pk_mul_f32 v[118:119], v[118:119], v[12:13] op_sel_hi:[1,0]\n"
    "ds_write_b16 v22, v113 offset:0\n"
    "ds_write_b16_d16_hi v23, v113 offset:0\n"
    "v_cvt_pk_f16_f32 v116, v116, v117\n"
    "ds_write_b16 v20, v112 offset:0\n"
    "ds_write_b16_d16_hi v21, v112 offset:0\n"
    "v_cvt_pk_f16_f32 v117, v118, v119\n"
    "s_mov_b64 exec, s[50:51]\n"
    "ds_write_b16 v24, v116 offset:0\n"
    "ds_write_b16_d16_hi v25, v116 offset:0\n"
    "ds_write_b16 v26, v117 offset:0\n"
    "ds_write_b16_d16_hi v27, v117 offset:0\n"
    "s_mov_b64 exec, 1\n"
    "ds_add_u32 v14, v15 offset:224\n"
    "s_mov_b64 exec, -1\n"
    "s_branch KT3\n"
    "KT1:\n"
    "s_nop 4\n"
    "v_pk_mul_f32 v[112:113], v[112:113], v[12:13] op_sel_hi:[1,0]\n"
    "v_pk_mul_f32 v[114:115], v[114:115], v[12:13] op_sel_hi:[1,0]\n"
    "v_cvt_pk_f16_f32 v112, v112, v113\n"
    "v_cvt_pk_f16_f32 v113, v114, v115\n"
    "v_pk_mul_f32 v[116:117], v[116:117], v[12:13] op_sel_hi:[1,0]\n"
    "v_perm_b32 v114, v113, v112, v17\n"
    "v_perm_b32 v115, v113, v112, v18\n"
    "v_perm_b32 v32, v113, v112, v30\n"
    "v_perm_b32 v33, v113, v113, s46\n"
    "v_perm_b32 v112, v113, v112, v19\n"
    "s_mov_b32 exec_hi, s50\n"
    "ds_write_b64 v16, v[114:115] offset:3328\n"
    "s_mov_b32 exec_lo, 0\n"
    "ds_write2_b32 v28, v114, v32 offset1:1\n"
    "ds_write2_b32 v29, v33, v113 offset1:1\n"
    "s_mov_b64 exec, -1\n"
    "v_pk_mul_f32 v[118:119], v[118:119], v[12:13] op_sel_hi:[1,0]\n"
    "ds_write_b16 v22, v113 offset:3328\n"
    "ds_write_b16_d16_hi v23, v113 offset:3328\n"
    "v_cvt_pk_f16_f32 v116, v116, v117\n"
    "ds_write_b16 v20, v112 offset:3328\n"
    "ds_write_b16_d16_hi v21, v112 offset:3328\n"
    "v_cvt_pk_f16_f32 v117, v118, v119\n"
    "s_mov_b64 exec, s[50:51]\n"
    "ds_write_b16 v24, v116 offset:3328\n"
    "ds_write_b16_d16_hi v25, v116 offset:3328\n"
    "ds_write_b16 v26, v117 offset:3328\n"
    "ds_write_b16_d16_hi v27, v117 offset:3328\n"
    "s_mov_b64 exec, 1\n"
    "ds_add_u32 v14, v15 offset:224\n"
    "s_mov_b64 exec, -1\n"
    "s_branch KT3\n"
    "KT2:\n"
    "s_nop 2\n"
    "v_pk_mul_f32 v[112:113], v[112:113], v[12:13] op_sel_hi:[1,0]\n"
    "v_pk_mul_f32 v[114:115], v[114:115], v[12:13] op_sel_hi:[1,0]\n"
    "v_cvt_pk_f16_f32 v112, v112, v113\n"
    "v_cvt_pk_f16_f32 v113, v114, v115\n"
    "v_pk_mul_f32 v[116:117], v[116:117], v[12:13] op_sel_hi:[1,0]\n"
    "v_perm_b32 v114, v113, v112, v17\n"
    "v_perm_b32 v115, v113, v112, v18\n"
    "v_perm_b32 v32, v113, v112, v30\n"
    "v_perm_b32 v33, v113, v113, s46\n"
    "v_perm_b32 v112, v113, v112, v19\n"
    "s_mov_b32 exec_hi, s50\n"
    "ds_write_b64 v16, v[114:115] offset:6656\n"
    "s_mov_b32 exec_lo, 0\n"
    "ds_write2_b32 v28, v114, v32 offset1:1\n"
    "ds_write2_b32 v29, v33, v113 offset1:1\n"
    "s_mov_b64 exec, -1\n"
    "v_pk_mul_f32 v[118:119], v[118:119], v[12:13] op_sel_hi:[1,0]\n"
    "ds_write_b16 v22, v113 offset:6656\n"
    "ds_write_b16_d16_hi v23, v113 offset:6656\n"
    "v_cvt_pk_f16_f32 v116, v116, v117\n"
    "ds_write_b16 v20, v112 offset:6656\n"
    "ds_write_b16_d16_hi v21, v112 offset:6656\n"
    "v_cvt_pk_f16_f32 v117, v118, v119\n"
    "s_mov_b64 exec, s[50:51]\n"
    "ds_write_b16 v24, v116 offset:6656\n"
    "ds_write_b16_d16_hi v25, v116 offset:6656\n"
    "ds_write_b16 v26, v117 offset:6656\n"
    "ds_write_b16_d16_hi v27, v117 offset:6656\n"
    "s_mov_b64 exec, 1\n"
    "ds_add_u32 v14, v15 offset:224\n"
    "s_mov_b64 exec, -1\n"
    "KT3:\n"
    "s_setprio 1\n"
    "s_cmp_eq_u32 s48, 0\n"
    "s_cbranch_scc1 NEAR1\n"
    "s_cmpk_eq_u32 s37, 6\n"
    "s_cbranch_scc1 HELP1\n"
    "s_branch FAR1\n"
    "AUX1:\n"
    "s_waitcnt lgkmcnt(0)\n"
    "s_load_dword s36, s[20:21], 0\n"
    "s_barrier\n"
    "v_lshlrev_b32 v1, 4, v0\n"
    "v_and_b32 v8, 63, v0\n"
    "v_mov_b32 v15, 1\n"
    "v_mov_b32 v14, 147968\n"
    "s_cmpk_gt_u32 s37, 13\n"
    "s_cbranch_scc1 AUXB\n"
    "s_getpc_b64 s[44:45]\n"
    "s_add_u32 s44, s44, mega_tab@rel32@lo+4\n"
    "s_addc_u32 s45, s45, mega_tab@rel32@hi+12\n"
    "s_mov_b32 m0, 107520\n"
    "v_lshlrev_b32 v2, 4, v8\n"
    "v_add_u32 v3, 6144, v2\n"
    "global_load_lds_dwordx4 v3, s[44:45] offset:0\n"
    "global_load_lds_dwordx4 v3, s[44:45] offset:1024\n"
    "global_load_lds_dwordx4 v3, s[44:45] offset:2048\n"
    "global_load_lds_dwordx4 v3, s[44:45] offset:3072\n"
    "s_mov_b32 m0, 111616\n"
    "v_add_u32 v3, 4096, v3\n"
    "global_load_lds_dwordx4 v3, s[44:45] offset:0\n"
    "global_load_lds_dwordx4 v3, s[44:45] offset:1024\n"
    "global_load_lds_dwordx4 v3, s[44:45] offset:2048\n"
    "global_load_lds_dwordx4 v3, s[44:45] offset:3072\n"
    "s_mov_b32 m0, 101376\n"
    "s_nop 0\n"
    "global_load_lds_dwordx4 v2, s[44:45] offset:0\n"
    "global_load_lds_dwordx4 v2, s[44:45] offset:1024\n"
    "global_load_lds_dwordx4 v2, s[44:45] offset:2048\n"
    "global_load_lds_dwordx4 v2, s[44:45] offset:3072\n"
    "s_mov_b32 m0, 105472\n"
    "v_add_u32 v2, 4096, v2\n"
    "global_load_lds_dwordx4 v2, s[44:45] offset:0\n"
    "global_load_lds_dwordx4 v2, s[44:45] offset:1024\n"
    "v_mov_b32 v4, 0\n"
    "v_mov_b32 v5, 0\n"
    "v_mov_b32 v6, 0\n"
    "v_mov_b32 v7, 0\n"
    "v_add_u32 v2, 78080, v1\n"
    "ds_write_b128 v2, v[4:7] offset:0\n"
    "ds_write_b128 v2, v[4:7] offset:1024\n"
    "ds_write_b128 v2, v[4:7] offset:2048\n"
    "ds_write_b128 v2, v[4:7] offset:3072\n"
    "ds_write_b128 v2, v[4:7] offset:4096\n"
    "ds_write_b128 v2, v[4:7] offset:5120\n"
    "ds_write_b128 v2, v[4:7] offset:6144\n"
    "ds_write_b128 v2, v[4:7] offset:7168\n"
    "ds_write_b128 v2, v[4:7] offset:8192\n"
    "v_min_u32 v2, 92144, v2\n"
    "ds_write_b128 v2, v[4:7] offset:9216\n"
    "v_min_u32 v2, 29, v8\n"
    "v_lshlrev_b32 v2, 4, v2\n"
    "v_add_u32 v2, 120832, v2\n"
    "ds_write_b128 v2, v[4:7] offset:0\n"
    "ds_write_b128 v2, v[4:7] offset:4576\n"
    "ds_write_b128 v2, v[4:7] offset:9152\n"
    "s_lshr_b32 s44, s28, 7\n"
    "s_add_u32 s44, s44, s2\n"
    "s_sub_u32 s44, 3, s44\n"
    "s_and_b32 s44, s44, 7\n"
    "s_add_u32 s44, s44, 1\n"
    "s_add_u32 s44, s44, 8\n"
    "s_mul_i32 s45, s44, 57\n"
    "s_lshr_b32 s45, s45, 9\n"
    "s_mul_i32 s45, s45, 9\n"
    "s_sub_u32 s44, s44, s45\n"
    "s_and_b32 s43, s2, 7\n"
    "s_mul_i32 s43, s43, 9\n"
    "s_add_u32 s43, s43, s44\n"
    "s_lshr_b32 s42, s2, 3\n"
    "s_mul_i32 s44, s42, 221184\n"
    "s_lshl_b32 s45, s43, 7\n"
    "s_add_u32 s44, s44, s45\n"
    "v_min_u32 v3, 23, v8\n"
    "v_mul_u32_u24 v2, 9216, v3\n"
    "v_add_u32 v2, s44, v2\n"
    "s_mul_i32 s43, s43, 24\n"
    "s_add_u32 s43, s43, s42\n"
    "s_mul_i32 s43, s43, 3072\n"
    "v_lshl_add_u32 v3, v3, 7, s43\n"
    "s_mov_b32 m0, 148480\n"
    "s_mov_b64 exec, 0xffffff\n"
    "global_load_lds_dword v2, s[28:29] nt\n"
    "global_load_lds_dword v2, s[30:31] nt\n"
    "global_load_lds_dword v3, s[32:33] nt\n"
    "s_mov_b64 exec, -1\n"
    "s_branch AUX2\n"
    "AUXB:\n"
    "s_lshl_b32 s42, s37, 9\n"
    "s_add_u32 s42, s42, 53760\n"
    "v_lshl_add_u32 v2, v8, 3, s42\n"
    "v_add_u32 v3, 116128, v1\n"
    "s_lshl_b32 s42, s37, 2\n"
    "s_add_u32 s42, s42, 147912\n"
    "v_mov_b32 v12, s42\n"
    "v_mov_b32 v13, 147976\n"
    "s_branch E_10\n"
    "R_9:\n"
    "s_sleep 1\n"
    "s_sub_u32 s39, s39, 1\n"
    "s_cbranch_scc1 ABORT\n"
    "E_10:\n"
    "ds_read_b32 v20, v12 offset:4\n"
    "ds_read_b128 v[16:19], v3\n"
    "s_waitcnt lgkmcnt(0)\n"
    "v_cmp_ne_u32 vcc, 1, v20\n"
    "s_cbranch_vccnz R_9\n"
    "s_add_u32 s36, s36, 1\n"
    "s_setprio 1\n"
    "s_branch BSIDE\n"
    "AUX2:\n"
    "s_mov_b64 exec, -1\n"
    "s_waitcnt vmcnt(3) lgkmcnt(0)\n"
    "s_add_u32 s36, s36, 1\n"
    "s_mov_b64 exec, 1\n"
    "ds_add_u32 v14, v15 offset:24\n"
    "s_mov_b64 exec, -1\n"
    "s_waitcnt vmcnt(0)\n"
    "s_mov_b64 exec, 0xffffff\n"
    "global_load_lds_dword v2, s[28:29]\n"
    "global_load_lds_dword v2, s[30:31]\n"
    "global_load_lds_dword v3, s[32:33]\n"
    "s_mov_b64 exec, -1\n"
    "AUX3:\n"
    "s_mov_b64 exec, -1\n"
    "s_setprio 0\n"
    "s_and_b32 s44, s2, 7\n"
    "s_lshr_b32 s45, s2, 3\n"
    "s_mul_i32 s44, s44, 24\n"
    "s_add_u32 s38, s44, s45\n"
    "v_mov_b32 v14, 147968\n"
    "ds_read_b32 v2, v14 offset:480\n"
    "s_waitcnt lgkmcnt(0)\n"
    "v_readfirstlane_b32 s96, v2\n"
    "s_lshl_b32 s57, s37, 2\n"
    "s_mov_b32 s58, 0x78650432\n"
    "s_lshr_b32 s54, s58, s57\n"
    "s_and_b32 s54, s54, 15\n"
    "s_add_u32 s55, s54, 7\n"
    "s_cmpk_eq_u32 s37, 3\n"
    "s_cselect_b32 s55, 1, s55\n"
    "s_and_b32 s56, s38, 1\n"
    "s_mul_i32 s56, s56, 6144\n"
    "s_lshr_b32 s57, s38, 1\n"
    "s_lshl_b32 s57, s57, 6\n"
    "s_add_u32 s56, s56, s57\n"
    "s_add_u32 s56, s56, 7341056\n"
    "s_mul_i32 s40, s54, 198656\n"
    "s_add_u32 s40, s40, s56\n"
    "s_mul_i32 s41, s55, 198656\n"
    "s_add_u32 s41, s41, s56\n"
    "v_bfe_u32 v66, v0, 2, 4\n"
    "v_mul_u32_u24 v66, 12416, v66\n"
    "v_and_b32 v67, 3, v0\n"
    "v_lshl_add_u32 v66, v67, 4, v66\n"
    "STAGE:\n"
    "v_mov_b32 v78, 147968\n"
    "v_and_b32 v67, 63, v0\n"
    "v_lshlrev_b32 v67, 2, v67\n"
    "s_lshl_b32 s43, s54, 8\n"
    "s_sub_u32 s43, 120828, s43\n"
    "v_sub_u32 v86, s43, v67\n"
    "s_lshl_b32 s43, s55, 8\n"
    "s_sub_u32 s43, 120828, s43\n"
    "v_sub_u32 v87, s43, v67\n"
    "v_mov_b32 v76, 148352\n"
    "v_and_b32 v111, 63, v0\n"
    "s_lshr_b32 s50, s2, 3\n"
    "s_and_b32 s51, s2, 7\n"
    "s_cmpk_ge_u32 s2, 128\n"
    "s_cbranch_scc1 PFA\n"
    "s_lshl_b32 s51, s51, 4\n"
    "s_add_u32 s51, s51, s50\n"
    "s_mul_i32 s51, s51, 18432\n"
    "s_mul_i32 s60, s37, 768\n"
    "s_add_u32 s51, s51, s60\n"
    "v_lshl_add_u32 v111, v111, 4, s51\n"
    "s_add_u32 s60, s60, 53760\n"
    "s_mov_b64 s[58:59], s[4:5]\n"
    "s_branch PFC\n"
    "PFA:\n"
    "s_sub_u32 s50, s50, 16\n"
    "s_lshr_b32 s44, s28, 7\n"
    "s_add_u32 s44, s44, s2\n"
    "s_sub_u32 s44, 3, s44\n"
    "s_and_b32 s44, s44, 7\n"
    "s_add_u32 s44, s44, 1\n"
    "s_add_u32 s44, s44, 7\n"
    "s_mul_i32 s45, s44, 57\n"
    "s_lshr_b32 s45, s45, 9\n"
    "s_mul_i32 s45, s45, 9\n"
    "s_sub_u32 s44, s44, s45\n"
    "s_and_b32 s51, s2, 7\n"
    "s_mul_i32 s51, s51, 9\n"
    "s_add_u32 s51, s51, s44\n"
    "s_mov_b32 s60, s44\n"
    "s_lshl_b32 s51, s51, 7\n"
    "s_mul_i32 s50, s50, 48\n"
    "s_add_u32 s50, s50, 192\n"
    "s_mul_i32 s50, s50, 9216\n"
    "s_add_u32 s51, s51, s50\n"
    "v_min_u32 v111, 47, v111\n"
    "v_mul_u32_u24 v111, 9216, v111\n"
    "v_add_u32 v111, s51, v111\n"
    "PFC:\n"
    "s_mul_i32 s42, s38, 6\n"
    "s_and_b32 s42, s42, -4\n"
    "s_load_dwordx2 s[52:53], s[24:25], s42\n"
    "s_cmpk_eq_u32 s37, 3\n"
    "s_cbranch_scc1 FAR\n"
    "s_branch EPS\n"
    "STG0:\n"
    "s_cmpk_lt_u32 s37, 4\n"
    "s_cbranch_scc1 STG1\n"
    "s_cmpk_lg_u32 s37, 13\n"
    "s_cbranch_scc1 XISS\n"
    "s_branch CONST\n"
    "STG1:\n"
    "s_setprio 2\n"
    "s_branch E_12\n"
    "R_11:\n"
    "s_sub_u32 s39, s39, 1\n"
    "s_cbranch_scc1 ABORT\n"
    "s_wqm_b64 exec, vcc\n"
    "buffer_load_dwordx4 v[68:71], v66, s[20:23], s40 offen sc1\n"
    "buffer_load_dwordx4 v[72:75], v66, s[20:23], s41 offen sc1\n"
    "s_mov_b64 exec, -1\n"
    "E_12:\n"
    "s_waitcnt vmcnt(1)\n"
    "ds_read_b32 v77, v78 offset:40\n"
    "ds_read_b128 v[80:83], v76\n"
    "ds_read_b64 v[84:85], v76 offset:16\n"
    "s_waitcnt vmcnt(0)\n"
    "v_bitop3_b32 v79, v71, v75, s36 bitop3:0x7e\n"
    "v_cmp_ne_u32 vcc, 0, v79\n"
    "s_cbranch_vccnz R_11\n"
    "v_lshlrev_b32 v88, 16, v68\n"
    "v_and_b32 v89, 0xffff0000, v68\n"
    "v_lshlrev_b32 v90, 16, v69\n"
    "v_and_b32 v91, 0xffff0000, v69\n"
    "v_lshlrev_b32 v92, 16, v70\n"
    "v_and_b32 v93, 0xffff0000, v70\n"
    "STG3:\n"
    "s_waitcnt lgkmcnt(0)\n"
    "v_cmp_ne_u32 vcc, 1, v77\n"
    "s_cbranch_vccnz STGW\n"
    "ds_read_b128 v[114:117], v76 offset:32\n"
    "ds_read_b64 v[118:119], v76 offset:48\n"
    "v_pk_mul_f32 v[88:89], v[88:89], v[80:81] op_sel_hi:[1,0]\n"
    "v_pk_mul_f32 v[90:91], v[90:91], v[82:83] op_sel_hi:[1,0]\n"
    "v_pk_mul_f32 v[92:93], v[92:93], v[84:85] op_sel_hi:[1,0]\n"
    "v_cvt_pk_bf16_f32 v88, 0, v88\n"
    "v_cvt_pk_bf16_f32 v89, 0, v89\n"
    "v_cvt_pk_bf16_f32 v90, 0, v90\n"
    "v_cvt_pk_bf16_f32 v91, 0, v91\n"
    "v_cvt_pk_bf16_f32 v92, 0, v92\n"
    "v_cvt_pk_bf16_f32 v93, 0, v93\n"
    "v_pk_add_f32 v[88:89], v[80:81], v[88:89] op_sel:[1,0] op_sel_hi:[1,1]\n"
    "v_pk_add_f32 v[90:91], v[82:83], v[90:91] op_sel:[1,0] op_sel_hi:[1,1]\n"
    "v_pk_add_f32 v[92:93], v[84:85], v[92:93] op_sel:[1,0] op_sel_hi:[1,1]\n"
    "v_cvt_pk_bf16_f32 v88, 0, v88\n"
    "v_cvt_pk_bf16_f32 v89, 0, v89\n"
    "v_cvt_pk_bf16_f32 v90, 0, v90\n"
    "v_cvt_pk_bf16_f32 v91, 0, v91\n"
    "v_cvt_pk_bf16_f32 v92, 0, v92\n"
    "v_cvt_pk_bf16_f32 v93, 0, v93\n"
    "v_cvt_pkrtz_f16_f32 v94, v89, v88\n"
    "ds_write_b32 v86, v94 offset:0\n"
    "v_cvt_pkrtz_f16_f32 v95, v91, v90\n"
    "ds_write_b32 v86, v95 offset:4576\n"
    "v_cvt_pkrtz_f16_f32 v96, v93, v92\n"
    "ds_write_b32 v86, v96 offset:9152\n"
    "v_lshlrev_b32 v88, 16, v72\n"
    "v_and_b32 v89, 0xffff0000, v72\n"
    "v_lshlrev_b32 v90, 16, v73\n"
    "v_and_b32 v91, 0xffff0000, v73\n"
    "v_lshlrev_b32 v92, 16, v74\n"
    "v_and_b32 v93, 0xffff0000, v74\n"
    "v_pk_mul_f32 v[88:89], v[88:89], v[80:81] op_sel_hi:[1,0]\n"
    "v_pk_mul_f32 v[90:91], v[90:91], v[82:83] op_sel_hi:[1,0]\n"
    "v_pk_mul_f32 v[92:93], v[92:93], v[84:85] op_sel_hi:[1,0]\n"
    "v_cvt_pk_bf16_f32 v88, 0, v88\n"
    "v_cvt_pk_bf16_f32 v89, 0, v89\n"
    "v_cvt_pk_bf16_f32 v90, 0, v90\n"
    "v_cvt_pk_bf16_f32 v91, 0, v91\n"
    "v_cvt_pk_bf16_f32 v92, 0, v92\n"
    "v_cvt_pk_bf16_f32 v93, 0, v93\n"
    "v_pk_add_f32 v[88:89], v[80:81], v[88:89] op_sel:[1,0] op_sel_hi:[1,1]\n"
    "v_pk_add_f32 v[90:91], v[82:83], v[90:91] op_sel:[1,0] op_sel_hi:[1,1]\n"
    "v_pk_add_f32 v[92:93], v[84:85], v[92:93] op_sel:[1,0] op_sel_hi:[1,1]\n"
    "v_cvt_pk_bf16_f32 v88, 0, v88\n"
    "v_cvt_pk_bf16_f32 v89, 0, v89\n"
    "v_cvt_pk_bf16_f32 v90, 0, v90\n"
    "v_cvt_pk_bf16_f32 v91, 0, v91\n"
    "v_cvt_pk_bf16_f32 v92, 0, v92\n"
    "v_cvt_pk_bf16_f32 v93, 0, v93\n"
    "v_cvt_pkrtz_f16_f32 v97, v89, v88\n"
    "ds_write_b32 v87, v97 offset:0\n"
    "v_cvt_pkrtz_f16_f32 v98, v91, v90\n"
    "ds_write_b32 v87, v98 offset:4576\n"
    "v_cvt_pkrtz_f16_f32 v99, v93, v92\n"
    "ds_write_b32 v87, v99 offset:9152\n"
    "s_waitcnt lgkmcnt(0)\n"
    "s_barrier\n"
    "ds_read_b128 v[120:123], v76 offset:64\n"
    "ds_read_b64 v[124:125], v76 offset:80\n"
    "s_cmpk_eq_u32 s37, 3\n"
    "s_cbranch_scc1 FAR3\n"
    "s_cmpk_ge_u32 s2, 128\n"
    "s_cbranch_scc1 PFD\n"
    "s_cmpk_ge_u32 s37, 12\n"
    "s_cbranch_scc1 EPI\n"
    "s_mov_b32 m0, s60\n"
    "s_mov_b32 exec_hi, 0xffff\n"
    "global_load_lds_dwordx4 v111, s[58:59] nt\n"
    "s_add_u32 m0, m0, 2320\n"
    "s_add_u32 s58, s58, 2304\n"
    "s_addc_u32 s59, s59, 0\n"
    "global_load_lds_dwordx4 v111, s[58:59] nt\n"
    "s_add_u32 m0, m0, 2320\n"
    "s_add_u32 s58, s58, 2304\n"
    "s_addc_u32 s59, s59, 0\n"
    "global_load_lds_dwordx4 v111, s[58:59] nt\n"
    "s_add_u32 m0, m0, 2320\n"
    "s_add_u32 s58, s58, 2304\n"
    "s_addc_u32 s59, s59, 0\n"
    "global_load_lds_dwordx4 v111, s[58:59] nt\n"
    "s_add_u32 m0, m0, 2320\n"
    "s_add_u32 s58, s58, 2304\n"
    "s_addc_u32 s59, s59, 0\n"
    "global_load_lds_dwordx4 v111, s[58:59] nt\n"
    "s_add_u32 m0, m0, 2320\n"
    "s_add_u32 s58, s58, 2304\n"
    "s_addc_u32 s59, s59, 0\n"
    "global_load_lds_dwordx4 v111, s[58:59] nt\n"
    "s_add_u32 m0, m0, 2320\n"
    "s_add_u32 s58, s58, 2304\n"
    "s_addc_u32 s59, s59, 0\n"
    "global_load_lds_dwordx4 v111, s[58:59] nt\n"
    "s_add_u32 m0, m0, 2320\n"
    "s_add_u32 s58, s58, 2304\n"
    "s_addc_u32 s59, s59, 0\n"
    "global_load_lds_dwordx4 v111, s[58:59] nt\n"
    "s_add_u32 m0, m0, 2320\n"
    "s_add_u32 s58, s58, 2304\n"
    "s_addc_u32 s59, s59, 0\n"
    "s_mov_b64 exec, -1\n"
    "s_branch EPI\n"
    "PFD:\n"
    "s_mov_b32 m0, 148480\n"
    "s_cmpk_lg_u32 s37, 2\n"
    "s_cbranch_scc1 EPI\n"
    "s_cmp_lg_u32 s60, 8\n"
    "s_cbranch_scc1 EPI\n"
    "global_load_lds_dword v111, s[28:29]\n"
    "global_load_lds_dword v111, s[30:31]\n"
    "s_branch EPI\n"
    "STGW:\n"
    "s_sub_u32 s39, s39, 1\n"
    "s_cbranch_scc1 ABORT\n"
    "ds_read_b32 v77, v78 offset:40\n"
    "ds_read_b128 v[80:83], v76\n"
    "ds_read_b64 v[84:85], v76 offset:16\n"
    "s_branch STG3\n"
    "CONST:\n"
    "v_mov_b32 v4, 0\n"
    "v_mov_b32 v6, 0\n"
    "s_setprio 3\n"
    "v_and_b32 v2, 15, v0\n"
    "v_bfe_u32 v3, v0, 4, 2\n"
    "v_mul_u32_u24 v3, 4608, v3\n"
    "s_movk_i32 s43, 13824\n"
    "v_mad_u32_u24 v3, v2, s43, v3\n"
    "s_mul_i32 s42, s38, 24\n"
    "s_add_u32 s42, s42, 10519552\n"
    "v_lshlrev_b32 v2, 1, v2\n"
    "v_add_u32 v2, 148352, v2\n"
    "s_mov_b32 s43, 0x42000000\n"
    "s_mov_b32 s52, 0x01ff01ff\n"
    "s_movk_i32 s53, 0x01ff\n"
    "s_mov_b32 s62, 0\n"
    "s_movk_i32 s63, 0x0111\n"
    "v_mov_b32 v8, 0\n"
    "s_mov_b64 exec, s[52:53]\n"
    "U_13:\n"
    "s_memrealtime s[98:99]\n"
    "s_waitcnt lgkmcnt(0)\n"
    "s_sub_u32 s98, s98, s96\n"
    "s_cmpk_lt_u32 s98, 200\n"
    "s_cbranch_scc1 U_13\n"
    "buffer_load_dwordx4 v[4:7], v3, s[20:23], s42 offen sc1\n"
    "buffer_load_dwordx2 v[8:9], v3, s[20:23], s42 offen offset:16 sc1\n"
    "s_mov_b64 exec, -1\n"
    "XISS:\n"
    "U_14:\n"
    "s_memrealtime s[98:99]\n"
    "s_waitcnt lgkmcnt(0)\n"
    "s_sub_u32 s98, s98, s96\n"
    "s_cmpk_lt_u32 s98, 210\n"
    "s_cbranch_scc1 U_14\n"
    "buffer_load_dwordx4 v[68:71], v66, s[20:23], s40 offen sc1\n"
    "buffer_load_dwordx4 v[72:75], v66, s[20:23], s41 offen sc1\n"
    "s_cmpk_lt_u32 s37, 4\n"
    "s_cbranch_scc1 STAGE\n"
    "s_cmpk_lg_u32 s37, 13\n"
    "s_cbranch_scc1 STG1\n"
    "s_mov_b64 exec, s[52:53]\n"
    "s_waitcnt vmcnt(2)\n"
    "s_branch E_16\n"
    "R_15:\n"
    "s_sub_u32 s39, s39, 1\n"
    "s_cbranch_scc1 ABORT\n"
    "s_and_b64 exec, exec, vcc\n"
    "buffer_load_dwordx4 v[4:7], v3, s[20:23], s42 offen sc1\n"
    "buffer_load_dwordx2 v[8:9], v3, s[20:23], s42 offen offset:16 sc1\n"
    "s_waitcnt vmcnt(0)\n"
    "E_16:\n"
    "v_bitop3_b32 v10, v5, v7, s36 bitop3:0x7e\n"
    "v_bitop3_b32 v10, v10, v9, s36 bitop3:0xf6\n"
    "v_cmp_ne_u32 vcc, 0, v10\n"
    "s_cbranch_vccnz R_15\n"
    "s_mov_b64 exec, -1\n"
    "v_add_f32_dpp v4, v4, v4 row_ror:8 row_mask:0xf bank_mask:0xf\n"
    "v_add_f32_dpp v6, v6, v6 row_ror:8 row_mask:0xf bank_mask:0xf\n"
    "v_add_f32_dpp v8, v8, v8 row_ror:8 row_mask:0xf bank_mask:0xf\n"
    "v_add_f32_dpp v4, v4, v4 row_ror:4 row_mask:0xf bank_mask:0xf\n"
    "v_add_f32_dpp v6, v6, v6 row_ror:4 row_mask:0xf bank_mask:0xf\n"
    "v_add_f32_dpp v8, v8, v8 row_ror:4 row_mask:0xf bank_mask:0xf\n"
    "s_nop 0\n"
    "v_mov_b32_dpp v4, v6 quad_perm:[0,1,2,3] row_mask:0xf bank_mask:0x2\n"
    "s_nop 1\n"
    "v_mov_b32_dpp v4, v8 quad_perm:[0,1,2,3] row_mask:0xf bank_mask:0x4\n"
    "s_nop 1\n"
    "v_add_f32_dpp v4, v4, v4 quad_perm:[1,0,3,2] row_mask:0xf bank_mask:0xf\n"
    "s_nop 1\n"
    "v_add_f32_dpp v4, v4, v4 quad_perm:[2,3,0,1] row_mask:0xf bank_mask:0xf\n"
    "v_cvt_pk_bf16_f32 v5, 0, v4\n"
    "v_add_f32 v4, 1.0, v5\n"
    "s_mov_b64 exec, s[62:63]\n"
    "ds_write_b32 v2, v5 offset:36\n"
    "s_mov_b64 exec, -1\n"
    "v_cvt_pk_bf16_f32 v4, 0, v4\n"
    "v_mov_b32 v9, v4\n"
    "s_nop 1\n"
    "v_permlane16_swap_b32 v9, v4\n"
    "v_fma_f32 v6, abs(v4), s43, abs(v5)\n"
    "v_frexp_exp_i32_f32 v6, v6\n"
    "v_sub_u32 v7, 11, v6\n"
    "v_ldexp_f32 v10, v4, v7\n"
    "v_ldexp_f32 v11, v5, v7\n"
    "v_subrev_u32 v8, 11, v6\n"
    "v_ldexp_f32 v8, 1.0, v8\n"
    "s_mov_b64 exec, 0x0111\n"
    "ds_write_b64 v2, v[10:11]\n"
    "ds_write_b32 v2, v8 offset:32\n"
    "ds_write_b32 v14, v15 offset:40\n"
    "s_mov_b64 exec, -1\n"
    "s_branch STG1\n"
    "FAR:\n"
    "s_lshr_b32 s42, s37, 2\n"
    "v_and_b32 v6, 15, v0\n"
    "v_bfe_u32 v7, v0, 4, 2\n"
    "v_and_b32 v1, 63, v0\n"
    "v_lshlrev_b32 v1, 4, v1\n"
    "v_add_u32 v1, 101376, v1\n"
    "s_mul_i32 s43, s42, 4576\n"
    "s_add_u32 s43, s43, 117216\n"
    "v_lshlrev_b32 v2, 1, v6\n"
    "v_sub_u32 v2, v7, v2\n"
    "v_lshl_add_u32 v2, v2, 4, s43\n"
    "s_mul_i32 s44, s42, 3328\n"
    "s_add_u32 s43, s44, 92596\n"
    "v_lshlrev_b32 v3, 2, v7\n"
    "v_sub_u32 v3, v6, v3\n"
    "v_lshl_add_u32 v3, v3, 2, s43\n"
    "s_mul_i32 s43, s42, 7680\n"
    "s_add_u32 s43, s43, 28160\n"
    "v_mul_u32_u24 v4, 160, v7\n"
    "v_lshl_add_u32 v4, v6, 3, v4\n"
    "v_add_u32 v4, s43, v4\n"
    "s_add_u32 s43, s42, 147968\n"
    "v_mov_b32 v5, s43\n"
    "v_mov_b32 v8, 1\n"
    "v_mov_b32 v11, 147968\n"
    "s_cmpk_ge_u32 s37, 6\n"
    "s_cbranch_scc1 S2\n"
    "s_branch STG0\n"
    "FAR1:\n"
    "s_branch E_18\n"
    "R_17:\n"
    "s_sleep 1\n"
    "s_sub_u32 s39, s39, 1\n"
    "s_cbranch_scc1 ABORT\n"
    "E_18:\n"
    "ds_read_b32 v12, v11 offset:224\n"
    "s_waitcnt lgkmcnt(0)\n"
    "v_cmp_ne_u32 vcc, 6, v12\n"
    "s_cbranch_vccnz R_17\n"
    "FAR3:\n"
    "ds_read_b128 v[12:15], v1 offset:0\n"
    "ds_read_b128 v[16:19], v1 offset:1024\n"
    "ds_read_b128 v[20:23], v1 offset:2048\n"
    "ds_read2_b32 v[24:25], v3 offset0:0 offset1:1\n"
    "ds_read2_b32 v[26:27], v3 offset0:2 offset1:3\n"
    "ds_read2_b32 v[36:37], v3 offset0:64 offset1:65\n"
    "ds_read2_b32 v[38:39], v3 offset0:66 offset1:67\n"
    "ds_read2_b32 v[28:29], v3 offset0:16 offset1:17\n"
    "ds_read2_b32 v[30:31], v3 offset0:18 offset1:19\n"
    "ds_read2_b32 v[40:41], v3 offset0:80 offset1:81\n"
    "ds_read2_b32 v[42:43], v3 offset0:82 offset1:83\n"
    "ds_read2_b32 v[32:33], v3 offset0:32 offset1:33\n"
    "ds_read2_b32 v[34:35], v3 offset0:34 offset1:35\n"
    "ds_read2_b32 v[44:45], v3 offset0:96 offset1:97\n"
    "ds_read2_b32 v[46:47], v3 offset0:98 offset1:99\n"
    "s_cmpk_eq_u32 s37, 3\n"
    "s_cbranch_scc1 FAR2\n"
    "s_barrier\n"
    "FAR2:\n"
    "s_setprio 3\n"
    "ds_read_b128 v[48:51], v2 offset:3584\n"
    "ds_read_b128 v[52:55], v2 offset:3600\n"
    "ds_read_b128 v[56:59], v2 offset:3072\n"
    "ds_read_b128 v[60:63], v2 offset:3088\n"
    "ds_read_b128 v[64:67], v2 offset:2560\n"
    "ds_read_b128 v[68:71], v2 offset:2576\n"
    "s_waitcnt lgkmcnt(4)\n"
    "v_mfma_f32_16x16x32_f16 v[72:75], v[48:51], v[12:15], 0\n"
    "v_mfma_f32_16x16x32_f16 v[76:79], v[48:51], v[16:19], 0\n"
    "v_mfma_f32_16x16x32_f16 v[80:83], v[48:51], v[20:23], 0\n"
    "v_mfma_f32_16x16x32_f16 v[84:87], v[52:55], v[20:23], 0\n"
    "s_nop 5\n"
    "v_cvt_pk_f16_f32 v88, v75, v79\n"
    "v_cvt_pk_f16_f32 v89, v74, v78\n"
    "v_cvt_pk_f16_f32 v90, v73, v77\n"
    "v_cvt_pk_f16_f32 v91, v72, v76\n"
    "v_cvt_pk_f16_f32 v92, v83, v87\n"
    "v_cvt_pk_f16_f32 v93, v82, v86\n"
    "v_cvt_pk_f16_f32 v94, v81, v85\n"
    "v_cvt_pk_f16_f32 v95, v80, v84\n"
    "s_waitcnt lgkmcnt(2)\n"
    "v_mfma_f32_16x16x32_f16 v[72:75], v[56:59], v[12:15], 0\n"
    "ds_read_b128 v[48:51], v2 offset:2048\n"
    "v_mfma_f32_16x16x32_f16 v[76:79], v[56:59], v[16:19], 0\n"
    "ds_read_b128 v[52:55], v2 offset:2064\n"
    "v_mfma_f32_16x16x32_f16 v[80:83], v[56:59], v[20:23], 0\n"
    "v_mfma_f32_16x16x32_f16 v[84:87], v[60:63], v[20:23], 0\n"
    "v_mfma_f32_16x16x32_f16 v[104:107], v[88:91], v[24:27], 0\n"
    "v_mfma_f32_16x16x32_f16 v[104:107], v[92:95], v[36:39], v[104:107]\n"
    "v_mfma_f32_16x16x32_f16 v[108:111], v[88:91], v[28:31], 0\n"
    "s_nop 1\n"
    "v_cvt_pk_f16_f32 v96, v75, v79\n"
    "v_cvt_pk_f16_f32 v97, v74, v78\n"
    "v_mfma_f32_16x16x32_f16 v[108:111], v[92:95], v[40:43], v[108:111]\n"
    "v_cvt_pk_f16_f32 v98, v73, v77\n"
    "v_cvt_pk_f16_f32 v99, v72, v76\n"
    "v_mfma_f32_16x16x32_f16 v[112:115], v[88:91], v[32:35], 0\n"
    "v_cvt_pk_f16_f32 v100, v83, v87\n"
    "v_cvt_pk_f16_f32 v101, v82, v86\n"
    "v_mfma_f32_16x16x32_f16 v[112:115], v[92:95], v[44:47], v[112:115]\n"
    "v_cvt_pk_f16_f32 v102, v81, v85\n"
    "v_cvt_pk_f16_f32 v103, v80, v84\n"
    "s_waitcnt lgkmcnt(2)\n"
    "v_mfma_f32_16x16x32_f16 v[72:75], v[64:67], v[12:15], 0\n"
    "ds_read_b128 v[56:59], v2 offset:1536\n"
    "v_mfma_f32_16x16x32_f16 v[76:79], v[64:67], v[16:19], 0\n"
    "ds_read_b128 v[60:63], v2 offset:1552\n"
    "v_mfma_f32_16x16x32_f16 v[80:83], v[64:67], v[20:23], 0\n"
    "ds_write_b64 v4, v[104:105] offset:2560\n"
    "v_mfma_f32_16x16x32_f16 v[84:87], v[68:71], v[20:23], 0\n"
    "ds_write_b64 v4, v[106:107] offset:3200\n"
    "v_mfma_f32_16x16x32_f16 v[108:111], v[96:99], v[24:27], v[108:111]\n"
    "ds_write_b8 v5, v8 offset:83\n"
    "v_mfma_f32_16x16x32_f16 v[108:111], v[100:103], v[36:39], v[108:111]\n"
    "v_mfma_f32_16x16x32_f16 v[112:115], v[96:99], v[28:31], v[112:115]\n"
    "v_cvt_pk_f16_f32 v88, v75, v79\n"
    "v_cvt_pk_f16_f32 v89, v74, v78\n"
    "v_mfma_f32_16x16x32_f16 v[112:115], v[100:103], v[40:43], v[112:115]\n"
    "v_cvt_pk_f16_f32 v90, v73, v77\n"
    "v_cvt_pk_f16_f32 v91, v72, v76\n"
    "v_mfma_f32_16x16x32_f16 v[116:119], v[96:99], v[32:35], 0\n"
    "v_cvt_pk_f16_f32 v92, v83, v87\n"
    "v_cvt_pk_f16_f32 v93, v82, v86\n"
    "v_mfma_f32_16x16x32_f16 v[116:119], v[100:103], v[44:47], v[116:119]\n"
    "v_cvt_pk_f16_f32 v94, v81, v85\n"
    "v_cvt_pk_f16_f32 v95, v80, v84\n"
    "s_setprio 2\n"
    "s_waitcnt lgkmcnt(5)\n"
    "v_mfma_f32_16x16x32_f16 v[72:75], v[48:51], v[12:15], 0\n"
    "v_mfma_f32_16x16x32_f16 v[76:79], v[48:51], v[16:19], 0\n"
    "v_mfma_f32_16x16x32_f16 v[80:83], v[48:51], v[20:23], 0\n"
    "ds_write_b64 v4, v[108:109] offset:3840\n"
    "v_mfma_f32_16x16x32_f16 v[84:87], v[52:55], v[20:23], 0\n"
    "ds_write_b64 v4, v[110:111] offset:4480\n"
    "v_mfma_f32_16x16x32_f16 v[112:115], v[88:91], v[24:27], v[112:115]\n"
    "ds_write_b8 v5, v8 offset:91\n"
    "v_mfma_f32_16x16x32_f16 v[112:115], v[92:95], v[36:39], v[112:115]\n"
    "v_mfma_f32_16x16x32_f16 v[116:119], v[88:91], v[28:31], v[116:119]\n"
    "v_cvt_pk_f16_f32 v96, v75, v79\n"
    "v_cvt_pk_f16_f32 v97, v74, v78\n"
    "v_mfma_f32_16x16x32_f16 v[116:119], v[92:95], v[40:43], v[116:119]\n"
    "v_cvt_pk_f16_f32 v98, v73, v77\n"
    "v_cvt_pk_f16_f32 v99, v72, v76\n"
    "v_mfma_f32_16x16x32_f16 v[120:123], v[88:91], v[32:35], 0\n"
    "v_cvt_pk_f16_f32 v100, v83, v87\n"
    "v_cvt_pk_f16_f32 v101, v82, v86\n"
    "v_mfma_f32_16x16x32_f16 v[120:123], v[92:95], v[44:47], v[120:123]\n"
    "v_cvt_pk_f16_f32 v102, v81, v85\n"
    "v_cvt_pk_f16_f32 v103, v80, v84\n"
    "s_setprio 1\n"
    "s_waitcnt lgkmcnt(6)\n"
    "v_mfma_f32_16x16x32_f16 v[72:75], v[56:59], v[12:15], 0\n"
    "v_mfma_f32_16x16x32_f16 v[76:79], v[56:59], v[16:19], 0\n"
    "v_mfma_f32_16x16x32_f16 v[80:83], v[56:59], v[20:23], 0\n"
    "ds_write_b64 v4, v[112:113] offset:5120\n"
    "v_mfma_f32_16x16x32_f16 v[84:87], v[60:63], v[20:23], 0\n"
    "ds_write_b64 v4, v[114:115] offset:5760\n"
    "v_mfma_f32_16x16x32_f16 v[116:119], v[96:99], v[24:27], v[116:119]\n"
    "ds_write_b8 v5, v8 offset:99\n"
    "v_mfma_f32_16x16x32_f16 v[116:119], v[100:103], v[36:39], v[116:119]\n"
    "v_mfma_f32_16x16x32_f16 v[120:123], v[96:99], v[28:31], v[120:123]\n"
    "v_cvt_pk_f16_f32 v88, v75, v79\n"
    "v_cvt_pk_f16_f32 v89, v74, v78\n"
    "v_mfma_f32_16x16x32_f16 v[120:123], v[100:103], v[40:43], v[120:123]\n"
    "v_cvt_pk_f16_f32 v90, v73, v77\n"
    "v_cvt_pk_f16_f32 v91, v72, v76\n"
    "v_mfma_f32_16x16x32_f16 v[124:127], v[96:99], v[32:35], 0\n"
    "v_cvt_pk_f16_f32 v92, v83, v87\n"
    "v_cvt_pk_f16_f32 v93, v82, v86\n"
    "v_mfma_f32_16x16x32_f16 v[124:127], v[100:103], v[44:47], v[124:127]\n"
    "v_cvt_pk_f16_f32 v94, v81, v85\n"
    "v_cvt_pk_f16_f32 v95, v80, v84\n"
    "s_setprio 0\n"
    "v_mfma_f32_16x16x32_f16 v[120:123], v[88:91], v[24:27], v[120:123]\n"
    "v_mfma_f32_16x16x32_f16 v[120:123], v[92:95], v[36:39], v[120:123]\n"
    "ds_write_b64 v4, v[116:117] offset:6400\n"
    "v_mfma_f32_16x16x32_f16 v[124:127], v[88:91], v[28:31], v[124:127]\n"
    "ds_write_b64 v4, v[118:119] offset:7040\n"
    "v_mfma_f32_16x16x32_f16 v[124:127], v[92:95], v[40:43], v[124:127]\n"
    "ds_write_b8 v5, v8 offset:107\n"
    "s_nop 2\n"
    "ds_write_b64 v4, v[120:121] offset:7680\n"
    "ds_write_b64 v4, v[122:123] offset:8320\n"
    "ds_write_b8 v5, v8 offset:115\n"
    "s_nop 0\n"
    "ds_write_b64 v4, v[124:125] offset:8960\n"
    "ds_write_b64 v4, v[126:127] offset:9600\n"
    "ds_write_b8 v5, v8 offset:123\n"
    "s_setprio 0\n"
    "s_branch LN2\n"
    "HELP:\n"
    "s_mov_b64 exec, -1\n"
    "s_sub_u32 s42, s37, 4\n"
    "v_and_b32 v6, 15, v0\n"
    "v_bfe_u32 v7, v0, 4, 2\n"
    "v_and_b32 v1, 63, v0\n"
    "v_lshlrev_b32 v1, 4, v1\n"
    "v_add_u32 v1, 101376, v1\n"
    "s_mul_i32 s43, s42, 4576\n"
    "s_add_u32 s43, s43, 117216\n"
    "v_lshlrev_b32 v2, 1, v6\n"
    "v_sub_u32 v2, v7, v2\n"
    "v_lshl_add_u32 v2, v2, 4, s43\n"
    "s_mul_i32 s44, s42, 3328\n"
    "s_add_u32 s43, s44, 92596\n"
    "v_lshlrev_b32 v3, 2, v7\n"
    "v_sub_u32 v3, v6, v3\n"
    "v_lshl_add_u32 v3, v3, 2, s43\n"
    "s_mul_i32 s43, s42, 10240\n"
    "s_add_u32 s43, s43, 0\n"
    "v_mul_u32_u24 v4, 160, v7\n"
    "v_lshl_add_u32 v4, v6, 3, v4\n"
    "v_add_u32 v4, s43, v4\n"
    "s_add_u32 s43, s42, 147968\n"
    "v_mov_b32 v5, s43\n"
    "v_mov_b32 v8, 1\n"
    "v_mov_b32 v11, 147968\n"
    "s_add_u32 s44, s44, 92990\n"
    "v_mov_b32 v9, s44\n"
    "s_mul_i32 s43, s42, 3\n"
    "v_add_u32 v5, s43, v5\n"
    "s_lshr_b32 s44, s28, 7\n"
    "s_add_u32 s44, s44, s2\n"
    "s_sub_u32 s44, 3, s44\n"
    "s_and_b32 s44, s44, 7\n"
    "s_add_u32 s44, s44, 1\n"
    "s_add_u32 s44, s44, 0\n"
    "s_mul_i32 s45, s44, 57\n"
    "s_lshr_b32 s45, s45, 9\n"
    "s_mul_i32 s45, s45, 9\n"
    "s_sub_u32 s44, s44, s45\n"
    "s_and_b32 s43, s2, 7\n"
    "s_mul_i32 s43, s43, 9\n"
    "s_add_u32 s43, s43, s44\n"
    "s_mul_i32 s44, s44, 9\n"
    "s_lshr_b32 s43, s2, 3\n"
    "s_mul_i32 s43, s43, 3\n"
    "s_add_u32 s43, s43, s37\n"
    "s_add_u32 s44, s44, s43\n"
    "s_sub_u32 s44, s44, 52\n"
    "s_lshl_b32 s44, s44, 6\n"
    "s_and_b32 s60, s2, 7\n"
    "s_mul_i32 s60, s60, 663552\n"
    "v_and_b32 v6, 63, v0\n"
    "v_add_u32 v6, s44, v6\n"
    "s_mov_b32 m0, 148480\n"
    "v_subrev_u32 v7, 5184, v6\n"
    "v_min_u32 v6, v6, v7\n"
    "v_lshl_add_u32 v7, v6, 7, s60\n"
    "v_add_u32 v6, 1536, v6\n"
    "s_cmpk_ge_u32 s37, 6\n"
    "s_cbranch_scc1 S2\n"
    "HELP1:\n"
    "s_mov_b32 s43, 0x01000100\n"
    "s_branch E_20\n"
    "R_19:\n"
    "s_sleep 1\n"
    "s_sub_u32 s39, s39, 1\n"
    "s_cbranch_scc1 ABORT\n"
    "E_20:\n"
    "ds_read_b32 v12, v11 offset:224\n"
    "s_waitcnt lgkmcnt(0)\n"
    "v_cmp_ne_u32 vcc, 6, v12\n"
    "s_cbranch_vccnz R_19\n"
    "ds_read_b128 v[12:15], v1 offset:3072\n"
    "ds_read_b128 v[16:19], v1 offset:4096\n"
    "ds_read_b128 v[20:23], v1 offset:5120\n"
    "ds_read_u16 v10, v9\n"
    "ds_read2_b32 v[24:25], v3 offset0:128 offset1:129\n"
    "ds_read2_b32 v[26:27], v3 offset0:130 offset1:131\n"
    "ds_read2_b32 v[28:29], v3 offset0:144 offset1:145\n"
    "ds_read2_b32 v[30:31], v3 offset0:146 offset1:147\n"
    "ds_read2_b32 v[32:33], v3 offset0:160 offset1:161\n"
    "ds_read2_b32 v[34:35], v3 offset0:162 offset1:163\n"
    "ds_read2_b32 v[36:37], v3 offset0:176 offset1:177\n"
    "ds_read2_b32 v[38:39], v3 offset0:178 offset1:179\n"
    "s_waitcnt lgkmcnt(8)\n"
    "v_perm_b32 v10, v10, v10, s43\n"
    "v_and_b32 v20, v20, v10\n"
    "v_and_b32 v21, v21, v10\n"
    "v_and_b32 v22, v22, v10\n"
    "v_and_b32 v23, v23, v10\n"
    "s_setprio 3\n"
    "s_cmpk_lt_u32 s2, 128\n"
    "s_barrier\n"
    "s_cbranch_scc1 HELP2\n"
    "global_load_lds_dword v7, s[32:33]\n"
    "HELP2:\n"
    "ds_read_b128 v[40:43], v2 offset:3584\n"
    "ds_read_b128 v[44:47], v2 offset:3600\n"
    "ds_read_b128 v[48:51], v2 offset:3072\n"
    "ds_read_b128 v[52:55], v2 offset:3088\n"
    "ds_read_b128 v[56:59], v2 offset:2560\n"
    "ds_read_b128 v[60:63], v2 offset:2576\n"
    "ds_read_b128 v[64:67], v2 offset:2048\n"
    "ds_read_b128 v[68:71], v2 offset:2064\n"
    "ds_read_b128 v[104:107], v2 offset:1040\n"
    "s_waitcnt lgkmcnt(7)\n"
    "v_mfma_f32_16x16x32_f16 v[72:75], v[40:43], v[12:15], 0\n"
    "v_mfma_f32_16x16x32_f16 v[76:79], v[44:47], v[16:19], 0\n"
    "ds_read_b128 v[44:47], v1 offset:0\n"
    "s_waitcnt lgkmcnt(6)\n"
    "v_mfma_f32_16x16x32_f16 v[80:83], v[48:51], v[12:15], 0\n"
    "v_mfma_f32_16x16x32_f16 v[84:87], v[52:55], v[16:19], 0\n"
    "ds_read_b128 v[52:55], v1 offset:1024\n"
    "s_waitcnt lgkmcnt(5)\n"
    "v_mfma_f32_16x16x32_f16 v[88:91], v[56:59], v[12:15], 0\n"
    "v_mfma_f32_16x16x32_f16 v[92:95], v[60:63], v[16:19], 0\n"
    "ds_read_b128 v[60:63], v1 offset:2048\n"
    "s_waitcnt lgkmcnt(4)\n"
    "v_mfma_f32_16x16x32_f16 v[96:99], v[64:67], v[12:15], 0\n"
    "v_mfma_f32_16x16x32_f16 v[100:103], v[68:71], v[16:19], 0\n"
    "ds_read_b128 v[68:71], v2 offset:1024\n"
    "v_mfma_f32_16x16x32_f16 v[108:111], v[20:23], v[40:43], 0\n"
    "v_cvt_pk_f16_f32 v12, v75, v79\n"
    "v_cvt_pk_f16_f32 v13, v74, v78\n"
    "v_mfma_f32_16x16x32_f16 v[112:115], v[20:23], v[48:51], 0\n"
    "v_cvt_pk_f16_f32 v14, v73, v77\n"
    "v_cvt_pk_f16_f32 v15, v72, v76\n"
    "v_mfma_f32_16x16x32_f16 v[116:119], v[20:23], v[56:59], 0\n"
    "v_cvt_pk_f16_f32 v16, v83, v87\n"
    "v_cvt_pk_f16_f32 v17, v82, v86\n"
    "v_mfma_f32_16x16x32_f16 v[120:123], v[20:23], v[64:67], 0\n"
    "v_cvt_pk_f16_f32 v18, v81, v85\n"
    "v_cvt_pk_f16_f32 v19, v80, v84\n"
    "ds_read2_b32 v[40:41], v3 offset0:0 offset1:1\n"
    "ds_read2_b32 v[42:43], v3 offset0:2 offset1:3\n"
    "ds_read2_b32 v[48:49], v3 offset0:64 offset1:65\n"
    "ds_read2_b32 v[50:51], v3 offset0:66 offset1:67\n"
    "v_mfma_f32_16x16x32_f16 v[108:111], v[12:15], v[24:27], v[108:111]\n"
    "v_cvt_pk_f16_f32 v20, v91, v95\n"
    "v_cvt_pk_f16_f32 v21, v90, v94\n"
    "v_mfma_f32_16x16x32_f16 v[112:115], v[12:15], v[28:31], v[112:115]\n"
    "v_cvt_pk_f16_f32 v22, v89, v93\n"
    "v_cvt_pk_f16_f32 v23, v88, v92\n"
    "v_mfma_f32_16x16x32_f16 v[116:119], v[12:15], v[32:35], v[116:119]\n"
    "v_cvt_pk_f16_f32 v124, v99, v103\n"
    "v_cvt_pk_f16_f32 v125, v98, v102\n"
    "v_mfma_f32_16x16x32_f16 v[120:123], v[12:15], v[36:39], v[120:123]\n"
    "v_cvt_pk_f16_f32 v126, v97, v101\n"
    "v_cvt_pk_f16_f32 v127, v96, v100\n"
    "s_waitcnt lgkmcnt(4)\n"
    "v_mfma_f32_16x16x32_f16 v[72:75], v[68:71], v[44:47], 0\n"
    "v_mfma_f32_16x16x32_f16 v[76:79], v[68:71], v[52:55], 0\n"
    "v_mfma_f32_16x16x32_f16 v[80:83], v[68:71], v[60:63], 0\n"
    "ds_write_b64 v4, v[108:109] offset:5120\n"
    "v_mfma_f32_16x16x32_f16 v[84:87], v[104:107], v[60:63], 0\n"
    "ds_write_b64 v4, v[110:111] offset:5760\n"
    "v_mfma_f32_16x16x32_f16 v[112:115], v[16:19], v[24:27], v[112:115]\n"
    "v_mfma_f32_16x16x32_f16 v[116:119], v[16:19], v[28:31], v[116:119]\n"
    "v_mfma_f32_16x16x32_f16 v[120:123], v[16:19], v[32:35], v[120:123]\n"
    "s_nop 0\n"
    "v_cvt_pk_f16_f32 v12, v75, v79\n"
    "v_cvt_pk_f16_f32 v13, v74, v78\n"
    "v_mfma_f32_16x16x32_f16 v[116:119], v[20:23], v[24:27], v[116:119]\n"
    "v_cvt_pk_f16_f32 v14, v73, v77\n"
    "v_cvt_pk_f16_f32 v15, v72, v76\n"
    "v_mfma_f32_16x16x32_f16 v[120:123], v[20:23], v[28:31], v[120:123]\n"
    "v_cvt_pk_f16_f32 v16, v83, v87\n"
    "v_cvt_pk_f16_f32 v17, v82, v86\n"
    "v_mfma_f32_16x16x32_f16 v[120:123], v[124:127], v[24:27], v[120:123]\n"
    "v_cvt_pk_f16_f32 v18, v81, v85\n"
    "v_cvt_pk_f16_f32 v19, v80, v84\n"
    "s_waitcnt lgkmcnt(2)\n"
    "v_mfma_f32_16x16x32_f16 v[120:123], v[12:15], v[40:43], v[120:123]\n"
    "ds_write_b64 v4, v[112:113] offset:6400\n"
    "ds_write_b64 v4, v[114:115] offset:7040\n"
    "v_mfma_f32_16x16x32_f16 v[120:123], v[16:19], v[48:51], v[120:123]\n"
    "ds_write_b64 v4, v[116:117] offset:7680\n"
    "ds_write_b64 v4, v[118:119] offset:8320\n"
    "s_nop 5\n"
    "ds_write_b64 v4, v[120:121] offset:8960\n"
    "ds_write_b64 v4, v[122:123] offset:9600\n"
    "ds_write_b8 v5, v8 offset:44\n"
    "s_branch MISC\n"
    "NEAR:\n"
    "v_and_b32 v6, 15, v0\n"
    "v_bfe_u32 v7, v0, 4, 2\n"
    "v_and_b32 v1, 63, v0\n"
    "v_lshlrev_b32 v1, 4, v1\n"
    "v_add_u32 v1, 101376, v1\n"
    "s_mul_i32 s43, s42, 4576\n"
    "s_add_u32 s43, s43, 117216\n"
    "v_lshlrev_b32 v2, 1, v6\n"
    "v_sub_u32 v2, v7, v2\n"
    "v_lshl_add_u32 v2, v2, 4, s43\n"
    "s_mul_i32 s44, s42, 3328\n"
    "s_add_u32 s43, s44, 92596\n"
    "v_lshlrev_b32 v3, 2, v7\n"
    "v_sub_u32 v3, v6, v3\n"
    "v_lshl_add_u32 v3, v3, 2, s43\n"
    "s_mul_i32 s43, s42, 10240\n"
    "s_add_u32 s43, s43, 0\n"
    "v_mul_u32_u24 v4, 160, v7\n"
    "v_lshl_add_u32 v4, v6, 3, v4\n"
    "v_add_u32 v4, s43, v4\n"
    "s_add_u32 s43, s42, 147968\n"
    "v_mov_b32 v5, s43\n"
    "v_mov_b32 v8, 1\n"
    "v_mov_b32 v11, 147968\n"
    "v_lshl_add_u32 v3, v7, 3, v6\n"
    "v_add_u32 v3, 1, v3\n"
    "v_and_b32 v9, 1, v3\n"
    "v_mul_u32_u24 v9, 2110, v9\n"
    "s_add_u32 s44, s44, 91392\n"
    "v_lshl_add_u32 v3, v3, 1, s44\n"
    "v_add_u32 v3, v3, v9\n"
    "v_add_u32 v1, 512, v3\n"
    "s_mul_i32 s43, s42, 3\n"
    "v_add_u32 v10, s43, v5\n"
    "s_branch S2\n"
    "NEAR1:\n"
    "s_branch E_22\n"
    "R_21:\n"
    "s_sub_u32 s39, s39, 1\n"
    "s_cbranch_scc1 ABORT\n"
    "E_22:\n"
    "ds_read_b32 v12, v11 offset:224\n"
    "s_waitcnt lgkmcnt(0)\n"
    "v_cmp_ne_u32 vcc, 6, v12\n"
    "s_cbranch_vccnz R_21\n"
    "ds_read2_b32 v[88:89], v3 offset0:112 offset1:113\n"
    "ds_read2_b32 v[90:91], v3 offset0:114 offset1:115\n"
    "ds_read2_b32 v[120:121], v1 offset0:112 offset1:113\n"
    "ds_read2_b32 v[122:123], v1 offset0:114 offset1:115\n"
    "ds_read2_b32 v[84:85], v3 offset0:96 offset1:97\n"
    "ds_read2_b32 v[86:87], v3 offset0:98 offset1:99\n"
    "ds_read2_b32 v[116:117], v1 offset0:96 offset1:97\n"
    "ds_read2_b32 v[118:119], v1 offset0:98 offset1:99\n"
    "ds_read2_b32 v[80:81], v3 offset0:80 offset1:81\n"
    "ds_read2_b32 v[82:83], v3 offset0:82 offset1:83\n"
    "ds_read2_b32 v[112:113], v1 offset0:80 offset1:81\n"
    "ds_read2_b32 v[114:115], v1 offset0:82 offset1:83\n"
    "ds_read2_b32 v[76:77], v3 offset0:64 offset1:65\n"
    "ds_read2_b32 v[78:79], v3 offset0:66 offset1:67\n"
    "ds_read2_b32 v[108:109], v1 offset0:64 offset1:65\n"
    "ds_read2_b32 v[110:111], v1 offset0:66 offset1:67\n"
    "s_setprio 3\n"
    "s_barrier\n"
    "ds_read_b128 v[24:27], v2 offset:4032\n"
    "ds_read_b128 v[28:31], v2 offset:3968\n"
    "ds_read_b128 v[32:35], v2 offset:3904\n"
    "s_waitcnt lgkmcnt(2)\n"
    "v_mfma_f32_16x16x32_f16 v[48:51], v[88:91], v[24:27], 0\n"
    "ds_read2_b32 v[72:73], v3 offset0:48 offset1:49\n"
    "ds_read2_b32 v[74:75], v3 offset0:50 offset1:51\n"
    "ds_read2_b32 v[104:105], v1 offset0:48 offset1:49\n"
    "ds_read2_b32 v[106:107], v1 offset0:50 offset1:51\n"
    "ds_read_b128 v[36:39], v2 offset:3840\n"
    "v_mfma_f32_16x16x32_f16 v[52:55], v[120:123], v[24:27], 0\n"
    "s_waitcnt lgkmcnt(6)\n"
    "v_mfma_f32_16x16x32_f16 v[48:51], v[84:87], v[28:31], v[48:51]\n"
    "ds_read2_b32 v[68:69], v3 offset0:32 offset1:33\n"
    "ds_read2_b32 v[70:71], v3 offset0:34 offset1:35\n"
    "ds_read2_b32 v[100:101], v1 offset0:32 offset1:33\n"
    "ds_read2_b32 v[102:103], v1 offset0:34 offset1:35\n"
    "ds_read_b128 v[40:43], v2 offset:3776\n"
    "v_mfma_f32_16x16x32_f16 v[52:55], v[116:119], v[28:31], v[52:55]\n"
    "s_waitcnt lgkmcnt(10)\n"
    "v_mfma_f32_16x16x32_f16 v[48:51], v[80:83], v[32:35], v[48:51]\n"
    "ds_read2_b32 v[64:65], v3 offset0:16 offset1:17\n"
    "ds_read2_b32 v[66:67], v3 offset0:18 offset1:19\n"
    "ds_read2_b32 v[96:97], v1 offset0:16 offset1:17\n"
    "ds_read2_b32 v[98:99], v1 offset0:18 offset1:19\n"
    "ds_read_b128 v[44:47], v2 offset:3712\n"
    "v_mfma_f32_16x16x32_f16 v[52:55], v[112:115], v[32:35], v[52:55]\n"
    "s_waitcnt lgkmcnt(10)\n"
    "v_mfma_f32_16x16x32_f16 v[48:51], v[76:79], v[36:39], v[48:51]\n"
    "ds_read2_b32 v[60:61], v3 offset0:0 offset1:1\n"
    "ds_read2_b32 v[62:63], v3 offset0:2 offset1:3\n"
    "ds_read2_b32 v[92:93], v1 offset0:0 offset1:1\n"
    "ds_read2_b32 v[94:95], v1 offset0:2 offset1:3\n"
    "ds_read2_b32 v[124:125], v1 offset0:128 offset1:129\n"
    "ds_read2_b32 v[126:127], v1 offset0:130 offset1:131\n"
    "ds_read_b128 v[24:27], v2 offset:3648\n"
    "v_mfma_f32_16x16x32_f16 v[52:55], v[108:111], v[36:39], v[52:55]\n"
    "s_waitcnt lgkmcnt(12)\n"
    "v_mfma_f32_16x16x32_f16 v[48:51], v[72:75], v[40:43], v[48:51]\n"
    "ds_read_b128 v[28:31], v2 offset:3584\n"
    "ds_read_b128 v[32:35], v2 offset:3520\n"
    "v_mfma_f32_16x16x32_f16 v[52:55], v[104:107], v[40:43], v[52:55]\n"
    "s_waitcnt lgkmcnt(9)\n"
    "v_mfma_f32_16x16x32_f16 v[48:51], v[68:71], v[44:47], v[48:51]\n"
    "ds_read_b128 v[36:39], v2 offset:3456\n"
    "ds_read_b128 v[40:43], v2 offset:3392\n"
    "v_mfma_f32_16x16x32_f16 v[52:55], v[100:103], v[44:47], v[52:55]\n"
    "s_waitcnt lgkmcnt(4)\n"
    "v_mfma_f32_16x16x32_f16 v[48:51], v[64:67], v[24:27], v[48:51]\n"
    "ds_read_b128 v[44:47], v2 offset:3328\n"
    "v_mfma_f32_16x16x32_f16 v[52:55], v[96:99], v[24:27], v[52:55]\n"
    "s_waitcnt lgkmcnt(4)\n"
    "v_mfma_f32_16x16x32_f16 v[48:51], v[60:63], v[28:31], v[48:51]\n"
    "ds_read_b128 v[24:27], v2 offset:3264\n"
    "v_mfma_f32_16x16x32_f16 v[52:55], v[92:95], v[28:31], v[52:55]\n"
    "v_mfma_f32_16x16x32_f16 v[56:59], v[124:127], v[28:31], 0\n"
    "s_waitcnt lgkmcnt(4)\n"
    "v_mfma_f32_16x16x32_f16 v[52:55], v[88:91], v[32:35], v[52:55]\n"
    "ds_read_b128 v[28:31], v2 offset:3200\n"
    "v_mfma_f32_16x16x32_f16 v[56:59], v[120:123], v[32:35], v[56:59]\n"
    "s_waitcnt lgkmcnt(4)\n"
    "v_mfma_f32_16x16x32_f16 v[52:55], v[84:87], v[36:39], v[52:55]\n"
    "ds_read_b128 v[32:35], v2 offset:3136\n"
    "v_mfma_f32_16x16x32_f16 v[56:59], v[116:119], v[36:39], v[56:59]\n"
    "ds_write_b64 v4, v[48:49] offset:0\n"
    "s_waitcnt lgkmcnt(5)\n"
    "v_mfma_f32_16x16x32_f16 v[52:55], v[80:83], v[40:43], v[52:55]\n"
    "ds_read_b128 v[36:39], v2 offset:3072\n"
    "v_mfma_f32_16x16x32_f16 v[56:59], v[112:115], v[40:43], v[56:59]\n"
    "ds_write_b64 v4, v[50:51] offset:640\n"
    "s_waitcnt lgkmcnt(6)\n"
    "v_mfma_f32_16x16x32_f16 v[52:55], v[76:79], v[44:47], v[52:55]\n"
    "ds_read_b128 v[40:43], v2 offset:3008\n"
    "v_mfma_f32_16x16x32_f16 v[56:59], v[108:111], v[44:47], v[56:59]\n"
    "ds_write_b8 v5, v8 offset:64\n"
    "s_waitcnt lgkmcnt(7)\n"
    "v_mfma_f32_16x16x32_f16 v[52:55], v[72:75], v[24:27], v[52:55]\n"
    "ds_read_b128 v[44:47], v2 offset:2944\n"
    "v_mfma_f32_16x16x32_f16 v[56:59], v[104:107], v[24:27], v[56:59]\n"
    "s_waitcnt lgkmcnt(7)\n"
    "v_mfma_f32_16x16x32_f16 v[52:55], v[68:71], v[28:31], v[52:55]\n"
    "ds_read_b128 v[24:27], v2 offset:2880\n"
    "v_mfma_f32_16x16x32_f16 v[56:59], v[100:103], v[28:31], v[56:59]\n"
    "s_waitcnt lgkmcnt(7)\n"
    "v_mfma_f32_16x16x32_f16 v[52:55], v[64:67], v[32:35], v[52:55]\n"
    "ds_read_b128 v[28:31], v2 offset:2816\n"
    "v_mfma_f32_16x16x32_f16 v[56:59], v[96:99], v[32:35], v[56:59]\n"
    "s_waitcnt lgkmcnt(6)\n"
    "v_mfma_f32_16x16x32_f16 v[52:55], v[60:63], v[36:39], v[52:55]\n"
    "ds_read_b128 v[32:35], v2 offset:2752\n"
    "v_mfma_f32_16x16x32_f16 v[56:59], v[92:95], v[36:39], v[56:59]\n"
    "v_mfma_f32_16x16x32_f16 v[48:51], v[124:127], v[36:39], 0\n"
    "s_waitcnt lgkmcnt(5)\n"
    "v_mfma_f32_16x16x32_f16 v[56:59], v[88:91], v[40:43], v[56:59]\n"
    "ds_read_b128 v[36:39], v2 offset:2688\n"
    "v_mfma_f32_16x16x32_f16 v[48:51], v[120:123], v[40:43], v[48:51]\n"
    "ds_read_b32 v11, v10 offset:44\n"
    "s_waitcnt lgkmcnt(5)\n"
    "v_mfma_f32_16x16x32_f16 v[56:59], v[84:87], v[44:47], v[56:59]\n"
    "ds_read_b128 v[40:43], v2 offset:2624\n"
    "v_mfma_f32_16x16x32_f16 v[48:51], v[116:119], v[44:47], v[48:51]\n"
    "ds_write_b64 v4, v[52:53] offset:1280\n"
    "s_waitcnt lgkmcnt(6)\n"
    "v_mfma_f32_16x16x32_f16 v[56:59], v[80:83], v[24:27], v[56:59]\n"
    "ds_read_b128 v[44:47], v2 offset:2560\n"
    "v_mfma_f32_16x16x32_f16 v[48:51], v[112:115], v[24:27], v[48:51]\n"
    "ds_write_b64 v4, v[54:55] offset:1920\n"
    "s_waitcnt lgkmcnt(7)\n"
    "v_mfma_f32_16x16x32_f16 v[56:59], v[76:79], v[28:31], v[56:59]\n"
    "ds_read_b128 v[24:27], v2 offset:2496\n"
    "v_mfma_f32_16x16x32_f16 v[48:51], v[108:111], v[28:31], v[48:51]\n"
    "ds_write_b8 v5, v8 offset:72\n"
    "s_waitcnt lgkmcnt(6)\n"
    "v_cmp_ne_u32 vcc, 1, v11\n"
    "v_mfma_f32_16x16x32_f16 v[56:59], v[72:75], v[32:35], v[56:59]\n"
    "ds_read_b128 v[28:31], v2 offset:2432\n"
    "v_mfma_f32_16x16x32_f16 v[48:51], v[104:107], v[32:35], v[48:51]\n"
    "s_cbranch_vccnz NEARW\n"
    "NEARC:\n"
    "ds_read_b64 v[52:53], v4 offset:5120\n"
    "ds_read_b64 v[54:55], v4 offset:5760\n"
    "v_mfma_f32_16x16x32_f16 v[56:59], v[68:71], v[36:39], v[56:59]\n"
    "ds_read_b128 v[32:35], v2 offset:2368\n"
    "v_mfma_f32_16x16x32_f16 v[48:51], v[100:103], v[36:39], v[48:51]\n"
    "s_waitcnt lgkmcnt(9)\n"
    "v_mfma_f32_16x16x32_f16 v[56:59], v[64:67], v[40:43], v[56:59]\n"
    "ds_read_b128 v[36:39], v2 offset:2304\n"
    "v_mfma_f32_16x16x32_f16 v[48:51], v[96:99], v[40:43], v[48:51]\n"
    "s_waitcnt lgkmcnt(8)\n"
    "v_mfma_f32_16x16x32_f16 v[56:59], v[60:63], v[44:47], v[56:59]\n"
    "ds_read_b128 v[40:43], v2 offset:2240\n"
    "v_mfma_f32_16x16x32_f16 v[48:51], v[92:95], v[44:47], v[48:51]\n"
    "s_waitcnt lgkmcnt(3)\n"
    "v_mfma_f32_16x16x32_f16 v[52:55], v[124:127], v[44:47], v[52:55]\n"
    "v_mfma_f32_16x16x32_f16 v[48:51], v[88:91], v[24:27], v[48:51]\n"
    "ds_read_b128 v[44:47], v2 offset:2176\n"
    "v_mfma_f32_16x16x32_f16 v[52:55], v[120:123], v[24:27], v[52:55]\n"
    "v_mfma_f32_16x16x32_f16 v[48:51], v[84:87], v[28:31], v[48:51]\n"
    "ds_read_b128 v[24:27], v2 offset:2112\n"
    "v_mfma_f32_16x16x32_f16 v[52:55], v[116:119], v[28:31], v[52:55]\n"
    "ds_write_b64 v4, v[56:57] offset:2560\n"
    "s_waitcnt lgkmcnt(5)\n"
    "v_mfma_f32_16x16x32_f16 v[48:51], v[80:83], v[32:35], v[48:51]\n"
    "ds_read_b128 v[28:31], v2 offset:2048\n"
    "v_mfma_f32_16x16x32_f16 v[52:55], v[112:115], v[32:35], v[52:55]\n"
    "ds_write_b64 v4, v[58:59] offset:3200\n"
    "s_waitcnt lgkmcnt(6)\n"
    "v_mfma_f32_16x16x32_f16 v[48:51], v[76:79], v[36:39], v[48:51]\n"
    "ds_read_b128 v[32:35], v2 offset:1984\n"
    "v_mfma_f32_16x16x32_f16 v[52:55], v[108:111], v[36:39], v[52:55]\n"
    "ds_write_b8 v5, v8 offset:80\n"
    "s_waitcnt lgkmcnt(7)\n"
    "v_mfma_f32_16x16x32_f16 v[48:51], v[72:75], v[40:43], v[48:51]\n"
    "ds_read_b128 v[36:39], v2 offset:1920\n"
    "v_mfma_f32_16x16x32_f16 v[52:55], v[104:107], v[40:43], v[52:55]\n"
    "ds_read_b64 v[56:57], v4 offset:6400\n"
    "ds_read_b64 v[58:59], v4 offset:7040\n"
    "s_waitcnt lgkmcnt(9)\n"
    "v_mfma_f32_16x16x32_f16 v[48:51], v[68:71], v[44:47], v[48:51]\n"
    "ds_read_b128 v[40:43], v2 offset:1856\n"
    "v_mfma_f32_16x16x32_f16 v[52:55], v[100:103], v[44:47], v[52:55]\n"
    "s_waitcnt lgkmcnt(9)\n"
    "v_mfma_f32_16x16x32_f16 v[48:51], v[64:67], v[24:27], v[48:51]\n"
    "ds_read_b128 v[44:47], v2 offset:1792\n"
    "v_mfma_f32_16x16x32_f16 v[52:55], v[96:99], v[24:27], v[52:55]\n"
    "s_waitcnt lgkmcnt(8)\n"
    "v_mfma_f32_16x16x32_f16 v[48:51], v[60:63], v[28:31], v[48:51]\n"
    "ds_read_b128 v[24:27], v2 offset:1728\n"
    "v_mfma_f32_16x16x32_f16 v[52:55], v[92:95], v[28:31], v[52:55]\n"
    "s_waitcnt lgkmcnt(3)\n"
    "v_mfma_f32_16x16x32_f16 v[56:59], v[124:127], v[28:31], v[56:59]\n"
    "v_mfma_f32_16x16x32_f16 v[52:55], v[88:91], v[32:35], v[52:55]\n"
    "ds_read_b128 v[28:31], v2 offset:1664\n"
    "v_mfma_f32_16x16x32_f16 v[56:59], v[120:123], v[32:35], v[56:59]\n"
    "v_mfma_f32_16x16x32_f16 v[52:55], v[84:87], v[36:39], v[52:55]\n"
    "ds_read_b128 v[32:35], v2 offset:1600\n"
    "v_mfma_f32_16x16x32_f16 v[56:59], v[116:119], v[36:39], v[56:59]\n"
    "ds_write_b64 v4, v[48:49] offset:3840\n"
    "s_waitcnt lgkmcnt(5)\n"
    "v_mfma_f32_16x16x32_f16 v[52:55], v[80:83], v[40:43], v[52:55]\n"
    "ds_read_b128 v[36:39], v2 offset:1536\n"
    "v_mfma_f32_16x16x32_f16 v[56:59], v[112:115], v[40:43], v[56:59]\n"
    "ds_write_b64 v4, v[50:51] offset:4480\n"
    "s_waitcnt lgkmcnt(6)\n"
    "v_mfma_f32_16x16x32_f16 v[52:55], v[76:79], v[44:47], v[52:55]\n"
    "ds_read_b128 v[40:43], v2 offset:1472\n"
    "v_mfma_f32_16x16x32_f16 v[56:59], v[108:111], v[44:47], v[56:59]\n"
    "ds_write_b8 v5, v8 offset:88\n"
    "s_waitcnt lgkmcnt(7)\n"
    "v_mfma_f32_16x16x32_f16 v[52:55], v[72:75], v[24:27], v[52:55]\n"
    "ds_read_b128 v[44:47], v2 offset:1408\n"
    "v_mfma_f32_16x16x32_f16 v[56:59], v[104:107], v[24:27], v[56:59]\n"
    "ds_read_b64 v[48:49], v4 offset:7680\n"
    "ds_read_b64 v[50:51], v4 offset:8320\n"
    "s_waitcnt lgkmcnt(9)\n"
    "v_mfma_f32_16x16x32_f16 v[52:55], v[68:71], v[28:31], v[52:55]\n"
    "ds_read_b128 v[24:27], v2 offset:1344\n"
    "v_mfma_f32_16x16x32_f16 v[56:59], v[100:103], v[28:31], v[56:59]\n"
    "s_waitcnt lgkmcnt(9)\n"
    "v_mfma_f32_16x16x32_f16 v[52:55], v[64:67], v[32:35], v[52:55]\n"
    "ds_read_b128 v[28:31], v2 offset:1280\n"
    "v_mfma_f32_16x16x32_f16 v[56:59], v[96:99], v[32:35], v[56:59]\n"
    "s_waitcnt lgkmcnt(8)\n"
    "v_mfma_f32_16x16x32_f16 v[52:55], v[60:63], v[36:39], v[52:55]\n"
    "ds_read_b128 v[32:35], v2 offset:1216\n"
    "v_mfma_f32_16x16x32_f16 v[56:59], v[92:95], v[36:39], v[56:59]\n"
    "s_waitcnt lgkmcnt(3)\n"
    "v_mfma_f32_16x16x32_f16 v[48:51], v[124:127], v[36:39], v[48:51]\n"
    "v_mfma_f32_16x16x32_f16 v[56:59], v[88:91], v[40:43], v[56:59]\n"
    "ds_read_b128 v[36:39], v2 offset:1152\n"
    "v_mfma_f32_16x16x32_f16 v[48:51], v[120:123], v[40:43], v[48:51]\n"
    "v_mfma_f32_16x16x32_f16 v[56:59], v[84:87], v[44:47], v[56:59]\n"
    "ds_read_b128 v[40:43], v2 offset:1088\n"
    "v_mfma_f32_16x16x32_f16 v[48:51], v[116:119], v[44:47], v[48:51]\n"
    "ds_write_b64 v4, v[52:53] offset:5120\n"
    "s_waitcnt lgkmcnt(5)\n"
    "v_mfma_f32_16x16x32_f16 v[56:59], v[80:83], v[24:27], v[56:59]\n"
    "ds_read_b128 v[44:47], v2 offset:1024\n"
    "v_mfma_f32_16x16x32_f16 v[48:51], v[112:115], v[24:27], v[48:51]\n"
    "ds_write_b64 v4, v[54:55] offset:5760\n"
    "s_waitcnt lgkmcnt(6)\n"
    "v_mfma_f32_16x16x32_f16 v[56:59], v[76:79], v[28:31], v[56:59]\n"
    "ds_read_b128 v[24:27], v2 offset:960\n"
    "v_mfma_f32_16x16x32_f16 v[48:51], v[108:111], v[28:31], v[48:51]\n"
    "ds_write_b8 v5, v8 offset:96\n"
    "s_waitcnt lgkmcnt(7)\n"
    "v_mfma_f32_16x16x32_f16 v[56:59], v[72:75], v[32:35], v[56:59]\n"
    "ds_read_b128 v[28:31], v2 offset:896\n"
    "v_mfma_f32_16x16x32_f16 v[48:51], v[104:107], v[32:35], v[48:51]\n"
    "ds_read_b64 v[52:53], v4 offset:8960\n"
    "ds_read_b64 v[54:55], v4 offset:9600\n"
    "s_waitcnt lgkmcnt(9)\n"
    "v_mfma_f32_16x16x32_f16 v[56:59], v[68:71], v[36:39], v[56:59]\n"
    "ds_read_b128 v[32:35], v2 offset:832\n"
    "v_mfma_f32_16x16x32_f16 v[48:51], v[100:103], v[36:39], v[48:51]\n"
    "s_waitcnt lgkmcnt(9)\n"
    "v_mfma_f32_16x16x32_f16 v[56:59], v[64:67], v[40:43], v[56:59]\n"
    "ds_read_b128 v[36:39], v2 offset:768\n"
    "v_mfma_f32_16x16x32_f16 v[48:51], v[96:99], v[40:43], v[48:51]\n"
    "s_waitcnt lgkmcnt(8)\n"
    "v_mfma_f32_16x16x32_f16 v[56:59], v[60:63], v[44:47], v[56:59]\n"
    "ds_read_b128 v[40:43], v2 offset:704\n"
    "v_mfma_f32_16x16x32_f16 v[48:51], v[92:95], v[44:47], v[48:51]\n"
    "s_waitcnt lgkmcnt(3)\n"
    "v_mfma_f32_16x16x32_f16 v[52:55], v[124:127], v[44:47], v[52:55]\n"
    "v_mfma_f32_16x16x32_f16 v[48:51], v[88:91], v[24:27], v[48:51]\n"
    "ds_read_b128 v[44:47], v2 offset:640\n"
    "v_mfma_f32_16x16x32_f16 v[52:55], v[120:123], v[24:27], v[52:55]\n"
    "v_mfma_f32_16x16x32_f16 v[48:51], v[84:87], v[28:31], v[48:51]\n"
    "ds_read_b128 v[24:27], v2 offset:576\n"
    "v_mfma_f32_16x16x32_f16 v[52:55], v[116:119], v[28:31], v[52:55]\n"
    "ds_write_b64 v4, v[56:57] offset:6400\n"
    "s_waitcnt lgkmcnt(5)\n"
    "v_mfma_f32_16x16x32_f16 v[48:51], v[80:83], v[32:35], v[48:51]\n"
    "ds_read_b128 v[28:31], v2 offset:512\n"
    "v_mfma_f32_16x16x32_f16 v[52:55], v[112:115], v[32:35], v[52:55]\n"
    "ds_write_b64 v4, v[58:59] offset:7040\n"
    "ds_write_b8 v5, v8 offset:104\n"
    "s_waitcnt lgkmcnt(7)\n"
    "v_mfma_f32_16x16x32_f16 v[48:51], v[76:79], v[36:39], v[48:51]\n"
    "ds_read_b128 v[32:35], v2 offset:448\n"
    "v_mfma_f32_16x16x32_f16 v[52:55], v[108:111], v[36:39], v[52:55]\n"
    "s_waitcnt lgkmcnt(7)\n"
    "v_mfma_f32_16x16x32_f16 v[48:51], v[72:75], v[40:43], v[48:51]\n"
    "ds_read_b128 v[36:39], v2 offset:384\n"
    "v_mfma_f32_16x16x32_f16 v[52:55], v[104:107], v[40:43], v[52:55]\n"
    "s_waitcnt lgkmcnt(7)\n"
    "v_mfma_f32_16x16x32_f16 v[48:51], v[68:71], v[44:47], v[48:51]\n"
    "ds_read_b128 v[40:43], v2 offset:320\n"
    "v_mfma_f32_16x16x32_f16 v[52:55], v[100:103], v[44:47], v[52:55]\n"
    "s_waitcnt lgkmcnt(7)\n"
    "v_mfma_f32_16x16x32_f16 v[48:51], v[64:67], v[24:27], v[48:51]\n"
    "ds_read_b128 v[44:47], v2 offset:256\n"
    "v_mfma_f32_16x16x32_f16 v[52:55], v[96:99], v[24:27], v[52:55]\n"
    "s_waitcnt lgkmcnt(6)\n"
    "v_mfma_f32_16x16x32_f16 v[48:51], v[60:63], v[28:31], v[48:51]\n"
    "ds_read_b128 v[24:27], v2 offset:192\n"
    "v_mfma_f32_16x16x32_f16 v[52:55], v[92:95], v[28:31], v[52:55]\n"
    "s_waitcnt lgkmcnt(4)\n"
    "v_mfma_f32_16x16x32_f16 v[52:55], v[88:91], v[32:35], v[52:55]\n"
    "ds_read_b128 v[28:31], v2 offset:128\n"
    "s_waitcnt lgkmcnt(4)\n"
    "v_mfma_f32_16x16x32_f16 v[52:55], v[84:87], v[36:39], v[52:55]\n"
    "ds_read_b128 v[32:35], v2 offset:64\n"
    "ds_write_b64 v4, v[48:49] offset:7680\n"
    "s_waitcnt lgkmcnt(5)\n"
    "v_mfma_f32_16x16x32_f16 v[52:55], v[80:83], v[40:43], v[52:55]\n"
    "ds_read_b128 v[36:39], v2 offset:0\n"
    "ds_write_b64 v4, v[50:51] offset:8320\n"
    "ds_write_b8 v5, v8 offset:112\n"
    "s_waitcnt lgkmcnt(7)\n"
    "v_mfma_f32_16x16x32_f16 v[52:55], v[76:79], v[44:47], v[52:55]\n"
    "s_waitcnt lgkmcnt(6)\n"
    "v_mfma_f32_16x16x32_f16 v[52:55], v[72:75], v[24:27], v[52:55]\n"
    "s_waitcnt lgkmcnt(5)\n"
    "v_mfma_f32_16x16x32_f16 v[52:55], v[68:71], v[28:31], v[52:55]\n"
    "s_waitcnt lgkmcnt(4)\n"
    "v_mfma_f32_16x16x32_f16 v[52:55], v[64:67], v[32:35], v[52:55]\n"
    "s_waitcnt lgkmcnt(2)\n"
    "v_mfma_f32_16x16x32_f16 v[52:55], v[60:63], v[36:39], v[52:55]\n"
    "s_nop 7\n"
    "ds_write_b64 v4, v[52:53] offset:8960\n"
    "ds_write_b64 v4, v[54:55] offset:9600\n"
    "ds_write_b8 v5, v8 offset:120\n"
    "s_setprio 0\n"
    "s_branch LN2\n"
    "NEARW:\n"
    "s_branch E_24\n"
    "R_23:\n"
    "s_sleep 1\n"
    "s_sub_u32 s39, s39, 1\n"
    "s_cbranch_scc1 ABORT\n"
    "E_24:\n"
    "ds_read_b32 v11, v10 offset:44\n"
    "s_waitcnt lgkmcnt(0)\n"
    "v_cmp_ne_u32 vcc, 1, v11\n"
    "s_cbranch_vccnz R_23\n"
    "s_branch NEARC\n"
    "EPS:\n"
    "v_mov_b32 v14, 147968\n"
    "v_mov_b32 v15, 1\n"
    "v_bfe_u32 v2, v0, 3, 3\n"
    "v_bfe_u32 v3, v0, 1, 2\n"
    "v_mul_u32_u24 v3, 160, v3\n"
    "v_lshl_add_u32 v2, v2, 3, v3\n"
    "v_and_b32 v3, 1, v0\n"
    "s_movk_i32 s43, 640\n"
    "v_mad_u32_u24 v2, v3, s43, v2\n"
    "s_mul_i32 s43, s38, 16512\n"
    "s_add_u32 s43, s43, 131072\n"
    "v_and_b32 v3, 63, v0\n"
    "v_lshl_add_u32 v1, v3, 4, s43\n"
    "v_mov_b32 v43, s36\n"
    "s_sub_u32 s57, 15, s37\n"
    "v_add_u32 v36, 1792, v86\n"
    "s_lshl_b32 s49, s57, 10\n"
    "s_lshr_b32 s46, s57, 1\n"
    "s_mul_i32 s43, s46, 1280\n"
    "s_and_b32 s47, s57, 1\n"
    "s_lshl_b32 s47, s47, 6\n"
    "s_add_u32 s43, s43, s47\n"
    "v_add_u32 v30, s43, v2\n"
    "s_lshl_b32 s46, s46, 3\n"
    "v_add_u32 v33, s46, v14\n"
    "s_lshl_b32 s50, s54, 10\n"
    "s_lshr_b32 s46, s54, 1\n"
    "s_mul_i32 s43, s46, 1280\n"
    "s_and_b32 s47, s54, 1\n"
    "s_lshl_b32 s47, s47, 6\n"
    "s_add_u32 s43, s43, s47\n"
    "v_add_u32 v31, s43, v2\n"
    "s_lshl_b32 s46, s46, 3\n"
    "v_add_u32 v34, s46, v14\n"
    "s_lshl_b32 s51, s55, 10\n"
    "s_lshr_b32 s46, s55, 1\n"
    "s_mul_i32 s43, s46, 1280\n"
    "s_and_b32 s47, s55, 1\n"
    "s_lshl_b32 s47, s47, 6\n"
    "s_add_u32 s43, s43, s47\n"
    "v_add_u32 v32, s43, v2\n"
    "s_lshl_b32 s46, s46, 3\n"
    "v_add_u32 v35, s46, v14\n"
    "s_mov_b32 s46, 0x01010101\n"
    "s_movk_i32 s47, 0x0101\n"
    "s_and_b32 s42, s38, 1\n"
    "s_lshl_b32 s42, s42, 4\n"
    "s_waitcnt lgkmcnt(0)\n"
    "s_lshr_b64 s[52:53], s[52:53], s42\n"
    "s_lshl_b32 s44, s52, 16\n"
    "s_and_b32 s45, s52, 0xffff0000\n"
    "s_lshl_b32 s48, s53, 16\n"
    "s_branch STG0\n"
    "EPI:\n"
    "s_setprio 2\n"
    "s_waitcnt lgkmcnt(0)\n"
    "v_mul_f32 v120, v121, v114\n"
    "v_mul_f32 v114, s44, v114\n"
    "v_mul_f32 v122, v123, v116\n"
    "v_mul_f32 v116, s45, v116\n"
    "v_mul_f32 v124, v125, v118\n"
    "v_mul_f32 v118, s48, v118\n"
    "v_fma_mix_f32 v70, v94, v114, 0 op_sel:[1,0,0] op_sel_hi:[1,0,0]\n"
    "v_fma_mix_f32 v71, v94, v114, 0 op_sel_hi:[1,0,0]\n"
    "v_fma_mix_f32 v72, v95, v116, 0 op_sel:[1,0,0] op_sel_hi:[1,0,0]\n"
    "v_fma_mix_f32 v73, v95, v116, 0 op_sel_hi:[1,0,0]\n"
    "v_fma_mix_f32 v74, v96, v118, 0 op_sel:[1,0,0] op_sel_hi:[1,0,0]\n"
    "v_fma_mix_f32 v75, v96, v118, 0 op_sel_hi:[1,0,0]\n"
    "v_cvt_pk_bf16_f32 v70, 0, v70\n"
    "v_cvt_pk_bf16_f32 v71, 0, v71\n"
    "v_cvt_pk_bf16_f32 v72, 0, v72\n"
    "v_cvt_pk_bf16_f32 v73, 0, v73\n"
    "v_cvt_pk_bf16_f32 v74, 0, v74\n"
    "v_cvt_pk_bf16_f32 v75, 0, v75\n"
    "v_fma_mix_f32 v76, v97, v114, 0 op_sel:[1,0,0] op_sel_hi:[1,0,0]\n"
    "v_fma_mix_f32 v77, v97, v114, 0 op_sel_hi:[1,0,0]\n"
    "v_fma_mix_f32 v78, v98, v116, 0 op_sel:[1,0,0] op_sel_hi:[1,0,0]\n"
    "v_fma_mix_f32 v79, v98, v116, 0 op_sel_hi:[1,0,0]\n"
    "v_fma_mix_f32 v80, v99, v118, 0 op_sel:[1,0,0] op_sel_hi:[1,0,0]\n"
    "v_fma_mix_f32 v81, v99, v118, 0 op_sel_hi:[1,0,0]\n"
    "v_cvt_pk_bf16_f32 v76, 0, v76\n"
    "v_cvt_pk_bf16_f32 v77, 0, v77\n"
    "v_cvt_pk_bf16_f32 v78, 0, v78\n"
    "v_cvt_pk_bf16_f32 v79, 0, v79\n"
    "v_cvt_pk_bf16_f32 v80, 0, v80\n"
    "v_cvt_pk_bf16_f32 v81, 0, v81\n"
    "s_cmpk_lt_u32 s37, 14\n"
    "s_cbranch_scc1 EP1\n"
    "ds_read_b32 v24, v36 offset:0\n"
    "ds_read_b32 v25, v36 offset:4576\n"
    "ds_read_b32 v26, v36 offset:9152\n"
    "s_waitcnt lgkmcnt(0)\n"
    "v_fma_mix_f32 v64, v24, v114, 0 op_sel:[1,0,0] op_sel_hi:[1,0,0]\n"
    "v_fma_mix_f32 v65, v24, v114, 0 op_sel_hi:[1,0,0]\n"
    "v_fma_mix_f32 v66, v25, v116, 0 op_sel:[1,0,0] op_sel_hi:[1,0,0]\n"
    "v_fma_mix_f32 v67, v25, v116, 0 op_sel_hi:[1,0,0]\n"
    "v_fma_mix_f32 v68, v26, v118, 0 op_sel:[1,0,0] op_sel_hi:[1,0,0]\n"
    "v_fma_mix_f32 v69, v26, v118, 0 op_sel_hi:[1,0,0]\n"
    "v_cvt_pk_bf16_f32 v64, 0, v64\n"
    "v_cvt_pk_bf16_f32 v65, 0, v65\n"
    "v_cvt_pk_bf16_f32 v66, 0, v66\n"
    "v_cvt_pk_bf16_f32 v67, 0, v67\n"
    "v_cvt_pk_bf16_f32 v68, 0, v68\n"
    "v_cvt_pk_bf16_f32 v69, 0, v69\n"
    "s_branch E_26\n"
    "R_25:\n"
    "s_sleep 1\n"
    "s_sub_u32 s39, s39, 1\n"
    "s_cbranch_scc1 ABORT\n"
    "E_26:\n"
    "ds_read_b32 v22, v33 offset:64\n"
    "s_waitcnt lgkmcnt(0)\n"
    "v_cmp_ne_u32 vcc, 0x010101, v22\n"
    "s_cbranch_vccnz R_25\n"
    "ds_read_b64 v[48:49], v30 offset:0\n"
    "ds_read_b64 v[50:51], v30 offset:10240\n"
    "ds_read_b64 v[52:53], v30 offset:20480\n"
    "s_waitcnt lgkmcnt(0)\n"
    "v_cvt_pk_bf16_f32 v48, 0, v48\n"
    "v_cvt_pk_bf16_f32 v49, 0, v49\n"
    "v_cvt_pk_bf16_f32 v50, 0, v50\n"
    "v_cvt_pk_bf16_f32 v51, 0, v51\n"
    "v_cvt_pk_bf16_f32 v52, 0, v52\n"
    "v_cvt_pk_bf16_f32 v53, 0, v53\n"
    "v_fma_f32 v48, v48, v120, v64\n"
    "v_fma_f32 v49, v49, v120, v65\n"
    "v_fma_f32 v50, v50, v122, v66\n"
    "v_fma_f32 v51, v51, v122, v67\n"
    "v_fma_f32 v52, v52, v124, v68\n"
    "v_fma_f32 v53, v53, v124, v69\n"
    "v_cvt_pk_bf16_f32 v48, 0, v48\n"
    "v_cvt_pk_bf16_f32 v49, 0, v49\n"
    "v_cvt_pk_bf16_f32 v50, 0, v50\n"
    "v_cvt_pk_bf16_f32 v51, 0, v51\n"
    "v_cvt_pk_bf16_f32 v52, 0, v52\n"
    "v_cvt_pk_bf16_f32 v53, 0, v53\n"
    "v_mul_f32 v48, v48, v115\n"
    "v_mul_f32 v49, v49, v115\n"
    "v_mul_f32 v50, v50, v117\n"
    "v_mul_f32 v51, v51, v117\n"
    "v_mul_f32 v52, v52, v119\n"
    "v_mul_f32 v53, v53, v119\n"
    "v_cvt_pk_bf16_f32 v40, v48, v49\n"
    "v_cvt_pk_bf16_f32 v41, v50, v51\n"
    "v_cvt_pk_bf16_f32 v42, v52, v53\n"
    "buffer_store_dwordx4 v[40:43], v1, s[20:23], s49 offen sc1\n"
    "s_mov_b64 exec, 1\n"
    "ds_add_u32 v14, v15 offset:60\n"
    "s_mov_b64 exec, -1\n"
    "EP1:\n"
    "s_cmpk_ge_u32 s37, 2\n"
    "s_cbranch_scc1 EP1F\n"
    "s_branch E_28\n"
    "R_27:\n"
    "s_sleep 1\n"
    "s_sub_u32 s39, s39, 1\n"
    "s_cbranch_scc1 ABORT\n"
    "E_28:\n"
    "ds_read_b32 v22, v34 offset:64\n"
    "s_waitcnt lgkmcnt(0)\n"
    "v_cmp_ne_u32 vcc, 0x010101, v22\n"
    "s_cbranch_vccnz R_27\n"
    "ds_read_b64 v[48:49], v31 offset:0\n"
    "ds_read_b64 v[50:51], v31 offset:10240\n"
    "ds_read_b64 v[52:53], v31 offset:20480\n"
    "s_waitcnt lgkmcnt(0)\n"
    "s_branch EP1M\n"
    "EP1F:\n"
    "s_branch E_30\n"
    "R_29:\n"
    "s_sleep 1\n"
    "s_sub_u32 s39, s39, 1\n"
    "s_cbranch_scc1 ABORT\n"
    "E_30:\n"
    "ds_read_b64 v[22:23], v34 offset:64\n"
    "s_waitcnt lgkmcnt(0)\n"
    "v_cmp_ne_u64 vcc, s[46:47], v[22:23]\n"
    "s_cbranch_vccnz R_29\n"
    "ds_read_b64 v[48:49], v31 offset:0\n"
    "ds_read_b64 v[54:55], v31 offset:28160\n"
    "ds_read_b64 v[50:51], v31 offset:10240\n"
    "ds_read_b64 v[56:57], v31 offset:35840\n"
    "ds_read_b64 v[52:53], v31 offset:20480\n"
    "ds_read_b64 v[58:59], v31 offset:43520\n"
    "s_waitcnt lgkmcnt(0)\n"
    "v_add_f32 v48, v48, v54\n"
    "v_add_f32 v49, v49, v55\n"
    "v_add_f32 v50, v50, v56\n"
    "v_add_f32 v51, v51, v57\n"
    "v_add_f32 v52, v52, v58\n"
    "v_add_f32 v53, v53, v59\n"
    "EP1M:\n"
    "v_cvt_pk_bf16_f32 v48, 0, v48\n"
    "v_cvt_pk_bf16_f32 v49, 0, v49\n"
    "v_cvt_pk_bf16_f32 v50, 0, v50\n"
    "v_cvt_pk_bf16_f32 v51, 0, v51\n"
    "v_cvt_pk_bf16_f32 v52, 0, v52\n"
    "v_cvt_pk_bf16_f32 v53, 0, v53\n"
    "v_fma_f32 v48, v48, v120, v70\n"
    "v_fma_f32 v49, v49, v120, v71\n"
    "v_fma_f32 v50, v50, v122, v72\n"
    "v_fma_f32 v51, v51, v122, v73\n"
    "v_fma_f32 v52, v52, v124, v74\n"
    "v_fma_f32 v53, v53, v124, v75\n"
    "v_cvt_pk_bf16_f32 v48, 0, v48\n"
    "v_cvt_pk_bf16_f32 v49, 0, v49\n"
    "v_cvt_pk_bf16_f32 v50, 0, v50\n"
    "v_cvt_pk_bf16_f32 v51, 0, v51\n"
    "v_cvt_pk_bf16_f32 v52, 0, v52\n"
    "v_cvt_pk_bf16_f32 v53, 0, v53\n"
    "v_mul_f32 v48, v48, v115\n"
    "v_mul_f32 v49, v49, v115\n"
    "v_mul_f32 v50, v50, v117\n"
    "v_mul_f32 v51, v51, v117\n"
    "v_mul_f32 v52, v52, v119\n"
    "v_mul_f32 v53, v53, v119\n"
    "v_cvt_pk_bf16_f32 v40, v48, v49\n"
    "v_cvt_pk_bf16_f32 v41, v50, v51\n"
    "v_cvt_pk_bf16_f32 v42, v52, v53\n"
    "buffer_store_dwordx4 v[40:43], v1, s[20:23], s50 offen sc1\n"
    "s_mov_b64 exec, 1\n"
    "ds_add_u32 v14, v15 offset:60\n"
    "s_mov_b64 exec, -1\n"
    "s_cmpk_ge_u32 s37, 12\n"
    "s_cbranch_scc1 EP2\n"
    "s_waitcnt vmcnt(1)\n"
    "s_mov_b64 exec, 1\n"
    "ds_add_u32 v14, v15 offset:56\n"
    "s_mov_b64 exec, -1\n"
    "EP2:\n"
    "s_cmpk_lg_u32 s37, 13\n"
    "s_cbranch_scc1 EP3\n"
    "s_memrealtime s[98:99]\n"
    "s_lshl_b32 s42, s2, 7\n"
    "s_add_u32 s42, s42, 94208\n"
    "v_mov_b32 v45, v43\n"
    "s_waitcnt lgkmcnt(0)\n"
    "v_mov_b32 v44, s98\n"
    "s_mov_b64 exec, 1\n"
    "buffer_store_dwordx2 v[44:45], off, s[20:23], s42 sc1\n"
    "s_mov_b64 exec, -1\n"
    "EP3:\n"
    "s_branch E_32\n"
    "R_31:\n"
    "s_sleep 1\n"
    "s_sub_u32 s39, s39, 1\n"
    "s_cbranch_scc1 ABORT\n"
    "E_32:\n"
    "ds_read_b64 v[22:23], v35 offset:56\n"
    "s_waitcnt lgkmcnt(0)\n"
    "v_cmp_ne_u64 vcc, s[46:47], v[22:23]\n"
    "s_cbranch_vccnz R_31\n"
    "s_branch E_34\n"
    "R_33:\n"
    "s_sub_u32 s39, s39, 1\n"
    "s_cbranch_scc1 ABORT\n"
    "E_34:\n"
    "ds_read_b64 v[22:23], v35 offset:64\n"
    "ds_read_b64 v[48:49], v32 offset:0\n"
    "ds_read_b64 v[54:55], v32 offset:28160\n"
    "ds_read_b64 v[50:51], v32 offset:10240\n"
    "ds_read_b64 v[56:57], v32 offset:35840\n"
    "ds_read_b64 v[52:53], v32 offset:20480\n"
    "ds_read_b64 v[58:59], v32 offset:43520\n"
    "s_waitcnt lgkmcnt(0)\n"
    "v_cmp_ne_u64 vcc, s[46:47], v[22:23]\n"
    "s_cbranch_vccnz R_33\n"
    "s_cmpk_ge_u32 s37, 14\n"
    "s_cbranch_scc1 EPZ\n"
    "v_add_f32 v48, v48, v54\n"
    "v_add_f32 v49, v49, v55\n"
    "v_add_f32 v50, v50, v56\n"
    "v_add_f32 v51, v51, v57\n"
    "v_add_f32 v52, v52, v58\n"
    "v_add_f32 v53, v53, v59\n"
    "v_cvt_pk_bf16_f32 v48, 0, v48\n"
    "v_cvt_pk_bf16_f32 v49, 0, v49\n"
    "v_cvt_pk_bf16_f32 v50, 0, v50\n"
    "v_cvt_pk_bf16_f32 v51, 0, v51\n"
    "v_cvt_pk_bf16_f32 v52, 0, v52\n"
    "v_cvt_pk_bf16_f32 v53, 0, v53\n"
    "v_fma_f32 v48, v48, v120, v76\n"
    "v_fma_f32 v49, v49, v120, v77\n"
    "v_fma_f32 v50, v50, v122, v78\n"
    "v_fma_f32 v51, v51, v122, v79\n"
    "v_fma_f32 v52, v52, v124, v80\n"
    "v_fma_f32 v53, v53, v124, v81\n"
    "v_cvt_pk_bf16_f32 v48, 0, v48\n"
    "v_cvt_pk_bf16_f32 v49, 0, v49\n"
    "v_cvt_pk_bf16_f32 v50, 0, v50\n"
    "v_cvt_pk_bf16_f32 v51, 0, v51\n"
    "v_cvt_pk_bf16_f32 v52, 0, v52\n"
    "v_cvt_pk_bf16_f32 v53, 0, v53\n"
    "v_mul_f32 v48, v48, v115\n"
    "v_mul_f32 v49, v49, v115\n"
    "v_mul_f32 v50, v50, v117\n"
    "v_mul_f32 v51, v51, v117\n"
    "v_mul_f32 v52, v52, v119\n"
    "v_mul_f32 v53, v53, v119\n"
    "v_cvt_pk_bf16_f32 v40, v48, v49\n"
    "v_cvt_pk_bf16_f32 v41, v50, v51\n"
    "v_cvt_pk_bf16_f32 v42, v52, v53\n"
    "buffer_store_dwordx4 v[40:43], v1, s[20:23], s51 offen sc1\n"
    "s_mov_b64 exec, 1\n"
    "ds_add_u32 v14, v15 offset:60\n"
    "s_mov_b64 exec, -1\n"
    "s_cmpk_ge_u32 s37, 12\n"
    "s_cbranch_scc1 LN2K\n"
    "s_setprio 0\n"
    "s_branch LN2\n"
    "EPZ:\n"
    "s_setprio 3\n"
    "v_pk_add_f32 v[48:49], v[48:49], v[54:55]\n"
    "v_pk_add_f32 v[50:51], v[50:51], v[56:57]\n"
    "v_pk_add_f32 v[52:53], v[52:53], v[58:59]\n"
    "v_cvt_pk_bf16_f32 v48, 0, v48\n"
    "v_cvt_pk_bf16_f32 v49, 0, v49\n"
    "v_cvt_pk_bf16_f32 v50, 0, v50\n"
    "v_cvt_pk_bf16_f32 v51, 0, v51\n"
    "v_cvt_pk_bf16_f32 v52, 0, v52\n"
    "v_cvt_pk_bf16_f32 v53, 0, v53\n"
    "v_pk_fma_f32 v[48:49], v[48:49], v[120:121], v[76:77] op_sel_hi:[1,0,1]\n"
    "v_pk_fma_f32 v[50:51], v[50:51], v[122:123], v[78:79] op_sel_hi:[1,0,1]\n"
    "v_pk_fma_f32 v[52:53], v[52:53], v[124:125], v[80:81] op_sel_hi:[1,0,1]\n"
    "v_cvt_pk_bf16_f32 v48, 0, v48\n"
    "v_cvt_pk_bf16_f32 v49, 0, v49\n"
    "v_cvt_pk_bf16_f32 v50, 0, v50\n"
    "v_cvt_pk_bf16_f32 v51, 0, v51\n"
    "v_cvt_pk_bf16_f32 v52, 0, v52\n"
    "v_cvt_pk_bf16_f32 v53, 0, v53\n"
    "v_pk_mul_f32 v[48:49], v[114:115], v[48:49] op_sel:[1,0] op_sel_hi:[1,1]\n"
    "v_pk_mul_f32 v[50:51], v[116:117], v[50:51] op_sel:[1,0] op_sel_hi:[1,1]\n"
    "v_pk_mul_f32 v[52:53], v[118:119], v[52:53] op_sel:[1,0] op_sel_hi:[1,1]\n"
    "v_cvt_pk_bf16_f32 v40, v48, v49\n"
    "v_cvt_pk_bf16_f32 v41, v50, v51\n"
    "v_cvt_pk_bf16_f32 v42, v52, v53\n"
    "buffer_store_dwordx4 v[40:43], v1, s[20:23], s51 offen sc1\n"
    "s_mov_b64 exec, 1\n"
    "ds_add_u32 v14, v15 offset:60\n"
    "s_mov_b64 exec, -1\n"
    "s_branch LN2K\n"
    "MISC:\n"
    "s_cmpk_ge_u32 s2, 128\n"
    "s_cbranch_scc1 PFZ\n"
    "v_and_b32 v109, 255, v0\n"
    "v_add_u32 v104, 192, v109\n"
    "v_min_u32 v104, 287, v104\n"
    "v_lshlrev_b32 v105, 2, v109\n"
    "v_lshlrev_b32 v106, 2, v104\n"
    "v_lshlrev_b32 v107, 4, v109\n"
    "v_lshlrev_b32 v108, 4, v104\n"
    "s_mov_b32 s44, 34304\n"
    "s_mov_b32 s45, 61952\n"
    "s_mov_b32 s46, 38912\n"
    "s_mov_b32 s47, 66560\n"
    "s_mov_b32 s48, 43520\n"
    "s_mov_b32 s49, 71168\n"
    "v_mov_b32 v14, 147968\n"
    "ds_read_b32 v4, v14 offset:480\n"
    "s_waitcnt lgkmcnt(0)\n"
    "v_readfirstlane_b32 s96, v4\n"
    "s_mov_b64 exec, -1\n"
    "global_load_dword v45, v105, s[26:27]\n"
    "global_load_dword v46, v106, s[26:27]\n"
    "s_setprio 1\n"
    "U_35:\n"
    "s_memrealtime s[98:99]\n"
    "s_waitcnt lgkmcnt(0)\n"
    "s_sub_u32 s98, s98, s96\n"
    "s_cmpk_lt_u32 s98, 300\n"
    "s_cbranch_scc1 U_35\n"
    "buffer_load_dwordx4 v[48:51], v107, s[20:23], s44 offen sc1\n"
    "buffer_load_dwordx4 v[52:55], v107, s[20:23], s45 offen sc1\n"
    "buffer_load_dwordx4 v[56:59], v107, s[20:23], s46 offen sc1\n"
    "buffer_load_dwordx4 v[60:63], v107, s[20:23], s47 offen sc1\n"
    "buffer_load_dwordx4 v[64:67], v107, s[20:23], s48 offen sc1\n"
    "buffer_load_dwordx4 v[68:71], v107, s[20:23], s49 offen sc1\n"
    "buffer_load_dwordx4 v[72:75], v108, s[20:23], s44 offen sc1\n"
    "buffer_load_dwordx4 v[76:79], v108, s[20:23], s45 offen sc1\n"
    "buffer_load_dwordx4 v[80:83], v108, s[20:23], s46 offen sc1\n"
    "buffer_load_dwordx4 v[84:87], v108, s[20:23], s47 offen sc1\n"
    "buffer_load_dwordx4 v[88:91], v108, s[20:23], s48 offen sc1\n"
    "buffer_load_dwordx4 v[92:95], v108, s[20:23], s49 offen sc1\n"
    "v_and_b32 v1, 63, v0\n"
    "v_lshlrev_b32 v1, 4, v1\n"
    "s_mov_b64 exec, -1\n"
    "v_mov_b32 v8, 0\n"
    "v_mov_b32 v9, 0\n"
    "v_mov_b32 v10, 0\n"
    "v_mov_b32 v11, 0\n"
    "v_lshrrev_b32 v2, 9, v1\n"
    "v_mul_u32_u24 v2, 1536, v2\n"
    "v_add_u32 v2, v2, v1\n"
    "v_add_u32 v2, 134048, v2\n"
    "ds_write_b128 v2, v[8:11]\n"
    "s_mov_b32 s42, 138240\n"
    "v_lshl_add_u32 v1, v107, 1, s42\n"
    "v_lshl_add_u32 v2, v108, 1, s42\n"
    "v_add_u32 v41, 72320, v105\n"
    "v_add_u32 v42, 72320, v106\n"
    "v_mov_b32 v15, 1\n"
    "v_mov_b32 v14, 147968\n"
    "s_branch E_37\n"
    "R_36:\n"
    "s_sub_u32 s39, s39, 1\n"
    "s_cbranch_scc1 ABORT\n"
    "s_sleep 4\n"
    "buffer_load_dwordx4 v[48:51], v107, s[20:23], s44 offen sc1\n"
    "buffer_load_dwordx4 v[52:55], v107, s[20:23], s45 offen sc1\n"
    "buffer_load_dwordx4 v[56:59], v107, s[20:23], s46 offen sc1\n"
    "buffer_load_dwordx4 v[60:63], v107, s[20:23], s47 offen sc1\n"
    "buffer_load_dwordx4 v[64:67], v107, s[20:23], s48 offen sc1\n"
    "buffer_load_dwordx4 v[68:71], v107, s[20:23], s49 offen sc1\n"
    "buffer_load_dwordx4 v[72:75], v108, s[20:23], s44 offen sc1\n"
    "buffer_load_dwordx4 v[76:79], v108, s[20:23], s45 offen sc1\n"
    "buffer_load_dwordx4 v[80:83], v108, s[20:23], s46 offen sc1\n"
    "buffer_load_dwordx4 v[84:87], v108, s[20:23], s47 offen sc1\n"
    "buffer_load_dwordx4 v[88:91], v108, s[20:23], s48 offen sc1\n"
    "buffer_load_dwordx4 v[92:95], v108, s[20:23], s49 offen sc1\n"
    "E_37:\n"
    "s_waitcnt vmcnt(0)\n"
    "v_bitop3_b32 v16, v49, v51, s36 bitop3:0x7e\n"
    "v_bitop3_b32 v17, v53, v55, s36 bitop3:0x7e\n"
    "v_bitop3_b32 v18, v57, v59, s36 bitop3:0x7e\n"
    "v_bitop3_b32 v19, v61, v63, s36 bitop3:0x7e\n"
    "v_bitop3_b32 v20, v65, v67, s36 bitop3:0x7e\n"
    "v_bitop3_b32 v21, v69, v71, s36 bitop3:0x7e\n"
    "v_bitop3_b32 v22, v73, v75, s36 bitop3:0x7e\n"
    "v_bitop3_b32 v23, v77, v79, s36 bitop3:0x7e\n"
    "v_bitop3_b32 v24, v81, v83, s36 bitop3:0x7e\n"
    "v_bitop3_b32 v25, v85, v87, s36 bitop3:0x7e\n"
    "v_bitop3_b32 v26, v89, v91, s36 bitop3:0x7e\n"
    "v_bitop3_b32 v27, v93, v95, s36 bitop3:0x7e\n"
    "v_or3_b32 v16, v16, v17, v18\n"
    "v_or3_b32 v19, v19, v20, v21\n"
    "v_or3_b32 v22, v22, v23, v24\n"
    "v_or3_b32 v25, v25, v26, v27\n"
    "v_or3_b32 v16, v16, v19, v22\n"
    "v_or_b32 v16, v16, v25\n"
    "v_cmp_ne_u32 vcc, 0, v16\n"
    "s_cbranch_vccnz R_36\n"
    "ds_write_b32 v41, v45\n"
    "v_add_f32 v16, v56, v60\n"
    "v_add_f32 v17, v48, v52\n"
    "v_add_f32 v19, v64, v68\n"
    "v_cvt_pk_bf16_f32 v16, 0, v16\n"
    "v_cvt_pk_bf16_f32 v17, 0, v17\n"
    "v_cvt_pk_bf16_f32 v19, 0, v19\n"
    "v_add_f32 v16, 1.0, v16\n"
    "v_lshlrev_b32 v32, 16, v45\n"
    "v_cvt_pk_bf16_f32 v16, 0, v16\n"
    "v_mul_f32 v18, v16, v32\n"
    "ds_write_b128 v1, v[16:19] offset:0\n"
    "v_add_f32 v20, v58, v62\n"
    "v_add_f32 v21, v50, v54\n"
    "v_add_f32 v23, v66, v70\n"
    "v_cvt_pk_bf16_f32 v20, 0, v20\n"
    "v_cvt_pk_bf16_f32 v21, 0, v21\n"
    "v_cvt_pk_bf16_f32 v23, 0, v23\n"
    "v_add_f32 v20, 1.0, v20\n"
    "v_and_b32 v33, 0xffff0000, v45\n"
    "v_cvt_pk_bf16_f32 v20, 0, v20\n"
    "v_mul_f32 v22, v20, v33\n"
    "ds_write_b128 v1, v[20:23] offset:16\n"
    "ds_write_b32 v42, v46\n"
    "v_add_f32 v24, v80, v84\n"
    "v_add_f32 v25, v72, v76\n"
    "v_add_f32 v27, v88, v92\n"
    "v_cvt_pk_bf16_f32 v24, 0, v24\n"
    "v_cvt_pk_bf16_f32 v25, 0, v25\n"
    "v_cvt_pk_bf16_f32 v27, 0, v27\n"
    "v_add_f32 v24, 1.0, v24\n"
    "v_lshlrev_b32 v34, 16, v46\n"
    "v_cvt_pk_bf16_f32 v24, 0, v24\n"
    "v_mul_f32 v26, v24, v34\n"
    "ds_write_b128 v2, v[24:27] offset:0\n"
    "v_add_f32 v28, v82, v86\n"
    "v_add_f32 v29, v74, v78\n"
    "v_add_f32 v31, v90, v94\n"
    "v_cvt_pk_bf16_f32 v28, 0, v28\n"
    "v_cvt_pk_bf16_f32 v29, 0, v29\n"
    "v_cvt_pk_bf16_f32 v31, 0, v31\n"
    "v_add_f32 v28, 1.0, v28\n"
    "v_and_b32 v35, 0xffff0000, v46\n"
    "v_cvt_pk_bf16_f32 v28, 0, v28\n"
    "v_mul_f32 v30, v28, v35\n"
    "ds_write_b128 v2, v[28:31] offset:16\n"
    "s_mov_b64 exec, 1\n"
    "ds_add_u32 v14, v15 offset:56\n"
    "s_mov_b64 exec, -1\n"
    "s_setprio 0\n"
    "v_cmp_gt_u32 vcc, 0x60, v109\n"
    "v_mul_f32 v32, v32, v17\n"
    "v_mul_f32 v33, v33, v21\n"
    "v_add_f32 v4, v18, v22\n"
    "v_mul_f32 v5, v18, v18\n"
    "v_fmac_f32 v5, v22, v22\n"
    "v_mul_f32 v5, 4.0, v5\n"
    "v_fmac_f32 v5, v32, v32\n"
    "v_fmac_f32 v5, v33, v33\n"
    "v_mul_f32 v34, v34, v25\n"
    "v_mul_f32 v35, v35, v29\n"
    "v_add_f32 v6, v26, v30\n"
    "v_mul_f32 v7, v26, v26\n"
    "v_fmac_f32 v7, v30, v30\n"
    "v_mul_f32 v7, 4.0, v7\n"
    "v_fmac_f32 v7, v34, v34\n"
    "v_fmac_f32 v7, v35, v35\n"
    "v_cndmask_b32 v6, 0, v6, vcc\n"
    "v_cndmask_b32 v7, 0, v7, vcc\n"
    "v_add_f32 v4, v4, v6\n"
    "v_add_f32 v5, v5, v7\n"
    "v_lshrrev_b32 v3, 4, v109\n"
    "v_lshlrev_b32 v3, 3, v3\n"
    "v_add_u32 v3, 148736, v3\n"
    "v_add_f32_dpp v4, v4, v4 row_ror:8 row_mask:0xf bank_mask:0xf\n"
    "v_add_f32_dpp v5, v5, v5 row_ror:8 row_mask:0xf bank_mask:0xf\n"
    "s_nop 0\n"
    "v_add_f32_dpp v4, v4, v4 row_ror:4 row_mask:0xf bank_mask:0xf\n"
    "v_add_f32_dpp v5, v5, v5 row_ror:4 row_mask:0xf bank_mask:0xf\n"
    "s_nop 0\n"
    "v_add_f32_dpp v4, v4, v4 row_ror:2 row_mask:0xf bank_mask:0xf\n"
    "v_add_f32_dpp v5, v5, v5 row_ror:2 row_mask:0xf bank_mask:0xf\n"
    "s_nop 0\n"
    "v_add_f32_dpp v4, v4, v4 row_ror:1 row_mask:0xf bank_mask:0xf\n"
    "v_add_f32_dpp v5, v5, v5 row_ror:1 row_mask:0xf bank_mask:0xf\n"
    "ds_write_b64 v3, v[4:5]\n"
    "LN2:\n"
    "s_cmpk_ge_u32 s2, 128\n"
    "s_cbranch_scc1 CDONE\n"
    "s_and_b32 s57, s2, 7\n"
    "s_lshl_b32 s57, s57, 4\n"
    "s_lshr_b32 s43, s2, 3\n"
    "s_add_u32 s57, s57, s43\n"
    "v_and_b32 v2, 7, v0\n"
    "v_lshrrev_b32 v3, 3, v0\n"
    "v_mul_u32_u24 v6, 12, v3\n"
    "s_movk_i32 s44, 0x900\n"
    "v_mad_u32_u24 v4, v2, s44, v6\n"
    "v_lshlrev_b32 v2, 4, v2\n"
    "v_add_u32 v4, v2, v4\n"
    "v_add_u32 v5, 72320, v6\n"
    "s_mov_b32 s44, 33024\n"
    "v_mad_u32_u24 v1, v3, s44, v2\n"
    "s_lshl_b32 s42, s57, 7\n"
    "s_add_u32 s42, s42, 131072\n"
    "s_add_u32 s43, s42, 16512\n"
    "s_mov_b32 s44, 138240\n"
    "v_lshl_add_u32 v6, v6, 3, s44\n"
    "v_mov_b32 v7, 147968\n"
    "v_and_b32 v16, 8, v0\n"
    "v_lshl_add_u32 v16, v16, 8, v2\n"
    "v_bfe_u32 v17, v0, 4, 2\n"
    "v_lshlrev_b32 v18, 2, v17\n"
    "s_lshl_b32 s44, s37, 7\n"
    "s_add_u32 s44, s44, 132512\n"
    "v_add3_u32 v16, v16, v18, s44\n"
    "v_and_b32 v18, 2, v17\n"
    "v_lshl_add_u32 v18, v18, 2, v2\n"
    "s_lshl_b32 s44, s37, 2\n"
    "s_add_u32 s44, s44, 136608\n"
    "v_mad_u32_u24 v18, v18, 6, s44\n"
    "v_and_b32 v17, 63, v0\n"
    "s_mov_b32 s44, 132512\n"
    "v_lshl_add_u32 v17, v17, 4, s44\n"
    "v_and_b32 v23, 7, v3\n"
    "v_mul_u32_u24 v22, 3072, v23\n"
    "v_add_u32 v22, v22, v2\n"
    "v_lshlrev_b32 v23, 2, v23\n"
    "v_lshl_add_u32 v23, v2, 1, v23\n"
    "s_mul_i32 s45, s57, 24576\n"
    "s_lshl_b32 s46, s37, 7\n"
    "s_add_u32 s45, s45, s46\n"
    "s_add_u32 s45, s45, 7341056\n"
    "s_add_u32 s46, s36, 1\n"
    "v_mov_b32 v87, s46\n"
    "v_mov_b32 v91, s46\n"
    "s_lshr_b32 s44, s57, 4\n"
    "s_lshl_b32 s44, s44, 1\n"
    "s_add_u32 s44, s44, 2\n"
    "s_branch E_39\n"
    "R_38:\n"
    "s_sleep 2\n"
    "s_sub_u32 s39, s39, 1\n"
    "s_cbranch_scc1 ABORT\n"
    "E_39:\n"
    "ds_read_b32 v19, v7 offset:56\n"
    "s_waitcnt lgkmcnt(0)\n"
    "v_cmp_ne_u32 vcc, 6, v19\n"
    "s_cbranch_vccnz R_38\n"
    "ds_read_b32 v8, v4 offset:53760\n"
    "ds_read_b32 v9, v4 offset:53764\n"
    "ds_read_b32 v10, v4 offset:53768\n"
    "ds_read_b32 v12, v4 offset:54912\n"
    "ds_read_b32 v13, v4 offset:54916\n"
    "ds_read_b32 v14, v4 offset:54920\n"
    "ds_read_b128 v[40:43], v6 offset:0\n"
    "ds_read_b128 v[44:47], v6 offset:16\n"
    "ds_read_b128 v[48:51], v6 offset:32\n"
    "ds_read_b128 v[52:55], v6 offset:48\n"
    "ds_read_b128 v[56:59], v6 offset:64\n"
    "ds_read_b128 v[60:63], v6 offset:80\n"
    "ds_read2_b32 v[36:37], v5 offset1:1\n"
    "ds_read_b32 v38, v5 offset:8\n"
    "s_waitcnt lgkmcnt(8)\n"
    "v_lshlrev_b32 v24, 16, v8\n"
    "v_lshlrev_b32 v25, 16, v12\n"
    "v_and_b32 v26, 0xffff0000, v8\n"
    "v_and_b32 v27, 0xffff0000, v12\n"
    "v_lshlrev_b32 v28, 16, v9\n"
    "v_lshlrev_b32 v29, 16, v13\n"
    "v_and_b32 v30, 0xffff0000, v9\n"
    "v_and_b32 v31, 0xffff0000, v13\n"
    "v_lshlrev_b32 v32, 16, v10\n"
    "v_lshlrev_b32 v33, 16, v14\n"
    "v_and_b32 v34, 0xffff0000, v10\n"
    "v_and_b32 v35, 0xffff0000, v14\n"
    "s_branch E_41\n"
    "R_40:\n"
    "s_sleep 1\n"
    "s_sub_u32 s39, s39, 1\n"
    "s_cbranch_scc1 ABORT\n"
    "E_41:\n"
    "ds_read_b32 v19, v7 offset:60\n"
    "s_waitcnt lgkmcnt(0)\n"
    "v_cmp_gt_u32 vcc, s44, v19\n"
    "s_cbranch_vccnz R_40\n"
    "s_cmpk_lt_u32 s57, 96\n"
    "s_cbranch_scc1 LN2g\n"
    "s_sleep 11\n"
    "LN2g:\n"
    "buffer_load_dwordx4 v[72:75], v1, s[20:23], s42 offen sc1\n"
    "buffer_load_dwordx4 v[76:79], v1, s[20:23], s43 offen sc1\n"
    "s_branch E_43\n"
    "R_42:\n"
    "s_sub_u32 s39, s39, 1\n"
    "s_cbranch_scc1 ABORT\n"
    "s_wqm_b64 exec, vcc\n"
    "buffer_load_dwordx4 v[72:75], v1, s[20:23], s42 offen sc1\n"
    "buffer_load_dwordx4 v[76:79], v1, s[20:23], s43 offen sc1\n"
    "s_mov_b64 exec, -1\n"
    "E_43:\n"
    "s_waitcnt vmcnt(0)\n"
    "v_bitop3_b32 v19, v75, v79, s36 bitop3:0x7e\n"
    "v_cmp_ne_u32 vcc, 0, v19\n"
    "s_cbranch_vccnz R_42\n"
    "v_lshlrev_b32 v20, 16, v72\n"
    "v_and_b32 v21, 0xffff0000, v72\n"
    "v_pk_add_f32 v[24:25], v[24:25], v[20:21]\n"
    "v_cvt_pk_bf16_f32 v24, 0, v24\n"
    "v_cvt_pk_bf16_f32 v25, 0, v25\n"
    "v_pk_mul_f32 v[10:11], v[24:25], v[24:25]\n"
    "v_pk_mul_f32 v[12:13], v[24:25], v[42:43] op_sel_hi:[1,0]\n"
    "v_lshlrev_b32 v20, 16, v73\n"
    "v_and_b32 v21, 0xffff0000, v73\n"
    "v_pk_add_f32 v[26:27], v[26:27], v[20:21]\n"
    "v_cvt_pk_bf16_f32 v26, 0, v26\n"
    "v_cvt_pk_bf16_f32 v27, 0, v27\n"
    "v_pk_add_f32 v[8:9], v[26:27], v[24:25]\n"
    "v_pk_fma_f32 v[10:11], v[26:27], v[26:27], v[10:11]\n"
    "v_pk_fma_f32 v[12:13], v[26:27], v[46:47], v[12:13] op_sel:[0,0,0] op_sel_hi:[1,0,1]\n"
    "v_lshlrev_b32 v20, 16, v74\n"
    "v_and_b32 v21, 0xffff0000, v74\n"
    "v_pk_add_f32 v[28:29], v[28:29], v[20:21]\n"
    "v_cvt_pk_bf16_f32 v28, 0, v28\n"
    "v_cvt_pk_bf16_f32 v29, 0, v29\n"
    "v_pk_add_f32 v[8:9], v[28:29], v[8:9]\n"
    "v_pk_fma_f32 v[10:11], v[28:29], v[28:29], v[10:11]\n"
    "v_pk_fma_f32 v[12:13], v[28:29], v[50:51], v[12:13] op_sel:[0,0,0] op_sel_hi:[1,0,1]\n"
    "v_lshlrev_b32 v20, 16, v76\n"
    "v_and_b32 v21, 0xffff0000, v76\n"
    "v_pk_add_f32 v[30:31], v[30:31], v[20:21]\n"
    "v_cvt_pk_bf16_f32 v30, 0, v30\n"
    "v_cvt_pk_bf16_f32 v31, 0, v31\n"
    "v_pk_add_f32 v[8:9], v[30:31], v[8:9]\n"
    "v_pk_fma_f32 v[10:11], v[30:31], v[30:31], v[10:11]\n"
    "v_pk_fma_f32 v[12:13], v[30:31], v[54:55], v[12:13] op_sel:[0,0,0] op_sel_hi:[1,0,1]\n"
    "v_lshlrev_b32 v20, 16, v77\n"
    "v_and_b32 v21, 0xffff0000, v77\n"
    "v_pk_add_f32 v[32:33], v[32:33], v[20:21]\n"
    "v_cvt_pk_bf16_f32 v32, 0, v32\n"
    "v_cvt_pk_bf16_f32 v33, 0, v33\n"
    "v_pk_add_f32 v[8:9], v[32:33], v[8:9]\n"
    "v_pk_fma_f32 v[10:11], v[32:33], v[32:33], v[10:11]\n"
    "v_pk_fma_f32 v[12:13], v[32:33], v[58:59], v[12:13] op_sel:[0,0,0] op_sel_hi:[1,0,1]\n"
    "v_lshlrev_b32 v20, 16, v78\n"
    "v_and_b32 v21, 0xffff0000, v78\n"
    "v_pk_add_f32 v[34:35], v[34:35], v[20:21]\n"
    "v_cvt_pk_bf16_f32 v34, 0, v34\n"
    "v_cvt_pk_bf16_f32 v35, 0, v35\n"
    "v_pk_add_f32 v[8:9], v[34:35], v[8:9]\n"
    "v_pk_fma_f32 v[10:11], v[34:35], v[34:35], v[10:11]\n"
    "v_pk_fma_f32 v[12:13], v[34:35], v[62:63], v[12:13] op_sel:[0,0,0] op_sel_hi:[1,0,1]\n"
    "v_permlane32_swap_b32 v8, v9\n"
    "v_permlane32_swap_b32 v10, v11\n"
    "v_permlane32_swap_b32 v12, v13\n"
    "v_add_f32 v8, v8, v9\n"
    "v_add_f32 v10, v10, v11\n"
    "v_add_f32 v12, v12, v13\n"
    "s_nop 0\n"
    "v_permlane16_swap_b32 v8, v10\n"
    "v_add_f32 v8, v8, v10\n"
    "s_nop 1\n"
    "v_add_f32_dpp v9, v8, v8 row_ror:8 row_mask:0xf bank_mask:0x3\n"
    "s_nop 1\n"
    "v_add_f32_dpp v9, v12, v12 row_ror:8 row_mask:0xf bank_mask:0xc\n"
    "ds_write_b32 v16, v9\n"
    "s_waitcnt lgkmcnt(0)\n"
    "s_barrier\n"
    "ds_read_b128 v[8:11], v17\n"
    "ds_read_b128 v[12:15], v17 offset:1024\n"
    "s_waitcnt lgkmcnt(0)\n"
    "v_pk_add_f32 v[8:9], v[8:9], v[12:13]\n"
    "v_pk_add_f32 v[10:11], v[10:11], v[14:15]\n"
    "s_nop 1\n"
    "v_permlane32_swap_b32 v8, v10\n"
    "v_permlane32_swap_b32 v9, v11\n"
    "v_pk_add_f32 v[8:9], v[8:9], v[10:11]\n"
    "s_nop 1\n"
    "v_add_f32_dpp v8, v8, v8 row_ror:8 row_mask:0xf bank_mask:0xf\n"
    "v_add_f32_dpp v9, v9, v9 row_ror:8 row_mask:0xf bank_mask:0xf\n"
    "v_mov_b32 v10, v8\n"
    "v_mov_b32 v11, v9\n"
    "s_nop 0\n"
    "v_permlane16_swap_b32 v8, v10\n"
    "v_permlane16_swap_b32 v9, v11\n"
    "v_pk_add_f32 v[8:9], v[8:9], v[10:11]\n"
    "v_mul_f32 v8, 0x3ae38e39, v8\n"
    "v_mul_f32 v9, 0x3ae38e39, v9\n"
    "v_fma_f32 v9, -v8, v8, v9\n"
    "v_max_f32 v9, 0, v9\n"
    "v_cvt_pk_bf16_f32 v9, 0, v9\n"
    "v_add_f32 v9, 0x3727c5ac, v9\n"
    "v_cvt_pk_bf16_f32 v9, 0, v9\n"
    "v_sqrt_f32 v9, v9\n"
    "s_nop 0\n"
    "v_cvt_pk_bf16_f32 v8, v8, v9\n"
    "v_mov_b32 v9, v8\n"
    "s_nop 1\n"
    "v_permlane32_swap_b32 v8, v9\n"
    "v_and_b32 v12, 0xffff0000, v8\n"
    "v_and_b32 v13, 0xffff0000, v9\n"
    "v_lshlrev_b32 v10, 16, v8\n"
    "v_lshlrev_b32 v11, 16, v9\n"
    "v_rcp_f32 v12, v12\n"
    "v_rcp_f32 v13, v13\n"
    "v_pk_add_f32 v[80:81], v[24:25], v[10:11] neg_lo:[0,1] neg_hi:[0,1]\n"
    "v_pk_add_f32 v[82:83], v[26:27], v[10:11] neg_lo:[0,1] neg_hi:[0,1]\n"
    "v_cvt_pk_bf16_f32 v80, 0, v80\n"
    "v_cvt_pk_bf16_f32 v81, 0, v81\n"
    "v_cvt_pk_bf16_f32 v82, 0, v82\n"
    "v_cvt_pk_bf16_f32 v83, 0, v83\n"
    "v_pk_mul_f32 v[80:81], v[80:81], v[12:13]\n"
    "v_pk_mul_f32 v[82:83], v[82:83], v[12:13]\n"
    "v_cvt_pk_bf16_f32 v80, 0, v80\n"
    "v_cvt_pk_bf16_f32 v81, 0, v81\n"
    "v_cvt_pk_bf16_f32 v82, 0, v82\n"
    "v_cvt_pk_bf16_f32 v83, 0, v83\n"
    "v_pk_mul_f32 v[80:81], v[80:81], v[40:41] op_sel_hi:[1,0]\n"
    "v_pk_mul_f32 v[82:83], v[82:83], v[44:45] op_sel_hi:[1,0]\n"
    "v_cvt_pk_bf16_f32 v80, 0, v80\n"
    "v_cvt_pk_bf16_f32 v81, 0, v81\n"
    "v_cvt_pk_bf16_f32 v82, 0, v82\n"
    "v_cvt_pk_bf16_f32 v83, 0, v83\n"
    "v_pk_add_f32 v[80:81], v[40:41], v[80:81] op_sel:[1,0] op_sel_hi:[1,1]\n"
    "v_pk_add_f32 v[82:83], v[44:45], v[82:83] op_sel:[1,0] op_sel_hi:[1,1]\n"
    "v_cvt_pk_bf16_f32 v72, v80, v82\n"
    "v_cvt_pk_bf16_f32 v76, v81, v83\n"
    "ds_bpermute_b32 v84, v23, v72\n"
    "ds_bpermute_b32 v88, v23, v76\n"
    "v_dot2_f32_bf16 v14, v72, v36, 0\n"
    "v_dot2_f32_bf16 v15, v76, v36, 0\n"
    "v_pk_add_f32 v[80:81], v[28:29], v[10:11] neg_lo:[0,1] neg_hi:[0,1]\n"
    "v_pk_add_f32 v[82:83], v[30:31], v[10:11] neg_lo:[0,1] neg_hi:[0,1]\n"
    "v_cvt_pk_bf16_f32 v80, 0, v80\n"
    "v_cvt_pk_bf16_f32 v81, 0, v81\n"
    "v_cvt_pk_bf16_f32 v82, 0, v82\n"
    "v_cvt_pk_bf16_f32 v83, 0, v83\n"
    "v_pk_mul_f32 v[80:81], v[80:81], v[12:13]\n"
    "v_pk_mul_f32 v[82:83], v[82:83], v[12:13]\n"
    "v_cvt_pk_bf16_f32 v80, 0, v80\n"
    "v_cvt_pk_bf16_f32 v81, 0, v81\n"
    "v_cvt_pk_bf16_f32 v82, 0, v82\n"
    "v_cvt_pk_bf16_f32 v83, 0, v83\n"
    "v_pk_mul_f32 v[80:81], v[80:81], v[48:49] op_sel_hi:[1,0]\n"
    "v_pk_mul_f32 v[82:83], v[82:83], v[52:53] op_sel_hi:[1,0]\n"
    "v_cvt_pk_bf16_f32 v80, 0, v80\n"
    "v_cvt_pk_bf16_f32 v81, 0, v81\n"
    "v_cvt_pk_bf16_f32 v82, 0, v82\n"
    "v_cvt_pk_bf16_f32 v83, 0, v83\n"
    "v_pk_add_f32 v[80:81], v[48:49], v[80:81] op_sel:[1,0] op_sel_hi:[1,1]\n"
    "v_pk_add_f32 v[82:83], v[52:53], v[82:83] op_sel:[1,0] op_sel_hi:[1,1]\n"
    "v_cvt_pk_bf16_f32 v73, v80, v82\n"
    "v_cvt_pk_bf16_f32 v77, v81, v83\n"
    "ds_bpermute_b32 v85, v23, v73\n"
    "ds_bpermute_b32 v89, v23, v77\n"
    "v_dot2_f32_bf16 v14, v73, v37, v14\n"
    "v_dot2_f32_bf16 v15, v77, v37, v15\n"
    "v_pk_add_f32 v[80:81], v[32:33], v[10:11] neg_lo:[0,1] neg_hi:[0,1]\n"
    "v_pk_add_f32 v[82:83], v[34:35], v[10:11] neg_lo:[0,1] neg_hi:[0,1]\n"
    "v_cvt_pk_bf16_f32 v80, 0, v80\n"
    "v_cvt_pk_bf16_f32 v81, 0, v81\n"
    "v_cvt_pk_bf16_f32 v82, 0, v82\n"
    "v_cvt_pk_bf16_f32 v83, 0, v83\n"
    "v_pk_mul_f32 v[80:81], v[80:81], v[12:13]\n"
    "v_pk_mul_f32 v[82:83], v[82:83], v[12:13]\n"
    "v_cvt_pk_bf16_f32 v80, 0, v80\n"
    "v_cvt_pk_bf16_f32 v81, 0, v81\n"
    "v_cvt_pk_bf16_f32 v82, 0, v82\n"
    "v_cvt_pk_bf16_f32 v83, 0, v83\n"
    "v_pk_mul_f32 v[80:81], v[80:81], v[56:57] op_sel_hi:[1,0]\n"
    "v_pk_mul_f32 v[82:83], v[82:83], v[60:61] op_sel_hi:[1,0]\n"
    "v_cvt_pk_bf16_f32 v80, 0, v80\n"
    "v_cvt_pk_bf16_f32 v81, 0, v81\n"
    "v_cvt_pk_bf16_f32 v82, 0, v82\n"
    "v_cvt_pk_bf16_f32 v83, 0, v83\n"
    "v_pk_add_f32 v[80:81], v[56:57], v[80:81] op_sel:[1,0] op_sel_hi:[1,1]\n"
    "v_pk_add_f32 v[82:83], v[60:61], v[82:83] op_sel:[1,0] op_sel_hi:[1,1]\n"
    "v_cvt_pk_bf16_f32 v74, v80, v82\n"
    "v_cvt_pk_bf16_f32 v78, v81, v83\n"
    "ds_bpermute_b32 v86, v23, v74\n"
    "ds_bpermute_b32 v90, v23, v78\n"
    "v_dot2_f32_bf16 v14, v74, v38, v14\n"
    "v_dot2_f32_bf16 v15, v78, v38, v15\n"
    "s_nop 2\n"
    "v_permlane32_swap_b32 v14, v15\n"
    "v_add_f32 v14, v14, v15\n"
    "s_nop 1\n"
    "v_add_f32_dpp v14, v14, v14 row_ror:8 row_mask:0xf bank_mask:0xf\n"
    "v_mov_b32 v15, v14\n"
    "s_nop 1\n"
    "v_permlane16_swap_b32 v14, v15\n"
    "v_add_f32 v14, v14, v15\n"
    "ds_write_b32 v18, v14\n"
    "s_waitcnt lgkmcnt(1)\n"
    "buffer_store_dwordx4 v[84:87], v22, s[20:23], s45 offen sc1\n"
    "buffer_store_dwordx4 v[88:91], v22, s[20:23], s45 offen offset:1536 sc1\n"
    "s_waitcnt lgkmcnt(0)\n"
    "s_barrier\n"
    "v_lshlrev_b32 v80, 16, v72\n"
    "v_lshlrev_b32 v81, 16, v76\n"
    "v_and_b32 v82, 0xffff0000, v72\n"
    "v_and_b32 v83, 0xffff0000, v76\n"
    "v_pk_mul_f32 v[80:81], v[42:43], v[80:81] op_sel:[1,0] op_sel_hi:[1,1]\n"
    "v_pk_mul_f32 v[82:83], v[46:47], v[82:83] op_sel:[1,0] op_sel_hi:[1,1]\n"
    "v_cvt_pk_bf16_f32 v80, 0, v80\n"
    "v_cvt_pk_bf16_f32 v81, 0, v81\n"
    "v_cvt_pk_bf16_f32 v82, 0, v82\n"
    "v_cvt_pk_bf16_f32 v83, 0, v83\n"
    "v_pk_add_f32 v[80:81], v[80:81], v[24:25]\n"
    "v_pk_add_f32 v[82:83], v[82:83], v[26:27]\n"
    "v_cvt_pk_bf16_f32 v8, v80, v82\n"
    "v_cvt_pk_bf16_f32 v12, v81, v83\n"
    "ds_write_b32 v4, v8 offset:18560\n"
    "ds_write_b32 v4, v12 offset:19712\n"
    "v_lshlrev_b32 v80, 16, v73\n"
    "v_lshlrev_b32 v81, 16, v77\n"
    "v_and_b32 v82, 0xffff0000, v73\n"
    "v_and_b32 v83, 0xffff0000, v77\n"
    "v_pk_mul_f32 v[80:81], v[50:51], v[80:81] op_sel:[1,0] op_sel_hi:[1,1]\n"
    "v_pk_mul_f32 v[82:83], v[54:55], v[82:83] op_sel:[1,0] op_sel_hi:[1,1]\n"
    "v_cvt_pk_bf16_f32 v80, 0, v80\n"
    "v_cvt_pk_bf16_f32 v81, 0, v81\n"
    "v_cvt_pk_bf16_f32 v82, 0, v82\n"
    "v_cvt_pk_bf16_f32 v83, 0, v83\n"
    "v_pk_add_f32 v[80:81], v[80:81], v[28:29]\n"
    "v_pk_add_f32 v[82:83], v[82:83], v[30:31]\n"
    "v_cvt_pk_bf16_f32 v9, v80, v82\n"
    "v_cvt_pk_bf16_f32 v13, v81, v83\n"
    "ds_write_b32 v4, v9 offset:18564\n"
    "ds_write_b32 v4, v13 offset:19716\n"
    "v_lshlrev_b32 v80, 16, v74\n"
    "v_lshlrev_b32 v81, 16, v78\n"
    "v_and_b32 v82, 0xffff0000, v74\n"
    "v_and_b32 v83, 0xffff0000, v78\n"
    "v_pk_mul_f32 v[80:81], v[58:59], v[80:81] op_sel:[1,0] op_sel_hi:[1,1]\n"
    "v_pk_mul_f32 v[82:83], v[62:63], v[82:83] op_sel:[1,0] op_sel_hi:[1,1]\n"
    "v_cvt_pk_bf16_f32 v80, 0, v80\n"
    "v_cvt_pk_bf16_f32 v81, 0, v81\n"
    "v_cvt_pk_bf16_f32 v82, 0, v82\n"
    "v_cvt_pk_bf16_f32 v83, 0, v83\n"
    "v_pk_add_f32 v[80:81], v[80:81], v[32:33]\n"
    "v_pk_add_f32 v[82:83], v[82:83], v[34:35]\n"
    "v_cvt_pk_bf16_f32 v10, v80, v82\n"
    "v_cvt_pk_bf16_f32 v14, v81, v83\n"
    "ds_write_b32 v4, v10 offset:18568\n"
    "ds_write_b32 v4, v14 offset:19720\n"
    "s_waitcnt lgkmcnt(0)\n"
    "s_barrier\n"
    "s_branch END\n"
    "LN2K:\n"
    "s_setprio 0\n"
    "s_cmpk_ge_u32 s2, 128\n"
    "s_cbranch_scc1 CDONE\n"
    "s_and_b32 s57, s2, 7\n"
    "s_lshl_b32 s57, s57, 4\n"
    "s_lshr_b32 s43, s2, 3\n"
    "s_add_u32 s57, s57, s43\n"
    "v_and_b32 v1, 63, v0\n"
    "s_cmpk_lg_u32 s37, 12\n"
    "s_cbranch_scc1 LN2C\n"
    "s_setprio 3\n"
    "v_lshlrev_b32 v17, 4, v1\n"
    "v_add_u32 v17, 132512, v17\n"
    "v_and_b32 v2, 7, v0\n"
    "v_lshrrev_b32 v3, 5, v1\n"
    "v_lshl_add_u32 v2, v2, 1, v3\n"
    "v_lshlrev_b32 v3, 3, v2\n"
    "s_lshl_b32 s42, s57, 4\n"
    "s_sub_u32 s42, 2047, s42\n"
    "v_sub_u32 v2, s42, v2\n"
    "v_and_b32 v4, 15, v0\n"
    "v_sub_u32 v5, s42, v4\n"
    "v_lshlrev_b32 v6, 3, v4\n"
    "v_mul_u32_u24 v4, 48, v4\n"
    "v_add_u32 v4, 136608, v4\n"
    "s_lshl_b32 s42, s57, 7\n"
    "s_add_u32 s43, s42, 4096\n"
    "s_add_u32 s42, s42, 77824\n"
    "s_brev_b32 s46, 1\n"
    "s_movk_i32 s47, 0x7ff\n"
    "v_mov_b32 v21, s36\n"
    "v_mov_b32 v23, s36\n"
    "v_min_u32 v7, 12, v1\n"
    "v_lshlrev_b32 v7, 3, v7\n"
    "v_add_u32 v7, 148736, v7\n"
    "v_mov_b32 v32, 0\n"
    "v_mov_b32 v33, 0\n"
    "s_mov_b64 exec, 0x1000\n"
    "ds_write_b64 v7, v[32:33]\n"
    "s_mov_b64 exec, -1\n"
    "s_waitcnt lgkmcnt(0)\n"
    "s_barrier\n"
    "ds_read_b128 v[8:11], v17\n"
    "ds_read_b128 v[12:15], v17 offset:1024\n"
    "ds_read_b128 v[24:27], v17 offset:2048\n"
    "ds_read_b128 v[28:31], v17 offset:3072\n"
    "ds_read_b64 v[32:33], v7\n"
    "s_waitcnt lgkmcnt(3)\n"
    "v_pk_add_f32 v[8:9], v[8:9], v[12:13]\n"
    "v_pk_add_f32 v[10:11], v[10:11], v[14:15]\n"
    "s_nop 1\n"
    "v_permlane32_swap_b32 v8, v10\n"
    "v_permlane32_swap_b32 v9, v11\n"
    "v_pk_add_f32 v[8:9], v[8:9], v[10:11]\n"
    "s_nop 1\n"
    "v_add_f32_dpp v8, v8, v8 row_ror:8 row_mask:0xf bank_mask:0xf\n"
    "v_add_f32_dpp v9, v9, v9 row_ror:8 row_mask:0xf bank_mask:0xf\n"
    "v_mov_b32 v10, v8\n"
    "v_mov_b32 v11, v9\n"
    "s_nop 0\n"
    "v_permlane16_swap_b32 v8, v10\n"
    "v_permlane16_swap_b32 v9, v11\n"
    "v_pk_add_f32 v[8:9], v[8:9], v[10:11]\n"
    "s_waitcnt lgkmcnt(0)\n"
    "v_pk_add_f32 v[24:25], v[24:25], v[28:29]\n"
    "v_pk_add_f32 v[26:27], v[26:27], v[30:31]\n"
    "v_add_f32_dpp v32, v32, v32 row_ror:8 row_mask:0xf bank_mask:0xf\n"
    "v_add_f32 v24, v24, v25\n"
    "v_add_f32 v26, v26, v27\n"
    "v_add_f32_dpp v32, v32, v32 row_ror:4 row_mask:0xf bank_mask:0xf\n"
    "v_mul_f32 v8, 0x3ae38e39, v8\n"
    "v_permlane32_swap_b32 v24, v26\n"
    "v_add_f32_dpp v32, v32, v32 row_ror:2 row_mask:0xf bank_mask:0xf\n"
    "v_add_f32 v24, v24, v26\n"
    "v_mul_f32 v9, 0x3ae38e39, v9\n"
    "v_add_f32_dpp v32, v32, v32 row_ror:1 row_mask:0xf bank_mask:0xf\n"
    "v_add_f32_dpp v24, v24, v24 row_ror:8 row_mask:0xf bank_mask:0xf\n"
    "v_fma_f32 v9, -v8, v8, v9\n"
    "v_mov_b32 v25, v24\n"
    "v_max_f32 v9, 0, v9\n"
    "v_cvt_pk_bf16_f32 v9, 0, v9\n"
    "v_permlane16_swap_b32 v24, v25\n"
    "v_add_f32 v9, 0x3727c5ac, v9\n"
    "v_readfirstlane_b32 s44, v32\n"
    "v_cvt_pk_bf16_f32 v9, 0, v9\n"
    "v_sqrt_f32 v9, v9\n"
    "v_add_f32 v24, v24, v25\n"
    "v_cvt_pk_bf16_f32 v8, v8, v9\n"
    "v_and_b32 v9, 0xffff0000, v8\n"
    "v_lshlrev_b32 v8, 16, v8\n"
    "v_rcp_f32 v9, v9\n"
    "v_fma_f32 v24, -v8, s44, v24\n"
    "v_mul_f32 v24, v24, v9\n"
    "v_and_b32 v9, 0x7ff, v24\n"
    "v_add_u32 v9, 0x7ff, v9\n"
    "v_or_b32 v24, v24, v9\n"
    "v_ashrrev_i32 v9, 31, v24\n"
    "v_bitop3_b32 v24, v9, v24, s46 bitop3:0x36\n"
    "v_bfi_b32 v20, s47, v2, v24\n"
    "v_mov_b32 v22, v20\n"
    "s_nop 1\n"
    "v_permlane32_swap_b32 v20, v22\n"
    "s_mov_b64 exec, 0xff\n"
    "buffer_store_dwordx4 v[20:23], v3, s[20:23], s42 offen sc1\n"
    "s_cmpk_lg_u32 s57, 0\n"
    "s_cbranch_scc1 LN2K2\n"
    "s_mov_b64 exec, -1\n"
    "v_add_f32_dpp v33, v33, v33 row_ror:8 row_mask:0xf bank_mask:0xf\n"
    "s_nop 1\n"
    "v_add_f32_dpp v33, v33, v33 row_ror:4 row_mask:0xf bank_mask:0xf\n"
    "s_nop 1\n"
    "v_add_f32_dpp v33, v33, v33 row_ror:2 row_mask:0xf bank_mask:0xf\n"
    "s_nop 1\n"
    "v_add_f32_dpp v33, v33, v33 row_ror:1 row_mask:0xf bank_mask:0xf\n"
    "s_mov_b64 exec, 1\n"
    "v_sqrt_f32 v34, v33\n"
    "v_mov_b32 v35, s36\n"
    "v_mul_f32 v34, 0x3c1374bc, v34\n"
    "buffer_store_dwordx2 v[34:35], off, s[20:23], 0 offset:2048 sc1\n"
    "LN2K2:\n"
    "s_mov_b64 exec, 0xffff\n"
    "s_waitcnt lgkmcnt(0)\n"
    "s_barrier\n"
    "ds_read_b128 v[8:11], v4\n"
    "ds_read_b128 v[12:15], v4 offset:16\n"
    "ds_read_b128 v[24:27], v4 offset:32\n"
    "s_waitcnt lgkmcnt(1)\n"
    "v_pk_add_f32 v[8:9], v[8:9], v[12:13]\n"
    "v_pk_add_f32 v[10:11], v[10:11], v[14:15]\n"
    "s_waitcnt lgkmcnt(0)\n"
    "v_pk_add_f32 v[8:9], v[8:9], v[24:25]\n"
    "v_pk_add_f32 v[10:11], v[10:11], v[26:27]\n"
    "v_pk_add_f32 v[8:9], v[8:9], v[10:11]\n"
    "v_add_f32 v8, v8, v9\n"
    "v_and_b32 v9, 0x7ff, v8\n"
    "v_add_u32 v9, 0x7ff, v9\n"
    "v_or_b32 v8, v8, v9\n"
    "v_ashrrev_i32 v9, 31, v8\n"
    "v_bitop3_b32 v8, v9, v8, s46 bitop3:0x36\n"
    "v_bfi_b32 v20, s47, v5, v8\n"
    "buffer_store_dwordx2 v[20:21], v6, s[20:23], s43 offen sc1\n"
    "s_mov_b64 exec, -1\n"
    "s_barrier\n"
    "s_branch END\n"
    "LN2C:\n"
    "s_setprio 2\n"
    "v_subrev_u32 v1, 832, v0\n"
    "v_add_u32 v2, 0, v1\n"
    "v_mul_u32_u24 v3, 7282, v2\n"
    "v_lshrrev_b32 v3, 20, v3\n"
    "v_add_lshl_u32 v8, v2, v3, 4\n"
    "v_add_u32 v2, 192, v1\n"
    "v_mul_u32_u24 v3, 7282, v2\n"
    "v_lshrrev_b32 v3, 20, v3\n"
    "v_add_lshl_u32 v9, v2, v3, 4\n"
    "v_add_u32 v2, 384, v1\n"
    "v_mul_u32_u24 v3, 7282, v2\n"
    "v_lshrrev_b32 v3, 20, v3\n"
    "v_add_lshl_u32 v10, v2, v3, 4\n"
    "v_add_u32 v2, 576, v1\n"
    "v_mul_u32_u24 v3, 7282, v2\n"
    "v_lshrrev_b32 v3, 20, v3\n"
    "v_add_lshl_u32 v11, v2, v3, 4\n"
    "v_add_u32 v2, 768, v1\n"
    "v_mul_u32_u24 v3, 7282, v2\n"
    "v_lshrrev_b32 v3, 20, v3\n"
    "v_add_lshl_u32 v12, v2, v3, 4\n"
    "v_add_u32 v2, 960, v1\n"
    "v_mul_u32_u24 v3, 7282, v2\n"
    "v_lshrrev_b32 v3, 20, v3\n"
    "v_add_lshl_u32 v13, v2, v3, 4\n"
    "s_mul_i32 s42, s57, 18432\n"
    "v_lshl_add_u32 v1, v1, 4, s42\n"
    "v_add_u32 v2, 6144, v1\n"
    "v_add_u32 v3, 12288, v1\n"
    "s_mov_b32 s42, 7341056\n"
    "s_lshl_b32 s43, s57, 2\n"
    "v_mov_b32 v4, s36\n"
    "s_barrier\n"
    "s_barrier\n"
    "s_barrier\n"
    "s_sleep 12\n"
    "ds_read_b128 v[40:43], v8 offset:18560\n"
    "ds_read_b128 v[44:47], v9 offset:18560\n"
    "ds_read_b128 v[48:51], v10 offset:18560\n"
    "ds_read_b128 v[52:55], v11 offset:18560\n"
    "ds_read_b128 v[56:59], v12 offset:18560\n"
    "ds_read_b128 v[60:63], v13 offset:18560\n"
    "s_waitcnt lgkmcnt(5)\n"
    "global_store_dwordx4 v1, v[40:43], s[34:35] offset:0 sc1\n"
    "s_waitcnt lgkmcnt(4)\n"
    "global_store_dwordx4 v1, v[44:47], s[34:35] offset:3072 sc1\n"
    "s_waitcnt lgkmcnt(3)\n"
    "global_store_dwordx4 v2, v[48:51], s[34:35] offset:0 sc1\n"
    "s_waitcnt lgkmcnt(2)\n"
    "global_store_dwordx4 v2, v[52:55], s[34:35] offset:3072 sc1\n"
    "s_waitcnt lgkmcnt(1)\n"
    "global_store_dwordx4 v3, v[56:59], s[34:35] offset:0 sc1\n"
    "s_waitcnt lgkmcnt(0)\n"
    "global_store_dwordx4 v3, v[60:63], s[34:35] offset:3072 sc1\n"
    "s_waitcnt vmcnt(0)\n"
    "s_barrier\n"
    "s_cmpk_lg_u32 s37, 15\n"
    "s_cbranch_scc1 END\n"
    "s_mov_b64 exec, 1\n"
    "buffer_store_dword v4, off, s[20:23], s43 offset:1024 sc1\n"
    "s_mov_b64 exec, -1\n"
    "s_branch END\n"
    "PFZ:\n"
    "s_setprio 0\n"
    "s_cmpk_ge_u32 s2, 184\n"
    "s_cbranch_scc1 CDONE\n"
    "s_branch E_45\n"
    "R_44:\n"
    "s_sleep 2\n"
    "s_sub_u32 s39, s39, 1\n"
    "s_cbranch_scc1 ABORT\n"
    "E_45:\n"
    "ds_read_b32 v4, v11 offset:60\n"
    "s_waitcnt lgkmcnt(0)\n"
    "v_cmp_ne_u32 vcc, 16, v4\n"
    "s_cbranch_vccnz R_44\n"
    "v_subrev_u32 v7, 5184, v6\n"
    "v_min_u32 v6, v6, v7\n"
    "v_lshl_add_u32 v7, v6, 7, s60\n"
    "v_add_u32 v6, 1536, v6\n"
    "global_load_lds_dword v7, s[32:33] nt\n"
    "v_subrev_u32 v7, 5184, v6\n"
    "v_min_u32 v6, v6, v7\n"
    "v_lshl_add_u32 v7, v6, 7, s60\n"
    "v_add_u32 v6, 1536, v6\n"
    "global_load_lds_dword v7, s[32:33] nt\n"
    "CDONE:\n"
    "s_cmpk_lt_u32 s2, 184\n"
    "s_cbranch_scc1 END\n"
    "s_branch ES0\n"
    "SIL1:\n"
    "s_barrier\n"
    "v_add_u32 v1, 148736, v1\n"
    "v_mov_b32 v14, 147968\n"
    "v_mov_b32 v15, 1\n"
    "s_waitcnt vmcnt(0)\n"
    "v_lshlrev_b32 v5, 16, v4\n"
    "v_and_b32 v4, 0xffff0000, v4\n"
    "v_mul_f32 v6, 0xbfb8aa3b, v5\n"
    "v_mul_f32 v7, 0xbfb8aa3b, v4\n"
    "v_exp_f32 v6, v6\n"
    "v_exp_f32 v7, v7\n"
    "v_add_f32 v6, 1.0, v6\n"
    "v_add_f32 v7, 1.0, v7\n"
    "v_rcp_f32 v6, v6\n"
    "v_rcp_f32 v7, v7\n"
    "v_mul_f32 v5, v5, v6\n"
    "v_mul_f32 v4, v4, v7\n"
    "v_cvt_pk_bf16_f32 v4, v5, v4\n"
    "ds_write_b32 v1, v4\n"
    "s_mov_b64 exec, 1\n"
    "ds_add_u32 v14, v15 offset:4\n"
    "s_mov_b64 exec, -1\n"
    "s_branch M9\n"
    "M1:\n"
    "s_waitcnt lgkmcnt(0)\n"
    "s_memrealtime s[96:97]\n"
    "s_load_dword s36, s[20:21], 0\n"
    "s_barrier\n"
    "s_cmpk_ge_u32 s2, 246\n"
    "s_cbranch_scc1 M8\n"
    "s_sub_u32 s42, s2, 192\n"
    "s_lshr_b32 s42, s42, 3\n"
    "s_min_u32 s42, s42, 8\n"
    "s_lshr_b32 s44, s28, 7\n"
    "s_add_u32 s44, s44, s2\n"
    "s_sub_u32 s44, 3, s44\n"
    "s_and_b32 s44, s44, 7\n"
    "s_add_u32 s44, s44, 1\n"
    "s_add_u32 s44, s44, s42\n"
    "s_mul_i32 s45, s44, 57\n"
    "s_lshr_b32 s45, s45, 9\n"
    "s_mul_i32 s45, s45, 9\n"
    "s_sub_u32 s44, s44, s45\n"
    "s_and_b32 s58, s2, 7\n"
    "s_mul_i32 s58, s58, 9\n"
    "s_add_u32 s58, s58, s44\n"
    "v_bfe_u32 v12, v0, 3, 3\n"
    "v_bfe_u32 v13, v0, 5, 2\n"
    "v_xor_b32 v13, v13, v0\n"
    "v_mul_u32_u24 v12, 9216, v12\n"
    "v_and_b32 v13, 7, v13\n"
    "v_lshl_add_u32 v12, v13, 4, v12\n"
    "s_lshr_b32 s42, s37, 1\n"
    "s_mul_i32 s42, s42, 73728\n"
    "s_lshl_b32 s44, s58, 7\n"
    "s_add_u32 s42, s42, s44\n"
    "v_add_u32 v12, s42, v12\n"
    "v_xor_b32 v13, 64, v12\n"
    "v_add_u32 v13, 294912, v13\n"
    "s_bitcmp1_b32 s37, 0\n"
    "s_cselect_b64 s[56:57], s[30:31], s[28:29]\n"
    "s_sub_u32 s47, s37, 12\n"
    "s_sub_u32 s42, s2, 192\n"
    "s_lshr_b32 s43, s42, 1\n"
    "s_add_u32 s43, s43, 27\n"
    "s_and_b32 s44, s2, 1\n"
    "s_mul_i32 s45, s44, 1990656\n"
    "s_lshl_b32 s46, s43, 7\n"
    "s_add_u32 s45, s45, s46\n"
    "v_bfe_u32 v1, v0, 3, 5\n"
    "v_mul_u32_u24 v1, 0x1b00, v1\n"
    "v_lshrrev_b32 v2, 3, v0\n"
    "v_lshrrev_b32 v3, 4, v0\n"
    "v_and_b32 v2, 2, v2\n"
    "v_and_b32 v3, 4, v3\n"
    "v_xor_b32 v2, v2, v0\n"
    "v_xor_b32 v2, v2, v3\n"
    "v_and_b32 v2, 7, v2\n"
    "v_lshl_add_u32 v1, v2, 4, v1\n"
    "v_add_u32 v1, s45, v1\n"
    "s_lshl_b32 s45, s47, 10\n"
    "s_add_u32 m0, s45, 98304\n"
    "s_nop 0\n"
    "global_load_lds_dwordx4 v1, s[16:17] nt\n"
    "s_add_u32 m0, m0, 4096\n"
    "v_add_u32 v1, 221184, v1\n"
    "global_load_lds_dwordx4 v1, s[16:17] nt\n"
    "s_add_u32 m0, m0, 4096\n"
    "v_add_u32 v1, 221184, v1\n"
    "global_load_lds_dwordx4 v1, s[16:17] nt\n"
    "s_add_u32 m0, m0, 4096\n"
    "v_add_u32 v1, 221184, v1\n"
    "global_load_lds_dwordx4 v1, s[16:17] nt\n"
    "s_add_u32 m0, m0, 4096\n"
    "v_add_u32 v1, 221184, v1\n"
    "global_load_lds_dwordx4 v1, s[16:17] nt\n"
    "s_add_u32 m0, m0, 4096\n"
    "v_add_u32 v1, 221184, v1\n"
    "global_load_lds_dwordx4 v1, s[16:17] nt\n"
    "s_add_u32 m0, m0, 4096\n"
    "v_add_u32 v1, 221184, v1\n"
    "global_load_lds_dwordx4 v1, s[16:17] nt\n"
    "s_add_u32 m0, m0, 4096\n"
    "v_add_u32 v1, 221184, v1\n"
    "global_load_lds_dwordx4 v1, s[16:17] nt\n"
    "s_add_u32 m0, m0, 4096\n"
    "v_add_u32 v1, 221184, v1\n"
    "global_load_lds_dwordx4 v1, s[16:17] nt\n"
    "s_add_u32 m0, m0, 4096\n"
    "v_add_u32 v1, 221184, v1\n"
    "s_setprio 3\n"
    "s_barrier\n"
    "v_bfe_u32 v5, v0, 4, 2\n"
    "v_bfe_u32 v6, v0, 2, 2\n"
    "v_bfe_u32 v7, v0, 1, 1\n"
    "s_lshl_b32 s45, s47, 1\n"
    "v_or_b32 v7, s45, v7\n"
    "v_and_b32 v8, 2, v6\n"
    "v_xor_b32 v7, v7, v8\n"
    "v_lshlrev_b32 v8, 2, v5\n"
    "v_and_b32 v8, 4, v8\n"
    "v_xor_b32 v7, v7, v8\n"
    "v_lshlrev_b32 v2, 10, v5\n"
    "v_lshl_add_u32 v2, v6, 7, v2\n"
    "v_lshl_add_u32 v2, v7, 4, v2\n"
    "v_and_b32 v8, 1, v0\n"
    "v_lshl_add_u32 v2, v8, 3, v2\n"
    "v_add_u32 v2, 98304, v2\n"
    "s_mul_i32 s45, s44, 576\n"
    "s_add_u32 s45, s45, 148736\n"
    "v_lshl_add_u32 v3, v5, 4, s45\n"
    "s_mul_i32 s45, s44, 27648\n"
    "s_lshl_b32 s46, s43, 9\n"
    "s_add_u32 s45, s45, s46\n"
    "s_lshl_b32 s46, s47, 7\n"
    "s_add_u32 s45, s45, s46\n"
    "s_add_u32 s45, s45, 20480\n"
    "v_and_b32 v4, 15, v0\n"
    "v_lshlrev_b32 v4, 3, v4\n"
    "v_mov_b32 v14, 147968\n"
    "v_mov_b32 v15, 1\n"
    "s_waitcnt lgkmcnt(0)\n"
    "s_add_u32 s36, s36, 1\n"
    "s_waitcnt vmcnt(0)\n"
    "s_mov_b64 exec, 1\n"
    "ds_add_u32 v14, v15 offset:4\n"
    "s_mov_b64 exec, -1\n"
    "s_branch E_47\n"
    "R_46:\n"
    "s_sub_u32 s39, s39, 1\n"
    "s_cbranch_scc1 ABORT\n"
    "E_47:\n"
    "ds_read_b32 v5, v14 offset:4\n"
    "s_waitcnt lgkmcnt(0)\n"
    "v_cmp_ne_u32 vcc, 9, v5\n"
    "s_cbranch_vccnz R_46\n"
    "ds_read_b128 v[16:19], v3 offset:0\n"
    "ds_read_b64_tr_b16 v[52:53], v2 offset:0\n"
    "ds_read_b64_tr_b16 v[54:55], v2 offset:512\n"
    "ds_read_b128 v[20:23], v3 offset:64\n"
    "ds_read_b64_tr_b16 v[56:57], v2 offset:4096\n"
    "ds_read_b64_tr_b16 v[58:59], v2 offset:4608\n"
    "ds_read_b128 v[24:27], v3 offset:128\n"
    "ds_read_b64_tr_b16 v[60:61], v2 offset:8192\n"
    "ds_read_b64_tr_b16 v[62:63], v2 offset:8704\n"
    "ds_read_b128 v[28:31], v3 offset:192\n"
    "ds_read_b64_tr_b16 v[64:65], v2 offset:12288\n"
    "ds_read_b64_tr_b16 v[66:67], v2 offset:12800\n"
    "ds_read_b128 v[32:35], v3 offset:256\n"
    "ds_read_b64_tr_b16 v[68:69], v2 offset:16384\n"
    "ds_read_b64_tr_b16 v[70:71], v2 offset:16896\n"
    "s_waitcnt lgkmcnt(12)\n"
    "v_mfma_f32_16x16x32_bf16 v[8:11], v[16:19], v[52:55], 0\n"
    "ds_read_b128 v[36:39], v3 offset:320\n"
    "ds_read_b64_tr_b16 v[72:73], v2 offset:20480\n"
    "ds_read_b64_tr_b16 v[74:75], v2 offset:20992\n"
    "s_waitcnt lgkmcnt(12)\n"
    "v_mfma_f32_16x16x32_bf16 v[8:11], v[20:23], v[56:59], v[8:11]\n"
    "ds_read_b128 v[40:43], v3 offset:384\n"
    "ds_read_b64_tr_b16 v[76:77], v2 offset:24576\n"
    "ds_read_b64_tr_b16 v[78:79], v2 offset:25088\n"
    "s_waitcnt lgkmcnt(12)\n"
    "v_mfma_f32_16x16x32_bf16 v[8:11], v[24:27], v[60:63], v[8:11]\n"
    "ds_read_b128 v[44:47], v3 offset:448\n"
    "ds_read_b64_tr_b16 v[80:81], v2 offset:28672\n"
    "ds_read_b64_tr_b16 v[82:83], v2 offset:29184\n"
    "s_waitcnt lgkmcnt(12)\n"
    "v_mfma_f32_16x16x32_bf16 v[8:11], v[28:31], v[64:67], v[8:11]\n"
    "ds_read_b128 v[48:51], v3 offset:512\n"
    "ds_read_b64_tr_b16 v[84:85], v2 offset:32768\n"
    "ds_read_b64_tr_b16 v[86:87], v2 offset:33280\n"
    "s_waitcnt lgkmcnt(12)\n"
    "v_mfma_f32_16x16x32_bf16 v[8:11], v[32:35], v[68:71], v[8:11]\n"
    "s_waitcnt lgkmcnt(9)\n"
    "v_mfma_f32_16x16x32_bf16 v[8:11], v[36:39], v[72:75], v[8:11]\n"
    "s_waitcnt lgkmcnt(6)\n"
    "v_mfma_f32_16x16x32_bf16 v[8:11], v[40:43], v[76:79], v[8:11]\n"
    "s_waitcnt lgkmcnt(3)\n"
    "v_mfma_f32_16x16x32_bf16 v[8:11], v[44:47], v[80:83], v[8:11]\n"
    "s_waitcnt lgkmcnt(0)\n"
    "v_mfma_f32_16x16x32_bf16 v[8:11], v[48:51], v[84:87], v[8:11]\n"
    "s_mov_b64 exec, 0xffff\n"
    "s_nop 6\n"
    "v_mov_b32 v9, s36\n"
    "buffer_store_dwordx2 v[8:9], v4, s[20:23], s45 offen sc1\n"
    "s_mov_b64 exec, -1\n"
    "s_mov_b64 exec, 1\n"
    "ds_add_u32 v14, v15 offset:232\n"
    "s_mov_b64 exec, -1\n"
    "s_setprio 0\n"
    "s_branch MOED\n"
    "M8:\n"
    "v_mov_b32 v14, 147968\n"
    "v_mov_b32 v15, 1\n"
    "s_mov_b64 exec, 1\n"
    "ds_add_u32 v14, v15 offset:232\n"
    "s_mov_b64 exec, -1\n"
    "M9:\n"
    "s_waitcnt lgkmcnt(0)\n"
    "s_add_u32 s36, s36, 1\n"
    "ES1:\n"
    "ES0:\n"
    "s_sub_u32 s42, s2, 192\n"
    "s_lshr_b32 s42, s42, 3\n"
    "s_min_u32 s42, s42, 8\n"
    "s_lshr_b32 s44, s28, 7\n"
    "s_add_u32 s44, s44, s2\n"
    "s_sub_u32 s44, 3, s44\n"
    "s_and_b32 s44, s44, 7\n"
    "s_add_u32 s44, s44, 1\n"
    "s_add_u32 s44, s44, s42\n"
    "s_mul_i32 s45, s44, 57\n"
    "s_lshr_b32 s45, s45, 9\n"
    "s_mul_i32 s45, s45, 9\n"
    "s_sub_u32 s44, s44, s45\n"
    "s_and_b32 s58, s2, 7\n"
    "s_mul_i32 s58, s58, 9\n"
    "s_add_u32 s58, s58, s44\n"
    "v_bfe_u32 v12, v0, 3, 3\n"
    "v_bfe_u32 v13, v0, 5, 2\n"
    "v_xor_b32 v13, v13, v0\n"
    "v_mul_u32_u24 v12, 9216, v12\n"
    "v_and_b32 v13, 7, v13\n"
    "v_lshl_add_u32 v12, v13, 4, v12\n"
    "s_lshr_b32 s42, s37, 1\n"
    "s_mul_i32 s42, s42, 73728\n"
    "s_lshl_b32 s44, s58, 7\n"
    "s_add_u32 s42, s42, s44\n"
    "v_add_u32 v12, s42, v12\n"
    "v_xor_b32 v13, 64, v12\n"
    "v_add_u32 v13, 294912, v13\n"
    "s_bitcmp1_b32 s37, 0\n"
    "s_cselect_b64 s[56:57], s[30:31], s[28:29]\n"
    "s_waitcnt lgkmcnt(0)\n"
    "s_barrier\n"
    "s_cmpk_ge_u32 s37, 8\n"
    "s_cbranch_scc1 MOED\n"
    "ES1c:\n"
    "s_lshl_b32 m0, s37, 10\n"
    "ES1d:\n"
    "s_waitcnt vmcnt(4)\n"
    "global_load_lds_dwordx4 v12, s[56:57] nt\n"
    "s_add_u32 m0, m0, 8192\n"
    "v_add_u32 v12, 589824, v12\n"
    "s_waitcnt vmcnt(4)\n"
    "global_load_lds_dwordx4 v13, s[56:57] nt\n"
    "s_add_u32 m0, m0, 8192\n"
    "v_add_u32 v13, 589824, v13\n"
    "s_waitcnt vmcnt(4)\n"
    "global_load_lds_dwordx4 v12, s[56:57] nt\n"
    "s_add_u32 m0, m0, 8192\n"
    "v_add_u32 v12, 589824, v12\n"
    "s_waitcnt vmcnt(4)\n"
    "global_load_lds_dwordx4 v13, s[56:57] nt\n"
    "s_add_u32 m0, m0, 8192\n"
    "v_add_u32 v13, 589824, v13\n"
    "s_waitcnt vmcnt(4)\n"
    "global_load_lds_dwordx4 v12, s[56:57] nt\n"
    "s_add_u32 m0, m0, 8192\n"
    "v_add_u32 v12, 589824, v12\n"
    "s_waitcnt vmcnt(4)\n"
    "global_load_lds_dwordx4 v13, s[56:57] nt\n"
    "s_add_u32 m0, m0, 8192\n"
    "v_add_u32 v13, 589824, v13\n"
    "s_waitcnt vmcnt(4)\n"
    "global_load_lds_dwordx4 v12, s[56:57] nt\n"
    "s_add_u32 m0, m0, 8192\n"
    "v_add_u32 v12, 589824, v12\n"
    "s_waitcnt vmcnt(4)\n"
    "global_load_lds_dwordx4 v13, s[56:57] nt\n"
    "s_add_u32 m0, m0, 8192\n"
    "v_add_u32 v13, 589824, v13\n"
    "s_waitcnt vmcnt(4)\n"
    "global_load_lds_dwordx4 v12, s[56:57] nt\n"
    "s_add_u32 m0, m0, 8192\n"
    "v_add_u32 v12, 589824, v12\n"
    "s_waitcnt vmcnt(4)\n"
    "global_load_lds_dwordx4 v13, s[56:57] nt\n"
    "s_add_u32 m0, m0, 8192\n"
    "v_add_u32 v13, 589824, v13\n"
    "s_waitcnt vmcnt(4)\n"
    "global_load_lds_dwordx4 v12, s[56:57] nt\n"
    "s_add_u32 m0, m0, 8192\n"
    "v_add_u32 v12, 589824, v12\n"
    "s_waitcnt vmcnt(4)\n"
    "global_load_lds_dwordx4 v13, s[56:57] nt\n"
    "s_add_u32 m0, m0, 8192\n"
    "v_add_u32 v13, 589824, v13\n"
    "v_mov_b32 v14, 148096\n"
    "v_mov_b32 v15, 1\n"
    "v_bfe_u32 v3, v0, 4, 2\n"
    "s_add_u32 s63, s36, 1\n"
    "v_bfe_u32 v6, v0, 1, 1\n"
    "v_bfe_u32 v7, v0, 3, 1\n"
    "v_and_b32 v9, 1, v3\n"
    "v_lshl_add_u32 v8, v6, 1, v9\n"
    "v_lshl_add_u32 v8, v7, 2, v8\n"
    "v_xor_b32 v8, s37, v8\n"
    "v_lshlrev_b32 v4, 13, v7\n"
    "v_lshl_add_u32 v4, v6, 10, v4\n"
    "v_lshl_add_u32 v4, v9, 9, v4\n"
    "v_lshl_add_u32 v4, v8, 4, v4\n"
    "v_lshrrev_b32 v9, 1, v3\n"
    "v_lshl_add_u32 v4, v9, 11, v4\n"
    "v_bfe_u32 v9, v0, 2, 1\n"
    "v_lshl_add_u32 v4, v9, 7, v4\n"
    "v_and_b32 v9, 1, v0\n"
    "v_lshl_add_u32 v4, v9, 3, v4\n"
    "v_add_u32 v5, 0x10000, v4\n"
    "v_add_u32 v6, 0x20000, v4\n"
    "v_lshrrev_b32 v2, 3, v0\n"
    "v_and_b32 v8, 2, v2\n"
    "v_mul_u32_u24 v2, 1152, v2\n"
    "v_lshrrev_b32 v7, 4, v0\n"
    "v_and_b32 v7, 4, v7\n"
    "v_xor_b32 v8, v8, v7\n"
    "v_xor_b32 v8, v8, v0\n"
    "v_and_b32 v8, 7, v8\n"
    "v_lshl_add_u32 v2, v8, 4, v2\n"
    "s_mul_i32 s42, s58, 73728\n"
    "v_add_u32 v2, s42, v2\n"
    "s_cmpk_lt_u32 s2, 192\n"
    "s_cbranch_scc1 ES3\n"
    "s_branch E_49\n"
    "R_48:\n"
    "s_sleep 1\n"
    "s_sub_u32 s39, s39, 1\n"
    "s_cbranch_scc1 ABORT\n"
    "E_49:\n"
    "ds_read_b32 v10, v14 offset:104\n"
    "s_waitcnt lgkmcnt(0)\n"
    "v_cmp_ne_u32 vcc, 4, v10\n"
    "s_cbranch_vccnz R_48\n"
    "ES3:\n"
    "s_waitcnt vmcnt(4)\n"
    "global_load_lds_dwordx4 v12, s[56:57] nt\n"
    "s_add_u32 m0, m0, 8192\n"
    "v_add_u32 v12, 589824, v12\n"
    "s_waitcnt vmcnt(4)\n"
    "global_load_lds_dwordx4 v13, s[56:57] nt\n"
    "s_add_u32 m0, m0, 8192\n"
    "v_add_u32 v13, 589824, v13\n"
    "s_waitcnt vmcnt(4)\n"
    "s_mov_b64 exec, 1\n"
    "ds_add_u32 v14, v15 offset:76\n"
    "s_mov_b64 exec, -1\n"
    "s_waitcnt vmcnt(4)\n"
    "global_load_lds_dwordx4 v12, s[56:57] nt\n"
    "s_add_u32 m0, m0, 8192\n"
    "v_add_u32 v12, 589824, v12\n"
    "s_branch E_51\n"
    "R_50:\n"
    "s_sub_u32 s39, s39, 1\n"
    "s_cbranch_scc1 ABORT\n"
    "E_51:\n"
    "ds_read_b32 v10, v14 offset:76\n"
    "s_waitcnt lgkmcnt(0)\n"
    "v_cmp_ne_u32 vcc, 8, v10\n"
    "s_cbranch_vccnz R_50\n"
    "ds_read_b64_tr_b16 v[56:57], v4 offset:0\n"
    "ds_read_b64_tr_b16 v[58:59], v4 offset:256\n"
    "ds_read_b64_tr_b16 v[60:61], v4 offset:4096\n"
    "ds_read_b64_tr_b16 v[62:63], v4 offset:4352\n"
    "ds_read_b64_tr_b16 v[64:65], v4 offset:16384\n"
    "ds_read_b64_tr_b16 v[66:67], v4 offset:16640\n"
    "ds_read_b64_tr_b16 v[68:69], v4 offset:20480\n"
    "ds_read_b64_tr_b16 v[70:71], v4 offset:20736\n"
    "ds_read_b64_tr_b16 v[72:73], v4 offset:32768\n"
    "ds_read_b64_tr_b16 v[74:75], v4 offset:33024\n"
    "ds_read_b64_tr_b16 v[76:77], v4 offset:36864\n"
    "ds_read_b64_tr_b16 v[78:79], v4 offset:37120\n"
    "ds_read_b64_tr_b16 v[80:81], v4 offset:49152\n"
    "ds_read_b64_tr_b16 v[82:83], v4 offset:49408\n"
    "ds_read_b64_tr_b16 v[84:85], v4 offset:53248\n"
    "ds_read_b64_tr_b16 v[86:87], v4 offset:53504\n"
    "ds_read_b64_tr_b16 v[88:89], v5 offset:0\n"
    "ds_read_b64_tr_b16 v[90:91], v5 offset:256\n"
    "ds_read_b64_tr_b16 v[92:93], v5 offset:4096\n"
    "ds_read_b64_tr_b16 v[94:95], v5 offset:4352\n"
    "s_mov_b64 exec, 1\n"
    "ds_add_u32 v14, v15 offset:80\n"
    "s_mov_b64 exec, -1\n"
    "s_waitcnt vmcnt(4)\n"
    "global_load_lds_dwordx4 v13, s[56:57] nt\n"
    "s_add_u32 m0, m0, 8192\n"
    "v_add_u32 v13, 589824, v13\n"
    "s_waitcnt vmcnt(4)\n"
    "global_load_lds_dwordx4 v12, s[56:57] nt\n"
    "s_add_u32 m0, m0, 8192\n"
    "v_add_u32 v12, 589824, v12\n"
    "s_waitcnt vmcnt(4)\n"
    "global_load_lds_dwordx4 v13, s[56:57] nt\n"
    "s_add_u32 m0, m0, 8192\n"
    "v_add_u32 v13, 589824, v13\n"
    "s_branch E_53\n"
    "R_52:\n"
    "s_sleep 1\n"
    "s_sub_u32 s39, s39, 1\n"
    "s_cbranch_scc1 ABORT\n"
    "E_53:\n"
    "ds_read_b32 v10, v14 offset:80\n"
    "s_waitcnt lgkmcnt(0)\n"
    "v_cmp_ne_u32 vcc, 8, v10\n"
    "s_cbranch_vccnz R_52\n"
    "s_mul_i32 m0, s37, 9216\n"
    "s_waitcnt vmcnt(4)\n"
    "global_load_lds_dwordx4 v2, s[32:33] offset:0 nt\n"
    "s_add_u32 m0, m0, 896\n"
    "s_waitcnt vmcnt(4)\n"
    "global_load_lds_dwordx4 v2, s[32:33] offset:128 nt\n"
    "s_add_u32 m0, m0, 896\n"
    "s_waitcnt vmcnt(4)\n"
    "global_load_lds_dwordx4 v2, s[32:33] offset:256 nt\n"
    "s_add_u32 m0, m0, 896\n"
    "s_waitcnt vmcnt(4)\n"
    "global_load_lds_dwordx4 v2, s[32:33] offset:384 nt\n"
    "s_add_u32 m0, m0, 896\n"
    "s_waitcnt vmcnt(4)\n"
    "s_mov_b64 exec, 1\n"
    "ds_add_u32 v14, v15 offset:84\n"
    "s_mov_b64 exec, -1\n"
    "s_waitcnt vmcnt(4)\n"
    "global_load_lds_dwordx4 v2, s[32:33] offset:512 nt\n"
    "s_add_u32 m0, m0, 896\n"
    "s_branch E_55\n"
    "R_54:\n"
    "s_sub_u32 s39, s39, 1\n"
    "s_cbranch_scc1 ABORT\n"
    "E_55:\n"
    "ds_read_b32 v10, v14 offset:84\n"
    "s_waitcnt lgkmcnt(0)\n"
    "v_cmp_ne_u32 vcc, 8, v10\n"
    "s_cbranch_vccnz R_54\n"
    "ds_read_b64_tr_b16 v[96:97], v5 offset:16384\n"
    "ds_read_b64_tr_b16 v[98:99], v5 offset:16640\n"
    "ds_read_b64_tr_b16 v[100:101], v5 offset:20480\n"
    "ds_read_b64_tr_b16 v[102:103], v5 offset:20736\n"
    "ds_read_b64_tr_b16 v[104:105], v5 offset:32768\n"
    "ds_read_b64_tr_b16 v[106:107], v5 offset:33024\n"
    "ds_read_b64_tr_b16 v[108:109], v5 offset:36864\n"
    "ds_read_b64_tr_b16 v[110:111], v5 offset:37120\n"
    "ds_read_b64_tr_b16 v[112:113], v5 offset:49152\n"
    "ds_read_b64_tr_b16 v[114:115], v5 offset:49408\n"
    "ds_read_b64_tr_b16 v[116:117], v5 offset:53248\n"
    "ds_read_b64_tr_b16 v[118:119], v5 offset:53504\n"
    "ds_read_b64_tr_b16 v[120:121], v6 offset:0\n"
    "ds_read_b64_tr_b16 v[122:123], v6 offset:256\n"
    "ds_read_b64_tr_b16 v[124:125], v6 offset:4096\n"
    "ds_read_b64_tr_b16 v[126:127], v6 offset:4352\n"
    "s_mov_b64 exec, 1\n"
    "ds_add_u32 v14, v15 offset:88\n"
    "s_mov_b64 exec, -1\n"
    "s_waitcnt vmcnt(4)\n"
    "global_load_lds_dwordx4 v2, s[32:33] offset:640 nt\n"
    "s_add_u32 m0, m0, 896\n"
    "s_waitcnt vmcnt(4)\n"
    "global_load_lds_dwordx4 v2, s[32:33] offset:768 nt\n"
    "s_add_u32 m0, m0, 896\n"
    "s_waitcnt vmcnt(4)\n"
    "global_load_lds_dwordx4 v2, s[32:33] offset:896 nt\n"
    "s_add_u32 m0, m0, 896\n"
    "s_waitcnt vmcnt(4)\n"
    "global_load_lds_dwordx4 v2, s[32:33] offset:1024 nt\n"
    "s_add_u32 m0, m0, 896\n"
    "CHE_0:\n"
    "s_cbranch_execz CHE_1\n"
    "GUT:\n"
    "v_and_b32 v7, 7, v0\n"
    "v_and_b32 v8, 1, v7\n"
    "v_mul_u32_u24 v4, 1344, v7\n"
    "v_lshlrev_b32 v8, 5, v8\n"
    "v_sub_u32 v4, v4, v8\n"
    "v_add_u32 v4, 73728, v4\n"
    "v_and_b32 v9, 8, v0\n"
    "v_lshlrev_b32 v5, 3, v9\n"
    "v_lshl_add_u32 v5, v3, 4, v5\n"
    "v_add_u32 v5, v5, v4\n"
    "v_and_b32 v6, 1, v3\n"
    "v_lshlrev_b32 v6, 2, v6\n"
    "v_and_b32 v8, 2, v3\n"
    "v_lshrrev_b32 v9, 3, v9\n"
    "v_add3_u32 v6, v6, v8, v9\n"
    "v_mul_u32_u24 v6, 144, v6\n"
    "v_lshl_add_u32 v6, v7, 1, v6\n"
    "s_lshl_b32 s42, s37, 4\n"
    "s_add_u32 s42, s42, 150016\n"
    "v_add_u32 v6, s42, v6\n"
    "v_mul_u32_u24 v8, 0xaaaa, v9\n"
    "v_add_u32 v8, 0x4444, v8\n"
    "v_mov_b32 v16, 0\n"
    "v_mov_b32 v17, 0\n"
    "v_mov_b32 v18, 0\n"
    "v_mov_b32 v19, 0\n"
    "s_setprio 2\n"
    "s_waitcnt vmcnt(0)\n"
    "s_mov_b64 exec, 1\n"
    "ds_add_u32 v14, v15 offset:108\n"
    "s_mov_b64 exec, -1\n"
    "s_waitcnt lgkmcnt(0)\n"
    "s_barrier\n"
    "ds_read_b128 v[20:23], v5 offset:0\n"
    "ds_read_b128 v[24:27], v5 offset:128\n"
    "ds_read_b128 v[28:31], v5 offset:256\n"
    "ds_read_b128 v[32:35], v5 offset:384\n"
    "ds_read_b128 v[36:39], v5 offset:512\n"
    "ds_read_b128 v[40:43], v5 offset:640\n"
    "ds_read_b128 v[44:47], v5 offset:768\n"
    "ds_read_b128 v[48:51], v5 offset:896\n"
    "ds_read_b128 v[52:55], v5 offset:1024\n"
    "ds_read_b32 v10, v14 offset:112\n"
    "s_waitcnt lgkmcnt(9)\n"
    "v_smfmac_f32_16x16x64_bf16 v[16:19], v[20:23], v[56:63], v8\n"
    "s_waitcnt lgkmcnt(8)\n"
    "v_smfmac_f32_16x16x64_bf16 v[16:19], v[24:27], v[64:71], v8\n"
    "s_waitcnt lgkmcnt(7)\n"
    "v_smfmac_f32_16x16x64_bf16 v[16:19], v[28:31], v[72:79], v8\n"
    "s_waitcnt lgkmcnt(6)\n"
    "v_smfmac_f32_16x16x64_bf16 v[16:19], v[32:35], v[80:87], v8\n"
    "s_waitcnt lgkmcnt(5)\n"
    "v_smfmac_f32_16x16x64_bf16 v[16:19], v[36:39], v[88:95], v8\n"
    "s_waitcnt lgkmcnt(4)\n"
    "v_smfmac_f32_16x16x64_bf16 v[16:19], v[40:43], v[96:103], v8\n"
    "s_waitcnt lgkmcnt(3)\n"
    "v_smfmac_f32_16x16x64_bf16 v[16:19], v[44:47], v[104:111], v8\n"
    "s_waitcnt lgkmcnt(2)\n"
    "v_smfmac_f32_16x16x64_bf16 v[16:19], v[48:51], v[112:119], v8\n"
    "s_waitcnt lgkmcnt(1)\n"
    "v_smfmac_f32_16x16x64_bf16 v[16:19], v[52:55], v[120:127], v8\n"
    "s_nop 7\n"
    "v_permlane32_swap_b32 v16, v18\n"
    "v_permlane32_swap_b32 v17, v19\n"
    "v_add_f32 v12, v16, v18\n"
    "v_add_f32 v14, v16, v18\n"
    "v_add_f32 v13, v17, v19\n"
    "s_nop 1\n"
    "v_mov_b32_dpp v12, v13 row_ror:8 row_mask:0xf bank_mask:0xc\n"
    "v_mov_b32_dpp v13, v14 row_ror:8 row_mask:0xf bank_mask:0x3\n"
    "v_cvt_pk_bf16_f32 v12, 0, v12\n"
    "v_mul_f32 v14, 0xbfb8aa3b, v12\n"
    "v_exp_f32 v14, v14\n"
    "v_cvt_pk_bf16_f32 v13, 0, v13\n"
    "v_add_f32 v14, 1.0, v14\n"
    "v_rcp_f32 v14, v14\n"
    "s_nop 0\n"
    "v_mul_f32 v12, v12, v14\n"
    "v_cvt_pk_bf16_f32 v12, 0, v12\n"
    "v_mul_f32 v12, v12, v13\n"
    "v_cvt_pk_bf16_f32 v12, v12, v12\n"
    "ds_write_b16 v6, v12\n"
    "s_waitcnt lgkmcnt(0)\n"
    "s_barrier\n"
    "s_cmpk_lt_u32 s2, 192\n"
    "s_cbranch_scc1 END\n"
    "v_readfirstlane_b32 s96, v10\n"
    "v_and_b32 v1, 63, v0\n"
    "v_and_b32 v2, 15, v0\n"
    "s_movk_i32 s61, 0x7ff\n"
    "s_setprio 0\n"
    "s_cmpk_eq_u32 s37, 7\n"
    "s_cbranch_scc1 FINP\n"
    "s_cmpk_lg_u32 s2, 192\n"
    "s_cbranch_scc1 END\n"
    "s_cmpk_lg_u32 s37, 0\n"
    "s_cbranch_scc1 END\n"
    "v_mov_b32 v1, s63\n"
    "s_mov_b64 exec, 1\n"
    "buffer_store_dword v1, off, s[20:23], 0 sc1\n"
    "s_mov_b64 exec, -1\n"
    "s_branch END\n"
    "CHE_1:\n"
    "s_cbranch_execz CHE_2\n"
    "MOED:\n"
    "v_and_b32 v1, 63, v0\n"
    "v_and_b32 v2, 15, v0\n"
    "v_bfe_u32 v3, v0, 4, 2\n"
    "v_mov_b32 v14, 148096\n"
    "v_mov_b32 v15, 1\n"
    "v_bfe_u32 v6, v0, 2, 2\n"
    "v_bfe_u32 v7, v0, 1, 1\n"
    "v_and_b32 v8, 2, v6\n"
    "v_or_b32 v7, v7, v8\n"
    "v_and_b32 v8, 1, v3\n"
    "v_lshl_add_u32 v7, v8, 2, v7\n"
    "v_mul_u32_u24 v8, 9216, v3\n"
    "v_lshl_add_u32 v8, v6, 7, v8\n"
    "v_lshl_add_u32 v8, v7, 4, v8\n"
    "v_and_b32 v7, 1, v0\n"
    "v_lshl_add_u32 v8, v7, 3, v8\n"
    "s_mul_i32 s42, s37, 6\n"
    "s_sub_u32 s42, s42, 48\n"
    "s_and_b32 s43, s42, 3\n"
    "s_lshl_b32 s43, s43, 5\n"
    "s_lshr_b32 s44, s42, 2\n"
    "s_lshl_b32 s44, s44, 10\n"
    "v_xor_b32 v104, s43, v8\n"
    "v_add_u32 v104, s44, v104\n"
    "s_add_u32 s42, s42, 1\n"
    "s_and_b32 s43, s42, 3\n"
    "s_lshl_b32 s43, s43, 5\n"
    "s_lshr_b32 s44, s42, 2\n"
    "s_lshl_b32 s44, s44, 10\n"
    "v_xor_b32 v105, s43, v8\n"
    "v_add_u32 v105, s44, v105\n"
    "s_add_u32 s42, s42, 1\n"
    "s_and_b32 s43, s42, 3\n"
    "s_lshl_b32 s43, s43, 5\n"
    "s_lshr_b32 s44, s42, 2\n"
    "s_lshl_b32 s44, s44, 10\n"
    "v_xor_b32 v106, s43, v8\n"
    "v_add_u32 v106, s44, v106\n"
    "s_add_u32 s42, s42, 1\n"
    "s_and_b32 s43, s42, 3\n"
    "s_lshl_b32 s43, s43, 5\n"
    "s_lshr_b32 s44, s42, 2\n"
    "s_lshl_b32 s44, s44, 10\n"
    "v_xor_b32 v107, s43, v8\n"
    "v_add_u32 v107, s44, v107\n"
    "s_add_u32 s42, s42, 1\n"
    "s_and_b32 s43, s42, 3\n"
    "s_lshl_b32 s43, s43, 5\n"
    "s_lshr_b32 s44, s42, 2\n"
    "s_lshl_b32 s44, s44, 10\n"
    "v_xor_b32 v108, s43, v8\n"
    "v_add_u32 v108, s44, v108\n"
    "s_add_u32 s42, s42, 1\n"
    "s_and_b32 s43, s42, 3\n"
    "s_lshl_b32 s43, s43, 5\n"
    "s_lshr_b32 s44, s42, 2\n"
    "s_lshl_b32 s44, s44, 10\n"
    "v_xor_b32 v109, s43, v8\n"
    "v_add_u32 v109, s44, v109\n"
    "s_add_u32 s42, s42, 1\n"
    "v_and_b32 v7, 7, v0\n"
    "v_mul_u32_u24 v7, 144, v7\n"
    "v_lshl_add_u32 v7, v3, 4, v7\n"
    "v_add_u32 v7, 150016, v7\n"
    "v_mov_b32 v8, 148224\n"
    "v_cmp_gt_u32 vcc, 8, v2\n"
    "s_nop 1\n"
    "v_cndmask_b32 v4, v8, v7, vcc\n"
    "v_cndmask_b32 v5, v7, v8, vcc\n"
    "v_and_b32 v6, 1, v3\n"
    "v_mul_u32_u24 v6, 6144, v6\n"
    "v_lshl_add_u32 v6, v2, 4, v6\n"
    "v_lshrrev_b32 v7, 1, v3\n"
    "v_lshl_add_u32 v6, v7, 8, v6\n"
    "s_mul_i32 s42, s58, 12416\n"
    "s_lshl_b32 s43, s37, 9\n"
    "s_add_u32 s42, s42, s43\n"
    "s_add_u32 s42, s42, 6443008\n"
    "v_add_u32 v6, s42, v6\n"
    "s_lshl_b32 s62, s37, 2\n"
    "s_add_u32 s62, s62, 151616\n"
    "v_mov_b32 v7, s62\n"
    "CHE_2:\n"
    "s_cbranch_execz CHE_3\n"
    "s_add_u32 s62, s62, 28\n"
    "v_lshlrev_b32 v8, 4, v1\n"
    "v_and_b32 v9, 31, v1\n"
    "v_add_u32 v9, 64, v9\n"
    "s_mov_b32 s42, 73728\n"
    "v_mad_u32_u24 v11, v1, 12, s42\n"
    "v_mad_u32_u24 v12, v9, 12, s42\n"
    "v_subrev_u32 v54, 320, v7\n"
    "v_lshlrev_b32 v55, 2, v1\n"
    "v_add_u32 v55, 151328, v55\n"
    "v_lshlrev_b32 v9, 4, v9\n"
    "s_mov_b32 s59, 7341056\n"
    "s_movk_i32 s60, 1536\n"
    "s_movk_i32 s61, 0x7ff\n"
    "s_add_u32 s63, s36, 1\n"
    "s_cmpk_ge_u32 s37, 14\n"
    "s_cselect_b32 s41, 99, 8\n"
    "s_cmpk_ge_u32 s37, 12\n"
    "s_cbranch_scc1 TOPK\n"
    "s_cmpk_lg_u32 s37, 10\n"
    "s_cbranch_scc1 CHE9\n"
    "s_mov_b64 exec, 0\n"
    "CHE:\n"
    "s_branch CHE_0\n"
    "CHE9:\n"
    "s_mov_b64 exec, -1\n"
    "s_cmpk_lg_u32 s37, 11\n"
    "s_cbranch_scc1 MOE2\n"
    "s_memrealtime s[96:97]\n"
    "s_waitcnt lgkmcnt(0)\n"
    "s_cmpk_lt_u32 s2, 192\n"
    "s_cbranch_scc1 HNT1\n"
    "U_56:\n"
    "s_sleep 8\n"
    "s_memrealtime s[98:99]\n"
    "s_waitcnt lgkmcnt(0)\n"
    "s_sub_u32 s98, s98, s96\n"
    "s_cmpk_lt_u32 s98, 400\n"
    "s_cbranch_scc1 U_56\n"
    "HNT1:\n"
    "v_mad_u32_u24 v18, v1, 3, s58\n"
    "v_subrev_u32 v17, 192, v18\n"
    "v_min_u32 v18, v18, v17\n"
    "v_lshlrev_b32 v18, 7, v18\n"
    "s_mov_b32 s42, 94208\n"
    "s_branch E_58\n"
    "R_57:\n"
    "s_sleep 6\n"
    "s_sub_u32 s39, s39, 1\n"
    "s_cbranch_scc1 ABORT\n"
    "E_58:\n"
    "buffer_load_dwordx2 v[16:17], v18, s[20:23], s42 offen sc1\n"
    "s_waitcnt vmcnt(0)\n"
    "v_cmp_eq_u32 vcc, s36, v17\n"
    "s_bcnt1_i32_b64 s43, vcc\n"
    "s_cmp_lt_u32 s43, 56\n"
    "s_cbranch_scc1 R_57\n"
    "v_subrev_u32 v16, s96, v16\n"
    "v_mov_b32 v17, -1000\n"
    "v_cndmask_b32 v16, v17, v16, vcc\n"
    "s_nop 1\n"
    "v_max_i32_dpp v16, v16, v16 row_ror:8 row_mask:0xf bank_mask:0xf\n"
    "s_nop 1\n"
    "v_max_i32_dpp v16, v16, v16 row_ror:4 row_mask:0xf bank_mask:0xf\n"
    "s_nop 1\n"
    "v_max_i32_dpp v16, v16, v16 row_ror:2 row_mask:0xf bank_mask:0xf\n"
    "s_nop 1\n"
    "v_max_i32_dpp v16, v16, v16 row_ror:1 row_mask:0xf bank_mask:0xf\n"
    "s_nop 1\n"
    "v_max_i32_dpp v16, v16, v16 row_bcast:15 row_mask:0xa bank_mask:0xf\n"
    "s_nop 1\n"
    "v_max_i32_dpp v16, v16, v16 row_bcast:31 row_mask:0xc bank_mask:0xf\n"
    "v_add_u32 v16, s96, v16\n"
    "v_mov_b32 v17, 1\n"
    "s_mov_b32 exec_lo, 0\n"
    "s_brev_b32 exec_hi, 1\n"
    "ds_write_b64 v14, v[16:17] offset:112\n"
    "s_mov_b64 exec, -1\n"
    "MOE2:\n"
    "s_sleep 4\n"
    "s_sub_u32 s39, s39, 1\n"
    "s_cbranch_scc1 ABORT\n"
    "ds_read_b32 v10, v14 offset:72\n"
    "ds_read_b32 v13, v14 offset:108\n"
    "s_waitcnt lgkmcnt(0)\n"
    "v_readfirstlane_b32 s42, v10\n"
    "v_readfirstlane_b32 s43, v13\n"
    "s_cmp_lg_u32 s42, 0\n"
    "s_cbranch_scc1 MOE2a\n"
    "s_cmp_eq_u32 s43, s41\n"
    "s_cbranch_scc1 DNRD\n"
    "s_branch MOE2\n"
    "CHE_3:\n"
    "s_cbranch_execz CHE_4\n"
    "MOE2a:\n"
    "s_setprio 2\n"
    "s_branch MOE3\n"
    "DNRD:\n"
    "ds_read_b64_tr_b16 v[56:57], v104 offset:0\n"
    "ds_read_b64_tr_b16 v[58:59], v104 offset:512\n"
    "ds_read_b64_tr_b16 v[60:61], v105 offset:0\n"
    "ds_read_b64_tr_b16 v[62:63], v105 offset:512\n"
    "ds_read_b64_tr_b16 v[64:65], v106 offset:0\n"
    "ds_read_b64_tr_b16 v[66:67], v106 offset:512\n"
    "ds_read_b64_tr_b16 v[68:69], v107 offset:0\n"
    "ds_read_b64_tr_b16 v[70:71], v107 offset:512\n"
    "ds_read_b64_tr_b16 v[72:73], v108 offset:0\n"
    "ds_read_b64_tr_b16 v[74:75], v108 offset:512\n"
    "ds_read_b64_tr_b16 v[76:77], v109 offset:0\n"
    "ds_read_b64_tr_b16 v[78:79], v109 offset:512\n"
    "ds_read_b64_tr_b16 v[80:81], v104 offset:36864\n"
    "ds_read_b64_tr_b16 v[82:83], v104 offset:37376\n"
    "ds_read_b64_tr_b16 v[84:85], v105 offset:36864\n"
    "ds_read_b64_tr_b16 v[86:87], v105 offset:37376\n"
    "ds_read_b64_tr_b16 v[88:89], v106 offset:36864\n"
    "ds_read_b64_tr_b16 v[90:91], v106 offset:37376\n"
    "ds_read_b64_tr_b16 v[92:93], v107 offset:36864\n"
    "ds_read_b64_tr_b16 v[94:95], v107 offset:37376\n"
    "ds_read_b64_tr_b16 v[96:97], v108 offset:36864\n"
    "ds_read_b64_tr_b16 v[98:99], v108 offset:37376\n"
    "ds_read_b64_tr_b16 v[100:101], v109 offset:36864\n"
    "ds_read_b64_tr_b16 v[102:103], v109 offset:37376\n"
    "s_cmp_eq_u32 s41, 0\n"
    "s_mov_b32 s41, 99\n"
    "s_cbranch_scc1 DOWN1\n"
    "s_cmpk_ge_u32 s37, 12\n"
    "s_cbranch_scc1 TOPK2\n"
    "s_branch MOE2\n"
    "TOPK:\n"
    "s_setprio 3\n"
    "s_mul_i32 s42, s37, 9\n"
    "s_add_u32 s42, s42, s58\n"
    "s_mul_i32 s43, s42, 1821\n"
    "s_lshr_b32 s43, s43, 16\n"
    "s_mul_i32 s43, s43, 36\n"
    "s_sub_u32 s42, s42, s43\n"
    "s_lshl_b32 s42, s42, 7\n"
    "s_sub_u32 s42, 94084, s42\n"
    "v_lshlrev_b32 v13, 4, v0\n"
    "s_mov_b32 s44, 65536\n"
    "s_mov_b32 s45, 69632\n"
    "s_mov_b32 s46, 73728\n"
    "s_mov_b32 s47, 77824\n"
    "v_lshl_add_u32 v16, v2, 2, v14\n"
    "v_lshl_add_u32 v17, v3, 4, v14\n"
    "s_brev_b32 s48, 1\n"
    "v_mov_b32 v18, 4\n"
    "v_lshlrev_b32 v19, 2, v1\n"
    "v_add_u32 v19, 151648, v19\n"
    "v_mov_b32 v21, 0\n"
    "ds_write_b32 v55, v21\n"
    "v_mov_b32 v20, 37912\n"
    "s_mov_b64 exec, 1\n"
    "ds_add_u32 v14, v20 offset:64\n"
    "s_mov_b64 exec, -1\n"
    "s_branch E_60\n"
    "CHE_4:\n"
    "s_cbranch_execz CHE_5\n"
    "R_59:\n"
    "s_sleep 4\n"
    "s_sub_u32 s39, s39, 1\n"
    "s_cbranch_scc1 ABORT\n"
    "E_60:\n"
    "ds_read_b64 v[20:21], v14 offset:112\n"
    "s_waitcnt lgkmcnt(0)\n"
    "v_cmp_ne_u32 vcc, 1, v21\n"
    "s_cbranch_vccnz R_59\n"
    "v_readfirstlane_b32 s96, v20\n"
    "U_61:\n"
    "s_sleep 2\n"
    "s_memrealtime s[98:99]\n"
    "s_waitcnt lgkmcnt(0)\n"
    "s_sub_u32 s98, s98, s96\n"
    "s_cmpk_lt_u32 s98, 193\n"
    "s_cbranch_scc1 U_61\n"
    "ds_read_b32 v20, v14 offset:108\n"
    "s_waitcnt lgkmcnt(0)\n"
    "v_readfirstlane_b32 s43, v20\n"
    "s_cmp_eq_u32 s43, s41\n"
    "s_cbranch_scc1 DNRD\n"
    "TOPK2:\n"
    "U_62:\n"
    "s_memrealtime s[98:99]\n"
    "s_waitcnt lgkmcnt(0)\n"
    "s_sub_u32 s98, s98, s96\n"
    "s_cmpk_lt_u32 s98, 218\n"
    "s_cbranch_scc1 U_62\n"
    "buffer_load_dwordx4 v[24:27], v13, s[20:23], s44 offen sc1\n"
    "buffer_load_dwordx4 v[28:31], v13, s[20:23], s45 offen sc1\n"
    "buffer_load_dwordx4 v[32:35], v13, s[20:23], s46 offen sc1\n"
    "buffer_load_dwordx4 v[36:39], v13, s[20:23], s47 offen sc1\n"
    "buffer_load_dwordx2 v[40:41], off, s[20:23], 0 offset:2048 sc1\n"
    "s_branch E_64\n"
    "R_63:\n"
    "s_sub_u32 s39, s39, 1\n"
    "s_cbranch_scc1 ABORT\n"
    "s_wqm_b64 exec, vcc\n"
    "buffer_load_dwordx4 v[24:27], v13, s[20:23], s44 offen sc1\n"
    "buffer_load_dwordx4 v[28:31], v13, s[20:23], s45 offen sc1\n"
    "buffer_load_dwordx4 v[32:35], v13, s[20:23], s46 offen sc1\n"
    "buffer_load_dwordx4 v[36:39], v13, s[20:23], s47 offen sc1\n"
    "buffer_load_dwordx2 v[40:41], off, s[20:23], 0 offset:2048 sc1\n"
    "s_mov_b64 exec, -1\n"
    "E_64:\n"
    "s_waitcnt vmcnt(3)\n"
    "v_bitop3_b32 v20, v25, v27, s36 bitop3:0x7e\n"
    "v_bitop3_b32 v21, v29, v31, s36 bitop3:0x7e\n"
    "v_max3_u32 v42, v24, v26, v28\n"
    "s_waitcnt vmcnt(2)\n"
    "v_bitop3_b32 v22, v33, v35, s36 bitop3:0x7e\n"
    "v_max3_u32 v42, v42, v30, v32\n"
    "v_or3_b32 v20, v20, v21, v22\n"
    "s_waitcnt vmcnt(0)\n"
    "v_bitop3_b32 v21, v37, v39, s36 bitop3:0x7e\n"
    "v_xor_b32 v22, s36, v41\n"
    "v_max3_u32 v42, v42, v34, v36\n"
    "v_or3_b32 v20, v20, v21, v22\n"
    "v_max_u32 v42, v42, v38\n"
    "v_cmp_ne_u32 vcc, 0, v20\n"
    "s_cbranch_vccnz R_63\n"
    "ds_max_u32 v16, v42\n"
    "s_mov_b64 exec, 1\n"
    "ds_add_u32 v14, v15 offset:72\n"
    "s_mov_b64 exec, -1\n"
    "ds_read_b32 v10, v14 offset:72\n"
    "ds_read_b32 v20, v16\n"
    "ds_read_b128 v[46:49], v17\n"
    "v_max3_u32 v45, v24, v26, v28\n"
    "v_med3_u32 v43, v24, v26, v28\n"
    "v_min3_u32 v44, v24, v26, v28\n"
    "v_med3_u32 v44, v43, v44, v30\n"
    "v_med3_u32 v43, v45, v43, v30\n"
    "v_max_u32 v45, v45, v30\n"
    "v_med3_u32 v44, v43, v44, v32\n"
    "v_med3_u32 v43, v45, v43, v32\n"
    "v_max_u32 v45, v45, v32\n"
    "s_branch E_66\n"
    "CHE_5:\n"
    "s_cbranch_execz CHE_6\n"
    "R_65:\n"
    "s_sub_u32 s39, s39, 1\n"
    "s_cbranch_scc1 ABORT\n"
    "ds_read_b32 v10, v14 offset:72\n"
    "ds_read_b32 v20, v16\n"
    "ds_read_b128 v[46:49], v17\n"
    "E_66:\n"
    "s_waitcnt lgkmcnt(0)\n"
    "v_cmp_ne_u32 vcc, 4, v10\n"
    "v_cmp_gt_u32 s[50:51], v46, v20\n"
    "v_cmp_gt_u32 s[52:53], v47, v20\n"
    "v_cmp_gt_u32 s[54:55], v48, v20\n"
    "v_cmp_gt_u32 s[56:57], v49, v20\n"
    "v_cndmask_b32 v21, 0, 1, s[50:51]\n"
    "s_cbranch_vccnz R_65\n"
    "v_addc_co_u32 v21, vcc, 0, v21, s[52:53]\n"
    "v_addc_co_u32 v21, vcc, 0, v21, s[54:55]\n"
    "v_addc_co_u32 v21, vcc, 0, v21, s[56:57]\n"
    "v_mov_b32 v22, v21\n"
    "v_ashrrev_i32 v23, 31, v20\n"
    "v_bitop3_b32 v23, v23, v20, s48 bitop3:0x63\n"
    "v_permlane16_swap_b32 v21, v22\n"
    "v_add_u32 v21, v21, v22\n"
    "v_mov_b32 v22, v21\n"
    "v_sub_f32 v23, v23, v40\n"
    "v_ashrrev_i32 v46, 31, v23\n"
    "v_permlane32_swap_b32 v21, v22\n"
    "v_add_u32 v21, v21, v22\n"
    "v_cmp_eq_u32 vcc, 7, v21\n"
    "v_bitop3_b32 v23, v46, v23, s48 bitop3:0x36\n"
    "v_min_u32 v23, v23, v20\n"
    "v_and_b32 v23, 0xfffff800, v23\n"
    "v_med3_u32 v44, v43, v44, v34\n"
    "v_med3_u32 v43, v45, v43, v34\n"
    "s_ff1_i32_b64 s49, vcc\n"
    "v_readlane_b32 s49, v23, s49\n"
    "v_max_u32 v45, v45, v34\n"
    "v_med3_u32 v44, v43, v44, v36\n"
    "v_cmpx_le_u32 vcc, s49, v42\n"
    "v_med3_u32 v43, v45, v43, v36\n"
    "v_max_u32 v45, v45, v36\n"
    "v_med3_u32 v44, v43, v44, v38\n"
    "v_med3_u32 v43, v45, v43, v38\n"
    "v_cmp_le_u32 s[50:51], s49, v43\n"
    "v_cmp_le_u32 s[52:53], s49, v44\n"
    "s_nop 0\n"
    "v_cndmask_b32 v21, 4, 8, s[50:51]\n"
    "s_cmp_lg_u64 s[52:53], 0\n"
    "s_cbranch_scc1 TOPS\n"
    "ds_add_rtn_u32 v21, v14, v21 offset:64\n"
    "s_waitcnt lgkmcnt(0)\n"
    "ds_write_b32 v21, v42\n"
    "s_mov_b64 exec, s[50:51]\n"
    "ds_write_b32 v21, v43 offset:4\n"
    "TOPC:\n"
    "s_mov_b64 exec, 1\n"
    "ds_add_u32 v14, v15 offset:68\n"
    "s_mov_b64 exec, -1\n"
    "s_cmpk_eq_u32 s37, 15\n"
    "s_cbranch_scc1 MOE15\n"
    "MOE3:\n"
    "ds_read_b64 v[20:21], v14 offset:64\n"
    "ds_read2_b32 v[22:23], v7 offset1:7\n"
    "ds_read_b32 v48, v7 offset:56\n"
    "s_sub_u32 s39, s39, 1\n"
    "s_cbranch_scc1 ABORT\n"
    "s_waitcnt lgkmcnt(0)\n"
    "v_cmp_ne_u32 vcc, 4, v21\n"
    "v_readfirstlane_b32 s42, v20\n"
    "v_bitop3_b32 v22, v22, s61, v22 bitop3:0x0c\n"
    "s_cbranch_vccnz MOE3\n"
    "s_cmp_gt_u32 s42, 151904\n"
    "s_cbranch_scc1 MANY\n"
    "MOE4:\n"
    "v_mad_u32_u24 v24, v22, s60, v8\n"
    "v_mad_u32_u24 v25, v22, s60, v9\n"
    "s_cmp_gt_u32 s42, s62\n"
    "s_cbranch_scc1 ROW2\n"
    "buffer_load_dwordx4 v[28:31], v24, s[20:23], s59 offen sc1\n"
    "buffer_load_dwordx4 v[32:35], v25, s[20:23], s59 offen sc1\n"
    "ROW1:\n"
    "s_branch E_68\n"
    "CHE_6:\n"
    "s_cbranch_execz CHE_7\n"
    "R_67:\n"
    "s_sub_u32 s39, s39, 1\n"
    "s_cbranch_scc1 ABORT\n"
    "E_68:\n"
    "ds_read_b32 v10, v14 offset:88\n"
    "ds_read2_b32 v[44:45], v54 offset1:7\n"
    "ds_read_b32 v46, v55\n"
    "s_waitcnt lgkmcnt(0)\n"
    "v_cmp_ne_u32 s[50:51], 8, v10\n"
    "v_cmp_eq_u32 vcc, 0, v44\n"
    "s_or_b64 vcc, vcc, s[50:51]\n"
    "s_cbranch_vccnz R_67\n"
    "v_cmp_gt_u32 vcc, v46, v44\n"
    "s_bcnt1_i32_b64 s43, vcc\n"
    "s_cmp_ge_u32 s43, 8\n"
    "s_cbranch_scc1 MOE5\n"
    "s_and_b32 s44, s43, 1\n"
    "s_mul_i32 s45, s43, 1344\n"
    "s_lshl_b32 s44, s44, 5\n"
    "s_sub_u32 s45, s45, s44\n"
    "v_add_u32 v50, s45, v11\n"
    "v_add_u32 v51, s45, v12\n"
    "s_waitcnt vmcnt(1)\n"
    "ds_write2_b32 v50, v28, v29 offset1:1\n"
    "ds_write_b32 v50, v30 offset:8\n"
    "v_lshl_add_u32 v52, s43, 2, v14\n"
    "s_waitcnt vmcnt(0)\n"
    "ds_write2_b32 v51, v32, v33 offset1:1\n"
    "ds_write_b32 v51, v34 offset:8\n"
    "v_bitop3_b32 v20, v31, v35, s63 bitop3:0x7e\n"
    "ds_write_b32 v52, v44 offset:3072\n"
    "v_cmp_ne_u32 vcc, 0, v20\n"
    "s_cbranch_vccnz ROWS\n"
    "MOE5:\n"
    "s_waitcnt lgkmcnt(0)\n"
    "s_barrier\n"
    "s_cmpk_lt_u32 s37, 14\n"
    "s_cbranch_scc1 DOWN\n"
    "s_cmpk_lt_u32 s2, 192\n"
    "s_cbranch_scc1 END\n"
    "s_waitcnt vmcnt(0)\n"
    "FINP:\n"
    "s_and_b32 s42, s2, 7\n"
    "s_lshl_b32 s42, s42, 3\n"
    "s_sub_u32 s43, s2, 192\n"
    "s_lshr_b32 s43, s43, 3\n"
    "s_add_u32 s42, s42, s43\n"
    "s_mul_i32 s42, s42, 3\n"
    "s_max_u32 s44, s37, 13\n"
    "s_add_u32 s42, s42, s44\n"
    "s_sub_u32 s42, s42, 13\n"
    "s_mul_i32 s43, s42, 1366\n"
    "s_lshr_b32 s43, s43, 16\n"
    "s_mul_i32 s44, s43, 48\n"
    "s_sub_u32 s42, s42, s44\n"
    "s_lshl_b32 s42, s42, 2\n"
    "s_lshl_b32 s44, s43, 3\n"
    "s_add_u32 s44, s44, 151168\n"
    "v_and_b32 v20, 1, v0\n"
    "v_lshl_add_u32 v21, v20, 2, s44\n"
    "ds_read_b32 v23, v21\n"
    "s_mul_i32 s44, s43, 2688\n"
    "s_add_u32 s44, s44, 73728\n"
    "v_mul_u32_u24 v22, 1312, v20\n"
    "v_add_u32 v22, s44, v22\n"
    "s_lshr_b32 s45, s42, 4\n"
    "s_lshl_b32 s45, s45, 5\n"
    "s_add_u32 s45, s45, s42\n"
    "v_lshrrev_b32 v25, 1, v2\n"
    "v_min_u32 v26, 2, v25\n"
    "v_lshl_add_u32 v27, v26, 4, v3\n"
    "v_add_u32 v27, s45, v27\n"
    "v_mul_u32_u24 v24, 683, v27\n"
    "v_lshrrev_b32 v24, 11, v24\n"
    "v_mul_u32_u24 v26, 3, v24\n"
    "v_sub_u32 v26, v27, v26\n"
    "v_lshlrev_b32 v28, 3, v27\n"
    "s_mov_b32 s45, 43520\n"
    "s_mov_b32 s46, 71168\n"
    "v_mul_u32_u24 v24, 16512, v24\n"
    "s_waitcnt lgkmcnt(0)\n"
    "v_bitop3_b32 v29, v23, s61, v23 bitop3:0x0c\n"
    "v_lshl_add_u32 v22, v27, 1, v22\n"
    "v_mov_b32 v40, 0\n"
    "v_mov_b32 v41, 0\n"
    "ds_read_u16_d16_hi v40, v22\n"
    "v_mul_u32_u24 v30, 1152, v29\n"
    "v_lshl_add_u32 v30, v27, 1, v30\n"
    "global_load_short_d16_hi v41, v30, s[4:5]\n"
    "v_lshrrev_b32 v31, 1, v29\n"
    "v_lshl_add_u32 v24, v31, 4, v24\n"
    "s_mov_b32 s47, 131072\n"
    "v_lshrrev_b32 v31, 4, v29\n"
    "v_lshlrev_b32 v31, 2, v31\n"
    "s_setprio 0\n"
    "s_waitcnt lgkmcnt(0)\n"
    "s_cmpk_eq_u32 s37, 7\n"
    "s_cbranch_scc1 FIN\n"
    "s_barrier\n"
    "s_branch FIN\n"
    "CHE_7:\n"
    "s_cbranch_execz CHE_8\n"
    "DOWN:\n"
    "s_cmp_eq_u32 s41, 99\n"
    "s_cbranch_scc1 DOWN1\n"
    "s_mov_b32 s41, 0\n"
    "s_setprio 0\n"
    "s_sleep 3\n"
    "s_branch DNRD\n"
    "DOWN1:\n"
    "s_setprio 3\n"
    "v_mov_b32 v47, s36\n"
    "v_mov_b32 v51, s36\n"
    "s_waitcnt lgkmcnt(0)\n"
    "s_barrier\n"
    "ds_read_b128 v[104:107], v4\n"
    "ds_read_b128 v[108:111], v4 offset:64\n"
    "ds_read_b128 v[112:115], v5\n"
    "ds_read_b128 v[116:119], v5 offset:64\n"
    "s_waitcnt lgkmcnt(3)\n"
    "v_mfma_f32_16x16x32_bf16 v[8:11], v[104:107], v[56:59], 0\n"
    "v_mfma_f32_16x16x32_bf16 v[12:15], v[104:107], v[60:63], 0\n"
    "v_mfma_f32_16x16x32_bf16 v[16:19], v[104:107], v[64:67], 0\n"
    "s_waitcnt lgkmcnt(2)\n"
    "v_mfma_f32_16x16x32_bf16 v[8:11], v[108:111], v[80:83], v[8:11]\n"
    "v_mfma_f32_16x16x32_bf16 v[12:15], v[108:111], v[84:87], v[12:15]\n"
    "v_mfma_f32_16x16x32_bf16 v[16:19], v[108:111], v[88:91], v[16:19]\n"
    "s_waitcnt lgkmcnt(1)\n"
    "v_mfma_f32_16x16x32_bf16 v[8:11], v[112:115], v[68:71], v[8:11]\n"
    "v_mfma_f32_16x16x32_bf16 v[12:15], v[112:115], v[72:75], v[12:15]\n"
    "v_mfma_f32_16x16x32_bf16 v[16:19], v[112:115], v[76:79], v[16:19]\n"
    "s_waitcnt lgkmcnt(0)\n"
    "v_mfma_f32_16x16x32_bf16 v[8:11], v[116:119], v[92:95], v[8:11]\n"
    "v_mfma_f32_16x16x32_bf16 v[12:15], v[116:119], v[96:99], v[12:15]\n"
    "v_mfma_f32_16x16x32_bf16 v[16:19], v[116:119], v[100:103], v[16:19]\n"
    "s_nop 5\n"
    "v_cvt_pk_bf16_f32 v44, v8, v9\n"
    "v_cvt_pk_bf16_f32 v45, v12, v13\n"
    "v_cvt_pk_bf16_f32 v46, v16, v17\n"
    "buffer_store_dwordx4 v[44:47], v6, s[20:23], 0 offen offset:0 sc1\n"
    "v_cvt_pk_bf16_f32 v48, v10, v11\n"
    "v_cvt_pk_bf16_f32 v49, v14, v15\n"
    "v_cvt_pk_bf16_f32 v50, v18, v19\n"
    "buffer_store_dwordx4 v[48:51], v6, s[20:23], 0 offen offset:3072 sc1\n"
    "s_branch END\n"
    "FIN:\n"
    "s_branch E_70\n"
    "CHE_8:\n"
    "s_cbranch_execz CHE_9\n"
    "R_69:\n"
    "s_sub_u32 s39, s39, 1\n"
    "s_cbranch_scc1 ABORT\n"
    "E_70:\n"
    "buffer_load_dwordx4 v[32:35], v24, s[20:23], s47 offen sc1\n"
    "buffer_load_dwordx2 v[36:37], v28, s[20:23], s45 offen sc1\n"
    "buffer_load_dwordx2 v[38:39], v28, s[20:23], s46 offen sc1\n"
    "s_waitcnt vmcnt(0)\n"
    "v_bitop3_b32 v42, v37, v39, s36 bitop3:0x7e\n"
    "v_xor_b32 v43, s36, v35\n"
    "v_or_b32 v42, v42, v43\n"
    "v_cmp_ne_u32 vcc, 0, v42\n"
    "s_cbranch_vccnz R_69\n"
    "v_cmp_eq_u32 vcc, 1, v26\n"
    "v_cmp_eq_u32 s[50:51], 2, v26\n"
    "v_and_b32 v42, 1, v29\n"
    "v_cndmask_b32 v32, v32, v33, vcc\n"
    "v_cndmask_b32 v32, v32, v34, s[50:51]\n"
    "v_lshlrev_b32 v42, 4, v42\n"
    "v_lshrrev_b32 v32, v42, v32\n"
    "v_lshlrev_b32 v32, 16, v32\n"
    "v_add_f32 v41, v41, v32\n"
    "v_add_f32 v36, v36, v38\n"
    "v_cvt_pk_bf16_f32 v41, 0, v41\n"
    "v_cvt_pk_bf16_f32 v36, 0, v36\n"
    "s_brev_b32 s48, 1\n"
    "v_ashrrev_i32 v42, 31, v23\n"
    "v_bitop3_b32 v23, v42, v23, s48 bitop3:0x63\n"
    "v_and_b32 v23, 0xfffff800, v23\n"
    "v_cvt_pk_bf16_f32 v23, 0, v23\n"
    "v_mul_f32 v23, 0xbfb8aa3b, v23\n"
    "v_exp_f32 v23, v23\n"
    "v_lshlrev_b32 v20, 4, v20\n"
    "v_add_f32 v23, 1.0, v23\n"
    "v_rcp_f32 v23, v23\n"
    "s_movk_i32 s44, 0x3f80\n"
    "v_lshlrev_b32_e64 v20, v20, s44\n"
    "v_cvt_pk_bf16_f32 v23, 0, v23\n"
    "v_cmp_eq_u32 vcc, 0, v25\n"
    "s_nop 1\n"
    "v_cndmask_b32 v32, 0, v20, vcc\n"
    "v_cmp_eq_u32 vcc, 1, v25\n"
    "s_nop 1\n"
    "v_cndmask_b32 v33, 0, v20, vcc\n"
    "v_cmp_eq_u32 vcc, 2, v25\n"
    "s_nop 1\n"
    "v_cndmask_b32 v34, 0, v20, vcc\n"
    "v_mov_b32 v35, 0\n"
    "v_lshrrev_b32 v42, 2, v1\n"
    "v_mul_u32_u24 v42, 12416, v42\n"
    "v_and_b32 v43, 3, v0\n"
    "v_lshl_add_u32 v42, v43, 4, v42\n"
    "s_mul_i32 s43, s43, 3072\n"
    "s_lshl_b32 s42, s42, 4\n"
    "s_add_u32 s42, s42, s43\n"
    "s_add_u32 s50, s42, 6447104\n"
    "s_add_u32 s51, s42, 6645760\n"
    "s_add_u32 s52, s42, 6844416\n"
    "s_add_u32 s53, s42, 7043072\n"
    "s_add_u32 s54, s42, 7241728\n"
    "v_cmp_gt_u32 vcc, 32, v1\n"
    "v_mov_b32 v43, 0x7ffffff0\n"
    "v_mov_b32 v21, s36\n"
    "v_cndmask_b32 v43, v43, v42, vcc\n"
    "v_cndmask_b32 v21, 0, v21, vcc\n"
    "s_mov_b32 s46, 0x003f003f\n"
    "s_mov_b32 s47, 0x003f003f\n"
    "U_71:\n"
    "s_memrealtime s[98:99]\n"
    "s_waitcnt lgkmcnt(0)\n"
    "s_sub_u32 s98, s98, s96\n"
    "s_cmpk_lt_u32 s98, 443\n"
    "s_cbranch_scc1 U_71\n"
    "buffer_load_dwordx4 v[104:107], v42, s[20:23], s50 offen sc1\n"
    "buffer_load_dwordx4 v[108:111], v42, s[20:23], s51 offen sc1\n"
    "buffer_load_dwordx4 v[112:115], v42, s[20:23], s52 offen sc1\n"
    "buffer_load_dwordx4 v[116:119], v42, s[20:23], s53 offen sc1\n"
    "buffer_load_dwordx4 v[120:123], v43, s[20:23], s54 offen sc1\n"
    "buffer_load_dword v22, v31, s[20:23], 0 offen offset:1024 sc1\n"
    "s_branch E_73\n"
    "CHE_9:\n"
    "s_cbranch_execz CHE_10\n"
    "R_72:\n"
    "s_sub_u32 s39, s39, 1\n"
    "s_cbranch_scc1 ABORT\n"
    "s_wqm_b64 exec, vcc\n"
    "buffer_load_dwordx4 v[104:107], v42, s[20:23], s50 offen sc1\n"
    "buffer_load_dwordx4 v[108:111], v42, s[20:23], s51 offen sc1\n"
    "buffer_load_dwordx4 v[112:115], v42, s[20:23], s52 offen sc1\n"
    "buffer_load_dwordx4 v[116:119], v42, s[20:23], s53 offen sc1\n"
    "buffer_load_dwordx4 v[120:123], v43, s[20:23], s54 offen sc1\n"
    "buffer_load_dword v22, v31, s[20:23], 0 offen offset:1024 sc1\n"
    "s_mov_b64 exec, -1\n"
    "E_73:\n"
    "s_waitcnt vmcnt(2)\n"
    "v_bitop3_b32 v24, v107, v111, s36 bitop3:0x7e\n"
    "v_bitop3_b32 v25, v115, v119, s36 bitop3:0x7e\n"
    "s_waitcnt vmcnt(0)\n"
    "v_xor_b32 v26, v21, v123\n"
    "v_bitop3_b32 v24, v24, v22, s36 bitop3:0xf6\n"
    "v_or3_b32 v24, v24, v25, v26\n"
    "v_cmp_ne_u32 vcc, 0, v24\n"
    "s_cbranch_vccnz R_72\n"
    "v_mov_b32 v107, 0\n"
    "v_mov_b32 v111, 0\n"
    "v_mov_b32 v115, 0\n"
    "v_mov_b32 v119, 0\n"
    "v_mov_b32 v123, 0\n"
    "v_mfma_f32_16x16x32_bf16 v[8:11], v[104:107], v[32:35], 0\n"
    "v_mfma_f32_16x16x32_bf16 v[8:11], v[108:111], v[32:35], v[8:11]\n"
    "v_mfma_f32_16x16x32_bf16 v[8:11], v[112:115], v[32:35], v[8:11]\n"
    "v_mfma_f32_16x16x32_bf16 v[8:11], v[116:119], v[32:35], v[8:11]\n"
    "v_mfma_f32_16x16x32_bf16 v[8:11], v[120:123], v[32:35], v[8:11]\n"
    "s_nop 7\n"
    "v_permlane32_swap_b32 v8, v10\n"
    "v_permlane32_swap_b32 v9, v11\n"
    "v_add_f32 v8, v8, v10\n"
    "v_add_f32 v9, v9, v11\n"
    "s_nop 1\n"
    "v_permlane16_swap_b32 v8, v9\n"
    "v_add_f32 v8, v8, v9\n"
    "v_cvt_pk_bf16_f32 v8, 0, v8\n"
    "v_mul_f32 v8, v8, v23\n"
    "v_cvt_pk_bf16_f32 v8, 0, v8\n"
    "v_add_f32 v8, v40, v8\n"
    "v_cvt_pk_bf16_f32 v8, 0, v8\n"
    "v_mul_f32 v8, v36, v8\n"
    "v_cvt_pk_bf16_f32 v8, 0, v8\n"
    "v_add_f32 v8, v41, v8\n"
    "v_cvt_pk_bf16_f32 v8, v8, v8\n"
    "s_mov_b64 exec, s[46:47]\n"
    "global_store_short v30, v8, s[34:35]\n"
    "s_mov_b64 exec, -1\n"
    "s_branch END\n"
    "ROW2:\n"
    "v_bitop3_b32 v23, v23, s61, v23 bitop3:0x0c\n"
    "v_mad_u32_u24 v26, v23, s60, v8\n"
    "v_mad_u32_u24 v27, v23, s60, v9\n"
    "s_add_u32 s46, s62, 28\n"
    "s_cmp_gt_u32 s42, s46\n"
    "s_cbranch_scc1 ROW6\n"
    "buffer_load_dwordx4 v[28:31], v24, s[20:23], s59 offen sc1\n"
    "buffer_load_dwordx4 v[32:35], v25, s[20:23], s59 offen sc1\n"
    "buffer_load_dwordx4 v[36:39], v26, s[20:23], s59 offen sc1\n"
    "buffer_load_dwordx4 v[40:43], v27, s[20:23], s59 offen sc1\n"
    "ROW3:\n"
    "s_branch E_75\n"
    "CHE_10:\n"
    "s_cbranch_execz CHE_11\n"
    "R_74:\n"
    "s_sub_u32 s39, s39, 1\n"
    "s_cbranch_scc1 ABORT\n"
    "E_75:\n"
    "ds_read_b32 v10, v14 offset:88\n"
    "ds_read2_b32 v[44:45], v54 offset1:7\n"
    "ds_read_b32 v46, v55\n"
    "s_waitcnt lgkmcnt(0)\n"
    "v_cmp_ne_u32 s[50:51], 8, v10\n"
    "v_cmp_eq_u32 vcc, 0, v44\n"
    "s_or_b64 vcc, vcc, s[50:51]\n"
    "s_cbranch_vccnz R_74\n"
    "v_cmp_gt_u32 vcc, v46, v44\n"
    "s_bcnt1_i32_b64 s43, vcc\n"
    "s_cmp_ge_u32 s43, 8\n"
    "s_cbranch_scc1 ROW4\n"
    "s_and_b32 s44, s43, 1\n"
    "s_mul_i32 s45, s43, 1344\n"
    "s_lshl_b32 s44, s44, 5\n"
    "s_sub_u32 s45, s45, s44\n"
    "v_add_u32 v50, s45, v11\n"
    "v_add_u32 v51, s45, v12\n"
    "s_waitcnt vmcnt(3)\n"
    "ds_write2_b32 v50, v28, v29 offset1:1\n"
    "ds_write_b32 v50, v30 offset:8\n"
    "v_lshl_add_u32 v52, s43, 2, v14\n"
    "s_waitcnt vmcnt(2)\n"
    "ds_write2_b32 v51, v32, v33 offset1:1\n"
    "ds_write_b32 v51, v34 offset:8\n"
    "v_bitop3_b32 v20, v31, v35, s63 bitop3:0x7e\n"
    "ds_write_b32 v52, v44 offset:3072\n"
    "v_cmp_ne_u32 vcc, 0, v20\n"
    "s_cbranch_vccnz ROWT\n"
    "ROW4:\n"
    "v_cmp_gt_u32 vcc, v46, v45\n"
    "s_bcnt1_i32_b64 s43, vcc\n"
    "s_cmp_ge_u32 s43, 8\n"
    "s_cbranch_scc1 MOE5\n"
    "s_and_b32 s44, s43, 1\n"
    "s_mul_i32 s45, s43, 1344\n"
    "s_lshl_b32 s44, s44, 5\n"
    "s_sub_u32 s45, s45, s44\n"
    "v_add_u32 v50, s45, v11\n"
    "v_add_u32 v51, s45, v12\n"
    "s_waitcnt vmcnt(1)\n"
    "ds_write2_b32 v50, v36, v37 offset1:1\n"
    "ds_write_b32 v50, v38 offset:8\n"
    "v_lshl_add_u32 v52, s43, 2, v14\n"
    "s_waitcnt vmcnt(0)\n"
    "ds_write2_b32 v51, v40, v41 offset1:1\n"
    "ds_write_b32 v51, v42 offset:8\n"
    "v_bitop3_b32 v20, v39, v43, s63 bitop3:0x7e\n"
    "ds_write_b32 v52, v45 offset:3072\n"
    "v_cmp_ne_u32 vcc, 0, v20\n"
    "s_cbranch_vccz MOE5\n"
    "s_branch ROWT\n"
    "CHE_11:\n"
    "s_cbranch_execz CHE_12\n"
    "ROW6:\n"
    "v_bitop3_b32 v49, v48, s61, v48 bitop3:0x0c\n"
    "v_mad_u32_u24 v48, v49, s60, v8\n"
    "v_mad_u32_u24 v49, v49, s60, v9\n"
    "ROW7:\n"
    "s_sub_u32 s39, s39, 1\n"
    "s_cbranch_scc1 ABORT\n"
    "buffer_load_dwordx4 v[28:31], v24, s[20:23], s59 offen sc1\n"
    "buffer_load_dwordx4 v[32:35], v25, s[20:23], s59 offen sc1\n"
    "buffer_load_dwordx4 v[36:39], v26, s[20:23], s59 offen sc1\n"
    "buffer_load_dwordx4 v[40:43], v27, s[20:23], s59 offen sc1\n"
    "buffer_load_dwordx4 v[112:115], v48, s[20:23], s59 offen sc1\n"
    "buffer_load_dwordx4 v[116:119], v49, s[20:23], s59 offen sc1\n"
    "s_branch E_77\n"
    "R_76:\n"
    "s_sub_u32 s39, s39, 1\n"
    "s_cbranch_scc1 ABORT\n"
    "E_77:\n"
    "ds_read_b32 v10, v14 offset:88\n"
    "ds_read2_b32 v[44:45], v54 offset1:7\n"
    "ds_read_b32 v47, v54 offset:56\n"
    "ds_read_b32 v46, v55\n"
    "s_waitcnt lgkmcnt(0)\n"
    "v_cmp_ne_u32 s[50:51], 8, v10\n"
    "v_cmp_eq_u32 vcc, 0, v44\n"
    "s_or_b64 vcc, vcc, s[50:51]\n"
    "s_cbranch_vccnz R_76\n"
    "v_cmp_gt_u32 vcc, v46, v44\n"
    "s_bcnt1_i32_b64 s43, vcc\n"
    "s_cmp_ge_u32 s43, 8\n"
    "s_cbranch_scc1 ROW8\n"
    "s_and_b32 s44, s43, 1\n"
    "s_mul_i32 s45, s43, 1344\n"
    "s_lshl_b32 s44, s44, 5\n"
    "s_sub_u32 s45, s45, s44\n"
    "v_add_u32 v50, s45, v11\n"
    "v_add_u32 v51, s45, v12\n"
    "s_waitcnt vmcnt(5)\n"
    "ds_write2_b32 v50, v28, v29 offset1:1\n"
    "ds_write_b32 v50, v30 offset:8\n"
    "v_lshl_add_u32 v52, s43, 2, v14\n"
    "s_waitcnt vmcnt(4)\n"
    "ds_write2_b32 v51, v32, v33 offset1:1\n"
    "ds_write_b32 v51, v34 offset:8\n"
    "v_bitop3_b32 v20, v31, v35, s63 bitop3:0x7e\n"
    "ds_write_b32 v52, v44 offset:3072\n"
    "v_cmp_ne_u32 vcc, 0, v20\n"
    "s_cbranch_vccnz ROW7\n"
    "ROW8:\n"
    "v_cmp_gt_u32 vcc, v46, v45\n"
    "s_bcnt1_i32_b64 s43, vcc\n"
    "s_cmp_ge_u32 s43, 8\n"
    "s_cbranch_scc1 ROW9\n"
    "s_and_b32 s44, s43, 1\n"
    "s_mul_i32 s45, s43, 1344\n"
    "s_lshl_b32 s44, s44, 5\n"
    "s_sub_u32 s45, s45, s44\n"
    "v_add_u32 v50, s45, v11\n"
    "v_add_u32 v51, s45, v12\n"
    "s_waitcnt vmcnt(3)\n"
    "ds_write2_b32 v50, v36, v37 offset1:1\n"
    "ds_write_b32 v50, v38 offset:8\n"
    "v_lshl_add_u32 v52, s43, 2, v14\n"
    "s_waitcnt vmcnt(2)\n"
    "ds_write2_b32 v51, v40, v41 offset1:1\n"
    "ds_write_b32 v51, v42 offset:8\n"
    "v_bitop3_b32 v20, v39, v43, s63 bitop3:0x7e\n"
    "ds_write_b32 v52, v45 offset:3072\n"
    "v_cmp_ne_u32 vcc, 0, v20\n"
    "s_cbranch_vccnz ROW7\n"
    "ROW9:\n"
    "v_cmp_gt_u32 vcc, v46, v47\n"
    "s_bcnt1_i32_b64 s43, vcc\n"
    "s_cmp_ge_u32 s43, 8\n"
    "s_cbranch_scc1 ROWA\n"
    "s_and_b32 s44, s43, 1\n"
    "s_mul_i32 s45, s43, 1344\n"
    "s_lshl_b32 s44, s44, 5\n"
    "s_sub_u32 s45, s45, s44\n"
    "v_add_u32 v50, s45, v11\n"
    "v_add_u32 v51, s45, v12\n"
    "s_waitcnt vmcnt(1)\n"
    "ds_write2_b32 v50, v112, v113 offset1:1\n"
    "ds_write_b32 v50, v114 offset:8\n"
    "v_lshl_add_u32 v52, s43, 2, v14\n"
    "s_waitcnt vmcnt(0)\n"
    "ds_write2_b32 v51, v116, v117 offset1:1\n"
    "ds_write_b32 v51, v118 offset:8\n"
    "v_bitop3_b32 v20, v115, v119, s63 bitop3:0x7e\n"
    "ds_write_b32 v52, v47 offset:3072\n"
    "v_cmp_ne_u32 vcc, 0, v20\n"
    "s_cbranch_vccnz ROW7\n"
    "ROWA:\n"
    "s_waitcnt vmcnt(0)\n"
    "s_add_u32 s46, s46, 28\n"
    "s_cmp_le_u32 s42, s46\n"
    "s_cbranch_scc1 MOE5\n"
    "s_sub_u32 s47, s46, 320\n"
    "v_mov_b32 v44, s47\n"
    "ds_read_b32 v44, v44\n"
    "s_waitcnt lgkmcnt(0)\n"
    "v_cmp_gt_u32 vcc, v46, v44\n"
    "s_bcnt1_i32_b64 s43, vcc\n"
    "s_cmp_ge_u32 s43, 8\n"
    "s_cbranch_scc1 ROWA\n"
    "v_bitop3_b32 v25, v44, s61, v44 bitop3:0x0c\n"
    "v_mad_u32_u24 v24, v25, s60, v8\n"
    "v_mad_u32_u24 v25, v25, s60, v9\n"
    "ROWB:\n"
    "s_sub_u32 s39, s39, 1\n"
    "s_cbranch_scc1 ABORT\n"
    "buffer_load_dwordx4 v[28:31], v24, s[20:23], s59 offen sc1\n"
    "buffer_load_dwordx4 v[32:35], v25, s[20:23], s59 offen sc1\n"
    "v_cmp_gt_u32 vcc, v46, v44\n"
    "s_bcnt1_i32_b64 s43, vcc\n"
    "s_cmp_ge_u32 s43, 8\n"
    "s_cbranch_scc1 ROWA\n"
    "s_and_b32 s44, s43, 1\n"
    "s_mul_i32 s45, s43, 1344\n"
    "s_lshl_b32 s44, s44, 5\n"
    "s_sub_u32 s45, s45, s44\n"
    "v_add_u32 v50, s45, v11\n"
    "v_add_u32 v51, s45, v12\n"
    "s_waitcnt vmcnt(1)\n"
    "ds_write2_b32 v50, v28, v29 offset1:1\n"
    "ds_write_b32 v50, v30 offset:8\n"
    "v_lshl_add_u32 v52, s43, 2, v14\n"
    "CHE_12:\n"
    "s_cbranch_execz CHE9\n"
    "s_waitcnt vmcnt(0)\n"
    "ds_write2_b32 v51, v32, v33 offset1:1\n"
    "ds_write_b32 v51, v34 offset:8\n"
    "v_bitop3_b32 v20, v31, v35, s63 bitop3:0x7e\n"
    "ds_write_b32 v52, v44 offset:3072\n"
    "v_cmp_ne_u32 vcc, 0, v20\n"
    "s_cbranch_vccnz ROWB\n"
    "s_branch ROWA\n"
    "MOE15:\n"
    "ds_read_b64 v[20:21], v14 offset:64\n"
    "ds_read_b32 v32, v19\n"
    "s_sub_u32 s39, s39, 1\n"
    "s_cbranch_scc1 ABORT\n"
    "s_waitcnt lgkmcnt(0)\n"
    "v_cmp_ne_u32 vcc, 4, v21\n"
    "v_readfirstlane_b32 s42, v20\n"
    "v_bitop3_b32 v32, v32, s61, v32 bitop3:0x0c\n"
    "s_cbranch_vccnz MOE15\n"
    "s_cmp_gt_u32 s42, 151904\n"
    "s_cbranch_scc1 MANY\n"
    "MOE16:\n"
    "v_cmp_gt_u32 s[50:51], s42, v19\n"
    "v_lshlrev_b32 v32, 3, v32\n"
    "s_movk_i32 s43, 4096\n"
    "s_mov_b64 exec, s[50:51]\n"
    "buffer_load_dwordx2 v[34:35], v32, s[20:23], s43 offen sc1\n"
    "s_mov_b64 exec, -1\n"
    "R_78:\n"
    "s_waitcnt vmcnt(0)\n"
    "v_cmp_ne_u32 vcc, s36, v35\n"
    "v_max_u32 v34, 1, v34\n"
    "v_cndmask_b32 v53, 0, v34, s[50:51]\n"
    "s_and_b64 vcc, vcc, s[50:51]\n"
    "s_cbranch_vccnz XKS\n"
    "ds_write_b32 v55, v53\n"
    "s_branch MOE5\n"
    "XKS:\n"
    "s_sub_u32 s39, s39, 1\n"
    "s_cbranch_scc1 ABORT\n"
    "s_mov_b64 exec, s[50:51]\n"
    "buffer_load_dwordx2 v[34:35], v32, s[20:23], s43 offen sc1\n"
    "s_mov_b64 exec, -1\n"
    "s_branch R_78\n"
    "ROWS:\n"
    "s_sub_u32 s39, s39, 1\n"
    "s_cbranch_scc1 ABORT\n"
    "buffer_load_dwordx4 v[28:31], v24, s[20:23], s59 offen sc1\n"
    "buffer_load_dwordx4 v[32:35], v25, s[20:23], s59 offen sc1\n"
    "s_branch ROW1\n"
    "ROWT:\n"
    "s_sub_u32 s39, s39, 1\n"
    "s_cbranch_scc1 ABORT\n"
    "buffer_load_dwordx4 v[28:31], v24, s[20:23], s59 offen sc1\n"
    "buffer_load_dwordx4 v[32:35], v25, s[20:23], s59 offen sc1\n"
    "buffer_load_dwordx4 v[36:39], v26, s[20:23], s59 offen sc1\n"
    "buffer_load_dwordx4 v[40:43], v27, s[20:23], s59 offen sc1\n"
    "s_branch ROW3\n"
    "TOPS:\n"
    "s_mov_b64 exec, -1\n"
    "v_cmpx_le_u32 vcc, s49, v24\n"
    "ds_add_rtn_u32 v21, v14, v18 offset:64\n"
    "s_waitcnt lgkmcnt(0)\n"
    "ds_write_b32 v21, v24\n"
    "s_mov_b64 exec, -1\n"
    "v_cmpx_le_u32 vcc, s49, v26\n"
    "ds_add_rtn_u32 v21, v14, v18 offset:64\n"
    "s_waitcnt lgkmcnt(0)\n"
    "ds_write_b32 v21, v26\n"
    "s_mov_b64 exec, -1\n"
    "v_cmpx_le_u32 vcc, s49, v28\n"
    "ds_add_rtn_u32 v21, v14, v18 offset:64\n"
    "s_waitcnt lgkmcnt(0)\n"
    "ds_write_b32 v21, v28\n"
    "s_mov_b64 exec, -1\n"
    "v_cmpx_le_u32 vcc, s49, v30\n"
    "ds_add_rtn_u32 v21, v14, v18 offset:64\n"
    "s_waitcnt lgkmcnt(0)\n"
    "ds_write_b32 v21, v30\n"
    "s_mov_b64 exec, -1\n"
    "v_cmpx_le_u32 vcc, s49, v32\n"
    "ds_add_rtn_u32 v21, v14, v18 offset:64\n"
    "s_waitcnt lgkmcnt(0)\n"
    "ds_write_b32 v21, v32\n"
    "s_mov_b64 exec, -1\n"
    "v_cmpx_le_u32 vcc, s49, v34\n"
    "ds_add_rtn_u32 v21, v14, v18 offset:64\n"
    "s_waitcnt lgkmcnt(0)\n"
    "ds_write_b32 v21, v34\n"
    "s_mov_b64 exec, -1\n"
    "v_cmpx_le_u32 vcc, s49, v36\n"
    "ds_add_rtn_u32 v21, v14, v18 offset:64\n"
    "s_waitcnt lgkmcnt(0)\n"
    "ds_write_b32 v21, v36\n"
    "s_mov_b64 exec, -1\n"
    "v_cmpx_le_u32 vcc, s49, v38\n"
    "ds_add_rtn_u32 v21, v14, v18 offset:64\n"
    "s_waitcnt lgkmcnt(0)\n"
    "ds_write_b32 v21, v38\n"
    "s_branch TOPC\n"
    "MANY:\n"
    "v_lshlrev_b32 v22, 2, v0\n"
    "v_add_u32 v22, 149600, v22\n"
    "MANY1:\n"
    "v_cmpx_gt_u32 vcc, s42, v22\n"
    "s_cbranch_execz MANY3\n"
    "ds_read_b32 v23, v22\n"
    "v_mov_b32 v24, 0\n"
    "s_mov_b32 s43, 151648\n"
    "MANY2:\n"
    "v_mov_b32 v25, s43\n"
    "ds_read_b32 v25, v25\n"
    "s_add_u32 s43, s43, 4\n"
    "s_waitcnt lgkmcnt(0)\n"
    "v_cmp_gt_u32 vcc, v25, v23\n"
    "s_nop 1\n"
    "v_addc_co_u32 v24, vcc, 0, v24, vcc\n"
    "s_cmp_lt_u32 s43, s42\n"
    "s_cbranch_scc1 MANY2\n"
    "v_min_u32 v24, 16, v24\n"
    "v_lshl_add_u32 v24, v24, 2, v14\n"
    "ds_write_b32 v24, v23 offset:3152\n"
    "v_add_u32 v22, 2048, v22\n"
    "s_branch MANY1\n"
    "MANY3:\n"
    "s_mov_b64 exec, -1\n"
    "s_mov_b64 exec, 1\n"
    "ds_add_u32 v14, v15 offset:92\n"
    "s_mov_b64 exec, -1\n"
    "s_branch E_80\n"
    "R_79:\n"
    "s_sleep 1\n"
    "s_sub_u32 s39, s39, 1\n"
    "s_cbranch_scc1 ABORT\n"
    "E_80:\n"
    "ds_read_b32 v10, v14 offset:92\n"
    "s_waitcnt lgkmcnt(0)\n"
    "v_cmp_ne_u32 vcc, 8, v10\n"
    "s_cbranch_vccnz R_79\n"
    "v_subrev_u32 v7, 400, v7\n"
    "v_lshl_add_u32 v24, v2, 2, v14\n"
    "ds_read2_b32 v[22:23], v7 offset1:7\n"
    "ds_read_b32 v48, v7 offset:56\n"
    "ds_read_b32 v32, v24 offset:3152\n"
    "s_mov_b32 s42, 151712\n"
    "s_waitcnt lgkmcnt(0)\n"
    "v_bitop3_b32 v22, v22, s61, v22 bitop3:0x0c\n"
    "v_bitop3_b32 v32, v32, s61, v32 bitop3:0x0c\n"
    "s_cmpk_eq_u32 s37, 15\n"
    "s_cbranch_scc1 MOE16\n"
    "s_branch MOE4\n"
    "END:\n"
    "s_endpgm\n"

    :: "{s[0:1]}"(__builtin_amdgcn_kernarg_segment_ptr()), "{s2}"(blockIdx.x), "{v0}"(threadIdx.x) : "v127", "s99", "vcc", "memory");
}

extern "C" void kernel_launch(void* const* d_in, const int* in_sizes, int n_in, void* d_out, int out_size, void* d_ws, size_t ws_size, hipStream_t stream) {
  (void)hipFuncSetAttribute((const void*)mega, hipFuncAttributeMaxDynamicSharedMemorySize, 159904);
  Args p;
  p.x = d_in[0]; p.lar = d_in[1]; p.aim = d_in[2]; p.ldt = d_in[5]; p.cvec = d_in[12]; p.wmod = d_in[13];
  p.ws = (char*)d_ws + ((size_t)0 << 20); p.ws_bytes = 10643968; p.ws_flags = 0x00020000u;
  p.cre = d_in[3]; p.cim = d_in[4]; p.dsk = d_in[6]; p.wr = d_in[8]; p.wg = d_in[9]; p.wu = d_in[10]; p.wd = d_in[11];
  p.out = d_out;
  mega<<<256, 1024, 159904, stream>>>(p);
}
```
